```python
import torch

def _softmax(x, axis=-1):
    return torch.softmax(x, dim=axis)
def _gelu(x):
    return torch.nn.functional.gelu(x, approximate="tanh")
def _silu(x):
    return torch.nn.functional.silu(x)
def _sigmoid(x):
    return torch.sigmoid(x)
def _layernorm(x, axis=-1, eps=1e-5):
    m = x.mean(dim=axis, keepdim=True)
    v = x.var(dim=axis, keepdim=True, unbiased=False)
    return (x - m) / torch.sqrt(v + eps)
def _scatter_add(tgt, idx, src, axis=0):
    return tgt.clone().index_add_(axis, idx, src)
def _segment_sum(src, seg, n):
    out = torch.zeros((n,) + src.shape[1:], dtype=src.dtype)
    return out.index_add_(0, seg, src)
def _pad(x, pw):
    flat = tuple(v for pair in reversed(pw) for v in pair)
    return torch.nn.functional.pad(x, flat)

kernel_name = 'standalone__convnext_block__vision'

def setup_inputs():

    _w = (lambda a: (lambda t: t.to(torch.bfloat16) if t.is_floating_point() else t)(torch.as_tensor(a)))
    H = W = 512
    C = 96
    return {
        "x": _w((lambda *s: torch.randn(*s, dtype=torch.bfloat16) * 0.05)(1, H, W, C)),
        "dw": _w((lambda *s: torch.randn(*s, dtype=torch.bfloat16) * 0.05)(7, 7, C)),
        "w1": _w((lambda *s: torch.randn(*s, dtype=torch.bfloat16) * 0.05)(C, 4 * C)),
        "w2": _w((lambda *s: torch.randn(*s, dtype=torch.bfloat16) * 0.05)(4 * C, C)),
    }

def reference(x, dw, w1, w2):
    B, H, W, C = x.shape
    pad = 3
    xp = _pad(x, ((0,0),(pad,pad),(pad,pad),(0,0)))
    out = torch.zeros_like(x)
    for i in range(7):
        for j in range(7):
            out = out + xp[:, i:i+H, j:j+W, :] * dw[i, j][None, None, None, :]
    h = _layernorm(out, axis=-1)
    return x + _gelu(h @ w1) @ w2
```

```cpp
#include <hip/hip_runtime.h>
#include <hip/hip_bf16.h>
#include <stdint.h>

namespace {

typedef __attribute__((ext_vector_type(4))) unsigned u32x4;
typedef __attribute__((ext_vector_type(2))) unsigned u32x2;
typedef __attribute__((address_space(3))) void* lds_ptr_t;

constexpr int IMG = 512;
constexpr int CH = 96;
constexpr int HID = 384;
constexpr int ROWB = IMG * CH * 2;
constexpr int LUTN = 27 * 128;
constexpr int W1OFF = LUTN * 2;
constexpr int W2OFF = W1OFF + 4 * 6144;
constexpr int ZOFF = W2OFF + 3 * 6160;
constexpr int STOFF = ZOFF + 64 * 208;
constexpr int FLOFF = STOFF + 2048;
constexpr int XOFF = 65536 + LUTN * 2;
constexpr int PS = 15184;
constexpr int PFOFF = XOFF + 6 * PS;
constexpr int LDS_BYTES = 160 * 1024;
static_assert(FLOFF + 16 <= 65536 && PFOFF + 256 <= LDS_BYTES, "LDS map");
constexpr unsigned OOB = 0x40000000u;

constexpr float DL = 1.0f + 1.0f / 362.0f;
constexpr float CT = 0.46866869f;
constexpr unsigned GH1 = 0xc4eac4eau;
constexpr unsigned GHC = 0x377a377au;

#define M_ASM \
  "s_setprio 2\n\t" \
  "s_waitcnt vmcnt(0)\n\t" \
  "s_barrier\n\t" \
  "ds_read_b64_tr_b16 v[208:209], v249 offset:0\n\t" \
  "ds_read_b64_tr_b16 v[210:211], v249 offset:256\n\t" \
  "ds_read_b64_tr_b16 v[212:213], v249 offset:2048\n\t" \
  "ds_read_b64_tr_b16 v[214:215], v249 offset:2304\n\t" \
  "ds_read_b64_tr_b16 v[216:217], v249 offset:4096\n\t" \
  "ds_read_b64_tr_b16 v[218:219], v249 offset:4352\n\t" \
  "ds_read_b64_tr_b16 v[220:221], v249 offset:512\n\t" \
  "ds_read_b64_tr_b16 v[222:223], v249 offset:768\n\t" \
  "ds_read_b128 v[96:99], v251 offset:0\n\t" \
  "ds_read_b128 v[100:103], v251 offset:64\n\t" \
  "ds_read_b128 v[104:107], v251 offset:128\n\t" \
  "s_waitcnt lgkmcnt(2)\n\t" \
  "v_mfma_f32_16x16x32_bf16 v[144:147], v[208:211], v[96:99], 0\n\t" \
  "ds_read_b64_tr_b16 v[208:209], v249 offset:2560\n\t" \
  "ds_read_b64_tr_b16 v[210:211], v249 offset:2816\n\t" \
  "s_waitcnt lgkmcnt(3)\n\t" \
  "v_mfma_f32_16x16x32_bf16 v[144:147], v[212:215], v[100:103], v[144:147]\n\t" \
  "ds_read_b64_tr_b16 v[212:213], v249 offset:4608\n\t" \
  "ds_read_b64_tr_b16 v[214:215], v249 offset:4864\n\t" \
  "s_waitcnt lgkmcnt(4)\n\t" \
  "v_mfma_f32_16x16x32_bf16 v[144:147], v[216:219], v[104:107], v[144:147]\n\t" \
  "s_barrier\n\t" \
  "ds_read_b64_tr_b16 v[216:217], v249 offset:6144\n\t" \
  "ds_read_b64_tr_b16 v[218:219], v249 offset:6400\n\t" \
  "v_mfma_f32_16x16x32_bf16 v[148:151], v[220:223], v[96:99], 0\n\t" \
  "ds_read_b64_tr_b16 v[220:221], v249 offset:8192\n\t" \
  "ds_read_b64_tr_b16 v[222:223], v249 offset:8448\n\t" \
  "s_nop 1\n\t" \
  "v_lshlrev_b32_sdwa v144, 1, v144 dst_sel:DWORD dst_unused:UNUSED_PAD src0_sel:DWORD src1_sel:WORD_1\n\t" \
  "s_waitcnt lgkmcnt(6)\n\t" \
  "v_mfma_f32_16x16x32_bf16 v[148:151], v[208:211], v[100:103], v[148:151]\n\t" \
  "ds_read_b64_tr_b16 v[208:209], v249 offset:10240\n\t" \
  "ds_read_b64_tr_b16 v[210:211], v249 offset:10496\n\t" \
  "s_waitcnt lgkmcnt(6)\n\t" \
  "v_mfma_f32_16x16x32_bf16 v[148:151], v[212:215], v[104:107], v[148:151]\n\t" \
  "ds_read_b64_tr_b16 v[212:213], v249 offset:6656\n\t" \
  "ds_read_b64_tr_b16 v[214:215], v249 offset:6912\n\t" \
  "v_lshlrev_b32_sdwa v145, 1, v145 dst_sel:DWORD dst_unused:UNUSED_PAD src0_sel:DWORD src1_sel:WORD_1\n\t" \
  "v_lshlrev_b32_sdwa v146, 1, v146 dst_sel:DWORD dst_unused:UNUSED_PAD src0_sel:DWORD src1_sel:WORD_1\n\t" \
  "v_lshlrev_b32_sdwa v147, 1, v147 dst_sel:DWORD dst_unused:UNUSED_PAD src0_sel:DWORD src1_sel:WORD_1\n\t" \
  "s_nop 2\n\t" \
  "v_lshlrev_b32_sdwa v148, 1, v148 dst_sel:DWORD dst_unused:UNUSED_PAD src0_sel:DWORD src1_sel:WORD_1\n\t" \
  "v_lshlrev_b32_sdwa v149, 1, v149 dst_sel:DWORD dst_unused:UNUSED_PAD src0_sel:DWORD src1_sel:WORD_1\n\t" \
  "ds_read_u16_d16_hi v144, v144 offset:0\n\t" \
  "ds_read_u16_d16_hi v145, v145 offset:0\n\t" \
  "ds_read_u16_d16_hi v146, v146 offset:0\n\t" \
  "ds_read_u16_d16_hi v147, v147 offset:0\n\t" \
  "ds_read_b64 v[230:231], v252 offset:0\n\t" \
  "s_waitcnt lgkmcnt(0)\n\t" \
  "v_mfma_f32_16x16x16_bf16 v[0:3], v[246:247], v[230:231], 0\n\t" \
  "ds_read_b64 v[232:233], v252 offset:8\n\t" \
  "ds_read_u16_d16_hi v148, v148 offset:0\n\t" \
  "v_mfma_f32_16x16x32_bf16 v[176:179], v[216:219], v[96:99], 0\n\t" \
  "ds_read_b64_tr_b16 v[216:217], v249 offset:8704\n\t" \
  "ds_read_b64_tr_b16 v[218:219], v249 offset:8960\n\t" \
  "ds_read_b64 v[234:235], v252 offset:64\n\t" \
  "s_waitcnt lgkmcnt(4)\n\t" \
  "v_mfma_f32_16x16x16_bf16 v[4:7], v[246:247], v[232:233], 0\n\t" \
  "v_lshlrev_b32_sdwa v150, 1, v150 dst_sel:DWORD dst_unused:UNUSED_PAD src0_sel:DWORD src1_sel:WORD_1\n\t" \
  "v_mfma_f32_16x16x32_bf16 v[176:179], v[220:223], v[100:103], v[176:179]\n\t" \
  "ds_read_b64_tr_b16 v[220:221], v249 offset:10752\n\t" \
  "ds_read_b64_tr_b16 v[222:223], v249 offset:11008\n\t" \
  "ds_read_b64 v[236:237], v252 offset:72\n\t" \
  "ds_read_u16_d16_hi v149, v149 offset:0\n\t" \
  "s_waitcnt lgkmcnt(4)\n\t" \
  "v_mfma_f32_16x16x16_bf16 v[8:11], v[246:247], v[234:235], 0\n\t" \
  "v_lshlrev_b32_sdwa v151, 1, v151 dst_sel:DWORD dst_unused:UNUSED_PAD src0_sel:DWORD src1_sel:WORD_1\n\t" \
  "v_mfma_f32_16x16x32_bf16 v[176:179], v[208:211], v[104:107], v[176:179]\n\t" \
  "s_barrier\n\t" \
  "ds_read_b64_tr_b16 v[208:209], v250 offset:12320\n\t" \
  "ds_read_b64_tr_b16 v[210:211], v250 offset:13088\n\t" \
  "ds_read_u16_d16_hi v150, v150 offset:0\n\t" \
  "ds_read_b64 v[238:239], v252 offset:128\n\t" \
  "s_waitcnt lgkmcnt(5)\n\t" \
  "v_mfma_f32_16x16x16_bf16 v[12:15], v[246:247], v[236:237], 0\n\t" \
  "v_mfma_f32_16x16x32_bf16 v[180:183], v[212:215], v[96:99], 0\n\t" \
  "ds_read_b64_tr_b16 v[212:213], v250 offset:12328\n\t" \
  "ds_read_b64_tr_b16 v[214:215], v250 offset:13096\n\t" \
  "ds_read_b64 v[240:241], v252 offset:136\n\t" \
  "s_waitcnt lgkmcnt(3)\n\t" \
  "v_mfma_f32_16x16x16_bf16 v[16:19], v[246:247], v[238:239], 0\n\t" \
  "ds_read_u16_d16_hi v151, v151 offset:0\n\t" \
  "v_lshlrev_b32_sdwa v176, 1, v176 dst_sel:DWORD dst_unused:UNUSED_PAD src0_sel:DWORD src1_sel:WORD_1\n\t" \
  "v_mfma_f32_16x16x32_bf16 v[180:183], v[216:219], v[100:103], v[180:183]\n\t" \
  "ds_read_b64_tr_b16 v[216:217], v250 offset:12384\n\t" \
  "ds_read_b64_tr_b16 v[218:219], v250 offset:13152\n\t" \
  "s_waitcnt lgkmcnt(3)\n\t" \
  "v_mfma_f32_16x16x16_bf16 v[20:23], v[246:247], v[240:241], 0\n\t" \
  "v_mfma_f32_16x16x32_bf16 v[180:183], v[220:223], v[104:107], v[180:183]\n\t" \
  "ds_read_b64_tr_b16 v[220:221], v250 offset:12392\n\t" \
  "ds_read_b64_tr_b16 v[222:223], v250 offset:13160\n\t" \
  "v_lshlrev_b32_sdwa v177, 1, v177 dst_sel:DWORD dst_unused:UNUSED_PAD src0_sel:DWORD src1_sel:WORD_1\n\t" \
  "s_waitcnt lgkmcnt(4)\n\t" \
  "v_smfmac_f32_16x16x64_bf16 v[0:3], v[208:211], v[144:151], v248\n\t" \
  "ds_read_b64_tr_b16 v[208:209], v250 offset:12448\n\t" \
  "ds_read_b64_tr_b16 v[210:211], v250 offset:13216\n\t" \
  "ds_read_u16_d16_hi v176, v176 offset:0\n\t" \
  "v_lshlrev_b32_sdwa v178, 1, v178 dst_sel:DWORD dst_unused:UNUSED_PAD src0_sel:DWORD src1_sel:WORD_1\n\t" \
  "v_smfmac_f32_16x16x64_bf16 v[4:7], v[212:215], v[144:151], v248\n\t" \
  "ds_read_b64_tr_b16 v[212:213], v250 offset:12456\n\t" \
  "ds_read_b64_tr_b16 v[214:215], v250 offset:13224\n\t" \
  "ds_read_u16_d16_hi v177, v177 offset:0\n\t" \
  "s_waitcnt lgkmcnt(8)\n\t" \
  "v_smfmac_f32_16x16x64_bf16 v[8:11], v[216:219], v[144:151], v248\n\t" \
  "ds_read_b64_tr_b16 v[216:217], v249 offset:12288\n\t" \
  "ds_read_b64_tr_b16 v[218:219], v249 offset:12544\n\t" \
  "v_lshlrev_b32_sdwa v179, 1, v179 dst_sel:DWORD dst_unused:UNUSED_PAD src0_sel:DWORD src1_sel:WORD_1\n\t" \
  "s_waitcnt lgkmcnt(8)\n\t" \
  "v_smfmac_f32_16x16x64_bf16 v[12:15], v[220:223], v[144:151], v248\n\t" \
  "ds_read_b64_tr_b16 v[220:221], v249 offset:14336\n\t" \
  "ds_read_b64_tr_b16 v[222:223], v249 offset:14592\n\t" \
  "ds_read_u16_d16_hi v178, v178 offset:0\n\t" \
  "v_lshlrev_b32_sdwa v180, 1, v180 dst_sel:DWORD dst_unused:UNUSED_PAD src0_sel:DWORD src1_sel:WORD_1\n\t" \
  "s_add_u32 %[rs], %[rs], 1\n\t" \
  "s_cmp_eq_u32 %[rs], 6\n\t" \
  "s_cselect_b32 %[rs], 0, %[rs]\n\t" \
  "s_cselect_b32 %[t0], -75920, %[ps]\n\t" \
  "v_add_u32 v252, %[t0], v252\n\t" \
  "s_waitcnt lgkmcnt(9)\n\t" \
  "v_smfmac_f32_16x16x64_bf16 v[16:19], v[208:211], v[144:151], v248\n\t" \
  "ds_read_b64_tr_b16 v[208:209], v249 offset:16384\n\t" \
  "ds_read_b64_tr_b16 v[210:211], v249 offset:16640\n\t" \
  "ds_read_u16_d16_hi v179, v179 offset:0\n\t" \
  "v_lshlrev_b32_sdwa v181, 1, v181 dst_sel:DWORD dst_unused:UNUSED_PAD src0_sel:DWORD src1_sel:WORD_1\n\t" \
  "s_waitcnt lgkmcnt(9)\n\t" \
  "v_smfmac_f32_16x16x64_bf16 v[20:23], v[212:215], v[144:151], v248\n\t" \
  "ds_read_b64_tr_b16 v[212:213], v249 offset:12800\n\t" \
  "ds_read_b64_tr_b16 v[214:215], v249 offset:13056\n\t" \
  "ds_read_u16_d16_hi v180, v180 offset:0\n\t" \
  "s_waitcnt lgkmcnt(9)\n\t" \
  "v_mfma_f32_16x16x32_bf16 v[144:147], v[216:219], v[96:99], 0\n\t" \
  "ds_read_b64_tr_b16 v[216:217], v249 offset:14848\n\t" \
  "ds_read_b64_tr_b16 v[218:219], v249 offset:15104\n\t" \
  "v_lshlrev_b32_sdwa v182, 1, v182 dst_sel:DWORD dst_unused:UNUSED_PAD src0_sel:DWORD src1_sel:WORD_1\n\t" \
  "s_waitcnt lgkmcnt(9)\n\t" \
  "v_mfma_f32_16x16x32_bf16 v[144:147], v[220:223], v[100:103], v[144:147]\n\t" \
  "ds_read_b64_tr_b16 v[220:221], v249 offset:16896\n\t" \
  "ds_read_b64_tr_b16 v[222:223], v249 offset:17152\n\t" \
  "ds_read_u16_d16_hi v181, v181 offset:0\n\t" \
  "v_lshlrev_b32_sdwa v183, 1, v183 dst_sel:DWORD dst_unused:UNUSED_PAD src0_sel:DWORD src1_sel:WORD_1\n\t" \
  "s_waitcnt lgkmcnt(9)\n\t" \
  "v_mfma_f32_16x16x32_bf16 v[144:147], v[208:211], v[104:107], v[144:147]\n\t" \
  "s_barrier\n\t" \
  "ds_read_b64_tr_b16 v[208:209], v250 offset:0\n\t" \
  "ds_read_b64_tr_b16 v[210:211], v250 offset:768\n\t" \
  "ds_read_u16_d16_hi v182, v182 offset:0\n\t" \
  "s_waitcnt lgkmcnt(9)\n\t" \
  "v_mfma_f32_16x16x32_bf16 v[148:151], v[212:215], v[96:99], 0\n\t" \
  "ds_read_b64_tr_b16 v[212:213], v250 offset:8\n\t" \
  "ds_read_b64_tr_b16 v[214:215], v250 offset:776\n\t" \
  "v_lshlrev_b32_sdwa v144, 1, v144 dst_sel:DWORD dst_unused:UNUSED_PAD src0_sel:DWORD src1_sel:WORD_1\n\t" \
  "s_waitcnt lgkmcnt(8)\n\t" \
  "v_mfma_f32_16x16x32_bf16 v[148:151], v[216:219], v[100:103], v[148:151]\n\t" \
  "ds_read_b64_tr_b16 v[216:217], v250 offset:64\n\t" \
  "ds_read_b64_tr_b16 v[218:219], v250 offset:832\n\t" \
  "ds_read_u16_d16_hi v183, v183 offset:0\n\t" \
  "s_waitcnt lgkmcnt(9)\n\t" \
  "v_mfma_f32_16x16x32_bf16 v[148:151], v[220:223], v[104:107], v[148:151]\n\t" \
  "ds_read_b64_tr_b16 v[220:221], v250 offset:72\n\t" \
  "ds_read_b64_tr_b16 v[222:223], v250 offset:840\n\t" \
  "v_lshlrev_b32_sdwa v145, 1, v145 dst_sel:DWORD dst_unused:UNUSED_PAD src0_sel:DWORD src1_sel:WORD_1\n\t" \
  "s_waitcnt lgkmcnt(2)\n\t" \
  "v_smfmac_f32_16x16x64_bf16 v[0:3], v[208:211], v[176:183], v248\n\t" \
  "ds_read_b64_tr_b16 v[208:209], v250 offset:128\n\t" \
  "ds_read_b64_tr_b16 v[210:211], v250 offset:896\n\t" \
  "ds_read_u16_d16_hi v144, v144 offset:0\n\t" \
  "v_lshlrev_b32_sdwa v146, 1, v146 dst_sel:DWORD dst_unused:UNUSED_PAD src0_sel:DWORD src1_sel:WORD_1\n\t" \
  "v_smfmac_f32_16x16x64_bf16 v[4:7], v[212:215], v[176:183], v248\n\t" \
  "ds_read_b64_tr_b16 v[212:213], v250 offset:136\n\t" \
  "ds_read_b64_tr_b16 v[214:215], v250 offset:904\n\t" \
  "ds_read_u16_d16_hi v145, v145 offset:0\n\t" \
  "ds_read_b128 v[108:111], v251 offset:0\n\t" \
  "v_smfmac_f32_16x16x64_bf16 v[8:11], v[216:219], v[176:183], v248\n\t" \
  "ds_read_b64_tr_b16 v[216:217], v249 offset:18432\n\t" \
  "ds_read_b64_tr_b16 v[218:219], v249 offset:18688\n\t" \
  "ds_read_b128 v[112:115], v251 offset:64\n\t" \
  "v_lshlrev_b32_sdwa v147, 1, v147 dst_sel:DWORD dst_unused:UNUSED_PAD src0_sel:DWORD src1_sel:WORD_1\n\t" \
  "s_waitcnt lgkmcnt(10)\n\t" \
  "v_smfmac_f32_16x16x64_bf16 v[12:15], v[220:223], v[176:183], v248\n\t" \
  "ds_read_b64_tr_b16 v[220:221], v249 offset:20480\n\t" \
  "ds_read_b64_tr_b16 v[222:223], v249 offset:20736\n\t" \
  "ds_read_u16_d16_hi v146, v146 offset:0\n\t" \
  "v_lshlrev_b32_sdwa v148, 1, v148 dst_sel:DWORD dst_unused:UNUSED_PAD src0_sel:DWORD src1_sel:WORD_1\n\t" \
  "s_waitcnt lgkmcnt(11)\n\t" \
  "v_smfmac_f32_16x16x64_bf16 v[16:19], v[208:211], v[176:183], v248\n\t" \
  "ds_read_b64_tr_b16 v[208:209], v249 offset:22528\n\t" \
  "ds_read_b64_tr_b16 v[210:211], v249 offset:22784\n\t" \
  "ds_read_b128 v[116:119], v251 offset:128\n\t" \
  "ds_read_u16_d16_hi v147, v147 offset:0\n\t" \
  "v_lshlrev_b32_sdwa v149, 1, v149 dst_sel:DWORD dst_unused:UNUSED_PAD src0_sel:DWORD src1_sel:WORD_1\n\t" \
  "s_waitcnt lgkmcnt(12)\n\t" \
  "v_smfmac_f32_16x16x64_bf16 v[20:23], v[212:215], v[176:183], v248\n\t" \
  "ds_read_b64_tr_b16 v[212:213], v249 offset:18944\n\t" \
  "ds_read_b64_tr_b16 v[214:215], v249 offset:19200\n\t" \
  "ds_read_u16_d16_hi v148, v148 offset:0\n\t" \
  "s_waitcnt lgkmcnt(11)\n\t" \
  "v_mfma_f32_16x16x32_bf16 v[176:179], v[216:219], v[96:99], 0\n\t" \
  "v_mfma_f32_16x16x32_bf16 v[184:187], v[216:219], v[108:111], 0\n\t" \
  "ds_read_b64_tr_b16 v[216:217], v249 offset:20992\n\t" \
  "ds_read_b64_tr_b16 v[218:219], v249 offset:21248\n\t" \
  "v_lshlrev_b32_sdwa v150, 1, v150 dst_sel:DWORD dst_unused:UNUSED_PAD src0_sel:DWORD src1_sel:WORD_1\n\t" \
  "s_waitcnt lgkmcnt(10)\n\t" \
  "v_mfma_f32_16x16x32_bf16 v[176:179], v[220:223], v[100:103], v[176:179]\n\t" \
  "ds_read_u16_d16_hi v149, v149 offset:0\n\t" \
  "v_mfma_f32_16x16x32_bf16 v[184:187], v[220:223], v[112:115], v[184:187]\n\t" \
  "ds_read_b64_tr_b16 v[220:221], v249 offset:23040\n\t" \
  "ds_read_b64_tr_b16 v[222:223], v249 offset:23296\n\t" \
  "v_lshlrev_b32_sdwa v151, 1, v151 dst_sel:DWORD dst_unused:UNUSED_PAD src0_sel:DWORD src1_sel:WORD_1\n\t" \
  "s_waitcnt lgkmcnt(10)\n\t" \
  "v_mfma_f32_16x16x32_bf16 v[176:179], v[208:211], v[104:107], v[176:179]\n\t" \
  "ds_read_u16_d16_hi v150, v150 offset:0\n\t" \
  "s_waitcnt lgkmcnt(10)\n\t" \
  "v_mfma_f32_16x16x32_bf16 v[184:187], v[208:211], v[116:119], v[184:187]\n\t" \
  "s_barrier\n\t" \
  "ds_read_b64_tr_b16 v[208:209], v250 offset:6160\n\t" \
  "ds_read_b64_tr_b16 v[210:211], v250 offset:6928\n\t" \
  "s_waitcnt lgkmcnt(9)\n\t" \
  "v_mfma_f32_16x16x32_bf16 v[180:183], v[212:215], v[96:99], 0\n\t" \
  "v_mfma_f32_16x16x32_bf16 v[188:191], v[212:215], v[108:111], 0\n\t" \
  "ds_read_b64_tr_b16 v[212:213], v250 offset:6168\n\t" \
  "ds_read_b64_tr_b16 v[214:215], v250 offset:6936\n\t" \
  "ds_read_u16_d16_hi v151, v151 offset:0\n\t" \
  "v_lshlrev_b32_sdwa v176, 1, v176 dst_sel:DWORD dst_unused:UNUSED_PAD src0_sel:DWORD src1_sel:WORD_1\n\t" \
  "s_waitcnt lgkmcnt(9)\n\t" \
  "v_mfma_f32_16x16x32_bf16 v[180:183], v[216:219], v[100:103], v[180:183]\n\t" \
  "v_mfma_f32_16x16x32_bf16 v[188:191], v[216:219], v[112:115], v[188:191]\n\t" \
  "ds_read_b64_tr_b16 v[216:217], v250 offset:6224\n\t" \
  "ds_read_b64_tr_b16 v[218:219], v250 offset:6992\n\t" \
  "v_lshlrev_b32_sdwa v177, 1, v177 dst_sel:DWORD dst_unused:UNUSED_PAD src0_sel:DWORD src1_sel:WORD_1\n\t" \
  "s_waitcnt lgkmcnt(8)\n\t" \
  "v_mfma_f32_16x16x32_bf16 v[180:183], v[220:223], v[104:107], v[180:183]\n\t" \
  "ds_read_u16_d16_hi v176, v176 offset:0\n\t" \
  "v_lshlrev_b32_sdwa v178, 1, v178 dst_sel:DWORD dst_unused:UNUSED_PAD src0_sel:DWORD src1_sel:WORD_1\n\t" \
  "v_mfma_f32_16x16x32_bf16 v[188:191], v[220:223], v[116:119], v[188:191]\n\t" \
  "ds_read_b64_tr_b16 v[220:221], v250 offset:6232\n\t" \
  "ds_read_b64_tr_b16 v[222:223], v250 offset:7000\n\t" \
  "ds_read_u16_d16_hi v177, v177 offset:0\n\t" \
  "v_lshlrev_b32_sdwa v179, 1, v179 dst_sel:DWORD dst_unused:UNUSED_PAD src0_sel:DWORD src1_sel:WORD_1\n\t" \
  "s_waitcnt lgkmcnt(6)\n\t" \
  "v_smfmac_f32_16x16x64_bf16 v[0:3], v[208:211], v[144:151], v248\n\t" \
  "ds_read_b64_tr_b16 v[208:209], v250 offset:6288\n\t" \
  "ds_read_b64_tr_b16 v[210:211], v250 offset:7056\n\t" \
  "ds_read_u16_d16_hi v178, v178 offset:0\n\t" \
  "v_lshlrev_b32_sdwa v184, 1, v184 dst_sel:DWORD dst_unused:UNUSED_PAD src0_sel:DWORD src1_sel:WORD_1\n\t" \
  "v_smfmac_f32_16x16x64_bf16 v[4:7], v[212:215], v[144:151], v248\n\t" \
  "ds_read_b64_tr_b16 v[212:213], v250 offset:6296\n\t" \
  "ds_read_b64_tr_b16 v[214:215], v250 offset:7064\n\t" \
  "ds_read_u16_d16_hi v179, v179 offset:0\n\t" \
  "v_lshlrev_b32_sdwa v185, 1, v185 dst_sel:DWORD dst_unused:UNUSED_PAD src0_sel:DWORD src1_sel:WORD_1\n\t" \
  "s_waitcnt lgkmcnt(10)\n\t" \
  "v_smfmac_f32_16x16x64_bf16 v[8:11], v[216:219], v[144:151], v248\n\t" \
  "ds_read_b64_tr_b16 v[216:217], v249 offset:0\n\t" \
  "ds_read_b64_tr_b16 v[218:219], v249 offset:256\n\t" \
  "ds_read_u16_d16_hi v184, v184 offset:0\n\t" \
  "v_lshlrev_b32_sdwa v186, 1, v186 dst_sel:DWORD dst_unused:UNUSED_PAD src0_sel:DWORD src1_sel:WORD_1\n\t" \
  "v_lshlrev_b32_sdwa v187, 1, v187 dst_sel:DWORD dst_unused:UNUSED_PAD src0_sel:DWORD src1_sel:WORD_1\n\t" \
  "s_waitcnt lgkmcnt(10)\n\t" \
  "v_smfmac_f32_16x16x64_bf16 v[12:15], v[220:223], v[144:151], v248\n\t" \
  "ds_read_b64_tr_b16 v[220:221], v249 offset:2048\n\t" \
  "ds_read_b64_tr_b16 v[222:223], v249 offset:2304\n\t" \
  "ds_read_u16_d16_hi v185, v185 offset:0\n\t" \
  "ds_read_u16_d16_hi v186, v186 offset:0\n\t" \
  "v_lshlrev_b32_sdwa v180, 1, v180 dst_sel:DWORD dst_unused:UNUSED_PAD src0_sel:DWORD src1_sel:WORD_1\n\t" \
  "s_waitcnt lgkmcnt(11)\n\t" \
  "v_smfmac_f32_16x16x64_bf16 v[16:19], v[208:211], v[144:151], v248\n\t" \
  "ds_read_b64_tr_b16 v[208:209], v249 offset:4096\n\t" \
  "ds_read_b64_tr_b16 v[210:211], v249 offset:4352\n\t" \
  "ds_read_u16_d16_hi v187, v187 offset:0\n\t" \
  "ds_read_b64 v[230:231], v252 offset:0\n\t" \
  "v_lshlrev_b32_sdwa v181, 1, v181 dst_sel:DWORD dst_unused:UNUSED_PAD src0_sel:DWORD src1_sel:WORD_1\n\t" \
  "v_lshlrev_b32_sdwa v182, 1, v182 dst_sel:DWORD dst_unused:UNUSED_PAD src0_sel:DWORD src1_sel:WORD_1\n\t" \
  "s_waitcnt lgkmcnt(12)\n\t" \
  "v_smfmac_f32_16x16x64_bf16 v[20:23], v[212:215], v[144:151], v248\n\t" \
  "ds_read_b64_tr_b16 v[212:213], v249 offset:512\n\t" \
  "ds_read_b64_tr_b16 v[214:215], v249 offset:768\n\t" \
  "ds_read_u16_d16_hi v180, v180 offset:0\n\t" \
  "ds_read_b64 v[232:233], v252 offset:8\n\t" \
  "ds_read_u16_d16_hi v181, v181 offset:0\n\t" \
  "s_waitcnt lgkmcnt(5)\n\t" \
  "v_mfma_f32_16x16x16_bf16 v[24:27], v[246:247], v[230:231], 0\n\t" \
  "v_lshlrev_b32_sdwa v183, 1, v183 dst_sel:DWORD dst_unused:UNUSED_PAD src0_sel:DWORD src1_sel:WORD_1\n\t" \
  "v_mfma_f32_16x16x32_bf16 v[144:147], v[216:219], v[96:99], 0\n\t" \
  "ds_read_u16_d16_hi v182, v182 offset:0\n\t" \
  "ds_read_b64 v[234:235], v252 offset:64\n\t" \
  "s_waitcnt lgkmcnt(3)\n\t" \
  "v_mfma_f32_16x16x16_bf16 v[28:31], v[246:247], v[232:233], 0\n\t" \
  "v_mfma_f32_16x16x32_bf16 v[152:155], v[216:219], v[108:111], 0\n\t" \
  "ds_read_b64_tr_b16 v[216:217], v249 offset:2560\n\t" \
  "ds_read_b64_tr_b16 v[218:219], v249 offset:2816\n\t" \
  "ds_read_b64 v[236:237], v252 offset:72\n\t" \
  "v_lshlrev_b32_sdwa v188, 1, v188 dst_sel:DWORD dst_unused:UNUSED_PAD src0_sel:DWORD src1_sel:WORD_1\n\t" \
  "v_lshlrev_b32_sdwa v189, 1, v189 dst_sel:DWORD dst_unused:UNUSED_PAD src0_sel:DWORD src1_sel:WORD_1\n\t" \
  "v_mfma_f32_16x16x32_bf16 v[144:147], v[220:223], v[100:103], v[144:147]\n\t" \
  "ds_read_u16_d16_hi v183, v183 offset:0\n\t" \
  "ds_read_u16_d16_hi v188, v188 offset:0\n\t" \
  "v_mfma_f32_16x16x32_bf16 v[152:155], v[220:223], v[112:115], v[152:155]\n\t" \
  "ds_read_b64_tr_b16 v[220:221], v249 offset:4608\n\t" \
  "ds_read_b64_tr_b16 v[222:223], v249 offset:4864\n\t" \
  "s_waitcnt lgkmcnt(7)\n\t" \
  "v_mfma_f32_16x16x16_bf16 v[32:35], v[246:247], v[234:235], 0\n\t" \
  "v_lshlrev_b32_sdwa v190, 1, v190 dst_sel:DWORD dst_unused:UNUSED_PAD src0_sel:DWORD src1_sel:WORD_1\n\t" \
  "v_mfma_f32_16x16x32_bf16 v[144:147], v[208:211], v[104:107], v[144:147]\n\t" \
  "ds_read_u16_d16_hi v189, v189 offset:0\n\t" \
  "ds_read_b64 v[238:239], v252 offset:128\n\t" \
  "s_waitcnt lgkmcnt(6)\n\t" \
  "v_mfma_f32_16x16x16_bf16 v[36:39], v[246:247], v[236:237], 0\n\t" \
  "v_mfma_f32_16x16x32_bf16 v[152:155], v[208:211], v[116:119], v[152:155]\n\t" \
  "s_barrier\n\t" \
  "ds_read_b64_tr_b16 v[208:209], v250 offset:12320\n\t" \
  "ds_read_b64_tr_b16 v[210:211], v250 offset:13088\n\t" \
  "ds_read_b64 v[240:241], v252 offset:136\n\t" \
  "v_lshlrev_b32_sdwa v191, 1, v191 dst_sel:DWORD dst_unused:UNUSED_PAD src0_sel:DWORD src1_sel:WORD_1\n\t" \
  "v_mfma_f32_16x16x32_bf16 v[148:151], v[212:215], v[96:99], 0\n\t" \
  "ds_read_u16_d16_hi v190, v190 offset:0\n\t" \
  "v_mfma_f32_16x16x32_bf16 v[156:159], v[212:215], v[108:111], 0\n\t" \
  "ds_read_b64_tr_b16 v[212:213], v250 offset:12328\n\t" \
  "ds_read_b64_tr_b16 v[214:215], v250 offset:13096\n\t" \
  "ds_read_u16_d16_hi v191, v191 offset:0\n\t" \
  "s_waitcnt lgkmcnt(7)\n\t" \
  "v_mfma_f32_16x16x16_bf16 v[40:43], v[246:247], v[238:239], 0\n\t" \
  "v_lshlrev_b32_sdwa v144, 1, v144 dst_sel:DWORD dst_unused:UNUSED_PAD src0_sel:DWORD src1_sel:WORD_1\n\t" \
  "v_mfma_f32_16x16x32_bf16 v[148:151], v[216:219], v[100:103], v[148:151]\n\t" \
  "s_waitcnt lgkmcnt(4)\n\t" \
  "v_mfma_f32_16x16x16_bf16 v[44:47], v[246:247], v[240:241], 0\n\t" \
  "v_mfma_f32_16x16x32_bf16 v[156:159], v[216:219], v[112:115], v[156:159]\n\t" \
  "ds_read_b64_tr_b16 v[216:217], v250 offset:12384\n\t" \
  "ds_read_b64_tr_b16 v[218:219], v250 offset:13152\n\t" \
  "v_lshlrev_b32_sdwa v145, 1, v145 dst_sel:DWORD dst_unused:UNUSED_PAD src0_sel:DWORD src1_sel:WORD_1\n\t" \
  "v_mfma_f32_16x16x32_bf16 v[148:151], v[220:223], v[104:107], v[148:151]\n\t" \
  "ds_read_u16_d16_hi v144, v144 offset:0\n\t" \
  "v_lshlrev_b32_sdwa v146, 1, v146 dst_sel:DWORD dst_unused:UNUSED_PAD src0_sel:DWORD src1_sel:WORD_1\n\t" \
  "v_mfma_f32_16x16x32_bf16 v[156:159], v[220:223], v[116:119], v[156:159]\n\t" \
  "ds_read_b64_tr_b16 v[220:221], v250 offset:12392\n\t" \
  "ds_read_b64_tr_b16 v[222:223], v250 offset:13160\n\t" \
  "ds_read_u16_d16_hi v145, v145 offset:0\n\t" \
  "v_lshlrev_b32_sdwa v147, 1, v147 dst_sel:DWORD dst_unused:UNUSED_PAD src0_sel:DWORD src1_sel:WORD_1\n\t" \
  "v_smfmac_f32_16x16x64_bf16 v[0:3], v[208:211], v[176:183], v248\n\t" \
  "ds_read_u16_d16_hi v146, v146 offset:0\n\t" \
  "s_waitcnt lgkmcnt(7)\n\t" \
  "v_smfmac_f32_16x16x64_bf16 v[24:27], v[208:211], v[184:191], v248\n\t" \
  "ds_read_b64_tr_b16 v[208:209], v250 offset:12448\n\t" \
  "ds_read_b64_tr_b16 v[210:211], v250 offset:13216\n\t" \
  "v_lshlrev_b32_sdwa v152, 1, v152 dst_sel:DWORD dst_unused:UNUSED_PAD src0_sel:DWORD src1_sel:WORD_1\n\t" \
  "v_smfmac_f32_16x16x64_bf16 v[4:7], v[212:215], v[176:183], v248\n\t" \
  "ds_read_u16_d16_hi v147, v147 offset:0\n\t" \
  "v_smfmac_f32_16x16x64_bf16 v[28:31], v[212:215], v[184:191], v248\n\t" \
  "ds_read_b64_tr_b16 v[212:213], v250 offset:12456\n\t" \
  "ds_read_b64_tr_b16 v[214:215], v250 offset:13224\n\t" \
  "v_lshlrev_b32_sdwa v153, 1, v153 dst_sel:DWORD dst_unused:UNUSED_PAD src0_sel:DWORD src1_sel:WORD_1\n\t" \
  "s_waitcnt lgkmcnt(10)\n\t" \
  "v_smfmac_f32_16x16x64_bf16 v[8:11], v[216:219], v[176:183], v248\n\t" \
  "ds_read_u16_d16_hi v152, v152 offset:0\n\t" \
  "v_lshlrev_b32_sdwa v154, 1, v154 dst_sel:DWORD dst_unused:UNUSED_PAD src0_sel:DWORD src1_sel:WORD_1\n\t" \
  "v_smfmac_f32_16x16x64_bf16 v[32:35], v[216:219], v[184:191], v248\n\t" \
  "ds_read_b64_tr_b16 v[216:217], v249 offset:6144\n\t" \
  "ds_read_b64_tr_b16 v[218:219], v249 offset:6400\n\t" \
  "ds_read_u16_d16_hi v153, v153 offset:0\n\t" \
  "v_lshlrev_b32_sdwa v155, 1, v155 dst_sel:DWORD dst_unused:UNUSED_PAD src0_sel:DWORD src1_sel:WORD_1\n\t" \
  "s_waitcnt lgkmcnt(11)\n\t" \
  "v_smfmac_f32_16x16x64_bf16 v[12:15], v[220:223], v[176:183], v248\n\t" \
  "ds_read_u16_d16_hi v154, v154 offset:0\n\t" \
  "v_smfmac_f32_16x16x64_bf16 v[36:39], v[220:223], v[184:191], v248\n\t" \
  "ds_read_b64_tr_b16 v[220:221], v249 offset:8192\n\t" \
  "ds_read_b64_tr_b16 v[222:223], v249 offset:8448\n\t" \
  "v_lshlrev_b32_sdwa v148, 1, v148 dst_sel:DWORD dst_unused:UNUSED_PAD src0_sel:DWORD src1_sel:WORD_1\n\t" \
  "s_add_u32 %[rs], %[rs], 1\n\t" \
  "s_cmp_eq_u32 %[rs], 6\n\t" \
  "s_cselect_b32 %[rs], 0, %[rs]\n\t" \
  "s_cselect_b32 %[t0], -75920, %[ps]\n\t" \
  "v_add_u32 v252, %[t0], v252\n\t" \
  "s_waitcnt lgkmcnt(10)\n\t" \
  "v_smfmac_f32_16x16x64_bf16 v[16:19], v[208:211], v[176:183], v248\n\t" \
  "ds_read_u16_d16_hi v155, v155 offset:0\n\t" \
  "v_lshlrev_b32_sdwa v149, 1, v149 dst_sel:DWORD dst_unused:UNUSED_PAD src0_sel:DWORD src1_sel:WORD_1\n\t" \
  "v_smfmac_f32_16x16x64_bf16 v[40:43], v[208:211], v[184:191], v248\n\t" \
  "ds_read_b64_tr_b16 v[208:209], v249 offset:10240\n\t" \
  "ds_read_b64_tr_b16 v[210:211], v249 offset:10496\n\t" \
  "ds_read_u16_d16_hi v148, v148 offset:0\n\t" \
  "v_lshlrev_b32_sdwa v150, 1, v150 dst_sel:DWORD dst_unused:UNUSED_PAD src0_sel:DWORD src1_sel:WORD_1\n\t" \
  "s_waitcnt lgkmcnt(11)\n\t" \
  "v_smfmac_f32_16x16x64_bf16 v[20:23], v[212:215], v[176:183], v248\n\t" \
  "ds_read_u16_d16_hi v149, v149 offset:0\n\t" \
  "v_smfmac_f32_16x16x64_bf16 v[44:47], v[212:215], v[184:191], v248\n\t" \
  "ds_read_b64_tr_b16 v[212:213], v249 offset:6656\n\t" \
  "ds_read_b64_tr_b16 v[214:215], v249 offset:6912\n\t" \
  "v_lshlrev_b32_sdwa v151, 1, v151 dst_sel:DWORD dst_unused:UNUSED_PAD src0_sel:DWORD src1_sel:WORD_1\n\t" \
  "s_waitcnt lgkmcnt(11)\n\t" \
  "v_mfma_f32_16x16x32_bf16 v[176:179], v[216:219], v[96:99], 0\n\t" \
  "ds_read_u16_d16_hi v150, v150 offset:0\n\t" \
  "v_mfma_f32_16x16x32_bf16 v[184:187], v[216:219], v[108:111], 0\n\t" \
  "ds_read_b64_tr_b16 v[216:217], v249 offset:8704\n\t" \
  "ds_read_b64_tr_b16 v[218:219], v249 offset:8960\n\t" \
  "v_lshlrev_b32_sdwa v156, 1, v156 dst_sel:DWORD dst_unused:UNUSED_PAD src0_sel:DWORD src1_sel:WORD_1\n\t" \
  "v_lshlrev_b32_sdwa v157, 1, v157 dst_sel:DWORD dst_unused:UNUSED_PAD src0_sel:DWORD src1_sel:WORD_1\n\t" \
  "s_waitcnt lgkmcnt(10)\n\t" \
  "v_mfma_f32_16x16x32_bf16 v[176:179], v[220:223], v[100:103], v[176:179]\n\t" \
  "ds_read_u16_d16_hi v151, v151 offset:0\n\t" \
  "ds_read_u16_d16_hi v156, v156 offset:0\n\t" \
  "v_mfma_f32_16x16x32_bf16 v[184:187], v[220:223], v[112:115], v[184:187]\n\t" \
  "ds_read_b64_tr_b16 v[220:221], v249 offset:10752\n\t" \
  "ds_read_b64_tr_b16 v[222:223], v249 offset:11008\n\t" \
  "v_lshlrev_b32_sdwa v158, 1, v158 dst_sel:DWORD dst_unused:UNUSED_PAD src0_sel:DWORD src1_sel:WORD_1\n\t" \
  "s_waitcnt lgkmcnt(11)\n\t" \
  "v_mfma_f32_16x16x32_bf16 v[176:179], v[208:211], v[104:107], v[176:179]\n\t" \
  "ds_read_u16_d16_hi v157, v157 offset:0\n\t" \
  "v_mfma_f32_16x16x32_bf16 v[184:187], v[208:211], v[116:119], v[184:187]\n\t" \
  "s_barrier\n\t" \
  "ds_read_b64_tr_b16 v[208:209], v250 offset:0\n\t" \
  "ds_read_b64_tr_b16 v[210:211], v250 offset:768\n\t" \
  "v_lshlrev_b32_sdwa v159, 1, v159 dst_sel:DWORD dst_unused:UNUSED_PAD src0_sel:DWORD src1_sel:WORD_1\n\t" \
  "s_waitcnt lgkmcnt(10)\n\t" \
  "v_mfma_f32_16x16x32_bf16 v[180:183], v[212:215], v[96:99], 0\n\t" \
  "ds_read_u16_d16_hi v158, v158 offset:0\n\t" \
  "v_mfma_f32_16x16x32_bf16 v[188:191], v[212:215], v[108:111], 0\n\t" \
  "ds_read_b64_tr_b16 v[212:213], v250 offset:8\n\t" \
  "ds_read_b64_tr_b16 v[214:215], v250 offset:776\n\t" \
  "ds_read_u16_d16_hi v159, v159 offset:0\n\t" \
  "v_lshlrev_b32_sdwa v176, 1, v176 dst_sel:DWORD dst_unused:UNUSED_PAD src0_sel:DWORD src1_sel:WORD_1\n\t" \
  "s_waitcnt lgkmcnt(11)\n\t" \
  "v_mfma_f32_16x16x32_bf16 v[180:183], v[216:219], v[100:103], v[180:183]\n\t" \
  "v_mfma_f32_16x16x32_bf16 v[188:191], v[216:219], v[112:115], v[188:191]\n\t" \
  "ds_read_b64_tr_b16 v[216:217], v250 offset:64\n\t" \
  "ds_read_b64_tr_b16 v[218:219], v250 offset:832\n\t" \
  "v_lshlrev_b32_sdwa v177, 1, v177 dst_sel:DWORD dst_unused:UNUSED_PAD src0_sel:DWORD src1_sel:WORD_1\n\t" \
  "s_waitcnt lgkmcnt(9)\n\t" \
  "v_mfma_f32_16x16x32_bf16 v[180:183], v[220:223], v[104:107], v[180:183]\n\t" \
  "ds_read_u16_d16_hi v176, v176 offset:0\n\t" \
  "v_lshlrev_b32_sdwa v178, 1, v178 dst_sel:DWORD dst_unused:UNUSED_PAD src0_sel:DWORD src1_sel:WORD_1\n\t" \
  "v_mfma_f32_16x16x32_bf16 v[188:191], v[220:223], v[116:119], v[188:191]\n\t" \
  "ds_read_b64_tr_b16 v[220:221], v250 offset:72\n\t" \
  "ds_read_b64_tr_b16 v[222:223], v250 offset:840\n\t" \
  "ds_read_u16_d16_hi v177, v177 offset:0\n\t" \
  "v_lshlrev_b32_sdwa v179, 1, v179 dst_sel:DWORD dst_unused:UNUSED_PAD src0_sel:DWORD src1_sel:WORD_1\n\t" \
  "s_waitcnt lgkmcnt(10)\n\t" \
  "v_smfmac_f32_16x16x64_bf16 v[0:3], v[208:211], v[144:151], v248\n\t" \
  "ds_read_u16_d16_hi v178, v178 offset:0\n\t" \
  "s_waitcnt lgkmcnt(7)\n\t" \
  "v_smfmac_f32_16x16x64_bf16 v[24:27], v[208:211], v[152:159], v248\n\t" \
  "ds_read_b64_tr_b16 v[208:209], v250 offset:128\n\t" \
  "ds_read_b64_tr_b16 v[210:211], v250 offset:896\n\t" \
  "v_lshlrev_b32_sdwa v184, 1, v184 dst_sel:DWORD dst_unused:UNUSED_PAD src0_sel:DWORD src1_sel:WORD_1\n\t" \
  "v_smfmac_f32_16x16x64_bf16 v[4:7], v[212:215], v[144:151], v248\n\t" \
  "ds_read_u16_d16_hi v179, v179 offset:0\n\t" \
  "ds_read_b128 v[120:123], v251 offset:0\n\t" \
  "v_smfmac_f32_16x16x64_bf16 v[28:31], v[212:215], v[152:159], v248\n\t" \
  "ds_read_b64_tr_b16 v[212:213], v250 offset:136\n\t" \
  "ds_read_b64_tr_b16 v[214:215], v250 offset:904\n\t" \
  "v_lshlrev_b32_sdwa v185, 1, v185 dst_sel:DWORD dst_unused:UNUSED_PAD src0_sel:DWORD src1_sel:WORD_1\n\t" \
  "s_waitcnt lgkmcnt(11)\n\t" \
  "v_smfmac_f32_16x16x64_bf16 v[8:11], v[216:219], v[144:151], v248\n\t" \
  "ds_read_u16_d16_hi v184, v184 offset:0\n\t" \
  "ds_read_b128 v[124:127], v251 offset:64\n\t" \
  "v_lshlrev_b32_sdwa v186, 1, v186 dst_sel:DWORD dst_unused:UNUSED_PAD src0_sel:DWORD src1_sel:WORD_1\n\t" \
  "v_smfmac_f32_16x16x64_bf16 v[32:35], v[216:219], v[152:159], v248\n\t" \
  "ds_read_b64_tr_b16 v[216:217], v249 offset:12288\n\t" \
  "ds_read_b64_tr_b16 v[218:219], v249 offset:12544\n\t" \
  "ds_read_u16_d16_hi v185, v185 offset:0\n\t" \
  "v_lshlrev_b32_sdwa v187, 1, v187 dst_sel:DWORD dst_unused:UNUSED_PAD src0_sel:DWORD src1_sel:WORD_1\n\t" \
  "s_waitcnt lgkmcnt(13)\n\t" \
  "v_smfmac_f32_16x16x64_bf16 v[12:15], v[220:223], v[144:151], v248\n\t" \
  "ds_read_u16_d16_hi v186, v186 offset:0\n\t" \
  "v_smfmac_f32_16x16x64_bf16 v[36:39], v[220:223], v[152:159], v248\n\t" \
  "ds_read_b64_tr_b16 v[220:221], v249 offset:14336\n\t" \
  "ds_read_b64_tr_b16 v[222:223], v249 offset:14592\n\t" \
  "ds_read_b128 v[128:131], v251 offset:128\n\t" \
  "v_lshlrev_b32_sdwa v180, 1, v180 dst_sel:DWORD dst_unused:UNUSED_PAD src0_sel:DWORD src1_sel:WORD_1\n\t" \
  "s_waitcnt lgkmcnt(13)\n\t" \
  "v_smfmac_f32_16x16x64_bf16 v[16:19], v[208:211], v[144:151], v248\n\t" \
  "ds_read_u16_d16_hi v187, v187 offset:0\n\t" \
  "v_lshlrev_b32_sdwa v181, 1, v181 dst_sel:DWORD dst_unused:UNUSED_PAD src0_sel:DWORD src1_sel:WORD_1\n\t" \
  "v_smfmac_f32_16x16x64_bf16 v[40:43], v[208:211], v[152:159], v248\n\t" \
  "ds_read_b64_tr_b16 v[208:209], v249 offset:16384\n\t" \
  "ds_read_b64_tr_b16 v[210:211], v249 offset:16640\n\t" \
  "ds_read_u16_d16_hi v180, v180 offset:0\n\t" \
  "v_lshlrev_b32_sdwa v182, 1, v182 dst_sel:DWORD dst_unused:UNUSED_PAD src0_sel:DWORD src1_sel:WORD_1\n\t" \
  "s_waitcnt lgkmcnt(13)\n\t" \
  "v_smfmac_f32_16x16x64_bf16 v[20:23], v[212:215], v[144:151], v248\n\t" \
  "ds_read_u16_d16_hi v181, v181 offset:0\n\t" \
  "v_smfmac_f32_16x16x64_bf16 v[44:47], v[212:215], v[152:159], v248\n\t" \
  "ds_read_b64_tr_b16 v[212:213], v249 offset:12800\n\t" \
  "ds_read_b64_tr_b16 v[214:215], v249 offset:13056\n\t" \
  "v_lshlrev_b32_sdwa v183, 1, v183 dst_sel:DWORD dst_unused:UNUSED_PAD src0_sel:DWORD src1_sel:WORD_1\n\t" \
  "s_waitcnt lgkmcnt(12)\n\t" \
  "v_mfma_f32_16x16x32_bf16 v[144:147], v[216:219], v[96:99], 0\n\t" \
  "ds_read_u16_d16_hi v182, v182 offset:0\n\t" \
  "v_mfma_f32_16x16x32_bf16 v[152:155], v[216:219], v[108:111], 0\n\t" \
  "v_lshlrev_b32_sdwa v188, 1, v188 dst_sel:DWORD dst_unused:UNUSED_PAD src0_sel:DWORD src1_sel:WORD_1\n\t" \
  "v_mfma_f32_16x16x32_bf16 v[160:163], v[216:219], v[120:123], 0\n\t" \
  "s_waitcnt lgkmcnt(9)\n\t" \
  "ds_read_b64_tr_b16 v[216:217], v249 offset:14848\n\t" \
  "v_lshlrev_b32_sdwa v189, 1, v189 dst_sel:DWORD dst_unused:UNUSED_PAD src0_sel:DWORD src1_sel:WORD_1\n\t" \
  "v_mfma_f32_16x16x32_bf16 v[144:147], v[220:223], v[100:103], v[144:147]\n\t" \
  "ds_read_u16_d16_hi v183, v183 offset:0\n\t" \
  "v_mfma_f32_16x16x32_bf16 v[152:155], v[220:223], v[112:115], v[152:155]\n\t" \
  "ds_read_b64_tr_b16 v[218:219], v249 offset:15104\n\t" \
  "v_mfma_f32_16x16x32_bf16 v[160:163], v[220:223], v[124:127], v[160:163]\n\t" \
  "s_waitcnt lgkmcnt(8)\n\t" \
  "ds_read_u16_d16_hi v188, v188 offset:0\n\t" \
  "v_lshlrev_b32_sdwa v190, 1, v190 dst_sel:DWORD dst_unused:UNUSED_PAD src0_sel:DWORD src1_sel:WORD_1\n\t" \
  "v_mfma_f32_16x16x32_bf16 v[144:147], v[208:211], v[104:107], v[144:147]\n\t" \
  "ds_read_b64_tr_b16 v[220:221], v249 offset:16896\n\t" \
  "v_mfma_f32_16x16x32_bf16 v[152:155], v[208:211], v[116:119], v[152:155]\n\t" \
  "ds_read_u16_d16_hi v189, v189 offset:0\n\t" \
  "v_lshlrev_b32_sdwa v191, 1, v191 dst_sel:DWORD dst_unused:UNUSED_PAD src0_sel:DWORD src1_sel:WORD_1\n\t" \
  "v_mfma_f32_16x16x32_bf16 v[160:163], v[208:211], v[128:131], v[160:163]\n\t" \
  "s_waitcnt lgkmcnt(7)\n\t" \
  "ds_read_b64_tr_b16 v[222:223], v249 offset:17152\n\t" \
  "s_barrier\n\t" \
  "ds_read_u16_d16_hi v190, v190 offset:0\n\t" \
  "v_mfma_f32_16x16x32_bf16 v[148:151], v[212:215], v[96:99], 0\n\t" \
  "ds_read_b64_tr_b16 v[208:209], v250 offset:6160\n\t" \
  "v_mfma_f32_16x16x32_bf16 v[156:159], v[212:215], v[108:111], 0\n\t" \
  "ds_read_u16_d16_hi v191, v191 offset:0\n\t" \
  "v_mfma_f32_16x16x32_bf16 v[164:167], v[212:215], v[120:123], 0\n\t" \
  "s_waitcnt lgkmcnt(7)\n\t" \
  "ds_read_b64_tr_b16 v[210:211], v250 offset:6928\n\t" \
  "v_lshlrev_b32_sdwa v144, 1, v144 dst_sel:DWORD dst_unused:UNUSED_PAD src0_sel:DWORD src1_sel:WORD_1\n\t" \
  "v_mfma_f32_16x16x32_bf16 v[148:151], v[216:219], v[100:103], v[148:151]\n\t" \
  "ds_read_b64_tr_b16 v[212:213], v250 offset:6168\n\t" \
  "v_lshlrev_b32_sdwa v145, 1, v145 dst_sel:DWORD dst_unused:UNUSED_PAD src0_sel:DWORD src1_sel:WORD_1\n\t" \
  "v_mfma_f32_16x16x32_bf16 v[156:159], v[216:219], v[112:115], v[156:159]\n\t" \
  "ds_read_u16_d16_hi v144, v144 offset:0\n\t" \
  "v_mfma_f32_16x16x32_bf16 v[164:167], v[216:219], v[124:127], v[164:167]\n\t" \
  "s_waitcnt lgkmcnt(6)\n\t" \
  "ds_read_b64_tr_b16 v[214:215], v250 offset:6936\n\t" \
  "v_lshlrev_b32_sdwa v146, 1, v146 dst_sel:DWORD dst_unused:UNUSED_PAD src0_sel:DWORD src1_sel:WORD_1\n\t" \
  "v_mfma_f32_16x16x32_bf16 v[148:151], v[220:223], v[104:107], v[148:151]\n\t" \
  "ds_read_u16_d16_hi v145, v145 offset:0\n\t" \
  "v_lshlrev_b32_sdwa v147, 1, v147 dst_sel:DWORD dst_unused:UNUSED_PAD src0_sel:DWORD src1_sel:WORD_1\n\t" \
  "v_mfma_f32_16x16x32_bf16 v[156:159], v[220:223], v[116:119], v[156:159]\n\t" \
  "ds_read_b64_tr_b16 v[216:217], v250 offset:6224\n\t" \
  "v_mfma_f32_16x16x32_bf16 v[164:167], v[220:223], v[128:131], v[164:167]\n\t" \
  "s_waitcnt lgkmcnt(5)\n\t" \
  "ds_read_b64_tr_b16 v[218:219], v250 offset:6992\n\t" \
  "v_lshlrev_b32_sdwa v152, 1, v152 dst_sel:DWORD dst_unused:UNUSED_PAD src0_sel:DWORD src1_sel:WORD_1\n\t" \
  "v_smfmac_f32_16x16x64_bf16 v[0:3], v[208:211], v[176:183], v248\n\t" \
  "ds_read_b64_tr_b16 v[220:221], v250 offset:6232\n\t" \
  "ds_read_b64_tr_b16 v[222:223], v250 offset:7000\n\t" \
  "ds_read_u16_d16_hi v146, v146 offset:0\n\t" \
  "ds_read_u16_d16_hi v147, v147 offset:0\n\t" \
  "v_lshlrev_b32_sdwa v153, 1, v153 dst_sel:DWORD dst_unused:UNUSED_PAD src0_sel:DWORD src1_sel:WORD_1\n\t" \
  "v_smfmac_f32_16x16x64_bf16 v[24:27], v[208:211], v[184:191], v248\n\t" \
  "ds_read_b64_tr_b16 v[208:209], v250 offset:6288\n\t" \
  "ds_read_b64_tr_b16 v[210:211], v250 offset:7056\n\t" \
  "ds_read_u16_d16_hi v152, v152 offset:0\n\t" \
  "v_lshlrev_b32_sdwa v154, 1, v154 dst_sel:DWORD dst_unused:UNUSED_PAD src0_sel:DWORD src1_sel:WORD_1\n\t" \
  "s_waitcnt lgkmcnt(10)\n\t" \
  "v_smfmac_f32_16x16x64_bf16 v[4:7], v[212:215], v[176:183], v248\n\t" \
  "ds_read_u16_d16_hi v153, v153 offset:0\n\t" \
  "v_lshlrev_b32_sdwa v155, 1, v155 dst_sel:DWORD dst_unused:UNUSED_PAD src0_sel:DWORD src1_sel:WORD_1\n\t" \
  "v_smfmac_f32_16x16x64_bf16 v[28:31], v[212:215], v[184:191], v248\n\t" \
  "ds_read_b64_tr_b16 v[212:213], v250 offset:6296\n\t" \
  "ds_read_b64_tr_b16 v[214:215], v250 offset:7064\n\t" \
  "ds_read_u16_d16_hi v154, v154 offset:0\n\t" \
  "v_lshlrev_b32_sdwa v160, 1, v160 dst_sel:DWORD dst_unused:UNUSED_PAD src0_sel:DWORD src1_sel:WORD_1\n\t" \
  "s_waitcnt lgkmcnt(11)\n\t" \
  "v_smfmac_f32_16x16x64_bf16 v[8:11], v[216:219], v[176:183], v248\n\t" \
  "ds_read_u16_d16_hi v155, v155 offset:0\n\t" \
  "v_lshlrev_b32_sdwa v161, 1, v161 dst_sel:DWORD dst_unused:UNUSED_PAD src0_sel:DWORD src1_sel:WORD_1\n\t" \
  "v_smfmac_f32_16x16x64_bf16 v[32:35], v[216:219], v[184:191], v248\n\t" \
  "ds_read_b64_tr_b16 v[216:217], v249 offset:18432\n\t" \
  "ds_read_b64_tr_b16 v[218:219], v249 offset:18688\n\t" \
  "ds_read_u16_d16_hi v160, v160 offset:0\n\t" \
  "v_lshlrev_b32_sdwa v162, 1, v162 dst_sel:DWORD dst_unused:UNUSED_PAD src0_sel:DWORD src1_sel:WORD_1\n\t" \
  "s_waitcnt lgkmcnt(13)\n\t" \
  "v_smfmac_f32_16x16x64_bf16 v[12:15], v[220:223], v[176:183], v248\n\t" \
  "ds_read_u16_d16_hi v161, v161 offset:0\n\t" \
  "v_lshlrev_b32_sdwa v163, 1, v163 dst_sel:DWORD dst_unused:UNUSED_PAD src0_sel:DWORD src1_sel:WORD_1\n\t" \
  "v_smfmac_f32_16x16x64_bf16 v[36:39], v[220:223], v[184:191], v248\n\t" \
  "ds_read_b64_tr_b16 v[220:221], v249 offset:20480\n\t" \
  "ds_read_b64_tr_b16 v[222:223], v249 offset:20736\n\t" \
  "ds_read_u16_d16_hi v162, v162 offset:0\n\t" \
  "v_lshlrev_b32_sdwa v148, 1, v148 dst_sel:DWORD dst_unused:UNUSED_PAD src0_sel:DWORD src1_sel:WORD_1\n\t" \
  "v_lshlrev_b32_sdwa v149, 1, v149 dst_sel:DWORD dst_unused:UNUSED_PAD src0_sel:DWORD src1_sel:WORD_1\n\t" \
  "s_waitcnt lgkmcnt(13)\n\t" \
  "v_smfmac_f32_16x16x64_bf16 v[16:19], v[208:211], v[176:183], v248\n\t" \
  "ds_read_u16_d16_hi v163, v163 offset:0\n\t" \
  "ds_read_u16_d16_hi v148, v148 offset:0\n\t" \
  "ds_read_b64 v[230:231], v252 offset:0\n\t" \
  "v_smfmac_f32_16x16x64_bf16 v[40:43], v[208:211], v[184:191], v248\n\t" \
  "ds_read_b64_tr_b16 v[208:209], v249 offset:22528\n\t" \
  "ds_read_b64_tr_b16 v[210:211], v249 offset:22784\n\t" \
  "ds_read_b64 v[232:233], v252 offset:8\n\t" \
  "v_lshlrev_b32_sdwa v150, 1, v150 dst_sel:DWORD dst_unused:UNUSED_PAD src0_sel:DWORD src1_sel:WORD_1\n\t" \
  "v_lshlrev_b32_sdwa v151, 1, v151 dst_sel:DWORD dst_unused:UNUSED_PAD src0_sel:DWORD src1_sel:WORD_1\n\t" \
  "s_waitcnt lgkmcnt(15)\n\t" \
  "v_smfmac_f32_16x16x64_bf16 v[20:23], v[212:215], v[176:183], v248\n\t" \
  "ds_read_u16_d16_hi v149, v149 offset:0\n\t" \
  "ds_read_u16_d16_hi v150, v150 offset:0\n\t" \
  "v_smfmac_f32_16x16x64_bf16 v[44:47], v[212:215], v[184:191], v248\n\t" \
  "ds_read_b64_tr_b16 v[212:213], v249 offset:18944\n\t" \
  "ds_read_b64_tr_b16 v[214:215], v249 offset:19200\n\t" \
  "v_lshlrev_b32_sdwa v156, 1, v156 dst_sel:DWORD dst_unused:UNUSED_PAD src0_sel:DWORD src1_sel:WORD_1\n\t" \
  "s_waitcnt lgkmcnt(7)\n\t" \
  "v_mfma_f32_16x16x16_bf16 v[48:51], v[246:247], v[230:231], 0\n\t" \
  "ds_read_u16_d16_hi v151, v151 offset:0\n\t" \
  "v_lshlrev_b32_sdwa v157, 1, v157 dst_sel:DWORD dst_unused:UNUSED_PAD src0_sel:DWORD src1_sel:WORD_1\n\t" \
  "v_mfma_f32_16x16x32_bf16 v[176:179], v[216:219], v[96:99], 0\n\t" \
  "s_waitcnt lgkmcnt(5)\n\t" \
  "ds_read_u16_d16_hi v156, v156 offset:0\n\t" \
  "v_mfma_f32_16x16x16_bf16 v[52:55], v[246:247], v[232:233], 0\n\t" \
  "ds_read_b64 v[234:235], v252 offset:64\n\t" \
  "v_mfma_f32_16x16x32_bf16 v[184:187], v[216:219], v[108:111], 0\n\t" \
  "ds_read_b64 v[236:237], v252 offset:72\n\t" \
  "v_lshlrev_b32_sdwa v158, 1, v158 dst_sel:DWORD dst_unused:UNUSED_PAD src0_sel:DWORD src1_sel:WORD_1\n\t" \
  "v_mfma_f32_16x16x32_bf16 v[192:195], v[216:219], v[120:123], 0\n\t" \
  "ds_read_b64_tr_b16 v[216:217], v249 offset:20992\n\t" \
  "v_lshlrev_b32_sdwa v159, 1, v159 dst_sel:DWORD dst_unused:UNUSED_PAD src0_sel:DWORD src1_sel:WORD_1\n\t" \
  "v_mfma_f32_16x16x32_bf16 v[176:179], v[220:223], v[100:103], v[176:179]\n\t" \
  "ds_read_u16_d16_hi v157, v157 offset:0\n\t" \
  "v_lshlrev_b32_sdwa v164, 1, v164 dst_sel:DWORD dst_unused:UNUSED_PAD src0_sel:DWORD src1_sel:WORD_1\n\t" \
  "v_mfma_f32_16x16x32_bf16 v[184:187], v[220:223], v[112:115], v[184:187]\n\t" \
  "ds_read_b64_tr_b16 v[218:219], v249 offset:21248\n\t" \
  "v_mfma_f32_16x16x32_bf16 v[192:195], v[220:223], v[124:127], v[192:195]\n\t" \
  "s_waitcnt lgkmcnt(4)\n\t" \
  "ds_read_u16_d16_hi v158, v158 offset:0\n\t" \
  "v_mfma_f32_16x16x16_bf16 v[56:59], v[246:247], v[234:235], 0\n\t" \
  "ds_read_b64_tr_b16 v[220:221], v249 offset:23040\n\t" \
  "v_lshlrev_b32_sdwa v165, 1, v165 dst_sel:DWORD dst_unused:UNUSED_PAD src0_sel:DWORD src1_sel:WORD_1\n\t" \
  "v_mfma_f32_16x16x32_bf16 v[176:179], v[208:211], v[104:107], v[176:179]\n\t" \
  "s_waitcnt lgkmcnt(5)\n\t" \
  "ds_read_u16_d16_hi v159, v159 offset:0\n\t" \
  "v_mfma_f32_16x16x16_bf16 v[60:63], v[246:247], v[236:237], 0\n\t" \
  "ds_read_b64_tr_b16 v[222:223], v249 offset:23296\n\t" \
  "v_lshlrev_b32_sdwa v166, 1, v166 dst_sel:DWORD dst_unused:UNUSED_PAD src0_sel:DWORD src1_sel:WORD_1\n\t" \
  "v_mfma_f32_16x16x32_bf16 v[184:187], v[208:211], v[116:119], v[184:187]\n\t" \
  "ds_read_u16_d16_hi v164, v164 offset:0\n\t" \
  "v_mfma_f32_16x16x32_bf16 v[192:195], v[208:211], v[128:131], v[192:195]\n\t" \
  "s_barrier\n\t" \
  "ds_read_b64_tr_b16 v[208:209], v250 offset:12320\n\t" \
  "ds_read_b64 v[238:239], v252 offset:128\n\t" \
  "v_lshlrev_b32_sdwa v167, 1, v167 dst_sel:DWORD dst_unused:UNUSED_PAD src0_sel:DWORD src1_sel:WORD_1\n\t" \
  "v_mfma_f32_16x16x32_bf16 v[180:183], v[212:215], v[96:99], 0\n\t" \
  "ds_read_b64_tr_b16 v[210:211], v250 offset:13088\n\t" \
  "v_mfma_f32_16x16x32_bf16 v[188:191], v[212:215], v[108:111], 0\n\t" \
  "ds_read_u16_d16_hi v165, v165 offset:0\n\t" \
  "v_mfma_f32_16x16x32_bf16 v[196:199], v[212:215], v[120:123], 0\n\t" \
  "s_waitcnt lgkmcnt(2)\n\t" \
  "ds_read_b64_tr_b16 v[212:213], v250 offset:12328\n\t" \
  "ds_read_b64 v[240:241], v252 offset:136\n\t" \
  "v_mfma_f32_16x16x16_bf16 v[64:67], v[246:247], v[238:239], 0\n\t" \
  "ds_read_b64_tr_b16 v[214:215], v250 offset:13096\n\t" \
  "v_lshlrev_b32_sdwa v176, 1, v176 dst_sel:DWORD dst_unused:UNUSED_PAD src0_sel:DWORD src1_sel:WORD_1\n\t" \
  "v_mfma_f32_16x16x32_bf16 v[180:183], v[216:219], v[100:103], v[180:183]\n\t" \
  "s_waitcnt lgkmcnt(1)\n\t" \
  "ds_read_u16_d16_hi v166, v166 offset:0\n\t" \
  "v_mfma_f32_16x16x16_bf16 v[68:71], v[246:247], v[240:241], 0\n\t" \
  "ds_read_u16_d16_hi v167, v167 offset:0\n\t" \
  "v_lshlrev_b32_sdwa v177, 1, v177 dst_sel:DWORD dst_unused:UNUSED_PAD src0_sel:DWORD src1_sel:WORD_1\n\t" \
  "v_mfma_f32_16x16x32_bf16 v[188:191], v[216:219], v[112:115], v[188:191]\n\t" \
  "ds_read_u16_d16_hi v176, v176 offset:0\n\t" \
  "v_mfma_f32_16x16x32_bf16 v[196:199], v[216:219], v[124:127], v[196:199]\n\t" \
  "ds_read_b64_tr_b16 v[216:217], v250 offset:12384\n\t" \
  "v_lshlrev_b32_sdwa v178, 1, v178 dst_sel:DWORD dst_unused:UNUSED_PAD src0_sel:DWORD src1_sel:WORD_1\n\t" \
  "v_mfma_f32_16x16x32_bf16 v[180:183], v[220:223], v[104:107], v[180:183]\n\t" \
  "ds_read_u16_d16_hi v177, v177 offset:0\n\t" \
  "v_lshlrev_b32_sdwa v179, 1, v179 dst_sel:DWORD dst_unused:UNUSED_PAD src0_sel:DWORD src1_sel:WORD_1\n\t" \
  "v_mfma_f32_16x16x32_bf16 v[188:191], v[220:223], v[116:119], v[188:191]\n\t" \
  "ds_read_b64_tr_b16 v[218:219], v250 offset:13152\n\t" \
  "v_mfma_f32_16x16x32_bf16 v[196:199], v[220:223], v[128:131], v[196:199]\n\t" \
  "ds_read_u16_d16_hi v178, v178 offset:0\n\t" \
  "v_lshlrev_b32_sdwa v184, 1, v184 dst_sel:DWORD dst_unused:UNUSED_PAD src0_sel:DWORD src1_sel:WORD_1\n\t" \
  "v_smfmac_f32_16x16x64_bf16 v[0:3], v[208:211], v[144:151], v248\n\t" \
  "ds_read_b64_tr_b16 v[220:221], v250 offset:12392\n\t" \
  "v_lshlrev_b32_sdwa v185, 1, v185 dst_sel:DWORD dst_unused:UNUSED_PAD src0_sel:DWORD src1_sel:WORD_1\n\t" \
  "v_smfmac_f32_16x16x64_bf16 v[24:27], v[208:211], v[152:159], v248\n\t" \
  "s_waitcnt lgkmcnt(6)\n\t" \
  "ds_read_u16_d16_hi v179, v179 offset:0\n\t" \
  "v_smfmac_f32_16x16x64_bf16 v[48:51], v[208:211], v[160:167], v248\n\t" \
  "ds_read_b64_tr_b16 v[222:223], v250 offset:13160\n\t" \
  "v_lshlrev_b32_sdwa v186, 1, v186 dst_sel:DWORD dst_unused:UNUSED_PAD src0_sel:DWORD src1_sel:WORD_1\n\t" \
  "v_smfmac_f32_16x16x64_bf16 v[4:7], v[212:215], v[144:151], v248\n\t" \
  "ds_read_u16_d16_hi v184, v184 offset:0\n\t" \
  "v_lshlrev_b32_sdwa v187, 1, v187 dst_sel:DWORD dst_unused:UNUSED_PAD src0_sel:DWORD src1_sel:WORD_1\n\t" \
  "v_smfmac_f32_16x16x64_bf16 v[28:31], v[212:215], v[152:159], v248\n\t" \
  "ds_read_b64_tr_b16 v[208:209], v250 offset:12448\n\t" \
  "v_smfmac_f32_16x16x64_bf16 v[52:55], v[212:215], v[160:167], v248\n\t" \
  "s_waitcnt lgkmcnt(6)\n\t" \
  "ds_read_b64_tr_b16 v[210:211], v250 offset:13216\n\t" \
  "ds_read_u16_d16_hi v185, v185 offset:0\n\t" \
  "v_lshlrev_b32_sdwa v192, 1, v192 dst_sel:DWORD dst_unused:UNUSED_PAD src0_sel:DWORD src1_sel:WORD_1\n\t" \
  "v_smfmac_f32_16x16x64_bf16 v[8:11], v[216:219], v[144:151], v248\n\t" \
  "s_add_u32 %[rs], %[rs], 1\n\t" \
  "ds_read_b64_tr_b16 v[212:213], v250 offset:12456\n\t" \
  "v_lshlrev_b32_sdwa v193, 1, v193 dst_sel:DWORD dst_unused:UNUSED_PAD src0_sel:DWORD src1_sel:WORD_1\n\t" \
  "v_smfmac_f32_16x16x64_bf16 v[32:35], v[216:219], v[152:159], v248\n\t" \
  "s_cmp_eq_u32 %[rs], 6\n\t" \
  "ds_read_u16_d16_hi v186, v186 offset:0\n\t" \
  "v_smfmac_f32_16x16x64_bf16 v[56:59], v[216:219], v[160:167], v248\n\t" \
  "s_waitcnt lgkmcnt(6)\n\t" \
  "ds_read_b64_tr_b16 v[214:215], v250 offset:13224\n\t" \
  "ds_read_u16_d16_hi v187, v187 offset:0\n\t" \
  "v_lshlrev_b32_sdwa v194, 1, v194 dst_sel:DWORD dst_unused:UNUSED_PAD src0_sel:DWORD src1_sel:WORD_1\n\t" \
  "v_smfmac_f32_16x16x64_bf16 v[12:15], v[220:223], v[144:151], v248\n\t" \
  "s_cselect_b32 %[rs], 0, %[rs]\n\t" \
  "ds_read_b64_tr_b16 v[216:217], v249 offset:0\n\t" \
  "v_lshlrev_b32_sdwa v195, 1, v195 dst_sel:DWORD dst_unused:UNUSED_PAD src0_sel:DWORD src1_sel:WORD_1\n\t" \
  "v_smfmac_f32_16x16x64_bf16 v[36:39], v[220:223], v[152:159], v248\n\t" \
  "s_cselect_b32 %[t0], -75920, %[ps]\n\t" \
  "ds_read_u16_d16_hi v192, v192 offset:0\n\t" \
  "v_smfmac_f32_16x16x64_bf16 v[60:63], v[220:223], v[160:167], v248\n\t" \
  "s_waitcnt lgkmcnt(7)\n\t" \
  "ds_read_b64_tr_b16 v[218:219], v249 offset:256\n\t" \
  "ds_read_u16_d16_hi v193, v193 offset:0\n\t" \
  "v_lshlrev_b32_sdwa v180, 1, v180 dst_sel:DWORD dst_unused:UNUSED_PAD src0_sel:DWORD src1_sel:WORD_1\n\t" \
  "v_add_u32 v252, %[t0], v252\n\t" \
  "v_lshlrev_b32_sdwa v181, 1, v181 dst_sel:DWORD dst_unused:UNUSED_PAD src0_sel:DWORD src1_sel:WORD_1\n\t" \
  "v_smfmac_f32_16x16x64_bf16 v[16:19], v[208:211], v[144:151], v248\n\t" \
  "ds_read_b64_tr_b16 v[220:221], v249 offset:2048\n\t" \
  "ds_read_u16_d16_hi v194, v194 offset:0\n\t" \
  "v_smfmac_f32_16x16x64_bf16 v[40:43], v[208:211], v[152:159], v248\n\t" \
  "ds_read_b64_tr_b16 v[222:223], v249 offset:2304\n\t" \
  "v_lshlrev_b32_sdwa v182, 1, v182 dst_sel:DWORD dst_unused:UNUSED_PAD src0_sel:DWORD src1_sel:WORD_1\n\t" \
  "v_smfmac_f32_16x16x64_bf16 v[64:67], v[208:211], v[160:167], v248\n\t" \
  "s_waitcnt lgkmcnt(8)\n\t" \
  "ds_read_u16_d16_hi v195, v195 offset:0\n\t" \
  "ds_read_b64_tr_b16 v[208:209], v249 offset:4096\n\t" \
  "v_lshlrev_b32_sdwa v183, 1, v183 dst_sel:DWORD dst_unused:UNUSED_PAD src0_sel:DWORD src1_sel:WORD_1\n\t" \
  "v_smfmac_f32_16x16x64_bf16 v[20:23], v[212:215], v[144:151], v248\n\t" \
  "ds_read_u16_d16_hi v180, v180 offset:0\n\t" \
  "v_smfmac_f32_16x16x64_bf16 v[44:47], v[212:215], v[152:159], v248\n\t" \
  "ds_read_b64_tr_b16 v[210:211], v249 offset:4352\n\t" \
  "v_lshlrev_b32_sdwa v188, 1, v188 dst_sel:DWORD dst_unused:UNUSED_PAD src0_sel:DWORD src1_sel:WORD_1\n\t" \
  "v_smfmac_f32_16x16x64_bf16 v[68:71], v[212:215], v[160:167], v248\n\t" \
  "s_waitcnt lgkmcnt(8)\n\t" \
  "ds_read_u16_d16_hi v181, v181 offset:0\n\t" \
  "ds_read_b64_tr_b16 v[212:213], v249 offset:512\n\t" \
  "v_lshlrev_b32_sdwa v189, 1, v189 dst_sel:DWORD dst_unused:UNUSED_PAD src0_sel:DWORD src1_sel:WORD_1\n\t" \
  "v_mfma_f32_16x16x32_bf16 v[144:147], v[216:219], v[96:99], 0\n\t" \
  "ds_read_u16_d16_hi v182, v182 offset:0\n\t" \
  "v_mfma_f32_16x16x32_bf16 v[152:155], v[216:219], v[108:111], 0\n\t" \
  "ds_read_b64_tr_b16 v[214:215], v249 offset:768\n\t" \
  "v_lshlrev_b32_sdwa v190, 1, v190 dst_sel:DWORD dst_unused:UNUSED_PAD src0_sel:DWORD src1_sel:WORD_1\n\t" \
  "v_mfma_f32_16x16x32_bf16 v[160:163], v[216:219], v[120:123], 0\n\t" \
  "s_waitcnt lgkmcnt(8)\n\t" \
  "ds_read_u16_d16_hi v183, v183 offset:0\n\t" \
  "ds_read_b64_tr_b16 v[216:217], v249 offset:2560\n\t" \
  "v_lshlrev_b32_sdwa v191, 1, v191 dst_sel:DWORD dst_unused:UNUSED_PAD src0_sel:DWORD src1_sel:WORD_1\n\t" \
  "v_mfma_f32_16x16x32_bf16 v[144:147], v[220:223], v[100:103], v[144:147]\n\t" \
  "ds_read_u16_d16_hi v188, v188 offset:0\n\t" \
  "v_lshlrev_b32_sdwa v196, 1, v196 dst_sel:DWORD dst_unused:UNUSED_PAD src0_sel:DWORD src1_sel:WORD_1\n\t" \
  "v_mfma_f32_16x16x32_bf16 v[152:155], v[220:223], v[112:115], v[152:155]\n\t" \
  "ds_read_b64_tr_b16 v[218:219], v249 offset:2816\n\t" \
  "v_mfma_f32_16x16x32_bf16 v[160:163], v[220:223], v[124:127], v[160:163]\n\t" \
  "s_waitcnt lgkmcnt(8)\n\t" \
  "ds_read_u16_d16_hi v189, v189 offset:0\n\t" \
  "ds_read_b64_tr_b16 v[220:221], v249 offset:4608\n\t" \
  "v_lshlrev_b32_sdwa v197, 1, v197 dst_sel:DWORD dst_unused:UNUSED_PAD src0_sel:DWORD src1_sel:WORD_1\n\t" \
  "v_mfma_f32_16x16x32_bf16 v[144:147], v[208:211], v[104:107], v[144:147]\n\t" \
  "ds_read_u16_d16_hi v190, v190 offset:0\n\t" \
  "v_lshlrev_b32_sdwa v198, 1, v198 dst_sel:DWORD dst_unused:UNUSED_PAD src0_sel:DWORD src1_sel:WORD_1\n\t" \
  "v_mfma_f32_16x16x32_bf16 v[152:155], v[208:211], v[116:119], v[152:155]\n\t" \
  "ds_read_b64_tr_b16 v[222:223], v249 offset:4864\n\t" \
  "v_mfma_f32_16x16x32_bf16 v[160:163], v[208:211], v[128:131], v[160:163]\n\t" \
  "s_barrier\n\t" \
  "ds_read_u16_d16_hi v191, v191 offset:0\n\t" \
  "ds_read_b64_tr_b16 v[208:209], v250 offset:0\n\t" \
  "v_lshlrev_b32_sdwa v199, 1, v199 dst_sel:DWORD dst_unused:UNUSED_PAD src0_sel:DWORD src1_sel:WORD_1\n\t" \
  "s_waitcnt lgkmcnt(10)\n\t" \
  "v_mfma_f32_16x16x32_bf16 v[148:151], v[212:215], v[96:99], 0\n\t" \
  "ds_read_u16_d16_hi v196, v196 offset:0\n\t" \
  "v_mfma_f32_16x16x32_bf16 v[156:159], v[212:215], v[108:111], 0\n\t" \
  "ds_read_b64_tr_b16 v[210:211], v250 offset:768\n\t" \
  "v_mfma_f32_16x16x32_bf16 v[164:167], v[212:215], v[120:123], 0\n\t" \
  "s_waitcnt lgkmcnt(8)\n\t" \
  "ds_read_u16_d16_hi v197, v197 offset:0\n\t" \
  "ds_read_b64_tr_b16 v[212:213], v250 offset:8\n\t" \
  "v_lshlrev_b32_sdwa v144, 1, v144 dst_sel:DWORD dst_unused:UNUSED_PAD src0_sel:DWORD src1_sel:WORD_1\n\t" \
  "v_mfma_f32_16x16x32_bf16 v[148:151], v[216:219], v[100:103], v[148:151]\n\t" \
  "ds_read_u16_d16_hi v198, v198 offset:0\n\t" \
  "v_lshlrev_b32_sdwa v145, 1, v145 dst_sel:DWORD dst_unused:UNUSED_PAD src0_sel:DWORD src1_sel:WORD_1\n\t" \
  "v_mfma_f32_16x16x32_bf16 v[156:159], v[216:219], v[112:115], v[156:159]\n\t" \
  "ds_read_b64_tr_b16 v[214:215], v250 offset:776\n\t" \
  "v_mfma_f32_16x16x32_bf16 v[164:167], v[216:219], v[124:127], v[164:167]\n\t" \
  "s_waitcnt lgkmcnt(8)\n\t" \
  "ds_read_u16_d16_hi v199, v199 offset:0\n\t" \
  "v_lshlrev_b32_sdwa v146, 1, v146 dst_sel:DWORD dst_unused:UNUSED_PAD src0_sel:DWORD src1_sel:WORD_1\n\t" \
  "v_mfma_f32_16x16x32_bf16 v[148:151], v[220:223], v[104:107], v[148:151]\n\t" \
  "ds_read_b64_tr_b16 v[216:217], v250 offset:64\n\t" \
  "v_lshlrev_b32_sdwa v147, 1, v147 dst_sel:DWORD dst_unused:UNUSED_PAD src0_sel:DWORD src1_sel:WORD_1\n\t" \
  "v_mfma_f32_16x16x32_bf16 v[156:159], v[220:223], v[116:119], v[156:159]\n\t" \
  "ds_read_u16_d16_hi v144, v144 offset:0\n\t" \
  "v_mfma_f32_16x16x32_bf16 v[164:167], v[220:223], v[128:131], v[164:167]\n\t" \
  "s_waitcnt lgkmcnt(7)\n\t" \
  "ds_read_b64_tr_b16 v[218:219], v250 offset:832\n\t" \
  "ds_read_u16_d16_hi v145, v145 offset:0\n\t" \
  "v_lshlrev_b32_sdwa v152, 1, v152 dst_sel:DWORD dst_unused:UNUSED_PAD src0_sel:DWORD src1_sel:WORD_1\n\t" \
  "v_smfmac_f32_16x16x64_bf16 v[0:3], v[208:211], v[176:183], v248\n\t" \
  "ds_read_b64_tr_b16 v[220:221], v250 offset:72\n\t" \
  "v_lshlrev_b32_sdwa v153, 1, v153 dst_sel:DWORD dst_unused:UNUSED_PAD src0_sel:DWORD src1_sel:WORD_1\n\t" \
  "v_smfmac_f32_16x16x64_bf16 v[24:27], v[208:211], v[184:191], v248\n\t" \
  "s_waitcnt lgkmcnt(5)\n\t" \
  "ds_read_u16_d16_hi v146, v146 offset:0\n\t" \
  "v_smfmac_f32_16x16x64_bf16 v[48:51], v[208:211], v[192:199], v248\n\t" \
  "ds_read_b64_tr_b16 v[222:223], v250 offset:840\n\t" \
  "ds_read_u16_d16_hi v147, v147 offset:0\n\t" \
  "v_lshlrev_b32_sdwa v154, 1, v154 dst_sel:DWORD dst_unused:UNUSED_PAD src0_sel:DWORD src1_sel:WORD_1\n\t" \
  "v_smfmac_f32_16x16x64_bf16 v[4:7], v[212:215], v[176:183], v248\n\t" \
  "ds_read_b64_tr_b16 v[208:209], v250 offset:128\n\t" \
  "ds_read_u16_d16_hi v152, v152 offset:0\n\t" \
  "v_lshlrev_b32_sdwa v155, 1, v155 dst_sel:DWORD dst_unused:UNUSED_PAD src0_sel:DWORD src1_sel:WORD_1\n\t" \
  "v_smfmac_f32_16x16x64_bf16 v[28:31], v[212:215], v[184:191], v248\n\t" \
  "ds_read_b64_tr_b16 v[210:211], v250 offset:896\n\t" \
  "v_smfmac_f32_16x16x64_bf16 v[52:55], v[212:215], v[192:199], v248\n\t" \
  "s_waitcnt lgkmcnt(8)\n\t" \
  "ds_read_u16_d16_hi v153, v153 offset:0\n\t" \
  "ds_read_b64_tr_b16 v[212:213], v250 offset:136\n\t" \
  "v_lshlrev_b32_sdwa v160, 1, v160 dst_sel:DWORD dst_unused:UNUSED_PAD src0_sel:DWORD src1_sel:WORD_1\n\t" \
  "v_smfmac_f32_16x16x64_bf16 v[8:11], v[216:219], v[176:183], v248\n\t" \
  "ds_read_b128 v[132:135], v251 offset:0\n\t" \
  "ds_read_b64_tr_b16 v[214:215], v250 offset:904\n\t" \
  "v_lshlrev_b32_sdwa v161, 1, v161 dst_sel:DWORD dst_unused:UNUSED_PAD src0_sel:DWORD src1_sel:WORD_1\n\t" \
  "v_smfmac_f32_16x16x64_bf16 v[32:35], v[216:219], v[184:191], v248\n\t" \
  "ds_read_u16_d16_hi v154, v154 offset:0\n\t" \
  "v_smfmac_f32_16x16x64_bf16 v[56:59], v[216:219], v[192:199], v248\n\t" \
  "s_waitcnt lgkmcnt(9)\n\t" \
  "ds_read_b64_tr_b16 v[216:217], v249 offset:6144\n\t" \
  "ds_read_u16_d16_hi v155, v155 offset:0\n\t" \
  "v_lshlrev_b32_sdwa v162, 1, v162 dst_sel:DWORD dst_unused:UNUSED_PAD src0_sel:DWORD src1_sel:WORD_1\n\t" \
  "v_smfmac_f32_16x16x64_bf16 v[12:15], v[220:223], v[176:183], v248\n\t" \
  "ds_read_b64_tr_b16 v[218:219], v249 offset:6400\n\t" \
  "v_lshlrev_b32_sdwa v163, 1, v163 dst_sel:DWORD dst_unused:UNUSED_PAD src0_sel:DWORD src1_sel:WORD_1\n\t" \
  "v_smfmac_f32_16x16x64_bf16 v[36:39], v[220:223], v[184:191], v248\n\t" \
  "ds_read_b128 v[136:139], v251 offset:64\n\t" \
  "ds_read_u16_d16_hi v160, v160 offset:0\n\t" \
  "v_smfmac_f32_16x16x64_bf16 v[60:63], v[220:223], v[192:199], v248\n\t" \
  "s_waitcnt lgkmcnt(10)\n\t" \
  "ds_read_b64_tr_b16 v[220:221], v249 offset:8192\n\t" \
  "ds_read_u16_d16_hi v161, v161 offset:0\n\t" \
  "v_lshlrev_b32_sdwa v148, 1, v148 dst_sel:DWORD dst_unused:UNUSED_PAD src0_sel:DWORD src1_sel:WORD_1\n\t" \
  "v_lshlrev_b32_sdwa v149, 1, v149 dst_sel:DWORD dst_unused:UNUSED_PAD src0_sel:DWORD src1_sel:WORD_1\n\t" \
  "v_smfmac_f32_16x16x64_bf16 v[16:19], v[208:211], v[176:183], v248\n\t" \
  "ds_read_b64_tr_b16 v[222:223], v249 offset:8448\n\t" \
  "ds_read_u16_d16_hi v162, v162 offset:0\n\t" \
  "v_smfmac_f32_16x16x64_bf16 v[40:43], v[208:211], v[184:191], v248\n\t" \
  "ds_read_b128 v[140:143], v251 offset:128\n\t" \
  "v_lshlrev_b32_sdwa v150, 1, v150 dst_sel:DWORD dst_unused:UNUSED_PAD src0_sel:DWORD src1_sel:WORD_1\n\t" \
  "v_smfmac_f32_16x16x64_bf16 v[64:67], v[208:211], v[192:199], v248\n\t" \
  "s_waitcnt lgkmcnt(11)\n\t" \
  "ds_read_b64_tr_b16 v[208:209], v249 offset:10240\n\t" \
  "ds_read_u16_d16_hi v163, v163 offset:0\n\t" \
  "v_lshlrev_b32_sdwa v151, 1, v151 dst_sel:DWORD dst_unused:UNUSED_PAD src0_sel:DWORD src1_sel:WORD_1\n\t" \
  "v_smfmac_f32_16x16x64_bf16 v[20:23], v[212:215], v[176:183], v248\n\t" \
  "ds_read_b64_tr_b16 v[210:211], v249 offset:10496\n\t" \
  "v_smfmac_f32_16x16x64_bf16 v[44:47], v[212:215], v[184:191], v248\n\t" \
  "ds_read_u16_d16_hi v148, v148 offset:0\n\t" \
  "v_lshlrev_b32_sdwa v156, 1, v156 dst_sel:DWORD dst_unused:UNUSED_PAD src0_sel:DWORD src1_sel:WORD_1\n\t" \
  "v_smfmac_f32_16x16x64_bf16 v[68:71], v[212:215], v[192:199], v248\n\t" \
  "s_waitcnt lgkmcnt(11)\n\t" \
  "ds_read_b64_tr_b16 v[212:213], v249 offset:6656\n\t" \
  "ds_read_u16_d16_hi v149, v149 offset:0\n\t" \
  "v_lshlrev_b32_sdwa v157, 1, v157 dst_sel:DWORD dst_unused:UNUSED_PAD src0_sel:DWORD src1_sel:WORD_1\n\t" \
  "v_mfma_f32_16x16x32_bf16 v[176:179], v[216:219], v[96:99], 0\n\t" \
  "ds_read_b64_tr_b16 v[214:215], v249 offset:6912\n\t" \
  "v_mfma_f32_16x16x32_bf16 v[184:187], v[216:219], v[108:111], 0\n\t" \
  "ds_read_u16_d16_hi v150, v150 offset:0\n\t" \
  "v_lshlrev_b32_sdwa v158, 1, v158 dst_sel:DWORD dst_unused:UNUSED_PAD src0_sel:DWORD src1_sel:WORD_1\n\t" \
  "v_mfma_f32_16x16x32_bf16 v[192:195], v[216:219], v[120:123], 0\n\t" \
  "ds_read_u16_d16_hi v151, v151 offset:0\n\t" \
  "v_lshlrev_b32_sdwa v159, 1, v159 dst_sel:DWORD dst_unused:UNUSED_PAD src0_sel:DWORD src1_sel:WORD_1\n\t" \
  "v_mfma_f32_16x16x32_bf16 v[200:203], v[216:219], v[132:135], 0\n\t" \
  "s_waitcnt lgkmcnt(11)\n\t" \
  "ds_read_b64_tr_b16 v[216:217], v249 offset:8704\n\t" \
  "ds_read_u16_d16_hi v156, v156 offset:0\n\t" \
  "v_mfma_f32_16x16x32_bf16 v[176:179], v[220:223], v[100:103], v[176:179]\n\t" \
  "ds_read_b64_tr_b16 v[218:219], v249 offset:8960\n\t" \
  "v_lshlrev_b32_sdwa v164, 1, v164 dst_sel:DWORD dst_unused:UNUSED_PAD src0_sel:DWORD src1_sel:WORD_1\n\t" \
  "v_mfma_f32_16x16x32_bf16 v[184:187], v[220:223], v[112:115], v[184:187]\n\t" \
  "ds_read_u16_d16_hi v157, v157 offset:0\n\t" \
  "v_mfma_f32_16x16x32_bf16 v[192:195], v[220:223], v[124:127], v[192:195]\n\t" \
  "ds_read_u16_d16_hi v158, v158 offset:0\n\t" \
  "v_lshlrev_b32_sdwa v165, 1, v165 dst_sel:DWORD dst_unused:UNUSED_PAD src0_sel:DWORD src1_sel:WORD_1\n\t" \
  "v_mfma_f32_16x16x32_bf16 v[200:203], v[220:223], v[136:139], v[200:203]\n\t" \
  "s_waitcnt lgkmcnt(11)\n\t" \
  "ds_read_b64_tr_b16 v[220:221], v249 offset:10752\n\t" \
  "v_mfma_f32_16x16x32_bf16 v[176:179], v[208:211], v[104:107], v[176:179]\n\t" \
  "ds_read_u16_d16_hi v159, v159 offset:0\n\t" \
  "v_lshlrev_b32_sdwa v166, 1, v166 dst_sel:DWORD dst_unused:UNUSED_PAD src0_sel:DWORD src1_sel:WORD_1\n\t" \
  "v_mfma_f32_16x16x32_bf16 v[184:187], v[208:211], v[116:119], v[184:187]\n\t" \
  "ds_read_b64_tr_b16 v[222:223], v249 offset:11008\n\t" \
  "v_mfma_f32_16x16x32_bf16 v[192:195], v[208:211], v[128:131], v[192:195]\n\t" \
  "ds_read_u16_d16_hi v164, v164 offset:0\n\t" \
  "v_lshlrev_b32_sdwa v167, 1, v167 dst_sel:DWORD dst_unused:UNUSED_PAD src0_sel:DWORD src1_sel:WORD_1\n\t" \
  "v_mfma_f32_16x16x32_bf16 v[200:203], v[208:211], v[140:143], v[200:203]\n\t" \
  "s_barrier\n\t" \
  "ds_read_b64_tr_b16 v[208:209], v250 offset:6160\n\t" \
  "s_waitcnt lgkmcnt(12)\n\t" \
  "v_mfma_f32_16x16x32_bf16 v[180:183], v[212:215], v[96:99], 0\n\t" \
  "ds_read_u16_d16_hi v165, v165 offset:0\n\t" \
  "v_mfma_f32_16x16x32_bf16 v[188:191], v[212:215], v[108:111], 0\n\t" \
  "ds_read_b64_tr_b16 v[210:211], v250 offset:6928\n\t" \
  "v_mfma_f32_16x16x32_bf16 v[196:199], v[212:215], v[120:123], 0\n\t" \
  "ds_read_u16_d16_hi v166, v166 offset:0\n\t" \
  "v_lshlrev_b32_sdwa v176, 1, v176 dst_sel:DWORD dst_unused:UNUSED_PAD src0_sel:DWORD src1_sel:WORD_1\n\t" \
  "v_cvt_pk_f16_f32 v224, v200, v201\n\t" \
  "v_mfma_f32_16x16x32_bf16 v[204:207], v[212:215], v[132:135], 0\n\t" \
  "s_waitcnt lgkmcnt(10)\n\t" \
  "ds_read_b64_tr_b16 v[212:213], v250 offset:6168\n\t" \
  "v_cvt_pk_f16_f32 v226, v202, v203\n\t" \
  "v_pk_fma_f16 v225, v224, v224, %[h1] neg_lo:[1,0,0] neg_hi:[1,0,0]\n\t" \
  "v_mfma_f32_16x16x32_bf16 v[180:183], v[216:219], v[100:103], v[180:183]\n\t" \
  "ds_read_u16_d16_hi v167, v167 offset:0\n\t" \
  "v_lshlrev_b32_sdwa v177, 1, v177 dst_sel:DWORD dst_unused:UNUSED_PAD src0_sel:DWORD src1_sel:WORD_1\n\t" \
  "v_mfma_f32_16x16x32_bf16 v[188:191], v[216:219], v[112:115], v[188:191]\n\t" \
  "ds_read_b64_tr_b16 v[214:215], v250 offset:6936\n\t" \
  "v_pk_fma_f16 v227, v226, v226, %[h1] neg_lo:[1,0,0] neg_hi:[1,0,0]\n\t" \
  "v_pk_mul_f16 v225, v224, v225\n\t" \
  "v_mfma_f32_16x16x32_bf16 v[196:199], v[216:219], v[124:127], v[196:199]\n\t" \
  "ds_read_u16_d16_hi v176, v176 offset:0\n\t" \
  "v_lshlrev_b32_sdwa v178, 1, v178 dst_sel:DWORD dst_unused:UNUSED_PAD src0_sel:DWORD src1_sel:WORD_1\n\t" \
  "v_pk_mul_f16 v227, v226, v227\n\t" \
  "v_mfma_f32_16x16x32_bf16 v[204:207], v[216:219], v[136:139], v[204:207]\n\t" \
  "s_waitcnt lgkmcnt(9)\n\t" \
  "ds_read_b64_tr_b16 v[216:217], v250 offset:6224\n\t" \
  "v_exp_f16 v224, v225\n\t" \
  "v_mfma_f32_16x16x32_bf16 v[180:183], v[220:223], v[104:107], v[180:183]\n\t" \
  "ds_read_u16_d16_hi v177, v177 offset:0\n\t" \
  "v_lshlrev_b32_sdwa v179, 1, v179 dst_sel:DWORD dst_unused:UNUSED_PAD src0_sel:DWORD src1_sel:WORD_1\n\t" \
  "v_mfma_f32_16x16x32_bf16 v[188:191], v[220:223], v[116:119], v[188:191]\n\t" \
  "ds_read_b64_tr_b16 v[218:219], v250 offset:6992\n\t" \
  "v_exp_f16 v226, v227\n\t" \
  "v_mfma_f32_16x16x32_bf16 v[196:199], v[220:223], v[128:131], v[196:199]\n\t" \
  "ds_read_u16_d16_hi v178, v178 offset:0\n\t" \
  "v_lshlrev_b32_sdwa v184, 1, v184 dst_sel:DWORD dst_unused:UNUSED_PAD src0_sel:DWORD src1_sel:WORD_1\n\t" \
  "v_mfma_f32_16x16x32_bf16 v[204:207], v[220:223], v[140:143], v[204:207]\n\t" \
  "s_waitcnt lgkmcnt(9)\n\t" \
  "ds_read_b64_tr_b16 v[220:221], v250 offset:6232\n\t" \
  "v_exp_f16_sdwa v224, v225 dst_sel:WORD_1 dst_unused:UNUSED_PRESERVE src0_sel:WORD_1\n\t" \
  "v_smfmac_f32_16x16x64_bf16 v[0:3], v[208:211], v[144:151], v248\n\t" \
  "ds_read_u16_d16_hi v179, v179 offset:0\n\t" \
  "v_lshlrev_b32_sdwa v185, 1, v185 dst_sel:DWORD dst_unused:UNUSED_PAD src0_sel:DWORD src1_sel:WORD_1\n\t" \
  "v_exp_f16_sdwa v226, v227 dst_sel:WORD_1 dst_unused:UNUSED_PRESERVE src0_sel:WORD_1\n\t" \
  "v_smfmac_f32_16x16x64_bf16 v[24:27], v[208:211], v[152:159], v248\n\t" \
  "s_waitcnt lgkmcnt(8)\n\t" \
  "ds_read_b64_tr_b16 v[222:223], v250 offset:7000\n\t" \
  "v_smfmac_f32_16x16x64_bf16 v[48:51], v[208:211], v[160:167], v248\n\t" \
  "s_waitcnt lgkmcnt(8)\n\t" \
  "ds_read_u16_d16_hi v184, v184 offset:0\n\t" \
  "v_lshlrev_b32_sdwa v186, 1, v186 dst_sel:DWORD dst_unused:UNUSED_PAD src0_sel:DWORD src1_sel:WORD_1\n\t" \
  "v_pk_fma_f16 v224, v224, %[hc], %[hc]\n\t" \
  "v_pk_fma_f16 v226, v226, %[hc], %[hc]\n\t" \
  "v_rcp_f16 v225, v224\n\t" \
  "v_smfmac_f32_16x16x64_bf16 v[4:7], v[212:215], v[144:151], v248\n\t" \
  "ds_read_b64_tr_b16 v[208:209], v250 offset:6288\n\t" \
  "v_smfmac_f32_16x16x64_bf16 v[28:31], v[212:215], v[152:159], v248\n\t" \
  "ds_read_u16_d16_hi v185, v185 offset:0\n\t" \
  "v_lshlrev_b32_sdwa v187, 1, v187 dst_sel:DWORD dst_unused:UNUSED_PAD src0_sel:DWORD src1_sel:WORD_1\n\t" \
  "v_rcp_f16 v227, v226\n\t" \
  "v_smfmac_f32_16x16x64_bf16 v[52:55], v[212:215], v[160:167], v248\n\t" \
  "s_waitcnt lgkmcnt(7)\n\t" \
  "ds_read_b64_tr_b16 v[210:211], v250 offset:7056\n\t" \
  "v_lshlrev_b32_sdwa v192, 1, v192 dst_sel:DWORD dst_unused:UNUSED_PAD src0_sel:DWORD src1_sel:WORD_1\n\t" \
  "v_rcp_f16_sdwa v224, v224 dst_sel:DWORD dst_unused:UNUSED_PAD src0_sel:WORD_1\n\t" \
  "v_smfmac_f32_16x16x64_bf16 v[8:11], v[216:219], v[144:151], v248\n\t" \
  "ds_read_u16_d16_hi v186, v186 offset:0\n\t" \
  "v_lshlrev_b32_sdwa v193, 1, v193 dst_sel:DWORD dst_unused:UNUSED_PAD src0_sel:DWORD src1_sel:WORD_1\n\t" \
  "v_smfmac_f32_16x16x64_bf16 v[32:35], v[216:219], v[152:159], v248\n\t" \
  "ds_read_b64_tr_b16 v[212:213], v250 offset:6296\n\t" \
  "v_rcp_f16_sdwa v226, v226 dst_sel:DWORD dst_unused:UNUSED_PAD src0_sel:WORD_1\n\t" \
  "v_smfmac_f32_16x16x64_bf16 v[56:59], v[216:219], v[160:167], v248\n\t" \
  "s_waitcnt lgkmcnt(6)\n\t" \
  "ds_read_b64_tr_b16 v[214:215], v250 offset:7064\n\t" \
  "ds_read_u16_d16_hi v187, v187 offset:0\n\t" \
  "v_lshlrev_b32_sdwa v194, 1, v194 dst_sel:DWORD dst_unused:UNUSED_PAD src0_sel:DWORD src1_sel:WORD_1\n\t" \
  "v_fma_mix_f32 v200, v200, v225, 0 op_sel_hi:[0,1,0]\n\t" \
  "v_fma_mix_f32 v202, v202, v227, 0 op_sel_hi:[0,1,0]\n\t" \
  "v_smfmac_f32_16x16x64_bf16 v[12:15], v[220:223], v[144:151], v248\n\t" \
  "ds_read_b64_tr_b16 v[216:217], v249 offset:12288\n\t" \
  "v_lshlrev_b32_sdwa v195, 1, v195 dst_sel:DWORD dst_unused:UNUSED_PAD src0_sel:DWORD src1_sel:WORD_1\n\t" \
  "v_fma_mix_f32 v201, v201, v224, 0 op_sel_hi:[0,1,0]\n\t" \
  "v_smfmac_f32_16x16x64_bf16 v[36:39], v[220:223], v[152:159], v248\n\t" \
  "ds_read_u16_d16_hi v192, v192 offset:0\n\t" \
  "v_fma_mix_f32 v203, v203, v226, 0 op_sel_hi:[0,1,0]\n\t" \
  "v_smfmac_f32_16x16x64_bf16 v[60:63], v[220:223], v[160:167], v248\n\t" \
  "s_waitcnt lgkmcnt(6)\n\t" \
  "ds_read_b64_tr_b16 v[218:219], v249 offset:12544\n\t" \
  "ds_read_u16_d16_hi v193, v193 offset:0\n\t" \
  "v_lshlrev_b32_sdwa v180, 1, v180 dst_sel:DWORD dst_unused:UNUSED_PAD src0_sel:DWORD src1_sel:WORD_1\n\t" \
  "v_cvt_pk_f16_f32 v224, v204, v205\n\t" \
  "v_cvt_pk_f16_f32 v226, v206, v207\n\t" \
  "v_pk_fma_f16 v225, v224, v224, %[h1] neg_lo:[1,0,0] neg_hi:[1,0,0]\n\t" \
  "v_smfmac_f32_16x16x64_bf16 v[16:19], v[208:211], v[144:151], v248\n\t" \
  "ds_read_b64_tr_b16 v[220:221], v249 offset:14336\n\t" \
  "ds_read_u16_d16_hi v194, v194 offset:0\n\t" \
  "v_lshlrev_b32_sdwa v181, 1, v181 dst_sel:DWORD dst_unused:UNUSED_PAD src0_sel:DWORD src1_sel:WORD_1\n\t" \
  "v_pk_fma_f16 v227, v226, v226, %[h1] neg_lo:[1,0,0] neg_hi:[1,0,0]\n\t" \
  "v_smfmac_f32_16x16x64_bf16 v[40:43], v[208:211], v[152:159], v248\n\t" \
  "ds_read_b64_tr_b16 v[222:223], v249 offset:14592\n\t" \
  "ds_read_u16_d16_hi v195, v195 offset:0\n\t" \
  "v_pk_mul_f16 v225, v224, v225\n\t" \
  "v_lshlrev_b32_sdwa v182, 1, v182 dst_sel:DWORD dst_unused:UNUSED_PAD src0_sel:DWORD src1_sel:WORD_1\n\t" \
  "v_smfmac_f32_16x16x64_bf16 v[64:67], v[208:211], v[160:167], v248\n\t" \
  "s_waitcnt lgkmcnt(9)\n\t" \
  "ds_read_b64_tr_b16 v[208:209], v249 offset:16384\n\t" \
  "ds_read_b64 v[230:231], v252 offset:0\n\t" \
  "v_pk_mul_f16 v227, v226, v227\n\t" \
  "v_exp_f16 v224, v225\n\t" \
  "v_smfmac_f32_16x16x64_bf16 v[20:23], v[212:215], v[144:151], v248\n\t" \
  "ds_read_b64_tr_b16 v[210:211], v249 offset:16640\n\t" \
  "v_lshlrev_b32_sdwa v183, 1, v183 dst_sel:DWORD dst_unused:UNUSED_PAD src0_sel:DWORD src1_sel:WORD_1\n\t" \
  "v_exp_f16 v226, v227\n\t" \
  "v_smfmac_f32_16x16x64_bf16 v[44:47], v[212:215], v[152:159], v248\n\t" \
  "ds_read_u16_d16_hi v180, v180 offset:0\n\t" \
  "v_lshlrev_b32_sdwa v188, 1, v188 dst_sel:DWORD dst_unused:UNUSED_PAD src0_sel:DWORD src1_sel:WORD_1\n\t" \
  "v_smfmac_f32_16x16x64_bf16 v[68:71], v[212:215], v[160:167], v248\n\t" \
  "s_waitcnt lgkmcnt(2)\n\t" \
  "ds_read_b64_tr_b16 v[212:213], v249 offset:12800\n\t" \
  "ds_read_b64 v[232:233], v252 offset:8\n\t" \
  "v_mfma_f32_16x16x16_bf16 v[72:75], v[246:247], v[230:231], 0\n\t" \
  "ds_read_b64_tr_b16 v[214:215], v249 offset:13056\n\t" \
  "v_exp_f16_sdwa v224, v225 dst_sel:WORD_1 dst_unused:UNUSED_PRESERVE src0_sel:WORD_1\n\t" \
  "v_lshlrev_b32_sdwa v189, 1, v189 dst_sel:DWORD dst_unused:UNUSED_PAD src0_sel:DWORD src1_sel:WORD_1\n\t" \
  "v_exp_f16_sdwa v226, v227 dst_sel:WORD_1 dst_unused:UNUSED_PRESERVE src0_sel:WORD_1\n\t" \
  "v_mfma_f32_16x16x32_bf16 v[144:147], v[216:219], v[96:99], 0\n\t" \
  "s_waitcnt lgkmcnt(1)\n\t" \
  "ds_read_u16_d16_hi v181, v181 offset:0\n\t" \
  "ds_read_u16_d16_hi v182, v182 offset:0\n\t" \
  "v_mfma_f32_16x16x16_bf16 v[76:79], v[246:247], v[232:233], 0\n\t" \
  "ds_read_u16_d16_hi v183, v183 offset:0\n\t" \
  "v_mfma_f32_16x16x32_bf16 v[152:155], v[216:219], v[108:111], 0\n\t" \
  "ds_read_u16_d16_hi v188, v188 offset:0\n\t" \
  "v_lshlrev_b32_sdwa v190, 1, v190 dst_sel:DWORD dst_unused:UNUSED_PAD src0_sel:DWORD src1_sel:WORD_1\n\t" \
  "v_pk_fma_f16 v224, v224, %[hc], %[hc]\n\t" \
  "v_mfma_f32_16x16x32_bf16 v[160:163], v[216:219], v[120:123], 0\n\t" \
  "ds_read_b64 v[234:235], v252 offset:64\n\t" \
  "v_pk_fma_f16 v226, v226, %[hc], %[hc]\n\t" \
  "v_rcp_f16 v225, v224\n\t" \
  "v_mfma_f32_16x16x32_bf16 v[168:171], v[216:219], v[132:135], 0\n\t" \
  "ds_read_b64_tr_b16 v[216:217], v249 offset:14848\n\t" \
  "v_lshlrev_b32_sdwa v191, 1, v191 dst_sel:DWORD dst_unused:UNUSED_PAD src0_sel:DWORD src1_sel:WORD_1\n\t" \
  "v_mfma_f32_16x16x32_bf16 v[144:147], v[220:223], v[100:103], v[144:147]\n\t" \
  "ds_read_b64 v[236:237], v252 offset:72\n\t" \
  "v_rcp_f16 v227, v226\n\t" \
  "v_mfma_f32_16x16x32_bf16 v[152:155], v[220:223], v[112:115], v[152:155]\n\t" \
  "ds_read_b64_tr_b16 v[218:219], v249 offset:15104\n\t" \
  "v_lshlrev_b32_sdwa v196, 1, v196 dst_sel:DWORD dst_unused:UNUSED_PAD src0_sel:DWORD src1_sel:WORD_1\n\t" \
  "v_rcp_f16_sdwa v224, v224 dst_sel:DWORD dst_unused:UNUSED_PAD src0_sel:WORD_1\n\t" \
  "v_mfma_f32_16x16x32_bf16 v[160:163], v[220:223], v[124:127], v[160:163]\n\t" \
  "s_waitcnt lgkmcnt(3)\n\t" \
  "ds_read_u16_d16_hi v189, v189 offset:0\n\t" \
  "v_mfma_f32_16x16x16_bf16 v[80:83], v[246:247], v[234:235], 0\n\t" \
  "ds_read_u16_d16_hi v190, v190 offset:0\n\t" \
  "v_mfma_f32_16x16x32_bf16 v[168:171], v[220:223], v[136:139], v[168:171]\n\t" \
  "ds_read_b64_tr_b16 v[220:221], v249 offset:16896\n\t" \
  "v_lshlrev_b32_sdwa v197, 1, v197 dst_sel:DWORD dst_unused:UNUSED_PAD src0_sel:DWORD src1_sel:WORD_1\n\t" \
  "v_rcp_f16_sdwa v226, v226 dst_sel:DWORD dst_unused:UNUSED_PAD src0_sel:WORD_1\n\t" \
  "v_mfma_f32_16x16x32_bf16 v[144:147], v[208:211], v[104:107], v[144:147]\n\t" \
  "s_waitcnt lgkmcnt(4)\n\t" \
  "ds_read_u16_d16_hi v191, v191 offset:0\n\t" \
  "v_mfma_f32_16x16x16_bf16 v[84:87], v[246:247], v[236:237], 0\n\t" \
  "ds_read_b64_tr_b16 v[222:223], v249 offset:17152\n\t" \
  "v_lshlrev_b32_sdwa v198, 1, v198 dst_sel:DWORD dst_unused:UNUSED_PAD src0_sel:DWORD src1_sel:WORD_1\n\t" \
  "v_mfma_f32_16x16x32_bf16 v[152:155], v[208:211], v[116:119], v[152:155]\n\t" \
  "ds_read_u16_d16_hi v196, v196 offset:0\n\t" \
  "v_fma_mix_f32 v204, v204, v225, 0 op_sel_hi:[0,1,0]\n\t" \
  "v_fma_mix_f32 v206, v206, v227, 0 op_sel_hi:[0,1,0]\n\t" \
  "v_mfma_f32_16x16x32_bf16 v[160:163], v[208:211], v[128:131], v[160:163]\n\t" \
  "ds_read_b64 v[238:239], v252 offset:128\n\t" \
  "v_lshlrev_b32_sdwa v199, 1, v199 dst_sel:DWORD dst_unused:UNUSED_PAD src0_sel:DWORD src1_sel:WORD_1\n\t" \
  "v_fma_mix_f32 v205, v205, v224, 0 op_sel_hi:[0,1,0]\n\t" \
  "v_mfma_f32_16x16x32_bf16 v[168:171], v[208:211], v[140:143], v[168:171]\n\t" \
  "s_barrier\n\t" \
  "ds_read_b64_tr_b16 v[208:209], v250 offset:12320\n\t" \
  "ds_read_u16_d16_hi v197, v197 offset:0\n\t" \
  "v_fma_mix_f32 v207, v207, v226, 0 op_sel_hi:[0,1,0]\n\t" \
  "v_mfma_f32_16x16x32_bf16 v[148:151], v[212:215], v[96:99], 0\n\t" \
  "ds_read_b64_tr_b16 v[210:211], v250 offset:13088\n\t" \
  "v_mfma_f32_16x16x32_bf16 v[156:159], v[212:215], v[108:111], 0\n\t" \
  "ds_read_b64 v[240:241], v252 offset:136\n\t" \
  "v_mfma_f32_16x16x32_bf16 v[164:167], v[212:215], v[120:123], 0\n\t" \
  "s_waitcnt lgkmcnt(4)\n\t" \
  "ds_read_u16_d16_hi v198, v198 offset:0\n\t" \
  "v_mfma_f32_16x16x16_bf16 v[88:91], v[246:247], v[238:239], 0\n\t" \
  "ds_read_u16_d16_hi v199, v199 offset:0\n\t" \
  "v_lshlrev_b32_sdwa v144, 1, v144 dst_sel:DWORD dst_unused:UNUSED_PAD src0_sel:DWORD src1_sel:WORD_1\n\t" \
  "v_cvt_pk_f16_f32 v224, v168, v169\n\t" \
  "v_mfma_f32_16x16x32_bf16 v[172:175], v[212:215], v[132:135], 0\n\t" \
  "ds_read_b64_tr_b16 v[212:213], v250 offset:12328\n\t" \
  "v_cvt_pk_f16_f32 v226, v170, v171\n\t" \
  "v_pk_fma_f16 v225, v224, v224, %[h1] neg_lo:[1,0,0] neg_hi:[1,0,0]\n\t" \
  "v_mfma_f32_16x16x32_bf16 v[148:151], v[216:219], v[100:103], v[148:151]\n\t" \
  "s_waitcnt lgkmcnt(3)\n\t" \
  "ds_read_b64_tr_b16 v[214:215], v250 offset:13096\n\t" \
  "v_mfma_f32_16x16x16_bf16 v[92:95], v[246:247], v[240:241], 0\n\t" \
  "v_lshlrev_b32_sdwa v145, 1, v145 dst_sel:DWORD dst_unused:UNUSED_PAD src0_sel:DWORD src1_sel:WORD_1\n\t" \
  "v_mfma_f32_16x16x32_bf16 v[156:159], v[216:219], v[112:115], v[156:159]\n\t" \
  "ds_read_u16_d16_hi v144, v144 offset:0\n\t" \
  "v_pk_fma_f16 v227, v226, v226, %[h1] neg_lo:[1,0,0] neg_hi:[1,0,0]\n\t" \
  "v_pk_mul_f16 v225, v224, v225\n\t" \
  "v_mfma_f32_16x16x32_bf16 v[164:167], v[216:219], v[124:127], v[164:167]\n\t" \
  "v_lshlrev_b32_sdwa v146, 1, v146 dst_sel:DWORD dst_unused:UNUSED_PAD src0_sel:DWORD src1_sel:WORD_1\n\t" \
  "v_pk_mul_f16 v227, v226, v227\n\t" \
  "v_mfma_f32_16x16x32_bf16 v[172:175], v[216:219], v[136:139], v[172:175]\n\t" \
  "ds_read_b64_tr_b16 v[216:217], v250 offset:12384\n\t" \
  "v_exp_f16 v224, v225\n\t" \
  "v_mfma_f32_16x16x32_bf16 v[148:151], v[220:223], v[104:107], v[148:151]\n\t" \
  "ds_read_u16_d16_hi v145, v145 offset:0\n\t" \
  "v_lshlrev_b32_sdwa v147, 1, v147 dst_sel:DWORD dst_unused:UNUSED_PAD src0_sel:DWORD src1_sel:WORD_1\n\t" \
  "v_mfma_f32_16x16x32_bf16 v[156:159], v[220:223], v[116:119], v[156:159]\n\t" \
  "ds_read_b64_tr_b16 v[218:219], v250 offset:13152\n\t" \
  "v_exp_f16 v226, v227\n\t" \
  "v_mfma_f32_16x16x32_bf16 v[164:167], v[220:223], v[128:131], v[164:167]\n\t" \
  "ds_read_u16_d16_hi v146, v146 offset:0\n\t" \
  "v_lshlrev_b32_sdwa v152, 1, v152 dst_sel:DWORD dst_unused:UNUSED_PAD src0_sel:DWORD src1_sel:WORD_1\n\t" \
  "v_mfma_f32_16x16x32_bf16 v[172:175], v[220:223], v[140:143], v[172:175]\n\t" \
  "ds_read_b64_tr_b16 v[220:221], v250 offset:12392\n\t" \
  "v_exp_f16_sdwa v224, v225 dst_sel:WORD_1 dst_unused:UNUSED_PRESERVE src0_sel:WORD_1\n\t" \
  "v_smfmac_f32_16x16x64_bf16 v[0:3], v[208:211], v[176:183], v248\n\t" \
  "ds_read_u16_d16_hi v147, v147 offset:0\n\t" \
  "v_lshlrev_b32_sdwa v153, 1, v153 dst_sel:DWORD dst_unused:UNUSED_PAD src0_sel:DWORD src1_sel:WORD_1\n\t" \
  "v_exp_f16_sdwa v226, v227 dst_sel:WORD_1 dst_unused:UNUSED_PRESERVE src0_sel:WORD_1\n\t" \
  "v_smfmac_f32_16x16x64_bf16 v[24:27], v[208:211], v[184:191], v248\n\t" \
  "s_waitcnt lgkmcnt(9)\n\t" \
  "ds_read_b64_tr_b16 v[222:223], v250 offset:13160\n\t" \
  "v_smfmac_f32_16x16x64_bf16 v[48:51], v[208:211], v[192:199], v248\n\t" \
  "ds_read_u16_d16_hi v152, v152 offset:0\n\t" \
  "v_lshlrev_b32_sdwa v154, 1, v154 dst_sel:DWORD dst_unused:UNUSED_PAD src0_sel:DWORD src1_sel:WORD_1\n\t" \
  "v_pk_fma_f16 v224, v224, %[hc], %[hc]\n\t" \
  "v_smfmac_f32_16x16x64_bf16 v[72:75], v[208:211], v[200:207], v248\n\t" \
  "s_waitcnt lgkmcnt(9)\n\t" \
  "ds_read_b64_tr_b16 v[208:209], v250 offset:12448\n\t" \
  "v_pk_fma_f16 v226, v226, %[hc], %[hc]\n\t" \
  "v_rcp_f16 v225, v224\n\t" \
  "v_smfmac_f32_16x16x64_bf16 v[4:7], v[212:215], v[176:183], v248\n\t" \
  "ds_read_u16_d16_hi v153, v153 offset:0\n\t" \
  "v_smfmac_f32_16x16x64_bf16 v[28:31], v[212:215], v[184:191], v248\n\t" \
  "ds_read_b64_tr_b16 v[210:211], v250 offset:13216\n\t" \
  "v_lshlrev_b32_sdwa v155, 1, v155 dst_sel:DWORD dst_unused:UNUSED_PAD src0_sel:DWORD src1_sel:WORD_1\n\t" \
  "v_rcp_f16 v227, v226\n\t" \
  "v_smfmac_f32_16x16x64_bf16 v[52:55], v[212:215], v[192:199], v248\n\t" \
  "ds_read_u16_d16_hi v154, v154 offset:0\n\t" \
  "v_lshlrev_b32_sdwa v160, 1, v160 dst_sel:DWORD dst_unused:UNUSED_PAD src0_sel:DWORD src1_sel:WORD_1\n\t" \
  "v_smfmac_f32_16x16x64_bf16 v[76:79], v[212:215], v[200:207], v248\n\t" \
  "s_waitcnt lgkmcnt(9)\n\t" \
  "ds_read_b64_tr_b16 v[212:213], v250 offset:12456\n\t" \
  "v_rcp_f16_sdwa v224, v224 dst_sel:DWORD dst_unused:UNUSED_PAD src0_sel:WORD_1\n\t" \
  "v_smfmac_f32_16x16x64_bf16 v[8:11], v[216:219], v[176:183], v248\n\t" \
  "s_add_u32 %[rs], %[rs], 1\n\t" \
  "ds_read_u16_d16_hi v155, v155 offset:0\n\t" \
  "v_lshlrev_b32_sdwa v161, 1, v161 dst_sel:DWORD dst_unused:UNUSED_PAD src0_sel:DWORD src1_sel:WORD_1\n\t" \
  "v_smfmac_f32_16x16x64_bf16 v[32:35], v[216:219], v[184:191], v248\n\t" \
  "s_cmp_eq_u32 %[rs], 6\n\t" \
  "ds_read_b64_tr_b16 v[214:215], v250 offset:13224\n\t" \
  "v_rcp_f16_sdwa v226, v226 dst_sel:DWORD dst_unused:UNUSED_PAD src0_sel:WORD_1\n\t" \
  "v_smfmac_f32_16x16x64_bf16 v[56:59], v[216:219], v[192:199], v248\n\t" \
  "ds_read_u16_d16_hi v160, v160 offset:0\n\t" \
  "v_lshlrev_b32_sdwa v162, 1, v162 dst_sel:DWORD dst_unused:UNUSED_PAD src0_sel:DWORD src1_sel:WORD_1\n\t" \
  "v_smfmac_f32_16x16x64_bf16 v[80:83], v[216:219], v[200:207], v248\n\t" \
  "s_waitcnt lgkmcnt(9)\n\t" \
  "ds_read_b64_tr_b16 v[216:217], v249 offset:18432\n\t" \
  "v_fma_mix_f32 v168, v168, v225, 0 op_sel_hi:[0,1,0]\n\t" \
  "v_fma_mix_f32 v170, v170, v227, 0 op_sel_hi:[0,1,0]\n\t" \
  "v_smfmac_f32_16x16x64_bf16 v[12:15], v[220:223], v[176:183], v248\n\t" \
  "s_cselect_b32 %[rs], 0, %[rs]\n\t" \
  "ds_read_u16_d16_hi v161, v161 offset:0\n\t" \
  "v_lshlrev_b32_sdwa v163, 1, v163 dst_sel:DWORD dst_unused:UNUSED_PAD src0_sel:DWORD src1_sel:WORD_1\n\t" \
  "v_fma_mix_f32 v169, v169, v224, 0 op_sel_hi:[0,1,0]\n\t" \
  "v_smfmac_f32_16x16x64_bf16 v[36:39], v[220:223], v[184:191], v248\n\t" \
  "s_cselect_b32 %[t0], -75920, %[ps]\n\t" \
  "ds_read_b64_tr_b16 v[218:219], v249 offset:18688\n\t" \
  "v_fma_mix_f32 v171, v171, v226, 0 op_sel_hi:[0,1,0]\n\t" \
  "v_smfmac_f32_16x16x64_bf16 v[60:63], v[220:223], v[192:199], v248\n\t" \
  "ds_read_u16_d16_hi v162, v162 offset:0\n\t" \
  "v_lshlrev_b32_sdwa v148, 1, v148 dst_sel:DWORD dst_unused:UNUSED_PAD src0_sel:DWORD src1_sel:WORD_1\n\t" \
  "v_cvt_pk_f16_f32 v224, v172, v173\n\t" \
  "v_add_u32 v252, %[t0], v252\n\t" \
  "v_smfmac_f32_16x16x64_bf16 v[84:87], v[220:223], v[200:207], v248\n\t" \
  "s_waitcnt lgkmcnt(9)\n\t" \
  "ds_read_b64_tr_b16 v[220:221], v249 offset:20480\n\t" \
  "v_cvt_pk_f16_f32 v226, v174, v175\n\t" \
  "v_pk_fma_f16 v225, v224, v224, %[h1] neg_lo:[1,0,0] neg_hi:[1,0,0]\n\t" \
  "v_smfmac_f32_16x16x64_bf16 v[16:19], v[208:211], v[176:183], v248\n\t" \
  "ds_read_u16_d16_hi v163, v163 offset:0\n\t" \
  "v_lshlrev_b32_sdwa v149, 1, v149 dst_sel:DWORD dst_unused:UNUSED_PAD src0_sel:DWORD src1_sel:WORD_1\n\t" \
  "v_pk_fma_f16 v227, v226, v226, %[h1] neg_lo:[1,0,0] neg_hi:[1,0,0]\n\t" \
  "v_smfmac_f32_16x16x64_bf16 v[40:43], v[208:211], v[184:191], v248\n\t" \
  "ds_read_b64_tr_b16 v[222:223], v249 offset:20736\n\t" \
  "v_pk_mul_f16 v225, v224, v225\n\t" \
  "v_lshlrev_b32_sdwa v150, 1, v150 dst_sel:DWORD dst_unused:UNUSED_PAD src0_sel:DWORD src1_sel:WORD_1\n\t" \
  "v_smfmac_f32_16x16x64_bf16 v[64:67], v[208:211], v[192:199], v248\n\t" \
  "ds_read_u16_d16_hi v148, v148 offset:0\n\t" \
  "v_pk_mul_f16 v227, v226, v227\n\t" \
  "v_exp_f16 v224, v225\n\t" \
  "v_smfmac_f32_16x16x64_bf16 v[88:91], v[208:211], v[200:207], v248\n\t" \
  "s_waitcnt lgkmcnt(9)\n\t" \
  "ds_read_b64_tr_b16 v[208:209], v249 offset:22528\n\t" \
  "v_smfmac_f32_16x16x64_bf16 v[20:23], v[212:215], v[176:183], v248\n\t" \
  "ds_read_u16_d16_hi v149, v149 offset:0\n\t" \
  "v_lshlrev_b32_sdwa v151, 1, v151 dst_sel:DWORD dst_unused:UNUSED_PAD src0_sel:DWORD src1_sel:WORD_1\n\t" \
  "v_exp_f16 v226, v227\n\t" \
  "v_smfmac_f32_16x16x64_bf16 v[44:47], v[212:215], v[184:191], v248\n\t" \
  "ds_read_b64_tr_b16 v[210:211], v249 offset:22784\n\t" \
  "v_lshlrev_b32_sdwa v156, 1, v156 dst_sel:DWORD dst_unused:UNUSED_PAD src0_sel:DWORD src1_sel:WORD_1\n\t" \
  "v_smfmac_f32_16x16x64_bf16 v[68:71], v[212:215], v[192:199], v248\n\t" \
  "ds_read_u16_d16_hi v150, v150 offset:0\n\t" \
  "v_exp_f16_sdwa v224, v225 dst_sel:WORD_1 dst_unused:UNUSED_PRESERVE src0_sel:WORD_1\n\t" \
  "v_smfmac_f32_16x16x64_bf16 v[92:95], v[212:215], v[200:207], v248\n\t" \
  "s_waitcnt lgkmcnt(9)\n\t" \
  "ds_read_b64_tr_b16 v[212:213], v249 offset:18944\n\t" \
  "v_lshlrev_b32_sdwa v157, 1, v157 dst_sel:DWORD dst_unused:UNUSED_PAD src0_sel:DWORD src1_sel:WORD_1\n\t" \
  "v_exp_f16_sdwa v226, v227 dst_sel:WORD_1 dst_unused:UNUSED_PRESERVE src0_sel:WORD_1\n\t" \
  "v_mfma_f32_16x16x32_bf16 v[176:179], v[216:219], v[96:99], 0\n\t" \
  "ds_read_u16_d16_hi v151, v151 offset:0\n\t" \
  "v_mfma_f32_16x16x32_bf16 v[184:187], v[216:219], v[108:111], 0\n\t" \
  "ds_read_b64_tr_b16 v[214:215], v249 offset:19200\n\t" \
  "v_lshlrev_b32_sdwa v158, 1, v158 dst_sel:DWORD dst_unused:UNUSED_PAD src0_sel:DWORD src1_sel:WORD_1\n\t" \
  "v_pk_fma_f16 v224, v224, %[hc], %[hc]\n\t" \
  "v_mfma_f32_16x16x32_bf16 v[192:195], v[216:219], v[120:123], 0\n\t" \
  "ds_read_u16_d16_hi v156, v156 offset:0\n\t" \
  "v_pk_fma_f16 v226, v226, %[hc], %[hc]\n\t" \
  "v_rcp_f16 v225, v224\n\t" \
  "v_mfma_f32_16x16x32_bf16 v[200:203], v[216:219], v[132:135], 0\n\t" \
  "s_waitcnt lgkmcnt(9)\n\t" \
  "ds_read_b64_tr_b16 v[216:217], v249 offset:20992\n\t" \
  "v_lshlrev_b32_sdwa v159, 1, v159 dst_sel:DWORD dst_unused:UNUSED_PAD src0_sel:DWORD src1_sel:WORD_1\n\t" \
  "v_mfma_f32_16x16x32_bf16 v[176:179], v[220:223], v[100:103], v[176:179]\n\t" \
  "ds_read_u16_d16_hi v157, v157 offset:0\n\t" \
  "v_rcp_f16 v227, v226\n\t" \
  "v_mfma_f32_16x16x32_bf16 v[184:187], v[220:223], v[112:115], v[184:187]\n\t" \
  "ds_read_b64_tr_b16 v[218:219], v249 offset:21248\n\t" \
  "v_lshlrev_b32_sdwa v164, 1, v164 dst_sel:DWORD dst_unused:UNUSED_PAD src0_sel:DWORD src1_sel:WORD_1\n\t" \
  "v_rcp_f16_sdwa v224, v224 dst_sel:DWORD dst_unused:UNUSED_PAD src0_sel:WORD_1\n\t" \
  "v_mfma_f32_16x16x32_bf16 v[192:195], v[220:223], v[124:127], v[192:195]\n\t" \
  "ds_read_u16_d16_hi v158, v158 offset:0\n\t" \
  "v_mfma_f32_16x16x32_bf16 v[200:203], v[220:223], v[136:139], v[200:203]\n\t" \
  "s_waitcnt lgkmcnt(9)\n\t" \
  "ds_read_b64_tr_b16 v[220:221], v249 offset:23040\n\t" \
  "v_lshlrev_b32_sdwa v165, 1, v165 dst_sel:DWORD dst_unused:UNUSED_PAD src0_sel:DWORD src1_sel:WORD_1\n\t" \
  "v_rcp_f16_sdwa v226, v226 dst_sel:DWORD dst_unused:UNUSED_PAD src0_sel:WORD_1\n\t" \
  "v_mfma_f32_16x16x32_bf16 v[176:179], v[208:211], v[104:107], v[176:179]\n\t" \
  "ds_read_u16_d16_hi v159, v159 offset:0\n\t" \
  "v_lshlrev_b32_sdwa v166, 1, v166 dst_sel:DWORD dst_unused:UNUSED_PAD src0_sel:DWORD src1_sel:WORD_1\n\t" \
  "v_mfma_f32_16x16x32_bf16 v[184:187], v[208:211], v[116:119], v[184:187]\n\t" \
  "ds_read_b64_tr_b16 v[222:223], v249 offset:23296\n\t" \
  "v_fma_mix_f32 v172, v172, v225, 0 op_sel_hi:[0,1,0]\n\t" \
  "v_fma_mix_f32 v174, v174, v227, 0 op_sel_hi:[0,1,0]\n\t" \
  "v_mfma_f32_16x16x32_bf16 v[192:195], v[208:211], v[128:131], v[192:195]\n\t" \
  "ds_read_u16_d16_hi v164, v164 offset:0\n\t" \
  "v_lshlrev_b32_sdwa v167, 1, v167 dst_sel:DWORD dst_unused:UNUSED_PAD src0_sel:DWORD src1_sel:WORD_1\n\t" \
  "v_fma_mix_f32 v173, v173, v224, 0 op_sel_hi:[0,1,0]\n\t" \
  "v_mfma_f32_16x16x32_bf16 v[200:203], v[208:211], v[140:143], v[200:203]\n\t" \
  "s_barrier\n\t" \
  "s_mov_b32 %[lc], 3\n\t" \
  ".Lcnx_m:\n\t" \
  "ds_read_b64_tr_b16 v[208:209], v250 offset:0\n\t" \
  "v_fma_mix_f32 v175, v175, v226, 0 op_sel_hi:[0,1,0]\n\t" \
  "s_waitcnt lgkmcnt(10)\n\t" \
  "v_mfma_f32_16x16x32_bf16 v[180:183], v[212:215], v[96:99], 0\n\t" \
  "ds_read_u16_d16_hi v165, v165 offset:0\n\t" \
  "v_mfma_f32_16x16x32_bf16 v[188:191], v[212:215], v[108:111], 0\n\t" \
  "ds_read_b64_tr_b16 v[210:211], v250 offset:768\n\t" \
  "v_mfma_f32_16x16x32_bf16 v[196:199], v[212:215], v[120:123], 0\n\t" \
  "ds_read_u16_d16_hi v166, v166 offset:0\n\t" \
  "v_lshlrev_b32_sdwa v176, 1, v176 dst_sel:DWORD dst_unused:UNUSED_PAD src0_sel:DWORD src1_sel:WORD_1\n\t" \
  "v_cvt_pk_f16_f32 v224, v200, v201\n\t" \
  "v_mfma_f32_16x16x32_bf16 v[204:207], v[212:215], v[132:135], 0\n\t" \
  "s_waitcnt lgkmcnt(9)\n\t" \
  "ds_read_b64_tr_b16 v[212:213], v250 offset:8\n\t" \
  "v_cvt_pk_f16_f32 v226, v202, v203\n\t" \
  "v_pk_fma_f16 v225, v224, v224, %[h1] neg_lo:[1,0,0] neg_hi:[1,0,0]\n\t" \
  "v_mfma_f32_16x16x32_bf16 v[180:183], v[216:219], v[100:103], v[180:183]\n\t" \
  "ds_read_u16_d16_hi v167, v167 offset:0\n\t" \
  "v_lshlrev_b32_sdwa v177, 1, v177 dst_sel:DWORD dst_unused:UNUSED_PAD src0_sel:DWORD src1_sel:WORD_1\n\t" \
  "v_mfma_f32_16x16x32_bf16 v[188:191], v[216:219], v[112:115], v[188:191]\n\t" \
  "ds_read_b64_tr_b16 v[214:215], v250 offset:776\n\t" \
  "v_pk_fma_f16 v227, v226, v226, %[h1] neg_lo:[1,0,0] neg_hi:[1,0,0]\n\t" \
  "v_pk_mul_f16 v225, v224, v225\n\t" \
  "v_mfma_f32_16x16x32_bf16 v[196:199], v[216:219], v[124:127], v[196:199]\n\t" \
  "ds_read_u16_d16_hi v176, v176 offset:0\n\t" \
  "v_lshlrev_b32_sdwa v178, 1, v178 dst_sel:DWORD dst_unused:UNUSED_PAD src0_sel:DWORD src1_sel:WORD_1\n\t" \
  "v_pk_mul_f16 v227, v226, v227\n\t" \
  "v_mfma_f32_16x16x32_bf16 v[204:207], v[216:219], v[136:139], v[204:207]\n\t" \
  "s_waitcnt lgkmcnt(9)\n\t" \
  "ds_read_b64_tr_b16 v[216:217], v250 offset:64\n\t" \
  "v_exp_f16 v224, v225\n\t" \
  "v_mfma_f32_16x16x32_bf16 v[180:183], v[220:223], v[104:107], v[180:183]\n\t" \
  "ds_read_u16_d16_hi v177, v177 offset:0\n\t" \
  "v_lshlrev_b32_sdwa v179, 1, v179 dst_sel:DWORD dst_unused:UNUSED_PAD src0_sel:DWORD src1_sel:WORD_1\n\t" \
  "v_mfma_f32_16x16x32_bf16 v[188:191], v[220:223], v[116:119], v[188:191]\n\t" \
  "ds_read_b64_tr_b16 v[218:219], v250 offset:832\n\t" \
  "v_exp_f16 v226, v227\n\t" \
  "v_mfma_f32_16x16x32_bf16 v[196:199], v[220:223], v[128:131], v[196:199]\n\t" \
  "ds_read_u16_d16_hi v178, v178 offset:0\n\t" \
  "v_lshlrev_b32_sdwa v184, 1, v184 dst_sel:DWORD dst_unused:UNUSED_PAD src0_sel:DWORD src1_sel:WORD_1\n\t" \
  "v_mfma_f32_16x16x32_bf16 v[204:207], v[220:223], v[140:143], v[204:207]\n\t" \
  "s_waitcnt lgkmcnt(9)\n\t" \
  "ds_read_b64_tr_b16 v[220:221], v250 offset:72\n\t" \
  "v_exp_f16_sdwa v224, v225 dst_sel:WORD_1 dst_unused:UNUSED_PRESERVE src0_sel:WORD_1\n\t" \
  "v_smfmac_f32_16x16x64_bf16 v[0:3], v[208:211], v[144:151], v248\n\t" \
  "ds_read_u16_d16_hi v179, v179 offset:0\n\t" \
  "v_lshlrev_b32_sdwa v185, 1, v185 dst_sel:DWORD dst_unused:UNUSED_PAD src0_sel:DWORD src1_sel:WORD_1\n\t" \
  "v_exp_f16_sdwa v226, v227 dst_sel:WORD_1 dst_unused:UNUSED_PRESERVE src0_sel:WORD_1\n\t" \
  "v_smfmac_f32_16x16x64_bf16 v[24:27], v[208:211], v[152:159], v248\n\t" \
  "s_waitcnt lgkmcnt(8)\n\t" \
  "ds_read_b64_tr_b16 v[222:223], v250 offset:840\n\t" \
  "v_smfmac_f32_16x16x64_bf16 v[48:51], v[208:211], v[160:167], v248\n\t" \
  "ds_read_u16_d16_hi v184, v184 offset:0\n\t" \
  "v_lshlrev_b32_sdwa v186, 1, v186 dst_sel:DWORD dst_unused:UNUSED_PAD src0_sel:DWORD src1_sel:WORD_1\n\t" \
  "v_pk_fma_f16 v224, v224, %[hc], %[hc]\n\t" \
  "v_smfmac_f32_16x16x64_bf16 v[72:75], v[208:211], v[168:175], v248\n\t" \
  "s_waitcnt lgkmcnt(9)\n\t" \
  "ds_read_b64_tr_b16 v[208:209], v250 offset:128\n\t" \
  "v_pk_fma_f16 v226, v226, %[hc], %[hc]\n\t" \
  "v_rcp_f16 v225, v224\n\t" \
  "v_smfmac_f32_16x16x64_bf16 v[4:7], v[212:215], v[144:151], v248\n\t" \
  "ds_read_u16_d16_hi v185, v185 offset:0\n\t" \
  "v_smfmac_f32_16x16x64_bf16 v[28:31], v[212:215], v[152:159], v248\n\t" \
  "ds_read_b64_tr_b16 v[210:211], v250 offset:896\n\t" \
  "v_lshlrev_b32_sdwa v187, 1, v187 dst_sel:DWORD dst_unused:UNUSED_PAD src0_sel:DWORD src1_sel:WORD_1\n\t" \
  "v_rcp_f16 v227, v226\n\t" \
  "v_smfmac_f32_16x16x64_bf16 v[52:55], v[212:215], v[160:167], v248\n\t" \
  "ds_read_b128 v[96:99], v251 offset:0\n\t" \
  "v_lshlrev_b32_sdwa v192, 1, v192 dst_sel:DWORD dst_unused:UNUSED_PAD src0_sel:DWORD src1_sel:WORD_1\n\t" \
  "v_smfmac_f32_16x16x64_bf16 v[76:79], v[212:215], v[168:175], v248\n\t" \
  "s_waitcnt lgkmcnt(9)\n\t" \
  "ds_read_b64_tr_b16 v[212:213], v250 offset:136\n\t" \
  "v_rcp_f16_sdwa v224, v224 dst_sel:DWORD dst_unused:UNUSED_PAD src0_sel:WORD_1\n\t" \
  "v_smfmac_f32_16x16x64_bf16 v[8:11], v[216:219], v[144:151], v248\n\t" \
  "ds_read_u16_d16_hi v186, v186 offset:0\n\t" \
  "v_lshlrev_b32_sdwa v193, 1, v193 dst_sel:DWORD dst_unused:UNUSED_PAD src0_sel:DWORD src1_sel:WORD_1\n\t" \
  "v_smfmac_f32_16x16x64_bf16 v[32:35], v[216:219], v[152:159], v248\n\t" \
  "ds_read_b64_tr_b16 v[214:215], v250 offset:904\n\t" \
  "v_rcp_f16_sdwa v226, v226 dst_sel:DWORD dst_unused:UNUSED_PAD src0_sel:WORD_1\n\t" \
  "v_smfmac_f32_16x16x64_bf16 v[56:59], v[216:219], v[160:167], v248\n\t" \
  "ds_read_u16_d16_hi v187, v187 offset:0\n\t" \
  "v_lshlrev_b32_sdwa v194, 1, v194 dst_sel:DWORD dst_unused:UNUSED_PAD src0_sel:DWORD src1_sel:WORD_1\n\t" \
  "v_smfmac_f32_16x16x64_bf16 v[80:83], v[216:219], v[168:175], v248\n\t" \
  "s_waitcnt lgkmcnt(9)\n\t" \
  "ds_read_b64_tr_b16 v[216:217], v249 offset:0\n\t" \
  "ds_read_b128 v[100:103], v251 offset:64\n\t" \
  "v_fma_mix_f32 v200, v200, v225, 0 op_sel_hi:[0,1,0]\n\t" \
  "v_fma_mix_f32 v202, v202, v227, 0 op_sel_hi:[0,1,0]\n\t" \
  "v_smfmac_f32_16x16x64_bf16 v[12:15], v[220:223], v[144:151], v248\n\t" \
  "ds_read_b64_tr_b16 v[218:219], v249 offset:256\n\t" \
  "v_lshlrev_b32_sdwa v195, 1, v195 dst_sel:DWORD dst_unused:UNUSED_PAD src0_sel:DWORD src1_sel:WORD_1\n\t" \
  "v_fma_mix_f32 v201, v201, v224, 0 op_sel_hi:[0,1,0]\n\t" \
  "v_smfmac_f32_16x16x64_bf16 v[36:39], v[220:223], v[152:159], v248\n\t" \
  "ds_read_u16_d16_hi v192, v192 offset:0\n\t" \
  "v_fma_mix_f32 v203, v203, v226, 0 op_sel_hi:[0,1,0]\n\t" \
  "v_smfmac_f32_16x16x64_bf16 v[60:63], v[220:223], v[160:167], v248\n\t" \
  "ds_read_u16_d16_hi v193, v193 offset:0\n\t" \
  "v_lshlrev_b32_sdwa v180, 1, v180 dst_sel:DWORD dst_unused:UNUSED_PAD src0_sel:DWORD src1_sel:WORD_1\n\t" \
  "v_cvt_pk_f16_f32 v224, v204, v205\n\t" \
  "v_smfmac_f32_16x16x64_bf16 v[84:87], v[220:223], v[168:175], v248\n\t" \
  "s_waitcnt lgkmcnt(10)\n\t" \
  "ds_read_b64_tr_b16 v[220:221], v249 offset:2048\n\t" \
  "ds_read_u16_d16_hi v194, v194 offset:0\n\t" \
  "v_cvt_pk_f16_f32 v226, v206, v207\n\t" \
  "v_pk_fma_f16 v225, v224, v224, %[h1] neg_lo:[1,0,0] neg_hi:[1,0,0]\n\t" \
  "v_smfmac_f32_16x16x64_bf16 v[16:19], v[208:211], v[144:151], v248\n\t" \
  "ds_read_b64_tr_b16 v[222:223], v249 offset:2304\n\t" \
  "v_lshlrev_b32_sdwa v181, 1, v181 dst_sel:DWORD dst_unused:UNUSED_PAD src0_sel:DWORD src1_sel:WORD_1\n\t" \
  "v_pk_fma_f16 v227, v226, v226, %[h1] neg_lo:[1,0,0] neg_hi:[1,0,0]\n\t" \
  "v_smfmac_f32_16x16x64_bf16 v[40:43], v[208:211], v[152:159], v248\n\t" \
  "ds_read_b128 v[104:107], v251 offset:128\n\t" \
  "v_pk_mul_f16 v225, v224, v225\n\t" \
  "v_lshlrev_b32_sdwa v182, 1, v182 dst_sel:DWORD dst_unused:UNUSED_PAD src0_sel:DWORD src1_sel:WORD_1\n\t" \
  "v_smfmac_f32_16x16x64_bf16 v[64:67], v[208:211], v[160:167], v248\n\t" \
  "ds_read_u16_d16_hi v195, v195 offset:0\n\t" \
  "v_pk_mul_f16 v227, v226, v227\n\t" \
  "v_exp_f16 v224, v225\n\t" \
  "v_smfmac_f32_16x16x64_bf16 v[88:91], v[208:211], v[168:175], v248\n\t" \
  "s_waitcnt lgkmcnt(11)\n\t" \
  "ds_read_b64_tr_b16 v[208:209], v249 offset:4096\n\t" \
  "v_smfmac_f32_16x16x64_bf16 v[20:23], v[212:215], v[144:151], v248\n\t" \
  "ds_read_u16_d16_hi v180, v180 offset:0\n\t" \
  "v_lshlrev_b32_sdwa v183, 1, v183 dst_sel:DWORD dst_unused:UNUSED_PAD src0_sel:DWORD src1_sel:WORD_1\n\t" \
  "v_exp_f16 v226, v227\n\t" \
  "v_smfmac_f32_16x16x64_bf16 v[44:47], v[212:215], v[152:159], v248\n\t" \
  "ds_read_b64_tr_b16 v[210:211], v249 offset:4352\n\t" \
  "v_lshlrev_b32_sdwa v188, 1, v188 dst_sel:DWORD dst_unused:UNUSED_PAD src0_sel:DWORD src1_sel:WORD_1\n\t" \
  "v_smfmac_f32_16x16x64_bf16 v[68:71], v[212:215], v[160:167], v248\n\t" \
  "ds_read_u16_d16_hi v181, v181 offset:0\n\t" \
  "v_exp_f16_sdwa v224, v225 dst_sel:WORD_1 dst_unused:UNUSED_PRESERVE src0_sel:WORD_1\n\t" \
  "v_smfmac_f32_16x16x64_bf16 v[92:95], v[212:215], v[168:175], v248\n\t" \
  "s_waitcnt lgkmcnt(11)\n\t" \
  "ds_read_b64_tr_b16 v[212:213], v249 offset:512\n\t" \
  "v_lshlrev_b32_sdwa v189, 1, v189 dst_sel:DWORD dst_unused:UNUSED_PAD src0_sel:DWORD src1_sel:WORD_1\n\t" \
  "v_exp_f16_sdwa v226, v227 dst_sel:WORD_1 dst_unused:UNUSED_PRESERVE src0_sel:WORD_1\n\t" \
  "v_mfma_f32_16x16x32_bf16 v[144:147], v[216:219], v[96:99], 0\n\t" \
  "ds_read_u16_d16_hi v182, v182 offset:0\n\t" \
  "v_mfma_f32_16x16x32_bf16 v[152:155], v[216:219], v[108:111], 0\n\t" \
  "ds_read_b64_tr_b16 v[214:215], v249 offset:768\n\t" \
  "v_lshlrev_b32_sdwa v190, 1, v190 dst_sel:DWORD dst_unused:UNUSED_PAD src0_sel:DWORD src1_sel:WORD_1\n\t" \
  "v_pk_fma_f16 v224, v224, %[hc], %[hc]\n\t" \
  "v_mfma_f32_16x16x32_bf16 v[160:163], v[216:219], v[120:123], 0\n\t" \
  "ds_read_u16_d16_hi v183, v183 offset:0\n\t" \
  "v_pk_fma_f16 v226, v226, %[hc], %[hc]\n\t" \
  "v_rcp_f16 v225, v224\n\t" \
  "v_mfma_f32_16x16x32_bf16 v[168:171], v[216:219], v[132:135], 0\n\t" \
  "s_waitcnt lgkmcnt(10)\n\t" \
  "ds_read_b64_tr_b16 v[216:217], v249 offset:2560\n\t" \
  "v_lshlrev_b32_sdwa v191, 1, v191 dst_sel:DWORD dst_unused:UNUSED_PAD src0_sel:DWORD src1_sel:WORD_1\n\t" \
  "v_mfma_f32_16x16x32_bf16 v[144:147], v[220:223], v[100:103], v[144:147]\n\t" \
  "ds_read_u16_d16_hi v188, v188 offset:0\n\t" \
  "v_rcp_f16 v227, v226\n\t" \
  "v_mfma_f32_16x16x32_bf16 v[152:155], v[220:223], v[112:115], v[152:155]\n\t" \
  "ds_read_b64_tr_b16 v[218:219], v249 offset:2816\n\t" \
  "v_lshlrev_b32_sdwa v196, 1, v196 dst_sel:DWORD dst_unused:UNUSED_PAD src0_sel:DWORD src1_sel:WORD_1\n\t" \
  "v_rcp_f16_sdwa v224, v224 dst_sel:DWORD dst_unused:UNUSED_PAD src0_sel:WORD_1\n\t" \
  "v_mfma_f32_16x16x32_bf16 v[160:163], v[220:223], v[124:127], v[160:163]\n\t" \
  "ds_read_u16_d16_hi v189, v189 offset:0\n\t" \
  "v_mfma_f32_16x16x32_bf16 v[168:171], v[220:223], v[136:139], v[168:171]\n\t" \
  "s_waitcnt lgkmcnt(9)\n\t" \
  "ds_read_b64_tr_b16 v[220:221], v249 offset:4608\n\t" \
  "v_lshlrev_b32_sdwa v197, 1, v197 dst_sel:DWORD dst_unused:UNUSED_PAD src0_sel:DWORD src1_sel:WORD_1\n\t" \
  "v_rcp_f16_sdwa v226, v226 dst_sel:DWORD dst_unused:UNUSED_PAD src0_sel:WORD_1\n\t" \
  "v_mfma_f32_16x16x32_bf16 v[144:147], v[208:211], v[104:107], v[144:147]\n\t" \
  "ds_read_u16_d16_hi v190, v190 offset:0\n\t" \
  "v_lshlrev_b32_sdwa v198, 1, v198 dst_sel:DWORD dst_unused:UNUSED_PAD src0_sel:DWORD src1_sel:WORD_1\n\t" \
  "v_mfma_f32_16x16x32_bf16 v[152:155], v[208:211], v[116:119], v[152:155]\n\t" \
  "ds_read_b64_tr_b16 v[222:223], v249 offset:4864\n\t" \
  "v_fma_mix_f32 v204, v204, v225, 0 op_sel_hi:[0,1,0]\n\t" \
  "v_fma_mix_f32 v206, v206, v227, 0 op_sel_hi:[0,1,0]\n\t" \
  "v_mfma_f32_16x16x32_bf16 v[160:163], v[208:211], v[128:131], v[160:163]\n\t" \
  "ds_read_u16_d16_hi v191, v191 offset:0\n\t" \
  "v_lshlrev_b32_sdwa v199, 1, v199 dst_sel:DWORD dst_unused:UNUSED_PAD src0_sel:DWORD src1_sel:WORD_1\n\t" \
  "v_fma_mix_f32 v205, v205, v224, 0 op_sel_hi:[0,1,0]\n\t" \
  "v_mfma_f32_16x16x32_bf16 v[168:171], v[208:211], v[140:143], v[168:171]\n\t" \
  "s_barrier\n\t" \
  "ds_read_b64_tr_b16 v[208:209], v250 offset:6160\n\t" \
  "ds_read_u16_d16_hi v196, v196 offset:0\n\t" \
  "v_fma_mix_f32 v207, v207, v226, 0 op_sel_hi:[0,1,0]\n\t" \
  "s_waitcnt lgkmcnt(11)\n\t" \
  "v_mfma_f32_16x16x32_bf16 v[148:151], v[212:215], v[96:99], 0\n\t" \
  "ds_read_b64_tr_b16 v[210:211], v250 offset:6928\n\t" \
  "v_mfma_f32_16x16x32_bf16 v[156:159], v[212:215], v[108:111], 0\n\t" \
  "ds_read_u16_d16_hi v197, v197 offset:0\n\t" \
  "v_mfma_f32_16x16x32_bf16 v[164:167], v[212:215], v[120:123], 0\n\t" \
  "ds_read_u16_d16_hi v198, v198 offset:0\n\t" \
  "v_lshlrev_b32_sdwa v144, 1, v144 dst_sel:DWORD dst_unused:UNUSED_PAD src0_sel:DWORD src1_sel:WORD_1\n\t" \
  "v_cvt_pk_f16_f32 v224, v168, v169\n\t" \
  "v_mfma_f32_16x16x32_bf16 v[172:175], v[212:215], v[132:135], 0\n\t" \
  "s_waitcnt lgkmcnt(10)\n\t" \
  "ds_read_b64_tr_b16 v[212:213], v250 offset:6168\n\t" \
  "v_cvt_pk_f16_f32 v226, v170, v171\n\t" \
  "v_pk_fma_f16 v225, v224, v224, %[h1] neg_lo:[1,0,0] neg_hi:[1,0,0]\n\t" \
  "v_mfma_f32_16x16x32_bf16 v[148:151], v[216:219], v[100:103], v[148:151]\n\t" \
  "ds_read_u16_d16_hi v199, v199 offset:0\n\t" \
  "v_lshlrev_b32_sdwa v145, 1, v145 dst_sel:DWORD dst_unused:UNUSED_PAD src0_sel:DWORD src1_sel:WORD_1\n\t" \
  "v_mfma_f32_16x16x32_bf16 v[156:159], v[216:219], v[112:115], v[156:159]\n\t" \
  "ds_read_b64_tr_b16 v[214:215], v250 offset:6936\n\t" \
  "v_pk_fma_f16 v227, v226, v226, %[h1] neg_lo:[1,0,0] neg_hi:[1,0,0]\n\t" \
  "v_pk_mul_f16 v225, v224, v225\n\t" \
  "v_mfma_f32_16x16x32_bf16 v[164:167], v[216:219], v[124:127], v[164:167]\n\t" \
  "ds_read_u16_d16_hi v144, v144 offset:0\n\t" \
  "v_lshlrev_b32_sdwa v146, 1, v146 dst_sel:DWORD dst_unused:UNUSED_PAD src0_sel:DWORD src1_sel:WORD_1\n\t" \
  "v_pk_mul_f16 v227, v226, v227\n\t" \
  "v_mfma_f32_16x16x32_bf16 v[172:175], v[216:219], v[136:139], v[172:175]\n\t" \
  "s_waitcnt lgkmcnt(10)\n\t" \
  "ds_read_b64_tr_b16 v[216:217], v250 offset:6224\n\t" \
  "v_exp_f16 v224, v225\n\t" \
  "v_mfma_f32_16x16x32_bf16 v[148:151], v[220:223], v[104:107], v[148:151]\n\t" \
  "ds_read_u16_d16_hi v145, v145 offset:0\n\t" \
  "v_lshlrev_b32_sdwa v147, 1, v147 dst_sel:DWORD dst_unused:UNUSED_PAD src0_sel:DWORD src1_sel:WORD_1\n\t" \
  "v_mfma_f32_16x16x32_bf16 v[156:159], v[220:223], v[116:119], v[156:159]\n\t" \
  "ds_read_b64_tr_b16 v[218:219], v250 offset:6992\n\t" \
  "v_exp_f16 v226, v227\n\t" \
  "v_mfma_f32_16x16x32_bf16 v[164:167], v[220:223], v[128:131], v[164:167]\n\t" \
  "ds_read_u16_d16_hi v146, v146 offset:0\n\t" \
  "v_lshlrev_b32_sdwa v152, 1, v152 dst_sel:DWORD dst_unused:UNUSED_PAD src0_sel:DWORD src1_sel:WORD_1\n\t" \
  "v_mfma_f32_16x16x32_bf16 v[172:175], v[220:223], v[140:143], v[172:175]\n\t" \
  "s_waitcnt lgkmcnt(10)\n\t" \
  "ds_read_b64_tr_b16 v[220:221], v250 offset:6232\n\t" \
  "v_exp_f16_sdwa v224, v225 dst_sel:WORD_1 dst_unused:UNUSED_PRESERVE src0_sel:WORD_1\n\t" \
  "v_smfmac_f32_16x16x64_bf16 v[0:3], v[208:211], v[176:183], v248\n\t" \
  "ds_read_u16_d16_hi v147, v147 offset:0\n\t" \
  "v_lshlrev_b32_sdwa v153, 1, v153 dst_sel:DWORD dst_unused:UNUSED_PAD src0_sel:DWORD src1_sel:WORD_1\n\t" \
  "v_exp_f16_sdwa v226, v227 dst_sel:WORD_1 dst_unused:UNUSED_PRESERVE src0_sel:WORD_1\n\t" \
  "v_smfmac_f32_16x16x64_bf16 v[24:27], v[208:211], v[184:191], v248\n\t" \
  "s_waitcnt lgkmcnt(8)\n\t" \
  "ds_read_b64_tr_b16 v[222:223], v250 offset:7000\n\t" \
  "v_smfmac_f32_16x16x64_bf16 v[48:51], v[208:211], v[192:199], v248\n\t" \
  "ds_read_u16_d16_hi v152, v152 offset:0\n\t" \
  "v_lshlrev_b32_sdwa v154, 1, v154 dst_sel:DWORD dst_unused:UNUSED_PAD src0_sel:DWORD src1_sel:WORD_1\n\t" \
  "v_pk_fma_f16 v224, v224, %[hc], %[hc]\n\t" \
  "v_smfmac_f32_16x16x64_bf16 v[72:75], v[208:211], v[200:207], v248\n\t" \
  "s_waitcnt lgkmcnt(9)\n\t" \
  "ds_read_b64_tr_b16 v[208:209], v250 offset:6288\n\t" \
  "v_pk_fma_f16 v226, v226, %[hc], %[hc]\n\t" \
  "v_rcp_f16 v225, v224\n\t" \
  "v_smfmac_f32_16x16x64_bf16 v[4:7], v[212:215], v[176:183], v248\n\t" \
  "ds_read_u16_d16_hi v153, v153 offset:0\n\t" \
  "v_smfmac_f32_16x16x64_bf16 v[28:31], v[212:215], v[184:191], v248\n\t" \
  "ds_read_b64_tr_b16 v[210:211], v250 offset:7056\n\t" \
  "v_lshlrev_b32_sdwa v155, 1, v155 dst_sel:DWORD dst_unused:UNUSED_PAD src0_sel:DWORD src1_sel:WORD_1\n\t" \
  "v_rcp_f16 v227, v226\n\t" \
  "v_smfmac_f32_16x16x64_bf16 v[52:55], v[212:215], v[192:199], v248\n\t" \
  "ds_read_u16_d16_hi v154, v154 offset:0\n\t" \
  "v_lshlrev_b32_sdwa v160, 1, v160 dst_sel:DWORD dst_unused:UNUSED_PAD src0_sel:DWORD src1_sel:WORD_1\n\t" \
  "v_smfmac_f32_16x16x64_bf16 v[76:79], v[212:215], v[200:207], v248\n\t" \
  "s_waitcnt lgkmcnt(9)\n\t" \
  "ds_read_b64_tr_b16 v[212:213], v250 offset:6296\n\t" \
  "v_rcp_f16_sdwa v224, v224 dst_sel:DWORD dst_unused:UNUSED_PAD src0_sel:WORD_1\n\t" \
  "v_smfmac_f32_16x16x64_bf16 v[8:11], v[216:219], v[176:183], v248\n\t" \
  "ds_read_u16_d16_hi v155, v155 offset:0\n\t" \
  "v_lshlrev_b32_sdwa v161, 1, v161 dst_sel:DWORD dst_unused:UNUSED_PAD src0_sel:DWORD src1_sel:WORD_1\n\t" \
  "v_smfmac_f32_16x16x64_bf16 v[32:35], v[216:219], v[184:191], v248\n\t" \
  "ds_read_b64_tr_b16 v[214:215], v250 offset:7064\n\t" \
  "v_rcp_f16_sdwa v226, v226 dst_sel:DWORD dst_unused:UNUSED_PAD src0_sel:WORD_1\n\t" \
  "v_smfmac_f32_16x16x64_bf16 v[56:59], v[216:219], v[192:199], v248\n\t" \
  "ds_read_u16_d16_hi v160, v160 offset:0\n\t" \
  "v_lshlrev_b32_sdwa v162, 1, v162 dst_sel:DWORD dst_unused:UNUSED_PAD src0_sel:DWORD src1_sel:WORD_1\n\t" \
  "v_cvt_pk_bf16_f32 v230, v0, v1\n\t" \
  "v_smfmac_f32_16x16x64_bf16 v[80:83], v[216:219], v[200:207], v248\n\t" \
  "s_waitcnt lgkmcnt(9)\n\t" \
  "ds_read_b64_tr_b16 v[216:217], v249 offset:6144\n\t" \
  "v_fma_mix_f32 v168, v168, v225, 0 op_sel_hi:[0,1,0]\n\t" \
  "v_fma_mix_f32 v170, v170, v227, 0 op_sel_hi:[0,1,0]\n\t" \
  "v_cvt_pk_bf16_f32 v231, v2, v3\n\t" \
  "v_smfmac_f32_16x16x64_bf16 v[12:15], v[220:223], v[176:183], v248\n\t" \
  "ds_read_u16_d16_hi v161, v161 offset:0\n\t" \
  "v_lshlrev_b32_sdwa v163, 1, v163 dst_sel:DWORD dst_unused:UNUSED_PAD src0_sel:DWORD src1_sel:WORD_1\n\t" \
  "v_fma_mix_f32 v169, v169, v224, 0 op_sel_hi:[0,1,0]\n\t" \
  "v_cvt_pk_bf16_f32 v232, v4, v5\n\t" \
  "v_smfmac_f32_16x16x64_bf16 v[36:39], v[220:223], v[184:191], v248\n\t" \
  "ds_read_b64_tr_b16 v[218:219], v249 offset:6400\n\t" \
  "v_fma_mix_f32 v171, v171, v226, 0 op_sel_hi:[0,1,0]\n\t" \
  "v_cvt_pk_bf16_f32 v233, v6, v7\n\t" \
  "v_smfmac_f32_16x16x64_bf16 v[60:63], v[220:223], v[192:199], v248\n\t" \
  "ds_read_u16_d16_hi v162, v162 offset:0\n\t" \
  "v_lshlrev_b32_sdwa v148, 1, v148 dst_sel:DWORD dst_unused:UNUSED_PAD src0_sel:DWORD src1_sel:WORD_1\n\t" \
  "v_cvt_pk_f16_f32 v224, v172, v173\n\t" \
  "v_smfmac_f32_16x16x64_bf16 v[84:87], v[220:223], v[200:207], v248\n\t" \
  "s_waitcnt lgkmcnt(9)\n\t" \
  "ds_read_b64_tr_b16 v[220:221], v249 offset:8192\n\t" \
  "v_cvt_pk_f16_f32 v226, v174, v175\n\t" \
  "v_pk_fma_f16 v225, v224, v224, %[h1] neg_lo:[1,0,0] neg_hi:[1,0,0]\n\t" \
  "v_smfmac_f32_16x16x64_bf16 v[16:19], v[208:211], v[176:183], v248\n\t" \
  "ds_read_u16_d16_hi v163, v163 offset:0\n\t" \
  "v_lshlrev_b32_sdwa v149, 1, v149 dst_sel:DWORD dst_unused:UNUSED_PAD src0_sel:DWORD src1_sel:WORD_1\n\t" \
  "v_pk_fma_f16 v227, v226, v226, %[h1] neg_lo:[1,0,0] neg_hi:[1,0,0]\n\t" \
  "v_smfmac_f32_16x16x64_bf16 v[40:43], v[208:211], v[184:191], v248\n\t" \
  "ds_read_b64_tr_b16 v[222:223], v249 offset:8448\n\t" \
  "buffer_store_dwordx4 v[230:233], v253, %[osrd], %[ob] offen offset:0 sc1\n\t" \
  "v_pk_mul_f16 v225, v224, v225\n\t" \
  "v_lshlrev_b32_sdwa v150, 1, v150 dst_sel:DWORD dst_unused:UNUSED_PAD src0_sel:DWORD src1_sel:WORD_1\n\t" \
  "v_smfmac_f32_16x16x64_bf16 v[64:67], v[208:211], v[192:199], v248\n\t" \
  "ds_read_b64 v[230:231], v252 offset:0\n\t" \
  "v_pk_mul_f16 v227, v226, v227\n\t" \
  "v_exp_f16 v224, v225\n\t" \
  "v_cvt_pk_bf16_f32 v234, v8, v9\n\t" \
  "v_smfmac_f32_16x16x64_bf16 v[88:91], v[208:211], v[200:207], v248\n\t" \
  "s_waitcnt lgkmcnt(9)\n\t" \
  "ds_read_b64_tr_b16 v[208:209], v249 offset:10240\n\t" \
  "ds_read_u16_d16_hi v148, v148 offset:0\n\t" \
  "v_cvt_pk_bf16_f32 v235, v10, v11\n\t" \
  "v_smfmac_f32_16x16x64_bf16 v[20:23], v[212:215], v[176:183], v248\n\t" \
  "ds_read_b64_tr_b16 v[210:211], v249 offset:10496\n\t" \
  "v_lshlrev_b32_sdwa v151, 1, v151 dst_sel:DWORD dst_unused:UNUSED_PAD src0_sel:DWORD src1_sel:WORD_1\n\t" \
  "v_exp_f16 v226, v227\n\t" \
  "v_cvt_pk_bf16_f32 v236, v12, v13\n\t" \
  "v_smfmac_f32_16x16x64_bf16 v[44:47], v[212:215], v[184:191], v248\n\t" \
  "ds_read_b64 v[232:233], v252 offset:8\n\t" \
  "v_lshlrev_b32_sdwa v156, 1, v156 dst_sel:DWORD dst_unused:UNUSED_PAD src0_sel:DWORD src1_sel:WORD_1\n\t" \
  "v_cvt_pk_bf16_f32 v237, v14, v15\n\t" \
  "v_smfmac_f32_16x16x64_bf16 v[68:71], v[212:215], v[192:199], v248\n\t" \
  "s_waitcnt lgkmcnt(4)\n\t" \
  "ds_read_u16_d16_hi v149, v149 offset:0\n\t" \
  "v_mfma_f32_16x16x16_bf16 v[0:3], v[246:247], v[230:231], 0\n\t" \
  "ds_read_u16_d16_hi v150, v150 offset:0\n\t" \
  "v_exp_f16_sdwa v224, v225 dst_sel:WORD_1 dst_unused:UNUSED_PRESERVE src0_sel:WORD_1\n\t" \
  "v_smfmac_f32_16x16x64_bf16 v[92:95], v[212:215], v[200:207], v248\n\t" \
  "ds_read_b64_tr_b16 v[212:213], v249 offset:6656\n\t" \
  "v_lshlrev_b32_sdwa v157, 1, v157 dst_sel:DWORD dst_unused:UNUSED_PAD src0_sel:DWORD src1_sel:WORD_1\n\t" \
  "v_exp_f16_sdwa v226, v227 dst_sel:WORD_1 dst_unused:UNUSED_PRESERVE src0_sel:WORD_1\n\t" \
  "v_mfma_f32_16x16x32_bf16 v[176:179], v[216:219], v[96:99], 0\n\t" \
  "s_waitcnt lgkmcnt(3)\n\t" \
  "ds_read_u16_d16_hi v151, v151 offset:0\n\t" \
  "ds_read_b64_tr_b16 v[214:215], v249 offset:6912\n\t" \
  "v_mfma_f32_16x16x16_bf16 v[4:7], v[246:247], v[232:233], 0\n\t" \
  "buffer_store_dwordx4 v[234:237], v253, %[osrd], %[ob] offen offset:64 sc1\n\t" \
  "v_mfma_f32_16x16x32_bf16 v[184:187], v[216:219], v[108:111], 0\n\t" \
  "ds_read_u16_d16_hi v156, v156 offset:0\n\t" \
  "v_lshlrev_b32_sdwa v158, 1, v158 dst_sel:DWORD dst_unused:UNUSED_PAD src0_sel:DWORD src1_sel:WORD_1\n\t" \
  "v_pk_fma_f16 v224, v224, %[hc], %[hc]\n\t" \
  "v_mfma_f32_16x16x32_bf16 v[192:195], v[216:219], v[120:123], 0\n\t" \
  "ds_read_b64 v[234:235], v252 offset:64\n\t" \
  "v_pk_fma_f16 v226, v226, %[hc], %[hc]\n\t" \
  "v_rcp_f16 v225, v224\n\t" \
  "v_cvt_pk_bf16_f32 v238, v16, v17\n\t" \
  "v_mfma_f32_16x16x32_bf16 v[200:203], v[216:219], v[132:135], 0\n\t" \
  "ds_read_b64_tr_b16 v[216:217], v249 offset:8704\n\t" \
  "v_lshlrev_b32_sdwa v159, 1, v159 dst_sel:DWORD dst_unused:UNUSED_PAD src0_sel:DWORD src1_sel:WORD_1\n\t" \
  "v_cvt_pk_bf16_f32 v239, v18, v19\n\t" \
  "v_mfma_f32_16x16x32_bf16 v[176:179], v[220:223], v[100:103], v[176:179]\n\t" \
  "ds_read_b64 v[236:237], v252 offset:72\n\t" \
  "v_rcp_f16 v227, v226\n\t" \
  "v_cvt_pk_bf16_f32 v240, v20, v21\n\t" \
  "v_mfma_f32_16x16x32_bf16 v[184:187], v[220:223], v[112:115], v[184:187]\n\t" \
  "ds_read_b64_tr_b16 v[218:219], v249 offset:8960\n\t" \
  "v_lshlrev_b32_sdwa v164, 1, v164 dst_sel:DWORD dst_unused:UNUSED_PAD src0_sel:DWORD src1_sel:WORD_1\n\t" \
  "v_rcp_f16_sdwa v224, v224 dst_sel:DWORD dst_unused:UNUSED_PAD src0_sel:WORD_1\n\t" \
  "v_cvt_pk_bf16_f32 v241, v22, v23\n\t" \
  "v_mfma_f32_16x16x32_bf16 v[192:195], v[220:223], v[124:127], v[192:195]\n\t" \
  "s_waitcnt lgkmcnt(3)\n\t" \
  "ds_read_u16_d16_hi v157, v157 offset:0\n\t" \
  "v_mfma_f32_16x16x16_bf16 v[8:11], v[246:247], v[234:235], 0\n\t" \
  "ds_read_u16_d16_hi v158, v158 offset:0\n\t" \
  "v_mfma_f32_16x16x32_bf16 v[200:203], v[220:223], v[136:139], v[200:203]\n\t" \
  "ds_read_b64_tr_b16 v[220:221], v249 offset:10752\n\t" \
  "v_lshlrev_b32_sdwa v165, 1, v165 dst_sel:DWORD dst_unused:UNUSED_PAD src0_sel:DWORD src1_sel:WORD_1\n\t" \
  "v_rcp_f16_sdwa v226, v226 dst_sel:DWORD dst_unused:UNUSED_PAD src0_sel:WORD_1\n\t" \
  "v_mfma_f32_16x16x32_bf16 v[176:179], v[208:211], v[104:107], v[176:179]\n\t" \
  "s_waitcnt lgkmcnt(4)\n\t" \
  "ds_read_u16_d16_hi v159, v159 offset:0\n\t" \
  "ds_read_b64_tr_b16 v[222:223], v249 offset:11008\n\t" \
  "v_mfma_f32_16x16x16_bf16 v[12:15], v[246:247], v[236:237], 0\n\t" \
  "buffer_store_dwordx4 v[238:241], v253, %[osrd], %[ob] offen offset:128 sc1\n\t" \
  "v_lshlrev_b32_sdwa v166, 1, v166 dst_sel:DWORD dst_unused:UNUSED_PAD src0_sel:DWORD src1_sel:WORD_1\n\t" \
  "v_mfma_f32_16x16x32_bf16 v[184:187], v[208:211], v[116:119], v[184:187]\n\t" \
  "ds_read_u16_d16_hi v164, v164 offset:0\n\t" \
  "v_fma_mix_f32 v172, v172, v225, 0 op_sel_hi:[0,1,0]\n\t" \
  "v_fma_mix_f32 v174, v174, v227, 0 op_sel_hi:[0,1,0]\n\t" \
  "v_mfma_f32_16x16x32_bf16 v[192:195], v[208:211], v[128:131], v[192:195]\n\t" \
  "ds_read_b64 v[238:239], v252 offset:128\n\t" \
  "v_lshlrev_b32_sdwa v167, 1, v167 dst_sel:DWORD dst_unused:UNUSED_PAD src0_sel:DWORD src1_sel:WORD_1\n\t" \
  "v_fma_mix_f32 v173, v173, v224, 0 op_sel_hi:[0,1,0]\n\t" \
  "v_mfma_f32_16x16x32_bf16 v[200:203], v[208:211], v[140:143], v[200:203]\n\t" \
  "s_barrier\n\t" \
  "ds_read_b64_tr_b16 v[208:209], v250 offset:12320\n\t" \
  "ds_read_u16_d16_hi v165, v165 offset:0\n\t" \
  "v_fma_mix_f32 v175, v175, v226, 0 op_sel_hi:[0,1,0]\n\t" \
  "v_mfma_f32_16x16x32_bf16 v[180:183], v[212:215], v[96:99], 0\n\t" \
  "ds_read_b64_tr_b16 v[210:211], v250 offset:13088\n\t" \
  "v_mfma_f32_16x16x32_bf16 v[188:191], v[212:215], v[108:111], 0\n\t" \
  "ds_read_b64 v[240:241], v252 offset:136\n\t" \
  "v_mfma_f32_16x16x32_bf16 v[196:199], v[212:215], v[120:123], 0\n\t" \
  "s_waitcnt lgkmcnt(4)\n\t" \
  "ds_read_u16_d16_hi v166, v166 offset:0\n\t" \
  "v_mfma_f32_16x16x16_bf16 v[16:19], v[246:247], v[238:239], 0\n\t" \
  "ds_read_u16_d16_hi v167, v167 offset:0\n\t" \
  "v_lshlrev_b32_sdwa v176, 1, v176 dst_sel:DWORD dst_unused:UNUSED_PAD src0_sel:DWORD src1_sel:WORD_1\n\t" \
  "v_cvt_pk_f16_f32 v224, v200, v201\n\t" \
  "v_mfma_f32_16x16x32_bf16 v[204:207], v[212:215], v[132:135], 0\n\t" \
  "ds_read_b64_tr_b16 v[212:213], v250 offset:12328\n\t" \
  "v_cvt_pk_f16_f32 v226, v202, v203\n\t" \
  "v_pk_fma_f16 v225, v224, v224, %[h1] neg_lo:[1,0,0] neg_hi:[1,0,0]\n\t" \
  "v_mfma_f32_16x16x32_bf16 v[180:183], v[216:219], v[100:103], v[180:183]\n\t" \
  "s_waitcnt lgkmcnt(3)\n\t" \
  "ds_read_b64_tr_b16 v[214:215], v250 offset:13096\n\t" \
  "v_mfma_f32_16x16x16_bf16 v[20:23], v[246:247], v[240:241], 0\n\t" \
  "v_lshlrev_b32_sdwa v177, 1, v177 dst_sel:DWORD dst_unused:UNUSED_PAD src0_sel:DWORD src1_sel:WORD_1\n\t" \
  "v_mfma_f32_16x16x32_bf16 v[188:191], v[216:219], v[112:115], v[188:191]\n\t" \
  "ds_read_u16_d16_hi v176, v176 offset:0\n\t" \
  "v_pk_fma_f16 v227, v226, v226, %[h1] neg_lo:[1,0,0] neg_hi:[1,0,0]\n\t" \
  "v_pk_mul_f16 v225, v224, v225\n\t" \
  "v_mfma_f32_16x16x32_bf16 v[196:199], v[216:219], v[124:127], v[196:199]\n\t" \
  "v_lshlrev_b32_sdwa v178, 1, v178 dst_sel:DWORD dst_unused:UNUSED_PAD src0_sel:DWORD src1_sel:WORD_1\n\t" \
  "v_pk_mul_f16 v227, v226, v227\n\t" \
  "v_mfma_f32_16x16x32_bf16 v[204:207], v[216:219], v[136:139], v[204:207]\n\t" \
  "ds_read_b64_tr_b16 v[216:217], v250 offset:12384\n\t" \
  "v_exp_f16 v224, v225\n\t" \
  "v_mfma_f32_16x16x32_bf16 v[180:183], v[220:223], v[104:107], v[180:183]\n\t" \
  "ds_read_u16_d16_hi v177, v177 offset:0\n\t" \
  "v_lshlrev_b32_sdwa v179, 1, v179 dst_sel:DWORD dst_unused:UNUSED_PAD src0_sel:DWORD src1_sel:WORD_1\n\t" \
  "v_mfma_f32_16x16x32_bf16 v[188:191], v[220:223], v[116:119], v[188:191]\n\t" \
  "ds_read_b64_tr_b16 v[218:219], v250 offset:13152\n\t" \
  "v_exp_f16 v226, v227\n\t" \
  "v_mfma_f32_16x16x32_bf16 v[196:199], v[220:223], v[128:131], v[196:199]\n\t" \
  "ds_read_u16_d16_hi v178, v178 offset:0\n\t" \
  "v_lshlrev_b32_sdwa v184, 1, v184 dst_sel:DWORD dst_unused:UNUSED_PAD src0_sel:DWORD src1_sel:WORD_1\n\t" \
  "v_mfma_f32_16x16x32_bf16 v[204:207], v[220:223], v[140:143], v[204:207]\n\t" \
  "ds_read_b64_tr_b16 v[220:221], v250 offset:12392\n\t" \
  "v_exp_f16_sdwa v224, v225 dst_sel:WORD_1 dst_unused:UNUSED_PRESERVE src0_sel:WORD_1\n\t" \
  "v_smfmac_f32_16x16x64_bf16 v[0:3], v[208:211], v[144:151], v248\n\t" \
  "ds_read_u16_d16_hi v179, v179 offset:0\n\t" \
  "v_lshlrev_b32_sdwa v185, 1, v185 dst_sel:DWORD dst_unused:UNUSED_PAD src0_sel:DWORD src1_sel:WORD_1\n\t" \
  "v_exp_f16_sdwa v226, v227 dst_sel:WORD_1 dst_unused:UNUSED_PRESERVE src0_sel:WORD_1\n\t" \
  "v_smfmac_f32_16x16x64_bf16 v[24:27], v[208:211], v[152:159], v248\n\t" \
  "s_waitcnt lgkmcnt(9)\n\t" \
  "ds_read_b64_tr_b16 v[222:223], v250 offset:13160\n\t" \
  "v_smfmac_f32_16x16x64_bf16 v[48:51], v[208:211], v[160:167], v248\n\t" \
  "ds_read_u16_d16_hi v184, v184 offset:0\n\t" \
  "v_lshlrev_b32_sdwa v186, 1, v186 dst_sel:DWORD dst_unused:UNUSED_PAD src0_sel:DWORD src1_sel:WORD_1\n\t" \
  "v_pk_fma_f16 v224, v224, %[hc], %[hc]\n\t" \
  "v_smfmac_f32_16x16x64_bf16 v[72:75], v[208:211], v[168:175], v248\n\t" \
  "s_waitcnt lgkmcnt(9)\n\t" \
  "ds_read_b64_tr_b16 v[208:209], v250 offset:12448\n\t" \
  "v_pk_fma_f16 v226, v226, %[hc], %[hc]\n\t" \
  "v_rcp_f16 v225, v224\n\t" \
  "v_smfmac_f32_16x16x64_bf16 v[4:7], v[212:215], v[144:151], v248\n\t" \
  "ds_read_u16_d16_hi v185, v185 offset:0\n\t" \
  "v_smfmac_f32_16x16x64_bf16 v[28:31], v[212:215], v[152:159], v248\n\t" \
  "s_add_u32 %[ob], %[ob], %[dob]\n\t" \
  "ds_read_b64_tr_b16 v[210:211], v250 offset:13216\n\t" \
  "v_lshlrev_b32_sdwa v187, 1, v187 dst_sel:DWORD dst_unused:UNUSED_PAD src0_sel:DWORD src1_sel:WORD_1\n\t" \
  "v_rcp_f16 v227, v226\n\t" \
  "v_smfmac_f32_16x16x64_bf16 v[52:55], v[212:215], v[160:167], v248\n\t" \
  "ds_read_u16_d16_hi v186, v186 offset:0\n\t" \
  "v_lshlrev_b32_sdwa v192, 1, v192 dst_sel:DWORD dst_unused:UNUSED_PAD src0_sel:DWORD src1_sel:WORD_1\n\t" \
  "v_smfmac_f32_16x16x64_bf16 v[76:79], v[212:215], v[168:175], v248\n\t" \
  "s_waitcnt lgkmcnt(9)\n\t" \
  "ds_read_b64_tr_b16 v[212:213], v250 offset:12456\n\t" \
  "v_rcp_f16_sdwa v224, v224 dst_sel:DWORD dst_unused:UNUSED_PAD src0_sel:WORD_1\n\t" \
  "v_smfmac_f32_16x16x64_bf16 v[8:11], v[216:219], v[144:151], v248\n\t" \
  "s_add_u32 %[rs], %[rs], 1\n\t" \
  "ds_read_u16_d16_hi v187, v187 offset:0\n\t" \
  "v_lshlrev_b32_sdwa v193, 1, v193 dst_sel:DWORD dst_unused:UNUSED_PAD src0_sel:DWORD src1_sel:WORD_1\n\t" \
  "v_smfmac_f32_16x16x64_bf16 v[32:35], v[216:219], v[152:159], v248\n\t" \
  "s_cmp_eq_u32 %[rs], 6\n\t" \
  "ds_read_b64_tr_b16 v[214:215], v250 offset:13224\n\t" \
  "v_rcp_f16_sdwa v226, v226 dst_sel:DWORD dst_unused:UNUSED_PAD src0_sel:WORD_1\n\t" \
  "v_smfmac_f32_16x16x64_bf16 v[56:59], v[216:219], v[160:167], v248\n\t" \
  "ds_read_u16_d16_hi v192, v192 offset:0\n\t" \
  "v_lshlrev_b32_sdwa v194, 1, v194 dst_sel:DWORD dst_unused:UNUSED_PAD src0_sel:DWORD src1_sel:WORD_1\n\t" \
  "v_smfmac_f32_16x16x64_bf16 v[80:83], v[216:219], v[168:175], v248\n\t" \
  "s_waitcnt lgkmcnt(9)\n\t" \
  "ds_read_b64_tr_b16 v[216:217], v249 offset:12288\n\t" \
  "v_fma_mix_f32 v200, v200, v225, 0 op_sel_hi:[0,1,0]\n\t" \
  "v_fma_mix_f32 v202, v202, v227, 0 op_sel_hi:[0,1,0]\n\t" \
  "v_smfmac_f32_16x16x64_bf16 v[12:15], v[220:223], v[144:151], v248\n\t" \
  "s_cselect_b32 %[rs], 0, %[rs]\n\t" \
  "ds_read_u16_d16_hi v193, v193 offset:0\n\t" \
  "v_lshlrev_b32_sdwa v195, 1, v195 dst_sel:DWORD dst_unused:UNUSED_PAD src0_sel:DWORD src1_sel:WORD_1\n\t" \
  "v_fma_mix_f32 v201, v201, v224, 0 op_sel_hi:[0,1,0]\n\t" \
  "v_smfmac_f32_16x16x64_bf16 v[36:39], v[220:223], v[152:159], v248\n\t" \
  "s_cselect_b32 %[t0], -75920, %[ps]\n\t" \
  "ds_read_b64_tr_b16 v[218:219], v249 offset:12544\n\t" \
  "v_fma_mix_f32 v203, v203, v226, 0 op_sel_hi:[0,1,0]\n\t" \
  "v_smfmac_f32_16x16x64_bf16 v[60:63], v[220:223], v[160:167], v248\n\t" \
  "ds_read_u16_d16_hi v194, v194 offset:0\n\t" \
  "v_lshlrev_b32_sdwa v180, 1, v180 dst_sel:DWORD dst_unused:UNUSED_PAD src0_sel:DWORD src1_sel:WORD_1\n\t" \
  "v_cvt_pk_f16_f32 v224, v204, v205\n\t" \
  "v_add_u32 v252, %[t0], v252\n\t" \
  "v_smfmac_f32_16x16x64_bf16 v[84:87], v[220:223], v[168:175], v248\n\t" \
  "s_waitcnt lgkmcnt(9)\n\t" \
  "ds_read_b64_tr_b16 v[220:221], v249 offset:14336\n\t" \
  "v_cvt_pk_f16_f32 v226, v206, v207\n\t" \
  "v_pk_fma_f16 v225, v224, v224, %[h1] neg_lo:[1,0,0] neg_hi:[1,0,0]\n\t" \
  "v_smfmac_f32_16x16x64_bf16 v[16:19], v[208:211], v[144:151], v248\n\t" \
  "ds_read_u16_d16_hi v195, v195 offset:0\n\t" \
  "v_lshlrev_b32_sdwa v181, 1, v181 dst_sel:DWORD dst_unused:UNUSED_PAD src0_sel:DWORD src1_sel:WORD_1\n\t" \
  "v_pk_fma_f16 v227, v226, v226, %[h1] neg_lo:[1,0,0] neg_hi:[1,0,0]\n\t" \
  "v_smfmac_f32_16x16x64_bf16 v[40:43], v[208:211], v[152:159], v248\n\t" \
  "ds_read_b64_tr_b16 v[222:223], v249 offset:14592\n\t" \
  "v_pk_mul_f16 v225, v224, v225\n\t" \
  "v_lshlrev_b32_sdwa v182, 1, v182 dst_sel:DWORD dst_unused:UNUSED_PAD src0_sel:DWORD src1_sel:WORD_1\n\t" \
  "v_smfmac_f32_16x16x64_bf16 v[64:67], v[208:211], v[160:167], v248\n\t" \
  "ds_read_u16_d16_hi v180, v180 offset:0\n\t" \
  "v_pk_mul_f16 v227, v226, v227\n\t" \
  "v_exp_f16 v224, v225\n\t" \
  "v_smfmac_f32_16x16x64_bf16 v[88:91], v[208:211], v[168:175], v248\n\t" \
  "s_waitcnt lgkmcnt(9)\n\t" \
  "ds_read_b64_tr_b16 v[208:209], v249 offset:16384\n\t" \
  "v_smfmac_f32_16x16x64_bf16 v[20:23], v[212:215], v[144:151], v248\n\t" \
  "ds_read_u16_d16_hi v181, v181 offset:0\n\t" \
  "v_lshlrev_b32_sdwa v183, 1, v183 dst_sel:DWORD dst_unused:UNUSED_PAD src0_sel:DWORD src1_sel:WORD_1\n\t" \
  "v_exp_f16 v226, v227\n\t" \
  "v_smfmac_f32_16x16x64_bf16 v[44:47], v[212:215], v[152:159], v248\n\t" \
  "ds_read_b64_tr_b16 v[210:211], v249 offset:16640\n\t" \
  "v_lshlrev_b32_sdwa v188, 1, v188 dst_sel:DWORD dst_unused:UNUSED_PAD src0_sel:DWORD src1_sel:WORD_1\n\t" \
  "v_smfmac_f32_16x16x64_bf16 v[68:71], v[212:215], v[160:167], v248\n\t" \
  "ds_read_u16_d16_hi v182, v182 offset:0\n\t" \
  "v_exp_f16_sdwa v224, v225 dst_sel:WORD_1 dst_unused:UNUSED_PRESERVE src0_sel:WORD_1\n\t" \
  "v_smfmac_f32_16x16x64_bf16 v[92:95], v[212:215], v[168:175], v248\n\t" \
  "s_waitcnt lgkmcnt(9)\n\t" \
  "ds_read_b64_tr_b16 v[212:213], v249 offset:12800\n\t" \
  "v_lshlrev_b32_sdwa v189, 1, v189 dst_sel:DWORD dst_unused:UNUSED_PAD src0_sel:DWORD src1_sel:WORD_1\n\t" \
  "v_exp_f16_sdwa v226, v227 dst_sel:WORD_1 dst_unused:UNUSED_PRESERVE src0_sel:WORD_1\n\t" \
  "v_mfma_f32_16x16x32_bf16 v[144:147], v[216:219], v[96:99], 0\n\t" \
  "ds_read_u16_d16_hi v183, v183 offset:0\n\t" \
  "v_mfma_f32_16x16x32_bf16 v[152:155], v[216:219], v[108:111], 0\n\t" \
  "ds_read_b64_tr_b16 v[214:215], v249 offset:13056\n\t" \
  "v_lshlrev_b32_sdwa v190, 1, v190 dst_sel:DWORD dst_unused:UNUSED_PAD src0_sel:DWORD src1_sel:WORD_1\n\t" \
  "v_pk_fma_f16 v224, v224, %[hc], %[hc]\n\t" \
  "v_mfma_f32_16x16x32_bf16 v[160:163], v[216:219], v[120:123], 0\n\t" \
  "ds_read_u16_d16_hi v188, v188 offset:0\n\t" \
  "v_pk_fma_f16 v226, v226, %[hc], %[hc]\n\t" \
  "v_rcp_f16 v225, v224\n\t" \
  "v_mfma_f32_16x16x32_bf16 v[168:171], v[216:219], v[132:135], 0\n\t" \
  "s_waitcnt lgkmcnt(9)\n\t" \
  "ds_read_b64_tr_b16 v[216:217], v249 offset:14848\n\t" \
  "v_lshlrev_b32_sdwa v191, 1, v191 dst_sel:DWORD dst_unused:UNUSED_PAD src0_sel:DWORD src1_sel:WORD_1\n\t" \
  "v_mfma_f32_16x16x32_bf16 v[144:147], v[220:223], v[100:103], v[144:147]\n\t" \
  "ds_read_u16_d16_hi v189, v189 offset:0\n\t" \
  "v_rcp_f16 v227, v226\n\t" \
  "v_mfma_f32_16x16x32_bf16 v[152:155], v[220:223], v[112:115], v[152:155]\n\t" \
  "ds_read_b64_tr_b16 v[218:219], v249 offset:15104\n\t" \
  "v_lshlrev_b32_sdwa v196, 1, v196 dst_sel:DWORD dst_unused:UNUSED_PAD src0_sel:DWORD src1_sel:WORD_1\n\t" \
  "v_rcp_f16_sdwa v224, v224 dst_sel:DWORD dst_unused:UNUSED_PAD src0_sel:WORD_1\n\t" \
  "v_mfma_f32_16x16x32_bf16 v[160:163], v[220:223], v[124:127], v[160:163]\n\t" \
  "ds_read_u16_d16_hi v190, v190 offset:0\n\t" \
  "v_mfma_f32_16x16x32_bf16 v[168:171], v[220:223], v[136:139], v[168:171]\n\t" \
  "s_waitcnt lgkmcnt(9)\n\t" \
  "ds_read_b64_tr_b16 v[220:221], v249 offset:16896\n\t" \
  "v_lshlrev_b32_sdwa v197, 1, v197 dst_sel:DWORD dst_unused:UNUSED_PAD src0_sel:DWORD src1_sel:WORD_1\n\t" \
  "v_rcp_f16_sdwa v226, v226 dst_sel:DWORD dst_unused:UNUSED_PAD src0_sel:WORD_1\n\t" \
  "v_mfma_f32_16x16x32_bf16 v[144:147], v[208:211], v[104:107], v[144:147]\n\t" \
  "ds_read_u16_d16_hi v191, v191 offset:0\n\t" \
  "v_lshlrev_b32_sdwa v198, 1, v198 dst_sel:DWORD dst_unused:UNUSED_PAD src0_sel:DWORD src1_sel:WORD_1\n\t" \
  "v_mfma_f32_16x16x32_bf16 v[152:155], v[208:211], v[116:119], v[152:155]\n\t" \
  "ds_read_b64_tr_b16 v[222:223], v249 offset:17152\n\t" \
  "v_fma_mix_f32 v204, v204, v225, 0 op_sel_hi:[0,1,0]\n\t" \
  "v_fma_mix_f32 v206, v206, v227, 0 op_sel_hi:[0,1,0]\n\t" \
  "v_mfma_f32_16x16x32_bf16 v[160:163], v[208:211], v[128:131], v[160:163]\n\t" \
  "ds_read_u16_d16_hi v196, v196 offset:0\n\t" \
  "v_lshlrev_b32_sdwa v199, 1, v199 dst_sel:DWORD dst_unused:UNUSED_PAD src0_sel:DWORD src1_sel:WORD_1\n\t" \
  "v_fma_mix_f32 v205, v205, v224, 0 op_sel_hi:[0,1,0]\n\t" \
  "v_mfma_f32_16x16x32_bf16 v[168:171], v[208:211], v[140:143], v[168:171]\n\t" \
  "s_barrier\n\t" \
  "ds_read_b64_tr_b16 v[208:209], v250 offset:0\n\t" \
  "v_fma_mix_f32 v207, v207, v226, 0 op_sel_hi:[0,1,0]\n\t" \
  "s_waitcnt lgkmcnt(10)\n\t" \
  "v_mfma_f32_16x16x32_bf16 v[148:151], v[212:215], v[96:99], 0\n\t" \
  "ds_read_u16_d16_hi v197, v197 offset:0\n\t" \
  "v_mfma_f32_16x16x32_bf16 v[156:159], v[212:215], v[108:111], 0\n\t" \
  "ds_read_b64_tr_b16 v[210:211], v250 offset:768\n\t" \
  "v_mfma_f32_16x16x32_bf16 v[164:167], v[212:215], v[120:123], 0\n\t" \
  "ds_read_u16_d16_hi v198, v198 offset:0\n\t" \
  "v_lshlrev_b32_sdwa v144, 1, v144 dst_sel:DWORD dst_unused:UNUSED_PAD src0_sel:DWORD src1_sel:WORD_1\n\t" \
  "v_cvt_pk_f16_f32 v224, v168, v169\n\t" \
  "v_mfma_f32_16x16x32_bf16 v[172:175], v[212:215], v[132:135], 0\n\t" \
  "s_waitcnt lgkmcnt(9)\n\t" \
  "ds_read_b64_tr_b16 v[212:213], v250 offset:8\n\t" \
  "v_cvt_pk_f16_f32 v226, v170, v171\n\t" \
  "v_pk_fma_f16 v225, v224, v224, %[h1] neg_lo:[1,0,0] neg_hi:[1,0,0]\n\t" \
  "v_mfma_f32_16x16x32_bf16 v[148:151], v[216:219], v[100:103], v[148:151]\n\t" \
  "ds_read_u16_d16_hi v199, v199 offset:0\n\t" \
  "v_lshlrev_b32_sdwa v145, 1, v145 dst_sel:DWORD dst_unused:UNUSED_PAD src0_sel:DWORD src1_sel:WORD_1\n\t" \
  "v_mfma_f32_16x16x32_bf16 v[156:159], v[216:219], v[112:115], v[156:159]\n\t" \
  "ds_read_b64_tr_b16 v[214:215], v250 offset:776\n\t" \
  "v_pk_fma_f16 v227, v226, v226, %[h1] neg_lo:[1,0,0] neg_hi:[1,0,0]\n\t" \
  "v_pk_mul_f16 v225, v224, v225\n\t" \
  "v_mfma_f32_16x16x32_bf16 v[164:167], v[216:219], v[124:127], v[164:167]\n\t" \
  "ds_read_u16_d16_hi v144, v144 offset:0\n\t" \
  "v_lshlrev_b32_sdwa v146, 1, v146 dst_sel:DWORD dst_unused:UNUSED_PAD src0_sel:DWORD src1_sel:WORD_1\n\t" \
  "v_pk_mul_f16 v227, v226, v227\n\t" \
  "v_mfma_f32_16x16x32_bf16 v[172:175], v[216:219], v[136:139], v[172:175]\n\t" \
  "s_waitcnt lgkmcnt(9)\n\t" \
  "ds_read_b64_tr_b16 v[216:217], v250 offset:64\n\t" \
  "v_exp_f16 v224, v225\n\t" \
  "v_mfma_f32_16x16x32_bf16 v[148:151], v[220:223], v[104:107], v[148:151]\n\t" \
  "ds_read_u16_d16_hi v145, v145 offset:0\n\t" \
  "v_lshlrev_b32_sdwa v147, 1, v147 dst_sel:DWORD dst_unused:UNUSED_PAD src0_sel:DWORD src1_sel:WORD_1\n\t" \
  "v_mfma_f32_16x16x32_bf16 v[156:159], v[220:223], v[116:119], v[156:159]\n\t" \
  "ds_read_b64_tr_b16 v[218:219], v250 offset:832\n\t" \
  "v_exp_f16 v226, v227\n\t" \
  "v_mfma_f32_16x16x32_bf16 v[164:167], v[220:223], v[128:131], v[164:167]\n\t" \
  "ds_read_u16_d16_hi v146, v146 offset:0\n\t" \
  "v_lshlrev_b32_sdwa v152, 1, v152 dst_sel:DWORD dst_unused:UNUSED_PAD src0_sel:DWORD src1_sel:WORD_1\n\t" \
  "v_mfma_f32_16x16x32_bf16 v[172:175], v[220:223], v[140:143], v[172:175]\n\t" \
  "s_waitcnt lgkmcnt(9)\n\t" \
  "ds_read_b64_tr_b16 v[220:221], v250 offset:72\n\t" \
  "v_exp_f16_sdwa v224, v225 dst_sel:WORD_1 dst_unused:UNUSED_PRESERVE src0_sel:WORD_1\n\t" \
  "v_smfmac_f32_16x16x64_bf16 v[0:3], v[208:211], v[176:183], v248\n\t" \
  "ds_read_u16_d16_hi v147, v147 offset:0\n\t" \
  "v_lshlrev_b32_sdwa v153, 1, v153 dst_sel:DWORD dst_unused:UNUSED_PAD src0_sel:DWORD src1_sel:WORD_1\n\t" \
  "v_exp_f16_sdwa v226, v227 dst_sel:WORD_1 dst_unused:UNUSED_PRESERVE src0_sel:WORD_1\n\t" \
  "v_smfmac_f32_16x16x64_bf16 v[24:27], v[208:211], v[184:191], v248\n\t" \
  "s_waitcnt lgkmcnt(8)\n\t" \
  "ds_read_b64_tr_b16 v[222:223], v250 offset:840\n\t" \
  "v_smfmac_f32_16x16x64_bf16 v[48:51], v[208:211], v[192:199], v248\n\t" \
  "ds_read_u16_d16_hi v152, v152 offset:0\n\t" \
  "v_lshlrev_b32_sdwa v154, 1, v154 dst_sel:DWORD dst_unused:UNUSED_PAD src0_sel:DWORD src1_sel:WORD_1\n\t" \
  "v_pk_fma_f16 v224, v224, %[hc], %[hc]\n\t" \
  "v_smfmac_f32_16x16x64_bf16 v[72:75], v[208:211], v[200:207], v248\n\t" \
  "s_waitcnt lgkmcnt(9)\n\t" \
  "ds_read_b64_tr_b16 v[208:209], v250 offset:128\n\t" \
  "v_pk_fma_f16 v226, v226, %[hc], %[hc]\n\t" \
  "v_rcp_f16 v225, v224\n\t" \
  "v_smfmac_f32_16x16x64_bf16 v[4:7], v[212:215], v[176:183], v248\n\t" \
  "ds_read_u16_d16_hi v153, v153 offset:0\n\t" \
  "v_smfmac_f32_16x16x64_bf16 v[28:31], v[212:215], v[184:191], v248\n\t" \
  "ds_read_b64_tr_b16 v[210:211], v250 offset:896\n\t" \
  "v_lshlrev_b32_sdwa v155, 1, v155 dst_sel:DWORD dst_unused:UNUSED_PAD src0_sel:DWORD src1_sel:WORD_1\n\t" \
  "v_rcp_f16 v227, v226\n\t" \
  "v_smfmac_f32_16x16x64_bf16 v[52:55], v[212:215], v[192:199], v248\n\t" \
  "ds_read_b128 v[108:111], v251 offset:0\n\t" \
  "v_lshlrev_b32_sdwa v160, 1, v160 dst_sel:DWORD dst_unused:UNUSED_PAD src0_sel:DWORD src1_sel:WORD_1\n\t" \
  "v_smfmac_f32_16x16x64_bf16 v[76:79], v[212:215], v[200:207], v248\n\t" \
  "s_waitcnt lgkmcnt(9)\n\t" \
  "ds_read_b64_tr_b16 v[212:213], v250 offset:136\n\t" \
  "v_rcp_f16_sdwa v224, v224 dst_sel:DWORD dst_unused:UNUSED_PAD src0_sel:WORD_1\n\t" \
  "v_smfmac_f32_16x16x64_bf16 v[8:11], v[216:219], v[176:183], v248\n\t" \
  "ds_read_u16_d16_hi v154, v154 offset:0\n\t" \
  "v_lshlrev_b32_sdwa v161, 1, v161 dst_sel:DWORD dst_unused:UNUSED_PAD src0_sel:DWORD src1_sel:WORD_1\n\t" \
  "v_smfmac_f32_16x16x64_bf16 v[32:35], v[216:219], v[184:191], v248\n\t" \
  "ds_read_b64_tr_b16 v[214:215], v250 offset:904\n\t" \
  "v_rcp_f16_sdwa v226, v226 dst_sel:DWORD dst_unused:UNUSED_PAD src0_sel:WORD_1\n\t" \
  "v_smfmac_f32_16x16x64_bf16 v[56:59], v[216:219], v[192:199], v248\n\t" \
  "ds_read_u16_d16_hi v155, v155 offset:0\n\t" \
  "v_lshlrev_b32_sdwa v162, 1, v162 dst_sel:DWORD dst_unused:UNUSED_PAD src0_sel:DWORD src1_sel:WORD_1\n\t" \
  "v_smfmac_f32_16x16x64_bf16 v[80:83], v[216:219], v[200:207], v248\n\t" \
  "s_waitcnt lgkmcnt(9)\n\t" \
  "ds_read_b64_tr_b16 v[216:217], v249 offset:18432\n\t" \
  "ds_read_b128 v[112:115], v251 offset:64\n\t" \
  "v_fma_mix_f32 v168, v168, v225, 0 op_sel_hi:[0,1,0]\n\t" \
  "v_fma_mix_f32 v170, v170, v227, 0 op_sel_hi:[0,1,0]\n\t" \
  "v_smfmac_f32_16x16x64_bf16 v[12:15], v[220:223], v[176:183], v248\n\t" \
  "ds_read_b64_tr_b16 v[218:219], v249 offset:18688\n\t" \
  "v_lshlrev_b32_sdwa v163, 1, v163 dst_sel:DWORD dst_unused:UNUSED_PAD src0_sel:DWORD src1_sel:WORD_1\n\t" \
  "v_fma_mix_f32 v169, v169, v224, 0 op_sel_hi:[0,1,0]\n\t" \
  "v_smfmac_f32_16x16x64_bf16 v[36:39], v[220:223], v[184:191], v248\n\t" \
  "ds_read_u16_d16_hi v160, v160 offset:0\n\t" \
  "v_fma_mix_f32 v171, v171, v226, 0 op_sel_hi:[0,1,0]\n\t" \
  "v_smfmac_f32_16x16x64_bf16 v[60:63], v[220:223], v[192:199], v248\n\t" \
  "ds_read_u16_d16_hi v161, v161 offset:0\n\t" \
  "v_lshlrev_b32_sdwa v148, 1, v148 dst_sel:DWORD dst_unused:UNUSED_PAD src0_sel:DWORD src1_sel:WORD_1\n\t" \
  "v_cvt_pk_f16_f32 v224, v172, v173\n\t" \
  "v_smfmac_f32_16x16x64_bf16 v[84:87], v[220:223], v[200:207], v248\n\t" \
  "s_waitcnt lgkmcnt(10)\n\t" \
  "ds_read_b64_tr_b16 v[220:221], v249 offset:20480\n\t" \
  "ds_read_u16_d16_hi v162, v162 offset:0\n\t" \
  "v_cvt_pk_f16_f32 v226, v174, v175\n\t" \
  "v_pk_fma_f16 v225, v224, v224, %[h1] neg_lo:[1,0,0] neg_hi:[1,0,0]\n\t" \
  "v_smfmac_f32_16x16x64_bf16 v[16:19], v[208:211], v[176:183], v248\n\t" \
  "ds_read_b64_tr_b16 v[222:223], v249 offset:20736\n\t" \
  "v_lshlrev_b32_sdwa v149, 1, v149 dst_sel:DWORD dst_unused:UNUSED_PAD src0_sel:DWORD src1_sel:WORD_1\n\t" \
  "v_pk_fma_f16 v227, v226, v226, %[h1] neg_lo:[1,0,0] neg_hi:[1,0,0]\n\t" \
  "v_smfmac_f32_16x16x64_bf16 v[40:43], v[208:211], v[184:191], v248\n\t" \
  "ds_read_b128 v[116:119], v251 offset:128\n\t" \
  "v_pk_mul_f16 v225, v224, v225\n\t" \
  "v_lshlrev_b32_sdwa v150, 1, v150 dst_sel:DWORD dst_unused:UNUSED_PAD src0_sel:DWORD src1_sel:WORD_1\n\t" \
  "v_smfmac_f32_16x16x64_bf16 v[64:67], v[208:211], v[192:199], v248\n\t" \
  "ds_read_u16_d16_hi v163, v163 offset:0\n\t" \
  "v_pk_mul_f16 v227, v226, v227\n\t" \
  "v_exp_f16 v224, v225\n\t" \
  "v_smfmac_f32_16x16x64_bf16 v[88:91], v[208:211], v[200:207], v248\n\t" \
  "s_waitcnt lgkmcnt(11)\n\t" \
  "ds_read_b64_tr_b16 v[208:209], v249 offset:22528\n\t" \
  "v_smfmac_f32_16x16x64_bf16 v[20:23], v[212:215], v[176:183], v248\n\t" \
  "ds_read_u16_d16_hi v148, v148 offset:0\n\t" \
  "v_lshlrev_b32_sdwa v151, 1, v151 dst_sel:DWORD dst_unused:UNUSED_PAD src0_sel:DWORD src1_sel:WORD_1\n\t" \
  "v_exp_f16 v226, v227\n\t" \
  "v_smfmac_f32_16x16x64_bf16 v[44:47], v[212:215], v[184:191], v248\n\t" \
  "ds_read_b64_tr_b16 v[210:211], v249 offset:22784\n\t" \
  "v_lshlrev_b32_sdwa v156, 1, v156 dst_sel:DWORD dst_unused:UNUSED_PAD src0_sel:DWORD src1_sel:WORD_1\n\t" \
  "v_smfmac_f32_16x16x64_bf16 v[68:71], v[212:215], v[192:199], v248\n\t" \
  "ds_read_u16_d16_hi v149, v149 offset:0\n\t" \
  "v_exp_f16_sdwa v224, v225 dst_sel:WORD_1 dst_unused:UNUSED_PRESERVE src0_sel:WORD_1\n\t" \
  "v_smfmac_f32_16x16x64_bf16 v[92:95], v[212:215], v[200:207], v248\n\t" \
  "s_waitcnt lgkmcnt(11)\n\t" \
  "ds_read_b64_tr_b16 v[212:213], v249 offset:18944\n\t" \
  "v_lshlrev_b32_sdwa v157, 1, v157 dst_sel:DWORD dst_unused:UNUSED_PAD src0_sel:DWORD src1_sel:WORD_1\n\t" \
  "v_exp_f16_sdwa v226, v227 dst_sel:WORD_1 dst_unused:UNUSED_PRESERVE src0_sel:WORD_1\n\t" \
  "v_mfma_f32_16x16x32_bf16 v[176:179], v[216:219], v[96:99], 0\n\t" \
  "ds_read_u16_d16_hi v150, v150 offset:0\n\t" \
  "v_mfma_f32_16x16x32_bf16 v[184:187], v[216:219], v[108:111], 0\n\t" \
  "ds_read_b64_tr_b16 v[214:215], v249 offset:19200\n\t" \
  "v_lshlrev_b32_sdwa v158, 1, v158 dst_sel:DWORD dst_unused:UNUSED_PAD src0_sel:DWORD src1_sel:WORD_1\n\t" \
  "v_pk_fma_f16 v224, v224, %[hc], %[hc]\n\t" \
  "v_mfma_f32_16x16x32_bf16 v[192:195], v[216:219], v[120:123], 0\n\t" \
  "ds_read_u16_d16_hi v151, v151 offset:0\n\t" \
  "v_pk_fma_f16 v226, v226, %[hc], %[hc]\n\t" \
  "v_rcp_f16 v225, v224\n\t" \
  "v_mfma_f32_16x16x32_bf16 v[200:203], v[216:219], v[132:135], 0\n\t" \
  "s_waitcnt lgkmcnt(10)\n\t" \
  "ds_read_b64_tr_b16 v[216:217], v249 offset:20992\n\t" \
  "v_lshlrev_b32_sdwa v159, 1, v159 dst_sel:DWORD dst_unused:UNUSED_PAD src0_sel:DWORD src1_sel:WORD_1\n\t" \
  "v_mfma_f32_16x16x32_bf16 v[176:179], v[220:223], v[100:103], v[176:179]\n\t" \
  "ds_read_u16_d16_hi v156, v156 offset:0\n\t" \
  "v_rcp_f16 v227, v226\n\t" \
  "v_mfma_f32_16x16x32_bf16 v[184:187], v[220:223], v[112:115], v[184:187]\n\t" \
  "ds_read_b64_tr_b16 v[218:219], v249 offset:21248\n\t" \
  "v_lshlrev_b32_sdwa v164, 1, v164 dst_sel:DWORD dst_unused:UNUSED_PAD src0_sel:DWORD src1_sel:WORD_1\n\t" \
  "v_rcp_f16_sdwa v224, v224 dst_sel:DWORD dst_unused:UNUSED_PAD src0_sel:WORD_1\n\t" \
  "v_mfma_f32_16x16x32_bf16 v[192:195], v[220:223], v[124:127], v[192:195]\n\t" \
  "ds_read_u16_d16_hi v157, v157 offset:0\n\t" \
  "v_mfma_f32_16x16x32_bf16 v[200:203], v[220:223], v[136:139], v[200:203]\n\t" \
  "s_waitcnt lgkmcnt(9)\n\t" \
  "ds_read_b64_tr_b16 v[220:221], v249 offset:23040\n\t" \
  "v_lshlrev_b32_sdwa v165, 1, v165 dst_sel:DWORD dst_unused:UNUSED_PAD src0_sel:DWORD src1_sel:WORD_1\n\t" \
  "v_rcp_f16_sdwa v226, v226 dst_sel:DWORD dst_unused:UNUSED_PAD src0_sel:WORD_1\n\t" \
  "v_mfma_f32_16x16x32_bf16 v[176:179], v[208:211], v[104:107], v[176:179]\n\t" \
  "ds_read_u16_d16_hi v158, v158 offset:0\n\t" \
  "v_lshlrev_b32_sdwa v166, 1, v166 dst_sel:DWORD dst_unused:UNUSED_PAD src0_sel:DWORD src1_sel:WORD_1\n\t" \
  "v_mfma_f32_16x16x32_bf16 v[184:187], v[208:211], v[116:119], v[184:187]\n\t" \
  "ds_read_b64_tr_b16 v[222:223], v249 offset:23296\n\t" \
  "v_fma_mix_f32 v172, v172, v225, 0 op_sel_hi:[0,1,0]\n\t" \
  "v_fma_mix_f32 v174, v174, v227, 0 op_sel_hi:[0,1,0]\n\t" \
  "v_mfma_f32_16x16x32_bf16 v[192:195], v[208:211], v[128:131], v[192:195]\n\t" \
  "ds_read_u16_d16_hi v159, v159 offset:0\n\t" \
  "v_lshlrev_b32_sdwa v167, 1, v167 dst_sel:DWORD dst_unused:UNUSED_PAD src0_sel:DWORD src1_sel:WORD_1\n\t" \
  "v_fma_mix_f32 v173, v173, v224, 0 op_sel_hi:[0,1,0]\n\t" \
  "v_mfma_f32_16x16x32_bf16 v[200:203], v[208:211], v[140:143], v[200:203]\n\t" \
  "s_barrier\n\t" \
  "ds_read_b64_tr_b16 v[208:209], v250 offset:6160\n\t" \
  "ds_read_u16_d16_hi v164, v164 offset:0\n\t" \
  "v_fma_mix_f32 v175, v175, v226, 0 op_sel_hi:[0,1,0]\n\t" \
  "s_waitcnt lgkmcnt(11)\n\t" \
  "v_mfma_f32_16x16x32_bf16 v[180:183], v[212:215], v[96:99], 0\n\t" \
  "ds_read_b64_tr_b16 v[210:211], v250 offset:6928\n\t" \
  "v_mfma_f32_16x16x32_bf16 v[188:191], v[212:215], v[108:111], 0\n\t" \
  "ds_read_u16_d16_hi v165, v165 offset:0\n\t" \
  "v_mfma_f32_16x16x32_bf16 v[196:199], v[212:215], v[120:123], 0\n\t" \
  "ds_read_u16_d16_hi v166, v166 offset:0\n\t" \
  "v_lshlrev_b32_sdwa v176, 1, v176 dst_sel:DWORD dst_unused:UNUSED_PAD src0_sel:DWORD src1_sel:WORD_1\n\t" \
  "v_cvt_pk_f16_f32 v224, v200, v201\n\t" \
  "v_mfma_f32_16x16x32_bf16 v[204:207], v[212:215], v[132:135], 0\n\t" \
  "s_waitcnt lgkmcnt(10)\n\t" \
  "ds_read_b64_tr_b16 v[212:213], v250 offset:6168\n\t" \
  "v_cvt_pk_f16_f32 v226, v202, v203\n\t" \
  "v_pk_fma_f16 v225, v224, v224, %[h1] neg_lo:[1,0,0] neg_hi:[1,0,0]\n\t" \
  "v_mfma_f32_16x16x32_bf16 v[180:183], v[216:219], v[100:103], v[180:183]\n\t" \
  "ds_read_u16_d16_hi v167, v167 offset:0\n\t" \
  "v_lshlrev_b32_sdwa v177, 1, v177 dst_sel:DWORD dst_unused:UNUSED_PAD src0_sel:DWORD src1_sel:WORD_1\n\t" \
  "v_mfma_f32_16x16x32_bf16 v[188:191], v[216:219], v[112:115], v[188:191]\n\t" \
  "ds_read_b64_tr_b16 v[214:215], v250 offset:6936\n\t" \
  "v_pk_fma_f16 v227, v226, v226, %[h1] neg_lo:[1,0,0] neg_hi:[1,0,0]\n\t" \
  "v_pk_mul_f16 v225, v224, v225\n\t" \
  "v_mfma_f32_16x16x32_bf16 v[196:199], v[216:219], v[124:127], v[196:199]\n\t" \
  "ds_read_u16_d16_hi v176, v176 offset:0\n\t" \
  "v_lshlrev_b32_sdwa v178, 1, v178 dst_sel:DWORD dst_unused:UNUSED_PAD src0_sel:DWORD src1_sel:WORD_1\n\t" \
  "v_pk_mul_f16 v227, v226, v227\n\t" \
  "v_mfma_f32_16x16x32_bf16 v[204:207], v[216:219], v[136:139], v[204:207]\n\t" \
  "s_waitcnt lgkmcnt(10)\n\t" \
  "ds_read_b64_tr_b16 v[216:217], v250 offset:6224\n\t" \
  "v_exp_f16 v224, v225\n\t" \
  "v_mfma_f32_16x16x32_bf16 v[180:183], v[220:223], v[104:107], v[180:183]\n\t" \
  "ds_read_u16_d16_hi v177, v177 offset:0\n\t" \
  "v_lshlrev_b32_sdwa v179, 1, v179 dst_sel:DWORD dst_unused:UNUSED_PAD src0_sel:DWORD src1_sel:WORD_1\n\t" \
  "v_mfma_f32_16x16x32_bf16 v[188:191], v[220:223], v[116:119], v[188:191]\n\t" \
  "ds_read_b64_tr_b16 v[218:219], v250 offset:6992\n\t" \
  "v_exp_f16 v226, v227\n\t" \
  "v_mfma_f32_16x16x32_bf16 v[196:199], v[220:223], v[128:131], v[196:199]\n\t" \
  "ds_read_u16_d16_hi v178, v178 offset:0\n\t" \
  "v_lshlrev_b32_sdwa v184, 1, v184 dst_sel:DWORD dst_unused:UNUSED_PAD src0_sel:DWORD src1_sel:WORD_1\n\t" \
  "v_mfma_f32_16x16x32_bf16 v[204:207], v[220:223], v[140:143], v[204:207]\n\t" \
  "s_waitcnt lgkmcnt(10)\n\t" \
  "ds_read_b64_tr_b16 v[220:221], v250 offset:6232\n\t" \
  "v_exp_f16_sdwa v224, v225 dst_sel:WORD_1 dst_unused:UNUSED_PRESERVE src0_sel:WORD_1\n\t" \
  "v_smfmac_f32_16x16x64_bf16 v[0:3], v[208:211], v[144:151], v248\n\t" \
  "ds_read_u16_d16_hi v179, v179 offset:0\n\t" \
  "v_lshlrev_b32_sdwa v185, 1, v185 dst_sel:DWORD dst_unused:UNUSED_PAD src0_sel:DWORD src1_sel:WORD_1\n\t" \
  "v_exp_f16_sdwa v226, v227 dst_sel:WORD_1 dst_unused:UNUSED_PRESERVE src0_sel:WORD_1\n\t" \
  "v_smfmac_f32_16x16x64_bf16 v[24:27], v[208:211], v[152:159], v248\n\t" \
  "s_waitcnt lgkmcnt(8)\n\t" \
  "ds_read_b64_tr_b16 v[222:223], v250 offset:7000\n\t" \
  "v_smfmac_f32_16x16x64_bf16 v[48:51], v[208:211], v[160:167], v248\n\t" \
  "ds_read_u16_d16_hi v184, v184 offset:0\n\t" \
  "v_lshlrev_b32_sdwa v186, 1, v186 dst_sel:DWORD dst_unused:UNUSED_PAD src0_sel:DWORD src1_sel:WORD_1\n\t" \
  "v_pk_fma_f16 v224, v224, %[hc], %[hc]\n\t" \
  "v_smfmac_f32_16x16x64_bf16 v[72:75], v[208:211], v[168:175], v248\n\t" \
  "s_waitcnt lgkmcnt(9)\n\t" \
  "ds_read_b64_tr_b16 v[208:209], v250 offset:6288\n\t" \
  "v_pk_fma_f16 v226, v226, %[hc], %[hc]\n\t" \
  "v_rcp_f16 v225, v224\n\t" \
  "v_smfmac_f32_16x16x64_bf16 v[4:7], v[212:215], v[144:151], v248\n\t" \
  "ds_read_u16_d16_hi v185, v185 offset:0\n\t" \
  "v_smfmac_f32_16x16x64_bf16 v[28:31], v[212:215], v[152:159], v248\n\t" \
  "ds_read_b64_tr_b16 v[210:211], v250 offset:7056\n\t" \
  "v_lshlrev_b32_sdwa v187, 1, v187 dst_sel:DWORD dst_unused:UNUSED_PAD src0_sel:DWORD src1_sel:WORD_1\n\t" \
  "v_rcp_f16 v227, v226\n\t" \
  "v_smfmac_f32_16x16x64_bf16 v[52:55], v[212:215], v[160:167], v248\n\t" \
  "ds_read_u16_d16_hi v186, v186 offset:0\n\t" \
  "v_lshlrev_b32_sdwa v192, 1, v192 dst_sel:DWORD dst_unused:UNUSED_PAD src0_sel:DWORD src1_sel:WORD_1\n\t" \
  "v_smfmac_f32_16x16x64_bf16 v[76:79], v[212:215], v[168:175], v248\n\t" \
  "s_waitcnt lgkmcnt(9)\n\t" \
  "ds_read_b64_tr_b16 v[212:213], v250 offset:6296\n\t" \
  "v_rcp_f16_sdwa v224, v224 dst_sel:DWORD dst_unused:UNUSED_PAD src0_sel:WORD_1\n\t" \
  "v_smfmac_f32_16x16x64_bf16 v[8:11], v[216:219], v[144:151], v248\n\t" \
  "ds_read_u16_d16_hi v187, v187 offset:0\n\t" \
  "v_lshlrev_b32_sdwa v193, 1, v193 dst_sel:DWORD dst_unused:UNUSED_PAD src0_sel:DWORD src1_sel:WORD_1\n\t" \
  "v_smfmac_f32_16x16x64_bf16 v[32:35], v[216:219], v[152:159], v248\n\t" \
  "ds_read_b64_tr_b16 v[214:215], v250 offset:7064\n\t" \
  "v_rcp_f16_sdwa v226, v226 dst_sel:DWORD dst_unused:UNUSED_PAD src0_sel:WORD_1\n\t" \
  "v_smfmac_f32_16x16x64_bf16 v[56:59], v[216:219], v[160:167], v248\n\t" \
  "ds_read_u16_d16_hi v192, v192 offset:0\n\t" \
  "v_lshlrev_b32_sdwa v194, 1, v194 dst_sel:DWORD dst_unused:UNUSED_PAD src0_sel:DWORD src1_sel:WORD_1\n\t" \
  "v_cvt_pk_bf16_f32 v230, v24, v25\n\t" \
  "v_smfmac_f32_16x16x64_bf16 v[80:83], v[216:219], v[168:175], v248\n\t" \
  "s_waitcnt lgkmcnt(9)\n\t" \
  "ds_read_b64_tr_b16 v[216:217], v249 offset:0\n\t" \
  "v_fma_mix_f32 v200, v200, v225, 0 op_sel_hi:[0,1,0]\n\t" \
  "v_fma_mix_f32 v202, v202, v227, 0 op_sel_hi:[0,1,0]\n\t" \
  "v_cvt_pk_bf16_f32 v231, v26, v27\n\t" \
  "v_smfmac_f32_16x16x64_bf16 v[12:15], v[220:223], v[144:151], v248\n\t" \
  "ds_read_u16_d16_hi v193, v193 offset:0\n\t" \
  "v_lshlrev_b32_sdwa v195, 1, v195 dst_sel:DWORD dst_unused:UNUSED_PAD src0_sel:DWORD src1_sel:WORD_1\n\t" \
  "v_fma_mix_f32 v201, v201, v224, 0 op_sel_hi:[0,1,0]\n\t" \
  "v_cvt_pk_bf16_f32 v232, v28, v29\n\t" \
  "v_smfmac_f32_16x16x64_bf16 v[36:39], v[220:223], v[152:159], v248\n\t" \
  "ds_read_b64_tr_b16 v[218:219], v249 offset:256\n\t" \
  "v_fma_mix_f32 v203, v203, v226, 0 op_sel_hi:[0,1,0]\n\t" \
  "v_cvt_pk_bf16_f32 v233, v30, v31\n\t" \
  "v_smfmac_f32_16x16x64_bf16 v[60:63], v[220:223], v[160:167], v248\n\t" \
  "ds_read_u16_d16_hi v194, v194 offset:0\n\t" \
  "v_lshlrev_b32_sdwa v180, 1, v180 dst_sel:DWORD dst_unused:UNUSED_PAD src0_sel:DWORD src1_sel:WORD_1\n\t" \
  "v_cvt_pk_f16_f32 v224, v204, v205\n\t" \
  "v_smfmac_f32_16x16x64_bf16 v[84:87], v[220:223], v[168:175], v248\n\t" \
  "s_waitcnt lgkmcnt(9)\n\t" \
  "ds_read_b64_tr_b16 v[220:221], v249 offset:2048\n\t" \
  "v_cvt_pk_f16_f32 v226, v206, v207\n\t" \
  "v_pk_fma_f16 v225, v224, v224, %[h1] neg_lo:[1,0,0] neg_hi:[1,0,0]\n\t" \
  "v_smfmac_f32_16x16x64_bf16 v[16:19], v[208:211], v[144:151], v248\n\t" \
  "ds_read_u16_d16_hi v195, v195 offset:0\n\t" \
  "v_lshlrev_b32_sdwa v181, 1, v181 dst_sel:DWORD dst_unused:UNUSED_PAD src0_sel:DWORD src1_sel:WORD_1\n\t" \
  "v_pk_fma_f16 v227, v226, v226, %[h1] neg_lo:[1,0,0] neg_hi:[1,0,0]\n\t" \
  "v_smfmac_f32_16x16x64_bf16 v[40:43], v[208:211], v[152:159], v248\n\t" \
  "ds_read_b64_tr_b16 v[222:223], v249 offset:2304\n\t" \
  "buffer_store_dwordx4 v[230:233], v253, %[osrd], %[ob] offen offset:0 sc1\n\t" \
  "v_pk_mul_f16 v225, v224, v225\n\t" \
  "v_lshlrev_b32_sdwa v182, 1, v182 dst_sel:DWORD dst_unused:UNUSED_PAD src0_sel:DWORD src1_sel:WORD_1\n\t" \
  "v_smfmac_f32_16x16x64_bf16 v[64:67], v[208:211], v[160:167], v248\n\t" \
  "ds_read_b64 v[230:231], v252 offset:0\n\t" \
  "v_pk_mul_f16 v227, v226, v227\n\t" \
  "v_exp_f16 v224, v225\n\t" \
  "v_cvt_pk_bf16_f32 v234, v32, v33\n\t" \
  "v_smfmac_f32_16x16x64_bf16 v[88:91], v[208:211], v[168:175], v248\n\t" \
  "s_waitcnt lgkmcnt(9)\n\t" \
  "ds_read_b64_tr_b16 v[208:209], v249 offset:4096\n\t" \
  "ds_read_u16_d16_hi v180, v180 offset:0\n\t" \
  "v_cvt_pk_bf16_f32 v235, v34, v35\n\t" \
  "v_smfmac_f32_16x16x64_bf16 v[20:23], v[212:215], v[144:151], v248\n\t" \
  "ds_read_b64_tr_b16 v[210:211], v249 offset:4352\n\t" \
  "v_lshlrev_b32_sdwa v183, 1, v183 dst_sel:DWORD dst_unused:UNUSED_PAD src0_sel:DWORD src1_sel:WORD_1\n\t" \
  "v_exp_f16 v226, v227\n\t" \
  "v_cvt_pk_bf16_f32 v236, v36, v37\n\t" \
  "v_smfmac_f32_16x16x64_bf16 v[44:47], v[212:215], v[152:159], v248\n\t" \
  "ds_read_b64 v[232:233], v252 offset:8\n\t" \
  "v_lshlrev_b32_sdwa v188, 1, v188 dst_sel:DWORD dst_unused:UNUSED_PAD src0_sel:DWORD src1_sel:WORD_1\n\t" \
  "v_cvt_pk_bf16_f32 v237, v38, v39\n\t" \
  "v_smfmac_f32_16x16x64_bf16 v[68:71], v[212:215], v[160:167], v248\n\t" \
  "s_waitcnt lgkmcnt(4)\n\t" \
  "ds_read_u16_d16_hi v181, v181 offset:0\n\t" \
  "v_mfma_f32_16x16x16_bf16 v[24:27], v[246:247], v[230:231], 0\n\t" \
  "ds_read_u16_d16_hi v182, v182 offset:0\n\t" \
  "v_exp_f16_sdwa v224, v225 dst_sel:WORD_1 dst_unused:UNUSED_PRESERVE src0_sel:WORD_1\n\t" \
  "v_smfmac_f32_16x16x64_bf16 v[92:95], v[212:215], v[168:175], v248\n\t" \
  "ds_read_b64_tr_b16 v[212:213], v249 offset:512\n\t" \
  "v_lshlrev_b32_sdwa v189, 1, v189 dst_sel:DWORD dst_unused:UNUSED_PAD src0_sel:DWORD src1_sel:WORD_1\n\t" \
  "v_exp_f16_sdwa v226, v227 dst_sel:WORD_1 dst_unused:UNUSED_PRESERVE src0_sel:WORD_1\n\t" \
  "v_mfma_f32_16x16x32_bf16 v[144:147], v[216:219], v[96:99], 0\n\t" \
  "s_waitcnt lgkmcnt(3)\n\t" \
  "ds_read_u16_d16_hi v183, v183 offset:0\n\t" \
  "ds_read_b64_tr_b16 v[214:215], v249 offset:768\n\t" \
  "v_mfma_f32_16x16x16_bf16 v[28:31], v[246:247], v[232:233], 0\n\t" \
  "buffer_store_dwordx4 v[234:237], v253, %[osrd], %[ob] offen offset:64 sc1\n\t" \
  "v_mfma_f32_16x16x32_bf16 v[152:155], v[216:219], v[108:111], 0\n\t" \
  "ds_read_u16_d16_hi v188, v188 offset:0\n\t" \
  "v_lshlrev_b32_sdwa v190, 1, v190 dst_sel:DWORD dst_unused:UNUSED_PAD src0_sel:DWORD src1_sel:WORD_1\n\t" \
  "v_pk_fma_f16 v224, v224, %[hc], %[hc]\n\t" \
  "v_mfma_f32_16x16x32_bf16 v[160:163], v[216:219], v[120:123], 0\n\t" \
  "ds_read_b64 v[234:235], v252 offset:64\n\t" \
  "v_pk_fma_f16 v226, v226, %[hc], %[hc]\n\t" \
  "v_rcp_f16 v225, v224\n\t" \
  "v_cvt_pk_bf16_f32 v238, v40, v41\n\t" \
  "v_mfma_f32_16x16x32_bf16 v[168:171], v[216:219], v[132:135], 0\n\t" \
  "ds_read_b64_tr_b16 v[216:217], v249 offset:2560\n\t" \
  "v_lshlrev_b32_sdwa v191, 1, v191 dst_sel:DWORD dst_unused:UNUSED_PAD src0_sel:DWORD src1_sel:WORD_1\n\t" \
  "v_cvt_pk_bf16_f32 v239, v42, v43\n\t" \
  "v_mfma_f32_16x16x32_bf16 v[144:147], v[220:223], v[100:103], v[144:147]\n\t" \
  "ds_read_b64 v[236:237], v252 offset:72\n\t" \
  "v_rcp_f16 v227, v226\n\t" \
  "v_cvt_pk_bf16_f32 v240, v44, v45\n\t" \
  "v_mfma_f32_16x16x32_bf16 v[152:155], v[220:223], v[112:115], v[152:155]\n\t" \
  "ds_read_b64_tr_b16 v[218:219], v249 offset:2816\n\t" \
  "v_lshlrev_b32_sdwa v196, 1, v196 dst_sel:DWORD dst_unused:UNUSED_PAD src0_sel:DWORD src1_sel:WORD_1\n\t" \
  "v_rcp_f16_sdwa v224, v224 dst_sel:DWORD dst_unused:UNUSED_PAD src0_sel:WORD_1\n\t" \
  "v_cvt_pk_bf16_f32 v241, v46, v47\n\t" \
  "v_mfma_f32_16x16x32_bf16 v[160:163], v[220:223], v[124:127], v[160:163]\n\t" \
  "s_waitcnt lgkmcnt(3)\n\t" \
  "ds_read_u16_d16_hi v189, v189 offset:0\n\t" \
  "v_mfma_f32_16x16x16_bf16 v[32:35], v[246:247], v[234:235], 0\n\t" \
  "ds_read_u16_d16_hi v190, v190 offset:0\n\t" \
  "v_mfma_f32_16x16x32_bf16 v[168:171], v[220:223], v[136:139], v[168:171]\n\t" \
  "ds_read_b64_tr_b16 v[220:221], v249 offset:4608\n\t" \
  "v_lshlrev_b32_sdwa v197, 1, v197 dst_sel:DWORD dst_unused:UNUSED_PAD src0_sel:DWORD src1_sel:WORD_1\n\t" \
  "v_rcp_f16_sdwa v226, v226 dst_sel:DWORD dst_unused:UNUSED_PAD src0_sel:WORD_1\n\t" \
  "v_mfma_f32_16x16x32_bf16 v[144:147], v[208:211], v[104:107], v[144:147]\n\t" \
  "s_waitcnt lgkmcnt(4)\n\t" \
  "ds_read_u16_d16_hi v191, v191 offset:0\n\t" \
  "ds_read_b64_tr_b16 v[222:223], v249 offset:4864\n\t" \
  "v_mfma_f32_16x16x16_bf16 v[36:39], v[246:247], v[236:237], 0\n\t" \
  "buffer_store_dwordx4 v[238:241], v253, %[osrd], %[ob] offen offset:128 sc1\n\t" \
  "v_lshlrev_b32_sdwa v198, 1, v198 dst_sel:DWORD dst_unused:UNUSED_PAD src0_sel:DWORD src1_sel:WORD_1\n\t" \
  "v_mfma_f32_16x16x32_bf16 v[152:155], v[208:211], v[116:119], v[152:155]\n\t" \
  "ds_read_u16_d16_hi v196, v196 offset:0\n\t" \
  "v_fma_mix_f32 v204, v204, v225, 0 op_sel_hi:[0,1,0]\n\t" \
  "v_fma_mix_f32 v206, v206, v227, 0 op_sel_hi:[0,1,0]\n\t" \
  "v_mfma_f32_16x16x32_bf16 v[160:163], v[208:211], v[128:131], v[160:163]\n\t" \
  "ds_read_b64 v[238:239], v252 offset:128\n\t" \
  "v_lshlrev_b32_sdwa v199, 1, v199 dst_sel:DWORD dst_unused:UNUSED_PAD src0_sel:DWORD src1_sel:WORD_1\n\t" \
  "v_fma_mix_f32 v205, v205, v224, 0 op_sel_hi:[0,1,0]\n\t" \
  "v_mfma_f32_16x16x32_bf16 v[168:171], v[208:211], v[140:143], v[168:171]\n\t" \
  "s_barrier\n\t" \
  "ds_read_b64_tr_b16 v[208:209], v250 offset:12320\n\t" \
  "ds_read_u16_d16_hi v197, v197 offset:0\n\t" \
  "v_fma_mix_f32 v207, v207, v226, 0 op_sel_hi:[0,1,0]\n\t" \
  "v_mfma_f32_16x16x32_bf16 v[148:151], v[212:215], v[96:99], 0\n\t" \
  "ds_read_b64_tr_b16 v[210:211], v250 offset:13088\n\t" \
  "v_mfma_f32_16x16x32_bf16 v[156:159], v[212:215], v[108:111], 0\n\t" \
  "ds_read_b64 v[240:241], v252 offset:136\n\t" \
  "v_mfma_f32_16x16x32_bf16 v[164:167], v[212:215], v[120:123], 0\n\t" \
  "s_waitcnt lgkmcnt(4)\n\t" \
  "ds_read_u16_d16_hi v198, v198 offset:0\n\t" \
  "v_mfma_f32_16x16x16_bf16 v[40:43], v[246:247], v[238:239], 0\n\t" \
  "ds_read_u16_d16_hi v199, v199 offset:0\n\t" \
  "v_lshlrev_b32_sdwa v144, 1, v144 dst_sel:DWORD dst_unused:UNUSED_PAD src0_sel:DWORD src1_sel:WORD_1\n\t" \
  "v_cvt_pk_f16_f32 v224, v168, v169\n\t" \
  "v_mfma_f32_16x16x32_bf16 v[172:175], v[212:215], v[132:135], 0\n\t" \
  "ds_read_b64_tr_b16 v[212:213], v250 offset:12328\n\t" \
  "v_cvt_pk_f16_f32 v226, v170, v171\n\t" \
  "v_pk_fma_f16 v225, v224, v224, %[h1] neg_lo:[1,0,0] neg_hi:[1,0,0]\n\t" \
  "v_mfma_f32_16x16x32_bf16 v[148:151], v[216:219], v[100:103], v[148:151]\n\t" \
  "s_waitcnt lgkmcnt(3)\n\t" \
  "ds_read_b64_tr_b16 v[214:215], v250 offset:13096\n\t" \
  "v_mfma_f32_16x16x16_bf16 v[44:47], v[246:247], v[240:241], 0\n\t" \
  "v_lshlrev_b32_sdwa v145, 1, v145 dst_sel:DWORD dst_unused:UNUSED_PAD src0_sel:DWORD src1_sel:WORD_1\n\t" \
  "v_mfma_f32_16x16x32_bf16 v[156:159], v[216:219], v[112:115], v[156:159]\n\t" \
  "ds_read_u16_d16_hi v144, v144 offset:0\n\t" \
  "v_pk_fma_f16 v227, v226, v226, %[h1] neg_lo:[1,0,0] neg_hi:[1,0,0]\n\t" \
  "v_pk_mul_f16 v225, v224, v225\n\t" \
  "v_mfma_f32_16x16x32_bf16 v[164:167], v[216:219], v[124:127], v[164:167]\n\t" \
  "v_lshlrev_b32_sdwa v146, 1, v146 dst_sel:DWORD dst_unused:UNUSED_PAD src0_sel:DWORD src1_sel:WORD_1\n\t" \
  "v_pk_mul_f16 v227, v226, v227\n\t" \
  "v_mfma_f32_16x16x32_bf16 v[172:175], v[216:219], v[136:139], v[172:175]\n\t" \
  "ds_read_b64_tr_b16 v[216:217], v250 offset:12384\n\t" \
  "v_exp_f16 v224, v225\n\t" \
  "v_mfma_f32_16x16x32_bf16 v[148:151], v[220:223], v[104:107], v[148:151]\n\t" \
  "ds_read_u16_d16_hi v145, v145 offset:0\n\t" \
  "v_lshlrev_b32_sdwa v147, 1, v147 dst_sel:DWORD dst_unused:UNUSED_PAD src0_sel:DWORD src1_sel:WORD_1\n\t" \
  "v_mfma_f32_16x16x32_bf16 v[156:159], v[220:223], v[116:119], v[156:159]\n\t" \
  "ds_read_b64_tr_b16 v[218:219], v250 offset:13152\n\t" \
  "v_exp_f16 v226, v227\n\t" \
  "v_mfma_f32_16x16x32_bf16 v[164:167], v[220:223], v[128:131], v[164:167]\n\t" \
  "ds_read_u16_d16_hi v146, v146 offset:0\n\t" \
  "v_lshlrev_b32_sdwa v152, 1, v152 dst_sel:DWORD dst_unused:UNUSED_PAD src0_sel:DWORD src1_sel:WORD_1\n\t" \
  "v_mfma_f32_16x16x32_bf16 v[172:175], v[220:223], v[140:143], v[172:175]\n\t" \
  "ds_read_b64_tr_b16 v[220:221], v250 offset:12392\n\t" \
  "v_exp_f16_sdwa v224, v225 dst_sel:WORD_1 dst_unused:UNUSED_PRESERVE src0_sel:WORD_1\n\t" \
  "v_smfmac_f32_16x16x64_bf16 v[0:3], v[208:211], v[176:183], v248\n\t" \
  "ds_read_u16_d16_hi v147, v147 offset:0\n\t" \
  "v_lshlrev_b32_sdwa v153, 1, v153 dst_sel:DWORD dst_unused:UNUSED_PAD src0_sel:DWORD src1_sel:WORD_1\n\t" \
  "v_exp_f16_sdwa v226, v227 dst_sel:WORD_1 dst_unused:UNUSED_PRESERVE src0_sel:WORD_1\n\t" \
  "v_smfmac_f32_16x16x64_bf16 v[24:27], v[208:211], v[184:191], v248\n\t" \
  "s_waitcnt lgkmcnt(9)\n\t" \
  "ds_read_b64_tr_b16 v[222:223], v250 offset:13160\n\t" \
  "v_smfmac_f32_16x16x64_bf16 v[48:51], v[208:211], v[192:199], v248\n\t" \
  "ds_read_u16_d16_hi v152, v152 offset:0\n\t" \
  "v_lshlrev_b32_sdwa v154, 1, v154 dst_sel:DWORD dst_unused:UNUSED_PAD src0_sel:DWORD src1_sel:WORD_1\n\t" \
  "v_pk_fma_f16 v224, v224, %[hc], %[hc]\n\t" \
  "v_smfmac_f32_16x16x64_bf16 v[72:75], v[208:211], v[200:207], v248\n\t" \
  "s_waitcnt lgkmcnt(9)\n\t" \
  "ds_read_b64_tr_b16 v[208:209], v250 offset:12448\n\t" \
  "v_pk_fma_f16 v226, v226, %[hc], %[hc]\n\t" \
  "v_rcp_f16 v225, v224\n\t" \
  "v_smfmac_f32_16x16x64_bf16 v[4:7], v[212:215], v[176:183], v248\n\t" \
  "ds_read_u16_d16_hi v153, v153 offset:0\n\t" \
  "v_smfmac_f32_16x16x64_bf16 v[28:31], v[212:215], v[184:191], v248\n\t" \
  "s_add_u32 %[ob], %[ob], %[dob]\n\t" \
  "ds_read_b64_tr_b16 v[210:211], v250 offset:13216\n\t" \
  "v_lshlrev_b32_sdwa v155, 1, v155 dst_sel:DWORD dst_unused:UNUSED_PAD src0_sel:DWORD src1_sel:WORD_1\n\t" \
  "v_rcp_f16 v227, v226\n\t" \
  "v_smfmac_f32_16x16x64_bf16 v[52:55], v[212:215], v[192:199], v248\n\t" \
  "ds_read_u16_d16_hi v154, v154 offset:0\n\t" \
  "v_lshlrev_b32_sdwa v160, 1, v160 dst_sel:DWORD dst_unused:UNUSED_PAD src0_sel:DWORD src1_sel:WORD_1\n\t" \
  "v_smfmac_f32_16x16x64_bf16 v[76:79], v[212:215], v[200:207], v248\n\t" \
  "s_waitcnt lgkmcnt(9)\n\t" \
  "ds_read_b64_tr_b16 v[212:213], v250 offset:12456\n\t" \
  "v_rcp_f16_sdwa v224, v224 dst_sel:DWORD dst_unused:UNUSED_PAD src0_sel:WORD_1\n\t" \
  "v_smfmac_f32_16x16x64_bf16 v[8:11], v[216:219], v[176:183], v248\n\t" \
  "s_add_u32 %[rs], %[rs], 1\n\t" \
  "ds_read_u16_d16_hi v155, v155 offset:0\n\t" \
  "v_lshlrev_b32_sdwa v161, 1, v161 dst_sel:DWORD dst_unused:UNUSED_PAD src0_sel:DWORD src1_sel:WORD_1\n\t" \
  "v_smfmac_f32_16x16x64_bf16 v[32:35], v[216:219], v[184:191], v248\n\t" \
  "s_cmp_eq_u32 %[rs], 6\n\t" \
  "ds_read_b64_tr_b16 v[214:215], v250 offset:13224\n\t" \
  "v_rcp_f16_sdwa v226, v226 dst_sel:DWORD dst_unused:UNUSED_PAD src0_sel:WORD_1\n\t" \
  "v_smfmac_f32_16x16x64_bf16 v[56:59], v[216:219], v[192:199], v248\n\t" \
  "ds_read_u16_d16_hi v160, v160 offset:0\n\t" \
  "v_lshlrev_b32_sdwa v162, 1, v162 dst_sel:DWORD dst_unused:UNUSED_PAD src0_sel:DWORD src1_sel:WORD_1\n\t" \
  "v_smfmac_f32_16x16x64_bf16 v[80:83], v[216:219], v[200:207], v248\n\t" \
  "s_waitcnt lgkmcnt(9)\n\t" \
  "ds_read_b64_tr_b16 v[216:217], v249 offset:6144\n\t" \
  "v_fma_mix_f32 v168, v168, v225, 0 op_sel_hi:[0,1,0]\n\t" \
  "v_fma_mix_f32 v170, v170, v227, 0 op_sel_hi:[0,1,0]\n\t" \
  "v_smfmac_f32_16x16x64_bf16 v[12:15], v[220:223], v[176:183], v248\n\t" \
  "s_cselect_b32 %[rs], 0, %[rs]\n\t" \
  "ds_read_u16_d16_hi v161, v161 offset:0\n\t" \
  "v_lshlrev_b32_sdwa v163, 1, v163 dst_sel:DWORD dst_unused:UNUSED_PAD src0_sel:DWORD src1_sel:WORD_1\n\t" \
  "v_fma_mix_f32 v169, v169, v224, 0 op_sel_hi:[0,1,0]\n\t" \
  "v_smfmac_f32_16x16x64_bf16 v[36:39], v[220:223], v[184:191], v248\n\t" \
  "s_cselect_b32 %[t0], -75920, %[ps]\n\t" \
  "ds_read_b64_tr_b16 v[218:219], v249 offset:6400\n\t" \
  "v_fma_mix_f32 v171, v171, v226, 0 op_sel_hi:[0,1,0]\n\t" \
  "v_smfmac_f32_16x16x64_bf16 v[60:63], v[220:223], v[192:199], v248\n\t" \
  "ds_read_u16_d16_hi v162, v162 offset:0\n\t" \
  "v_lshlrev_b32_sdwa v148, 1, v148 dst_sel:DWORD dst_unused:UNUSED_PAD src0_sel:DWORD src1_sel:WORD_1\n\t" \
  "v_cvt_pk_f16_f32 v224, v172, v173\n\t" \
  "v_add_u32 v252, %[t0], v252\n\t" \
  "v_smfmac_f32_16x16x64_bf16 v[84:87], v[220:223], v[200:207], v248\n\t" \
  "s_waitcnt lgkmcnt(9)\n\t" \
  "ds_read_b64_tr_b16 v[220:221], v249 offset:8192\n\t" \
  "v_cvt_pk_f16_f32 v226, v174, v175\n\t" \
  "v_pk_fma_f16 v225, v224, v224, %[h1] neg_lo:[1,0,0] neg_hi:[1,0,0]\n\t" \
  "v_smfmac_f32_16x16x64_bf16 v[16:19], v[208:211], v[176:183], v248\n\t" \
  "ds_read_u16_d16_hi v163, v163 offset:0\n\t" \
  "v_lshlrev_b32_sdwa v149, 1, v149 dst_sel:DWORD dst_unused:UNUSED_PAD src0_sel:DWORD src1_sel:WORD_1\n\t" \
  "v_pk_fma_f16 v227, v226, v226, %[h1] neg_lo:[1,0,0] neg_hi:[1,0,0]\n\t" \
  "v_smfmac_f32_16x16x64_bf16 v[40:43], v[208:211], v[184:191], v248\n\t" \
  "ds_read_b64_tr_b16 v[222:223], v249 offset:8448\n\t" \
  "v_pk_mul_f16 v225, v224, v225\n\t" \
  "v_lshlrev_b32_sdwa v150, 1, v150 dst_sel:DWORD dst_unused:UNUSED_PAD src0_sel:DWORD src1_sel:WORD_1\n\t" \
  "v_smfmac_f32_16x16x64_bf16 v[64:67], v[208:211], v[192:199], v248\n\t" \
  "ds_read_u16_d16_hi v148, v148 offset:0\n\t" \
  "v_pk_mul_f16 v227, v226, v227\n\t" \
  "v_exp_f16 v224, v225\n\t" \
  "v_smfmac_f32_16x16x64_bf16 v[88:91], v[208:211], v[200:207], v248\n\t" \
  "s_waitcnt lgkmcnt(9)\n\t" \
  "ds_read_b64_tr_b16 v[208:209], v249 offset:10240\n\t" \
  "v_smfmac_f32_16x16x64_bf16 v[20:23], v[212:215], v[176:183], v248\n\t" \
  "ds_read_u16_d16_hi v149, v149 offset:0\n\t" \
  "v_lshlrev_b32_sdwa v151, 1, v151 dst_sel:DWORD dst_unused:UNUSED_PAD src0_sel:DWORD src1_sel:WORD_1\n\t" \
  "v_exp_f16 v226, v227\n\t" \
  "v_smfmac_f32_16x16x64_bf16 v[44:47], v[212:215], v[184:191], v248\n\t" \
  "ds_read_b64_tr_b16 v[210:211], v249 offset:10496\n\t" \
  "v_lshlrev_b32_sdwa v156, 1, v156 dst_sel:DWORD dst_unused:UNUSED_PAD src0_sel:DWORD src1_sel:WORD_1\n\t" \
  "v_smfmac_f32_16x16x64_bf16 v[68:71], v[212:215], v[192:199], v248\n\t" \
  "ds_read_u16_d16_hi v150, v150 offset:0\n\t" \
  "v_exp_f16_sdwa v224, v225 dst_sel:WORD_1 dst_unused:UNUSED_PRESERVE src0_sel:WORD_1\n\t" \
  "v_smfmac_f32_16x16x64_bf16 v[92:95], v[212:215], v[200:207], v248\n\t" \
  "s_waitcnt lgkmcnt(9)\n\t" \
  "ds_read_b64_tr_b16 v[212:213], v249 offset:6656\n\t" \
  "v_lshlrev_b32_sdwa v157, 1, v157 dst_sel:DWORD dst_unused:UNUSED_PAD src0_sel:DWORD src1_sel:WORD_1\n\t" \
  "v_exp_f16_sdwa v226, v227 dst_sel:WORD_1 dst_unused:UNUSED_PRESERVE src0_sel:WORD_1\n\t" \
  "v_mfma_f32_16x16x32_bf16 v[176:179], v[216:219], v[96:99], 0\n\t" \
  "ds_read_u16_d16_hi v151, v151 offset:0\n\t" \
  "v_mfma_f32_16x16x32_bf16 v[184:187], v[216:219], v[108:111], 0\n\t" \
  "ds_read_b64_tr_b16 v[214:215], v249 offset:6912\n\t" \
  "v_lshlrev_b32_sdwa v158, 1, v158 dst_sel:DWORD dst_unused:UNUSED_PAD src0_sel:DWORD src1_sel:WORD_1\n\t" \
  "v_pk_fma_f16 v224, v224, %[hc], %[hc]\n\t" \
  "v_mfma_f32_16x16x32_bf16 v[192:195], v[216:219], v[120:123], 0\n\t" \
  "ds_read_u16_d16_hi v156, v156 offset:0\n\t" \
  "v_pk_fma_f16 v226, v226, %[hc], %[hc]\n\t" \
  "v_rcp_f16 v225, v224\n\t" \
  "v_mfma_f32_16x16x32_bf16 v[200:203], v[216:219], v[132:135], 0\n\t" \
  "s_waitcnt lgkmcnt(9)\n\t" \
  "ds_read_b64_tr_b16 v[216:217], v249 offset:8704\n\t" \
  "v_lshlrev_b32_sdwa v159, 1, v159 dst_sel:DWORD dst_unused:UNUSED_PAD src0_sel:DWORD src1_sel:WORD_1\n\t" \
  "v_mfma_f32_16x16x32_bf16 v[176:179], v[220:223], v[100:103], v[176:179]\n\t" \
  "ds_read_u16_d16_hi v157, v157 offset:0\n\t" \
  "v_rcp_f16 v227, v226\n\t" \
  "v_mfma_f32_16x16x32_bf16 v[184:187], v[220:223], v[112:115], v[184:187]\n\t" \
  "ds_read_b64_tr_b16 v[218:219], v249 offset:8960\n\t" \
  "v_lshlrev_b32_sdwa v164, 1, v164 dst_sel:DWORD dst_unused:UNUSED_PAD src0_sel:DWORD src1_sel:WORD_1\n\t" \
  "v_rcp_f16_sdwa v224, v224 dst_sel:DWORD dst_unused:UNUSED_PAD src0_sel:WORD_1\n\t" \
  "v_mfma_f32_16x16x32_bf16 v[192:195], v[220:223], v[124:127], v[192:195]\n\t" \
  "ds_read_u16_d16_hi v158, v158 offset:0\n\t" \
  "v_mfma_f32_16x16x32_bf16 v[200:203], v[220:223], v[136:139], v[200:203]\n\t" \
  "s_waitcnt lgkmcnt(9)\n\t" \
  "ds_read_b64_tr_b16 v[220:221], v249 offset:10752\n\t" \
  "v_lshlrev_b32_sdwa v165, 1, v165 dst_sel:DWORD dst_unused:UNUSED_PAD src0_sel:DWORD src1_sel:WORD_1\n\t" \
  "v_rcp_f16_sdwa v226, v226 dst_sel:DWORD dst_unused:UNUSED_PAD src0_sel:WORD_1\n\t" \
  "v_mfma_f32_16x16x32_bf16 v[176:179], v[208:211], v[104:107], v[176:179]\n\t" \
  "ds_read_u16_d16_hi v159, v159 offset:0\n\t" \
  "v_lshlrev_b32_sdwa v166, 1, v166 dst_sel:DWORD dst_unused:UNUSED_PAD src0_sel:DWORD src1_sel:WORD_1\n\t" \
  "v_mfma_f32_16x16x32_bf16 v[184:187], v[208:211], v[116:119], v[184:187]\n\t" \
  "ds_read_b64_tr_b16 v[222:223], v249 offset:11008\n\t" \
  "v_fma_mix_f32 v172, v172, v225, 0 op_sel_hi:[0,1,0]\n\t" \
  "v_fma_mix_f32 v174, v174, v227, 0 op_sel_hi:[0,1,0]\n\t" \
  "v_mfma_f32_16x16x32_bf16 v[192:195], v[208:211], v[128:131], v[192:195]\n\t" \
  "ds_read_u16_d16_hi v164, v164 offset:0\n\t" \
  "v_lshlrev_b32_sdwa v167, 1, v167 dst_sel:DWORD dst_unused:UNUSED_PAD src0_sel:DWORD src1_sel:WORD_1\n\t" \
  "v_fma_mix_f32 v173, v173, v224, 0 op_sel_hi:[0,1,0]\n\t" \
  "v_mfma_f32_16x16x32_bf16 v[200:203], v[208:211], v[140:143], v[200:203]\n\t" \
  "s_barrier\n\t" \
  "ds_read_b64_tr_b16 v[208:209], v250 offset:0\n\t" \
  "v_fma_mix_f32 v175, v175, v226, 0 op_sel_hi:[0,1,0]\n\t" \
  "s_waitcnt lgkmcnt(10)\n\t" \
  "v_mfma_f32_16x16x32_bf16 v[180:183], v[212:215], v[96:99], 0\n\t" \
  "ds_read_u16_d16_hi v165, v165 offset:0\n\t" \
  "v_mfma_f32_16x16x32_bf16 v[188:191], v[212:215], v[108:111], 0\n\t" \
  "ds_read_b64_tr_b16 v[210:211], v250 offset:768\n\t" \
  "v_mfma_f32_16x16x32_bf16 v[196:199], v[212:215], v[120:123], 0\n\t" \
  "ds_read_u16_d16_hi v166, v166 offset:0\n\t" \
  "v_lshlrev_b32_sdwa v176, 1, v176 dst_sel:DWORD dst_unused:UNUSED_PAD src0_sel:DWORD src1_sel:WORD_1\n\t" \
  "v_cvt_pk_f16_f32 v224, v200, v201\n\t" \
  "v_mfma_f32_16x16x32_bf16 v[204:207], v[212:215], v[132:135], 0\n\t" \
  "s_waitcnt lgkmcnt(9)\n\t" \
  "ds_read_b64_tr_b16 v[212:213], v250 offset:8\n\t" \
  "v_cvt_pk_f16_f32 v226, v202, v203\n\t" \
  "v_pk_fma_f16 v225, v224, v224, %[h1] neg_lo:[1,0,0] neg_hi:[1,0,0]\n\t" \
  "v_mfma_f32_16x16x32_bf16 v[180:183], v[216:219], v[100:103], v[180:183]\n\t" \
  "ds_read_u16_d16_hi v167, v167 offset:0\n\t" \
  "v_lshlrev_b32_sdwa v177, 1, v177 dst_sel:DWORD dst_unused:UNUSED_PAD src0_sel:DWORD src1_sel:WORD_1\n\t" \
  "v_mfma_f32_16x16x32_bf16 v[188:191], v[216:219], v[112:115], v[188:191]\n\t" \
  "ds_read_b64_tr_b16 v[214:215], v250 offset:776\n\t" \
  "v_pk_fma_f16 v227, v226, v226, %[h1] neg_lo:[1,0,0] neg_hi:[1,0,0]\n\t" \
  "v_pk_mul_f16 v225, v224, v225\n\t" \
  "v_mfma_f32_16x16x32_bf16 v[196:199], v[216:219], v[124:127], v[196:199]\n\t" \
  "ds_read_u16_d16_hi v176, v176 offset:0\n\t" \
  "v_lshlrev_b32_sdwa v178, 1, v178 dst_sel:DWORD dst_unused:UNUSED_PAD src0_sel:DWORD src1_sel:WORD_1\n\t" \
  "v_pk_mul_f16 v227, v226, v227\n\t" \
  "v_mfma_f32_16x16x32_bf16 v[204:207], v[216:219], v[136:139], v[204:207]\n\t" \
  "s_waitcnt lgkmcnt(9)\n\t" \
  "ds_read_b64_tr_b16 v[216:217], v250 offset:64\n\t" \
  "v_exp_f16 v224, v225\n\t" \
  "v_mfma_f32_16x16x32_bf16 v[180:183], v[220:223], v[104:107], v[180:183]\n\t" \
  "ds_read_u16_d16_hi v177, v177 offset:0\n\t" \
  "v_lshlrev_b32_sdwa v179, 1, v179 dst_sel:DWORD dst_unused:UNUSED_PAD src0_sel:DWORD src1_sel:WORD_1\n\t" \
  "v_mfma_f32_16x16x32_bf16 v[188:191], v[220:223], v[116:119], v[188:191]\n\t" \
  "ds_read_b64_tr_b16 v[218:219], v250 offset:832\n\t" \
  "v_exp_f16 v226, v227\n\t" \
  "v_mfma_f32_16x16x32_bf16 v[196:199], v[220:223], v[128:131], v[196:199]\n\t" \
  "ds_read_u16_d16_hi v178, v178 offset:0\n\t" \
  "v_lshlrev_b32_sdwa v184, 1, v184 dst_sel:DWORD dst_unused:UNUSED_PAD src0_sel:DWORD src1_sel:WORD_1\n\t" \
  "v_mfma_f32_16x16x32_bf16 v[204:207], v[220:223], v[140:143], v[204:207]\n\t" \
  "s_waitcnt lgkmcnt(9)\n\t" \
  "ds_read_b64_tr_b16 v[220:221], v250 offset:72\n\t" \
  "v_exp_f16_sdwa v224, v225 dst_sel:WORD_1 dst_unused:UNUSED_PRESERVE src0_sel:WORD_1\n\t" \
  "v_smfmac_f32_16x16x64_bf16 v[0:3], v[208:211], v[144:151], v248\n\t" \
  "ds_read_u16_d16_hi v179, v179 offset:0\n\t" \
  "v_lshlrev_b32_sdwa v185, 1, v185 dst_sel:DWORD dst_unused:UNUSED_PAD src0_sel:DWORD src1_sel:WORD_1\n\t" \
  "v_exp_f16_sdwa v226, v227 dst_sel:WORD_1 dst_unused:UNUSED_PRESERVE src0_sel:WORD_1\n\t" \
  "v_smfmac_f32_16x16x64_bf16 v[24:27], v[208:211], v[152:159], v248\n\t" \
  "s_waitcnt lgkmcnt(8)\n\t" \
  "ds_read_b64_tr_b16 v[222:223], v250 offset:840\n\t" \
  "v_smfmac_f32_16x16x64_bf16 v[48:51], v[208:211], v[160:167], v248\n\t" \
  "ds_read_u16_d16_hi v184, v184 offset:0\n\t" \
  "v_lshlrev_b32_sdwa v186, 1, v186 dst_sel:DWORD dst_unused:UNUSED_PAD src0_sel:DWORD src1_sel:WORD_1\n\t" \
  "v_pk_fma_f16 v224, v224, %[hc], %[hc]\n\t" \
  "v_smfmac_f32_16x16x64_bf16 v[72:75], v[208:211], v[168:175], v248\n\t" \
  "s_waitcnt lgkmcnt(9)\n\t" \
  "ds_read_b64_tr_b16 v[208:209], v250 offset:128\n\t" \
  "v_pk_fma_f16 v226, v226, %[hc], %[hc]\n\t" \
  "v_rcp_f16 v225, v224\n\t" \
  "v_smfmac_f32_16x16x64_bf16 v[4:7], v[212:215], v[144:151], v248\n\t" \
  "ds_read_u16_d16_hi v185, v185 offset:0\n\t" \
  "v_smfmac_f32_16x16x64_bf16 v[28:31], v[212:215], v[152:159], v248\n\t" \
  "ds_read_b64_tr_b16 v[210:211], v250 offset:896\n\t" \
  "v_lshlrev_b32_sdwa v187, 1, v187 dst_sel:DWORD dst_unused:UNUSED_PAD src0_sel:DWORD src1_sel:WORD_1\n\t" \
  "v_rcp_f16 v227, v226\n\t" \
  "v_smfmac_f32_16x16x64_bf16 v[52:55], v[212:215], v[160:167], v248\n\t" \
  "ds_read_b128 v[120:123], v251 offset:0\n\t" \
  "v_lshlrev_b32_sdwa v192, 1, v192 dst_sel:DWORD dst_unused:UNUSED_PAD src0_sel:DWORD src1_sel:WORD_1\n\t" \
  "v_smfmac_f32_16x16x64_bf16 v[76:79], v[212:215], v[168:175], v248\n\t" \
  "s_waitcnt lgkmcnt(9)\n\t" \
  "ds_read_b64_tr_b16 v[212:213], v250 offset:136\n\t" \
  "v_rcp_f16_sdwa v224, v224 dst_sel:DWORD dst_unused:UNUSED_PAD src0_sel:WORD_1\n\t" \
  "v_smfmac_f32_16x16x64_bf16 v[8:11], v[216:219], v[144:151], v248\n\t" \
  "ds_read_u16_d16_hi v186, v186 offset:0\n\t" \
  "v_lshlrev_b32_sdwa v193, 1, v193 dst_sel:DWORD dst_unused:UNUSED_PAD src0_sel:DWORD src1_sel:WORD_1\n\t" \
  "v_smfmac_f32_16x16x64_bf16 v[32:35], v[216:219], v[152:159], v248\n\t" \
  "ds_read_b64_tr_b16 v[214:215], v250 offset:904\n\t" \
  "v_rcp_f16_sdwa v226, v226 dst_sel:DWORD dst_unused:UNUSED_PAD src0_sel:WORD_1\n\t" \
  "v_smfmac_f32_16x16x64_bf16 v[56:59], v[216:219], v[160:167], v248\n\t" \
  "ds_read_u16_d16_hi v187, v187 offset:0\n\t" \
  "v_lshlrev_b32_sdwa v194, 1, v194 dst_sel:DWORD dst_unused:UNUSED_PAD src0_sel:DWORD src1_sel:WORD_1\n\t" \
  "v_smfmac_f32_16x16x64_bf16 v[80:83], v[216:219], v[168:175], v248\n\t" \
  "s_waitcnt lgkmcnt(9)\n\t" \
  "ds_read_b64_tr_b16 v[216:217], v249 offset:12288\n\t" \
  "ds_read_b128 v[124:127], v251 offset:64\n\t" \
  "v_fma_mix_f32 v200, v200, v225, 0 op_sel_hi:[0,1,0]\n\t" \
  "v_fma_mix_f32 v202, v202, v227, 0 op_sel_hi:[0,1,0]\n\t" \
  "v_smfmac_f32_16x16x64_bf16 v[12:15], v[220:223], v[144:151], v248\n\t" \
  "ds_read_b64_tr_b16 v[218:219], v249 offset:12544\n\t" \
  "v_lshlrev_b32_sdwa v195, 1, v195 dst_sel:DWORD dst_unused:UNUSED_PAD src0_sel:DWORD src1_sel:WORD_1\n\t" \
  "v_fma_mix_f32 v201, v201, v224, 0 op_sel_hi:[0,1,0]\n\t" \
  "v_smfmac_f32_16x16x64_bf16 v[36:39], v[220:223], v[152:159], v248\n\t" \
  "ds_read_u16_d16_hi v192, v192 offset:0\n\t" \
  "v_fma_mix_f32 v203, v203, v226, 0 op_sel_hi:[0,1,0]\n\t" \
  "v_smfmac_f32_16x16x64_bf16 v[60:63], v[220:223], v[160:167], v248\n\t" \
  "ds_read_u16_d16_hi v193, v193 offset:0\n\t" \
  "v_lshlrev_b32_sdwa v180, 1, v180 dst_sel:DWORD dst_unused:UNUSED_PAD src0_sel:DWORD src1_sel:WORD_1\n\t" \
  "v_cvt_pk_f16_f32 v224, v204, v205\n\t" \
  "v_smfmac_f32_16x16x64_bf16 v[84:87], v[220:223], v[168:175], v248\n\t" \
  "s_waitcnt lgkmcnt(10)\n\t" \
  "ds_read_b64_tr_b16 v[220:221], v249 offset:14336\n\t" \
  "ds_read_u16_d16_hi v194, v194 offset:0\n\t" \
  "v_cvt_pk_f16_f32 v226, v206, v207\n\t" \
  "v_pk_fma_f16 v225, v224, v224, %[h1] neg_lo:[1,0,0] neg_hi:[1,0,0]\n\t" \
  "v_smfmac_f32_16x16x64_bf16 v[16:19], v[208:211], v[144:151], v248\n\t" \
  "ds_read_b64_tr_b16 v[222:223], v249 offset:14592\n\t" \
  "v_lshlrev_b32_sdwa v181, 1, v181 dst_sel:DWORD dst_unused:UNUSED_PAD src0_sel:DWORD src1_sel:WORD_1\n\t" \
  "v_pk_fma_f16 v227, v226, v226, %[h1] neg_lo:[1,0,0] neg_hi:[1,0,0]\n\t" \
  "v_smfmac_f32_16x16x64_bf16 v[40:43], v[208:211], v[152:159], v248\n\t" \
  "ds_read_b128 v[128:131], v251 offset:128\n\t" \
  "v_pk_mul_f16 v225, v224, v225\n\t" \
  "v_lshlrev_b32_sdwa v182, 1, v182 dst_sel:DWORD dst_unused:UNUSED_PAD src0_sel:DWORD src1_sel:WORD_1\n\t" \
  "v_smfmac_f32_16x16x64_bf16 v[64:67], v[208:211], v[160:167], v248\n\t" \
  "ds_read_u16_d16_hi v195, v195 offset:0\n\t" \
  "v_pk_mul_f16 v227, v226, v227\n\t" \
  "v_exp_f16 v224, v225\n\t" \
  "v_smfmac_f32_16x16x64_bf16 v[88:91], v[208:211], v[168:175], v248\n\t" \
  "s_waitcnt lgkmcnt(11)\n\t" \
  "ds_read_b64_tr_b16 v[208:209], v249 offset:16384\n\t" \
  "v_smfmac_f32_16x16x64_bf16 v[20:23], v[212:215], v[144:151], v248\n\t" \
  "ds_read_u16_d16_hi v180, v180 offset:0\n\t" \
  "v_lshlrev_b32_sdwa v183, 1, v183 dst_sel:DWORD dst_unused:UNUSED_PAD src0_sel:DWORD src1_sel:WORD_1\n\t" \
  "v_exp_f16 v226, v227\n\t" \
  "v_smfmac_f32_16x16x64_bf16 v[44:47], v[212:215], v[152:159], v248\n\t" \
  "ds_read_b64_tr_b16 v[210:211], v249 offset:16640\n\t" \
  "v_lshlrev_b32_sdwa v188, 1, v188 dst_sel:DWORD dst_unused:UNUSED_PAD src0_sel:DWORD src1_sel:WORD_1\n\t" \
  "v_smfmac_f32_16x16x64_bf16 v[68:71], v[212:215], v[160:167], v248\n\t" \
  "ds_read_u16_d16_hi v181, v181 offset:0\n\t" \
  "v_exp_f16_sdwa v224, v225 dst_sel:WORD_1 dst_unused:UNUSED_PRESERVE src0_sel:WORD_1\n\t" \
  "v_smfmac_f32_16x16x64_bf16 v[92:95], v[212:215], v[168:175], v248\n\t" \
  "s_waitcnt lgkmcnt(11)\n\t" \
  "ds_read_b64_tr_b16 v[212:213], v249 offset:12800\n\t" \
  "v_lshlrev_b32_sdwa v189, 1, v189 dst_sel:DWORD dst_unused:UNUSED_PAD src0_sel:DWORD src1_sel:WORD_1\n\t" \
  "v_exp_f16_sdwa v226, v227 dst_sel:WORD_1 dst_unused:UNUSED_PRESERVE src0_sel:WORD_1\n\t" \
  "v_mfma_f32_16x16x32_bf16 v[144:147], v[216:219], v[96:99], 0\n\t" \
  "ds_read_u16_d16_hi v182, v182 offset:0\n\t" \
  "v_mfma_f32_16x16x32_bf16 v[152:155], v[216:219], v[108:111], 0\n\t" \
  "ds_read_b64_tr_b16 v[214:215], v249 offset:13056\n\t" \
  "v_lshlrev_b32_sdwa v190, 1, v190 dst_sel:DWORD dst_unused:UNUSED_PAD src0_sel:DWORD src1_sel:WORD_1\n\t" \
  "v_pk_fma_f16 v224, v224, %[hc], %[hc]\n\t" \
  "v_mfma_f32_16x16x32_bf16 v[160:163], v[216:219], v[120:123], 0\n\t" \
  "ds_read_u16_d16_hi v183, v183 offset:0\n\t" \
  "v_pk_fma_f16 v226, v226, %[hc], %[hc]\n\t" \
  "v_rcp_f16 v225, v224\n\t" \
  "v_mfma_f32_16x16x32_bf16 v[168:171], v[216:219], v[132:135], 0\n\t" \
  "s_waitcnt lgkmcnt(10)\n\t" \
  "ds_read_b64_tr_b16 v[216:217], v249 offset:14848\n\t" \
  "v_lshlrev_b32_sdwa v191, 1, v191 dst_sel:DWORD dst_unused:UNUSED_PAD src0_sel:DWORD src1_sel:WORD_1\n\t" \
  "v_mfma_f32_16x16x32_bf16 v[144:147], v[220:223], v[100:103], v[144:147]\n\t" \
  "ds_read_u16_d16_hi v188, v188 offset:0\n\t" \
  "v_rcp_f16 v227, v226\n\t" \
  "v_mfma_f32_16x16x32_bf16 v[152:155], v[220:223], v[112:115], v[152:155]\n\t" \
  "ds_read_b64_tr_b16 v[218:219], v249 offset:15104\n\t" \
  "v_lshlrev_b32_sdwa v196, 1, v196 dst_sel:DWORD dst_unused:UNUSED_PAD src0_sel:DWORD src1_sel:WORD_1\n\t" \
  "v_rcp_f16_sdwa v224, v224 dst_sel:DWORD dst_unused:UNUSED_PAD src0_sel:WORD_1\n\t" \
  "v_mfma_f32_16x16x32_bf16 v[160:163], v[220:223], v[124:127], v[160:163]\n\t" \
  "ds_read_u16_d16_hi v189, v189 offset:0\n\t" \
  "v_mfma_f32_16x16x32_bf16 v[168:171], v[220:223], v[136:139], v[168:171]\n\t" \
  "s_waitcnt lgkmcnt(9)\n\t" \
  "ds_read_b64_tr_b16 v[220:221], v249 offset:16896\n\t" \
  "v_lshlrev_b32_sdwa v197, 1, v197 dst_sel:DWORD dst_unused:UNUSED_PAD src0_sel:DWORD src1_sel:WORD_1\n\t" \
  "v_rcp_f16_sdwa v226, v226 dst_sel:DWORD dst_unused:UNUSED_PAD src0_sel:WORD_1\n\t" \
  "v_mfma_f32_16x16x32_bf16 v[144:147], v[208:211], v[104:107], v[144:147]\n\t" \
  "ds_read_u16_d16_hi v190, v190 offset:0\n\t" \
  "v_lshlrev_b32_sdwa v198, 1, v198 dst_sel:DWORD dst_unused:UNUSED_PAD src0_sel:DWORD src1_sel:WORD_1\n\t" \
  "v_mfma_f32_16x16x32_bf16 v[152:155], v[208:211], v[116:119], v[152:155]\n\t" \
  "ds_read_b64_tr_b16 v[222:223], v249 offset:17152\n\t" \
  "v_fma_mix_f32 v204, v204, v225, 0 op_sel_hi:[0,1,0]\n\t" \
  "v_fma_mix_f32 v206, v206, v227, 0 op_sel_hi:[0,1,0]\n\t" \
  "v_mfma_f32_16x16x32_bf16 v[160:163], v[208:211], v[128:131], v[160:163]\n\t" \
  "ds_read_u16_d16_hi v191, v191 offset:0\n\t" \
  "v_lshlrev_b32_sdwa v199, 1, v199 dst_sel:DWORD dst_unused:UNUSED_PAD src0_sel:DWORD src1_sel:WORD_1\n\t" \
  "v_fma_mix_f32 v205, v205, v224, 0 op_sel_hi:[0,1,0]\n\t" \
  "v_mfma_f32_16x16x32_bf16 v[168:171], v[208:211], v[140:143], v[168:171]\n\t" \
  "s_barrier\n\t" \
  "ds_read_b64_tr_b16 v[208:209], v250 offset:6160\n\t" \
  "ds_read_u16_d16_hi v196, v196 offset:0\n\t" \
  "v_fma_mix_f32 v207, v207, v226, 0 op_sel_hi:[0,1,0]\n\t" \
  "s_waitcnt lgkmcnt(11)\n\t" \
  "v_mfma_f32_16x16x32_bf16 v[148:151], v[212:215], v[96:99], 0\n\t" \
  "ds_read_b64_tr_b16 v[210:211], v250 offset:6928\n\t" \
  "v_mfma_f32_16x16x32_bf16 v[156:159], v[212:215], v[108:111], 0\n\t" \
  "ds_read_u16_d16_hi v197, v197 offset:0\n\t" \
  "v_mfma_f32_16x16x32_bf16 v[164:167], v[212:215], v[120:123], 0\n\t" \
  "ds_read_u16_d16_hi v198, v198 offset:0\n\t" \
  "v_lshlrev_b32_sdwa v144, 1, v144 dst_sel:DWORD dst_unused:UNUSED_PAD src0_sel:DWORD src1_sel:WORD_1\n\t" \
  "v_cvt_pk_f16_f32 v224, v168, v169\n\t" \
  "v_mfma_f32_16x16x32_bf16 v[172:175], v[212:215], v[132:135], 0\n\t" \
  "s_waitcnt lgkmcnt(10)\n\t" \
  "ds_read_b64_tr_b16 v[212:213], v250 offset:6168\n\t" \
  "v_cvt_pk_f16_f32 v226, v170, v171\n\t" \
  "v_pk_fma_f16 v225, v224, v224, %[h1] neg_lo:[1,0,0] neg_hi:[1,0,0]\n\t" \
  "v_mfma_f32_16x16x32_bf16 v[148:151], v[216:219], v[100:103], v[148:151]\n\t" \
  "ds_read_u16_d16_hi v199, v199 offset:0\n\t" \
  "v_lshlrev_b32_sdwa v145, 1, v145 dst_sel:DWORD dst_unused:UNUSED_PAD src0_sel:DWORD src1_sel:WORD_1\n\t" \
  "v_mfma_f32_16x16x32_bf16 v[156:159], v[216:219], v[112:115], v[156:159]\n\t" \
  "ds_read_b64_tr_b16 v[214:215], v250 offset:6936\n\t" \
  "v_pk_fma_f16 v227, v226, v226, %[h1] neg_lo:[1,0,0] neg_hi:[1,0,0]\n\t" \
  "v_pk_mul_f16 v225, v224, v225\n\t" \
  "v_mfma_f32_16x16x32_bf16 v[164:167], v[216:219], v[124:127], v[164:167]\n\t" \
  "ds_read_u16_d16_hi v144, v144 offset:0\n\t" \
  "v_lshlrev_b32_sdwa v146, 1, v146 dst_sel:DWORD dst_unused:UNUSED_PAD src0_sel:DWORD src1_sel:WORD_1\n\t" \
  "v_pk_mul_f16 v227, v226, v227\n\t" \
  "v_mfma_f32_16x16x32_bf16 v[172:175], v[216:219], v[136:139], v[172:175]\n\t" \
  "s_waitcnt lgkmcnt(10)\n\t" \
  "ds_read_b64_tr_b16 v[216:217], v250 offset:6224\n\t" \
  "v_exp_f16 v224, v225\n\t" \
  "v_mfma_f32_16x16x32_bf16 v[148:151], v[220:223], v[104:107], v[148:151]\n\t" \
  "ds_read_u16_d16_hi v145, v145 offset:0\n\t" \
  "v_lshlrev_b32_sdwa v147, 1, v147 dst_sel:DWORD dst_unused:UNUSED_PAD src0_sel:DWORD src1_sel:WORD_1\n\t" \
  "v_mfma_f32_16x16x32_bf16 v[156:159], v[220:223], v[116:119], v[156:159]\n\t" \
  "ds_read_b64_tr_b16 v[218:219], v250 offset:6992\n\t" \
  "v_exp_f16 v226, v227\n\t" \
  "v_mfma_f32_16x16x32_bf16 v[164:167], v[220:223], v[128:131], v[164:167]\n\t" \
  "ds_read_u16_d16_hi v146, v146 offset:0\n\t" \
  "v_lshlrev_b32_sdwa v152, 1, v152 dst_sel:DWORD dst_unused:UNUSED_PAD src0_sel:DWORD src1_sel:WORD_1\n\t" \
  "v_mfma_f32_16x16x32_bf16 v[172:175], v[220:223], v[140:143], v[172:175]\n\t" \
  "s_waitcnt lgkmcnt(10)\n\t" \
  "ds_read_b64_tr_b16 v[220:221], v250 offset:6232\n\t" \
  "v_exp_f16_sdwa v224, v225 dst_sel:WORD_1 dst_unused:UNUSED_PRESERVE src0_sel:WORD_1\n\t" \
  "v_smfmac_f32_16x16x64_bf16 v[0:3], v[208:211], v[176:183], v248\n\t" \
  "ds_read_u16_d16_hi v147, v147 offset:0\n\t" \
  "v_lshlrev_b32_sdwa v153, 1, v153 dst_sel:DWORD dst_unused:UNUSED_PAD src0_sel:DWORD src1_sel:WORD_1\n\t" \
  "v_exp_f16_sdwa v226, v227 dst_sel:WORD_1 dst_unused:UNUSED_PRESERVE src0_sel:WORD_1\n\t" \
  "v_smfmac_f32_16x16x64_bf16 v[24:27], v[208:211], v[184:191], v248\n\t" \
  "s_waitcnt lgkmcnt(8)\n\t" \
  "ds_read_b64_tr_b16 v[222:223], v250 offset:7000\n\t" \
  "v_smfmac_f32_16x16x64_bf16 v[48:51], v[208:211], v[192:199], v248\n\t" \
  "ds_read_u16_d16_hi v152, v152 offset:0\n\t" \
  "v_lshlrev_b32_sdwa v154, 1, v154 dst_sel:DWORD dst_unused:UNUSED_PAD src0_sel:DWORD src1_sel:WORD_1\n\t" \
  "v_pk_fma_f16 v224, v224, %[hc], %[hc]\n\t" \
  "v_smfmac_f32_16x16x64_bf16 v[72:75], v[208:211], v[200:207], v248\n\t" \
  "s_waitcnt lgkmcnt(9)\n\t" \
  "ds_read_b64_tr_b16 v[208:209], v250 offset:6288\n\t" \
  "v_pk_fma_f16 v226, v226, %[hc], %[hc]\n\t" \
  "v_rcp_f16 v225, v224\n\t" \
  "v_smfmac_f32_16x16x64_bf16 v[4:7], v[212:215], v[176:183], v248\n\t" \
  "ds_read_u16_d16_hi v153, v153 offset:0\n\t" \
  "v_smfmac_f32_16x16x64_bf16 v[28:31], v[212:215], v[184:191], v248\n\t" \
  "ds_read_b64_tr_b16 v[210:211], v250 offset:7056\n\t" \
  "v_lshlrev_b32_sdwa v155, 1, v155 dst_sel:DWORD dst_unused:UNUSED_PAD src0_sel:DWORD src1_sel:WORD_1\n\t" \
  "v_rcp_f16 v227, v226\n\t" \
  "v_smfmac_f32_16x16x64_bf16 v[52:55], v[212:215], v[192:199], v248\n\t" \
  "ds_read_u16_d16_hi v154, v154 offset:0\n\t" \
  "v_lshlrev_b32_sdwa v160, 1, v160 dst_sel:DWORD dst_unused:UNUSED_PAD src0_sel:DWORD src1_sel:WORD_1\n\t" \
  "v_smfmac_f32_16x16x64_bf16 v[76:79], v[212:215], v[200:207], v248\n\t" \
  "s_waitcnt lgkmcnt(9)\n\t" \
  "ds_read_b64_tr_b16 v[212:213], v250 offset:6296\n\t" \
  "v_rcp_f16_sdwa v224, v224 dst_sel:DWORD dst_unused:UNUSED_PAD src0_sel:WORD_1\n\t" \
  "v_smfmac_f32_16x16x64_bf16 v[8:11], v[216:219], v[176:183], v248\n\t" \
  "ds_read_u16_d16_hi v155, v155 offset:0\n\t" \
  "v_lshlrev_b32_sdwa v161, 1, v161 dst_sel:DWORD dst_unused:UNUSED_PAD src0_sel:DWORD src1_sel:WORD_1\n\t" \
  "v_smfmac_f32_16x16x64_bf16 v[32:35], v[216:219], v[184:191], v248\n\t" \
  "ds_read_b64_tr_b16 v[214:215], v250 offset:7064\n\t" \
  "v_rcp_f16_sdwa v226, v226 dst_sel:DWORD dst_unused:UNUSED_PAD src0_sel:WORD_1\n\t" \
  "v_smfmac_f32_16x16x64_bf16 v[56:59], v[216:219], v[192:199], v248\n\t" \
  "ds_read_u16_d16_hi v160, v160 offset:0\n\t" \
  "v_lshlrev_b32_sdwa v162, 1, v162 dst_sel:DWORD dst_unused:UNUSED_PAD src0_sel:DWORD src1_sel:WORD_1\n\t" \
  "v_cvt_pk_bf16_f32 v230, v48, v49\n\t" \
  "v_smfmac_f32_16x16x64_bf16 v[80:83], v[216:219], v[200:207], v248\n\t" \
  "s_waitcnt lgkmcnt(9)\n\t" \
  "ds_read_b64_tr_b16 v[216:217], v249 offset:18432\n\t" \
  "v_fma_mix_f32 v168, v168, v225, 0 op_sel_hi:[0,1,0]\n\t" \
  "v_fma_mix_f32 v170, v170, v227, 0 op_sel_hi:[0,1,0]\n\t" \
  "v_cvt_pk_bf16_f32 v231, v50, v51\n\t" \
  "v_smfmac_f32_16x16x64_bf16 v[12:15], v[220:223], v[176:183], v248\n\t" \
  "ds_read_u16_d16_hi v161, v161 offset:0\n\t" \
  "v_lshlrev_b32_sdwa v163, 1, v163 dst_sel:DWORD dst_unused:UNUSED_PAD src0_sel:DWORD src1_sel:WORD_1\n\t" \
  "v_fma_mix_f32 v169, v169, v224, 0 op_sel_hi:[0,1,0]\n\t" \
  "v_cvt_pk_bf16_f32 v232, v52, v53\n\t" \
  "v_smfmac_f32_16x16x64_bf16 v[36:39], v[220:223], v[184:191], v248\n\t" \
  "ds_read_b64_tr_b16 v[218:219], v249 offset:18688\n\t" \
  "v_fma_mix_f32 v171, v171, v226, 0 op_sel_hi:[0,1,0]\n\t" \
  "v_cvt_pk_bf16_f32 v233, v54, v55\n\t" \
  "v_smfmac_f32_16x16x64_bf16 v[60:63], v[220:223], v[192:199], v248\n\t" \
  "ds_read_u16_d16_hi v162, v162 offset:0\n\t" \
  "v_lshlrev_b32_sdwa v148, 1, v148 dst_sel:DWORD dst_unused:UNUSED_PAD src0_sel:DWORD src1_sel:WORD_1\n\t" \
  "v_cvt_pk_f16_f32 v224, v172, v173\n\t" \
  "v_smfmac_f32_16x16x64_bf16 v[84:87], v[220:223], v[200:207], v248\n\t" \
  "s_waitcnt lgkmcnt(9)\n\t" \
  "ds_read_b64_tr_b16 v[220:221], v249 offset:20480\n\t" \
  "v_cvt_pk_f16_f32 v226, v174, v175\n\t" \
  "v_pk_fma_f16 v225, v224, v224, %[h1] neg_lo:[1,0,0] neg_hi:[1,0,0]\n\t" \
  "v_smfmac_f32_16x16x64_bf16 v[16:19], v[208:211], v[176:183], v248\n\t" \
  "ds_read_u16_d16_hi v163, v163 offset:0\n\t" \
  "v_lshlrev_b32_sdwa v149, 1, v149 dst_sel:DWORD dst_unused:UNUSED_PAD src0_sel:DWORD src1_sel:WORD_1\n\t" \
  "v_pk_fma_f16 v227, v226, v226, %[h1] neg_lo:[1,0,0] neg_hi:[1,0,0]\n\t" \
  "v_smfmac_f32_16x16x64_bf16 v[40:43], v[208:211], v[184:191], v248\n\t" \
  "ds_read_b64_tr_b16 v[222:223], v249 offset:20736\n\t" \
  "buffer_store_dwordx4 v[230:233], v253, %[osrd], %[ob] offen offset:0 sc1\n\t" \
  "v_pk_mul_f16 v225, v224, v225\n\t" \
  "v_lshlrev_b32_sdwa v150, 1, v150 dst_sel:DWORD dst_unused:UNUSED_PAD src0_sel:DWORD src1_sel:WORD_1\n\t" \
  "v_smfmac_f32_16x16x64_bf16 v[64:67], v[208:211], v[192:199], v248\n\t" \
  "ds_read_b64 v[230:231], v252 offset:0\n\t" \
  "v_pk_mul_f16 v227, v226, v227\n\t" \
  "v_exp_f16 v224, v225\n\t" \
  "v_cvt_pk_bf16_f32 v234, v56, v57\n\t" \
  "v_smfmac_f32_16x16x64_bf16 v[88:91], v[208:211], v[200:207], v248\n\t" \
  "s_waitcnt lgkmcnt(9)\n\t" \
  "ds_read_b64_tr_b16 v[208:209], v249 offset:22528\n\t" \
  "ds_read_u16_d16_hi v148, v148 offset:0\n\t" \
  "v_cvt_pk_bf16_f32 v235, v58, v59\n\t" \
  "v_smfmac_f32_16x16x64_bf16 v[20:23], v[212:215], v[176:183], v248\n\t" \
  "ds_read_b64_tr_b16 v[210:211], v249 offset:22784\n\t" \
  "v_lshlrev_b32_sdwa v151, 1, v151 dst_sel:DWORD dst_unused:UNUSED_PAD src0_sel:DWORD src1_sel:WORD_1\n\t" \
  "v_exp_f16 v226, v227\n\t" \
  "v_cvt_pk_bf16_f32 v236, v60, v61\n\t" \
  "v_smfmac_f32_16x16x64_bf16 v[44:47], v[212:215], v[184:191], v248\n\t" \
  "ds_read_b64 v[232:233], v252 offset:8\n\t" \
  "v_lshlrev_b32_sdwa v156, 1, v156 dst_sel:DWORD dst_unused:UNUSED_PAD src0_sel:DWORD src1_sel:WORD_1\n\t" \
  "v_cvt_pk_bf16_f32 v237, v62, v63\n\t" \
  "v_smfmac_f32_16x16x64_bf16 v[68:71], v[212:215], v[192:199], v248\n\t" \
  "s_waitcnt lgkmcnt(4)\n\t" \
  "ds_read_u16_d16_hi v149, v149 offset:0\n\t" \
  "v_mfma_f32_16x16x16_bf16 v[48:51], v[246:247], v[230:231], 0\n\t" \
  "ds_read_u16_d16_hi v150, v150 offset:0\n\t" \
  "v_exp_f16_sdwa v224, v225 dst_sel:WORD_1 dst_unused:UNUSED_PRESERVE src0_sel:WORD_1\n\t" \
  "v_smfmac_f32_16x16x64_bf16 v[92:95], v[212:215], v[200:207], v248\n\t" \
  "ds_read_b64_tr_b16 v[212:213], v249 offset:18944\n\t" \
  "v_lshlrev_b32_sdwa v157, 1, v157 dst_sel:DWORD dst_unused:UNUSED_PAD src0_sel:DWORD src1_sel:WORD_1\n\t" \
  "v_exp_f16_sdwa v226, v227 dst_sel:WORD_1 dst_unused:UNUSED_PRESERVE src0_sel:WORD_1\n\t" \
  "v_mfma_f32_16x16x32_bf16 v[176:179], v[216:219], v[96:99], 0\n\t" \
  "s_waitcnt lgkmcnt(3)\n\t" \
  "ds_read_u16_d16_hi v151, v151 offset:0\n\t" \
  "ds_read_b64_tr_b16 v[214:215], v249 offset:19200\n\t" \
  "v_mfma_f32_16x16x16_bf16 v[52:55], v[246:247], v[232:233], 0\n\t" \
  "buffer_store_dwordx4 v[234:237], v253, %[osrd], %[ob] offen offset:64 sc1\n\t" \
  "v_mfma_f32_16x16x32_bf16 v[184:187], v[216:219], v[108:111], 0\n\t" \
  "ds_read_u16_d16_hi v156, v156 offset:0\n\t" \
  "v_lshlrev_b32_sdwa v158, 1, v158 dst_sel:DWORD dst_unused:UNUSED_PAD src0_sel:DWORD src1_sel:WORD_1\n\t" \
  "v_pk_fma_f16 v224, v224, %[hc], %[hc]\n\t" \
  "v_mfma_f32_16x16x32_bf16 v[192:195], v[216:219], v[120:123], 0\n\t" \
  "ds_read_b64 v[234:235], v252 offset:64\n\t" \
  "v_pk_fma_f16 v226, v226, %[hc], %[hc]\n\t" \
  "v_rcp_f16 v225, v224\n\t" \
  "v_cvt_pk_bf16_f32 v238, v64, v65\n\t" \
  "v_mfma_f32_16x16x32_bf16 v[200:203], v[216:219], v[132:135], 0\n\t" \
  "ds_read_b64_tr_b16 v[216:217], v249 offset:20992\n\t" \
  "v_lshlrev_b32_sdwa v159, 1, v159 dst_sel:DWORD dst_unused:UNUSED_PAD src0_sel:DWORD src1_sel:WORD_1\n\t" \
  "v_cvt_pk_bf16_f32 v239, v66, v67\n\t" \
  "v_mfma_f32_16x16x32_bf16 v[176:179], v[220:223], v[100:103], v[176:179]\n\t" \
  "ds_read_b64 v[236:237], v252 offset:72\n\t" \
  "v_rcp_f16 v227, v226\n\t" \
  "v_cvt_pk_bf16_f32 v240, v68, v69\n\t" \
  "v_mfma_f32_16x16x32_bf16 v[184:187], v[220:223], v[112:115], v[184:187]\n\t" \
  "ds_read_b64_tr_b16 v[218:219], v249 offset:21248\n\t" \
  "v_lshlrev_b32_sdwa v164, 1, v164 dst_sel:DWORD dst_unused:UNUSED_PAD src0_sel:DWORD src1_sel:WORD_1\n\t" \
  "v_rcp_f16_sdwa v224, v224 dst_sel:DWORD dst_unused:UNUSED_PAD src0_sel:WORD_1\n\t" \
  "v_cvt_pk_bf16_f32 v241, v70, v71\n\t" \
  "v_mfma_f32_16x16x32_bf16 v[192:195], v[220:223], v[124:127], v[192:195]\n\t" \
  "s_waitcnt lgkmcnt(3)\n\t" \
  "ds_read_u16_d16_hi v157, v157 offset:0\n\t" \
  "v_mfma_f32_16x16x16_bf16 v[56:59], v[246:247], v[234:235], 0\n\t" \
  "ds_read_u16_d16_hi v158, v158 offset:0\n\t" \
  "v_mfma_f32_16x16x32_bf16 v[200:203], v[220:223], v[136:139], v[200:203]\n\t" \
  "ds_read_b64_tr_b16 v[220:221], v249 offset:23040\n\t" \
  "v_lshlrev_b32_sdwa v165, 1, v165 dst_sel:DWORD dst_unused:UNUSED_PAD src0_sel:DWORD src1_sel:WORD_1\n\t" \
  "v_rcp_f16_sdwa v226, v226 dst_sel:DWORD dst_unused:UNUSED_PAD src0_sel:WORD_1\n\t" \
  "v_mfma_f32_16x16x32_bf16 v[176:179], v[208:211], v[104:107], v[176:179]\n\t" \
  "s_waitcnt lgkmcnt(4)\n\t" \
  "ds_read_u16_d16_hi v159, v159 offset:0\n\t" \
  "ds_read_b64_tr_b16 v[222:223], v249 offset:23296\n\t" \
  "v_mfma_f32_16x16x16_bf16 v[60:63], v[246:247], v[236:237], 0\n\t" \
  "buffer_store_dwordx4 v[238:241], v253, %[osrd], %[ob] offen offset:128 sc1\n\t" \
  "v_lshlrev_b32_sdwa v166, 1, v166 dst_sel:DWORD dst_unused:UNUSED_PAD src0_sel:DWORD src1_sel:WORD_1\n\t" \
  "v_mfma_f32_16x16x32_bf16 v[184:187], v[208:211], v[116:119], v[184:187]\n\t" \
  "ds_read_u16_d16_hi v164, v164 offset:0\n\t" \
  "v_fma_mix_f32 v172, v172, v225, 0 op_sel_hi:[0,1,0]\n\t" \
  "v_fma_mix_f32 v174, v174, v227, 0 op_sel_hi:[0,1,0]\n\t" \
  "v_mfma_f32_16x16x32_bf16 v[192:195], v[208:211], v[128:131], v[192:195]\n\t" \
  "ds_read_b64 v[238:239], v252 offset:128\n\t" \
  "v_lshlrev_b32_sdwa v167, 1, v167 dst_sel:DWORD dst_unused:UNUSED_PAD src0_sel:DWORD src1_sel:WORD_1\n\t" \
  "v_fma_mix_f32 v173, v173, v224, 0 op_sel_hi:[0,1,0]\n\t" \
  "v_mfma_f32_16x16x32_bf16 v[200:203], v[208:211], v[140:143], v[200:203]\n\t" \
  "s_barrier\n\t" \
  "ds_read_b64_tr_b16 v[208:209], v250 offset:12320\n\t" \
  "ds_read_u16_d16_hi v165, v165 offset:0\n\t" \
  "v_fma_mix_f32 v175, v175, v226, 0 op_sel_hi:[0,1,0]\n\t" \
  "v_mfma_f32_16x16x32_bf16 v[180:183], v[212:215], v[96:99], 0\n\t" \
  "ds_read_b64_tr_b16 v[210:211], v250 offset:13088\n\t" \
  "v_mfma_f32_16x16x32_bf16 v[188:191], v[212:215], v[108:111], 0\n\t" \
  "ds_read_b64 v[240:241], v252 offset:136\n\t" \
  "v_mfma_f32_16x16x32_bf16 v[196:199], v[212:215], v[120:123], 0\n\t" \
  "s_waitcnt lgkmcnt(4)\n\t" \
  "ds_read_u16_d16_hi v166, v166 offset:0\n\t" \
  "v_mfma_f32_16x16x16_bf16 v[64:67], v[246:247], v[238:239], 0\n\t" \
  "ds_read_u16_d16_hi v167, v167 offset:0\n\t" \
  "v_lshlrev_b32_sdwa v176, 1, v176 dst_sel:DWORD dst_unused:UNUSED_PAD src0_sel:DWORD src1_sel:WORD_1\n\t" \
  "v_cvt_pk_f16_f32 v224, v200, v201\n\t" \
  "v_mfma_f32_16x16x32_bf16 v[204:207], v[212:215], v[132:135], 0\n\t" \
  "ds_read_b64_tr_b16 v[212:213], v250 offset:12328\n\t" \
  "v_cvt_pk_f16_f32 v226, v202, v203\n\t" \
  "v_pk_fma_f16 v225, v224, v224, %[h1] neg_lo:[1,0,0] neg_hi:[1,0,0]\n\t" \
  "v_mfma_f32_16x16x32_bf16 v[180:183], v[216:219], v[100:103], v[180:183]\n\t" \
  "s_waitcnt lgkmcnt(3)\n\t" \
  "ds_read_b64_tr_b16 v[214:215], v250 offset:13096\n\t" \
  "v_mfma_f32_16x16x16_bf16 v[68:71], v[246:247], v[240:241], 0\n\t" \
  "v_lshlrev_b32_sdwa v177, 1, v177 dst_sel:DWORD dst_unused:UNUSED_PAD src0_sel:DWORD src1_sel:WORD_1\n\t" \
  "v_mfma_f32_16x16x32_bf16 v[188:191], v[216:219], v[112:115], v[188:191]\n\t" \
  "ds_read_u16_d16_hi v176, v176 offset:0\n\t" \
  "v_pk_fma_f16 v227, v226, v226, %[h1] neg_lo:[1,0,0] neg_hi:[1,0,0]\n\t" \
  "v_pk_mul_f16 v225, v224, v225\n\t" \
  "v_mfma_f32_16x16x32_bf16 v[196:199], v[216:219], v[124:127], v[196:199]\n\t" \
  "v_lshlrev_b32_sdwa v178, 1, v178 dst_sel:DWORD dst_unused:UNUSED_PAD src0_sel:DWORD src1_sel:WORD_1\n\t" \
  "v_pk_mul_f16 v227, v226, v227\n\t" \
  "v_mfma_f32_16x16x32_bf16 v[204:207], v[216:219], v[136:139], v[204:207]\n\t" \
  "ds_read_b64_tr_b16 v[216:217], v250 offset:12384\n\t" \
  "v_exp_f16 v224, v225\n\t" \
  "v_mfma_f32_16x16x32_bf16 v[180:183], v[220:223], v[104:107], v[180:183]\n\t" \
  "ds_read_u16_d16_hi v177, v177 offset:0\n\t" \
  "v_lshlrev_b32_sdwa v179, 1, v179 dst_sel:DWORD dst_unused:UNUSED_PAD src0_sel:DWORD src1_sel:WORD_1\n\t" \
  "v_mfma_f32_16x16x32_bf16 v[188:191], v[220:223], v[116:119], v[188:191]\n\t" \
  "ds_read_b64_tr_b16 v[218:219], v250 offset:13152\n\t" \
  "v_exp_f16 v226, v227\n\t" \
  "v_mfma_f32_16x16x32_bf16 v[196:199], v[220:223], v[128:131], v[196:199]\n\t" \
  "ds_read_u16_d16_hi v178, v178 offset:0\n\t" \
  "v_lshlrev_b32_sdwa v184, 1, v184 dst_sel:DWORD dst_unused:UNUSED_PAD src0_sel:DWORD src1_sel:WORD_1\n\t" \
  "v_mfma_f32_16x16x32_bf16 v[204:207], v[220:223], v[140:143], v[204:207]\n\t" \
  "ds_read_b64_tr_b16 v[220:221], v250 offset:12392\n\t" \
  "v_exp_f16_sdwa v224, v225 dst_sel:WORD_1 dst_unused:UNUSED_PRESERVE src0_sel:WORD_1\n\t" \
  "v_smfmac_f32_16x16x64_bf16 v[0:3], v[208:211], v[144:151], v248\n\t" \
  "ds_read_u16_d16_hi v179, v179 offset:0\n\t" \
  "v_lshlrev_b32_sdwa v185, 1, v185 dst_sel:DWORD dst_unused:UNUSED_PAD src0_sel:DWORD src1_sel:WORD_1\n\t" \
  "v_exp_f16_sdwa v226, v227 dst_sel:WORD_1 dst_unused:UNUSED_PRESERVE src0_sel:WORD_1\n\t" \
  "v_smfmac_f32_16x16x64_bf16 v[24:27], v[208:211], v[152:159], v248\n\t" \
  "s_waitcnt lgkmcnt(9)\n\t" \
  "ds_read_b64_tr_b16 v[222:223], v250 offset:13160\n\t" \
  "v_smfmac_f32_16x16x64_bf16 v[48:51], v[208:211], v[160:167], v248\n\t" \
  "ds_read_u16_d16_hi v184, v184 offset:0\n\t" \
  "v_lshlrev_b32_sdwa v186, 1, v186 dst_sel:DWORD dst_unused:UNUSED_PAD src0_sel:DWORD src1_sel:WORD_1\n\t" \
  "v_pk_fma_f16 v224, v224, %[hc], %[hc]\n\t" \
  "v_smfmac_f32_16x16x64_bf16 v[72:75], v[208:211], v[168:175], v248\n\t" \
  "s_waitcnt lgkmcnt(9)\n\t" \
  "ds_read_b64_tr_b16 v[208:209], v250 offset:12448\n\t" \
  "v_pk_fma_f16 v226, v226, %[hc], %[hc]\n\t" \
  "v_rcp_f16 v225, v224\n\t" \
  "v_smfmac_f32_16x16x64_bf16 v[4:7], v[212:215], v[144:151], v248\n\t" \
  "ds_read_u16_d16_hi v185, v185 offset:0\n\t" \
  "v_smfmac_f32_16x16x64_bf16 v[28:31], v[212:215], v[152:159], v248\n\t" \
  "s_add_u32 %[ob], %[ob], %[dob]\n\t" \
  "ds_read_b64_tr_b16 v[210:211], v250 offset:13216\n\t" \
  "v_lshlrev_b32_sdwa v187, 1, v187 dst_sel:DWORD dst_unused:UNUSED_PAD src0_sel:DWORD src1_sel:WORD_1\n\t" \
  "v_rcp_f16 v227, v226\n\t" \
  "v_smfmac_f32_16x16x64_bf16 v[52:55], v[212:215], v[160:167], v248\n\t" \
  "ds_read_u16_d16_hi v186, v186 offset:0\n\t" \
  "v_lshlrev_b32_sdwa v192, 1, v192 dst_sel:DWORD dst_unused:UNUSED_PAD src0_sel:DWORD src1_sel:WORD_1\n\t" \
  "v_smfmac_f32_16x16x64_bf16 v[76:79], v[212:215], v[168:175], v248\n\t" \
  "s_waitcnt lgkmcnt(9)\n\t" \
  "ds_read_b64_tr_b16 v[212:213], v250 offset:12456\n\t" \
  "v_rcp_f16_sdwa v224, v224 dst_sel:DWORD dst_unused:UNUSED_PAD src0_sel:WORD_1\n\t" \
  "v_smfmac_f32_16x16x64_bf16 v[8:11], v[216:219], v[144:151], v248\n\t" \
  "s_add_u32 %[rs], %[rs], 1\n\t" \
  "ds_read_u16_d16_hi v187, v187 offset:0\n\t" \
  "v_lshlrev_b32_sdwa v193, 1, v193 dst_sel:DWORD dst_unused:UNUSED_PAD src0_sel:DWORD src1_sel:WORD_1\n\t" \
  "v_smfmac_f32_16x16x64_bf16 v[32:35], v[216:219], v[152:159], v248\n\t" \
  "s_cmp_eq_u32 %[rs], 6\n\t" \
  "ds_read_b64_tr_b16 v[214:215], v250 offset:13224\n\t" \
  "v_rcp_f16_sdwa v226, v226 dst_sel:DWORD dst_unused:UNUSED_PAD src0_sel:WORD_1\n\t" \
  "v_smfmac_f32_16x16x64_bf16 v[56:59], v[216:219], v[160:167], v248\n\t" \
  "ds_read_u16_d16_hi v192, v192 offset:0\n\t" \
  "v_lshlrev_b32_sdwa v194, 1, v194 dst_sel:DWORD dst_unused:UNUSED_PAD src0_sel:DWORD src1_sel:WORD_1\n\t" \
  "v_smfmac_f32_16x16x64_bf16 v[80:83], v[216:219], v[168:175], v248\n\t" \
  "s_waitcnt lgkmcnt(9)\n\t" \
  "ds_read_b64_tr_b16 v[216:217], v249 offset:0\n\t" \
  "v_fma_mix_f32 v200, v200, v225, 0 op_sel_hi:[0,1,0]\n\t" \
  "v_fma_mix_f32 v202, v202, v227, 0 op_sel_hi:[0,1,0]\n\t" \
  "v_smfmac_f32_16x16x64_bf16 v[12:15], v[220:223], v[144:151], v248\n\t" \
  "s_cselect_b32 %[rs], 0, %[rs]\n\t" \
  "ds_read_u16_d16_hi v193, v193 offset:0\n\t" \
  "v_lshlrev_b32_sdwa v195, 1, v195 dst_sel:DWORD dst_unused:UNUSED_PAD src0_sel:DWORD src1_sel:WORD_1\n\t" \
  "v_fma_mix_f32 v201, v201, v224, 0 op_sel_hi:[0,1,0]\n\t" \
  "v_smfmac_f32_16x16x64_bf16 v[36:39], v[220:223], v[152:159], v248\n\t" \
  "s_cselect_b32 %[t0], -75920, %[ps]\n\t" \
  "ds_read_b64_tr_b16 v[218:219], v249 offset:256\n\t" \
  "v_fma_mix_f32 v203, v203, v226, 0 op_sel_hi:[0,1,0]\n\t" \
  "v_smfmac_f32_16x16x64_bf16 v[60:63], v[220:223], v[160:167], v248\n\t" \
  "ds_read_u16_d16_hi v194, v194 offset:0\n\t" \
  "v_lshlrev_b32_sdwa v180, 1, v180 dst_sel:DWORD dst_unused:UNUSED_PAD src0_sel:DWORD src1_sel:WORD_1\n\t" \
  "v_cvt_pk_f16_f32 v224, v204, v205\n\t" \
  "v_add_u32 v252, %[t0], v252\n\t" \
  "v_smfmac_f32_16x16x64_bf16 v[84:87], v[220:223], v[168:175], v248\n\t" \
  "s_waitcnt lgkmcnt(9)\n\t" \
  "ds_read_b64_tr_b16 v[220:221], v249 offset:2048\n\t" \
  "v_cvt_pk_f16_f32 v226, v206, v207\n\t" \
  "v_pk_fma_f16 v225, v224, v224, %[h1] neg_lo:[1,0,0] neg_hi:[1,0,0]\n\t" \
  "v_smfmac_f32_16x16x64_bf16 v[16:19], v[208:211], v[144:151], v248\n\t" \
  "ds_read_u16_d16_hi v195, v195 offset:0\n\t" \
  "v_lshlrev_b32_sdwa v181, 1, v181 dst_sel:DWORD dst_unused:UNUSED_PAD src0_sel:DWORD src1_sel:WORD_1\n\t" \
  "v_pk_fma_f16 v227, v226, v226, %[h1] neg_lo:[1,0,0] neg_hi:[1,0,0]\n\t" \
  "v_smfmac_f32_16x16x64_bf16 v[40:43], v[208:211], v[152:159], v248\n\t" \
  "ds_read_b64_tr_b16 v[222:223], v249 offset:2304\n\t" \
  "v_pk_mul_f16 v225, v224, v225\n\t" \
  "v_lshlrev_b32_sdwa v182, 1, v182 dst_sel:DWORD dst_unused:UNUSED_PAD src0_sel:DWORD src1_sel:WORD_1\n\t" \
  "v_smfmac_f32_16x16x64_bf16 v[64:67], v[208:211], v[160:167], v248\n\t" \
  "ds_read_u16_d16_hi v180, v180 offset:0\n\t" \
  "v_pk_mul_f16 v227, v226, v227\n\t" \
  "v_exp_f16 v224, v225\n\t" \
  "v_smfmac_f32_16x16x64_bf16 v[88:91], v[208:211], v[168:175], v248\n\t" \
  "s_waitcnt lgkmcnt(9)\n\t" \
  "ds_read_b64_tr_b16 v[208:209], v249 offset:4096\n\t" \
  "v_smfmac_f32_16x16x64_bf16 v[20:23], v[212:215], v[144:151], v248\n\t" \
  "ds_read_u16_d16_hi v181, v181 offset:0\n\t" \
  "v_lshlrev_b32_sdwa v183, 1, v183 dst_sel:DWORD dst_unused:UNUSED_PAD src0_sel:DWORD src1_sel:WORD_1\n\t" \
  "v_exp_f16 v226, v227\n\t" \
  "v_smfmac_f32_16x16x64_bf16 v[44:47], v[212:215], v[152:159], v248\n\t" \
  "ds_read_b64_tr_b16 v[210:211], v249 offset:4352\n\t" \
  "v_lshlrev_b32_sdwa v188, 1, v188 dst_sel:DWORD dst_unused:UNUSED_PAD src0_sel:DWORD src1_sel:WORD_1\n\t" \
  "v_smfmac_f32_16x16x64_bf16 v[68:71], v[212:215], v[160:167], v248\n\t" \
  "ds_read_u16_d16_hi v182, v182 offset:0\n\t" \
  "v_exp_f16_sdwa v224, v225 dst_sel:WORD_1 dst_unused:UNUSED_PRESERVE src0_sel:WORD_1\n\t" \
  "v_smfmac_f32_16x16x64_bf16 v[92:95], v[212:215], v[168:175], v248\n\t" \
  "s_waitcnt lgkmcnt(9)\n\t" \
  "ds_read_b64_tr_b16 v[212:213], v249 offset:512\n\t" \
  "v_lshlrev_b32_sdwa v189, 1, v189 dst_sel:DWORD dst_unused:UNUSED_PAD src0_sel:DWORD src1_sel:WORD_1\n\t" \
  "v_exp_f16_sdwa v226, v227 dst_sel:WORD_1 dst_unused:UNUSED_PRESERVE src0_sel:WORD_1\n\t" \
  "v_mfma_f32_16x16x32_bf16 v[144:147], v[216:219], v[96:99], 0\n\t" \
  "ds_read_u16_d16_hi v183, v183 offset:0\n\t" \
  "v_mfma_f32_16x16x32_bf16 v[152:155], v[216:219], v[108:111], 0\n\t" \
  "ds_read_b64_tr_b16 v[214:215], v249 offset:768\n\t" \
  "v_lshlrev_b32_sdwa v190, 1, v190 dst_sel:DWORD dst_unused:UNUSED_PAD src0_sel:DWORD src1_sel:WORD_1\n\t" \
  "v_pk_fma_f16 v224, v224, %[hc], %[hc]\n\t" \
  "v_mfma_f32_16x16x32_bf16 v[160:163], v[216:219], v[120:123], 0\n\t" \
  "ds_read_u16_d16_hi v188, v188 offset:0\n\t" \
  "v_pk_fma_f16 v226, v226, %[hc], %[hc]\n\t" \
  "v_rcp_f16 v225, v224\n\t" \
  "v_mfma_f32_16x16x32_bf16 v[168:171], v[216:219], v[132:135], 0\n\t" \
  "s_waitcnt lgkmcnt(9)\n\t" \
  "ds_read_b64_tr_b16 v[216:217], v249 offset:2560\n\t" \
  "v_lshlrev_b32_sdwa v191, 1, v191 dst_sel:DWORD dst_unused:UNUSED_PAD src0_sel:DWORD src1_sel:WORD_1\n\t" \
  "v_mfma_f32_16x16x32_bf16 v[144:147], v[220:223], v[100:103], v[144:147]\n\t" \
  "ds_read_u16_d16_hi v189, v189 offset:0\n\t" \
  "v_rcp_f16 v227, v226\n\t" \
  "v_mfma_f32_16x16x32_bf16 v[152:155], v[220:223], v[112:115], v[152:155]\n\t" \
  "ds_read_b64_tr_b16 v[218:219], v249 offset:2816\n\t" \
  "v_lshlrev_b32_sdwa v196, 1, v196 dst_sel:DWORD dst_unused:UNUSED_PAD src0_sel:DWORD src1_sel:WORD_1\n\t" \
  "v_rcp_f16_sdwa v224, v224 dst_sel:DWORD dst_unused:UNUSED_PAD src0_sel:WORD_1\n\t" \
  "v_mfma_f32_16x16x32_bf16 v[160:163], v[220:223], v[124:127], v[160:163]\n\t" \
  "ds_read_u16_d16_hi v190, v190 offset:0\n\t" \
  "v_mfma_f32_16x16x32_bf16 v[168:171], v[220:223], v[136:139], v[168:171]\n\t" \
  "s_waitcnt lgkmcnt(9)\n\t" \
  "ds_read_b64_tr_b16 v[220:221], v249 offset:4608\n\t" \
  "v_lshlrev_b32_sdwa v197, 1, v197 dst_sel:DWORD dst_unused:UNUSED_PAD src0_sel:DWORD src1_sel:WORD_1\n\t" \
  "v_rcp_f16_sdwa v226, v226 dst_sel:DWORD dst_unused:UNUSED_PAD src0_sel:WORD_1\n\t" \
  "v_mfma_f32_16x16x32_bf16 v[144:147], v[208:211], v[104:107], v[144:147]\n\t" \
  "ds_read_u16_d16_hi v191, v191 offset:0\n\t" \
  "v_lshlrev_b32_sdwa v198, 1, v198 dst_sel:DWORD dst_unused:UNUSED_PAD src0_sel:DWORD src1_sel:WORD_1\n\t" \
  "v_mfma_f32_16x16x32_bf16 v[152:155], v[208:211], v[116:119], v[152:155]\n\t" \
  "ds_read_b64_tr_b16 v[222:223], v249 offset:4864\n\t" \
  "v_fma_mix_f32 v204, v204, v225, 0 op_sel_hi:[0,1,0]\n\t" \
  "v_fma_mix_f32 v206, v206, v227, 0 op_sel_hi:[0,1,0]\n\t" \
  "v_mfma_f32_16x16x32_bf16 v[160:163], v[208:211], v[128:131], v[160:163]\n\t" \
  "ds_read_u16_d16_hi v196, v196 offset:0\n\t" \
  "v_lshlrev_b32_sdwa v199, 1, v199 dst_sel:DWORD dst_unused:UNUSED_PAD src0_sel:DWORD src1_sel:WORD_1\n\t" \
  "v_fma_mix_f32 v205, v205, v224, 0 op_sel_hi:[0,1,0]\n\t" \
  "v_mfma_f32_16x16x32_bf16 v[168:171], v[208:211], v[140:143], v[168:171]\n\t" \
  "s_barrier\n\t" \
  "ds_read_b64_tr_b16 v[208:209], v250 offset:0\n\t" \
  "v_fma_mix_f32 v207, v207, v226, 0 op_sel_hi:[0,1,0]\n\t" \
  "s_waitcnt lgkmcnt(10)\n\t" \
  "v_mfma_f32_16x16x32_bf16 v[148:151], v[212:215], v[96:99], 0\n\t" \
  "ds_read_u16_d16_hi v197, v197 offset:0\n\t" \
  "v_mfma_f32_16x16x32_bf16 v[156:159], v[212:215], v[108:111], 0\n\t" \
  "ds_read_b64_tr_b16 v[210:211], v250 offset:768\n\t" \
  "v_mfma_f32_16x16x32_bf16 v[164:167], v[212:215], v[120:123], 0\n\t" \
  "ds_read_u16_d16_hi v198, v198 offset:0\n\t" \
  "v_lshlrev_b32_sdwa v144, 1, v144 dst_sel:DWORD dst_unused:UNUSED_PAD src0_sel:DWORD src1_sel:WORD_1\n\t" \
  "v_cvt_pk_f16_f32 v224, v168, v169\n\t" \
  "v_mfma_f32_16x16x32_bf16 v[172:175], v[212:215], v[132:135], 0\n\t" \
  "s_waitcnt lgkmcnt(9)\n\t" \
  "ds_read_b64_tr_b16 v[212:213], v250 offset:8\n\t" \
  "v_cvt_pk_f16_f32 v226, v170, v171\n\t" \
  "v_pk_fma_f16 v225, v224, v224, %[h1] neg_lo:[1,0,0] neg_hi:[1,0,0]\n\t" \
  "v_mfma_f32_16x16x32_bf16 v[148:151], v[216:219], v[100:103], v[148:151]\n\t" \
  "ds_read_u16_d16_hi v199, v199 offset:0\n\t" \
  "v_lshlrev_b32_sdwa v145, 1, v145 dst_sel:DWORD dst_unused:UNUSED_PAD src0_sel:DWORD src1_sel:WORD_1\n\t" \
  "v_mfma_f32_16x16x32_bf16 v[156:159], v[216:219], v[112:115], v[156:159]\n\t" \
  "ds_read_b64_tr_b16 v[214:215], v250 offset:776\n\t" \
  "v_pk_fma_f16 v227, v226, v226, %[h1] neg_lo:[1,0,0] neg_hi:[1,0,0]\n\t" \
  "v_pk_mul_f16 v225, v224, v225\n\t" \
  "v_mfma_f32_16x16x32_bf16 v[164:167], v[216:219], v[124:127], v[164:167]\n\t" \
  "ds_read_u16_d16_hi v144, v144 offset:0\n\t" \
  "v_lshlrev_b32_sdwa v146, 1, v146 dst_sel:DWORD dst_unused:UNUSED_PAD src0_sel:DWORD src1_sel:WORD_1\n\t" \
  "v_pk_mul_f16 v227, v226, v227\n\t" \
  "v_mfma_f32_16x16x32_bf16 v[172:175], v[216:219], v[136:139], v[172:175]\n\t" \
  "s_waitcnt lgkmcnt(9)\n\t" \
  "ds_read_b64_tr_b16 v[216:217], v250 offset:64\n\t" \
  "v_exp_f16 v224, v225\n\t" \
  "v_mfma_f32_16x16x32_bf16 v[148:151], v[220:223], v[104:107], v[148:151]\n\t" \
  "ds_read_u16_d16_hi v145, v145 offset:0\n\t" \
  "v_lshlrev_b32_sdwa v147, 1, v147 dst_sel:DWORD dst_unused:UNUSED_PAD src0_sel:DWORD src1_sel:WORD_1\n\t" \
  "v_mfma_f32_16x16x32_bf16 v[156:159], v[220:223], v[116:119], v[156:159]\n\t" \
  "ds_read_b64_tr_b16 v[218:219], v250 offset:832\n\t" \
  "v_exp_f16 v226, v227\n\t" \
  "v_mfma_f32_16x16x32_bf16 v[164:167], v[220:223], v[128:131], v[164:167]\n\t" \
  "ds_read_u16_d16_hi v146, v146 offset:0\n\t" \
  "v_lshlrev_b32_sdwa v152, 1, v152 dst_sel:DWORD dst_unused:UNUSED_PAD src0_sel:DWORD src1_sel:WORD_1\n\t" \
  "v_mfma_f32_16x16x32_bf16 v[172:175], v[220:223], v[140:143], v[172:175]\n\t" \
  "s_waitcnt lgkmcnt(9)\n\t" \
  "ds_read_b64_tr_b16 v[220:221], v250 offset:72\n\t" \
  "v_exp_f16_sdwa v224, v225 dst_sel:WORD_1 dst_unused:UNUSED_PRESERVE src0_sel:WORD_1\n\t" \
  "v_smfmac_f32_16x16x64_bf16 v[0:3], v[208:211], v[176:183], v248\n\t" \
  "ds_read_u16_d16_hi v147, v147 offset:0\n\t" \
  "v_lshlrev_b32_sdwa v153, 1, v153 dst_sel:DWORD dst_unused:UNUSED_PAD src0_sel:DWORD src1_sel:WORD_1\n\t" \
  "v_exp_f16_sdwa v226, v227 dst_sel:WORD_1 dst_unused:UNUSED_PRESERVE src0_sel:WORD_1\n\t" \
  "v_smfmac_f32_16x16x64_bf16 v[24:27], v[208:211], v[184:191], v248\n\t" \
  "s_waitcnt lgkmcnt(8)\n\t" \
  "ds_read_b64_tr_b16 v[222:223], v250 offset:840\n\t" \
  "v_smfmac_f32_16x16x64_bf16 v[48:51], v[208:211], v[192:199], v248\n\t" \
  "ds_read_u16_d16_hi v152, v152 offset:0\n\t" \
  "v_lshlrev_b32_sdwa v154, 1, v154 dst_sel:DWORD dst_unused:UNUSED_PAD src0_sel:DWORD src1_sel:WORD_1\n\t" \
  "v_pk_fma_f16 v224, v224, %[hc], %[hc]\n\t" \
  "v_smfmac_f32_16x16x64_bf16 v[72:75], v[208:211], v[200:207], v248\n\t" \
  "s_waitcnt lgkmcnt(9)\n\t" \
  "ds_read_b64_tr_b16 v[208:209], v250 offset:128\n\t" \
  "v_pk_fma_f16 v226, v226, %[hc], %[hc]\n\t" \
  "v_rcp_f16 v225, v224\n\t" \
  "v_smfmac_f32_16x16x64_bf16 v[4:7], v[212:215], v[176:183], v248\n\t" \
  "ds_read_u16_d16_hi v153, v153 offset:0\n\t" \
  "v_smfmac_f32_16x16x64_bf16 v[28:31], v[212:215], v[184:191], v248\n\t" \
  "ds_read_b64_tr_b16 v[210:211], v250 offset:896\n\t" \
  "v_lshlrev_b32_sdwa v155, 1, v155 dst_sel:DWORD dst_unused:UNUSED_PAD src0_sel:DWORD src1_sel:WORD_1\n\t" \
  "v_rcp_f16 v227, v226\n\t" \
  "v_smfmac_f32_16x16x64_bf16 v[52:55], v[212:215], v[192:199], v248\n\t" \
  "ds_read_b128 v[132:135], v251 offset:0\n\t" \
  "v_lshlrev_b32_sdwa v160, 1, v160 dst_sel:DWORD dst_unused:UNUSED_PAD src0_sel:DWORD src1_sel:WORD_1\n\t" \
  "v_smfmac_f32_16x16x64_bf16 v[76:79], v[212:215], v[200:207], v248\n\t" \
  "s_waitcnt lgkmcnt(9)\n\t" \
  "ds_read_b64_tr_b16 v[212:213], v250 offset:136\n\t" \
  "v_rcp_f16_sdwa v224, v224 dst_sel:DWORD dst_unused:UNUSED_PAD src0_sel:WORD_1\n\t" \
  "v_smfmac_f32_16x16x64_bf16 v[8:11], v[216:219], v[176:183], v248\n\t" \
  "ds_read_u16_d16_hi v154, v154 offset:0\n\t" \
  "v_lshlrev_b32_sdwa v161, 1, v161 dst_sel:DWORD dst_unused:UNUSED_PAD src0_sel:DWORD src1_sel:WORD_1\n\t" \
  "v_smfmac_f32_16x16x64_bf16 v[32:35], v[216:219], v[184:191], v248\n\t" \
  "ds_read_b64_tr_b16 v[214:215], v250 offset:904\n\t" \
  "v_rcp_f16_sdwa v226, v226 dst_sel:DWORD dst_unused:UNUSED_PAD src0_sel:WORD_1\n\t" \
  "v_smfmac_f32_16x16x64_bf16 v[56:59], v[216:219], v[192:199], v248\n\t" \
  "ds_read_u16_d16_hi v155, v155 offset:0\n\t" \
  "v_lshlrev_b32_sdwa v162, 1, v162 dst_sel:DWORD dst_unused:UNUSED_PAD src0_sel:DWORD src1_sel:WORD_1\n\t" \
  "v_smfmac_f32_16x16x64_bf16 v[80:83], v[216:219], v[200:207], v248\n\t" \
  "s_waitcnt lgkmcnt(9)\n\t" \
  "ds_read_b64_tr_b16 v[216:217], v249 offset:6144\n\t" \
  "ds_read_b128 v[136:139], v251 offset:64\n\t" \
  "v_fma_mix_f32 v168, v168, v225, 0 op_sel_hi:[0,1,0]\n\t" \
  "v_fma_mix_f32 v170, v170, v227, 0 op_sel_hi:[0,1,0]\n\t" \
  "v_smfmac_f32_16x16x64_bf16 v[12:15], v[220:223], v[176:183], v248\n\t" \
  "ds_read_b64_tr_b16 v[218:219], v249 offset:6400\n\t" \
  "v_lshlrev_b32_sdwa v163, 1, v163 dst_sel:DWORD dst_unused:UNUSED_PAD src0_sel:DWORD src1_sel:WORD_1\n\t" \
  "v_fma_mix_f32 v169, v169, v224, 0 op_sel_hi:[0,1,0]\n\t" \
  "v_smfmac_f32_16x16x64_bf16 v[36:39], v[220:223], v[184:191], v248\n\t" \
  "ds_read_u16_d16_hi v160, v160 offset:0\n\t" \
  "v_fma_mix_f32 v171, v171, v226, 0 op_sel_hi:[0,1,0]\n\t" \
  "v_smfmac_f32_16x16x64_bf16 v[60:63], v[220:223], v[192:199], v248\n\t" \
  "ds_read_u16_d16_hi v161, v161 offset:0\n\t" \
  "v_lshlrev_b32_sdwa v148, 1, v148 dst_sel:DWORD dst_unused:UNUSED_PAD src0_sel:DWORD src1_sel:WORD_1\n\t" \
  "v_cvt_pk_f16_f32 v224, v172, v173\n\t" \
  "v_smfmac_f32_16x16x64_bf16 v[84:87], v[220:223], v[200:207], v248\n\t" \
  "s_waitcnt lgkmcnt(10)\n\t" \
  "ds_read_b64_tr_b16 v[220:221], v249 offset:8192\n\t" \
  "ds_read_u16_d16_hi v162, v162 offset:0\n\t" \
  "v_cvt_pk_f16_f32 v226, v174, v175\n\t" \
  "v_pk_fma_f16 v225, v224, v224, %[h1] neg_lo:[1,0,0] neg_hi:[1,0,0]\n\t" \
  "v_smfmac_f32_16x16x64_bf16 v[16:19], v[208:211], v[176:183], v248\n\t" \
  "ds_read_b64_tr_b16 v[222:223], v249 offset:8448\n\t" \
  "v_lshlrev_b32_sdwa v149, 1, v149 dst_sel:DWORD dst_unused:UNUSED_PAD src0_sel:DWORD src1_sel:WORD_1\n\t" \
  "v_pk_fma_f16 v227, v226, v226, %[h1] neg_lo:[1,0,0] neg_hi:[1,0,0]\n\t" \
  "v_smfmac_f32_16x16x64_bf16 v[40:43], v[208:211], v[184:191], v248\n\t" \
  "ds_read_b128 v[140:143], v251 offset:128\n\t" \
  "v_pk_mul_f16 v225, v224, v225\n\t" \
  "v_lshlrev_b32_sdwa v150, 1, v150 dst_sel:DWORD dst_unused:UNUSED_PAD src0_sel:DWORD src1_sel:WORD_1\n\t" \
  "v_smfmac_f32_16x16x64_bf16 v[64:67], v[208:211], v[192:199], v248\n\t" \
  "ds_read_u16_d16_hi v163, v163 offset:0\n\t" \
  "v_pk_mul_f16 v227, v226, v227\n\t" \
  "v_exp_f16 v224, v225\n\t" \
  "v_smfmac_f32_16x16x64_bf16 v[88:91], v[208:211], v[200:207], v248\n\t" \
  "s_waitcnt lgkmcnt(11)\n\t" \
  "ds_read_b64_tr_b16 v[208:209], v249 offset:10240\n\t" \
  "v_smfmac_f32_16x16x64_bf16 v[20:23], v[212:215], v[176:183], v248\n\t" \
  "ds_read_u16_d16_hi v148, v148 offset:0\n\t" \
  "v_lshlrev_b32_sdwa v151, 1, v151 dst_sel:DWORD dst_unused:UNUSED_PAD src0_sel:DWORD src1_sel:WORD_1\n\t" \
  "v_exp_f16 v226, v227\n\t" \
  "v_smfmac_f32_16x16x64_bf16 v[44:47], v[212:215], v[184:191], v248\n\t" \
  "ds_read_b64_tr_b16 v[210:211], v249 offset:10496\n\t" \
  "v_lshlrev_b32_sdwa v156, 1, v156 dst_sel:DWORD dst_unused:UNUSED_PAD src0_sel:DWORD src1_sel:WORD_1\n\t" \
  "v_smfmac_f32_16x16x64_bf16 v[68:71], v[212:215], v[192:199], v248\n\t" \
  "ds_read_u16_d16_hi v149, v149 offset:0\n\t" \
  "v_exp_f16_sdwa v224, v225 dst_sel:WORD_1 dst_unused:UNUSED_PRESERVE src0_sel:WORD_1\n\t" \
  "v_smfmac_f32_16x16x64_bf16 v[92:95], v[212:215], v[200:207], v248\n\t" \
  "s_waitcnt lgkmcnt(11)\n\t" \
  "ds_read_b64_tr_b16 v[212:213], v249 offset:6656\n\t" \
  "v_lshlrev_b32_sdwa v157, 1, v157 dst_sel:DWORD dst_unused:UNUSED_PAD src0_sel:DWORD src1_sel:WORD_1\n\t" \
  "v_exp_f16_sdwa v226, v227 dst_sel:WORD_1 dst_unused:UNUSED_PRESERVE src0_sel:WORD_1\n\t" \
  "v_mfma_f32_16x16x32_bf16 v[176:179], v[216:219], v[96:99], 0\n\t" \
  "ds_read_u16_d16_hi v150, v150 offset:0\n\t" \
  "v_mfma_f32_16x16x32_bf16 v[184:187], v[216:219], v[108:111], 0\n\t" \
  "ds_read_b64_tr_b16 v[214:215], v249 offset:6912\n\t" \
  "v_lshlrev_b32_sdwa v158, 1, v158 dst_sel:DWORD dst_unused:UNUSED_PAD src0_sel:DWORD src1_sel:WORD_1\n\t" \
  "v_pk_fma_f16 v224, v224, %[hc], %[hc]\n\t" \
  "v_mfma_f32_16x16x32_bf16 v[192:195], v[216:219], v[120:123], 0\n\t" \
  "ds_read_u16_d16_hi v151, v151 offset:0\n\t" \
  "v_pk_fma_f16 v226, v226, %[hc], %[hc]\n\t" \
  "v_rcp_f16 v225, v224\n\t" \
  "v_mfma_f32_16x16x32_bf16 v[200:203], v[216:219], v[132:135], 0\n\t" \
  "s_waitcnt lgkmcnt(10)\n\t" \
  "ds_read_b64_tr_b16 v[216:217], v249 offset:8704\n\t" \
  "v_lshlrev_b32_sdwa v159, 1, v159 dst_sel:DWORD dst_unused:UNUSED_PAD src0_sel:DWORD src1_sel:WORD_1\n\t" \
  "v_mfma_f32_16x16x32_bf16 v[176:179], v[220:223], v[100:103], v[176:179]\n\t" \
  "ds_read_u16_d16_hi v156, v156 offset:0\n\t" \
  "v_rcp_f16 v227, v226\n\t" \
  "v_mfma_f32_16x16x32_bf16 v[184:187], v[220:223], v[112:115], v[184:187]\n\t" \
  "ds_read_b64_tr_b16 v[218:219], v249 offset:8960\n\t" \
  "v_lshlrev_b32_sdwa v164, 1, v164 dst_sel:DWORD dst_unused:UNUSED_PAD src0_sel:DWORD src1_sel:WORD_1\n\t" \
  "v_rcp_f16_sdwa v224, v224 dst_sel:DWORD dst_unused:UNUSED_PAD src0_sel:WORD_1\n\t" \
  "v_mfma_f32_16x16x32_bf16 v[192:195], v[220:223], v[124:127], v[192:195]\n\t" \
  "ds_read_u16_d16_hi v157, v157 offset:0\n\t" \
  "v_mfma_f32_16x16x32_bf16 v[200:203], v[220:223], v[136:139], v[200:203]\n\t" \
  "s_waitcnt lgkmcnt(9)\n\t" \
  "ds_read_b64_tr_b16 v[220:221], v249 offset:10752\n\t" \
  "v_lshlrev_b32_sdwa v165, 1, v165 dst_sel:DWORD dst_unused:UNUSED_PAD src0_sel:DWORD src1_sel:WORD_1\n\t" \
  "v_rcp_f16_sdwa v226, v226 dst_sel:DWORD dst_unused:UNUSED_PAD src0_sel:WORD_1\n\t" \
  "v_mfma_f32_16x16x32_bf16 v[176:179], v[208:211], v[104:107], v[176:179]\n\t" \
  "ds_read_u16_d16_hi v158, v158 offset:0\n\t" \
  "v_lshlrev_b32_sdwa v166, 1, v166 dst_sel:DWORD dst_unused:UNUSED_PAD src0_sel:DWORD src1_sel:WORD_1\n\t" \
  "v_mfma_f32_16x16x32_bf16 v[184:187], v[208:211], v[116:119], v[184:187]\n\t" \
  "ds_read_b64_tr_b16 v[222:223], v249 offset:11008\n\t" \
  "v_fma_mix_f32 v172, v172, v225, 0 op_sel_hi:[0,1,0]\n\t" \
  "v_fma_mix_f32 v174, v174, v227, 0 op_sel_hi:[0,1,0]\n\t" \
  "v_mfma_f32_16x16x32_bf16 v[192:195], v[208:211], v[128:131], v[192:195]\n\t" \
  "ds_read_u16_d16_hi v159, v159 offset:0\n\t" \
  "v_lshlrev_b32_sdwa v167, 1, v167 dst_sel:DWORD dst_unused:UNUSED_PAD src0_sel:DWORD src1_sel:WORD_1\n\t" \
  "v_fma_mix_f32 v173, v173, v224, 0 op_sel_hi:[0,1,0]\n\t" \
  "v_mfma_f32_16x16x32_bf16 v[200:203], v[208:211], v[140:143], v[200:203]\n\t" \
  "s_barrier\n\t" \
  "ds_read_b64_tr_b16 v[208:209], v250 offset:6160\n\t" \
  "ds_read_u16_d16_hi v164, v164 offset:0\n\t" \
  "v_fma_mix_f32 v175, v175, v226, 0 op_sel_hi:[0,1,0]\n\t" \
  "s_waitcnt lgkmcnt(11)\n\t" \
  "v_mfma_f32_16x16x32_bf16 v[180:183], v[212:215], v[96:99], 0\n\t" \
  "ds_read_b64_tr_b16 v[210:211], v250 offset:6928\n\t" \
  "v_mfma_f32_16x16x32_bf16 v[188:191], v[212:215], v[108:111], 0\n\t" \
  "ds_read_u16_d16_hi v165, v165 offset:0\n\t" \
  "v_mfma_f32_16x16x32_bf16 v[196:199], v[212:215], v[120:123], 0\n\t" \
  "ds_read_u16_d16_hi v166, v166 offset:0\n\t" \
  "v_lshlrev_b32_sdwa v176, 1, v176 dst_sel:DWORD dst_unused:UNUSED_PAD src0_sel:DWORD src1_sel:WORD_1\n\t" \
  "v_cvt_pk_f16_f32 v224, v200, v201\n\t" \
  "v_mfma_f32_16x16x32_bf16 v[204:207], v[212:215], v[132:135], 0\n\t" \
  "s_waitcnt lgkmcnt(10)\n\t" \
  "ds_read_b64_tr_b16 v[212:213], v250 offset:6168\n\t" \
  "v_cvt_pk_f16_f32 v226, v202, v203\n\t" \
  "v_pk_fma_f16 v225, v224, v224, %[h1] neg_lo:[1,0,0] neg_hi:[1,0,0]\n\t" \
  "v_mfma_f32_16x16x32_bf16 v[180:183], v[216:219], v[100:103], v[180:183]\n\t" \
  "ds_read_u16_d16_hi v167, v167 offset:0\n\t" \
  "v_lshlrev_b32_sdwa v177, 1, v177 dst_sel:DWORD dst_unused:UNUSED_PAD src0_sel:DWORD src1_sel:WORD_1\n\t" \
  "v_mfma_f32_16x16x32_bf16 v[188:191], v[216:219], v[112:115], v[188:191]\n\t" \
  "ds_read_b64_tr_b16 v[214:215], v250 offset:6936\n\t" \
  "v_pk_fma_f16 v227, v226, v226, %[h1] neg_lo:[1,0,0] neg_hi:[1,0,0]\n\t" \
  "v_pk_mul_f16 v225, v224, v225\n\t" \
  "v_mfma_f32_16x16x32_bf16 v[196:199], v[216:219], v[124:127], v[196:199]\n\t" \
  "ds_read_u16_d16_hi v176, v176 offset:0\n\t" \
  "v_lshlrev_b32_sdwa v178, 1, v178 dst_sel:DWORD dst_unused:UNUSED_PAD src0_sel:DWORD src1_sel:WORD_1\n\t" \
  "v_pk_mul_f16 v227, v226, v227\n\t" \
  "v_mfma_f32_16x16x32_bf16 v[204:207], v[216:219], v[136:139], v[204:207]\n\t" \
  "s_waitcnt lgkmcnt(10)\n\t" \
  "ds_read_b64_tr_b16 v[216:217], v250 offset:6224\n\t" \
  "v_exp_f16 v224, v225\n\t" \
  "v_mfma_f32_16x16x32_bf16 v[180:183], v[220:223], v[104:107], v[180:183]\n\t" \
  "ds_read_u16_d16_hi v177, v177 offset:0\n\t" \
  "v_lshlrev_b32_sdwa v179, 1, v179 dst_sel:DWORD dst_unused:UNUSED_PAD src0_sel:DWORD src1_sel:WORD_1\n\t" \
  "v_mfma_f32_16x16x32_bf16 v[188:191], v[220:223], v[116:119], v[188:191]\n\t" \
  "ds_read_b64_tr_b16 v[218:219], v250 offset:6992\n\t" \
  "v_exp_f16 v226, v227\n\t" \
  "v_mfma_f32_16x16x32_bf16 v[196:199], v[220:223], v[128:131], v[196:199]\n\t" \
  "ds_read_u16_d16_hi v178, v178 offset:0\n\t" \
  "v_lshlrev_b32_sdwa v184, 1, v184 dst_sel:DWORD dst_unused:UNUSED_PAD src0_sel:DWORD src1_sel:WORD_1\n\t" \
  "v_mfma_f32_16x16x32_bf16 v[204:207], v[220:223], v[140:143], v[204:207]\n\t" \
  "s_waitcnt lgkmcnt(10)\n\t" \
  "ds_read_b64_tr_b16 v[220:221], v250 offset:6232\n\t" \
  "v_exp_f16_sdwa v224, v225 dst_sel:WORD_1 dst_unused:UNUSED_PRESERVE src0_sel:WORD_1\n\t" \
  "v_smfmac_f32_16x16x64_bf16 v[0:3], v[208:211], v[144:151], v248\n\t" \
  "ds_read_u16_d16_hi v179, v179 offset:0\n\t" \
  "v_lshlrev_b32_sdwa v185, 1, v185 dst_sel:DWORD dst_unused:UNUSED_PAD src0_sel:DWORD src1_sel:WORD_1\n\t" \
  "v_exp_f16_sdwa v226, v227 dst_sel:WORD_1 dst_unused:UNUSED_PRESERVE src0_sel:WORD_1\n\t" \
  "v_smfmac_f32_16x16x64_bf16 v[24:27], v[208:211], v[152:159], v248\n\t" \
  "s_waitcnt lgkmcnt(8)\n\t" \
  "ds_read_b64_tr_b16 v[222:223], v250 offset:7000\n\t" \
  "v_smfmac_f32_16x16x64_bf16 v[48:51], v[208:211], v[160:167], v248\n\t" \
  "ds_read_u16_d16_hi v184, v184 offset:0\n\t" \
  "v_lshlrev_b32_sdwa v186, 1, v186 dst_sel:DWORD dst_unused:UNUSED_PAD src0_sel:DWORD src1_sel:WORD_1\n\t" \
  "v_pk_fma_f16 v224, v224, %[hc], %[hc]\n\t" \
  "v_smfmac_f32_16x16x64_bf16 v[72:75], v[208:211], v[168:175], v248\n\t" \
  "s_waitcnt lgkmcnt(9)\n\t" \
  "ds_read_b64_tr_b16 v[208:209], v250 offset:6288\n\t" \
  "v_pk_fma_f16 v226, v226, %[hc], %[hc]\n\t" \
  "v_rcp_f16 v225, v224\n\t" \
  "v_smfmac_f32_16x16x64_bf16 v[4:7], v[212:215], v[144:151], v248\n\t" \
  "ds_read_u16_d16_hi v185, v185 offset:0\n\t" \
  "v_smfmac_f32_16x16x64_bf16 v[28:31], v[212:215], v[152:159], v248\n\t" \
  "ds_read_b64_tr_b16 v[210:211], v250 offset:7056\n\t" \
  "v_lshlrev_b32_sdwa v187, 1, v187 dst_sel:DWORD dst_unused:UNUSED_PAD src0_sel:DWORD src1_sel:WORD_1\n\t" \
  "v_rcp_f16 v227, v226\n\t" \
  "v_smfmac_f32_16x16x64_bf16 v[52:55], v[212:215], v[160:167], v248\n\t" \
  "ds_read_u16_d16_hi v186, v186 offset:0\n\t" \
  "v_lshlrev_b32_sdwa v192, 1, v192 dst_sel:DWORD dst_unused:UNUSED_PAD src0_sel:DWORD src1_sel:WORD_1\n\t" \
  "v_smfmac_f32_16x16x64_bf16 v[76:79], v[212:215], v[168:175], v248\n\t" \
  "s_waitcnt lgkmcnt(9)\n\t" \
  "ds_read_b64_tr_b16 v[212:213], v250 offset:6296\n\t" \
  "v_rcp_f16_sdwa v224, v224 dst_sel:DWORD dst_unused:UNUSED_PAD src0_sel:WORD_1\n\t" \
  "v_smfmac_f32_16x16x64_bf16 v[8:11], v[216:219], v[144:151], v248\n\t" \
  "ds_read_u16_d16_hi v187, v187 offset:0\n\t" \
  "v_lshlrev_b32_sdwa v193, 1, v193 dst_sel:DWORD dst_unused:UNUSED_PAD src0_sel:DWORD src1_sel:WORD_1\n\t" \
  "v_smfmac_f32_16x16x64_bf16 v[32:35], v[216:219], v[152:159], v248\n\t" \
  "ds_read_b64_tr_b16 v[214:215], v250 offset:7064\n\t" \
  "v_rcp_f16_sdwa v226, v226 dst_sel:DWORD dst_unused:UNUSED_PAD src0_sel:WORD_1\n\t" \
  "v_smfmac_f32_16x16x64_bf16 v[56:59], v[216:219], v[160:167], v248\n\t" \
  "ds_read_u16_d16_hi v192, v192 offset:0\n\t" \
  "v_lshlrev_b32_sdwa v194, 1, v194 dst_sel:DWORD dst_unused:UNUSED_PAD src0_sel:DWORD src1_sel:WORD_1\n\t" \
  "v_cvt_pk_bf16_f32 v230, v72, v73\n\t" \
  "v_smfmac_f32_16x16x64_bf16 v[80:83], v[216:219], v[168:175], v248\n\t" \
  "s_waitcnt lgkmcnt(9)\n\t" \
  "ds_read_b64_tr_b16 v[216:217], v249 offset:12288\n\t" \
  "v_fma_mix_f32 v200, v200, v225, 0 op_sel_hi:[0,1,0]\n\t" \
  "v_fma_mix_f32 v202, v202, v227, 0 op_sel_hi:[0,1,0]\n\t" \
  "v_cvt_pk_bf16_f32 v231, v74, v75\n\t" \
  "v_smfmac_f32_16x16x64_bf16 v[12:15], v[220:223], v[144:151], v248\n\t" \
  "ds_read_u16_d16_hi v193, v193 offset:0\n\t" \
  "v_lshlrev_b32_sdwa v195, 1, v195 dst_sel:DWORD dst_unused:UNUSED_PAD src0_sel:DWORD src1_sel:WORD_1\n\t" \
  "v_fma_mix_f32 v201, v201, v224, 0 op_sel_hi:[0,1,0]\n\t" \
  "v_cvt_pk_bf16_f32 v232, v76, v77\n\t" \
  "v_smfmac_f32_16x16x64_bf16 v[36:39], v[220:223], v[152:159], v248\n\t" \
  "ds_read_b64_tr_b16 v[218:219], v249 offset:12544\n\t" \
  "v_fma_mix_f32 v203, v203, v226, 0 op_sel_hi:[0,1,0]\n\t" \
  "v_cvt_pk_bf16_f32 v233, v78, v79\n\t" \
  "v_smfmac_f32_16x16x64_bf16 v[60:63], v[220:223], v[160:167], v248\n\t" \
  "ds_read_u16_d16_hi v194, v194 offset:0\n\t" \
  "v_lshlrev_b32_sdwa v180, 1, v180 dst_sel:DWORD dst_unused:UNUSED_PAD src0_sel:DWORD src1_sel:WORD_1\n\t" \
  "v_cvt_pk_f16_f32 v224, v204, v205\n\t" \
  "v_smfmac_f32_16x16x64_bf16 v[84:87], v[220:223], v[168:175], v248\n\t" \
  "s_waitcnt lgkmcnt(9)\n\t" \
  "ds_read_b64_tr_b16 v[220:221], v249 offset:14336\n\t" \
  "v_cvt_pk_f16_f32 v226, v206, v207\n\t" \
  "v_pk_fma_f16 v225, v224, v224, %[h1] neg_lo:[1,0,0] neg_hi:[1,0,0]\n\t" \
  "v_smfmac_f32_16x16x64_bf16 v[16:19], v[208:211], v[144:151], v248\n\t" \
  "ds_read_u16_d16_hi v195, v195 offset:0\n\t" \
  "v_lshlrev_b32_sdwa v181, 1, v181 dst_sel:DWORD dst_unused:UNUSED_PAD src0_sel:DWORD src1_sel:WORD_1\n\t" \
  "v_pk_fma_f16 v227, v226, v226, %[h1] neg_lo:[1,0,0] neg_hi:[1,0,0]\n\t" \
  "v_smfmac_f32_16x16x64_bf16 v[40:43], v[208:211], v[152:159], v248\n\t" \
  "ds_read_b64_tr_b16 v[222:223], v249 offset:14592\n\t" \
  "buffer_store_dwordx4 v[230:233], v253, %[osrd], %[ob] offen offset:0 sc1\n\t" \
  "v_pk_mul_f16 v225, v224, v225\n\t" \
  "v_lshlrev_b32_sdwa v182, 1, v182 dst_sel:DWORD dst_unused:UNUSED_PAD src0_sel:DWORD src1_sel:WORD_1\n\t" \
  "v_smfmac_f32_16x16x64_bf16 v[64:67], v[208:211], v[160:167], v248\n\t" \
  "ds_read_b64 v[230:231], v252 offset:0\n\t" \
  "v_pk_mul_f16 v227, v226, v227\n\t" \
  "v_exp_f16 v224, v225\n\t" \
  "v_cvt_pk_bf16_f32 v234, v80, v81\n\t" \
  "v_smfmac_f32_16x16x64_bf16 v[88:91], v[208:211], v[168:175], v248\n\t" \
  "s_waitcnt lgkmcnt(9)\n\t" \
  "ds_read_b64_tr_b16 v[208:209], v249 offset:16384\n\t" \
  "ds_read_u16_d16_hi v180, v180 offset:0\n\t" \
  "v_cvt_pk_bf16_f32 v235, v82, v83\n\t" \
  "v_smfmac_f32_16x16x64_bf16 v[20:23], v[212:215], v[144:151], v248\n\t" \
  "ds_read_b64_tr_b16 v[210:211], v249 offset:16640\n\t" \
  "v_lshlrev_b32_sdwa v183, 1, v183 dst_sel:DWORD dst_unused:UNUSED_PAD src0_sel:DWORD src1_sel:WORD_1\n\t" \
  "v_exp_f16 v226, v227\n\t" \
  "v_cvt_pk_bf16_f32 v236, v84, v85\n\t" \
  "v_smfmac_f32_16x16x64_bf16 v[44:47], v[212:215], v[152:159], v248\n\t" \
  "ds_read_b64 v[232:233], v252 offset:8\n\t" \
  "v_lshlrev_b32_sdwa v188, 1, v188 dst_sel:DWORD dst_unused:UNUSED_PAD src0_sel:DWORD src1_sel:WORD_1\n\t" \
  "v_cvt_pk_bf16_f32 v237, v86, v87\n\t" \
  "v_smfmac_f32_16x16x64_bf16 v[68:71], v[212:215], v[160:167], v248\n\t" \
  "s_waitcnt lgkmcnt(4)\n\t" \
  "ds_read_u16_d16_hi v181, v181 offset:0\n\t" \
  "v_mfma_f32_16x16x16_bf16 v[72:75], v[246:247], v[230:231], 0\n\t" \
  "ds_read_u16_d16_hi v182, v182 offset:0\n\t" \
  "v_exp_f16_sdwa v224, v225 dst_sel:WORD_1 dst_unused:UNUSED_PRESERVE src0_sel:WORD_1\n\t" \
  "v_smfmac_f32_16x16x64_bf16 v[92:95], v[212:215], v[168:175], v248\n\t" \
  "ds_read_b64_tr_b16 v[212:213], v249 offset:12800\n\t" \
  "v_lshlrev_b32_sdwa v189, 1, v189 dst_sel:DWORD dst_unused:UNUSED_PAD src0_sel:DWORD src1_sel:WORD_1\n\t" \
  "v_exp_f16_sdwa v226, v227 dst_sel:WORD_1 dst_unused:UNUSED_PRESERVE src0_sel:WORD_1\n\t" \
  "v_mfma_f32_16x16x32_bf16 v[144:147], v[216:219], v[96:99], 0\n\t" \
  "s_waitcnt lgkmcnt(3)\n\t" \
  "ds_read_u16_d16_hi v183, v183 offset:0\n\t" \
  "ds_read_b64_tr_b16 v[214:215], v249 offset:13056\n\t" \
  "v_mfma_f32_16x16x16_bf16 v[76:79], v[246:247], v[232:233], 0\n\t" \
  "buffer_store_dwordx4 v[234:237], v253, %[osrd], %[ob] offen offset:64 sc1\n\t" \
  "v_mfma_f32_16x16x32_bf16 v[152:155], v[216:219], v[108:111], 0\n\t" \
  "ds_read_u16_d16_hi v188, v188 offset:0\n\t" \
  "v_lshlrev_b32_sdwa v190, 1, v190 dst_sel:DWORD dst_unused:UNUSED_PAD src0_sel:DWORD src1_sel:WORD_1\n\t" \
  "v_pk_fma_f16 v224, v224, %[hc], %[hc]\n\t" \
  "v_mfma_f32_16x16x32_bf16 v[160:163], v[216:219], v[120:123], 0\n\t" \
  "ds_read_b64 v[234:235], v252 offset:64\n\t" \
  "v_pk_fma_f16 v226, v226, %[hc], %[hc]\n\t" \
  "v_rcp_f16 v225, v224\n\t" \
  "v_cvt_pk_bf16_f32 v238, v88, v89\n\t" \
  "v_mfma_f32_16x16x32_bf16 v[168:171], v[216:219], v[132:135], 0\n\t" \
  "ds_read_b64_tr_b16 v[216:217], v249 offset:14848\n\t" \
  "v_lshlrev_b32_sdwa v191, 1, v191 dst_sel:DWORD dst_unused:UNUSED_PAD src0_sel:DWORD src1_sel:WORD_1\n\t" \
  "v_cvt_pk_bf16_f32 v239, v90, v91\n\t" \
  "v_mfma_f32_16x16x32_bf16 v[144:147], v[220:223], v[100:103], v[144:147]\n\t" \
  "ds_read_b64 v[236:237], v252 offset:72\n\t" \
  "v_rcp_f16 v227, v226\n\t" \
  "v_cvt_pk_bf16_f32 v240, v92, v93\n\t" \
  "v_mfma_f32_16x16x32_bf16 v[152:155], v[220:223], v[112:115], v[152:155]\n\t" \
  "ds_read_b64_tr_b16 v[218:219], v249 offset:15104\n\t" \
  "v_lshlrev_b32_sdwa v196, 1, v196 dst_sel:DWORD dst_unused:UNUSED_PAD src0_sel:DWORD src1_sel:WORD_1\n\t" \
  "v_rcp_f16_sdwa v224, v224 dst_sel:DWORD dst_unused:UNUSED_PAD src0_sel:WORD_1\n\t" \
  "v_cvt_pk_bf16_f32 v241, v94, v95\n\t" \
  "v_mfma_f32_16x16x32_bf16 v[160:163], v[220:223], v[124:127], v[160:163]\n\t" \
  "s_waitcnt lgkmcnt(3)\n\t" \
  "ds_read_u16_d16_hi v189, v189 offset:0\n\t" \
  "v_mfma_f32_16x16x16_bf16 v[80:83], v[246:247], v[234:235], 0\n\t" \
  "ds_read_u16_d16_hi v190, v190 offset:0\n\t" \
  "v_mfma_f32_16x16x32_bf16 v[168:171], v[220:223], v[136:139], v[168:171]\n\t" \
  "ds_read_b64_tr_b16 v[220:221], v249 offset:16896\n\t" \
  "v_lshlrev_b32_sdwa v197, 1, v197 dst_sel:DWORD dst_unused:UNUSED_PAD src0_sel:DWORD src1_sel:WORD_1\n\t" \
  "v_rcp_f16_sdwa v226, v226 dst_sel:DWORD dst_unused:UNUSED_PAD src0_sel:WORD_1\n\t" \
  "v_mfma_f32_16x16x32_bf16 v[144:147], v[208:211], v[104:107], v[144:147]\n\t" \
  "s_waitcnt lgkmcnt(4)\n\t" \
  "ds_read_u16_d16_hi v191, v191 offset:0\n\t" \
  "ds_read_b64_tr_b16 v[222:223], v249 offset:17152\n\t" \
  "v_mfma_f32_16x16x16_bf16 v[84:87], v[246:247], v[236:237], 0\n\t" \
  "buffer_store_dwordx4 v[238:241], v253, %[osrd], %[ob] offen offset:128 sc1\n\t" \
  "v_lshlrev_b32_sdwa v198, 1, v198 dst_sel:DWORD dst_unused:UNUSED_PAD src0_sel:DWORD src1_sel:WORD_1\n\t" \
  "v_mfma_f32_16x16x32_bf16 v[152:155], v[208:211], v[116:119], v[152:155]\n\t" \
  "ds_read_u16_d16_hi v196, v196 offset:0\n\t" \
  "v_fma_mix_f32 v204, v204, v225, 0 op_sel_hi:[0,1,0]\n\t" \
  "v_fma_mix_f32 v206, v206, v227, 0 op_sel_hi:[0,1,0]\n\t" \
  "v_mfma_f32_16x16x32_bf16 v[160:163], v[208:211], v[128:131], v[160:163]\n\t" \
  "ds_read_b64 v[238:239], v252 offset:128\n\t" \
  "v_lshlrev_b32_sdwa v199, 1, v199 dst_sel:DWORD dst_unused:UNUSED_PAD src0_sel:DWORD src1_sel:WORD_1\n\t" \
  "v_fma_mix_f32 v205, v205, v224, 0 op_sel_hi:[0,1,0]\n\t" \
  "v_mfma_f32_16x16x32_bf16 v[168:171], v[208:211], v[140:143], v[168:171]\n\t" \
  "s_barrier\n\t" \
  "ds_read_b64_tr_b16 v[208:209], v250 offset:12320\n\t" \
  "ds_read_u16_d16_hi v197, v197 offset:0\n\t" \
  "v_fma_mix_f32 v207, v207, v226, 0 op_sel_hi:[0,1,0]\n\t" \
  "v_mfma_f32_16x16x32_bf16 v[148:151], v[212:215], v[96:99], 0\n\t" \
  "ds_read_b64_tr_b16 v[210:211], v250 offset:13088\n\t" \
  "v_mfma_f32_16x16x32_bf16 v[156:159], v[212:215], v[108:111], 0\n\t" \
  "ds_read_b64 v[240:241], v252 offset:136\n\t" \
  "v_mfma_f32_16x16x32_bf16 v[164:167], v[212:215], v[120:123], 0\n\t" \
  "s_waitcnt lgkmcnt(4)\n\t" \
  "ds_read_u16_d16_hi v198, v198 offset:0\n\t" \
  "v_mfma_f32_16x16x16_bf16 v[88:91], v[246:247], v[238:239], 0\n\t" \
  "ds_read_u16_d16_hi v199, v199 offset:0\n\t" \
  "v_lshlrev_b32_sdwa v144, 1, v144 dst_sel:DWORD dst_unused:UNUSED_PAD src0_sel:DWORD src1_sel:WORD_1\n\t" \
  "v_cvt_pk_f16_f32 v224, v168, v169\n\t" \
  "v_mfma_f32_16x16x32_bf16 v[172:175], v[212:215], v[132:135], 0\n\t" \
  "ds_read_b64_tr_b16 v[212:213], v250 offset:12328\n\t" \
  "v_cvt_pk_f16_f32 v226, v170, v171\n\t" \
  "v_pk_fma_f16 v225, v224, v224, %[h1] neg_lo:[1,0,0] neg_hi:[1,0,0]\n\t" \
  "v_mfma_f32_16x16x32_bf16 v[148:151], v[216:219], v[100:103], v[148:151]\n\t" \
  "s_waitcnt lgkmcnt(3)\n\t" \
  "ds_read_b64_tr_b16 v[214:215], v250 offset:13096\n\t" \
  "v_mfma_f32_16x16x16_bf16 v[92:95], v[246:247], v[240:241], 0\n\t" \
  "v_lshlrev_b32_sdwa v145, 1, v145 dst_sel:DWORD dst_unused:UNUSED_PAD src0_sel:DWORD src1_sel:WORD_1\n\t" \
  "v_mfma_f32_16x16x32_bf16 v[156:159], v[216:219], v[112:115], v[156:159]\n\t" \
  "ds_read_u16_d16_hi v144, v144 offset:0\n\t" \
  "v_pk_fma_f16 v227, v226, v226, %[h1] neg_lo:[1,0,0] neg_hi:[1,0,0]\n\t" \
  "v_pk_mul_f16 v225, v224, v225\n\t" \
  "v_mfma_f32_16x16x32_bf16 v[164:167], v[216:219], v[124:127], v[164:167]\n\t" \
  "v_lshlrev_b32_sdwa v146, 1, v146 dst_sel:DWORD dst_unused:UNUSED_PAD src0_sel:DWORD src1_sel:WORD_1\n\t" \
  "v_pk_mul_f16 v227, v226, v227\n\t" \
  "v_mfma_f32_16x16x32_bf16 v[172:175], v[216:219], v[136:139], v[172:175]\n\t" \
  "ds_read_b64_tr_b16 v[216:217], v250 offset:12384\n\t" \
  "v_exp_f16 v224, v225\n\t" \
  "v_mfma_f32_16x16x32_bf16 v[148:151], v[220:223], v[104:107], v[148:151]\n\t" \
  "ds_read_u16_d16_hi v145, v145 offset:0\n\t" \
  "v_lshlrev_b32_sdwa v147, 1, v147 dst_sel:DWORD dst_unused:UNUSED_PAD src0_sel:DWORD src1_sel:WORD_1\n\t" \
  "v_mfma_f32_16x16x32_bf16 v[156:159], v[220:223], v[116:119], v[156:159]\n\t" \
  "ds_read_b64_tr_b16 v[218:219], v250 offset:13152\n\t" \
  "v_exp_f16 v226, v227\n\t" \
  "v_mfma_f32_16x16x32_bf16 v[164:167], v[220:223], v[128:131], v[164:167]\n\t" \
  "ds_read_u16_d16_hi v146, v146 offset:0\n\t" \
  "v_lshlrev_b32_sdwa v152, 1, v152 dst_sel:DWORD dst_unused:UNUSED_PAD src0_sel:DWORD src1_sel:WORD_1\n\t" \
  "v_mfma_f32_16x16x32_bf16 v[172:175], v[220:223], v[140:143], v[172:175]\n\t" \
  "ds_read_b64_tr_b16 v[220:221], v250 offset:12392\n\t" \
  "v_exp_f16_sdwa v224, v225 dst_sel:WORD_1 dst_unused:UNUSED_PRESERVE src0_sel:WORD_1\n\t" \
  "v_smfmac_f32_16x16x64_bf16 v[0:3], v[208:211], v[176:183], v248\n\t" \
  "ds_read_u16_d16_hi v147, v147 offset:0\n\t" \
  "v_lshlrev_b32_sdwa v153, 1, v153 dst_sel:DWORD dst_unused:UNUSED_PAD src0_sel:DWORD src1_sel:WORD_1\n\t" \
  "v_exp_f16_sdwa v226, v227 dst_sel:WORD_1 dst_unused:UNUSED_PRESERVE src0_sel:WORD_1\n\t" \
  "v_smfmac_f32_16x16x64_bf16 v[24:27], v[208:211], v[184:191], v248\n\t" \
  "s_waitcnt lgkmcnt(9)\n\t" \
  "ds_read_b64_tr_b16 v[222:223], v250 offset:13160\n\t" \
  "v_smfmac_f32_16x16x64_bf16 v[48:51], v[208:211], v[192:199], v248\n\t" \
  "ds_read_u16_d16_hi v152, v152 offset:0\n\t" \
  "v_lshlrev_b32_sdwa v154, 1, v154 dst_sel:DWORD dst_unused:UNUSED_PAD src0_sel:DWORD src1_sel:WORD_1\n\t" \
  "v_pk_fma_f16 v224, v224, %[hc], %[hc]\n\t" \
  "v_smfmac_f32_16x16x64_bf16 v[72:75], v[208:211], v[200:207], v248\n\t" \
  "s_waitcnt lgkmcnt(9)\n\t" \
  "ds_read_b64_tr_b16 v[208:209], v250 offset:12448\n\t" \
  "v_pk_fma_f16 v226, v226, %[hc], %[hc]\n\t" \
  "v_rcp_f16 v225, v224\n\t" \
  "v_smfmac_f32_16x16x64_bf16 v[4:7], v[212:215], v[176:183], v248\n\t" \
  "ds_read_u16_d16_hi v153, v153 offset:0\n\t" \
  "v_smfmac_f32_16x16x64_bf16 v[28:31], v[212:215], v[184:191], v248\n\t" \
  "s_add_u32 %[ob], %[ob], %[dob]\n\t" \
  "ds_read_b64_tr_b16 v[210:211], v250 offset:13216\n\t" \
  "v_lshlrev_b32_sdwa v155, 1, v155 dst_sel:DWORD dst_unused:UNUSED_PAD src0_sel:DWORD src1_sel:WORD_1\n\t" \
  "v_rcp_f16 v227, v226\n\t" \
  "v_smfmac_f32_16x16x64_bf16 v[52:55], v[212:215], v[192:199], v248\n\t" \
  "ds_read_u16_d16_hi v154, v154 offset:0\n\t" \
  "v_lshlrev_b32_sdwa v160, 1, v160 dst_sel:DWORD dst_unused:UNUSED_PAD src0_sel:DWORD src1_sel:WORD_1\n\t" \
  "v_smfmac_f32_16x16x64_bf16 v[76:79], v[212:215], v[200:207], v248\n\t" \
  "s_waitcnt lgkmcnt(9)\n\t" \
  "ds_read_b64_tr_b16 v[212:213], v250 offset:12456\n\t" \
  "v_rcp_f16_sdwa v224, v224 dst_sel:DWORD dst_unused:UNUSED_PAD src0_sel:WORD_1\n\t" \
  "v_smfmac_f32_16x16x64_bf16 v[8:11], v[216:219], v[176:183], v248\n\t" \
  "s_add_u32 %[rs], %[rs], 1\n\t" \
  "ds_read_u16_d16_hi v155, v155 offset:0\n\t" \
  "v_lshlrev_b32_sdwa v161, 1, v161 dst_sel:DWORD dst_unused:UNUSED_PAD src0_sel:DWORD src1_sel:WORD_1\n\t" \
  "v_smfmac_f32_16x16x64_bf16 v[32:35], v[216:219], v[184:191], v248\n\t" \
  "s_cmp_eq_u32 %[rs], 6\n\t" \
  "ds_read_b64_tr_b16 v[214:215], v250 offset:13224\n\t" \
  "v_rcp_f16_sdwa v226, v226 dst_sel:DWORD dst_unused:UNUSED_PAD src0_sel:WORD_1\n\t" \
  "v_smfmac_f32_16x16x64_bf16 v[56:59], v[216:219], v[192:199], v248\n\t" \
  "ds_read_u16_d16_hi v160, v160 offset:0\n\t" \
  "v_lshlrev_b32_sdwa v162, 1, v162 dst_sel:DWORD dst_unused:UNUSED_PAD src0_sel:DWORD src1_sel:WORD_1\n\t" \
  "v_smfmac_f32_16x16x64_bf16 v[80:83], v[216:219], v[200:207], v248\n\t" \
  "s_waitcnt lgkmcnt(9)\n\t" \
  "ds_read_b64_tr_b16 v[216:217], v249 offset:18432\n\t" \
  "v_fma_mix_f32 v168, v168, v225, 0 op_sel_hi:[0,1,0]\n\t" \
  "v_fma_mix_f32 v170, v170, v227, 0 op_sel_hi:[0,1,0]\n\t" \
  "v_smfmac_f32_16x16x64_bf16 v[12:15], v[220:223], v[176:183], v248\n\t" \
  "s_cselect_b32 %[rs], 0, %[rs]\n\t" \
  "ds_read_u16_d16_hi v161, v161 offset:0\n\t" \
  "v_lshlrev_b32_sdwa v163, 1, v163 dst_sel:DWORD dst_unused:UNUSED_PAD src0_sel:DWORD src1_sel:WORD_1\n\t" \
  "v_fma_mix_f32 v169, v169, v224, 0 op_sel_hi:[0,1,0]\n\t" \
  "v_smfmac_f32_16x16x64_bf16 v[36:39], v[220:223], v[184:191], v248\n\t" \
  "s_cselect_b32 %[t0], -75920, %[ps]\n\t" \
  "ds_read_b64_tr_b16 v[218:219], v249 offset:18688\n\t" \
  "v_fma_mix_f32 v171, v171, v226, 0 op_sel_hi:[0,1,0]\n\t" \
  "v_smfmac_f32_16x16x64_bf16 v[60:63], v[220:223], v[192:199], v248\n\t" \
  "ds_read_u16_d16_hi v162, v162 offset:0\n\t" \
  "v_lshlrev_b32_sdwa v148, 1, v148 dst_sel:DWORD dst_unused:UNUSED_PAD src0_sel:DWORD src1_sel:WORD_1\n\t" \
  "v_cvt_pk_f16_f32 v224, v172, v173\n\t" \
  "v_add_u32 v252, %[t0], v252\n\t" \
  "v_smfmac_f32_16x16x64_bf16 v[84:87], v[220:223], v[200:207], v248\n\t" \
  "s_waitcnt lgkmcnt(9)\n\t" \
  "ds_read_b64_tr_b16 v[220:221], v249 offset:20480\n\t" \
  "v_cvt_pk_f16_f32 v226, v174, v175\n\t" \
  "v_pk_fma_f16 v225, v224, v224, %[h1] neg_lo:[1,0,0] neg_hi:[1,0,0]\n\t" \
  "v_smfmac_f32_16x16x64_bf16 v[16:19], v[208:211], v[176:183], v248\n\t" \
  "ds_read_u16_d16_hi v163, v163 offset:0\n\t" \
  "v_lshlrev_b32_sdwa v149, 1, v149 dst_sel:DWORD dst_unused:UNUSED_PAD src0_sel:DWORD src1_sel:WORD_1\n\t" \
  "v_pk_fma_f16 v227, v226, v226, %[h1] neg_lo:[1,0,0] neg_hi:[1,0,0]\n\t" \
  "v_smfmac_f32_16x16x64_bf16 v[40:43], v[208:211], v[184:191], v248\n\t" \
  "ds_read_b64_tr_b16 v[222:223], v249 offset:20736\n\t" \
  "v_pk_mul_f16 v225, v224, v225\n\t" \
  "v_lshlrev_b32_sdwa v150, 1, v150 dst_sel:DWORD dst_unused:UNUSED_PAD src0_sel:DWORD src1_sel:WORD_1\n\t" \
  "v_smfmac_f32_16x16x64_bf16 v[64:67], v[208:211], v[192:199], v248\n\t" \
  "ds_read_u16_d16_hi v148, v148 offset:0\n\t" \
  "v_pk_mul_f16 v227, v226, v227\n\t" \
  "v_exp_f16 v224, v225\n\t" \
  "v_smfmac_f32_16x16x64_bf16 v[88:91], v[208:211], v[200:207], v248\n\t" \
  "s_waitcnt lgkmcnt(9)\n\t" \
  "ds_read_b64_tr_b16 v[208:209], v249 offset:22528\n\t" \
  "v_smfmac_f32_16x16x64_bf16 v[20:23], v[212:215], v[176:183], v248\n\t" \
  "ds_read_u16_d16_hi v149, v149 offset:0\n\t" \
  "v_lshlrev_b32_sdwa v151, 1, v151 dst_sel:DWORD dst_unused:UNUSED_PAD src0_sel:DWORD src1_sel:WORD_1\n\t" \
  "v_exp_f16 v226, v227\n\t" \
  "v_smfmac_f32_16x16x64_bf16 v[44:47], v[212:215], v[184:191], v248\n\t" \
  "ds_read_b64_tr_b16 v[210:211], v249 offset:22784\n\t" \
  "v_lshlrev_b32_sdwa v156, 1, v156 dst_sel:DWORD dst_unused:UNUSED_PAD src0_sel:DWORD src1_sel:WORD_1\n\t" \
  "v_smfmac_f32_16x16x64_bf16 v[68:71], v[212:215], v[192:199], v248\n\t" \
  "ds_read_u16_d16_hi v150, v150 offset:0\n\t" \
  "v_exp_f16_sdwa v224, v225 dst_sel:WORD_1 dst_unused:UNUSED_PRESERVE src0_sel:WORD_1\n\t" \
  "v_smfmac_f32_16x16x64_bf16 v[92:95], v[212:215], v[200:207], v248\n\t" \
  "s_waitcnt lgkmcnt(9)\n\t" \
  "ds_read_b64_tr_b16 v[212:213], v249 offset:18944\n\t" \
  "v_lshlrev_b32_sdwa v157, 1, v157 dst_sel:DWORD dst_unused:UNUSED_PAD src0_sel:DWORD src1_sel:WORD_1\n\t" \
  "v_exp_f16_sdwa v226, v227 dst_sel:WORD_1 dst_unused:UNUSED_PRESERVE src0_sel:WORD_1\n\t" \
  "v_mfma_f32_16x16x32_bf16 v[176:179], v[216:219], v[96:99], 0\n\t" \
  "ds_read_u16_d16_hi v151, v151 offset:0\n\t" \
  "v_mfma_f32_16x16x32_bf16 v[184:187], v[216:219], v[108:111], 0\n\t" \
  "ds_read_b64_tr_b16 v[214:215], v249 offset:19200\n\t" \
  "v_lshlrev_b32_sdwa v158, 1, v158 dst_sel:DWORD dst_unused:UNUSED_PAD src0_sel:DWORD src1_sel:WORD_1\n\t" \
  "v_pk_fma_f16 v224, v224, %[hc], %[hc]\n\t" \
  "v_mfma_f32_16x16x32_bf16 v[192:195], v[216:219], v[120:123], 0\n\t" \
  "ds_read_u16_d16_hi v156, v156 offset:0\n\t" \
  "v_pk_fma_f16 v226, v226, %[hc], %[hc]\n\t" \
  "v_rcp_f16 v225, v224\n\t" \
  "v_mfma_f32_16x16x32_bf16 v[200:203], v[216:219], v[132:135], 0\n\t" \
  "s_waitcnt lgkmcnt(9)\n\t" \
  "ds_read_b64_tr_b16 v[216:217], v249 offset:20992\n\t" \
  "v_lshlrev_b32_sdwa v159, 1, v159 dst_sel:DWORD dst_unused:UNUSED_PAD src0_sel:DWORD src1_sel:WORD_1\n\t" \
  "v_mfma_f32_16x16x32_bf16 v[176:179], v[220:223], v[100:103], v[176:179]\n\t" \
  "ds_read_u16_d16_hi v157, v157 offset:0\n\t" \
  "v_rcp_f16 v227, v226\n\t" \
  "v_mfma_f32_16x16x32_bf16 v[184:187], v[220:223], v[112:115], v[184:187]\n\t" \
  "ds_read_b64_tr_b16 v[218:219], v249 offset:21248\n\t" \
  "v_lshlrev_b32_sdwa v164, 1, v164 dst_sel:DWORD dst_unused:UNUSED_PAD src0_sel:DWORD src1_sel:WORD_1\n\t" \
  "v_rcp_f16_sdwa v224, v224 dst_sel:DWORD dst_unused:UNUSED_PAD src0_sel:WORD_1\n\t" \
  "v_mfma_f32_16x16x32_bf16 v[192:195], v[220:223], v[124:127], v[192:195]\n\t" \
  "ds_read_u16_d16_hi v158, v158 offset:0\n\t" \
  "v_mfma_f32_16x16x32_bf16 v[200:203], v[220:223], v[136:139], v[200:203]\n\t" \
  "s_waitcnt lgkmcnt(9)\n\t" \
  "ds_read_b64_tr_b16 v[220:221], v249 offset:23040\n\t" \
  "v_lshlrev_b32_sdwa v165, 1, v165 dst_sel:DWORD dst_unused:UNUSED_PAD src0_sel:DWORD src1_sel:WORD_1\n\t" \
  "v_rcp_f16_sdwa v226, v226 dst_sel:DWORD dst_unused:UNUSED_PAD src0_sel:WORD_1\n\t" \
  "v_mfma_f32_16x16x32_bf16 v[176:179], v[208:211], v[104:107], v[176:179]\n\t" \
  "ds_read_u16_d16_hi v159, v159 offset:0\n\t" \
  "v_lshlrev_b32_sdwa v166, 1, v166 dst_sel:DWORD dst_unused:UNUSED_PAD src0_sel:DWORD src1_sel:WORD_1\n\t" \
  "v_mfma_f32_16x16x32_bf16 v[184:187], v[208:211], v[116:119], v[184:187]\n\t" \
  "ds_read_b64_tr_b16 v[222:223], v249 offset:23296\n\t" \
  "v_fma_mix_f32 v172, v172, v225, 0 op_sel_hi:[0,1,0]\n\t" \
  "v_fma_mix_f32 v174, v174, v227, 0 op_sel_hi:[0,1,0]\n\t" \
  "v_mfma_f32_16x16x32_bf16 v[192:195], v[208:211], v[128:131], v[192:195]\n\t" \
  "ds_read_u16_d16_hi v164, v164 offset:0\n\t" \
  "v_lshlrev_b32_sdwa v167, 1, v167 dst_sel:DWORD dst_unused:UNUSED_PAD src0_sel:DWORD src1_sel:WORD_1\n\t" \
  "v_fma_mix_f32 v173, v173, v224, 0 op_sel_hi:[0,1,0]\n\t" \
  "v_mfma_f32_16x16x32_bf16 v[200:203], v[208:211], v[140:143], v[200:203]\n\t" \
  "s_barrier\n\t" \
  "s_sub_u32 %[lc], %[lc], 1\n\t" \
  "s_cmp_lg_u32 %[lc], 0\n\t" \
  "s_cbranch_scc1 .Lcnx_m\n\t" \
  "ds_read_b64_tr_b16 v[208:209], v250 offset:0\n\t" \
  "v_fma_mix_f32 v175, v175, v226, 0 op_sel_hi:[0,1,0]\n\t" \
  "s_waitcnt lgkmcnt(10)\n\t" \
  "v_mfma_f32_16x16x32_bf16 v[180:183], v[212:215], v[96:99], 0\n\t" \
  "ds_read_u16_d16_hi v165, v165 offset:0\n\t" \
  "v_mfma_f32_16x16x32_bf16 v[188:191], v[212:215], v[108:111], 0\n\t" \
  "ds_read_b64_tr_b16 v[210:211], v250 offset:768\n\t" \
  "v_mfma_f32_16x16x32_bf16 v[196:199], v[212:215], v[120:123], 0\n\t" \
  "ds_read_u16_d16_hi v166, v166 offset:0\n\t" \
  "v_lshlrev_b32_sdwa v176, 1, v176 dst_sel:DWORD dst_unused:UNUSED_PAD src0_sel:DWORD src1_sel:WORD_1\n\t" \
  "v_cvt_pk_f16_f32 v224, v200, v201\n\t" \
  "v_mfma_f32_16x16x32_bf16 v[204:207], v[212:215], v[132:135], 0\n\t" \
  "s_waitcnt lgkmcnt(9)\n\t" \
  "ds_read_b64_tr_b16 v[212:213], v250 offset:8\n\t" \
  "v_cvt_pk_f16_f32 v226, v202, v203\n\t" \
  "v_pk_fma_f16 v225, v224, v224, %[h1] neg_lo:[1,0,0] neg_hi:[1,0,0]\n\t" \
  "v_mfma_f32_16x16x32_bf16 v[180:183], v[216:219], v[100:103], v[180:183]\n\t" \
  "ds_read_u16_d16_hi v167, v167 offset:0\n\t" \
  "v_lshlrev_b32_sdwa v177, 1, v177 dst_sel:DWORD dst_unused:UNUSED_PAD src0_sel:DWORD src1_sel:WORD_1\n\t" \
  "v_mfma_f32_16x16x32_bf16 v[188:191], v[216:219], v[112:115], v[188:191]\n\t" \
  "ds_read_b64_tr_b16 v[214:215], v250 offset:776\n\t" \
  "v_pk_fma_f16 v227, v226, v226, %[h1] neg_lo:[1,0,0] neg_hi:[1,0,0]\n\t" \
  "v_pk_mul_f16 v225, v224, v225\n\t" \
  "v_mfma_f32_16x16x32_bf16 v[196:199], v[216:219], v[124:127], v[196:199]\n\t" \
  "ds_read_u16_d16_hi v176, v176 offset:0\n\t" \
  "v_lshlrev_b32_sdwa v178, 1, v178 dst_sel:DWORD dst_unused:UNUSED_PAD src0_sel:DWORD src1_sel:WORD_1\n\t" \
  "v_pk_mul_f16 v227, v226, v227\n\t" \
  "v_mfma_f32_16x16x32_bf16 v[204:207], v[216:219], v[136:139], v[204:207]\n\t" \
  "s_waitcnt lgkmcnt(9)\n\t" \
  "ds_read_b64_tr_b16 v[216:217], v250 offset:64\n\t" \
  "v_exp_f16 v224, v225\n\t" \
  "v_mfma_f32_16x16x32_bf16 v[180:183], v[220:223], v[104:107], v[180:183]\n\t" \
  "ds_read_u16_d16_hi v177, v177 offset:0\n\t" \
  "v_lshlrev_b32_sdwa v179, 1, v179 dst_sel:DWORD dst_unused:UNUSED_PAD src0_sel:DWORD src1_sel:WORD_1\n\t" \
  "v_mfma_f32_16x16x32_bf16 v[188:191], v[220:223], v[116:119], v[188:191]\n\t" \
  "ds_read_b64_tr_b16 v[218:219], v250 offset:832\n\t" \
  "v_exp_f16 v226, v227\n\t" \
  "v_mfma_f32_16x16x32_bf16 v[196:199], v[220:223], v[128:131], v[196:199]\n\t" \
  "ds_read_u16_d16_hi v178, v178 offset:0\n\t" \
  "v_lshlrev_b32_sdwa v184, 1, v184 dst_sel:DWORD dst_unused:UNUSED_PAD src0_sel:DWORD src1_sel:WORD_1\n\t" \
  "v_mfma_f32_16x16x32_bf16 v[204:207], v[220:223], v[140:143], v[204:207]\n\t" \
  "s_waitcnt lgkmcnt(9)\n\t" \
  "ds_read_b64_tr_b16 v[220:221], v250 offset:72\n\t" \
  "v_exp_f16_sdwa v224, v225 dst_sel:WORD_1 dst_unused:UNUSED_PRESERVE src0_sel:WORD_1\n\t" \
  "v_smfmac_f32_16x16x64_bf16 v[0:3], v[208:211], v[144:151], v248\n\t" \
  "ds_read_u16_d16_hi v179, v179 offset:0\n\t" \
  "v_lshlrev_b32_sdwa v185, 1, v185 dst_sel:DWORD dst_unused:UNUSED_PAD src0_sel:DWORD src1_sel:WORD_1\n\t" \
  "v_exp_f16_sdwa v226, v227 dst_sel:WORD_1 dst_unused:UNUSED_PRESERVE src0_sel:WORD_1\n\t" \
  "v_smfmac_f32_16x16x64_bf16 v[24:27], v[208:211], v[152:159], v248\n\t" \
  "s_waitcnt lgkmcnt(8)\n\t" \
  "ds_read_b64_tr_b16 v[222:223], v250 offset:840\n\t" \
  "v_smfmac_f32_16x16x64_bf16 v[48:51], v[208:211], v[160:167], v248\n\t" \
  "ds_read_u16_d16_hi v184, v184 offset:0\n\t" \
  "v_lshlrev_b32_sdwa v186, 1, v186 dst_sel:DWORD dst_unused:UNUSED_PAD src0_sel:DWORD src1_sel:WORD_1\n\t" \
  "v_pk_fma_f16 v224, v224, %[hc], %[hc]\n\t" \
  "v_smfmac_f32_16x16x64_bf16 v[72:75], v[208:211], v[168:175], v248\n\t" \
  "s_waitcnt lgkmcnt(9)\n\t" \
  "ds_read_b64_tr_b16 v[208:209], v250 offset:128\n\t" \
  "v_pk_fma_f16 v226, v226, %[hc], %[hc]\n\t" \
  "v_rcp_f16 v225, v224\n\t" \
  "v_smfmac_f32_16x16x64_bf16 v[4:7], v[212:215], v[144:151], v248\n\t" \
  "ds_read_u16_d16_hi v185, v185 offset:0\n\t" \
  "v_smfmac_f32_16x16x64_bf16 v[28:31], v[212:215], v[152:159], v248\n\t" \
  "ds_read_b64_tr_b16 v[210:211], v250 offset:896\n\t" \
  "v_lshlrev_b32_sdwa v187, 1, v187 dst_sel:DWORD dst_unused:UNUSED_PAD src0_sel:DWORD src1_sel:WORD_1\n\t" \
  "v_rcp_f16 v227, v226\n\t" \
  "v_smfmac_f32_16x16x64_bf16 v[52:55], v[212:215], v[160:167], v248\n\t" \
  "ds_read_u16_d16_hi v186, v186 offset:0\n\t" \
  "v_lshlrev_b32_sdwa v192, 1, v192 dst_sel:DWORD dst_unused:UNUSED_PAD src0_sel:DWORD src1_sel:WORD_1\n\t" \
  "v_smfmac_f32_16x16x64_bf16 v[76:79], v[212:215], v[168:175], v248\n\t" \
  "s_waitcnt lgkmcnt(9)\n\t" \
  "ds_read_b64_tr_b16 v[212:213], v250 offset:136\n\t" \
  "v_rcp_f16_sdwa v224, v224 dst_sel:DWORD dst_unused:UNUSED_PAD src0_sel:WORD_1\n\t" \
  "v_smfmac_f32_16x16x64_bf16 v[8:11], v[216:219], v[144:151], v248\n\t" \
  "ds_read_u16_d16_hi v187, v187 offset:0\n\t" \
  "v_lshlrev_b32_sdwa v193, 1, v193 dst_sel:DWORD dst_unused:UNUSED_PAD src0_sel:DWORD src1_sel:WORD_1\n\t" \
  "v_smfmac_f32_16x16x64_bf16 v[32:35], v[216:219], v[152:159], v248\n\t" \
  "ds_read_b64_tr_b16 v[214:215], v250 offset:904\n\t" \
  "v_rcp_f16_sdwa v226, v226 dst_sel:DWORD dst_unused:UNUSED_PAD src0_sel:WORD_1\n\t" \
  "v_smfmac_f32_16x16x64_bf16 v[56:59], v[216:219], v[160:167], v248\n\t" \
  "ds_read_u16_d16_hi v192, v192 offset:0\n\t" \
  "v_lshlrev_b32_sdwa v194, 1, v194 dst_sel:DWORD dst_unused:UNUSED_PAD src0_sel:DWORD src1_sel:WORD_1\n\t" \
  "v_smfmac_f32_16x16x64_bf16 v[80:83], v[216:219], v[168:175], v248\n\t" \
  "s_waitcnt lgkmcnt(9)\n\t" \
  "ds_read_b64_tr_b16 v[216:217], v249 offset:0\n\t" \
  "v_fma_mix_f32 v200, v200, v225, 0 op_sel_hi:[0,1,0]\n\t" \
  "v_fma_mix_f32 v202, v202, v227, 0 op_sel_hi:[0,1,0]\n\t" \
  "v_smfmac_f32_16x16x64_bf16 v[12:15], v[220:223], v[144:151], v248\n\t" \
  "ds_read_u16_d16_hi v193, v193 offset:0\n\t" \
  "v_lshlrev_b32_sdwa v195, 1, v195 dst_sel:DWORD dst_unused:UNUSED_PAD src0_sel:DWORD src1_sel:WORD_1\n\t" \
  "v_fma_mix_f32 v201, v201, v224, 0 op_sel_hi:[0,1,0]\n\t" \
  "v_smfmac_f32_16x16x64_bf16 v[36:39], v[220:223], v[152:159], v248\n\t" \
  "ds_read_b64_tr_b16 v[218:219], v249 offset:256\n\t" \
  "v_fma_mix_f32 v203, v203, v226, 0 op_sel_hi:[0,1,0]\n\t" \
  "v_smfmac_f32_16x16x64_bf16 v[60:63], v[220:223], v[160:167], v248\n\t" \
  "ds_read_u16_d16_hi v194, v194 offset:0\n\t" \
  "v_lshlrev_b32_sdwa v180, 1, v180 dst_sel:DWORD dst_unused:UNUSED_PAD src0_sel:DWORD src1_sel:WORD_1\n\t" \
  "v_cvt_pk_f16_f32 v224, v204, v205\n\t" \
  "v_smfmac_f32_16x16x64_bf16 v[84:87], v[220:223], v[168:175], v248\n\t" \
  "s_waitcnt lgkmcnt(9)\n\t" \
  "ds_read_b64_tr_b16 v[220:221], v249 offset:2048\n\t" \
  "v_cvt_pk_f16_f32 v226, v206, v207\n\t" \
  "v_pk_fma_f16 v225, v224, v224, %[h1] neg_lo:[1,0,0] neg_hi:[1,0,0]\n\t" \
  "v_smfmac_f32_16x16x64_bf16 v[16:19], v[208:211], v[144:151], v248\n\t" \
  "ds_read_u16_d16_hi v195, v195 offset:0\n\t" \
  "v_lshlrev_b32_sdwa v181, 1, v181 dst_sel:DWORD dst_unused:UNUSED_PAD src0_sel:DWORD src1_sel:WORD_1\n\t" \
  "v_pk_fma_f16 v227, v226, v226, %[h1] neg_lo:[1,0,0] neg_hi:[1,0,0]\n\t" \
  "v_smfmac_f32_16x16x64_bf16 v[40:43], v[208:211], v[152:159], v248\n\t" \
  "ds_read_b64_tr_b16 v[222:223], v249 offset:2304\n\t" \
  "v_pk_mul_f16 v225, v224, v225\n\t" \
  "v_lshlrev_b32_sdwa v182, 1, v182 dst_sel:DWORD dst_unused:UNUSED_PAD src0_sel:DWORD src1_sel:WORD_1\n\t" \
  "v_smfmac_f32_16x16x64_bf16 v[64:67], v[208:211], v[160:167], v248\n\t" \
  "ds_read_u16_d16_hi v180, v180 offset:0\n\t" \
  "v_pk_mul_f16 v227, v226, v227\n\t" \
  "v_exp_f16 v224, v225\n\t" \
  "v_smfmac_f32_16x16x64_bf16 v[88:91], v[208:211], v[168:175], v248\n\t" \
  "s_waitcnt lgkmcnt(9)\n\t" \
  "ds_read_b64_tr_b16 v[208:209], v249 offset:4096\n\t" \
  "v_smfmac_f32_16x16x64_bf16 v[20:23], v[212:215], v[144:151], v248\n\t" \
  "ds_read_u16_d16_hi v181, v181 offset:0\n\t" \
  "v_lshlrev_b32_sdwa v183, 1, v183 dst_sel:DWORD dst_unused:UNUSED_PAD src0_sel:DWORD src1_sel:WORD_1\n\t" \
  "v_exp_f16 v226, v227\n\t" \
  "v_smfmac_f32_16x16x64_bf16 v[44:47], v[212:215], v[152:159], v248\n\t" \
  "ds_read_b64_tr_b16 v[210:211], v249 offset:4352\n\t" \
  "v_lshlrev_b32_sdwa v188, 1, v188 dst_sel:DWORD dst_unused:UNUSED_PAD src0_sel:DWORD src1_sel:WORD_1\n\t" \
  "v_smfmac_f32_16x16x64_bf16 v[68:71], v[212:215], v[160:167], v248\n\t" \
  "ds_read_u16_d16_hi v182, v182 offset:0\n\t" \
  "v_exp_f16_sdwa v224, v225 dst_sel:WORD_1 dst_unused:UNUSED_PRESERVE src0_sel:WORD_1\n\t" \
  "v_smfmac_f32_16x16x64_bf16 v[92:95], v[212:215], v[168:175], v248\n\t" \
  "s_waitcnt lgkmcnt(9)\n\t" \
  "ds_read_b64_tr_b16 v[212:213], v249 offset:512\n\t" \
  "v_lshlrev_b32_sdwa v189, 1, v189 dst_sel:DWORD dst_unused:UNUSED_PAD src0_sel:DWORD src1_sel:WORD_1\n\t" \
  "v_exp_f16_sdwa v226, v227 dst_sel:WORD_1 dst_unused:UNUSED_PRESERVE src0_sel:WORD_1\n\t" \
  "v_mfma_f32_16x16x32_bf16 v[152:155], v[216:219], v[108:111], 0\n\t" \
  "ds_read_u16_d16_hi v183, v183 offset:0\n\t" \
  "v_lshlrev_b32_sdwa v190, 1, v190 dst_sel:DWORD dst_unused:UNUSED_PAD src0_sel:DWORD src1_sel:WORD_1\n\t" \
  "v_pk_fma_f16 v224, v224, %[hc], %[hc]\n\t" \
  "v_mfma_f32_16x16x32_bf16 v[160:163], v[216:219], v[120:123], 0\n\t" \
  "ds_read_b64_tr_b16 v[214:215], v249 offset:768\n\t" \
  "v_pk_fma_f16 v226, v226, %[hc], %[hc]\n\t" \
  "v_rcp_f16 v225, v224\n\t" \
  "v_mfma_f32_16x16x32_bf16 v[168:171], v[216:219], v[132:135], 0\n\t" \
  "s_waitcnt lgkmcnt(8)\n\t" \
  "ds_read_u16_d16_hi v188, v188 offset:0\n\t" \
  "v_lshlrev_b32_sdwa v191, 1, v191 dst_sel:DWORD dst_unused:UNUSED_PAD src0_sel:DWORD src1_sel:WORD_1\n\t" \
  "v_rcp_f16 v227, v226\n\t" \
  "v_mfma_f32_16x16x32_bf16 v[152:155], v[220:223], v[112:115], v[152:155]\n\t" \
  "ds_read_b64_tr_b16 v[216:217], v249 offset:2560\n\t" \
  "v_lshlrev_b32_sdwa v196, 1, v196 dst_sel:DWORD dst_unused:UNUSED_PAD src0_sel:DWORD src1_sel:WORD_1\n\t" \
  "v_rcp_f16_sdwa v224, v224 dst_sel:DWORD dst_unused:UNUSED_PAD src0_sel:WORD_1\n\t" \
  "v_mfma_f32_16x16x32_bf16 v[160:163], v[220:223], v[124:127], v[160:163]\n\t" \
  "ds_read_u16_d16_hi v189, v189 offset:0\n\t" \
  "v_mfma_f32_16x16x32_bf16 v[168:171], v[220:223], v[136:139], v[168:171]\n\t" \
  "s_waitcnt lgkmcnt(7)\n\t" \
  "ds_read_b64_tr_b16 v[218:219], v249 offset:2816\n\t" \
  "ds_read_u16_d16_hi v190, v190 offset:0\n\t" \
  "v_lshlrev_b32_sdwa v197, 1, v197 dst_sel:DWORD dst_unused:UNUSED_PAD src0_sel:DWORD src1_sel:WORD_1\n\t" \
  "v_rcp_f16_sdwa v226, v226 dst_sel:DWORD dst_unused:UNUSED_PAD src0_sel:WORD_1\n\t" \
  "v_lshlrev_b32_sdwa v198, 1, v198 dst_sel:DWORD dst_unused:UNUSED_PAD src0_sel:DWORD src1_sel:WORD_1\n\t" \
  "v_mfma_f32_16x16x32_bf16 v[152:155], v[208:211], v[116:119], v[152:155]\n\t" \
  "ds_read_b64_tr_b16 v[220:221], v249 offset:4608\n\t" \
  "ds_read_u16_d16_hi v191, v191 offset:0\n\t" \
  "v_fma_mix_f32 v204, v204, v225, 0 op_sel_hi:[0,1,0]\n\t" \
  "v_fma_mix_f32 v206, v206, v227, 0 op_sel_hi:[0,1,0]\n\t" \
  "v_mfma_f32_16x16x32_bf16 v[160:163], v[208:211], v[128:131], v[160:163]\n\t" \
  "ds_read_b64_tr_b16 v[222:223], v249 offset:4864\n\t" \
  "v_lshlrev_b32_sdwa v199, 1, v199 dst_sel:DWORD dst_unused:UNUSED_PAD src0_sel:DWORD src1_sel:WORD_1\n\t" \
  "v_fma_mix_f32 v205, v205, v224, 0 op_sel_hi:[0,1,0]\n\t" \
  "v_mfma_f32_16x16x32_bf16 v[168:171], v[208:211], v[140:143], v[168:171]\n\t" \
  "s_barrier\n\t" \
  "ds_read_u16_d16_hi v196, v196 offset:0\n\t" \
  "ds_read_b64_tr_b16 v[208:209], v250 offset:6160\n\t" \
  "v_fma_mix_f32 v207, v207, v226, 0 op_sel_hi:[0,1,0]\n\t" \
  "s_waitcnt lgkmcnt(10)\n\t" \
  "v_mfma_f32_16x16x32_bf16 v[156:159], v[212:215], v[108:111], 0\n\t" \
  "ds_read_u16_d16_hi v197, v197 offset:0\n\t" \
  "v_mfma_f32_16x16x32_bf16 v[164:167], v[212:215], v[120:123], 0\n\t" \
  "ds_read_b64_tr_b16 v[210:211], v250 offset:6928\n\t" \
  "v_lshlrev_b32_sdwa v152, 1, v152 dst_sel:DWORD dst_unused:UNUSED_PAD src0_sel:DWORD src1_sel:WORD_1\n\t" \
  "v_cvt_pk_f16_f32 v224, v168, v169\n\t" \
  "v_mfma_f32_16x16x32_bf16 v[172:175], v[212:215], v[132:135], 0\n\t" \
  "s_waitcnt lgkmcnt(8)\n\t" \
  "ds_read_u16_d16_hi v198, v198 offset:0\n\t" \
  "v_cvt_pk_f16_f32 v226, v170, v171\n\t" \
  "v_pk_fma_f16 v225, v224, v224, %[h1] neg_lo:[1,0,0] neg_hi:[1,0,0]\n\t" \
  "v_pk_fma_f16 v227, v226, v226, %[h1] neg_lo:[1,0,0] neg_hi:[1,0,0]\n\t" \
  "v_mfma_f32_16x16x32_bf16 v[156:159], v[216:219], v[112:115], v[156:159]\n\t" \
  "ds_read_b64_tr_b16 v[212:213], v250 offset:6168\n\t" \
  "v_lshlrev_b32_sdwa v153, 1, v153 dst_sel:DWORD dst_unused:UNUSED_PAD src0_sel:DWORD src1_sel:WORD_1\n\t" \
  "v_mfma_f32_16x16x32_bf16 v[164:167], v[216:219], v[124:127], v[164:167]\n\t" \
  "ds_read_u16_d16_hi v199, v199 offset:0\n\t" \
  "v_pk_mul_f16 v225, v224, v225\n\t" \
  "v_pk_mul_f16 v227, v226, v227\n\t" \
  "v_mfma_f32_16x16x32_bf16 v[172:175], v[216:219], v[136:139], v[172:175]\n\t" \
  "s_waitcnt lgkmcnt(7)\n\t" \
  "ds_read_b64_tr_b16 v[214:215], v250 offset:6936\n\t" \
  "v_exp_f16 v224, v225\n\t" \
  "v_lshlrev_b32_sdwa v154, 1, v154 dst_sel:DWORD dst_unused:UNUSED_PAD src0_sel:DWORD src1_sel:WORD_1\n\t" \
  "v_mfma_f32_16x16x32_bf16 v[156:159], v[220:223], v[116:119], v[156:159]\n\t" \
  "ds_read_u16_d16_hi v152, v152 offset:0\n\t" \
  "v_exp_f16 v226, v227\n\t" \
  "v_mfma_f32_16x16x32_bf16 v[164:167], v[220:223], v[128:131], v[164:167]\n\t" \
  "ds_read_b64_tr_b16 v[216:217], v250 offset:6224\n\t" \
  "v_exp_f16_sdwa v224, v225 dst_sel:WORD_1 dst_unused:UNUSED_PRESERVE src0_sel:WORD_1\n\t" \
  "v_mfma_f32_16x16x32_bf16 v[172:175], v[220:223], v[140:143], v[172:175]\n\t" \
  "s_waitcnt lgkmcnt(6)\n\t" \
  "ds_read_b64_tr_b16 v[218:219], v250 offset:6992\n\t" \
  "v_smfmac_f32_16x16x64_bf16 v[0:3], v[208:211], v[176:183], v248\n\t" \
  "ds_read_u16_d16_hi v153, v153 offset:0\n\t" \
  "v_lshlrev_b32_sdwa v155, 1, v155 dst_sel:DWORD dst_unused:UNUSED_PAD src0_sel:DWORD src1_sel:WORD_1\n\t" \
  "v_exp_f16_sdwa v226, v227 dst_sel:WORD_1 dst_unused:UNUSED_PRESERVE src0_sel:WORD_1\n\t" \
  "v_smfmac_f32_16x16x64_bf16 v[24:27], v[208:211], v[184:191], v248\n\t" \
  "s_waitcnt lgkmcnt(5)\n\t" \
  "ds_read_b64_tr_b16 v[220:221], v250 offset:6232\n\t" \
  "v_smfmac_f32_16x16x64_bf16 v[48:51], v[208:211], v[192:199], v248\n\t" \
  "ds_read_u16_d16_hi v154, v154 offset:0\n\t" \
  "v_lshlrev_b32_sdwa v160, 1, v160 dst_sel:DWORD dst_unused:UNUSED_PAD src0_sel:DWORD src1_sel:WORD_1\n\t" \
  "v_pk_fma_f16 v224, v224, %[hc], %[hc]\n\t" \
  "v_smfmac_f32_16x16x64_bf16 v[72:75], v[208:211], v[200:207], v248\n\t" \
  "s_waitcnt lgkmcnt(6)\n\t" \
  "ds_read_b64_tr_b16 v[222:223], v250 offset:7000\n\t" \
  "v_pk_fma_f16 v226, v226, %[hc], %[hc]\n\t" \
  "v_smfmac_f32_16x16x64_bf16 v[4:7], v[212:215], v[176:183], v248\n\t" \
  "ds_read_u16_d16_hi v155, v155 offset:0\n\t" \
  "v_rcp_f16 v225, v224\n\t" \
  "v_smfmac_f32_16x16x64_bf16 v[28:31], v[212:215], v[184:191], v248\n\t" \
  "ds_read_b64_tr_b16 v[208:209], v250 offset:6288\n\t" \
  "v_lshlrev_b32_sdwa v161, 1, v161 dst_sel:DWORD dst_unused:UNUSED_PAD src0_sel:DWORD src1_sel:WORD_1\n\t" \
  "v_smfmac_f32_16x16x64_bf16 v[52:55], v[212:215], v[192:199], v248\n\t" \
  "ds_read_u16_d16_hi v160, v160 offset:0\n\t" \
  "v_rcp_f16 v227, v226\n\t" \
  "v_smfmac_f32_16x16x64_bf16 v[76:79], v[212:215], v[200:207], v248\n\t" \
  "s_waitcnt lgkmcnt(7)\n\t" \
  "ds_read_b64_tr_b16 v[210:211], v250 offset:7056\n\t" \
  "v_rcp_f16_sdwa v224, v224 dst_sel:DWORD dst_unused:UNUSED_PAD src0_sel:WORD_1\n\t" \
  "v_smfmac_f32_16x16x64_bf16 v[8:11], v[216:219], v[176:183], v248\n\t" \
  "ds_read_b64_tr_b16 v[212:213], v250 offset:6296\n\t" \
  "v_lshlrev_b32_sdwa v162, 1, v162 dst_sel:DWORD dst_unused:UNUSED_PAD src0_sel:DWORD src1_sel:WORD_1\n\t" \
  "v_smfmac_f32_16x16x64_bf16 v[32:35], v[216:219], v[184:191], v248\n\t" \
  "ds_read_u16_d16_hi v161, v161 offset:0\n\t" \
  "v_rcp_f16_sdwa v226, v226 dst_sel:DWORD dst_unused:UNUSED_PAD src0_sel:WORD_1\n\t" \
  "v_smfmac_f32_16x16x64_bf16 v[56:59], v[216:219], v[192:199], v248\n\t" \
  "ds_read_b64_tr_b16 v[214:215], v250 offset:7064\n\t" \
  "v_fma_mix_f32 v168, v168, v225, 0 op_sel_hi:[0,1,0]\n\t" \
  "v_cvt_pk_bf16_f32 v230, v0, v1\n\t" \
  "v_smfmac_f32_16x16x64_bf16 v[80:83], v[216:219], v[200:207], v248\n\t" \
  "s_waitcnt lgkmcnt(7)\n\t" \
  "ds_read_b64_tr_b16 v[216:217], v249 offset:6144\n\t" \
  "v_lshlrev_b32_sdwa v163, 1, v163 dst_sel:DWORD dst_unused:UNUSED_PAD src0_sel:DWORD src1_sel:WORD_1\n\t" \
  "v_cvt_pk_bf16_f32 v231, v2, v3\n\t" \
  "v_smfmac_f32_16x16x64_bf16 v[12:15], v[220:223], v[176:183], v248\n\t" \
  "ds_read_u16_d16_hi v162, v162 offset:0\n\t" \
  "v_fma_mix_f32 v170, v170, v227, 0 op_sel_hi:[0,1,0]\n\t" \
  "v_fma_mix_f32 v169, v169, v224, 0 op_sel_hi:[0,1,0]\n\t" \
  "v_cvt_pk_bf16_f32 v232, v4, v5\n\t" \
  "v_smfmac_f32_16x16x64_bf16 v[36:39], v[220:223], v[184:191], v248\n\t" \
  "ds_read_b64_tr_b16 v[218:219], v249 offset:6400\n\t" \
  "v_fma_mix_f32 v171, v171, v226, 0 op_sel_hi:[0,1,0]\n\t" \
  "v_cvt_pk_bf16_f32 v233, v6, v7\n\t" \
  "v_smfmac_f32_16x16x64_bf16 v[60:63], v[220:223], v[192:199], v248\n\t" \
  "v_lshlrev_b32_sdwa v156, 1, v156 dst_sel:DWORD dst_unused:UNUSED_PAD src0_sel:DWORD src1_sel:WORD_1\n\t" \
  "v_cvt_pk_f16_f32 v224, v172, v173\n\t" \
  "v_smfmac_f32_16x16x64_bf16 v[84:87], v[220:223], v[200:207], v248\n\t" \
  "s_waitcnt lgkmcnt(6)\n\t" \
  "ds_read_u16_d16_hi v163, v163 offset:0\n\t" \
  "v_cvt_pk_f16_f32 v226, v174, v175\n\t" \
  "v_pk_fma_f16 v225, v224, v224, %[h1] neg_lo:[1,0,0] neg_hi:[1,0,0]\n\t" \
  "v_smfmac_f32_16x16x64_bf16 v[16:19], v[208:211], v[176:183], v248\n\t" \
  "ds_read_b64_tr_b16 v[220:221], v249 offset:8192\n\t" \
  "v_pk_fma_f16 v227, v226, v226, %[h1] neg_lo:[1,0,0] neg_hi:[1,0,0]\n\t" \
  "v_smfmac_f32_16x16x64_bf16 v[40:43], v[208:211], v[184:191], v248\n\t" \
  "buffer_store_dwordx4 v[230:233], v253, %[osrd], %[ob] offen offset:0 sc1\n\t" \
  "v_lshlrev_b32_sdwa v157, 1, v157 dst_sel:DWORD dst_unused:UNUSED_PAD src0_sel:DWORD src1_sel:WORD_1\n\t" \
  "v_pk_mul_f16 v225, v224, v225\n\t" \
  "v_smfmac_f32_16x16x64_bf16 v[64:67], v[208:211], v[192:199], v248\n\t" \
  "ds_read_b64_tr_b16 v[222:223], v249 offset:8448\n\t" \
  "v_pk_mul_f16 v227, v226, v227\n\t" \
  "v_exp_f16 v224, v225\n\t" \
  "v_cvt_pk_bf16_f32 v234, v8, v9\n\t" \
  "v_smfmac_f32_16x16x64_bf16 v[88:91], v[208:211], v[200:207], v248\n\t" \
  "s_waitcnt lgkmcnt(6)\n\t" \
  "ds_read_u16_d16_hi v156, v156 offset:0\n\t" \
  "v_cvt_pk_bf16_f32 v235, v10, v11\n\t" \
  "v_smfmac_f32_16x16x64_bf16 v[20:23], v[212:215], v[176:183], v248\n\t" \
  "ds_read_b64_tr_b16 v[208:209], v249 offset:10240\n\t" \
  "v_lshlrev_b32_sdwa v158, 1, v158 dst_sel:DWORD dst_unused:UNUSED_PAD src0_sel:DWORD src1_sel:WORD_1\n\t" \
  "v_exp_f16 v226, v227\n\t" \
  "v_cvt_pk_bf16_f32 v236, v12, v13\n\t" \
  "v_smfmac_f32_16x16x64_bf16 v[44:47], v[212:215], v[184:191], v248\n\t" \
  "ds_read_u16_d16_hi v157, v157 offset:0\n\t" \
  "v_exp_f16_sdwa v224, v225 dst_sel:WORD_1 dst_unused:UNUSED_PRESERVE src0_sel:WORD_1\n\t" \
  "v_cvt_pk_bf16_f32 v237, v14, v15\n\t" \
  "v_smfmac_f32_16x16x64_bf16 v[68:71], v[212:215], v[192:199], v248\n\t" \
  "ds_read_b64_tr_b16 v[210:211], v249 offset:10496\n\t" \
  "v_smfmac_f32_16x16x64_bf16 v[92:95], v[212:215], v[200:207], v248\n\t" \
  "s_waitcnt lgkmcnt(7)\n\t" \
  "buffer_store_dwordx4 v[234:237], v253, %[osrd], %[ob] offen offset:64 sc1\n\t" \
  "v_lshlrev_b32_sdwa v159, 1, v159 dst_sel:DWORD dst_unused:UNUSED_PAD src0_sel:DWORD src1_sel:WORD_1\n\t" \
  "v_exp_f16_sdwa v226, v227 dst_sel:WORD_1 dst_unused:UNUSED_PRESERVE src0_sel:WORD_1\n\t" \
  "v_mfma_f32_16x16x32_bf16 v[184:187], v[216:219], v[108:111], 0\n\t" \
  "ds_read_b64_tr_b16 v[212:213], v249 offset:6656\n\t" \
  "v_lshlrev_b32_sdwa v164, 1, v164 dst_sel:DWORD dst_unused:UNUSED_PAD src0_sel:DWORD src1_sel:WORD_1\n\t" \
  "v_pk_fma_f16 v224, v224, %[hc], %[hc]\n\t" \
  "v_mfma_f32_16x16x32_bf16 v[192:195], v[216:219], v[120:123], 0\n\t" \
  "ds_read_u16_d16_hi v158, v158 offset:0\n\t" \
  "v_pk_fma_f16 v226, v226, %[hc], %[hc]\n\t" \
  "v_rcp_f16 v225, v224\n\t" \
  "v_cvt_pk_bf16_f32 v238, v16, v17\n\t" \
  "v_mfma_f32_16x16x32_bf16 v[200:203], v[216:219], v[132:135], 0\n\t" \
  "s_waitcnt lgkmcnt(6)\n\t" \
  "ds_read_b64_tr_b16 v[214:215], v249 offset:6912\n\t" \
  "v_cvt_pk_bf16_f32 v239, v18, v19\n\t" \
  "v_lshlrev_b32_sdwa v165, 1, v165 dst_sel:DWORD dst_unused:UNUSED_PAD src0_sel:DWORD src1_sel:WORD_1\n\t" \
  "v_rcp_f16 v227, v226\n\t" \
  "v_cvt_pk_bf16_f32 v240, v20, v21\n\t" \
  "v_mfma_f32_16x16x32_bf16 v[184:187], v[220:223], v[112:115], v[184:187]\n\t" \
  "ds_read_u16_d16_hi v159, v159 offset:0\n\t" \
  "v_rcp_f16_sdwa v224, v224 dst_sel:DWORD dst_unused:UNUSED_PAD src0_sel:WORD_1\n\t" \
  "v_cvt_pk_bf16_f32 v241, v22, v23\n\t" \
  "v_mfma_f32_16x16x32_bf16 v[192:195], v[220:223], v[124:127], v[192:195]\n\t" \
  "ds_read_b64_tr_b16 v[216:217], v249 offset:8704\n\t" \
  "v_mfma_f32_16x16x32_bf16 v[200:203], v[220:223], v[136:139], v[200:203]\n\t" \
  "s_waitcnt lgkmcnt(5)\n\t" \
  "ds_read_b64_tr_b16 v[218:219], v249 offset:8960\n\t" \
  "ds_read_u16_d16_hi v164, v164 offset:0\n\t" \
  "v_lshlrev_b32_sdwa v166, 1, v166 dst_sel:DWORD dst_unused:UNUSED_PAD src0_sel:DWORD src1_sel:WORD_1\n\t" \
  "v_rcp_f16_sdwa v226, v226 dst_sel:DWORD dst_unused:UNUSED_PAD src0_sel:WORD_1\n\t" \
  "v_fma_mix_f32 v172, v172, v225, 0 op_sel_hi:[0,1,0]\n\t" \
  "v_mfma_f32_16x16x32_bf16 v[184:187], v[208:211], v[116:119], v[184:187]\n\t" \
  "ds_read_b64_tr_b16 v[220:221], v249 offset:10752\n\t" \
  "v_lshlrev_b32_sdwa v167, 1, v167 dst_sel:DWORD dst_unused:UNUSED_PAD src0_sel:DWORD src1_sel:WORD_1\n\t" \
  "v_mfma_f32_16x16x32_bf16 v[192:195], v[208:211], v[128:131], v[192:195]\n\t" \
  "buffer_store_dwordx4 v[238:241], v253, %[osrd], %[ob] offen offset:128 sc1\n\t" \
  "v_fma_mix_f32 v174, v174, v227, 0 op_sel_hi:[0,1,0]\n\t" \
  "v_fma_mix_f32 v173, v173, v224, 0 op_sel_hi:[0,1,0]\n\t" \
  "v_mfma_f32_16x16x32_bf16 v[200:203], v[208:211], v[140:143], v[200:203]\n\t" \
  "s_waitcnt lgkmcnt(5)\n\t" \
  "ds_read_b64_tr_b16 v[222:223], v249 offset:11008\n\t" \
  "s_barrier\n\t" \
  "ds_read_u16_d16_hi v165, v165 offset:0\n\t" \
  "v_fma_mix_f32 v175, v175, v226, 0 op_sel_hi:[0,1,0]\n\t" \
  "v_mfma_f32_16x16x32_bf16 v[188:191], v[212:215], v[108:111], 0\n\t" \
  "ds_read_b64_tr_b16 v[208:209], v250 offset:12320\n\t" \
  "v_mfma_f32_16x16x32_bf16 v[196:199], v[212:215], v[120:123], 0\n\t" \
  "ds_read_u16_d16_hi v166, v166 offset:0\n\t" \
  "v_lshlrev_b32_sdwa v184, 1, v184 dst_sel:DWORD dst_unused:UNUSED_PAD src0_sel:DWORD src1_sel:WORD_1\n\t" \
  "v_cvt_pk_f16_f32 v224, v200, v201\n\t" \
  "v_mfma_f32_16x16x32_bf16 v[204:207], v[212:215], v[132:135], 0\n\t" \
  "s_waitcnt lgkmcnt(6)\n\t" \
  "ds_read_b64_tr_b16 v[210:211], v250 offset:13088\n\t" \
  "v_cvt_pk_f16_f32 v226, v202, v203\n\t" \
  "v_pk_fma_f16 v225, v224, v224, %[h1] neg_lo:[1,0,0] neg_hi:[1,0,0]\n\t" \
  "v_pk_fma_f16 v227, v226, v226, %[h1] neg_lo:[1,0,0] neg_hi:[1,0,0]\n\t" \
  "v_mfma_f32_16x16x32_bf16 v[188:191], v[216:219], v[112:115], v[188:191]\n\t" \
  "ds_read_u16_d16_hi v167, v167 offset:0\n\t" \
  "v_lshlrev_b32_sdwa v185, 1, v185 dst_sel:DWORD dst_unused:UNUSED_PAD src0_sel:DWORD src1_sel:WORD_1\n\t" \
  "v_mfma_f32_16x16x32_bf16 v[196:199], v[216:219], v[124:127], v[196:199]\n\t" \
  "ds_read_b64_tr_b16 v[212:213], v250 offset:12328\n\t" \
  "v_pk_mul_f16 v225, v224, v225\n\t" \
  "v_pk_mul_f16 v227, v226, v227\n\t" \
  "v_mfma_f32_16x16x32_bf16 v[204:207], v[216:219], v[136:139], v[204:207]\n\t" \
  "s_waitcnt lgkmcnt(6)\n\t" \
  "ds_read_b64_tr_b16 v[214:215], v250 offset:13096\n\t" \
  "v_exp_f16 v224, v225\n\t" \
  "v_lshlrev_b32_sdwa v186, 1, v186 dst_sel:DWORD dst_unused:UNUSED_PAD src0_sel:DWORD src1_sel:WORD_1\n\t" \
  "v_mfma_f32_16x16x32_bf16 v[188:191], v[220:223], v[116:119], v[188:191]\n\t" \
  "ds_read_u16_d16_hi v184, v184 offset:0\n\t" \
  "v_exp_f16 v226, v227\n\t" \
  "v_mfma_f32_16x16x32_bf16 v[196:199], v[220:223], v[128:131], v[196:199]\n\t" \
  "ds_read_b64_tr_b16 v[216:217], v250 offset:12384\n\t" \
  "v_exp_f16_sdwa v224, v225 dst_sel:WORD_1 dst_unused:UNUSED_PRESERVE src0_sel:WORD_1\n\t" \
  "v_mfma_f32_16x16x32_bf16 v[204:207], v[220:223], v[140:143], v[204:207]\n\t" \
  "s_waitcnt lgkmcnt(5)\n\t" \
  "ds_read_b64_tr_b16 v[218:219], v250 offset:13152\n\t" \
  "v_lshlrev_b32_sdwa v187, 1, v187 dst_sel:DWORD dst_unused:UNUSED_PAD src0_sel:DWORD src1_sel:WORD_1\n\t" \
  "v_exp_f16_sdwa v226, v227 dst_sel:WORD_1 dst_unused:UNUSED_PRESERVE src0_sel:WORD_1\n\t" \
  "v_smfmac_f32_16x16x64_bf16 v[24:27], v[208:211], v[152:159], v248\n\t" \
  "s_waitcnt lgkmcnt(5)\n\t" \
  "ds_read_u16_d16_hi v185, v185 offset:0\n\t" \
  "v_smfmac_f32_16x16x64_bf16 v[48:51], v[208:211], v[160:167], v248\n\t" \
  "ds_read_b64_tr_b16 v[220:221], v250 offset:12392\n\t" \
  "v_lshlrev_b32_sdwa v192, 1, v192 dst_sel:DWORD dst_unused:UNUSED_PAD src0_sel:DWORD src1_sel:WORD_1\n\t" \
  "v_pk_fma_f16 v224, v224, %[hc], %[hc]\n\t" \
  "v_smfmac_f32_16x16x64_bf16 v[72:75], v[208:211], v[168:175], v248\n\t" \
  "s_waitcnt lgkmcnt(5)\n\t" \
  "ds_read_b64_tr_b16 v[222:223], v250 offset:13160\n\t" \
  "ds_read_u16_d16_hi v186, v186 offset:0\n\t" \
  "v_pk_fma_f16 v226, v226, %[hc], %[hc]\n\t" \
  "v_rcp_f16 v225, v224\n\t" \
  "v_smfmac_f32_16x16x64_bf16 v[28:31], v[212:215], v[152:159], v248\n\t" \
  "s_add_u32 %[ob], %[ob], %[dob]\n\t" \
  "ds_read_b64_tr_b16 v[208:209], v250 offset:12448\n\t" \
  "v_lshlrev_b32_sdwa v193, 1, v193 dst_sel:DWORD dst_unused:UNUSED_PAD src0_sel:DWORD src1_sel:WORD_1\n\t" \
  "v_smfmac_f32_16x16x64_bf16 v[52:55], v[212:215], v[160:167], v248\n\t" \
  "ds_read_u16_d16_hi v187, v187 offset:0\n\t" \
  "v_rcp_f16 v227, v226\n\t" \
  "v_smfmac_f32_16x16x64_bf16 v[76:79], v[212:215], v[168:175], v248\n\t" \
  "s_waitcnt lgkmcnt(6)\n\t" \
  "ds_read_b64_tr_b16 v[210:211], v250 offset:13216\n\t" \
  "v_rcp_f16_sdwa v224, v224 dst_sel:DWORD dst_unused:UNUSED_PAD src0_sel:WORD_1\n\t" \
  "v_lshlrev_b32_sdwa v194, 1, v194 dst_sel:DWORD dst_unused:UNUSED_PAD src0_sel:DWORD src1_sel:WORD_1\n\t" \
  "v_smfmac_f32_16x16x64_bf16 v[32:35], v[216:219], v[152:159], v248\n\t" \
  "ds_read_u16_d16_hi v192, v192 offset:0\n\t" \
  "v_rcp_f16_sdwa v226, v226 dst_sel:DWORD dst_unused:UNUSED_PAD src0_sel:WORD_1\n\t" \
  "v_smfmac_f32_16x16x64_bf16 v[56:59], v[216:219], v[160:167], v248\n\t" \
  "ds_read_b64_tr_b16 v[212:213], v250 offset:12456\n\t" \
  "v_fma_mix_f32 v200, v200, v225, 0 op_sel_hi:[0,1,0]\n\t" \
  "v_smfmac_f32_16x16x64_bf16 v[80:83], v[216:219], v[168:175], v248\n\t" \
  "s_waitcnt lgkmcnt(6)\n\t" \
  "ds_read_b64_tr_b16 v[214:215], v250 offset:13224\n\t" \
  "v_lshlrev_b32_sdwa v195, 1, v195 dst_sel:DWORD dst_unused:UNUSED_PAD src0_sel:DWORD src1_sel:WORD_1\n\t" \
  "v_fma_mix_f32 v202, v202, v227, 0 op_sel_hi:[0,1,0]\n\t" \
  "v_fma_mix_f32 v201, v201, v224, 0 op_sel_hi:[0,1,0]\n\t" \
  "v_smfmac_f32_16x16x64_bf16 v[36:39], v[220:223], v[152:159], v248\n\t" \
  "ds_read_u16_d16_hi v193, v193 offset:0\n\t" \
  "v_fma_mix_f32 v203, v203, v226, 0 op_sel_hi:[0,1,0]\n\t" \
  "v_smfmac_f32_16x16x64_bf16 v[60:63], v[220:223], v[160:167], v248\n\t" \
  "ds_read_b64_tr_b16 v[216:217], v249 offset:12288\n\t" \
  "v_lshlrev_b32_sdwa v188, 1, v188 dst_sel:DWORD dst_unused:UNUSED_PAD src0_sel:DWORD src1_sel:WORD_1\n\t" \
  "v_cvt_pk_f16_f32 v224, v204, v205\n\t" \
  "v_smfmac_f32_16x16x64_bf16 v[84:87], v[220:223], v[168:175], v248\n\t" \
  "s_waitcnt lgkmcnt(5)\n\t" \
  "ds_read_b64_tr_b16 v[218:219], v249 offset:12544\n\t" \
  "ds_read_u16_d16_hi v194, v194 offset:0\n\t" \
  "v_cvt_pk_f16_f32 v226, v206, v207\n\t" \
  "v_pk_fma_f16 v225, v224, v224, %[h1] neg_lo:[1,0,0] neg_hi:[1,0,0]\n\t" \
  "v_pk_fma_f16 v227, v226, v226, %[h1] neg_lo:[1,0,0] neg_hi:[1,0,0]\n\t" \
  "v_smfmac_f32_16x16x64_bf16 v[40:43], v[208:211], v[152:159], v248\n\t" \
  "ds_read_b64_tr_b16 v[220:221], v249 offset:14336\n\t" \
  "v_lshlrev_b32_sdwa v189, 1, v189 dst_sel:DWORD dst_unused:UNUSED_PAD src0_sel:DWORD src1_sel:WORD_1\n\t" \
  "v_pk_mul_f16 v225, v224, v225\n\t" \
  "v_smfmac_f32_16x16x64_bf16 v[64:67], v[208:211], v[160:167], v248\n\t" \
  "ds_read_u16_d16_hi v195, v195 offset:0\n\t" \
  "v_pk_mul_f16 v227, v226, v227\n\t" \
  "v_exp_f16 v224, v225\n\t" \
  "v_smfmac_f32_16x16x64_bf16 v[88:91], v[208:211], v[168:175], v248\n\t" \
  "s_waitcnt lgkmcnt(6)\n\t" \
  "ds_read_b64_tr_b16 v[222:223], v249 offset:14592\n\t" \
  "v_lshlrev_b32_sdwa v190, 1, v190 dst_sel:DWORD dst_unused:UNUSED_PAD src0_sel:DWORD src1_sel:WORD_1\n\t" \
  "v_exp_f16 v226, v227\n\t" \
  "v_smfmac_f32_16x16x64_bf16 v[44:47], v[212:215], v[152:159], v248\n\t" \
  "ds_read_u16_d16_hi v188, v188 offset:0\n\t" \
  "v_exp_f16_sdwa v224, v225 dst_sel:WORD_1 dst_unused:UNUSED_PRESERVE src0_sel:WORD_1\n\t" \
  "v_smfmac_f32_16x16x64_bf16 v[68:71], v[212:215], v[160:167], v248\n\t" \
  "ds_read_b64_tr_b16 v[208:209], v249 offset:16384\n\t" \
  "v_smfmac_f32_16x16x64_bf16 v[92:95], v[212:215], v[168:175], v248\n\t" \
  "s_waitcnt lgkmcnt(6)\n\t" \
  "ds_read_b64_tr_b16 v[210:211], v249 offset:16640\n\t" \
  "v_lshlrev_b32_sdwa v191, 1, v191 dst_sel:DWORD dst_unused:UNUSED_PAD src0_sel:DWORD src1_sel:WORD_1\n\t" \
  "v_exp_f16_sdwa v226, v227 dst_sel:WORD_1 dst_unused:UNUSED_PRESERVE src0_sel:WORD_1\n\t" \
  "v_mfma_f32_16x16x32_bf16 v[152:155], v[216:219], v[108:111], 0\n\t" \
  "ds_read_u16_d16_hi v189, v189 offset:0\n\t" \
  "v_lshlrev_b32_sdwa v196, 1, v196 dst_sel:DWORD dst_unused:UNUSED_PAD src0_sel:DWORD src1_sel:WORD_1\n\t" \
  "v_pk_fma_f16 v224, v224, %[hc], %[hc]\n\t" \
  "v_mfma_f32_16x16x32_bf16 v[160:163], v[216:219], v[120:123], 0\n\t" \
  "ds_read_b64_tr_b16 v[212:213], v249 offset:12800\n\t" \
  "v_pk_fma_f16 v226, v226, %[hc], %[hc]\n\t" \
  "v_rcp_f16 v225, v224\n\t" \
  "v_mfma_f32_16x16x32_bf16 v[168:171], v[216:219], v[132:135], 0\n\t" \
  "s_waitcnt lgkmcnt(5)\n\t" \
  "ds_read_b64_tr_b16 v[214:215], v249 offset:13056\n\t" \
  "ds_read_u16_d16_hi v190, v190 offset:0\n\t" \
  "v_lshlrev_b32_sdwa v197, 1, v197 dst_sel:DWORD dst_unused:UNUSED_PAD src0_sel:DWORD src1_sel:WORD_1\n\t" \
  "v_rcp_f16 v227, v226\n\t" \
  "v_mfma_f32_16x16x32_bf16 v[152:155], v[220:223], v[112:115], v[152:155]\n\t" \
  "ds_read_b64_tr_b16 v[216:217], v249 offset:14848\n\t" \
  "v_rcp_f16_sdwa v224, v224 dst_sel:DWORD dst_unused:UNUSED_PAD src0_sel:WORD_1\n\t" \
  "v_mfma_f32_16x16x32_bf16 v[160:163], v[220:223], v[124:127], v[160:163]\n\t" \
  "ds_read_u16_d16_hi v191, v191 offset:0\n\t" \
  "v_mfma_f32_16x16x32_bf16 v[168:171], v[220:223], v[136:139], v[168:171]\n\t" \
  "s_waitcnt lgkmcnt(6)\n\t" \
  "ds_read_b64_tr_b16 v[218:219], v249 offset:15104\n\t" \
  "v_lshlrev_b32_sdwa v198, 1, v198 dst_sel:DWORD dst_unused:UNUSED_PAD src0_sel:DWORD src1_sel:WORD_1\n\t" \
  "v_rcp_f16_sdwa v226, v226 dst_sel:DWORD dst_unused:UNUSED_PAD src0_sel:WORD_1\n\t" \
  "v_fma_mix_f32 v204, v204, v225, 0 op_sel_hi:[0,1,0]\n\t" \
  "v_mfma_f32_16x16x32_bf16 v[152:155], v[208:211], v[116:119], v[152:155]\n\t" \
  "ds_read_u16_d16_hi v196, v196 offset:0\n\t" \
  "v_lshlrev_b32_sdwa v199, 1, v199 dst_sel:DWORD dst_unused:UNUSED_PAD src0_sel:DWORD src1_sel:WORD_1\n\t" \
  "v_mfma_f32_16x16x32_bf16 v[160:163], v[208:211], v[128:131], v[160:163]\n\t" \
  "ds_read_b64_tr_b16 v[220:221], v249 offset:16896\n\t" \
  "v_fma_mix_f32 v206, v206, v227, 0 op_sel_hi:[0,1,0]\n\t" \
  "v_fma_mix_f32 v205, v205, v224, 0 op_sel_hi:[0,1,0]\n\t" \
  "v_mfma_f32_16x16x32_bf16 v[168:171], v[208:211], v[140:143], v[168:171]\n\t" \
  "s_waitcnt lgkmcnt(6)\n\t" \
  "ds_read_b64_tr_b16 v[222:223], v249 offset:17152\n\t" \
  "s_barrier\n\t" \
  "ds_read_u16_d16_hi v197, v197 offset:0\n\t" \
  "v_fma_mix_f32 v207, v207, v226, 0 op_sel_hi:[0,1,0]\n\t" \
  "v_mfma_f32_16x16x32_bf16 v[156:159], v[212:215], v[108:111], 0\n\t" \
  "ds_read_b64_tr_b16 v[208:209], v250 offset:0\n\t" \
  "v_mfma_f32_16x16x32_bf16 v[164:167], v[212:215], v[120:123], 0\n\t" \
  "ds_read_u16_d16_hi v198, v198 offset:0\n\t" \
  "v_lshlrev_b32_sdwa v152, 1, v152 dst_sel:DWORD dst_unused:UNUSED_PAD src0_sel:DWORD src1_sel:WORD_1\n\t" \
  "v_cvt_pk_f16_f32 v224, v168, v169\n\t" \
  "v_mfma_f32_16x16x32_bf16 v[172:175], v[212:215], v[132:135], 0\n\t" \
  "s_waitcnt lgkmcnt(6)\n\t" \
  "ds_read_b64_tr_b16 v[210:211], v250 offset:768\n\t" \
  "v_cvt_pk_f16_f32 v226, v170, v171\n\t" \
  "v_pk_fma_f16 v225, v224, v224, %[h1] neg_lo:[1,0,0] neg_hi:[1,0,0]\n\t" \
  "v_pk_fma_f16 v227, v226, v226, %[h1] neg_lo:[1,0,0] neg_hi:[1,0,0]\n\t" \
  "v_mfma_f32_16x16x32_bf16 v[156:159], v[216:219], v[112:115], v[156:159]\n\t" \
  "ds_read_u16_d16_hi v199, v199 offset:0\n\t" \
  "v_lshlrev_b32_sdwa v153, 1, v153 dst_sel:DWORD dst_unused:UNUSED_PAD src0_sel:DWORD src1_sel:WORD_1\n\t" \
  "v_mfma_f32_16x16x32_bf16 v[164:167], v[216:219], v[124:127], v[164:167]\n\t" \
  "ds_read_b64_tr_b16 v[212:213], v250 offset:8\n\t" \
  "v_pk_mul_f16 v225, v224, v225\n\t" \
  "v_pk_mul_f16 v227, v226, v227\n\t" \
  "v_mfma_f32_16x16x32_bf16 v[172:175], v[216:219], v[136:139], v[172:175]\n\t" \
  "s_waitcnt lgkmcnt(6)\n\t" \
  "ds_read_b64_tr_b16 v[214:215], v250 offset:776\n\t" \
  "v_exp_f16 v224, v225\n\t" \
  "v_lshlrev_b32_sdwa v154, 1, v154 dst_sel:DWORD dst_unused:UNUSED_PAD src0_sel:DWORD src1_sel:WORD_1\n\t" \
  "v_mfma_f32_16x16x32_bf16 v[156:159], v[220:223], v[116:119], v[156:159]\n\t" \
  "ds_read_u16_d16_hi v152, v152 offset:0\n\t" \
  "v_exp_f16 v226, v227\n\t" \
  "v_mfma_f32_16x16x32_bf16 v[164:167], v[220:223], v[128:131], v[164:167]\n\t" \
  "ds_read_b64_tr_b16 v[216:217], v250 offset:64\n\t" \
  "v_exp_f16_sdwa v224, v225 dst_sel:WORD_1 dst_unused:UNUSED_PRESERVE src0_sel:WORD_1\n\t" \
  "v_mfma_f32_16x16x32_bf16 v[172:175], v[220:223], v[140:143], v[172:175]\n\t" \
  "s_waitcnt lgkmcnt(5)\n\t" \
  "ds_read_b64_tr_b16 v[218:219], v250 offset:832\n\t" \
  "v_lshlrev_b32_sdwa v155, 1, v155 dst_sel:DWORD dst_unused:UNUSED_PAD src0_sel:DWORD src1_sel:WORD_1\n\t" \
  "v_exp_f16_sdwa v226, v227 dst_sel:WORD_1 dst_unused:UNUSED_PRESERVE src0_sel:WORD_1\n\t" \
  "v_smfmac_f32_16x16x64_bf16 v[24:27], v[208:211], v[184:191], v248\n\t" \
  "s_waitcnt lgkmcnt(5)\n\t" \
  "ds_read_u16_d16_hi v153, v153 offset:0\n\t" \
  "v_smfmac_f32_16x16x64_bf16 v[48:51], v[208:211], v[192:199], v248\n\t" \
  "ds_read_b64_tr_b16 v[220:221], v250 offset:72\n\t" \
  "v_lshlrev_b32_sdwa v160, 1, v160 dst_sel:DWORD dst_unused:UNUSED_PAD src0_sel:DWORD src1_sel:WORD_1\n\t" \
  "v_pk_fma_f16 v224, v224, %[hc], %[hc]\n\t" \
  "v_smfmac_f32_16x16x64_bf16 v[72:75], v[208:211], v[200:207], v248\n\t" \
  "s_waitcnt lgkmcnt(5)\n\t" \
  "ds_read_b64_tr_b16 v[222:223], v250 offset:840\n\t" \
  "ds_read_u16_d16_hi v154, v154 offset:0\n\t" \
  "v_pk_fma_f16 v226, v226, %[hc], %[hc]\n\t" \
  "v_rcp_f16 v225, v224\n\t" \
  "v_smfmac_f32_16x16x64_bf16 v[28:31], v[212:215], v[184:191], v248\n\t" \
  "ds_read_b64_tr_b16 v[208:209], v250 offset:128\n\t" \
  "v_lshlrev_b32_sdwa v161, 1, v161 dst_sel:DWORD dst_unused:UNUSED_PAD src0_sel:DWORD src1_sel:WORD_1\n\t" \
  "v_smfmac_f32_16x16x64_bf16 v[52:55], v[212:215], v[192:199], v248\n\t" \
  "ds_read_u16_d16_hi v155, v155 offset:0\n\t" \
  "v_rcp_f16 v227, v226\n\t" \
  "v_smfmac_f32_16x16x64_bf16 v[76:79], v[212:215], v[200:207], v248\n\t" \
  "s_waitcnt lgkmcnt(6)\n\t" \
  "ds_read_b64_tr_b16 v[210:211], v250 offset:896\n\t" \
  "v_rcp_f16_sdwa v224, v224 dst_sel:DWORD dst_unused:UNUSED_PAD src0_sel:WORD_1\n\t" \
  "v_lshlrev_b32_sdwa v162, 1, v162 dst_sel:DWORD dst_unused:UNUSED_PAD src0_sel:DWORD src1_sel:WORD_1\n\t" \
  "v_smfmac_f32_16x16x64_bf16 v[32:35], v[216:219], v[184:191], v248\n\t" \
  "ds_read_u16_d16_hi v160, v160 offset:0\n\t" \
  "v_rcp_f16_sdwa v226, v226 dst_sel:DWORD dst_unused:UNUSED_PAD src0_sel:WORD_1\n\t" \
  "v_smfmac_f32_16x16x64_bf16 v[56:59], v[216:219], v[192:199], v248\n\t" \
  "ds_read_b64_tr_b16 v[212:213], v250 offset:136\n\t" \
  "v_fma_mix_f32 v168, v168, v225, 0 op_sel_hi:[0,1,0]\n\t" \
  "v_smfmac_f32_16x16x64_bf16 v[80:83], v[216:219], v[200:207], v248\n\t" \
  "s_waitcnt lgkmcnt(6)\n\t" \
  "ds_read_b64_tr_b16 v[214:215], v250 offset:904\n\t" \
  "v_lshlrev_b32_sdwa v163, 1, v163 dst_sel:DWORD dst_unused:UNUSED_PAD src0_sel:DWORD src1_sel:WORD_1\n\t" \
  "v_fma_mix_f32 v170, v170, v227, 0 op_sel_hi:[0,1,0]\n\t" \
  "v_fma_mix_f32 v169, v169, v224, 0 op_sel_hi:[0,1,0]\n\t" \
  "v_smfmac_f32_16x16x64_bf16 v[36:39], v[220:223], v[184:191], v248\n\t" \
  "ds_read_u16_d16_hi v161, v161 offset:0\n\t" \
  "v_fma_mix_f32 v171, v171, v226, 0 op_sel_hi:[0,1,0]\n\t" \
  "v_smfmac_f32_16x16x64_bf16 v[60:63], v[220:223], v[192:199], v248\n\t" \
  "ds_read_b64_tr_b16 v[216:217], v249 offset:18432\n\t" \
  "v_lshlrev_b32_sdwa v156, 1, v156 dst_sel:DWORD dst_unused:UNUSED_PAD src0_sel:DWORD src1_sel:WORD_1\n\t" \
  "v_cvt_pk_f16_f32 v224, v172, v173\n\t" \
  "v_smfmac_f32_16x16x64_bf16 v[84:87], v[220:223], v[200:207], v248\n\t" \
  "s_waitcnt lgkmcnt(5)\n\t" \
  "ds_read_b64_tr_b16 v[218:219], v249 offset:18688\n\t" \
  "ds_read_u16_d16_hi v162, v162 offset:0\n\t" \
  "v_cvt_pk_f16_f32 v226, v174, v175\n\t" \
  "v_pk_fma_f16 v225, v224, v224, %[h1] neg_lo:[1,0,0] neg_hi:[1,0,0]\n\t" \
  "v_pk_fma_f16 v227, v226, v226, %[h1] neg_lo:[1,0,0] neg_hi:[1,0,0]\n\t" \
  "v_smfmac_f32_16x16x64_bf16 v[40:43], v[208:211], v[184:191], v248\n\t" \
  "ds_read_b64_tr_b16 v[220:221], v249 offset:20480\n\t" \
  "v_lshlrev_b32_sdwa v157, 1, v157 dst_sel:DWORD dst_unused:UNUSED_PAD src0_sel:DWORD src1_sel:WORD_1\n\t" \
  "v_pk_mul_f16 v225, v224, v225\n\t" \
  "v_smfmac_f32_16x16x64_bf16 v[64:67], v[208:211], v[192:199], v248\n\t" \
  "ds_read_u16_d16_hi v163, v163 offset:0\n\t" \
  "v_pk_mul_f16 v227, v226, v227\n\t" \
  "v_exp_f16 v224, v225\n\t" \
  "v_smfmac_f32_16x16x64_bf16 v[88:91], v[208:211], v[200:207], v248\n\t" \
  "s_waitcnt lgkmcnt(6)\n\t" \
  "ds_read_b64_tr_b16 v[222:223], v249 offset:20736\n\t" \
  "v_lshlrev_b32_sdwa v158, 1, v158 dst_sel:DWORD dst_unused:UNUSED_PAD src0_sel:DWORD src1_sel:WORD_1\n\t" \
  "v_exp_f16 v226, v227\n\t" \
  "v_smfmac_f32_16x16x64_bf16 v[44:47], v[212:215], v[184:191], v248\n\t" \
  "ds_read_u16_d16_hi v156, v156 offset:0\n\t" \
  "v_exp_f16_sdwa v224, v225 dst_sel:WORD_1 dst_unused:UNUSED_PRESERVE src0_sel:WORD_1\n\t" \
  "v_smfmac_f32_16x16x64_bf16 v[68:71], v[212:215], v[192:199], v248\n\t" \
  "ds_read_b64_tr_b16 v[208:209], v249 offset:22528\n\t" \
  "v_smfmac_f32_16x16x64_bf16 v[92:95], v[212:215], v[200:207], v248\n\t" \
  "s_waitcnt lgkmcnt(6)\n\t" \
  "ds_read_b64_tr_b16 v[210:211], v249 offset:22784\n\t" \
  "v_lshlrev_b32_sdwa v159, 1, v159 dst_sel:DWORD dst_unused:UNUSED_PAD src0_sel:DWORD src1_sel:WORD_1\n\t" \
  "v_exp_f16_sdwa v226, v227 dst_sel:WORD_1 dst_unused:UNUSED_PRESERVE src0_sel:WORD_1\n\t" \
  "v_lshlrev_b32_sdwa v164, 1, v164 dst_sel:DWORD dst_unused:UNUSED_PAD src0_sel:DWORD src1_sel:WORD_1\n\t" \
  "v_pk_fma_f16 v224, v224, %[hc], %[hc]\n\t" \
  "v_mfma_f32_16x16x32_bf16 v[192:195], v[216:219], v[120:123], 0\n\t" \
  "ds_read_b64_tr_b16 v[212:213], v249 offset:18944\n\t" \
  "ds_read_b64_tr_b16 v[214:215], v249 offset:19200\n\t" \
  "ds_read_u16_d16_hi v157, v157 offset:0\n\t" \
  "ds_read_u16_d16_hi v158, v158 offset:0\n\t" \
  "ds_read_u16_d16_hi v159, v159 offset:0\n\t" \
  "v_pk_fma_f16 v226, v226, %[hc], %[hc]\n\t" \
  "v_rcp_f16 v225, v224\n\t" \
  "v_mfma_f32_16x16x32_bf16 v[200:203], v[216:219], v[132:135], 0\n\t" \
  "ds_read_b64_tr_b16 v[216:217], v249 offset:20992\n\t" \
  "ds_read_b64_tr_b16 v[218:219], v249 offset:21248\n\t" \
  "v_lshlrev_b32_sdwa v165, 1, v165 dst_sel:DWORD dst_unused:UNUSED_PAD src0_sel:DWORD src1_sel:WORD_1\n\t" \
  "v_rcp_f16 v227, v226\n\t" \
  "v_rcp_f16_sdwa v224, v224 dst_sel:DWORD dst_unused:UNUSED_PAD src0_sel:WORD_1\n\t" \
  "s_waitcnt lgkmcnt(10)\n\t" \
  "v_mfma_f32_16x16x32_bf16 v[192:195], v[220:223], v[124:127], v[192:195]\n\t" \
  "ds_read_u16_d16_hi v164, v164 offset:0\n\t" \
  "v_mfma_f32_16x16x32_bf16 v[200:203], v[220:223], v[136:139], v[200:203]\n\t" \
  "ds_read_b64_tr_b16 v[220:221], v249 offset:23040\n\t" \
  "ds_read_b64_tr_b16 v[222:223], v249 offset:23296\n\t" \
  "v_lshlrev_b32_sdwa v166, 1, v166 dst_sel:DWORD dst_unused:UNUSED_PAD src0_sel:DWORD src1_sel:WORD_1\n\t" \
  "v_rcp_f16_sdwa v226, v226 dst_sel:DWORD dst_unused:UNUSED_PAD src0_sel:WORD_1\n\t" \
  "v_fma_mix_f32 v172, v172, v225, 0 op_sel_hi:[0,1,0]\n\t" \
  "v_lshlrev_b32_sdwa v167, 1, v167 dst_sel:DWORD dst_unused:UNUSED_PAD src0_sel:DWORD src1_sel:WORD_1\n\t" \
  "s_waitcnt lgkmcnt(10)\n\t" \
  "v_mfma_f32_16x16x32_bf16 v[192:195], v[208:211], v[128:131], v[192:195]\n\t" \
  "ds_read_u16_d16_hi v165, v165 offset:0\n\t" \
  "ds_read_u16_d16_hi v166, v166 offset:0\n\t" \
  "v_fma_mix_f32 v174, v174, v227, 0 op_sel_hi:[0,1,0]\n\t" \
  "v_fma_mix_f32 v173, v173, v224, 0 op_sel_hi:[0,1,0]\n\t" \
  "v_mfma_f32_16x16x32_bf16 v[200:203], v[208:211], v[140:143], v[200:203]\n\t" \
  "s_barrier\n\t" \
  "ds_read_b64_tr_b16 v[208:209], v250 offset:6160\n\t" \
  "ds_read_b64_tr_b16 v[210:211], v250 offset:6928\n\t" \
  "v_fma_mix_f32 v175, v175, v226, 0 op_sel_hi:[0,1,0]\n\t" \
  "s_waitcnt lgkmcnt(12)\n\t" \
  "v_mfma_f32_16x16x32_bf16 v[196:199], v[212:215], v[120:123], 0\n\t" \
  "ds_read_u16_d16_hi v167, v167 offset:0\n\t" \
  "v_lshlrev_b32_sdwa v192, 1, v192 dst_sel:DWORD dst_unused:UNUSED_PAD src0_sel:DWORD src1_sel:WORD_1\n\t" \
  "v_cvt_pk_f16_f32 v224, v200, v201\n\t" \
  "v_mfma_f32_16x16x32_bf16 v[204:207], v[212:215], v[132:135], 0\n\t" \
  "ds_read_b64_tr_b16 v[212:213], v250 offset:6168\n\t" \
  "ds_read_b64_tr_b16 v[214:215], v250 offset:6936\n\t" \
  "v_cvt_pk_f16_f32 v226, v202, v203\n\t" \
  "v_pk_fma_f16 v225, v224, v224, %[h1] neg_lo:[1,0,0] neg_hi:[1,0,0]\n\t" \
  "v_pk_fma_f16 v227, v226, v226, %[h1] neg_lo:[1,0,0] neg_hi:[1,0,0]\n\t" \
  "v_pk_mul_f16 v225, v224, v225\n\t" \
  "s_waitcnt lgkmcnt(10)\n\t" \
  "v_mfma_f32_16x16x32_bf16 v[196:199], v[216:219], v[124:127], v[196:199]\n\t" \
  "v_pk_mul_f16 v227, v226, v227\n\t" \
  "v_mfma_f32_16x16x32_bf16 v[204:207], v[216:219], v[136:139], v[204:207]\n\t" \
  "ds_read_b64_tr_b16 v[216:217], v250 offset:6224\n\t" \
  "ds_read_b64_tr_b16 v[218:219], v250 offset:6992\n\t" \
  "v_exp_f16 v224, v225\n\t" \
  "v_lshlrev_b32_sdwa v193, 1, v193 dst_sel:DWORD dst_unused:UNUSED_PAD src0_sel:DWORD src1_sel:WORD_1\n\t" \
  "v_exp_f16 v226, v227\n\t" \
  "s_waitcnt lgkmcnt(9)\n\t" \
  "v_mfma_f32_16x16x32_bf16 v[196:199], v[220:223], v[128:131], v[196:199]\n\t" \
  "ds_read_u16_d16_hi v192, v192 offset:0\n\t" \
  "v_mfma_f32_16x16x32_bf16 v[204:207], v[220:223], v[140:143], v[204:207]\n\t" \
  "ds_read_b64_tr_b16 v[220:221], v250 offset:6232\n\t" \
  "ds_read_b64_tr_b16 v[222:223], v250 offset:7000\n\t" \
  "v_exp_f16_sdwa v224, v225 dst_sel:WORD_1 dst_unused:UNUSED_PRESERVE src0_sel:WORD_1\n\t" \
  "v_exp_f16_sdwa v226, v227 dst_sel:WORD_1 dst_unused:UNUSED_PRESERVE src0_sel:WORD_1\n\t" \
  "s_waitcnt lgkmcnt(8)\n\t" \
  "v_smfmac_f32_16x16x64_bf16 v[24:27], v[208:211], v[152:159], v248\n\t" \
  "s_waitcnt lgkmcnt(7)\n\t" \
  "v_smfmac_f32_16x16x64_bf16 v[48:51], v[208:211], v[160:167], v248\n\t" \
  "v_lshlrev_b32_sdwa v194, 1, v194 dst_sel:DWORD dst_unused:UNUSED_PAD src0_sel:DWORD src1_sel:WORD_1\n\t" \
  "v_pk_fma_f16 v224, v224, %[hc], %[hc]\n\t" \
  "v_smfmac_f32_16x16x64_bf16 v[72:75], v[208:211], v[168:175], v248\n\t" \
  "s_waitcnt lgkmcnt(5)\n\t" \
  "ds_read_u16_d16_hi v193, v193 offset:0\n\t" \
  "v_pk_fma_f16 v226, v226, %[hc], %[hc]\n\t" \
  "v_rcp_f16 v225, v224\n\t" \
  "v_smfmac_f32_16x16x64_bf16 v[28:31], v[212:215], v[152:159], v248\n\t" \
  "ds_read_b64_tr_b16 v[208:209], v250 offset:6288\n\t" \
  "v_rcp_f16 v227, v226\n\t" \
  "v_smfmac_f32_16x16x64_bf16 v[52:55], v[212:215], v[160:167], v248\n\t" \
  "ds_read_b64_tr_b16 v[210:211], v250 offset:7056\n\t" \
  "v_smfmac_f32_16x16x64_bf16 v[76:79], v[212:215], v[168:175], v248\n\t" \
  "s_waitcnt lgkmcnt(6)\n\t" \
  "ds_read_b64_tr_b16 v[212:213], v250 offset:6296\n\t" \
  "v_rcp_f16_sdwa v224, v224 dst_sel:DWORD dst_unused:UNUSED_PAD src0_sel:WORD_1\n\t" \
  "v_lshlrev_b32_sdwa v195, 1, v195 dst_sel:DWORD dst_unused:UNUSED_PAD src0_sel:DWORD src1_sel:WORD_1\n\t" \
  "v_smfmac_f32_16x16x64_bf16 v[32:35], v[216:219], v[152:159], v248\n\t" \
  "ds_read_u16_d16_hi v194, v194 offset:0\n\t" \
  "v_rcp_f16_sdwa v226, v226 dst_sel:DWORD dst_unused:UNUSED_PAD src0_sel:WORD_1\n\t" \
  "v_smfmac_f32_16x16x64_bf16 v[56:59], v[216:219], v[160:167], v248\n\t" \
  "ds_read_b64_tr_b16 v[214:215], v250 offset:7064\n\t" \
  "v_cvt_pk_bf16_f32 v230, v24, v25\n\t" \
  "v_smfmac_f32_16x16x64_bf16 v[80:83], v[216:219], v[168:175], v248\n\t" \
  "s_waitcnt lgkmcnt(6)\n\t" \
  "ds_read_b64_tr_b16 v[216:217], v249 offset:0\n\t" \
  "v_fma_mix_f32 v200, v200, v225, 0 op_sel_hi:[0,1,0]\n\t" \
  "v_fma_mix_f32 v202, v202, v227, 0 op_sel_hi:[0,1,0]\n\t" \
  "v_cvt_pk_bf16_f32 v231, v26, v27\n\t" \
  "v_fma_mix_f32 v201, v201, v224, 0 op_sel_hi:[0,1,0]\n\t" \
  "v_cvt_pk_bf16_f32 v232, v28, v29\n\t" \
  "v_smfmac_f32_16x16x64_bf16 v[36:39], v[220:223], v[152:159], v248\n\t" \
  "ds_read_b64_tr_b16 v[218:219], v249 offset:256\n\t" \
  "v_fma_mix_f32 v203, v203, v226, 0 op_sel_hi:[0,1,0]\n\t" \
  "v_cvt_pk_bf16_f32 v233, v30, v31\n\t" \
  "v_smfmac_f32_16x16x64_bf16 v[60:63], v[220:223], v[160:167], v248\n\t" \
  "v_lshlrev_b32_sdwa v196, 1, v196 dst_sel:DWORD dst_unused:UNUSED_PAD src0_sel:DWORD src1_sel:WORD_1\n\t" \
  "v_cvt_pk_f16_f32 v224, v204, v205\n\t" \
  "v_smfmac_f32_16x16x64_bf16 v[84:87], v[220:223], v[168:175], v248\n\t" \
  "s_waitcnt lgkmcnt(5)\n\t" \
  "ds_read_u16_d16_hi v195, v195 offset:0\n\t" \
  "v_cvt_pk_f16_f32 v226, v206, v207\n\t" \
  "v_pk_fma_f16 v225, v224, v224, %[h1] neg_lo:[1,0,0] neg_hi:[1,0,0]\n\t" \
  "v_pk_fma_f16 v227, v226, v226, %[h1] neg_lo:[1,0,0] neg_hi:[1,0,0]\n\t" \
  "v_smfmac_f32_16x16x64_bf16 v[40:43], v[208:211], v[152:159], v248\n\t" \
  "ds_read_b64_tr_b16 v[220:221], v249 offset:2048\n\t" \
  "v_pk_mul_f16 v225, v224, v225\n\t" \
  "v_smfmac_f32_16x16x64_bf16 v[64:67], v[208:211], v[160:167], v248\n\t" \
  "buffer_store_dwordx4 v[230:233], v253, %[osrd], %[ob] offen offset:0 sc1\n\t" \
  "v_pk_mul_f16 v227, v226, v227\n\t" \
  "v_exp_f16 v224, v225\n\t" \
  "v_cvt_pk_bf16_f32 v234, v32, v33\n\t" \
  "v_smfmac_f32_16x16x64_bf16 v[88:91], v[208:211], v[168:175], v248\n\t" \
  "s_waitcnt lgkmcnt(4)\n\t" \
  "ds_read_b64_tr_b16 v[222:223], v249 offset:2304\n\t" \
  "v_cvt_pk_bf16_f32 v235, v34, v35\n\t" \
  "v_lshlrev_b32_sdwa v197, 1, v197 dst_sel:DWORD dst_unused:UNUSED_PAD src0_sel:DWORD src1_sel:WORD_1\n\t" \
  "v_exp_f16 v226, v227\n\t" \
  "v_cvt_pk_bf16_f32 v236, v36, v37\n\t" \
  "v_smfmac_f32_16x16x64_bf16 v[44:47], v[212:215], v[152:159], v248\n\t" \
  "ds_read_u16_d16_hi v196, v196 offset:0\n\t" \
  "v_cvt_pk_bf16_f32 v237, v38, v39\n\t" \
  "v_smfmac_f32_16x16x64_bf16 v[68:71], v[212:215], v[160:167], v248\n\t" \
  "ds_read_b64_tr_b16 v[208:209], v249 offset:4096\n\t" \
  "v_exp_f16_sdwa v224, v225 dst_sel:WORD_1 dst_unused:UNUSED_PRESERVE src0_sel:WORD_1\n\t" \
  "v_smfmac_f32_16x16x64_bf16 v[92:95], v[212:215], v[168:175], v248\n\t" \
  "s_waitcnt lgkmcnt(5)\n\t" \
  "ds_read_b64_tr_b16 v[210:211], v249 offset:4352\n\t" \
  "v_exp_f16_sdwa v226, v227 dst_sel:WORD_1 dst_unused:UNUSED_PRESERVE src0_sel:WORD_1\n\t" \
  "v_lshlrev_b32_sdwa v198, 1, v198 dst_sel:DWORD dst_unused:UNUSED_PAD src0_sel:DWORD src1_sel:WORD_1\n\t" \
  "v_pk_fma_f16 v224, v224, %[hc], %[hc]\n\t" \
  "v_mfma_f32_16x16x32_bf16 v[160:163], v[216:219], v[120:123], 0\n\t" \
  "ds_read_b64_tr_b16 v[212:213], v249 offset:512\n\t" \
  "ds_read_b64_tr_b16 v[214:215], v249 offset:768\n\t" \
  "buffer_store_dwordx4 v[234:237], v253, %[osrd], %[ob] offen offset:64 sc1\n\t" \
  "ds_read_u16_d16_hi v197, v197 offset:0\n\t" \
  "v_pk_fma_f16 v226, v226, %[hc], %[hc]\n\t" \
  "v_cvt_pk_bf16_f32 v238, v40, v41\n\t" \
  "v_mfma_f32_16x16x32_bf16 v[168:171], v[216:219], v[132:135], 0\n\t" \
  "ds_read_b64_tr_b16 v[216:217], v249 offset:2560\n\t" \
  "ds_read_b64_tr_b16 v[218:219], v249 offset:2816\n\t" \
  "v_rcp_f16 v225, v224\n\t" \
  "v_cvt_pk_bf16_f32 v239, v42, v43\n\t" \
  "v_rcp_f16 v227, v226\n\t" \
  "v_cvt_pk_bf16_f32 v240, v44, v45\n\t" \
  "v_rcp_f16_sdwa v224, v224 dst_sel:DWORD dst_unused:UNUSED_PAD src0_sel:WORD_1\n\t" \
  "v_cvt_pk_bf16_f32 v241, v46, v47\n\t" \
  "s_waitcnt lgkmcnt(8)\n\t" \
  "v_mfma_f32_16x16x32_bf16 v[160:163], v[220:223], v[124:127], v[160:163]\n\t" \
  "v_mfma_f32_16x16x32_bf16 v[168:171], v[220:223], v[136:139], v[168:171]\n\t" \
  "ds_read_b64_tr_b16 v[220:221], v249 offset:4608\n\t" \
  "ds_read_b64_tr_b16 v[222:223], v249 offset:4864\n\t" \
  "buffer_store_dwordx4 v[238:241], v253, %[osrd], %[ob] offen offset:128 sc1\n\t" \
  "v_lshlrev_b32_sdwa v199, 1, v199 dst_sel:DWORD dst_unused:UNUSED_PAD src0_sel:DWORD src1_sel:WORD_1\n\t" \
  "v_rcp_f16_sdwa v226, v226 dst_sel:DWORD dst_unused:UNUSED_PAD src0_sel:WORD_1\n\t" \
  "v_fma_mix_f32 v204, v204, v225, 0 op_sel_hi:[0,1,0]\n\t" \
  "v_fma_mix_f32 v206, v206, v227, 0 op_sel_hi:[0,1,0]\n\t" \
  "s_waitcnt lgkmcnt(7)\n\t" \
  "v_mfma_f32_16x16x32_bf16 v[160:163], v[208:211], v[128:131], v[160:163]\n\t" \
  "ds_read_u16_d16_hi v198, v198 offset:0\n\t" \
  "v_fma_mix_f32 v205, v205, v224, 0 op_sel_hi:[0,1,0]\n\t" \
  "v_mfma_f32_16x16x32_bf16 v[168:171], v[208:211], v[140:143], v[168:171]\n\t" \
  "s_barrier\n\t" \
  "ds_read_b64_tr_b16 v[208:209], v250 offset:12320\n\t" \
  "ds_read_b64_tr_b16 v[210:211], v250 offset:13088\n\t" \
  "v_fma_mix_f32 v207, v207, v226, 0 op_sel_hi:[0,1,0]\n\t" \
  "s_waitcnt lgkmcnt(8)\n\t" \
  "v_mfma_f32_16x16x32_bf16 v[164:167], v[212:215], v[120:123], 0\n\t" \
  "ds_read_u16_d16_hi v199, v199 offset:0\n\t" \
  "v_lshlrev_b32_sdwa v160, 1, v160 dst_sel:DWORD dst_unused:UNUSED_PAD src0_sel:DWORD src1_sel:WORD_1\n\t" \
  "v_cvt_pk_f16_f32 v224, v168, v169\n\t" \
  "v_mfma_f32_16x16x32_bf16 v[172:175], v[212:215], v[132:135], 0\n\t" \
  "ds_read_b64_tr_b16 v[212:213], v250 offset:12328\n\t" \
  "ds_read_b64_tr_b16 v[214:215], v250 offset:13096\n\t" \
  "v_cvt_pk_f16_f32 v226, v170, v171\n\t" \
  "v_pk_fma_f16 v225, v224, v224, %[h1] neg_lo:[1,0,0] neg_hi:[1,0,0]\n\t" \
  "v_pk_fma_f16 v227, v226, v226, %[h1] neg_lo:[1,0,0] neg_hi:[1,0,0]\n\t" \
  "v_pk_mul_f16 v225, v224, v225\n\t" \
  "s_waitcnt lgkmcnt(8)\n\t" \
  "v_mfma_f32_16x16x32_bf16 v[164:167], v[216:219], v[124:127], v[164:167]\n\t" \
  "v_pk_mul_f16 v227, v226, v227\n\t" \
  "v_mfma_f32_16x16x32_bf16 v[172:175], v[216:219], v[136:139], v[172:175]\n\t" \
  "ds_read_b64_tr_b16 v[216:217], v250 offset:12384\n\t" \
  "ds_read_b64_tr_b16 v[218:219], v250 offset:13152\n\t" \
  "v_exp_f16 v224, v225\n\t" \
  "v_lshlrev_b32_sdwa v161, 1, v161 dst_sel:DWORD dst_unused:UNUSED_PAD src0_sel:DWORD src1_sel:WORD_1\n\t" \
  "v_exp_f16 v226, v227\n\t" \
  "s_waitcnt lgkmcnt(8)\n\t" \
  "v_mfma_f32_16x16x32_bf16 v[164:167], v[220:223], v[128:131], v[164:167]\n\t" \
  "ds_read_u16_d16_hi v160, v160 offset:0\n\t" \
  "v_mfma_f32_16x16x32_bf16 v[172:175], v[220:223], v[140:143], v[172:175]\n\t" \
  "ds_read_b64_tr_b16 v[220:221], v250 offset:12392\n\t" \
  "ds_read_b64_tr_b16 v[222:223], v250 offset:13160\n\t" \
  "v_exp_f16_sdwa v224, v225 dst_sel:WORD_1 dst_unused:UNUSED_PRESERVE src0_sel:WORD_1\n\t" \
  "v_exp_f16_sdwa v226, v227 dst_sel:WORD_1 dst_unused:UNUSED_PRESERVE src0_sel:WORD_1\n\t" \
  "s_waitcnt lgkmcnt(7)\n\t" \
  "v_smfmac_f32_16x16x64_bf16 v[48:51], v[208:211], v[192:199], v248\n\t" \
  "v_lshlrev_b32_sdwa v162, 1, v162 dst_sel:DWORD dst_unused:UNUSED_PAD src0_sel:DWORD src1_sel:WORD_1\n\t" \
  "v_pk_fma_f16 v224, v224, %[hc], %[hc]\n\t" \
  "v_smfmac_f32_16x16x64_bf16 v[72:75], v[208:211], v[200:207], v248\n\t" \
  "ds_read_b64_tr_b16 v[208:209], v250 offset:12448\n\t" \
  "ds_read_b64_tr_b16 v[210:211], v250 offset:13216\n\t" \
  "ds_read_u16_d16_hi v161, v161 offset:0\n\t" \
  "v_pk_fma_f16 v226, v226, %[hc], %[hc]\n\t" \
  "v_rcp_f16 v225, v224\n\t" \
  "v_rcp_f16 v227, v226\n\t" \
  "s_waitcnt lgkmcnt(8)\n\t" \
  "v_smfmac_f32_16x16x64_bf16 v[52:55], v[212:215], v[192:199], v248\n\t" \
  "v_smfmac_f32_16x16x64_bf16 v[76:79], v[212:215], v[200:207], v248\n\t" \
  "ds_read_b64_tr_b16 v[212:213], v250 offset:12456\n\t" \
  "ds_read_b64_tr_b16 v[214:215], v250 offset:13224\n\t" \
  "v_rcp_f16_sdwa v224, v224 dst_sel:DWORD dst_unused:UNUSED_PAD src0_sel:WORD_1\n\t" \
  "v_lshlrev_b32_sdwa v163, 1, v163 dst_sel:DWORD dst_unused:UNUSED_PAD src0_sel:DWORD src1_sel:WORD_1\n\t" \
  "v_rcp_f16_sdwa v226, v226 dst_sel:DWORD dst_unused:UNUSED_PAD src0_sel:WORD_1\n\t" \
  "s_waitcnt lgkmcnt(8)\n\t" \
  "v_smfmac_f32_16x16x64_bf16 v[56:59], v[216:219], v[192:199], v248\n\t" \
  "ds_read_u16_d16_hi v162, v162 offset:0\n\t" \
  "v_smfmac_f32_16x16x64_bf16 v[80:83], v[216:219], v[200:207], v248\n\t" \
  "ds_read_b64_tr_b16 v[216:217], v249 offset:6144\n\t" \
  "ds_read_b64_tr_b16 v[218:219], v249 offset:6400\n\t" \
  "v_fma_mix_f32 v168, v168, v225, 0 op_sel_hi:[0,1,0]\n\t" \
  "v_fma_mix_f32 v170, v170, v227, 0 op_sel_hi:[0,1,0]\n\t" \
  "v_fma_mix_f32 v169, v169, v224, 0 op_sel_hi:[0,1,0]\n\t" \
  "v_fma_mix_f32 v171, v171, v226, 0 op_sel_hi:[0,1,0]\n\t" \
  "s_waitcnt lgkmcnt(8)\n\t" \
  "v_smfmac_f32_16x16x64_bf16 v[60:63], v[220:223], v[192:199], v248\n\t" \
  "v_lshlrev_b32_sdwa v164, 1, v164 dst_sel:DWORD dst_unused:UNUSED_PAD src0_sel:DWORD src1_sel:WORD_1\n\t" \
  "v_cvt_pk_f16_f32 v224, v172, v173\n\t" \
  "v_smfmac_f32_16x16x64_bf16 v[84:87], v[220:223], v[200:207], v248\n\t" \
  "ds_read_b64_tr_b16 v[220:221], v249 offset:8192\n\t" \
  "ds_read_b64_tr_b16 v[222:223], v249 offset:8448\n\t" \
  "ds_read_u16_d16_hi v163, v163 offset:0\n\t" \
  "v_cvt_pk_f16_f32 v226, v174, v175\n\t" \
  "v_pk_fma_f16 v225, v224, v224, %[h1] neg_lo:[1,0,0] neg_hi:[1,0,0]\n\t" \
  "v_pk_fma_f16 v227, v226, v226, %[h1] neg_lo:[1,0,0] neg_hi:[1,0,0]\n\t" \
  "v_pk_mul_f16 v225, v224, v225\n\t" \
  "s_waitcnt lgkmcnt(9)\n\t" \
  "v_smfmac_f32_16x16x64_bf16 v[64:67], v[208:211], v[192:199], v248\n\t" \
  "v_pk_mul_f16 v227, v226, v227\n\t" \
  "v_exp_f16 v224, v225\n\t" \
  "v_smfmac_f32_16x16x64_bf16 v[88:91], v[208:211], v[200:207], v248\n\t" \
  "ds_read_b64_tr_b16 v[208:209], v249 offset:10240\n\t" \
  "ds_read_b64_tr_b16 v[210:211], v249 offset:10496\n\t" \
  "v_lshlrev_b32_sdwa v165, 1, v165 dst_sel:DWORD dst_unused:UNUSED_PAD src0_sel:DWORD src1_sel:WORD_1\n\t" \
  "v_exp_f16 v226, v227\n\t" \
  "s_waitcnt lgkmcnt(8)\n\t" \
  "v_smfmac_f32_16x16x64_bf16 v[68:71], v[212:215], v[192:199], v248\n\t" \
  "ds_read_u16_d16_hi v164, v164 offset:0\n\t" \
  "v_exp_f16_sdwa v224, v225 dst_sel:WORD_1 dst_unused:UNUSED_PRESERVE src0_sel:WORD_1\n\t" \
  "v_smfmac_f32_16x16x64_bf16 v[92:95], v[212:215], v[200:207], v248\n\t" \
  "ds_read_b64_tr_b16 v[212:213], v249 offset:6656\n\t" \
  "ds_read_b64_tr_b16 v[214:215], v249 offset:6912\n\t" \
  "v_exp_f16_sdwa v226, v227 dst_sel:WORD_1 dst_unused:UNUSED_PRESERVE src0_sel:WORD_1\n\t" \
  "v_lshlrev_b32_sdwa v166, 1, v166 dst_sel:DWORD dst_unused:UNUSED_PAD src0_sel:DWORD src1_sel:WORD_1\n\t" \
  "v_pk_fma_f16 v224, v224, %[hc], %[hc]\n\t" \
  "s_waitcnt lgkmcnt(8)\n\t" \
  "v_mfma_f32_16x16x32_bf16 v[192:195], v[216:219], v[120:123], 0\n\t" \
  "ds_read_u16_d16_hi v165, v165 offset:0\n\t" \
  "v_pk_fma_f16 v226, v226, %[hc], %[hc]\n\t" \
  "v_mfma_f32_16x16x32_bf16 v[200:203], v[216:219], v[132:135], 0\n\t" \
  "ds_read_b64_tr_b16 v[216:217], v249 offset:8704\n\t" \
  "ds_read_b64_tr_b16 v[218:219], v249 offset:8960\n\t" \
  "v_rcp_f16 v225, v224\n\t" \
  "v_rcp_f16 v227, v226\n\t" \
  "v_rcp_f16_sdwa v224, v224 dst_sel:DWORD dst_unused:UNUSED_PAD src0_sel:WORD_1\n\t" \
  "s_waitcnt lgkmcnt(9)\n\t" \
  "v_mfma_f32_16x16x32_bf16 v[192:195], v[220:223], v[124:127], v[192:195]\n\t" \
  "v_mfma_f32_16x16x32_bf16 v[200:203], v[220:223], v[136:139], v[200:203]\n\t" \
  "ds_read_b64_tr_b16 v[220:221], v249 offset:10752\n\t" \
  "ds_read_b64_tr_b16 v[222:223], v249 offset:11008\n\t" \
  "v_lshlrev_b32_sdwa v167, 1, v167 dst_sel:DWORD dst_unused:UNUSED_PAD src0_sel:DWORD src1_sel:WORD_1\n\t" \
  "v_rcp_f16_sdwa v226, v226 dst_sel:DWORD dst_unused:UNUSED_PAD src0_sel:WORD_1\n\t" \
  "v_fma_mix_f32 v172, v172, v225, 0 op_sel_hi:[0,1,0]\n\t" \
  "v_fma_mix_f32 v174, v174, v227, 0 op_sel_hi:[0,1,0]\n\t" \
  "s_waitcnt lgkmcnt(8)\n\t" \
  "v_mfma_f32_16x16x32_bf16 v[192:195], v[208:211], v[128:131], v[192:195]\n\t" \
  "ds_read_u16_d16_hi v166, v166 offset:0\n\t" \
  "v_fma_mix_f32 v173, v173, v224, 0 op_sel_hi:[0,1,0]\n\t" \
  "v_mfma_f32_16x16x32_bf16 v[200:203], v[208:211], v[140:143], v[200:203]\n\t" \
  "s_add_u32 %[ob], %[ob], %[dob]\n\t" \
  "s_barrier\n\t" \
  "ds_read_b64_tr_b16 v[208:209], v250 offset:0\n\t" \
  "ds_read_b64_tr_b16 v[210:211], v250 offset:768\n\t" \
  "v_fma_mix_f32 v175, v175, v226, 0 op_sel_hi:[0,1,0]\n\t" \
  "s_waitcnt lgkmcnt(8)\n\t" \
  "v_mfma_f32_16x16x32_bf16 v[196:199], v[212:215], v[120:123], 0\n\t" \
  "ds_read_u16_d16_hi v167, v167 offset:0\n\t" \
  "v_lshlrev_b32_sdwa v192, 1, v192 dst_sel:DWORD dst_unused:UNUSED_PAD src0_sel:DWORD src1_sel:WORD_1\n\t" \
  "v_cvt_pk_f16_f32 v224, v200, v201\n\t" \
  "v_mfma_f32_16x16x32_bf16 v[204:207], v[212:215], v[132:135], 0\n\t" \
  "ds_read_b64_tr_b16 v[212:213], v250 offset:8\n\t" \
  "ds_read_b64_tr_b16 v[214:215], v250 offset:776\n\t" \
  "v_cvt_pk_f16_f32 v226, v202, v203\n\t" \
  "v_pk_fma_f16 v225, v224, v224, %[h1] neg_lo:[1,0,0] neg_hi:[1,0,0]\n\t" \
  "v_pk_fma_f16 v227, v226, v226, %[h1] neg_lo:[1,0,0] neg_hi:[1,0,0]\n\t" \
  "v_pk_mul_f16 v225, v224, v225\n\t" \
  "s_waitcnt lgkmcnt(8)\n\t" \
  "v_mfma_f32_16x16x32_bf16 v[196:199], v[216:219], v[124:127], v[196:199]\n\t" \
  "v_pk_mul_f16 v227, v226, v227\n\t" \
  "v_mfma_f32_16x16x32_bf16 v[204:207], v[216:219], v[136:139], v[204:207]\n\t" \
  "ds_read_b64_tr_b16 v[216:217], v250 offset:64\n\t" \
  "ds_read_b64_tr_b16 v[218:219], v250 offset:832\n\t" \
  "v_exp_f16 v224, v225\n\t" \
  "v_lshlrev_b32_sdwa v193, 1, v193 dst_sel:DWORD dst_unused:UNUSED_PAD src0_sel:DWORD src1_sel:WORD_1\n\t" \
  "v_exp_f16 v226, v227\n\t" \
  "s_waitcnt lgkmcnt(8)\n\t" \
  "v_mfma_f32_16x16x32_bf16 v[196:199], v[220:223], v[128:131], v[196:199]\n\t" \
  "ds_read_u16_d16_hi v192, v192 offset:0\n\t" \
  "v_mfma_f32_16x16x32_bf16 v[204:207], v[220:223], v[140:143], v[204:207]\n\t" \
  "ds_read_b64_tr_b16 v[220:221], v250 offset:72\n\t" \
  "ds_read_b64_tr_b16 v[222:223], v250 offset:840\n\t" \
  "v_exp_f16_sdwa v224, v225 dst_sel:WORD_1 dst_unused:UNUSED_PRESERVE src0_sel:WORD_1\n\t" \
  "v_exp_f16_sdwa v226, v227 dst_sel:WORD_1 dst_unused:UNUSED_PRESERVE src0_sel:WORD_1\n\t" \
  "s_waitcnt lgkmcnt(7)\n\t" \
  "v_smfmac_f32_16x16x64_bf16 v[48:51], v[208:211], v[160:167], v248\n\t" \
  "v_lshlrev_b32_sdwa v194, 1, v194 dst_sel:DWORD dst_unused:UNUSED_PAD src0_sel:DWORD src1_sel:WORD_1\n\t" \
  "v_pk_fma_f16 v224, v224, %[hc], %[hc]\n\t" \
  "v_smfmac_f32_16x16x64_bf16 v[72:75], v[208:211], v[168:175], v248\n\t" \
  "ds_read_b64_tr_b16 v[208:209], v250 offset:128\n\t" \
  "ds_read_b64_tr_b16 v[210:211], v250 offset:896\n\t" \
  "ds_read_u16_d16_hi v193, v193 offset:0\n\t" \
  "v_pk_fma_f16 v226, v226, %[hc], %[hc]\n\t" \
  "v_rcp_f16 v225, v224\n\t" \
  "v_rcp_f16 v227, v226\n\t" \
  "s_waitcnt lgkmcnt(8)\n\t" \
  "v_smfmac_f32_16x16x64_bf16 v[52:55], v[212:215], v[160:167], v248\n\t" \
  "v_smfmac_f32_16x16x64_bf16 v[76:79], v[212:215], v[168:175], v248\n\t" \
  "ds_read_b64_tr_b16 v[212:213], v250 offset:136\n\t" \
  "ds_read_b64_tr_b16 v[214:215], v250 offset:904\n\t" \
  "v_rcp_f16_sdwa v224, v224 dst_sel:DWORD dst_unused:UNUSED_PAD src0_sel:WORD_1\n\t" \
  "v_lshlrev_b32_sdwa v195, 1, v195 dst_sel:DWORD dst_unused:UNUSED_PAD src0_sel:DWORD src1_sel:WORD_1\n\t" \
  "v_rcp_f16_sdwa v226, v226 dst_sel:DWORD dst_unused:UNUSED_PAD src0_sel:WORD_1\n\t" \
  "s_waitcnt lgkmcnt(8)\n\t" \
  "v_smfmac_f32_16x16x64_bf16 v[56:59], v[216:219], v[160:167], v248\n\t" \
  "ds_read_u16_d16_hi v194, v194 offset:0\n\t" \
  "v_smfmac_f32_16x16x64_bf16 v[80:83], v[216:219], v[168:175], v248\n\t" \
  "ds_read_b64_tr_b16 v[216:217], v249 offset:12288\n\t" \
  "ds_read_b64_tr_b16 v[218:219], v249 offset:12544\n\t" \
  "v_fma_mix_f32 v200, v200, v225, 0 op_sel_hi:[0,1,0]\n\t" \
  "v_fma_mix_f32 v202, v202, v227, 0 op_sel_hi:[0,1,0]\n\t" \
  "v_fma_mix_f32 v201, v201, v224, 0 op_sel_hi:[0,1,0]\n\t" \
  "v_fma_mix_f32 v203, v203, v226, 0 op_sel_hi:[0,1,0]\n\t" \
  "s_waitcnt lgkmcnt(8)\n\t" \
  "v_smfmac_f32_16x16x64_bf16 v[60:63], v[220:223], v[160:167], v248\n\t" \
  "v_lshlrev_b32_sdwa v196, 1, v196 dst_sel:DWORD dst_unused:UNUSED_PAD src0_sel:DWORD src1_sel:WORD_1\n\t" \
  "v_cvt_pk_f16_f32 v224, v204, v205\n\t" \
  "v_smfmac_f32_16x16x64_bf16 v[84:87], v[220:223], v[168:175], v248\n\t" \
  "ds_read_b64_tr_b16 v[220:221], v249 offset:14336\n\t" \
  "ds_read_b64_tr_b16 v[222:223], v249 offset:14592\n\t" \
  "ds_read_u16_d16_hi v195, v195 offset:0\n\t" \
  "v_cvt_pk_f16_f32 v226, v206, v207\n\t" \
  "v_pk_fma_f16 v225, v224, v224, %[h1] neg_lo:[1,0,0] neg_hi:[1,0,0]\n\t" \
  "v_pk_fma_f16 v227, v226, v226, %[h1] neg_lo:[1,0,0] neg_hi:[1,0,0]\n\t" \
  "v_pk_mul_f16 v225, v224, v225\n\t" \
  "s_waitcnt lgkmcnt(9)\n\t" \
  "v_smfmac_f32_16x16x64_bf16 v[64:67], v[208:211], v[160:167], v248\n\t" \
  "v_pk_mul_f16 v227, v226, v227\n\t" \
  "v_exp_f16 v224, v225\n\t" \
  "v_smfmac_f32_16x16x64_bf16 v[88:91], v[208:211], v[168:175], v248\n\t" \
  "ds_read_b64_tr_b16 v[208:209], v249 offset:16384\n\t" \
  "ds_read_b64_tr_b16 v[210:211], v249 offset:16640\n\t" \
  "v_lshlrev_b32_sdwa v197, 1, v197 dst_sel:DWORD dst_unused:UNUSED_PAD src0_sel:DWORD src1_sel:WORD_1\n\t" \
  "v_exp_f16 v226, v227\n\t" \
  "s_waitcnt lgkmcnt(8)\n\t" \
  "v_smfmac_f32_16x16x64_bf16 v[68:71], v[212:215], v[160:167], v248\n\t" \
  "ds_read_u16_d16_hi v196, v196 offset:0\n\t" \
  "v_exp_f16_sdwa v224, v225 dst_sel:WORD_1 dst_unused:UNUSED_PRESERVE src0_sel:WORD_1\n\t" \
  "v_smfmac_f32_16x16x64_bf16 v[92:95], v[212:215], v[168:175], v248\n\t" \
  "ds_read_b64_tr_b16 v[212:213], v249 offset:12800\n\t" \
  "ds_read_b64_tr_b16 v[214:215], v249 offset:13056\n\t" \
  "v_exp_f16_sdwa v226, v227 dst_sel:WORD_1 dst_unused:UNUSED_PRESERVE src0_sel:WORD_1\n\t" \
  "v_lshlrev_b32_sdwa v198, 1, v198 dst_sel:DWORD dst_unused:UNUSED_PAD src0_sel:DWORD src1_sel:WORD_1\n\t" \
  "v_pk_fma_f16 v224, v224, %[hc], %[hc]\n\t" \
  "v_pk_fma_f16 v226, v226, %[hc], %[hc]\n\t" \
  "s_waitcnt lgkmcnt(8)\n\t" \
  "v_mfma_f32_16x16x32_bf16 v[168:171], v[216:219], v[132:135], 0\n\t" \
  "ds_read_b64_tr_b16 v[216:217], v249 offset:14848\n\t" \
  "ds_read_b64_tr_b16 v[218:219], v249 offset:15104\n\t" \
  "ds_read_u16_d16_hi v197, v197 offset:0\n\t" \
  "v_rcp_f16 v225, v224\n\t" \
  "v_rcp_f16 v227, v226\n\t" \
  "v_rcp_f16_sdwa v224, v224 dst_sel:DWORD dst_unused:UNUSED_PAD src0_sel:WORD_1\n\t" \
  "s_waitcnt lgkmcnt(9)\n\t" \
  "v_mfma_f32_16x16x32_bf16 v[168:171], v[220:223], v[136:139], v[168:171]\n\t" \
  "ds_read_b64_tr_b16 v[220:221], v249 offset:16896\n\t" \
  "ds_read_b64_tr_b16 v[222:223], v249 offset:17152\n\t" \
  "v_lshlrev_b32_sdwa v199, 1, v199 dst_sel:DWORD dst_unused:UNUSED_PAD src0_sel:DWORD src1_sel:WORD_1\n\t" \
  "v_rcp_f16_sdwa v226, v226 dst_sel:DWORD dst_unused:UNUSED_PAD src0_sel:WORD_1\n\t" \
  "v_fma_mix_f32 v204, v204, v225, 0 op_sel_hi:[0,1,0]\n\t" \
  "v_fma_mix_f32 v206, v206, v227, 0 op_sel_hi:[0,1,0]\n\t" \
  "v_fma_mix_f32 v205, v205, v224, 0 op_sel_hi:[0,1,0]\n\t" \
  "s_waitcnt lgkmcnt(8)\n\t" \
  "v_mfma_f32_16x16x32_bf16 v[168:171], v[208:211], v[140:143], v[168:171]\n\t" \
  "s_barrier\n\t" \
  "ds_read_b64_tr_b16 v[208:209], v250 offset:6160\n\t" \
  "ds_read_b64_tr_b16 v[210:211], v250 offset:6928\n\t" \
  "ds_read_u16_d16_hi v198, v198 offset:0\n\t" \
  "v_fma_mix_f32 v207, v207, v226, 0 op_sel_hi:[0,1,0]\n\t" \
  "s_nop 2\n\t" \
  "v_cvt_pk_f16_f32 v224, v168, v169\n\t" \
  "v_cvt_pk_f16_f32 v226, v170, v171\n\t" \
  "s_waitcnt lgkmcnt(8)\n\t" \
  "v_mfma_f32_16x16x32_bf16 v[172:175], v[212:215], v[132:135], 0\n\t" \
  "ds_read_b64_tr_b16 v[212:213], v250 offset:6168\n\t" \
  "ds_read_b64_tr_b16 v[214:215], v250 offset:6936\n\t" \
  "ds_read_u16_d16_hi v199, v199 offset:0\n\t" \
  "v_pk_fma_f16 v225, v224, v224, %[h1] neg_lo:[1,0,0] neg_hi:[1,0,0]\n\t" \
  "v_pk_fma_f16 v227, v226, v226, %[h1] neg_lo:[1,0,0] neg_hi:[1,0,0]\n\t" \
  "v_pk_mul_f16 v225, v224, v225\n\t" \
  "v_pk_mul_f16 v227, v226, v227\n\t" \
  "s_waitcnt lgkmcnt(9)\n\t" \
  "v_mfma_f32_16x16x32_bf16 v[172:175], v[216:219], v[136:139], v[172:175]\n\t" \
  "ds_read_b64_tr_b16 v[216:217], v250 offset:6224\n\t" \
  "ds_read_b64_tr_b16 v[218:219], v250 offset:6992\n\t" \
  "v_exp_f16 v224, v225\n\t" \
  "v_exp_f16 v226, v227\n\t" \
  "v_exp_f16_sdwa v224, v225 dst_sel:WORD_1 dst_unused:UNUSED_PRESERVE src0_sel:WORD_1\n\t" \
  "s_waitcnt lgkmcnt(8)\n\t" \
  "v_mfma_f32_16x16x32_bf16 v[172:175], v[220:223], v[140:143], v[172:175]\n\t" \
  "ds_read_b64_tr_b16 v[220:221], v250 offset:6232\n\t" \
  "ds_read_b64_tr_b16 v[222:223], v250 offset:7000\n\t" \
  "v_exp_f16_sdwa v226, v227 dst_sel:WORD_1 dst_unused:UNUSED_PRESERVE src0_sel:WORD_1\n\t" \
  "s_waitcnt lgkmcnt(4)\n\t" \
  "v_smfmac_f32_16x16x64_bf16 v[48:51], v[208:211], v[192:199], v248\n\t" \
  "v_pk_fma_f16 v224, v224, %[hc], %[hc]\n\t" \
  "v_pk_fma_f16 v226, v226, %[hc], %[hc]\n\t" \
  "v_smfmac_f32_16x16x64_bf16 v[72:75], v[208:211], v[200:207], v248\n\t" \
  "ds_read_b64_tr_b16 v[208:209], v250 offset:6288\n\t" \
  "ds_read_b64_tr_b16 v[210:211], v250 offset:7056\n\t" \
  "v_rcp_f16 v225, v224\n\t" \
  "v_rcp_f16 v227, v226\n\t" \
  "v_smfmac_f32_16x16x64_bf16 v[52:55], v[212:215], v[192:199], v248\n\t" \
  "v_smfmac_f32_16x16x64_bf16 v[76:79], v[212:215], v[200:207], v248\n\t" \
  "ds_read_b64_tr_b16 v[212:213], v250 offset:6296\n\t" \
  "ds_read_b64_tr_b16 v[214:215], v250 offset:7064\n\t" \
  "v_rcp_f16_sdwa v224, v224 dst_sel:DWORD dst_unused:UNUSED_PAD src0_sel:WORD_1\n\t" \
  "v_rcp_f16_sdwa v226, v226 dst_sel:DWORD dst_unused:UNUSED_PAD src0_sel:WORD_1\n\t" \
  "s_waitcnt lgkmcnt(6)\n\t" \
  "v_smfmac_f32_16x16x64_bf16 v[56:59], v[216:219], v[192:199], v248\n\t" \
  "v_fma_mix_f32 v168, v168, v225, 0 op_sel_hi:[0,1,0]\n\t" \
  "v_cvt_pk_bf16_f32 v230, v48, v49\n\t" \
  "v_smfmac_f32_16x16x64_bf16 v[80:83], v[216:219], v[200:207], v248\n\t" \
  "ds_read_b64_tr_b16 v[216:217], v249 offset:18432\n\t" \
  "ds_read_b64_tr_b16 v[218:219], v249 offset:18688\n\t" \
  "v_fma_mix_f32 v170, v170, v227, 0 op_sel_hi:[0,1,0]\n\t" \
  "v_cvt_pk_bf16_f32 v231, v50, v51\n\t" \
  "v_fma_mix_f32 v169, v169, v224, 0 op_sel_hi:[0,1,0]\n\t" \
  "v_cvt_pk_bf16_f32 v232, v52, v53\n\t" \
  "v_fma_mix_f32 v171, v171, v226, 0 op_sel_hi:[0,1,0]\n\t" \
  "v_cvt_pk_bf16_f32 v233, v54, v55\n\t" \
  "s_waitcnt lgkmcnt(6)\n\t" \
  "v_smfmac_f32_16x16x64_bf16 v[60:63], v[220:223], v[192:199], v248\n\t" \
  "v_cvt_pk_f16_f32 v224, v172, v173\n\t" \
  "v_cvt_pk_f16_f32 v226, v174, v175\n\t" \
  "v_smfmac_f32_16x16x64_bf16 v[84:87], v[220:223], v[200:207], v248\n\t" \
  "ds_read_b64_tr_b16 v[220:221], v249 offset:20480\n\t" \
  "ds_read_b64_tr_b16 v[222:223], v249 offset:20736\n\t" \
  "buffer_store_dwordx4 v[230:233], v253, %[osrd], %[ob] offen offset:0 sc1\n\t" \
  "v_pk_fma_f16 v225, v224, v224, %[h1] neg_lo:[1,0,0] neg_hi:[1,0,0]\n\t" \
  "v_pk_fma_f16 v227, v226, v226, %[h1] neg_lo:[1,0,0] neg_hi:[1,0,0]\n\t" \
  "v_pk_mul_f16 v225, v224, v225\n\t" \
  "v_pk_mul_f16 v227, v226, v227\n\t" \
  "s_waitcnt lgkmcnt(6)\n\t" \
  "v_smfmac_f32_16x16x64_bf16 v[64:67], v[208:211], v[192:199], v248\n\t" \
  "v_exp_f16 v224, v225\n\t" \
  "v_cvt_pk_bf16_f32 v234, v56, v57\n\t" \
  "v_smfmac_f32_16x16x64_bf16 v[88:91], v[208:211], v[200:207], v248\n\t" \
  "ds_read_b64_tr_b16 v[208:209], v249 offset:22528\n\t" \
  "ds_read_b64_tr_b16 v[210:211], v249 offset:22784\n\t" \
  "v_cvt_pk_bf16_f32 v235, v58, v59\n\t" \
  "v_exp_f16 v226, v227\n\t" \
  "v_cvt_pk_bf16_f32 v236, v60, v61\n\t" \
  "v_exp_f16_sdwa v224, v225 dst_sel:WORD_1 dst_unused:UNUSED_PRESERVE src0_sel:WORD_1\n\t" \
  "v_cvt_pk_bf16_f32 v237, v62, v63\n\t" \
  "s_waitcnt lgkmcnt(6)\n\t" \
  "v_smfmac_f32_16x16x64_bf16 v[68:71], v[212:215], v[192:199], v248\n\t" \
  "v_smfmac_f32_16x16x64_bf16 v[92:95], v[212:215], v[200:207], v248\n\t" \
  "ds_read_b64_tr_b16 v[212:213], v249 offset:18944\n\t" \
  "ds_read_b64_tr_b16 v[214:215], v249 offset:19200\n\t" \
  "buffer_store_dwordx4 v[234:237], v253, %[osrd], %[ob] offen offset:64 sc1\n\t" \
  "v_exp_f16_sdwa v226, v227 dst_sel:WORD_1 dst_unused:UNUSED_PRESERVE src0_sel:WORD_1\n\t" \
  "v_pk_fma_f16 v224, v224, %[hc], %[hc]\n\t" \
  "v_pk_fma_f16 v226, v226, %[hc], %[hc]\n\t" \
  "v_rcp_f16 v225, v224\n\t" \
  "v_cvt_pk_bf16_f32 v238, v64, v65\n\t" \
  "s_waitcnt lgkmcnt(6)\n\t" \
  "v_mfma_f32_16x16x32_bf16 v[200:203], v[216:219], v[132:135], 0\n\t" \
  "ds_read_b64_tr_b16 v[216:217], v249 offset:20992\n\t" \
  "ds_read_b64_tr_b16 v[218:219], v249 offset:21248\n\t" \
  "v_cvt_pk_bf16_f32 v239, v66, v67\n\t" \
  "v_rcp_f16 v227, v226\n\t" \
  "v_cvt_pk_bf16_f32 v240, v68, v69\n\t" \
  "v_rcp_f16_sdwa v224, v224 dst_sel:DWORD dst_unused:UNUSED_PAD src0_sel:WORD_1\n\t" \
  "v_cvt_pk_bf16_f32 v241, v70, v71\n\t" \
  "s_waitcnt lgkmcnt(6)\n\t" \
  "v_mfma_f32_16x16x32_bf16 v[200:203], v[220:223], v[136:139], v[200:203]\n\t" \
  "ds_read_b64_tr_b16 v[220:221], v249 offset:23040\n\t" \
  "ds_read_b64_tr_b16 v[222:223], v249 offset:23296\n\t" \
  "buffer_store_dwordx4 v[238:241], v253, %[osrd], %[ob] offen offset:128 sc1\n\t" \
  "v_rcp_f16_sdwa v226, v226 dst_sel:DWORD dst_unused:UNUSED_PAD src0_sel:WORD_1\n\t" \
  "v_fma_mix_f32 v172, v172, v225, 0 op_sel_hi:[0,1,0]\n\t" \
  "v_fma_mix_f32 v174, v174, v227, 0 op_sel_hi:[0,1,0]\n\t" \
  "v_fma_mix_f32 v173, v173, v224, 0 op_sel_hi:[0,1,0]\n\t" \
  "s_waitcnt lgkmcnt(6)\n\t" \
  "v_mfma_f32_16x16x32_bf16 v[200:203], v[208:211], v[140:143], v[200:203]\n\t" \
  "s_barrier\n\t" \
  "ds_read_b64_tr_b16 v[208:209], v250 offset:12320\n\t" \
  "ds_read_b64_tr_b16 v[210:211], v250 offset:13088\n\t" \
  "v_fma_mix_f32 v175, v175, v226, 0 op_sel_hi:[0,1,0]\n\t" \
  "s_nop 3\n\t" \
  "v_cvt_pk_f16_f32 v224, v200, v201\n\t" \
  "v_cvt_pk_f16_f32 v226, v202, v203\n\t" \
  "s_waitcnt lgkmcnt(6)\n\t" \
  "v_mfma_f32_16x16x32_bf16 v[204:207], v[212:215], v[132:135], 0\n\t" \
  "ds_read_b64_tr_b16 v[212:213], v250 offset:12328\n\t" \
  "ds_read_b64_tr_b16 v[214:215], v250 offset:13096\n\t" \
  "v_pk_fma_f16 v225, v224, v224, %[h1] neg_lo:[1,0,0] neg_hi:[1,0,0]\n\t" \
  "v_pk_fma_f16 v227, v226, v226, %[h1] neg_lo:[1,0,0] neg_hi:[1,0,0]\n\t" \
  "v_pk_mul_f16 v225, v224, v225\n\t" \
  "v_pk_mul_f16 v227, v226, v227\n\t" \
  "s_waitcnt lgkmcnt(6)\n\t" \
  "v_mfma_f32_16x16x32_bf16 v[204:207], v[216:219], v[136:139], v[204:207]\n\t" \
  "ds_read_b64_tr_b16 v[216:217], v250 offset:12384\n\t" \
  "ds_read_b64_tr_b16 v[218:219], v250 offset:13152\n\t" \
  "v_exp_f16 v224, v225\n\t" \
  "v_exp_f16 v226, v227\n\t" \
  "v_exp_f16_sdwa v224, v225 dst_sel:WORD_1 dst_unused:UNUSED_PRESERVE src0_sel:WORD_1\n\t" \
  "s_waitcnt lgkmcnt(6)\n\t" \
  "v_mfma_f32_16x16x32_bf16 v[204:207], v[220:223], v[140:143], v[204:207]\n\t" \
  "ds_read_b64_tr_b16 v[220:221], v250 offset:12392\n\t" \
  "ds_read_b64_tr_b16 v[222:223], v250 offset:13160\n\t" \
  "v_exp_f16_sdwa v226, v227 dst_sel:WORD_1 dst_unused:UNUSED_PRESERVE src0_sel:WORD_1\n\t" \
  "v_pk_fma_f16 v224, v224, %[hc], %[hc]\n\t" \
  "v_pk_fma_f16 v226, v226, %[hc], %[hc]\n\t" \
  "s_waitcnt lgkmcnt(6)\n\t" \
  "v_smfmac_f32_16x16x64_bf16 v[72:75], v[208:211], v[168:175], v248\n\t" \
  "ds_read_b64_tr_b16 v[208:209], v250 offset:12448\n\t" \
  "ds_read_b64_tr_b16 v[210:211], v250 offset:13216\n\t" \
  "v_rcp_f16 v225, v224\n\t" \
  "v_rcp_f16 v227, v226\n\t" \
  "s_waitcnt lgkmcnt(6)\n\t" \
  "v_smfmac_f32_16x16x64_bf16 v[76:79], v[212:215], v[168:175], v248\n\t" \
  "ds_read_b64_tr_b16 v[212:213], v250 offset:12456\n\t" \
  "ds_read_b64_tr_b16 v[214:215], v250 offset:13224\n\t" \
  "v_rcp_f16_sdwa v224, v224 dst_sel:DWORD dst_unused:UNUSED_PAD src0_sel:WORD_1\n\t" \
  "v_rcp_f16_sdwa v226, v226 dst_sel:DWORD dst_unused:UNUSED_PAD src0_sel:WORD_1\n\t" \
  "v_fma_mix_f32 v200, v200, v225, 0 op_sel_hi:[0,1,0]\n\t" \
  "s_waitcnt lgkmcnt(6)\n\t" \
  "v_smfmac_f32_16x16x64_bf16 v[80:83], v[216:219], v[168:175], v248\n\t" \
  "ds_read_b64_tr_b16 v[216:217], v249 offset:0\n\t" \
  "ds_read_b64_tr_b16 v[218:219], v249 offset:256\n\t" \
  "v_fma_mix_f32 v202, v202, v227, 0 op_sel_hi:[0,1,0]\n\t" \
  "v_fma_mix_f32 v201, v201, v224, 0 op_sel_hi:[0,1,0]\n\t" \
  "v_fma_mix_f32 v203, v203, v226, 0 op_sel_hi:[0,1,0]\n\t" \
  "v_cvt_pk_f16_f32 v224, v204, v205\n\t" \
  "v_cvt_pk_f16_f32 v226, v206, v207\n\t" \
  "s_waitcnt lgkmcnt(6)\n\t" \
  "v_smfmac_f32_16x16x64_bf16 v[84:87], v[220:223], v[168:175], v248\n\t" \
  "ds_read_b64_tr_b16 v[220:221], v249 offset:2048\n\t" \
  "ds_read_b64_tr_b16 v[222:223], v249 offset:2304\n\t" \
  "v_pk_fma_f16 v225, v224, v224, %[h1] neg_lo:[1,0,0] neg_hi:[1,0,0]\n\t" \
  "v_pk_fma_f16 v227, v226, v226, %[h1] neg_lo:[1,0,0] neg_hi:[1,0,0]\n\t" \
  "v_pk_mul_f16 v225, v224, v225\n\t" \
  "v_pk_mul_f16 v227, v226, v227\n\t" \
  "v_exp_f16 v224, v225\n\t" \
  "s_waitcnt lgkmcnt(6)\n\t" \
  "v_smfmac_f32_16x16x64_bf16 v[88:91], v[208:211], v[168:175], v248\n\t" \
  "ds_read_b64_tr_b16 v[208:209], v249 offset:4096\n\t" \
  "ds_read_b64_tr_b16 v[210:211], v249 offset:4352\n\t" \
  "v_exp_f16 v226, v227\n\t" \
  "v_exp_f16_sdwa v224, v225 dst_sel:WORD_1 dst_unused:UNUSED_PRESERVE src0_sel:WORD_1\n\t" \
  "s_waitcnt lgkmcnt(6)\n\t" \
  "v_smfmac_f32_16x16x64_bf16 v[92:95], v[212:215], v[168:175], v248\n\t" \
  "ds_read_b64_tr_b16 v[212:213], v249 offset:512\n\t" \
  "ds_read_b64_tr_b16 v[214:215], v249 offset:768\n\t" \
  "v_exp_f16_sdwa v226, v227 dst_sel:WORD_1 dst_unused:UNUSED_PRESERVE src0_sel:WORD_1\n\t" \
  "v_pk_fma_f16 v224, v224, %[hc], %[hc]\n\t" \
  "v_pk_fma_f16 v226, v226, %[hc], %[hc]\n\t" \
  "v_rcp_f16 v225, v224\n\t" \
  "s_waitcnt lgkmcnt(6)\n\t" \
  "v_mfma_f32_16x16x32_bf16 v[168:171], v[216:219], v[132:135], 0\n\t" \
  "ds_read_b64_tr_b16 v[216:217], v249 offset:2560\n\t" \
  "ds_read_b64_tr_b16 v[218:219], v249 offset:2816\n\t" \
  "v_rcp_f16 v227, v226\n\t" \
  "v_rcp_f16_sdwa v224, v224 dst_sel:DWORD dst_unused:UNUSED_PAD src0_sel:WORD_1\n\t" \
  "s_waitcnt lgkmcnt(6)\n\t" \
  "v_mfma_f32_16x16x32_bf16 v[168:171], v[220:223], v[136:139], v[168:171]\n\t" \
  "ds_read_b64_tr_b16 v[220:221], v249 offset:4608\n\t" \
  "ds_read_b64_tr_b16 v[222:223], v249 offset:4864\n\t" \
  "v_rcp_f16_sdwa v226, v226 dst_sel:DWORD dst_unused:UNUSED_PAD src0_sel:WORD_1\n\t" \
  "v_fma_mix_f32 v204, v204, v225, 0 op_sel_hi:[0,1,0]\n\t" \
  "v_fma_mix_f32 v206, v206, v227, 0 op_sel_hi:[0,1,0]\n\t" \
  "v_fma_mix_f32 v205, v205, v224, 0 op_sel_hi:[0,1,0]\n\t" \
  "s_waitcnt lgkmcnt(6)\n\t" \
  "v_mfma_f32_16x16x32_bf16 v[168:171], v[208:211], v[140:143], v[168:171]\n\t" \
  "s_add_u32 %[ob], %[ob], %[dob]\n\t" \
  "s_barrier\n\t" \
  "ds_read_b64_tr_b16 v[208:209], v250 offset:0\n\t" \
  "ds_read_b64_tr_b16 v[210:211], v250 offset:768\n\t" \
  "v_fma_mix_f32 v207, v207, v226, 0 op_sel_hi:[0,1,0]\n\t" \
  "s_nop 2\n\t" \
  "v_cvt_pk_f16_f32 v224, v168, v169\n\t" \
  "v_cvt_pk_f16_f32 v226, v170, v171\n\t" \
  "s_waitcnt lgkmcnt(6)\n\t" \
  "v_mfma_f32_16x16x32_bf16 v[172:175], v[212:215], v[132:135], 0\n\t" \
  "ds_read_b64_tr_b16 v[212:213], v250 offset:8\n\t" \
  "ds_read_b64_tr_b16 v[214:215], v250 offset:776\n\t" \
  "v_pk_fma_f16 v225, v224, v224, %[h1] neg_lo:[1,0,0] neg_hi:[1,0,0]\n\t" \
  "v_pk_fma_f16 v227, v226, v226, %[h1] neg_lo:[1,0,0] neg_hi:[1,0,0]\n\t" \
  "v_pk_mul_f16 v225, v224, v225\n\t" \
  "v_pk_mul_f16 v227, v226, v227\n\t" \
  "s_waitcnt lgkmcnt(6)\n\t" \
  "v_mfma_f32_16x16x32_bf16 v[172:175], v[216:219], v[136:139], v[172:175]\n\t" \
  "ds_read_b64_tr_b16 v[216:217], v250 offset:64\n\t" \
  "ds_read_b64_tr_b16 v[218:219], v250 offset:832\n\t" \
  "v_exp_f16 v224, v225\n\t" \
  "v_exp_f16 v226, v227\n\t" \
  "v_exp_f16_sdwa v224, v225 dst_sel:WORD_1 dst_unused:UNUSED_PRESERVE src0_sel:WORD_1\n\t" \
  "s_waitcnt lgkmcnt(6)\n\t" \
  "v_mfma_f32_16x16x32_bf16 v[172:175], v[220:223], v[140:143], v[172:175]\n\t" \
  "ds_read_b64_tr_b16 v[220:221], v250 offset:72\n\t" \
  "ds_read_b64_tr_b16 v[222:223], v250 offset:840\n\t" \
  "v_exp_f16_sdwa v226, v227 dst_sel:WORD_1 dst_unused:UNUSED_PRESERVE src0_sel:WORD_1\n\t" \
  "v_pk_fma_f16 v224, v224, %[hc], %[hc]\n\t" \
  "v_pk_fma_f16 v226, v226, %[hc], %[hc]\n\t" \
  "s_waitcnt lgkmcnt(6)\n\t" \
  "v_smfmac_f32_16x16x64_bf16 v[72:75], v[208:211], v[200:207], v248\n\t" \
  "ds_read_b64_tr_b16 v[208:209], v250 offset:128\n\t" \
  "ds_read_b64_tr_b16 v[210:211], v250 offset:896\n\t" \
  "v_rcp_f16 v225, v224\n\t" \
  "v_rcp_f16 v227, v226\n\t" \
  "s_waitcnt lgkmcnt(6)\n\t" \
  "v_smfmac_f32_16x16x64_bf16 v[76:79], v[212:215], v[200:207], v248\n\t" \
  "ds_read_b64_tr_b16 v[212:213], v250 offset:136\n\t" \
  "ds_read_b64_tr_b16 v[214:215], v250 offset:904\n\t" \
  "v_rcp_f16_sdwa v224, v224 dst_sel:DWORD dst_unused:UNUSED_PAD src0_sel:WORD_1\n\t" \
  "v_rcp_f16_sdwa v226, v226 dst_sel:DWORD dst_unused:UNUSED_PAD src0_sel:WORD_1\n\t" \
  "v_fma_mix_f32 v168, v168, v225, 0 op_sel_hi:[0,1,0]\n\t" \
  "s_waitcnt lgkmcnt(6)\n\t" \
  "v_smfmac_f32_16x16x64_bf16 v[80:83], v[216:219], v[200:207], v248\n\t" \
  "s_waitcnt lgkmcnt(0)\n\t" \
  "s_barrier\n\t" \
  "ds_read_b64_tr_b16 v[216:217], v250 offset:6160\n\t" \
  "ds_read_b64_tr_b16 v[218:219], v250 offset:6928\n\t" \
  "v_fma_mix_f32 v170, v170, v227, 0 op_sel_hi:[0,1,0]\n\t" \
  "v_fma_mix_f32 v169, v169, v224, 0 op_sel_hi:[0,1,0]\n\t" \
  "v_fma_mix_f32 v171, v171, v226, 0 op_sel_hi:[0,1,0]\n\t" \
  "v_cvt_pk_f16_f32 v224, v172, v173\n\t" \
  "v_cvt_pk_f16_f32 v226, v174, v175\n\t" \
  "v_smfmac_f32_16x16x64_bf16 v[84:87], v[220:223], v[200:207], v248\n\t" \
  "ds_read_b64_tr_b16 v[220:221], v250 offset:6168\n\t" \
  "ds_read_b64_tr_b16 v[222:223], v250 offset:6936\n\t" \
  "v_pk_fma_f16 v225, v224, v224, %[h1] neg_lo:[1,0,0] neg_hi:[1,0,0]\n\t" \
  "v_pk_fma_f16 v227, v226, v226, %[h1] neg_lo:[1,0,0] neg_hi:[1,0,0]\n\t" \
  "v_pk_mul_f16 v225, v224, v225\n\t" \
  "v_pk_mul_f16 v227, v226, v227\n\t" \
  "v_exp_f16 v224, v225\n\t" \
  "v_smfmac_f32_16x16x64_bf16 v[88:91], v[208:211], v[200:207], v248\n\t" \
  "ds_read_b64_tr_b16 v[208:209], v250 offset:6224\n\t" \
  "ds_read_b64_tr_b16 v[210:211], v250 offset:6992\n\t" \
  "v_exp_f16 v226, v227\n\t" \
  "v_exp_f16_sdwa v224, v225 dst_sel:WORD_1 dst_unused:UNUSED_PRESERVE src0_sel:WORD_1\n\t" \
  "v_smfmac_f32_16x16x64_bf16 v[92:95], v[212:215], v[200:207], v248\n\t" \
  "ds_read_b64_tr_b16 v[212:213], v250 offset:6232\n\t" \
  "ds_read_b64_tr_b16 v[214:215], v250 offset:7000\n\t" \
  "v_exp_f16_sdwa v226, v227 dst_sel:WORD_1 dst_unused:UNUSED_PRESERVE src0_sel:WORD_1\n\t" \
  "v_pk_fma_f16 v224, v224, %[hc], %[hc]\n\t" \
  "v_pk_fma_f16 v226, v226, %[hc], %[hc]\n\t" \
  "v_rcp_f16 v225, v224\n\t" \
  "v_rcp_f16 v227, v226\n\t" \
  "v_rcp_f16_sdwa v224, v224 dst_sel:DWORD dst_unused:UNUSED_PAD src0_sel:WORD_1\n\t" \
  "v_rcp_f16_sdwa v226, v226 dst_sel:DWORD dst_unused:UNUSED_PAD src0_sel:WORD_1\n\t" \
  "v_fma_mix_f32 v172, v172, v225, 0 op_sel_hi:[0,1,0]\n\t" \
  "v_fma_mix_f32 v174, v174, v227, 0 op_sel_hi:[0,1,0]\n\t" \
  "v_fma_mix_f32 v173, v173, v224, 0 op_sel_hi:[0,1,0]\n\t" \
  "v_fma_mix_f32 v175, v175, v226, 0 op_sel_hi:[0,1,0]\n\t" \
  "s_waitcnt lgkmcnt(6)\n\t" \
  "s_nop 0\n\t" \
  "v_smfmac_f32_16x16x64_bf16 v[72:75], v[216:219], v[168:175], v248\n\t" \
  "ds_read_b64_tr_b16 v[216:217], v250 offset:6288\n\t" \
  "ds_read_b64_tr_b16 v[218:219], v250 offset:7056\n\t" \
  "s_waitcnt lgkmcnt(6)\n\t" \
  "v_smfmac_f32_16x16x64_bf16 v[76:79], v[220:223], v[168:175], v248\n\t" \
  "ds_read_b64_tr_b16 v[220:221], v250 offset:6296\n\t" \
  "ds_read_b64_tr_b16 v[222:223], v250 offset:7064\n\t" \
  "s_nop 1\n\t" \
  "v_cvt_pk_bf16_f32 v230, v72, v73\n\t" \
  "s_waitcnt lgkmcnt(6)\n\t" \
  "v_smfmac_f32_16x16x64_bf16 v[80:83], v[208:211], v[168:175], v248\n\t" \
  "v_cvt_pk_bf16_f32 v231, v74, v75\n\t" \
  "v_cvt_pk_bf16_f32 v232, v76, v77\n\t" \
  "v_cvt_pk_bf16_f32 v233, v78, v79\n\t" \
  "s_waitcnt lgkmcnt(4)\n\t" \
  "v_smfmac_f32_16x16x64_bf16 v[84:87], v[212:215], v[168:175], v248\n\t" \
  "buffer_store_dwordx4 v[230:233], v253, %[osrd], %[ob] offen offset:0 sc1\n\t" \
  "s_nop 1\n\t" \
  "v_cvt_pk_bf16_f32 v234, v80, v81\n\t" \
  "s_waitcnt lgkmcnt(2)\n\t" \
  "v_smfmac_f32_16x16x64_bf16 v[88:91], v[216:219], v[168:175], v248\n\t" \
  "v_cvt_pk_bf16_f32 v235, v82, v83\n\t" \
  "s_nop 0\n\t" \
  "v_cvt_pk_bf16_f32 v236, v84, v85\n\t" \
  "v_cvt_pk_bf16_f32 v237, v86, v87\n\t" \
  "s_waitcnt lgkmcnt(0)\n\t" \
  "v_smfmac_f32_16x16x64_bf16 v[92:95], v[220:223], v[168:175], v248\n\t" \
  "buffer_store_dwordx4 v[234:237], v253, %[osrd], %[ob] offen offset:64 sc1\n\t" \
  "s_nop 0\n\t" \
  "v_cvt_pk_bf16_f32 v238, v88, v89\n\t" \
  "v_cvt_pk_bf16_f32 v239, v90, v91\n\t" \
  "s_nop 3\n\t" \
  "v_cvt_pk_bf16_f32 v240, v92, v93\n\t" \
  "v_cvt_pk_bf16_f32 v241, v94, v95\n\t" \
  "buffer_store_dwordx4 v[238:241], v253, %[osrd], %[ob] offen offset:128 sc1\n\t" \
  ""
#define F_ASM \
  "s_setprio 3\n\t" \
  "v_mov_b32 v252, 0\n\t" \
  "v_mov_b32 v253, 0\n\t" \
  "s_nop 0\n\t" \
  "s_nop 0\n\t" \
  "v_mfma_f32_32x32x8_bf16 v[0:15], v[252:253], v[252:253], 0\n\t" \
  "v_mfma_f32_32x32x8_bf16 v[16:31], v[252:253], v[252:253], 0\n\t" \
  "v_mfma_f32_32x32x8_bf16 v[32:47], v[252:253], v[252:253], 0\n\t" \
  "v_mfma_f32_32x32x8_bf16 v[48:63], v[252:253], v[252:253], 0\n\t" \
  "v_mfma_f32_32x32x8_bf16 v[64:79], v[252:253], v[252:253], 0\n\t" \
  "v_mfma_f32_32x32x8_bf16 v[80:95], v[252:253], v[252:253], 0\n\t" \
  "s_barrier\n\t" \
  "ds_read_u16 v96, v251 offset:0\n\t" \
  "ds_read_u16 v97, v251 offset:96\n\t" \
  "ds_read_u16 v98, v251 offset:384\n\t" \
  "ds_read_u16 v99, v251 offset:480\n\t" \
  "ds_read_u16 v112, v251 offset:8\n\t" \
  "ds_read_u16 v113, v251 offset:104\n\t" \
  "ds_read_u16 v114, v251 offset:392\n\t" \
  "ds_read_u16 v115, v251 offset:488\n\t" \
  "ds_read_u16 v128, v251 offset:16\n\t" \
  "ds_read_u16 v129, v251 offset:112\n\t" \
  "ds_read_u16 v130, v251 offset:400\n\t" \
  "ds_read_u16 v131, v251 offset:496\n\t" \
  "ds_read_u16 v144, v251 offset:24\n\t" \
  "ds_read_u16 v145, v251 offset:120\n\t" \
  "ds_read_u16 v146, v251 offset:408\n\t" \
  "ds_read_u16 v147, v251 offset:504\n\t" \
  "ds_read_u16 v160, v251 offset:32\n\t" \
  "ds_read_u16 v161, v251 offset:128\n\t" \
  "ds_read_u16 v162, v251 offset:416\n\t" \
  "ds_read_u16 v163, v251 offset:512\n\t" \
  "ds_read_u16 v176, v251 offset:40\n\t" \
  "ds_read_u16 v177, v251 offset:136\n\t" \
  "ds_read_u16 v178, v251 offset:424\n\t" \
  "ds_read_u16 v179, v251 offset:520\n\t" \
  "ds_read_u16 v100, v251 offset:768\n\t" \
  "ds_read_u16 v101, v251 offset:864\n\t" \
  "ds_read_u16 v102, v251 offset:1152\n\t" \
  "ds_read_u16 v103, v251 offset:1248\n\t" \
  "ds_read_u16 v116, v251 offset:776\n\t" \
  "ds_read_u16 v117, v251 offset:872\n\t" \
  "ds_read_u16 v118, v251 offset:1160\n\t" \
  "ds_read_u16 v119, v251 offset:1256\n\t" \
  "ds_read_u16 v132, v251 offset:784\n\t" \
  "ds_read_u16 v133, v251 offset:880\n\t" \
  "ds_read_u16 v134, v251 offset:1168\n\t" \
  "ds_read_u16 v135, v251 offset:1264\n\t" \
  "ds_read_u16 v148, v251 offset:792\n\t" \
  "ds_read_u16 v149, v251 offset:888\n\t" \
  "ds_read_u16 v150, v251 offset:1176\n\t" \
  "ds_read_u16 v151, v251 offset:1272\n\t" \
  "ds_read_u16 v164, v251 offset:800\n\t" \
  "ds_read_u16 v165, v251 offset:896\n\t" \
  "ds_read_u16 v166, v251 offset:1184\n\t" \
  "ds_read_u16 v167, v251 offset:1280\n\t" \
  "ds_read_u16 v180, v251 offset:808\n\t" \
  "ds_read_u16 v181, v251 offset:904\n\t" \
  "ds_read_u16 v182, v251 offset:1192\n\t" \
  "ds_read_u16 v183, v251 offset:1288\n\t" \
  "ds_read_u16 v104, v251 offset:1536\n\t" \
  "ds_read_u16 v105, v251 offset:1632\n\t" \
  "ds_read_u16 v106, v251 offset:1920\n\t" \
  "ds_read_u16 v107, v251 offset:2016\n\t" \
  "ds_read_u16 v120, v251 offset:1544\n\t" \
  "ds_read_u16 v121, v251 offset:1640\n\t" \
  "ds_read_u16 v122, v251 offset:1928\n\t" \
  "ds_read_u16 v123, v251 offset:2024\n\t" \
  "ds_read_u16 v136, v251 offset:1552\n\t" \
  "ds_read_u16 v137, v251 offset:1648\n\t" \
  "ds_read_u16 v138, v251 offset:1936\n\t" \
  "ds_read_u16 v139, v251 offset:2032\n\t" \
  "ds_read_u16 v152, v251 offset:1560\n\t" \
  "ds_read_u16 v153, v251 offset:1656\n\t" \
  "ds_read_u16 v154, v251 offset:1944\n\t" \
  "ds_read_u16 v155, v251 offset:2040\n\t" \
  "ds_read_u16 v168, v251 offset:1568\n\t" \
  "ds_read_u16 v169, v251 offset:1664\n\t" \
  "ds_read_u16 v170, v251 offset:1952\n\t" \
  "ds_read_u16 v171, v251 offset:2048\n\t" \
  "ds_read_u16 v184, v251 offset:1576\n\t" \
  "ds_read_u16 v185, v251 offset:1672\n\t" \
  "ds_read_u16 v186, v251 offset:1960\n\t" \
  "ds_read_u16 v187, v251 offset:2056\n\t" \
  "ds_read_u16 v108, v251 offset:2304\n\t" \
  "ds_read_u16 v109, v251 offset:2400\n\t" \
  "ds_read_u16 v110, v251 offset:2688\n\t" \
  "ds_read_u16 v111, v251 offset:2784\n\t" \
  "ds_read_u16 v124, v251 offset:2312\n\t" \
  "ds_read_u16 v125, v251 offset:2408\n\t" \
  "ds_read_u16 v126, v251 offset:2696\n\t" \
  "ds_read_u16 v127, v251 offset:2792\n\t" \
  "ds_read_u16 v140, v251 offset:2320\n\t" \
  "ds_read_u16 v141, v251 offset:2416\n\t" \
  "ds_read_u16 v142, v251 offset:2704\n\t" \
  "ds_read_u16 v143, v251 offset:2800\n\t" \
  "ds_read_u16 v156, v251 offset:2328\n\t" \
  "ds_read_u16 v157, v251 offset:2424\n\t" \
  "ds_read_u16 v158, v251 offset:2712\n\t" \
  "ds_read_u16 v159, v251 offset:2808\n\t" \
  "ds_read_u16 v172, v251 offset:2336\n\t" \
  "ds_read_u16 v173, v251 offset:2432\n\t" \
  "ds_read_u16 v174, v251 offset:2720\n\t" \
  "ds_read_u16 v175, v251 offset:2816\n\t" \
  "ds_read_u16 v188, v251 offset:2344\n\t" \
  "ds_read_u16 v189, v251 offset:2440\n\t" \
  "ds_read_u16 v190, v251 offset:2728\n\t" \
  "ds_read_u16 v191, v251 offset:2824\n\t" \
  "s_barrier\n\t" \
  "ds_read_b64 v[192:193], v241 offset:0\n\t" \
  "ds_read_b64 v[194:195], v241 offset:400\n\t" \
  "ds_read_b64 v[196:197], v241 offset:800\n\t" \
  "ds_read_b64 v[198:199], v241 offset:1200\n\t" \
  "ds_read_b64 v[200:201], v241 offset:8\n\t" \
  "ds_read_b64 v[202:203], v241 offset:408\n\t" \
  "ds_read_b64 v[204:205], v241 offset:808\n\t" \
  "ds_read_b64 v[206:207], v241 offset:1208\n\t" \
  "ds_read_b64 v[208:209], v241 offset:16\n\t" \
  "ds_read_b64 v[210:211], v241 offset:416\n\t" \
  "ds_read_b64 v[212:213], v241 offset:816\n\t" \
  "ds_read_b64 v[214:215], v241 offset:1216\n\t" \
  "s_waitcnt lgkmcnt(15)\n\t" \
  "ds_read_b64 v[216:217], v241 offset:24\n\t" \
  "ds_read_b64 v[218:219], v241 offset:424\n\t" \
  "ds_read_b64 v[220:221], v241 offset:824\n\t" \
  "ds_read_b64 v[222:223], v241 offset:1224\n\t" \
  "s_waitcnt lgkmcnt(12)\n\t" \
  "v_smfmac_f32_16x16x64_bf16 v[0:3], v[96:99], v[192:199], v240\n\t" \
  "ds_read_b64 v[192:193], v241 offset:32\n\t" \
  "ds_read_b64 v[194:195], v241 offset:432\n\t" \
  "ds_read_b64 v[196:197], v241 offset:832\n\t" \
  "ds_read_b64 v[198:199], v241 offset:1232\n\t" \
  "s_waitcnt lgkmcnt(12)\n\t" \
  "v_smfmac_f32_16x16x64_bf16 v[4:7], v[112:115], v[200:207], v240\n\t" \
  "ds_read_b64 v[200:201], v241 offset:40\n\t" \
  "ds_read_b64 v[202:203], v241 offset:440\n\t" \
  "ds_read_b64 v[204:205], v241 offset:840\n\t" \
  "ds_read_b64 v[206:207], v241 offset:1240\n\t" \
  "s_add_u32 %[cs], %[cs], 1\n\t" \
  "s_cmp_eq_u32 %[cs], 6\n\t" \
  "s_cselect_b32 %[cs], 0, %[cs]\n\t" \
  "s_cselect_b32 %[t0], -75920, %[ps]\n\t" \
  "v_add_u32 v241, %[t0], v241\n\t" \
  "s_waitcnt lgkmcnt(12)\n\t" \
  "v_smfmac_f32_16x16x64_bf16 v[8:11], v[128:131], v[208:215], v240\n\t" \
  "s_waitcnt lgkmcnt(8)\n\t" \
  "v_smfmac_f32_16x16x64_bf16 v[12:15], v[144:147], v[216:223], v240\n\t" \
  "s_waitcnt lgkmcnt(4)\n\t" \
  "v_smfmac_f32_16x16x64_bf16 v[16:19], v[160:163], v[192:199], v240\n\t" \
  "s_waitcnt lgkmcnt(0)\n\t" \
  "v_smfmac_f32_16x16x64_bf16 v[20:23], v[176:179], v[200:207], v240\n\t" \
  "s_barrier\n\t" \
  "ds_read_b64 v[208:209], v241 offset:0\n\t" \
  "ds_read_b64 v[210:211], v241 offset:400\n\t" \
  "ds_read_b64 v[212:213], v241 offset:800\n\t" \
  "ds_read_b64 v[214:215], v241 offset:1200\n\t" \
  "ds_read_b64 v[216:217], v241 offset:8\n\t" \
  "ds_read_b64 v[218:219], v241 offset:408\n\t" \
  "ds_read_b64 v[220:221], v241 offset:808\n\t" \
  "ds_read_b64 v[222:223], v241 offset:1208\n\t" \
  "ds_read_b64 v[192:193], v241 offset:16\n\t" \
  "ds_read_b64 v[194:195], v241 offset:416\n\t" \
  "ds_read_b64 v[196:197], v241 offset:816\n\t" \
  "ds_read_b64 v[198:199], v241 offset:1216\n\t" \
  "ds_read_b64 v[200:201], v241 offset:24\n\t" \
  "ds_read_b64 v[202:203], v241 offset:424\n\t" \
  "ds_read_b64 v[204:205], v241 offset:824\n\t" \
  "ds_read_b64 v[206:207], v241 offset:1224\n\t" \
  "s_waitcnt lgkmcnt(12)\n\t" \
  "v_smfmac_f32_16x16x64_bf16 v[0:3], v[100:103], v[208:215], v240\n\t" \
  "v_smfmac_f32_16x16x64_bf16 v[24:27], v[96:99], v[208:215], v240\n\t" \
  "ds_read_b64 v[208:209], v241 offset:32\n\t" \
  "ds_read_b64 v[210:211], v241 offset:432\n\t" \
  "ds_read_b64 v[212:213], v241 offset:832\n\t" \
  "ds_read_b64 v[214:215], v241 offset:1232\n\t" \
  "s_waitcnt lgkmcnt(12)\n\t" \
  "v_smfmac_f32_16x16x64_bf16 v[4:7], v[116:119], v[216:223], v240\n\t" \
  "v_smfmac_f32_16x16x64_bf16 v[28:31], v[112:115], v[216:223], v240\n\t" \
  "ds_read_b64 v[216:217], v241 offset:40\n\t" \
  "ds_read_b64 v[218:219], v241 offset:440\n\t" \
  "ds_read_b64 v[220:221], v241 offset:840\n\t" \
  "ds_read_b64 v[222:223], v241 offset:1240\n\t" \
  "s_add_u32 %[cs], %[cs], 1\n\t" \
  "s_cmp_eq_u32 %[cs], 6\n\t" \
  "s_cselect_b32 %[cs], 0, %[cs]\n\t" \
  "s_cselect_b32 %[t0], -75920, %[ps]\n\t" \
  "v_add_u32 v241, %[t0], v241\n\t" \
  "s_waitcnt lgkmcnt(12)\n\t" \
  "v_smfmac_f32_16x16x64_bf16 v[8:11], v[132:135], v[192:199], v240\n\t" \
  "v_smfmac_f32_16x16x64_bf16 v[32:35], v[128:131], v[192:199], v240\n\t" \
  "s_waitcnt lgkmcnt(8)\n\t" \
  "v_smfmac_f32_16x16x64_bf16 v[12:15], v[148:151], v[200:207], v240\n\t" \
  "v_smfmac_f32_16x16x64_bf16 v[36:39], v[144:147], v[200:207], v240\n\t" \
  "s_waitcnt lgkmcnt(4)\n\t" \
  "v_smfmac_f32_16x16x64_bf16 v[16:19], v[164:167], v[208:215], v240\n\t" \
  "v_smfmac_f32_16x16x64_bf16 v[40:43], v[160:163], v[208:215], v240\n\t" \
  "s_waitcnt lgkmcnt(0)\n\t" \
  "v_smfmac_f32_16x16x64_bf16 v[20:23], v[180:183], v[216:223], v240\n\t" \
  "v_smfmac_f32_16x16x64_bf16 v[44:47], v[176:179], v[216:223], v240\n\t" \
  "s_barrier\n\t" \
  "ds_read_b64 v[192:193], v241 offset:0\n\t" \
  "ds_read_b64 v[194:195], v241 offset:400\n\t" \
  "ds_read_b64 v[196:197], v241 offset:800\n\t" \
  "ds_read_b64 v[198:199], v241 offset:1200\n\t" \
  "ds_read_b64 v[200:201], v241 offset:8\n\t" \
  "ds_read_b64 v[202:203], v241 offset:408\n\t" \
  "ds_read_b64 v[204:205], v241 offset:808\n\t" \
  "ds_read_b64 v[206:207], v241 offset:1208\n\t" \
  "ds_read_b64 v[208:209], v241 offset:16\n\t" \
  "ds_read_b64 v[210:211], v241 offset:416\n\t" \
  "ds_read_b64 v[212:213], v241 offset:816\n\t" \
  "ds_read_b64 v[214:215], v241 offset:1216\n\t" \
  "ds_read_b64 v[216:217], v241 offset:24\n\t" \
  "ds_read_b64 v[218:219], v241 offset:424\n\t" \
  "ds_read_b64 v[220:221], v241 offset:824\n\t" \
  "ds_read_b64 v[222:223], v241 offset:1224\n\t" \
  "s_waitcnt lgkmcnt(12)\n\t" \
  "v_smfmac_f32_16x16x64_bf16 v[0:3], v[104:107], v[192:199], v240\n\t" \
  "v_smfmac_f32_16x16x64_bf16 v[24:27], v[100:103], v[192:199], v240\n\t" \
  "v_smfmac_f32_16x16x64_bf16 v[48:51], v[96:99], v[192:199], v240\n\t" \
  "ds_read_b64 v[192:193], v241 offset:32\n\t" \
  "ds_read_b64 v[194:195], v241 offset:432\n\t" \
  "ds_read_b64 v[196:197], v241 offset:832\n\t" \
  "ds_read_b64 v[198:199], v241 offset:1232\n\t" \
  "s_waitcnt lgkmcnt(12)\n\t" \
  "v_smfmac_f32_16x16x64_bf16 v[4:7], v[120:123], v[200:207], v240\n\t" \
  "v_smfmac_f32_16x16x64_bf16 v[28:31], v[116:119], v[200:207], v240\n\t" \
  "v_smfmac_f32_16x16x64_bf16 v[52:55], v[112:115], v[200:207], v240\n\t" \
  "ds_read_b64 v[200:201], v241 offset:40\n\t" \
  "ds_read_b64 v[202:203], v241 offset:440\n\t" \
  "ds_read_b64 v[204:205], v241 offset:840\n\t" \
  "ds_read_b64 v[206:207], v241 offset:1240\n\t" \
  "s_add_u32 %[cs], %[cs], 1\n\t" \
  "s_cmp_eq_u32 %[cs], 6\n\t" \
  "s_cselect_b32 %[cs], 0, %[cs]\n\t" \
  "s_cselect_b32 %[t0], -75920, %[ps]\n\t" \
  "v_add_u32 v241, %[t0], v241\n\t" \
  "s_waitcnt lgkmcnt(12)\n\t" \
  "v_smfmac_f32_16x16x64_bf16 v[8:11], v[136:139], v[208:215], v240\n\t" \
  "v_smfmac_f32_16x16x64_bf16 v[32:35], v[132:135], v[208:215], v240\n\t" \
  "v_smfmac_f32_16x16x64_bf16 v[56:59], v[128:131], v[208:215], v240\n\t" \
  "s_waitcnt lgkmcnt(8)\n\t" \
  "v_smfmac_f32_16x16x64_bf16 v[12:15], v[152:155], v[216:223], v240\n\t" \
  "v_smfmac_f32_16x16x64_bf16 v[36:39], v[148:151], v[216:223], v240\n\t" \
  "v_smfmac_f32_16x16x64_bf16 v[60:63], v[144:147], v[216:223], v240\n\t" \
  "s_waitcnt lgkmcnt(4)\n\t" \
  "v_smfmac_f32_16x16x64_bf16 v[16:19], v[168:171], v[192:199], v240\n\t" \
  "v_smfmac_f32_16x16x64_bf16 v[40:43], v[164:167], v[192:199], v240\n\t" \
  "v_smfmac_f32_16x16x64_bf16 v[64:67], v[160:163], v[192:199], v240\n\t" \
  "s_waitcnt lgkmcnt(0)\n\t" \
  "v_smfmac_f32_16x16x64_bf16 v[20:23], v[184:187], v[200:207], v240\n\t" \
  "v_smfmac_f32_16x16x64_bf16 v[44:47], v[180:183], v[200:207], v240\n\t" \
  "v_smfmac_f32_16x16x64_bf16 v[68:71], v[176:179], v[200:207], v240\n\t" \
  "s_barrier\n\t" \
  "ds_read_b64 v[208:209], v241 offset:0\n\t" \
  "ds_read_b64 v[210:211], v241 offset:400\n\t" \
  "ds_read_b64 v[212:213], v241 offset:800\n\t" \
  "ds_read_b64 v[214:215], v241 offset:1200\n\t" \
  "ds_read_b64 v[216:217], v241 offset:8\n\t" \
  "ds_read_b64 v[218:219], v241 offset:408\n\t" \
  "ds_read_b64 v[220:221], v241 offset:808\n\t" \
  "ds_read_b64 v[222:223], v241 offset:1208\n\t" \
  "ds_read_b64 v[192:193], v241 offset:16\n\t" \
  "ds_read_b64 v[194:195], v241 offset:416\n\t" \
  "ds_read_b64 v[196:197], v241 offset:816\n\t" \
  "ds_read_b64 v[198:199], v241 offset:1216\n\t" \
  "ds_read_b64 v[200:201], v241 offset:24\n\t" \
  "ds_read_b64 v[202:203], v241 offset:424\n\t" \
  "ds_read_b64 v[204:205], v241 offset:824\n\t" \
  "ds_read_b64 v[206:207], v241 offset:1224\n\t" \
  "s_waitcnt lgkmcnt(12)\n\t" \
  "v_smfmac_f32_16x16x64_bf16 v[0:3], v[108:111], v[208:215], v240\n\t" \
  "v_smfmac_f32_16x16x64_bf16 v[24:27], v[104:107], v[208:215], v240\n\t" \
  "v_smfmac_f32_16x16x64_bf16 v[48:51], v[100:103], v[208:215], v240\n\t" \
  "v_smfmac_f32_16x16x64_bf16 v[72:75], v[96:99], v[208:215], v240\n\t" \
  "ds_read_b64 v[208:209], v241 offset:32\n\t" \
  "ds_read_b64 v[210:211], v241 offset:432\n\t" \
  "ds_read_b64 v[212:213], v241 offset:832\n\t" \
  "ds_read_b64 v[214:215], v241 offset:1232\n\t" \
  "s_waitcnt lgkmcnt(12)\n\t" \
  "v_smfmac_f32_16x16x64_bf16 v[4:7], v[124:127], v[216:223], v240\n\t" \
  "v_smfmac_f32_16x16x64_bf16 v[28:31], v[120:123], v[216:223], v240\n\t" \
  "v_smfmac_f32_16x16x64_bf16 v[52:55], v[116:119], v[216:223], v240\n\t" \
  "v_smfmac_f32_16x16x64_bf16 v[76:79], v[112:115], v[216:223], v240\n\t" \
  "v_mul_f32 v237, v0, v0\n\t" \
  "v_add_f32 v236, v0, v1\n\t" \
  "v_fmac_f32 v237, v1, v1\n\t" \
  "v_add_f32 v236, v236, v2\n\t" \
  "v_fmac_f32 v237, v2, v2\n\t" \
  "v_add_f32 v236, v236, v3\n\t" \
  "v_fmac_f32 v237, v3, v3\n\t" \
  "ds_read_b64 v[216:217], v241 offset:40\n\t" \
  "ds_read_b64 v[218:219], v241 offset:440\n\t" \
  "ds_read_b64 v[220:221], v241 offset:840\n\t" \
  "ds_read_b64 v[222:223], v241 offset:1240\n\t" \
  "s_add_u32 %[cs], %[cs], 1\n\t" \
  "s_cmp_eq_u32 %[cs], 6\n\t" \
  "s_cselect_b32 %[cs], 0, %[cs]\n\t" \
  "s_cselect_b32 %[t0], -75920, %[ps]\n\t" \
  "v_add_u32 v241, %[t0], v241\n\t" \
  "s_waitcnt lgkmcnt(12)\n\t" \
  "v_smfmac_f32_16x16x64_bf16 v[8:11], v[140:143], v[192:199], v240\n\t" \
  "v_smfmac_f32_16x16x64_bf16 v[32:35], v[136:139], v[192:199], v240\n\t" \
  "v_smfmac_f32_16x16x64_bf16 v[56:59], v[132:135], v[192:199], v240\n\t" \
  "v_smfmac_f32_16x16x64_bf16 v[80:83], v[128:131], v[192:199], v240\n\t" \
  "v_add_f32 v236, v236, v4\n\t" \
  "v_fmac_f32 v237, v4, v4\n\t" \
  "v_add_f32 v236, v236, v5\n\t" \
  "v_fmac_f32 v237, v5, v5\n\t" \
  "v_add_f32 v236, v236, v6\n\t" \
  "v_fmac_f32 v237, v6, v6\n\t" \
  "v_add_f32 v236, v236, v7\n\t" \
  "v_fmac_f32 v237, v7, v7\n\t" \
  "s_waitcnt lgkmcnt(8)\n\t" \
  "v_smfmac_f32_16x16x64_bf16 v[12:15], v[156:159], v[200:207], v240\n\t" \
  "v_smfmac_f32_16x16x64_bf16 v[36:39], v[152:155], v[200:207], v240\n\t" \
  "v_smfmac_f32_16x16x64_bf16 v[60:63], v[148:151], v[200:207], v240\n\t" \
  "v_smfmac_f32_16x16x64_bf16 v[84:87], v[144:147], v[200:207], v240\n\t" \
  "v_add_f32 v236, v236, v8\n\t" \
  "v_fmac_f32 v237, v8, v8\n\t" \
  "v_add_f32 v236, v236, v9\n\t" \
  "v_fmac_f32 v237, v9, v9\n\t" \
  "v_add_f32 v236, v236, v10\n\t" \
  "v_fmac_f32 v237, v10, v10\n\t" \
  "v_add_f32 v236, v236, v11\n\t" \
  "v_fmac_f32 v237, v11, v11\n\t" \
  "s_waitcnt lgkmcnt(4)\n\t" \
  "v_smfmac_f32_16x16x64_bf16 v[16:19], v[172:175], v[208:215], v240\n\t" \
  "v_smfmac_f32_16x16x64_bf16 v[40:43], v[168:171], v[208:215], v240\n\t" \
  "v_smfmac_f32_16x16x64_bf16 v[64:67], v[164:167], v[208:215], v240\n\t" \
  "v_smfmac_f32_16x16x64_bf16 v[88:91], v[160:163], v[208:215], v240\n\t" \
  "v_add_f32 v236, v236, v12\n\t" \
  "v_fmac_f32 v237, v12, v12\n\t" \
  "v_add_f32 v236, v236, v13\n\t" \
  "v_fmac_f32 v237, v13, v13\n\t" \
  "v_add_f32 v236, v236, v14\n\t" \
  "v_fmac_f32 v237, v14, v14\n\t" \
  "v_add_f32 v236, v236, v15\n\t" \
  "v_fmac_f32 v237, v15, v15\n\t" \
  "s_waitcnt lgkmcnt(0)\n\t" \
  "v_smfmac_f32_16x16x64_bf16 v[20:23], v[188:191], v[216:223], v240\n\t" \
  "v_smfmac_f32_16x16x64_bf16 v[44:47], v[184:187], v[216:223], v240\n\t" \
  "v_smfmac_f32_16x16x64_bf16 v[68:71], v[180:183], v[216:223], v240\n\t" \
  "v_smfmac_f32_16x16x64_bf16 v[92:95], v[176:179], v[216:223], v240\n\t" \
  "v_add_f32 v236, v236, v16\n\t" \
  "v_fmac_f32 v237, v16, v16\n\t" \
  "v_add_f32 v236, v236, v17\n\t" \
  "v_fmac_f32 v237, v17, v17\n\t" \
  "v_add_f32 v236, v236, v18\n\t" \
  "v_fmac_f32 v237, v18, v18\n\t" \
  "v_add_f32 v236, v236, v19\n\t" \
  "v_fmac_f32 v237, v19, v19\n\t" \
  "s_nop 3\n\t" \
  "v_add_f32 v236, v236, v20\n\t" \
  "v_fmac_f32 v237, v20, v20\n\t" \
  "v_add_f32 v236, v236, v21\n\t" \
  "v_fmac_f32 v237, v21, v21\n\t" \
  "v_add_f32 v236, v236, v22\n\t" \
  "v_fmac_f32 v237, v22, v22\n\t" \
  "v_add_f32 v236, v236, v23\n\t" \
  "v_fmac_f32 v237, v23, v23\n\t" \
  "ds_write_b64 v243, v[236:237]\n\t" \
  "s_waitcnt lgkmcnt(0)\n\t" \
  "s_barrier\n\t" \
  "ds_read_b64 v[192:193], v241 offset:0\n\t" \
  "ds_read_b64 v[194:195], v241 offset:400\n\t" \
  "ds_read_b64 v[196:197], v241 offset:800\n\t" \
  "ds_read_b64 v[198:199], v241 offset:1200\n\t" \
  "ds_read_b64 v[200:201], v241 offset:8\n\t" \
  "ds_read_b64 v[202:203], v241 offset:408\n\t" \
  "ds_read_b64 v[204:205], v241 offset:808\n\t" \
  "ds_read_b64 v[206:207], v241 offset:1208\n\t" \
  "ds_read_b128 v[224:227], v244\n\t" \
  "ds_read_b128 v[228:231], v244 offset:1024\n\t" \
  "s_waitcnt lgkmcnt(0)\n\t" \
  "v_add_f32 v224, v224, v226\n\t" \
  "v_add_f32 v225, v225, v227\n\t" \
  "v_add_f32 v228, v228, v230\n\t" \
  "v_add_f32 v229, v229, v231\n\t" \
  "v_add_f32 v224, v224, v228\n\t" \
  "v_add_f32 v225, v225, v229\n\t" \
  "v_mul_f32 v224, 0x3c2aaaab, v224\n\t" \
  "v_mul_f32 v225, 0x3c2aaaab, v225\n\t" \
  "v_fma_f32 v225, -v224, v224, v225\n\t" \
  "v_max_f32 v225, 0, v225\n\t" \
  "v_add_f32 v225, 0x3727c5ac, v225\n\t" \
  "v_rsq_f32 v225, v225\n\t" \
  "s_nop 0\n\t" \
  "v_mul_f32 v238, %[gl], v225\n\t" \
  "v_mul_f32 v239, -v224, v238\n\t" \
  "v_fma_f32 v0, v0, v238, v239\n\t" \
  "v_fma_f32 v1, v1, v238, v239\n\t" \
  "v_fma_f32 v2, v2, v238, v239\n\t" \
  "v_fma_f32 v3, v3, v238, v239\n\t" \
  "v_cvt_pk_bf16_f32 v224, v0, v1\n\t" \
  "v_cvt_pk_bf16_f32 v225, v2, v3\n\t" \
  "v_fma_f32 v4, v4, v238, v239\n\t" \
  "v_fma_f32 v5, v5, v238, v239\n\t" \
  "v_fma_f32 v6, v6, v238, v239\n\t" \
  "v_fma_f32 v7, v7, v238, v239\n\t" \
  "v_cvt_pk_bf16_f32 v226, v4, v5\n\t" \
  "v_cvt_pk_bf16_f32 v227, v6, v7\n\t" \
  "v_mfma_f32_16x16x16_bf16 v[0:3], v[252:253], v[252:253], 0\n\t" \
  "ds_write_b128 v242, v[224:227] offset:0\n\t" \
  "v_fma_f32 v8, v8, v238, v239\n\t" \
  "v_mfma_f32_16x16x16_bf16 v[4:7], v[252:253], v[252:253], 0\n\t" \
  "ds_read_b64 v[208:209], v241 offset:16\n\t" \
  "v_fma_f32 v9, v9, v238, v239\n\t" \
  "v_smfmac_f32_16x16x64_bf16 v[24:27], v[108:111], v[192:199], v240\n\t" \
  "ds_read_b64 v[210:211], v241 offset:416\n\t" \
  "v_fma_f32 v10, v10, v238, v239\n\t" \
  "v_smfmac_f32_16x16x64_bf16 v[48:51], v[104:107], v[192:199], v240\n\t" \
  "ds_read_b64 v[212:213], v241 offset:816\n\t" \
  "v_fma_f32 v11, v11, v238, v239\n\t" \
  "v_smfmac_f32_16x16x64_bf16 v[72:75], v[100:103], v[192:199], v240\n\t" \
  "ds_read_b64 v[214:215], v241 offset:1216\n\t" \
  "v_cvt_pk_bf16_f32 v228, v8, v9\n\t" \
  "v_smfmac_f32_16x16x64_bf16 v[0:3], v[96:99], v[192:199], v240\n\t" \
  "ds_read_b64 v[216:217], v241 offset:24\n\t" \
  "v_cvt_pk_bf16_f32 v229, v10, v11\n\t" \
  "v_smfmac_f32_16x16x64_bf16 v[28:31], v[124:127], v[200:207], v240\n\t" \
  "ds_read_b64 v[218:219], v241 offset:424\n\t" \
  "v_fma_f32 v12, v12, v238, v239\n\t" \
  "v_smfmac_f32_16x16x64_bf16 v[52:55], v[120:123], v[200:207], v240\n\t" \
  "ds_read_b64 v[220:221], v241 offset:824\n\t" \
  "v_fma_f32 v13, v13, v238, v239\n\t" \
  "v_smfmac_f32_16x16x64_bf16 v[76:79], v[116:119], v[200:207], v240\n\t" \
  "ds_read_b64 v[222:223], v241 offset:1224\n\t" \
  "v_fma_f32 v14, v14, v238, v239\n\t" \
  "v_smfmac_f32_16x16x64_bf16 v[4:7], v[112:115], v[200:207], v240\n\t" \
  "v_fma_f32 v15, v15, v238, v239\n\t" \
  "v_cvt_pk_bf16_f32 v230, v12, v13\n\t" \
  "v_cvt_pk_bf16_f32 v231, v14, v15\n\t" \
  "v_fma_f32 v16, v16, v238, v239\n\t" \
  "v_fma_f32 v17, v17, v238, v239\n\t" \
  "v_fma_f32 v18, v18, v238, v239\n\t" \
  "v_fma_f32 v19, v19, v238, v239\n\t" \
  "v_cvt_pk_bf16_f32 v232, v16, v17\n\t" \
  "v_cvt_pk_bf16_f32 v233, v18, v19\n\t" \
  "v_fma_f32 v20, v20, v238, v239\n\t" \
  "v_fma_f32 v21, v21, v238, v239\n\t" \
  "v_fma_f32 v22, v22, v238, v239\n\t" \
  "v_fma_f32 v23, v23, v238, v239\n\t" \
  "v_cvt_pk_bf16_f32 v234, v20, v21\n\t" \
  "v_cvt_pk_bf16_f32 v235, v22, v23\n\t" \
  "v_mfma_f32_32x32x8_bf16 v[8:23], v[252:253], v[252:253], 0\n\t" \
  "s_waitcnt lgkmcnt(4)\n\t" \
  "ds_write_b128 v242, v[228:231] offset:16\n\t" \
  "ds_read_b64 v[192:193], v241 offset:32\n\t" \
  "v_mul_f32 v237, v24, v24\n\t" \
  "v_smfmac_f32_16x16x64_bf16 v[32:35], v[140:143], v[208:215], v240\n\t" \
  "ds_read_b64 v[194:195], v241 offset:432\n\t" \
  "v_add_f32 v236, v24, v25\n\t" \
  "v_smfmac_f32_16x16x64_bf16 v[56:59], v[136:139], v[208:215], v240\n\t" \
  "ds_read_b64 v[196:197], v241 offset:832\n\t" \
  "v_fmac_f32 v237, v25, v25\n\t" \
  "v_smfmac_f32_16x16x64_bf16 v[80:83], v[132:135], v[208:215], v240\n\t" \
  "ds_read_b64 v[198:199], v241 offset:1232\n\t" \
  "v_add_f32 v236, v236, v26\n\t" \
  "v_smfmac_f32_16x16x64_bf16 v[8:11], v[128:131], v[208:215], v240\n\t" \
  "s_waitcnt lgkmcnt(5)\n\t" \
  "ds_write_b128 v242, v[232:235] offset:32\n\t" \
  "v_fmac_f32 v237, v26, v26\n\t" \
  "v_smfmac_f32_16x16x64_bf16 v[36:39], v[156:159], v[216:223], v240\n\t" \
  "ds_read_b64 v[200:201], v241 offset:40\n\t" \
  "v_add_f32 v236, v236, v27\n\t" \
  "v_smfmac_f32_16x16x64_bf16 v[60:63], v[152:155], v[216:223], v240\n\t" \
  "ds_read_b64 v[202:203], v241 offset:440\n\t" \
  "v_fmac_f32 v237, v27, v27\n\t" \
  "v_smfmac_f32_16x16x64_bf16 v[84:87], v[148:151], v[216:223], v240\n\t" \
  "ds_read_b64 v[204:205], v241 offset:840\n\t" \
  "v_add_f32 v236, v236, v28\n\t" \
  "v_smfmac_f32_16x16x64_bf16 v[12:15], v[144:147], v[216:223], v240\n\t" \
  "ds_read_b64 v[206:207], v241 offset:1240\n\t" \
  "v_fmac_f32 v237, v28, v28\n\t" \
  "v_add_f32 v236, v236, v29\n\t" \
  "v_fmac_f32 v237, v29, v29\n\t" \
  "v_add_f32 v236, v236, v30\n\t" \
  "v_fmac_f32 v237, v30, v30\n\t" \
  "v_add_f32 v236, v236, v31\n\t" \
  "v_fmac_f32 v237, v31, v31\n\t" \
  "s_add_u32 %[cs], %[cs], 1\n\t" \
  "s_cmp_eq_u32 %[cs], 6\n\t" \
  "s_cselect_b32 %[cs], 0, %[cs]\n\t" \
  "s_cselect_b32 %[t0], -75920, %[ps]\n\t" \
  "v_add_u32 v241, %[t0], v241\n\t" \
  "s_waitcnt lgkmcnt(4)\n\t" \
  "s_barrier\n\t" \
  "v_smfmac_f32_16x16x64_bf16 v[40:43], v[172:175], v[192:199], v240\n\t" \
  "ds_read_b64 v[208:209], v241 offset:0\n\t" \
  "v_add_f32 v236, v236, v32\n\t" \
  "v_smfmac_f32_16x16x64_bf16 v[64:67], v[168:171], v[192:199], v240\n\t" \
  "ds_read_b64 v[210:211], v241 offset:400\n\t" \
  "v_fmac_f32 v237, v32, v32\n\t" \
  "v_smfmac_f32_16x16x64_bf16 v[88:91], v[164:167], v[192:199], v240\n\t" \
  "ds_read_b64 v[212:213], v241 offset:800\n\t" \
  "v_add_f32 v236, v236, v33\n\t" \
  "v_smfmac_f32_16x16x64_bf16 v[16:19], v[160:163], v[192:199], v240\n\t" \
  "s_waitcnt lgkmcnt(3)\n\t" \
  "ds_read_b64 v[214:215], v241 offset:1200\n\t" \
  "v_fmac_f32 v237, v33, v33\n\t" \
  "v_smfmac_f32_16x16x64_bf16 v[44:47], v[188:191], v[200:207], v240\n\t" \
  "ds_read_b64 v[216:217], v241 offset:8\n\t" \
  "v_add_f32 v236, v236, v34\n\t" \
  "v_smfmac_f32_16x16x64_bf16 v[68:71], v[184:187], v[200:207], v240\n\t" \
  "ds_read_b64 v[218:219], v241 offset:408\n\t" \
  "v_fmac_f32 v237, v34, v34\n\t" \
  "v_smfmac_f32_16x16x64_bf16 v[92:95], v[180:183], v[200:207], v240\n\t" \
  "ds_read_b64 v[220:221], v241 offset:808\n\t" \
  "v_add_f32 v236, v236, v35\n\t" \
  "v_smfmac_f32_16x16x64_bf16 v[20:23], v[176:179], v[200:207], v240\n\t" \
  "ds_read_b64 v[222:223], v241 offset:1208\n\t" \
  "v_fmac_f32 v237, v35, v35\n\t" \
  "s_setprio 0\n\t" \
  "s_mov_b32 m0, %[wld]\n\t" \
  "s_add_u32 %[t1], %[wso], %[wpd]\n\t" \
  "buffer_load_dwordx4 v245, %[wrs], %[wso] offen lds\n\t" \
  "s_add_u32 %[wso], %[wso], %[wcs]\n\t" \
  "s_cmp_eq_u32 %[wso], %[wse]\n\t" \
  "s_cselect_b32 %[wso], %[wsb], %[wso]\n\t" \
  "v_add_f32 v236, v236, v36\n\t" \
  "v_fmac_f32 v237, v36, v36\n\t" \
  "v_add_f32 v236, v236, v37\n\t" \
  "buffer_load_dwordx4 v245, %[wrs], %[t1] offen offset:1024 lds\n\t" \
  "s_add_u32 %[t1], %[t1], %[wpd]\n\t" \
  "s_add_u32 %[wld], %[wld], %[wst]\n\t" \
  "s_cmp_eq_u32 %[wld], %[wle]\n\t" \
  "s_cselect_b32 %[wld], %[wlb], %[wld]\n\t" \
  "v_fmac_f32 v237, v37, v37\n\t" \
  "v_add_f32 v236, v236, v38\n\t" \
  "buffer_load_dwordx4 v245, %[wrs], %[t1] offen offset:2048 lds\n\t" \
  "v_fmac_f32 v237, v38, v38\n\t" \
  "v_add_f32 v236, v236, v39\n\t" \
  "s_mul_i32 %[xso], %[gy], 0x18000\n\t" \
  "s_cmp_lt_u32 %[gy], 0x200\n\t" \
  "s_cselect_b32 %[xso], %[xso], 0x40000000\n\t" \
  "s_add_u32 %[gy], %[gy], %[dgy]\n\t" \
  "s_cmp_eq_u32 %[gy], %[gye]\n\t" \
  "s_cselect_b32 %[gy], 0x100000, %[gy]\n\t" \
  "s_add_u32 m0, %[xld], 0\n\t" \
  "s_nop 0\n\t" \
  "buffer_load_dwordx4 v246, %[xrs], %[xso] offen lds\n\t" \
  "v_fmac_f32 v237, v39, v39\n\t" \
  "v_add_f32 v236, v236, v40\n\t" \
  "v_fmac_f32 v237, v40, v40\n\t" \
  "s_add_u32 m0, %[xld], 1024\n\t" \
  "s_nop 0\n\t" \
  "buffer_load_dwordx4 v247, %[xrs], %[xso] offen lds\n\t" \
  "v_add_f32 v236, v236, v41\n\t" \
  "v_fmac_f32 v237, v41, v41\n\t" \
  "s_add_u32 m0, %[xld], 2048\n\t" \
  "s_nop 0\n\t" \
  "buffer_load_dwordx4 v248, %[xrs], %[xso] offen lds\n\t" \
  "v_add_f32 v236, v236, v42\n\t" \
  "v_fmac_f32 v237, v42, v42\n\t" \
  "s_add_u32 m0, %[xld], 3072\n\t" \
  "s_mov_b64 exec, %[xm]\n\t" \
  "buffer_load_dwordx4 v249, %[xrs], %[xso] offen lds\n\t" \
  "s_mov_b64 exec, -1\n\t" \
  "s_add_u32 %[xld], %[xld], 15184\n\t" \
  "s_cmp_eq_u32 %[xld], %[xle]\n\t" \
  "s_cselect_b32 %[xld], %[xlb], %[xld]\n\t" \
  "v_add_f32 v236, v236, v43\n\t" \
  "v_fmac_f32 v237, v43, v43\n\t" \
  "v_add_f32 v236, v236, v44\n\t" \
  "v_fmac_f32 v237, v44, v44\n\t" \
  "v_add_f32 v236, v236, v45\n\t" \
  "v_fmac_f32 v237, v45, v45\n\t" \
  "v_add_f32 v236, v236, v46\n\t" \
  "v_fmac_f32 v237, v46, v46\n\t" \
  "v_add_f32 v236, v236, v47\n\t" \
  "v_fmac_f32 v237, v47, v47\n\t" \
  "ds_write_b64 v243, v[236:237]\n\t" \
  "s_waitcnt lgkmcnt(0)\n\t" \
  "s_barrier\n\t" \
  "s_mov_b32 %[lc], 4\n\t" \
  ".Lcnx_f:\n\t" \
  "ds_read_b128 v[224:227], v244\n\t" \
  "ds_read_b128 v[228:231], v244 offset:1024\n\t" \
  "s_mov_b32 m0, %[wld]\n\t" \
  "s_add_u32 %[t1], %[wso], %[wpd]\n\t" \
  "buffer_load_dwordx4 v245, %[wrs], %[wso] offen lds\n\t" \
  "s_add_u32 %[wso], %[wso], %[wcs]\n\t" \
  "s_cmp_eq_u32 %[wso], %[wse]\n\t" \
  "s_cselect_b32 %[wso], %[wsb], %[wso]\n\t" \
  "buffer_load_dwordx4 v245, %[wrs], %[t1] offen offset:1024 lds\n\t" \
  "s_add_u32 %[t1], %[t1], %[wpd]\n\t" \
  "s_add_u32 %[wld], %[wld], %[wst]\n\t" \
  "s_cmp_eq_u32 %[wld], %[wle]\n\t" \
  "s_cselect_b32 %[wld], %[wlb], %[wld]\n\t" \
  "buffer_load_dwordx4 v245, %[wrs], %[t1] offen offset:2048 lds\n\t" \
  "s_waitcnt lgkmcnt(0)\n\t" \
  "v_add_f32 v224, v224, v226\n\t" \
  "v_add_f32 v225, v225, v227\n\t" \
  "v_add_f32 v228, v228, v230\n\t" \
  "v_add_f32 v229, v229, v231\n\t" \
  "v_add_f32 v224, v224, v228\n\t" \
  "v_add_f32 v225, v225, v229\n\t" \
  "v_mul_f32 v224, 0x3c2aaaab, v224\n\t" \
  "v_mul_f32 v225, 0x3c2aaaab, v225\n\t" \
  "v_fma_f32 v225, -v224, v224, v225\n\t" \
  "v_max_f32 v225, 0, v225\n\t" \
  "v_add_f32 v225, 0x3727c5ac, v225\n\t" \
  "v_rsq_f32 v225, v225\n\t" \
  "s_nop 0\n\t" \
  "v_mul_f32 v238, %[gl], v225\n\t" \
  "v_mul_f32 v239, -v224, v238\n\t" \
  "v_fma_f32 v24, v24, v238, v239\n\t" \
  "v_fma_f32 v25, v25, v238, v239\n\t" \
  "v_fma_f32 v26, v26, v238, v239\n\t" \
  "v_fma_f32 v27, v27, v238, v239\n\t" \
  "v_cvt_pk_bf16_f32 v224, v24, v25\n\t" \
  "v_cvt_pk_bf16_f32 v225, v26, v27\n\t" \
  "v_fma_f32 v28, v28, v238, v239\n\t" \
  "v_fma_f32 v29, v29, v238, v239\n\t" \
  "v_fma_f32 v30, v30, v238, v239\n\t" \
  "v_fma_f32 v31, v31, v238, v239\n\t" \
  "v_cvt_pk_bf16_f32 v226, v28, v29\n\t" \
  "v_cvt_pk_bf16_f32 v227, v30, v31\n\t" \
  "s_setprio 3\n\t" \
  "v_mfma_f32_16x16x16_bf16 v[24:27], v[252:253], v[252:253], 0\n\t" \
  "ds_write_b128 v242, v[224:227] offset:0\n\t" \
  "v_fma_f32 v32, v32, v238, v239\n\t" \
  "v_mfma_f32_16x16x16_bf16 v[28:31], v[252:253], v[252:253], 0\n\t" \
  "ds_read_b64 v[192:193], v241 offset:16\n\t" \
  "v_fma_f32 v33, v33, v238, v239\n\t" \
  "v_smfmac_f32_16x16x64_bf16 v[48:51], v[108:111], v[208:215], v240\n\t" \
  "ds_read_b64 v[194:195], v241 offset:416\n\t" \
  "v_fma_f32 v34, v34, v238, v239\n\t" \
  "v_smfmac_f32_16x16x64_bf16 v[72:75], v[104:107], v[208:215], v240\n\t" \
  "ds_read_b64 v[196:197], v241 offset:816\n\t" \
  "v_fma_f32 v35, v35, v238, v239\n\t" \
  "v_smfmac_f32_16x16x64_bf16 v[0:3], v[100:103], v[208:215], v240\n\t" \
  "ds_read_b64 v[198:199], v241 offset:1216\n\t" \
  "v_cvt_pk_bf16_f32 v228, v32, v33\n\t" \
  "v_smfmac_f32_16x16x64_bf16 v[24:27], v[96:99], v[208:215], v240\n\t" \
  "ds_read_b64 v[200:201], v241 offset:24\n\t" \
  "v_cvt_pk_bf16_f32 v229, v34, v35\n\t" \
  "v_smfmac_f32_16x16x64_bf16 v[52:55], v[124:127], v[216:223], v240\n\t" \
  "ds_read_b64 v[202:203], v241 offset:424\n\t" \
  "v_fma_f32 v36, v36, v238, v239\n\t" \
  "v_smfmac_f32_16x16x64_bf16 v[76:79], v[120:123], v[216:223], v240\n\t" \
  "ds_read_b64 v[204:205], v241 offset:824\n\t" \
  "v_fma_f32 v37, v37, v238, v239\n\t" \
  "v_smfmac_f32_16x16x64_bf16 v[4:7], v[116:119], v[216:223], v240\n\t" \
  "ds_read_b64 v[206:207], v241 offset:1224\n\t" \
  "v_fma_f32 v38, v38, v238, v239\n\t" \
  "v_smfmac_f32_16x16x64_bf16 v[28:31], v[112:115], v[216:223], v240\n\t" \
  "v_fma_f32 v39, v39, v238, v239\n\t" \
  "s_setprio 0\n\t" \
  "s_waitcnt vmcnt(7)\n\t" \
  "s_barrier\n\t" \
  "s_mov_b32 m0, %[wld]\n\t" \
  "s_add_u32 %[t1], %[wso], %[wpd]\n\t" \
  "buffer_load_dwordx4 v245, %[wrs], %[wso] offen lds\n\t" \
  "s_add_u32 %[wso], %[wso], %[wcs]\n\t" \
  "s_cmp_eq_u32 %[wso], %[wse]\n\t" \
  "s_cselect_b32 %[wso], %[wsb], %[wso]\n\t" \
  "v_cvt_pk_bf16_f32 v230, v36, v37\n\t" \
  "v_cvt_pk_bf16_f32 v231, v38, v39\n\t" \
  "v_fma_f32 v40, v40, v238, v239\n\t" \
  "v_fma_f32 v41, v41, v238, v239\n\t" \
  "buffer_load_dwordx4 v245, %[wrs], %[t1] offen offset:1024 lds\n\t" \
  "s_add_u32 %[t1], %[t1], %[wpd]\n\t" \
  "s_add_u32 %[wld], %[wld], %[wst]\n\t" \
  "s_cmp_eq_u32 %[wld], %[wle]\n\t" \
  "s_cselect_b32 %[wld], %[wlb], %[wld]\n\t" \
  "v_fma_f32 v42, v42, v238, v239\n\t" \
  "v_fma_f32 v43, v43, v238, v239\n\t" \
  "v_cvt_pk_bf16_f32 v232, v40, v41\n\t" \
  "buffer_load_dwordx4 v245, %[wrs], %[t1] offen offset:2048 lds\n\t" \
  "v_cvt_pk_bf16_f32 v233, v42, v43\n\t" \
  "v_fma_f32 v44, v44, v238, v239\n\t" \
  "v_fma_f32 v45, v45, v238, v239\n\t" \
  "v_fma_f32 v46, v46, v238, v239\n\t" \
  "v_fma_f32 v47, v47, v238, v239\n\t" \
  "v_cvt_pk_bf16_f32 v234, v44, v45\n\t" \
  "v_cvt_pk_bf16_f32 v235, v46, v47\n\t" \
  "s_setprio 3\n\t" \
  "v_mfma_f32_32x32x8_bf16 v[32:47], v[252:253], v[252:253], 0\n\t" \
  "s_waitcnt lgkmcnt(4)\n\t" \
  "ds_write_b128 v242, v[228:231] offset:16\n\t" \
  "ds_read_b64 v[208:209], v241 offset:32\n\t" \
  "v_mul_f32 v237, v48, v48\n\t" \
  "v_smfmac_f32_16x16x64_bf16 v[56:59], v[140:143], v[192:199], v240\n\t" \
  "ds_read_b64 v[210:211], v241 offset:432\n\t" \
  "v_add_f32 v236, v48, v49\n\t" \
  "v_smfmac_f32_16x16x64_bf16 v[80:83], v[136:139], v[192:199], v240\n\t" \
  "ds_read_b64 v[212:213], v241 offset:832\n\t" \
  "v_fmac_f32 v237, v49, v49\n\t" \
  "v_smfmac_f32_16x16x64_bf16 v[8:11], v[132:135], v[192:199], v240\n\t" \
  "ds_read_b64 v[214:215], v241 offset:1232\n\t" \
  "v_add_f32 v236, v236, v50\n\t" \
  "v_smfmac_f32_16x16x64_bf16 v[32:35], v[128:131], v[192:199], v240\n\t" \
  "s_waitcnt lgkmcnt(5)\n\t" \
  "ds_write_b128 v242, v[232:235] offset:32\n\t" \
  "v_fmac_f32 v237, v50, v50\n\t" \
  "v_smfmac_f32_16x16x64_bf16 v[60:63], v[156:159], v[200:207], v240\n\t" \
  "ds_read_b64 v[216:217], v241 offset:40\n\t" \
  "v_add_f32 v236, v236, v51\n\t" \
  "v_smfmac_f32_16x16x64_bf16 v[84:87], v[152:155], v[200:207], v240\n\t" \
  "ds_read_b64 v[218:219], v241 offset:440\n\t" \
  "v_fmac_f32 v237, v51, v51\n\t" \
  "v_smfmac_f32_16x16x64_bf16 v[12:15], v[148:151], v[200:207], v240\n\t" \
  "ds_read_b64 v[220:221], v241 offset:840\n\t" \
  "v_add_f32 v236, v236, v52\n\t" \
  "v_smfmac_f32_16x16x64_bf16 v[36:39], v[144:147], v[200:207], v240\n\t" \
  "ds_read_b64 v[222:223], v241 offset:1240\n\t" \
  "v_fmac_f32 v237, v52, v52\n\t" \
  "s_setprio 0\n\t" \
  "v_add_f32 v236, v236, v53\n\t" \
  "v_fmac_f32 v237, v53, v53\n\t" \
  "v_add_f32 v236, v236, v54\n\t" \
  "v_fmac_f32 v237, v54, v54\n\t" \
  "v_add_f32 v236, v236, v55\n\t" \
  "v_fmac_f32 v237, v55, v55\n\t" \
  "s_add_u32 %[cs], %[cs], 1\n\t" \
  "s_cmp_eq_u32 %[cs], 6\n\t" \
  "s_cselect_b32 %[cs], 0, %[cs]\n\t" \
  "s_cselect_b32 %[t0], -75920, %[ps]\n\t" \
  "v_add_u32 v241, %[t0], v241\n\t" \
  "s_waitcnt vmcnt(3)\n\t" \
  "s_waitcnt lgkmcnt(4)\n\t" \
  "s_barrier\n\t" \
  "s_setprio 3\n\t" \
  "v_smfmac_f32_16x16x64_bf16 v[64:67], v[172:175], v[208:215], v240\n\t" \
  "ds_read_b64 v[192:193], v241 offset:0\n\t" \
  "v_add_f32 v236, v236, v56\n\t" \
  "v_smfmac_f32_16x16x64_bf16 v[88:91], v[168:171], v[208:215], v240\n\t" \
  "ds_read_b64 v[194:195], v241 offset:400\n\t" \
  "v_fmac_f32 v237, v56, v56\n\t" \
  "v_smfmac_f32_16x16x64_bf16 v[16:19], v[164:167], v[208:215], v240\n\t" \
  "ds_read_b64 v[196:197], v241 offset:800\n\t" \
  "v_add_f32 v236, v236, v57\n\t" \
  "v_smfmac_f32_16x16x64_bf16 v[40:43], v[160:163], v[208:215], v240\n\t" \
  "s_waitcnt lgkmcnt(3)\n\t" \
  "ds_read_b64 v[198:199], v241 offset:1200\n\t" \
  "v_fmac_f32 v237, v57, v57\n\t" \
  "v_smfmac_f32_16x16x64_bf16 v[68:71], v[188:191], v[216:223], v240\n\t" \
  "ds_read_b64 v[200:201], v241 offset:8\n\t" \
  "v_add_f32 v236, v236, v58\n\t" \
  "v_smfmac_f32_16x16x64_bf16 v[92:95], v[184:187], v[216:223], v240\n\t" \
  "ds_read_b64 v[202:203], v241 offset:408\n\t" \
  "v_fmac_f32 v237, v58, v58\n\t" \
  "v_smfmac_f32_16x16x64_bf16 v[20:23], v[180:183], v[216:223], v240\n\t" \
  "ds_read_b64 v[204:205], v241 offset:808\n\t" \
  "v_add_f32 v236, v236, v59\n\t" \
  "v_smfmac_f32_16x16x64_bf16 v[44:47], v[176:179], v[216:223], v240\n\t" \
  "ds_read_b64 v[206:207], v241 offset:1208\n\t" \
  "v_fmac_f32 v237, v59, v59\n\t" \
  "s_setprio 0\n\t" \
  "s_mov_b32 m0, %[wld]\n\t" \
  "s_add_u32 %[t1], %[wso], %[wpd]\n\t" \
  "buffer_load_dwordx4 v245, %[wrs], %[wso] offen lds\n\t" \
  "s_add_u32 %[wso], %[wso], %[wcs]\n\t" \
  "s_cmp_eq_u32 %[wso], %[wse]\n\t" \
  "s_cselect_b32 %[wso], %[wsb], %[wso]\n\t" \
  "v_add_f32 v236, v236, v60\n\t" \
  "v_fmac_f32 v237, v60, v60\n\t" \
  "v_add_f32 v236, v236, v61\n\t" \
  "buffer_load_dwordx4 v245, %[wrs], %[t1] offen offset:1024 lds\n\t" \
  "s_add_u32 %[t1], %[t1], %[wpd]\n\t" \
  "s_add_u32 %[wld], %[wld], %[wst]\n\t" \
  "s_cmp_eq_u32 %[wld], %[wle]\n\t" \
  "s_cselect_b32 %[wld], %[wlb], %[wld]\n\t" \
  "v_fmac_f32 v237, v61, v61\n\t" \
  "v_add_f32 v236, v236, v62\n\t" \
  "buffer_load_dwordx4 v245, %[wrs], %[t1] offen offset:2048 lds\n\t" \
  "v_fmac_f32 v237, v62, v62\n\t" \
  "v_add_f32 v236, v236, v63\n\t" \
  "s_mul_i32 %[xso], %[gy], 0x18000\n\t" \
  "s_cmp_lt_u32 %[gy], 0x200\n\t" \
  "s_cselect_b32 %[xso], %[xso], 0x40000000\n\t" \
  "s_add_u32 %[gy], %[gy], %[dgy]\n\t" \
  "s_cmp_eq_u32 %[gy], %[gye]\n\t" \
  "s_cselect_b32 %[gy], 0x100000, %[gy]\n\t" \
  "s_add_u32 m0, %[xld], 0\n\t" \
  "s_nop 0\n\t" \
  "buffer_load_dwordx4 v246, %[xrs], %[xso] offen lds\n\t" \
  "v_fmac_f32 v237, v63, v63\n\t" \
  "v_add_f32 v236, v236, v64\n\t" \
  "v_fmac_f32 v237, v64, v64\n\t" \
  "s_add_u32 m0, %[xld], 1024\n\t" \
  "s_nop 0\n\t" \
  "buffer_load_dwordx4 v247, %[xrs], %[xso] offen lds\n\t" \
  "v_add_f32 v236, v236, v65\n\t" \
  "v_fmac_f32 v237, v65, v65\n\t" \
  "s_add_u32 m0, %[xld], 2048\n\t" \
  "s_nop 0\n\t" \
  "buffer_load_dwordx4 v248, %[xrs], %[xso] offen lds\n\t" \
  "v_add_f32 v236, v236, v66\n\t" \
  "v_fmac_f32 v237, v66, v66\n\t" \
  "s_add_u32 m0, %[xld], 3072\n\t" \
  "s_mov_b64 exec, %[xm]\n\t" \
  "buffer_load_dwordx4 v249, %[xrs], %[xso] offen lds\n\t" \
  "s_mov_b64 exec, -1\n\t" \
  "s_add_u32 %[xld], %[xld], 15184\n\t" \
  "s_cmp_eq_u32 %[xld], %[xle]\n\t" \
  "s_cselect_b32 %[xld], %[xlb], %[xld]\n\t" \
  "v_add_f32 v236, v236, v67\n\t" \
  "v_fmac_f32 v237, v67, v67\n\t" \
  "v_add_f32 v236, v236, v68\n\t" \
  "v_fmac_f32 v237, v68, v68\n\t" \
  "v_add_f32 v236, v236, v69\n\t" \
  "v_fmac_f32 v237, v69, v69\n\t" \
  "v_add_f32 v236, v236, v70\n\t" \
  "v_fmac_f32 v237, v70, v70\n\t" \
  "v_add_f32 v236, v236, v71\n\t" \
  "v_fmac_f32 v237, v71, v71\n\t" \
  "ds_write_b64 v243, v[236:237]\n\t" \
  "s_waitcnt vmcnt(7)\n\t" \
  "s_waitcnt lgkmcnt(0)\n\t" \
  "s_barrier\n\t" \
  "ds_read_b128 v[224:227], v244\n\t" \
  "ds_read_b128 v[228:231], v244 offset:1024\n\t" \
  "s_mov_b32 m0, %[wld]\n\t" \
  "s_add_u32 %[t1], %[wso], %[wpd]\n\t" \
  "buffer_load_dwordx4 v245, %[wrs], %[wso] offen lds\n\t" \
  "s_add_u32 %[wso], %[wso], %[wcs]\n\t" \
  "s_cmp_eq_u32 %[wso], %[wse]\n\t" \
  "s_cselect_b32 %[wso], %[wsb], %[wso]\n\t" \
  "buffer_load_dwordx4 v245, %[wrs], %[t1] offen offset:1024 lds\n\t" \
  "s_add_u32 %[t1], %[t1], %[wpd]\n\t" \
  "s_add_u32 %[wld], %[wld], %[wst]\n\t" \
  "s_cmp_eq_u32 %[wld], %[wle]\n\t" \
  "s_cselect_b32 %[wld], %[wlb], %[wld]\n\t" \
  "buffer_load_dwordx4 v245, %[wrs], %[t1] offen offset:2048 lds\n\t" \
  "s_waitcnt lgkmcnt(0)\n\t" \
  "v_add_f32 v224, v224, v226\n\t" \
  "v_add_f32 v225, v225, v227\n\t" \
  "v_add_f32 v228, v228, v230\n\t" \
  "v_add_f32 v229, v229, v231\n\t" \
  "v_add_f32 v224, v224, v228\n\t" \
  "v_add_f32 v225, v225, v229\n\t" \
  "v_mul_f32 v224, 0x3c2aaaab, v224\n\t" \
  "v_mul_f32 v225, 0x3c2aaaab, v225\n\t" \
  "v_fma_f32 v225, -v224, v224, v225\n\t" \
  "v_max_f32 v225, 0, v225\n\t" \
  "v_add_f32 v225, 0x3727c5ac, v225\n\t" \
  "v_rsq_f32 v225, v225\n\t" \
  "s_nop 0\n\t" \
  "v_mul_f32 v238, %[gl], v225\n\t" \
  "v_mul_f32 v239, -v224, v238\n\t" \
  "v_fma_f32 v48, v48, v238, v239\n\t" \
  "v_fma_f32 v49, v49, v238, v239\n\t" \
  "v_fma_f32 v50, v50, v238, v239\n\t" \
  "v_fma_f32 v51, v51, v238, v239\n\t" \
  "v_cvt_pk_bf16_f32 v224, v48, v49\n\t" \
  "v_cvt_pk_bf16_f32 v225, v50, v51\n\t" \
  "v_fma_f32 v52, v52, v238, v239\n\t" \
  "v_fma_f32 v53, v53, v238, v239\n\t" \
  "v_fma_f32 v54, v54, v238, v239\n\t" \
  "v_fma_f32 v55, v55, v238, v239\n\t" \
  "v_cvt_pk_bf16_f32 v226, v52, v53\n\t" \
  "v_cvt_pk_bf16_f32 v227, v54, v55\n\t" \
  "s_setprio 3\n\t" \
  "v_mfma_f32_16x16x16_bf16 v[48:51], v[252:253], v[252:253], 0\n\t" \
  "ds_write_b128 v242, v[224:227] offset:0\n\t" \
  "v_fma_f32 v56, v56, v238, v239\n\t" \
  "v_mfma_f32_16x16x16_bf16 v[52:55], v[252:253], v[252:253], 0\n\t" \
  "ds_read_b64 v[208:209], v241 offset:16\n\t" \
  "v_fma_f32 v57, v57, v238, v239\n\t" \
  "v_smfmac_f32_16x16x64_bf16 v[72:75], v[108:111], v[192:199], v240\n\t" \
  "ds_read_b64 v[210:211], v241 offset:416\n\t" \
  "v_fma_f32 v58, v58, v238, v239\n\t" \
  "v_smfmac_f32_16x16x64_bf16 v[0:3], v[104:107], v[192:199], v240\n\t" \
  "ds_read_b64 v[212:213], v241 offset:816\n\t" \
  "v_fma_f32 v59, v59, v238, v239\n\t" \
  "v_smfmac_f32_16x16x64_bf16 v[24:27], v[100:103], v[192:199], v240\n\t" \
  "ds_read_b64 v[214:215], v241 offset:1216\n\t" \
  "v_cvt_pk_bf16_f32 v228, v56, v57\n\t" \
  "v_smfmac_f32_16x16x64_bf16 v[48:51], v[96:99], v[192:199], v240\n\t" \
  "ds_read_b64 v[216:217], v241 offset:24\n\t" \
  "v_cvt_pk_bf16_f32 v229, v58, v59\n\t" \
  "v_smfmac_f32_16x16x64_bf16 v[76:79], v[124:127], v[200:207], v240\n\t" \
  "ds_read_b64 v[218:219], v241 offset:424\n\t" \
  "v_fma_f32 v60, v60, v238, v239\n\t" \
  "v_smfmac_f32_16x16x64_bf16 v[4:7], v[120:123], v[200:207], v240\n\t" \
  "ds_read_b64 v[220:221], v241 offset:824\n\t" \
  "v_fma_f32 v61, v61, v238, v239\n\t" \
  "v_smfmac_f32_16x16x64_bf16 v[28:31], v[116:119], v[200:207], v240\n\t" \
  "ds_read_b64 v[222:223], v241 offset:1224\n\t" \
  "v_fma_f32 v62, v62, v238, v239\n\t" \
  "v_smfmac_f32_16x16x64_bf16 v[52:55], v[112:115], v[200:207], v240\n\t" \
  "v_fma_f32 v63, v63, v238, v239\n\t" \
  "s_setprio 0\n\t" \
  "s_waitcnt vmcnt(7)\n\t" \
  "s_barrier\n\t" \
  "s_mov_b32 m0, %[wld]\n\t" \
  "s_add_u32 %[t1], %[wso], %[wpd]\n\t" \
  "buffer_load_dwordx4 v245, %[wrs], %[wso] offen lds\n\t" \
  "s_add_u32 %[wso], %[wso], %[wcs]\n\t" \
  "s_cmp_eq_u32 %[wso], %[wse]\n\t" \
  "s_cselect_b32 %[wso], %[wsb], %[wso]\n\t" \
  "v_cvt_pk_bf16_f32 v230, v60, v61\n\t" \
  "v_cvt_pk_bf16_f32 v231, v62, v63\n\t" \
  "v_fma_f32 v64, v64, v238, v239\n\t" \
  "v_fma_f32 v65, v65, v238, v239\n\t" \
  "buffer_load_dwordx4 v245, %[wrs], %[t1] offen offset:1024 lds\n\t" \
  "s_add_u32 %[t1], %[t1], %[wpd]\n\t" \
  "s_add_u32 %[wld], %[wld], %[wst]\n\t" \
  "s_cmp_eq_u32 %[wld], %[wle]\n\t" \
  "s_cselect_b32 %[wld], %[wlb], %[wld]\n\t" \
  "v_fma_f32 v66, v66, v238, v239\n\t" \
  "v_fma_f32 v67, v67, v238, v239\n\t" \
  "v_cvt_pk_bf16_f32 v232, v64, v65\n\t" \
  "buffer_load_dwordx4 v245, %[wrs], %[t1] offen offset:2048 lds\n\t" \
  "v_cvt_pk_bf16_f32 v233, v66, v67\n\t" \
  "v_fma_f32 v68, v68, v238, v239\n\t" \
  "v_fma_f32 v69, v69, v238, v239\n\t" \
  "v_fma_f32 v70, v70, v238, v239\n\t" \
  "v_fma_f32 v71, v71, v238, v239\n\t" \
  "v_cvt_pk_bf16_f32 v234, v68, v69\n\t" \
  "v_cvt_pk_bf16_f32 v235, v70, v71\n\t" \
  "s_setprio 3\n\t" \
  "v_mfma_f32_32x32x8_bf16 v[56:71], v[252:253], v[252:253], 0\n\t" \
  "s_waitcnt lgkmcnt(4)\n\t" \
  "ds_write_b128 v242, v[228:231] offset:16\n\t" \
  "ds_read_b64 v[192:193], v241 offset:32\n\t" \
  "v_mul_f32 v237, v72, v72\n\t" \
  "v_smfmac_f32_16x16x64_bf16 v[80:83], v[140:143], v[208:215], v240\n\t" \
  "ds_read_b64 v[194:195], v241 offset:432\n\t" \
  "v_add_f32 v236, v72, v73\n\t" \
  "v_smfmac_f32_16x16x64_bf16 v[8:11], v[136:139], v[208:215], v240\n\t" \
  "ds_read_b64 v[196:197], v241 offset:832\n\t" \
  "v_fmac_f32 v237, v73, v73\n\t" \
  "v_smfmac_f32_16x16x64_bf16 v[32:35], v[132:135], v[208:215], v240\n\t" \
  "ds_read_b64 v[198:199], v241 offset:1232\n\t" \
  "v_add_f32 v236, v236, v74\n\t" \
  "v_smfmac_f32_16x16x64_bf16 v[56:59], v[128:131], v[208:215], v240\n\t" \
  "s_waitcnt lgkmcnt(5)\n\t" \
  "ds_write_b128 v242, v[232:235] offset:32\n\t" \
  "v_fmac_f32 v237, v74, v74\n\t" \
  "v_smfmac_f32_16x16x64_bf16 v[84:87], v[156:159], v[216:223], v240\n\t" \
  "ds_read_b64 v[200:201], v241 offset:40\n\t" \
  "v_add_f32 v236, v236, v75\n\t" \
  "v_smfmac_f32_16x16x64_bf16 v[12:15], v[152:155], v[216:223], v240\n\t" \
  "ds_read_b64 v[202:203], v241 offset:440\n\t" \
  "v_fmac_f32 v237, v75, v75\n\t" \
  "v_smfmac_f32_16x16x64_bf16 v[36:39], v[148:151], v[216:223], v240\n\t" \
  "ds_read_b64 v[204:205], v241 offset:840\n\t" \
  "v_add_f32 v236, v236, v76\n\t" \
  "v_smfmac_f32_16x16x64_bf16 v[60:63], v[144:147], v[216:223], v240\n\t" \
  "ds_read_b64 v[206:207], v241 offset:1240\n\t" \
  "v_fmac_f32 v237, v76, v76\n\t" \
  "s_setprio 0\n\t" \
  "v_add_f32 v236, v236, v77\n\t" \
  "v_fmac_f32 v237, v77, v77\n\t" \
  "v_add_f32 v236, v236, v78\n\t" \
  "v_fmac_f32 v237, v78, v78\n\t" \
  "v_add_f32 v236, v236, v79\n\t" \
  "v_fmac_f32 v237, v79, v79\n\t" \
  "s_add_u32 %[cs], %[cs], 1\n\t" \
  "s_cmp_eq_u32 %[cs], 6\n\t" \
  "s_cselect_b32 %[cs], 0, %[cs]\n\t" \
  "s_cselect_b32 %[t0], -75920, %[ps]\n\t" \
  "v_add_u32 v241, %[t0], v241\n\t" \
  "s_waitcnt vmcnt(3)\n\t" \
  "s_waitcnt lgkmcnt(4)\n\t" \
  "s_barrier\n\t" \
  "s_setprio 3\n\t" \
  "v_smfmac_f32_16x16x64_bf16 v[88:91], v[172:175], v[192:199], v240\n\t" \
  "ds_read_b64 v[208:209], v241 offset:0\n\t" \
  "v_add_f32 v236, v236, v80\n\t" \
  "v_smfmac_f32_16x16x64_bf16 v[16:19], v[168:171], v[192:199], v240\n\t" \
  "ds_read_b64 v[210:211], v241 offset:400\n\t" \
  "v_fmac_f32 v237, v80, v80\n\t" \
  "v_smfmac_f32_16x16x64_bf16 v[40:43], v[164:167], v[192:199], v240\n\t" \
  "ds_read_b64 v[212:213], v241 offset:800\n\t" \
  "v_add_f32 v236, v236, v81\n\t" \
  "v_smfmac_f32_16x16x64_bf16 v[64:67], v[160:163], v[192:199], v240\n\t" \
  "s_waitcnt lgkmcnt(3)\n\t" \
  "ds_read_b64 v[214:215], v241 offset:1200\n\t" \
  "v_fmac_f32 v237, v81, v81\n\t" \
  "v_smfmac_f32_16x16x64_bf16 v[92:95], v[188:191], v[200:207], v240\n\t" \
  "ds_read_b64 v[216:217], v241 offset:8\n\t" \
  "v_add_f32 v236, v236, v82\n\t" \
  "v_smfmac_f32_16x16x64_bf16 v[20:23], v[184:187], v[200:207], v240\n\t" \
  "ds_read_b64 v[218:219], v241 offset:408\n\t" \
  "v_fmac_f32 v237, v82, v82\n\t" \
  "v_smfmac_f32_16x16x64_bf16 v[44:47], v[180:183], v[200:207], v240\n\t" \
  "ds_read_b64 v[220:221], v241 offset:808\n\t" \
  "v_add_f32 v236, v236, v83\n\t" \
  "v_smfmac_f32_16x16x64_bf16 v[68:71], v[176:179], v[200:207], v240\n\t" \
  "ds_read_b64 v[222:223], v241 offset:1208\n\t" \
  "v_fmac_f32 v237, v83, v83\n\t" \
  "s_setprio 0\n\t" \
  "s_mov_b32 m0, %[wld]\n\t" \
  "s_add_u32 %[t1], %[wso], %[wpd]\n\t" \
  "buffer_load_dwordx4 v245, %[wrs], %[wso] offen lds\n\t" \
  "s_add_u32 %[wso], %[wso], %[wcs]\n\t" \
  "s_cmp_eq_u32 %[wso], %[wse]\n\t" \
  "s_cselect_b32 %[wso], %[wsb], %[wso]\n\t" \
  "v_add_f32 v236, v236, v84\n\t" \
  "v_fmac_f32 v237, v84, v84\n\t" \
  "v_add_f32 v236, v236, v85\n\t" \
  "buffer_load_dwordx4 v245, %[wrs], %[t1] offen offset:1024 lds\n\t" \
  "s_add_u32 %[t1], %[t1], %[wpd]\n\t" \
  "s_add_u32 %[wld], %[wld], %[wst]\n\t" \
  "s_cmp_eq_u32 %[wld], %[wle]\n\t" \
  "s_cselect_b32 %[wld], %[wlb], %[wld]\n\t" \
  "v_fmac_f32 v237, v85, v85\n\t" \
  "v_add_f32 v236, v236, v86\n\t" \
  "buffer_load_dwordx4 v245, %[wrs], %[t1] offen offset:2048 lds\n\t" \
  "v_fmac_f32 v237, v86, v86\n\t" \
  "v_add_f32 v236, v236, v87\n\t" \
  "s_mul_i32 %[xso], %[gy], 0x18000\n\t" \
  "s_cmp_lt_u32 %[gy], 0x200\n\t" \
  "s_cselect_b32 %[xso], %[xso], 0x40000000\n\t" \
  "s_add_u32 %[gy], %[gy], %[dgy]\n\t" \
  "s_cmp_eq_u32 %[gy], %[gye]\n\t" \
  "s_cselect_b32 %[gy], 0x100000, %[gy]\n\t" \
  "s_add_u32 m0, %[xld], 0\n\t" \
  "s_nop 0\n\t" \
  "buffer_load_dwordx4 v246, %[xrs], %[xso] offen lds\n\t" \
  "v_fmac_f32 v237, v87, v87\n\t" \
  "v_add_f32 v236, v236, v88\n\t" \
  "v_fmac_f32 v237, v88, v88\n\t" \
  "s_add_u32 m0, %[xld], 1024\n\t" \
  "s_nop 0\n\t" \
  "buffer_load_dwordx4 v247, %[xrs], %[xso] offen lds\n\t" \
  "v_add_f32 v236, v236, v89\n\t" \
  "v_fmac_f32 v237, v89, v89\n\t" \
  "s_add_u32 m0, %[xld], 2048\n\t" \
  "s_nop 0\n\t" \
  "buffer_load_dwordx4 v248, %[xrs], %[xso] offen lds\n\t" \
  "v_add_f32 v236, v236, v90\n\t" \
  "v_fmac_f32 v237, v90, v90\n\t" \
  "s_add_u32 m0, %[xld], 3072\n\t" \
  "s_mov_b64 exec, %[xm]\n\t" \
  "buffer_load_dwordx4 v249, %[xrs], %[xso] offen lds\n\t" \
  "s_mov_b64 exec, -1\n\t" \
  "s_add_u32 %[xld], %[xld], 15184\n\t" \
  "s_cmp_eq_u32 %[xld], %[xle]\n\t" \
  "s_cselect_b32 %[xld], %[xlb], %[xld]\n\t" \
  "v_add_f32 v236, v236, v91\n\t" \
  "v_fmac_f32 v237, v91, v91\n\t" \
  "v_add_f32 v236, v236, v92\n\t" \
  "v_fmac_f32 v237, v92, v92\n\t" \
  "v_add_f32 v236, v236, v93\n\t" \
  "v_fmac_f32 v237, v93, v93\n\t" \
  "v_add_f32 v236, v236, v94\n\t" \
  "v_fmac_f32 v237, v94, v94\n\t" \
  "v_add_f32 v236, v236, v95\n\t" \
  "v_fmac_f32 v237, v95, v95\n\t" \
  "ds_write_b64 v243, v[236:237]\n\t" \
  "s_waitcnt vmcnt(7)\n\t" \
  "s_waitcnt lgkmcnt(0)\n\t" \
  "s_barrier\n\t" \
  "ds_read_b128 v[224:227], v244\n\t" \
  "ds_read_b128 v[228:231], v244 offset:1024\n\t" \
  "s_mov_b32 m0, %[wld]\n\t" \
  "s_add_u32 %[t1], %[wso], %[wpd]\n\t" \
  "buffer_load_dwordx4 v245, %[wrs], %[wso] offen lds\n\t" \
  "s_add_u32 %[wso], %[wso], %[wcs]\n\t" \
  "s_cmp_eq_u32 %[wso], %[wse]\n\t" \
  "s_cselect_b32 %[wso], %[wsb], %[wso]\n\t" \
  "buffer_load_dwordx4 v245, %[wrs], %[t1] offen offset:1024 lds\n\t" \
  "s_add_u32 %[t1], %[t1], %[wpd]\n\t" \
  "s_add_u32 %[wld], %[wld], %[wst]\n\t" \
  "s_cmp_eq_u32 %[wld], %[wle]\n\t" \
  "s_cselect_b32 %[wld], %[wlb], %[wld]\n\t" \
  "buffer_load_dwordx4 v245, %[wrs], %[t1] offen offset:2048 lds\n\t" \
  "s_waitcnt lgkmcnt(0)\n\t" \
  "v_add_f32 v224, v224, v226\n\t" \
  "v_add_f32 v225, v225, v227\n\t" \
  "v_add_f32 v228, v228, v230\n\t" \
  "v_add_f32 v229, v229, v231\n\t" \
  "v_add_f32 v224, v224, v228\n\t" \
  "v_add_f32 v225, v225, v229\n\t" \
  "v_mul_f32 v224, 0x3c2aaaab, v224\n\t" \
  "v_mul_f32 v225, 0x3c2aaaab, v225\n\t" \
  "v_fma_f32 v225, -v224, v224, v225\n\t" \
  "v_max_f32 v225, 0, v225\n\t" \
  "v_add_f32 v225, 0x3727c5ac, v225\n\t" \
  "v_rsq_f32 v225, v225\n\t" \
  "s_nop 0\n\t" \
  "v_mul_f32 v238, %[gt], v225\n\t" \
  "v_mul_f32 v239, -v224, v238\n\t" \
  "v_fma_f32 v72, v72, v238, v239\n\t" \
  "v_fma_f32 v73, v73, v238, v239\n\t" \
  "v_fma_f32 v74, v74, v238, v239\n\t" \
  "v_fma_f32 v75, v75, v238, v239\n\t" \
  "v_cvt_pk_bf16_f32 v224, v72, v73\n\t" \
  "v_cvt_pk_bf16_f32 v225, v74, v75\n\t" \
  "v_fma_f32 v76, v76, v238, v239\n\t" \
  "v_fma_f32 v77, v77, v238, v239\n\t" \
  "v_fma_f32 v78, v78, v238, v239\n\t" \
  "v_fma_f32 v79, v79, v238, v239\n\t" \
  "v_cvt_pk_bf16_f32 v226, v76, v77\n\t" \
  "v_cvt_pk_bf16_f32 v227, v78, v79\n\t" \
  "s_setprio 3\n\t" \
  "v_mfma_f32_16x16x16_bf16 v[72:75], v[252:253], v[252:253], 0\n\t" \
  "ds_write_b128 v242, v[224:227] offset:0\n\t" \
  "v_fma_f32 v80, v80, v238, v239\n\t" \
  "v_mfma_f32_16x16x16_bf16 v[76:79], v[252:253], v[252:253], 0\n\t" \
  "ds_read_b64 v[192:193], v241 offset:16\n\t" \
  "v_fma_f32 v81, v81, v238, v239\n\t" \
  "v_smfmac_f32_16x16x64_bf16 v[0:3], v[108:111], v[208:215], v240\n\t" \
  "ds_read_b64 v[194:195], v241 offset:416\n\t" \
  "v_fma_f32 v82, v82, v238, v239\n\t" \
  "v_smfmac_f32_16x16x64_bf16 v[24:27], v[104:107], v[208:215], v240\n\t" \
  "ds_read_b64 v[196:197], v241 offset:816\n\t" \
  "v_fma_f32 v83, v83, v238, v239\n\t" \
  "v_smfmac_f32_16x16x64_bf16 v[48:51], v[100:103], v[208:215], v240\n\t" \
  "ds_read_b64 v[198:199], v241 offset:1216\n\t" \
  "v_cvt_pk_bf16_f32 v228, v80, v81\n\t" \
  "v_smfmac_f32_16x16x64_bf16 v[72:75], v[96:99], v[208:215], v240\n\t" \
  "ds_read_b64 v[200:201], v241 offset:24\n\t" \
  "v_cvt_pk_bf16_f32 v229, v82, v83\n\t" \
  "v_smfmac_f32_16x16x64_bf16 v[4:7], v[124:127], v[216:223], v240\n\t" \
  "ds_read_b64 v[202:203], v241 offset:424\n\t" \
  "v_fma_f32 v84, v84, v238, v239\n\t" \
  "v_smfmac_f32_16x16x64_bf16 v[28:31], v[120:123], v[216:223], v240\n\t" \
  "ds_read_b64 v[204:205], v241 offset:824\n\t" \
  "v_fma_f32 v85, v85, v238, v239\n\t" \
  "v_smfmac_f32_16x16x64_bf16 v[52:55], v[116:119], v[216:223], v240\n\t" \
  "ds_read_b64 v[206:207], v241 offset:1224\n\t" \
  "v_fma_f32 v86, v86, v238, v239\n\t" \
  "v_smfmac_f32_16x16x64_bf16 v[76:79], v[112:115], v[216:223], v240\n\t" \
  "v_fma_f32 v87, v87, v238, v239\n\t" \
  "s_setprio 0\n\t" \
  "s_waitcnt vmcnt(7)\n\t" \
  "s_barrier\n\t" \
  "s_mov_b32 m0, %[wld]\n\t" \
  "s_add_u32 %[t1], %[wso], %[wpd]\n\t" \
  "buffer_load_dwordx4 v245, %[wrs], %[wso] offen lds\n\t" \
  "s_add_u32 %[wso], %[wso], %[wcs]\n\t" \
  "s_cmp_eq_u32 %[wso], %[wse]\n\t" \
  "s_cselect_b32 %[wso], %[wsb], %[wso]\n\t" \
  "v_cvt_pk_bf16_f32 v230, v84, v85\n\t" \
  "v_cvt_pk_bf16_f32 v231, v86, v87\n\t" \
  "v_fma_f32 v88, v88, v238, v239\n\t" \
  "v_fma_f32 v89, v89, v238, v239\n\t" \
  "buffer_load_dwordx4 v245, %[wrs], %[t1] offen offset:1024 lds\n\t" \
  "s_add_u32 %[t1], %[t1], %[wpd]\n\t" \
  "s_add_u32 %[wld], %[wld], %[wst]\n\t" \
  "s_cmp_eq_u32 %[wld], %[wle]\n\t" \
  "s_cselect_b32 %[wld], %[wlb], %[wld]\n\t" \
  "v_fma_f32 v90, v90, v238, v239\n\t" \
  "v_fma_f32 v91, v91, v238, v239\n\t" \
  "v_cvt_pk_bf16_f32 v232, v88, v89\n\t" \
  "buffer_load_dwordx4 v245, %[wrs], %[t1] offen offset:2048 lds\n\t" \
  "v_cvt_pk_bf16_f32 v233, v90, v91\n\t" \
  "v_fma_f32 v92, v92, v238, v239\n\t" \
  "v_fma_f32 v93, v93, v238, v239\n\t" \
  "v_fma_f32 v94, v94, v238, v239\n\t" \
  "v_fma_f32 v95, v95, v238, v239\n\t" \
  "v_cvt_pk_bf16_f32 v234, v92, v93\n\t" \
  "v_cvt_pk_bf16_f32 v235, v94, v95\n\t" \
  "s_setprio 3\n\t" \
  "v_mfma_f32_32x32x8_bf16 v[80:95], v[252:253], v[252:253], 0\n\t" \
  "s_waitcnt lgkmcnt(4)\n\t" \
  "ds_write_b128 v242, v[228:231] offset:16\n\t" \
  "ds_read_b64 v[208:209], v241 offset:32\n\t" \
  "v_mul_f32 v237, v0, v0\n\t" \
  "v_smfmac_f32_16x16x64_bf16 v[8:11], v[140:143], v[192:199], v240\n\t" \
  "ds_read_b64 v[210:211], v241 offset:432\n\t" \
  "v_add_f32 v236, v0, v1\n\t" \
  "v_smfmac_f32_16x16x64_bf16 v[32:35], v[136:139], v[192:199], v240\n\t" \
  "ds_read_b64 v[212:213], v241 offset:832\n\t" \
  "v_fmac_f32 v237, v1, v1\n\t" \
  "v_smfmac_f32_16x16x64_bf16 v[56:59], v[132:135], v[192:199], v240\n\t" \
  "ds_read_b64 v[214:215], v241 offset:1232\n\t" \
  "v_add_f32 v236, v236, v2\n\t" \
  "v_smfmac_f32_16x16x64_bf16 v[80:83], v[128:131], v[192:199], v240\n\t" \
  "s_waitcnt lgkmcnt(5)\n\t" \
  "ds_write_b128 v242, v[232:235] offset:32\n\t" \
  "v_fmac_f32 v237, v2, v2\n\t" \
  "v_smfmac_f32_16x16x64_bf16 v[12:15], v[156:159], v[200:207], v240\n\t" \
  "ds_read_b64 v[216:217], v241 offset:40\n\t" \
  "v_add_f32 v236, v236, v3\n\t" \
  "v_smfmac_f32_16x16x64_bf16 v[36:39], v[152:155], v[200:207], v240\n\t" \
  "ds_read_b64 v[218:219], v241 offset:440\n\t" \
  "v_fmac_f32 v237, v3, v3\n\t" \
  "v_smfmac_f32_16x16x64_bf16 v[60:63], v[148:151], v[200:207], v240\n\t" \
  "ds_read_b64 v[220:221], v241 offset:840\n\t" \
  "v_add_f32 v236, v236, v4\n\t" \
  "v_smfmac_f32_16x16x64_bf16 v[84:87], v[144:147], v[200:207], v240\n\t" \
  "ds_read_b64 v[222:223], v241 offset:1240\n\t" \
  "v_fmac_f32 v237, v4, v4\n\t" \
  "s_setprio 0\n\t" \
  "v_add_f32 v236, v236, v5\n\t" \
  "v_fmac_f32 v237, v5, v5\n\t" \
  "v_add_f32 v236, v236, v6\n\t" \
  "v_fmac_f32 v237, v6, v6\n\t" \
  "v_add_f32 v236, v236, v7\n\t" \
  "v_fmac_f32 v237, v7, v7\n\t" \
  "s_add_u32 %[cs], %[cs], 1\n\t" \
  "s_cmp_eq_u32 %[cs], 6\n\t" \
  "s_cselect_b32 %[cs], 0, %[cs]\n\t" \
  "s_cselect_b32 %[t0], -75920, %[ps]\n\t" \
  "v_add_u32 v241, %[t0], v241\n\t" \
  "s_waitcnt vmcnt(3)\n\t" \
  "s_waitcnt lgkmcnt(4)\n\t" \
  "s_barrier\n\t" \
  "s_sub_u32 %[lc], %[lc], 1\n\t" \
  "s_cmp_eq_u32 %[lc], 0\n\t" \
  "s_cbranch_scc1 .Lcnx_fx\n\t" \
  "s_setprio 3\n\t" \
  "v_smfmac_f32_16x16x64_bf16 v[16:19], v[172:175], v[208:215], v240\n\t" \
  "ds_read_b64 v[192:193], v241 offset:0\n\t" \
  "v_add_f32 v236, v236, v8\n\t" \
  "v_smfmac_f32_16x16x64_bf16 v[40:43], v[168:171], v[208:215], v240\n\t" \
  "ds_read_b64 v[194:195], v241 offset:400\n\t" \
  "v_fmac_f32 v237, v8, v8\n\t" \
  "v_smfmac_f32_16x16x64_bf16 v[64:67], v[164:167], v[208:215], v240\n\t" \
  "ds_read_b64 v[196:197], v241 offset:800\n\t" \
  "v_add_f32 v236, v236, v9\n\t" \
  "v_smfmac_f32_16x16x64_bf16 v[88:91], v[160:163], v[208:215], v240\n\t" \
  "s_waitcnt lgkmcnt(3)\n\t" \
  "ds_read_b64 v[198:199], v241 offset:1200\n\t" \
  "v_fmac_f32 v237, v9, v9\n\t" \
  "v_smfmac_f32_16x16x64_bf16 v[20:23], v[188:191], v[216:223], v240\n\t" \
  "ds_read_b64 v[200:201], v241 offset:8\n\t" \
  "v_add_f32 v236, v236, v10\n\t" \
  "v_smfmac_f32_16x16x64_bf16 v[44:47], v[184:187], v[216:223], v240\n\t" \
  "ds_read_b64 v[202:203], v241 offset:408\n\t" \
  "v_fmac_f32 v237, v10, v10\n\t" \
  "v_smfmac_f32_16x16x64_bf16 v[68:71], v[180:183], v[216:223], v240\n\t" \
  "ds_read_b64 v[204:205], v241 offset:808\n\t" \
  "v_add_f32 v236, v236, v11\n\t" \
  "v_smfmac_f32_16x16x64_bf16 v[92:95], v[176:179], v[216:223], v240\n\t" \
  "ds_read_b64 v[206:207], v241 offset:1208\n\t" \
  "v_fmac_f32 v237, v11, v11\n\t" \
  "s_setprio 0\n\t" \
  "s_mov_b32 m0, %[wld]\n\t" \
  "s_add_u32 %[t1], %[wso], %[wpd]\n\t" \
  "buffer_load_dwordx4 v245, %[wrs], %[wso] offen lds\n\t" \
  "s_add_u32 %[wso], %[wso], %[wcs]\n\t" \
  "s_cmp_eq_u32 %[wso], %[wse]\n\t" \
  "s_cselect_b32 %[wso], %[wsb], %[wso]\n\t" \
  "v_add_f32 v236, v236, v12\n\t" \
  "v_fmac_f32 v237, v12, v12\n\t" \
  "v_add_f32 v236, v236, v13\n\t" \
  "buffer_load_dwordx4 v245, %[wrs], %[t1] offen offset:1024 lds\n\t" \
  "s_add_u32 %[t1], %[t1], %[wpd]\n\t" \
  "s_add_u32 %[wld], %[wld], %[wst]\n\t" \
  "s_cmp_eq_u32 %[wld], %[wle]\n\t" \
  "s_cselect_b32 %[wld], %[wlb], %[wld]\n\t" \
  "v_fmac_f32 v237, v13, v13\n\t" \
  "v_add_f32 v236, v236, v14\n\t" \
  "buffer_load_dwordx4 v245, %[wrs], %[t1] offen offset:2048 lds\n\t" \
  "v_fmac_f32 v237, v14, v14\n\t" \
  "v_add_f32 v236, v236, v15\n\t" \
  "s_mul_i32 %[xso], %[gy], 0x18000\n\t" \
  "s_cmp_lt_u32 %[gy], 0x200\n\t" \
  "s_cselect_b32 %[xso], %[xso], 0x40000000\n\t" \
  "s_add_u32 %[gy], %[gy], %[dgy]\n\t" \
  "s_cmp_eq_u32 %[gy], %[gye]\n\t" \
  "s_cselect_b32 %[gy], 0x100000, %[gy]\n\t" \
  "s_add_u32 m0, %[xld], 0\n\t" \
  "s_nop 0\n\t" \
  "buffer_load_dwordx4 v246, %[xrs], %[xso] offen lds\n\t" \
  "v_fmac_f32 v237, v15, v15\n\t" \
  "v_add_f32 v236, v236, v16\n\t" \
  "v_fmac_f32 v237, v16, v16\n\t" \
  "s_add_u32 m0, %[xld], 1024\n\t" \
  "s_nop 0\n\t" \
  "buffer_load_dwordx4 v247, %[xrs], %[xso] offen lds\n\t" \
  "v_add_f32 v236, v236, v17\n\t" \
  "v_fmac_f32 v237, v17, v17\n\t" \
  "s_add_u32 m0, %[xld], 2048\n\t" \
  "s_nop 0\n\t" \
  "buffer_load_dwordx4 v248, %[xrs], %[xso] offen lds\n\t" \
  "v_add_f32 v236, v236, v18\n\t" \
  "v_fmac_f32 v237, v18, v18\n\t" \
  "s_add_u32 m0, %[xld], 3072\n\t" \
  "s_mov_b64 exec, %[xm]\n\t" \
  "buffer_load_dwordx4 v249, %[xrs], %[xso] offen lds\n\t" \
  "s_mov_b64 exec, -1\n\t" \
  "s_add_u32 %[xld], %[xld], 15184\n\t" \
  "s_cmp_eq_u32 %[xld], %[xle]\n\t" \
  "s_cselect_b32 %[xld], %[xlb], %[xld]\n\t" \
  "v_add_f32 v236, v236, v19\n\t" \
  "v_fmac_f32 v237, v19, v19\n\t" \
  "v_add_f32 v236, v236, v20\n\t" \
  "v_fmac_f32 v237, v20, v20\n\t" \
  "v_add_f32 v236, v236, v21\n\t" \
  "v_fmac_f32 v237, v21, v21\n\t" \
  "v_add_f32 v236, v236, v22\n\t" \
  "v_fmac_f32 v237, v22, v22\n\t" \
  "v_add_f32 v236, v236, v23\n\t" \
  "v_fmac_f32 v237, v23, v23\n\t" \
  "ds_write_b64 v243, v[236:237]\n\t" \
  "s_waitcnt vmcnt(7)\n\t" \
  "s_waitcnt lgkmcnt(0)\n\t" \
  "s_barrier\n\t" \
  "ds_read_b128 v[224:227], v244\n\t" \
  "ds_read_b128 v[228:231], v244 offset:1024\n\t" \
  "s_mov_b32 m0, %[wld]\n\t" \
  "s_add_u32 %[t1], %[wso], %[wpd]\n\t" \
  "buffer_load_dwordx4 v245, %[wrs], %[wso] offen lds\n\t" \
  "s_add_u32 %[wso], %[wso], %[wcs]\n\t" \
  "s_cmp_eq_u32 %[wso], %[wse]\n\t" \
  "s_cselect_b32 %[wso], %[wsb], %[wso]\n\t" \
  "buffer_load_dwordx4 v245, %[wrs], %[t1] offen offset:1024 lds\n\t" \
  "s_add_u32 %[t1], %[t1], %[wpd]\n\t" \
  "s_add_u32 %[wld], %[wld], %[wst]\n\t" \
  "s_cmp_eq_u32 %[wld], %[wle]\n\t" \
  "s_cselect_b32 %[wld], %[wlb], %[wld]\n\t" \
  "buffer_load_dwordx4 v245, %[wrs], %[t1] offen offset:2048 lds\n\t" \
  "s_waitcnt lgkmcnt(0)\n\t" \
  "v_add_f32 v224, v224, v226\n\t" \
  "v_add_f32 v225, v225, v227\n\t" \
  "v_add_f32 v228, v228, v230\n\t" \
  "v_add_f32 v229, v229, v231\n\t" \
  "v_add_f32 v224, v224, v228\n\t" \
  "v_add_f32 v225, v225, v229\n\t" \
  "v_mul_f32 v224, 0x3c2aaaab, v224\n\t" \
  "v_mul_f32 v225, 0x3c2aaaab, v225\n\t" \
  "v_fma_f32 v225, -v224, v224, v225\n\t" \
  "v_max_f32 v225, 0, v225\n\t" \
  "v_add_f32 v225, 0x3727c5ac, v225\n\t" \
  "v_rsq_f32 v225, v225\n\t" \
  "s_nop 0\n\t" \
  "v_mul_f32 v238, %[gl], v225\n\t" \
  "v_mul_f32 v239, -v224, v238\n\t" \
  "v_fma_f32 v0, v0, v238, v239\n\t" \
  "v_fma_f32 v1, v1, v238, v239\n\t" \
  "v_fma_f32 v2, v2, v238, v239\n\t" \
  "v_fma_f32 v3, v3, v238, v239\n\t" \
  "v_cvt_pk_bf16_f32 v224, v0, v1\n\t" \
  "v_cvt_pk_bf16_f32 v225, v2, v3\n\t" \
  "v_fma_f32 v4, v4, v238, v239\n\t" \
  "v_fma_f32 v5, v5, v238, v239\n\t" \
  "v_fma_f32 v6, v6, v238, v239\n\t" \
  "v_fma_f32 v7, v7, v238, v239\n\t" \
  "v_cvt_pk_bf16_f32 v226, v4, v5\n\t" \
  "v_cvt_pk_bf16_f32 v227, v6, v7\n\t" \
  "s_setprio 3\n\t" \
  "v_mfma_f32_16x16x16_bf16 v[0:3], v[252:253], v[252:253], 0\n\t" \
  "ds_write_b128 v242, v[224:227] offset:0\n\t" \
  "v_fma_f32 v8, v8, v238, v239\n\t" \
  "v_mfma_f32_16x16x16_bf16 v[4:7], v[252:253], v[252:253], 0\n\t" \
  "ds_read_b64 v[208:209], v241 offset:16\n\t" \
  "v_fma_f32 v9, v9, v238, v239\n\t" \
  "v_smfmac_f32_16x16x64_bf16 v[24:27], v[108:111], v[192:199], v240\n\t" \
  "ds_read_b64 v[210:211], v241 offset:416\n\t" \
  "v_fma_f32 v10, v10, v238, v239\n\t" \
  "v_smfmac_f32_16x16x64_bf16 v[48:51], v[104:107], v[192:199], v240\n\t" \
  "ds_read_b64 v[212:213], v241 offset:816\n\t" \
  "v_fma_f32 v11, v11, v238, v239\n\t" \
  "v_smfmac_f32_16x16x64_bf16 v[72:75], v[100:103], v[192:199], v240\n\t" \
  "ds_read_b64 v[214:215], v241 offset:1216\n\t" \
  "v_cvt_pk_bf16_f32 v228, v8, v9\n\t" \
  "v_smfmac_f32_16x16x64_bf16 v[0:3], v[96:99], v[192:199], v240\n\t" \
  "ds_read_b64 v[216:217], v241 offset:24\n\t" \
  "v_cvt_pk_bf16_f32 v229, v10, v11\n\t" \
  "v_smfmac_f32_16x16x64_bf16 v[28:31], v[124:127], v[200:207], v240\n\t" \
  "ds_read_b64 v[218:219], v241 offset:424\n\t" \
  "v_fma_f32 v12, v12, v238, v239\n\t" \
  "v_smfmac_f32_16x16x64_bf16 v[52:55], v[120:123], v[200:207], v240\n\t" \
  "ds_read_b64 v[220:221], v241 offset:824\n\t" \
  "v_fma_f32 v13, v13, v238, v239\n\t" \
  "v_smfmac_f32_16x16x64_bf16 v[76:79], v[116:119], v[200:207], v240\n\t" \
  "ds_read_b64 v[222:223], v241 offset:1224\n\t" \
  "v_fma_f32 v14, v14, v238, v239\n\t" \
  "v_smfmac_f32_16x16x64_bf16 v[4:7], v[112:115], v[200:207], v240\n\t" \
  "v_fma_f32 v15, v15, v238, v239\n\t" \
  "s_setprio 0\n\t" \
  "s_waitcnt vmcnt(7)\n\t" \
  "s_barrier\n\t" \
  "s_mov_b32 m0, %[wld]\n\t" \
  "s_add_u32 %[t1], %[wso], %[wpd]\n\t" \
  "buffer_load_dwordx4 v245, %[wrs], %[wso] offen lds\n\t" \
  "s_add_u32 %[wso], %[wso], %[wcs]\n\t" \
  "s_cmp_eq_u32 %[wso], %[wse]\n\t" \
  "s_cselect_b32 %[wso], %[wsb], %[wso]\n\t" \
  "v_cvt_pk_bf16_f32 v230, v12, v13\n\t" \
  "v_cvt_pk_bf16_f32 v231, v14, v15\n\t" \
  "v_fma_f32 v16, v16, v238, v239\n\t" \
  "v_fma_f32 v17, v17, v238, v239\n\t" \
  "buffer_load_dwordx4 v245, %[wrs], %[t1] offen offset:1024 lds\n\t" \
  "s_add_u32 %[t1], %[t1], %[wpd]\n\t" \
  "s_add_u32 %[wld], %[wld], %[wst]\n\t" \
  "s_cmp_eq_u32 %[wld], %[wle]\n\t" \
  "s_cselect_b32 %[wld], %[wlb], %[wld]\n\t" \
  "v_fma_f32 v18, v18, v238, v239\n\t" \
  "v_fma_f32 v19, v19, v238, v239\n\t" \
  "v_cvt_pk_bf16_f32 v232, v16, v17\n\t" \
  "buffer_load_dwordx4 v245, %[wrs], %[t1] offen offset:2048 lds\n\t" \
  "v_cvt_pk_bf16_f32 v233, v18, v19\n\t" \
  "v_fma_f32 v20, v20, v238, v239\n\t" \
  "v_fma_f32 v21, v21, v238, v239\n\t" \
  "v_fma_f32 v22, v22, v238, v239\n\t" \
  "v_fma_f32 v23, v23, v238, v239\n\t" \
  "v_cvt_pk_bf16_f32 v234, v20, v21\n\t" \
  "v_cvt_pk_bf16_f32 v235, v22, v23\n\t" \
  "s_setprio 3\n\t" \
  "v_mfma_f32_32x32x8_bf16 v[8:23], v[252:253], v[252:253], 0\n\t" \
  "s_waitcnt lgkmcnt(4)\n\t" \
  "ds_write_b128 v242, v[228:231] offset:16\n\t" \
  "ds_read_b64 v[192:193], v241 offset:32\n\t" \
  "v_mul_f32 v237, v24, v24\n\t" \
  "v_smfmac_f32_16x16x64_bf16 v[32:35], v[140:143], v[208:215], v240\n\t" \
  "ds_read_b64 v[194:195], v241 offset:432\n\t" \
  "v_add_f32 v236, v24, v25\n\t" \
  "v_smfmac_f32_16x16x64_bf16 v[56:59], v[136:139], v[208:215], v240\n\t" \
  "ds_read_b64 v[196:197], v241 offset:832\n\t" \
  "v_fmac_f32 v237, v25, v25\n\t" \
  "v_smfmac_f32_16x16x64_bf16 v[80:83], v[132:135], v[208:215], v240\n\t" \
  "ds_read_b64 v[198:199], v241 offset:1232\n\t" \
  "v_add_f32 v236, v236, v26\n\t" \
  "v_smfmac_f32_16x16x64_bf16 v[8:11], v[128:131], v[208:215], v240\n\t" \
  "s_waitcnt lgkmcnt(5)\n\t" \
  "ds_write_b128 v242, v[232:235] offset:32\n\t" \
  "v_fmac_f32 v237, v26, v26\n\t" \
  "v_smfmac_f32_16x16x64_bf16 v[36:39], v[156:159], v[216:223], v240\n\t" \
  "ds_read_b64 v[200:201], v241 offset:40\n\t" \
  "v_add_f32 v236, v236, v27\n\t" \
  "v_smfmac_f32_16x16x64_bf16 v[60:63], v[152:155], v[216:223], v240\n\t" \
  "ds_read_b64 v[202:203], v241 offset:440\n\t" \
  "v_fmac_f32 v237, v27, v27\n\t" \
  "v_smfmac_f32_16x16x64_bf16 v[84:87], v[148:151], v[216:223], v240\n\t" \
  "ds_read_b64 v[204:205], v241 offset:840\n\t" \
  "v_add_f32 v236, v236, v28\n\t" \
  "v_smfmac_f32_16x16x64_bf16 v[12:15], v[144:147], v[216:223], v240\n\t" \
  "ds_read_b64 v[206:207], v241 offset:1240\n\t" \
  "v_fmac_f32 v237, v28, v28\n\t" \
  "s_setprio 0\n\t" \
  "v_add_f32 v236, v236, v29\n\t" \
  "v_fmac_f32 v237, v29, v29\n\t" \
  "v_add_f32 v236, v236, v30\n\t" \
  "v_fmac_f32 v237, v30, v30\n\t" \
  "v_add_f32 v236, v236, v31\n\t" \
  "v_fmac_f32 v237, v31, v31\n\t" \
  "s_add_u32 %[cs], %[cs], 1\n\t" \
  "s_cmp_eq_u32 %[cs], 6\n\t" \
  "s_cselect_b32 %[cs], 0, %[cs]\n\t" \
  "s_cselect_b32 %[t0], -75920, %[ps]\n\t" \
  "v_add_u32 v241, %[t0], v241\n\t" \
  "s_waitcnt vmcnt(3)\n\t" \
  "s_waitcnt lgkmcnt(4)\n\t" \
  "s_barrier\n\t" \
  "s_setprio 3\n\t" \
  "v_smfmac_f32_16x16x64_bf16 v[40:43], v[172:175], v[192:199], v240\n\t" \
  "ds_read_b64 v[208:209], v241 offset:0\n\t" \
  "v_add_f32 v236, v236, v32\n\t" \
  "v_smfmac_f32_16x16x64_bf16 v[64:67], v[168:171], v[192:199], v240\n\t" \
  "ds_read_b64 v[210:211], v241 offset:400\n\t" \
  "v_fmac_f32 v237, v32, v32\n\t" \
  "v_smfmac_f32_16x16x64_bf16 v[88:91], v[164:167], v[192:199], v240\n\t" \
  "ds_read_b64 v[212:213], v241 offset:800\n\t" \
  "v_add_f32 v236, v236, v33\n\t" \
  "v_smfmac_f32_16x16x64_bf16 v[16:19], v[160:163], v[192:199], v240\n\t" \
  "s_waitcnt lgkmcnt(3)\n\t" \
  "ds_read_b64 v[214:215], v241 offset:1200\n\t" \
  "v_fmac_f32 v237, v33, v33\n\t" \
  "v_smfmac_f32_16x16x64_bf16 v[44:47], v[188:191], v[200:207], v240\n\t" \
  "ds_read_b64 v[216:217], v241 offset:8\n\t" \
  "v_add_f32 v236, v236, v34\n\t" \
  "v_smfmac_f32_16x16x64_bf16 v[68:71], v[184:187], v[200:207], v240\n\t" \
  "ds_read_b64 v[218:219], v241 offset:408\n\t" \
  "v_fmac_f32 v237, v34, v34\n\t" \
  "v_smfmac_f32_16x16x64_bf16 v[92:95], v[180:183], v[200:207], v240\n\t" \
  "ds_read_b64 v[220:221], v241 offset:808\n\t" \
  "v_add_f32 v236, v236, v35\n\t" \
  "v_smfmac_f32_16x16x64_bf16 v[20:23], v[176:179], v[200:207], v240\n\t" \
  "ds_read_b64 v[222:223], v241 offset:1208\n\t" \
  "v_fmac_f32 v237, v35, v35\n\t" \
  "s_setprio 0\n\t" \
  "s_mov_b32 m0, %[wld]\n\t" \
  "s_add_u32 %[t1], %[wso], %[wpd]\n\t" \
  "buffer_load_dwordx4 v245, %[wrs], %[wso] offen lds\n\t" \
  "s_add_u32 %[wso], %[wso], %[wcs]\n\t" \
  "s_cmp_eq_u32 %[wso], %[wse]\n\t" \
  "s_cselect_b32 %[wso], %[wsb], %[wso]\n\t" \
  "v_add_f32 v236, v236, v36\n\t" \
  "v_fmac_f32 v237, v36, v36\n\t" \
  "v_add_f32 v236, v236, v37\n\t" \
  "buffer_load_dwordx4 v245, %[wrs], %[t1] offen offset:1024 lds\n\t" \
  "s_add_u32 %[t1], %[t1], %[wpd]\n\t" \
  "s_add_u32 %[wld], %[wld], %[wst]\n\t" \
  "s_cmp_eq_u32 %[wld], %[wle]\n\t" \
  "s_cselect_b32 %[wld], %[wlb], %[wld]\n\t" \
  "v_fmac_f32 v237, v37, v37\n\t" \
  "v_add_f32 v236, v236, v38\n\t" \
  "buffer_load_dwordx4 v245, %[wrs], %[t1] offen offset:2048 lds\n\t" \
  "v_fmac_f32 v237, v38, v38\n\t" \
  "v_add_f32 v236, v236, v39\n\t" \
  "s_mul_i32 %[xso], %[gy], 0x18000\n\t" \
  "s_cmp_lt_u32 %[gy], 0x200\n\t" \
  "s_cselect_b32 %[xso], %[xso], 0x40000000\n\t" \
  "s_add_u32 %[gy], %[gy], %[dgy]\n\t" \
  "s_cmp_eq_u32 %[gy], %[gye]\n\t" \
  "s_cselect_b32 %[gy], 0x100000, %[gy]\n\t" \
  "s_add_u32 m0, %[xld], 0\n\t" \
  "s_nop 0\n\t" \
  "buffer_load_dwordx4 v246, %[xrs], %[xso] offen lds\n\t" \
  "v_fmac_f32 v237, v39, v39\n\t" \
  "v_add_f32 v236, v236, v40\n\t" \
  "v_fmac_f32 v237, v40, v40\n\t" \
  "s_add_u32 m0, %[xld], 1024\n\t" \
  "s_nop 0\n\t" \
  "buffer_load_dwordx4 v247, %[xrs], %[xso] offen lds\n\t" \
  "v_add_f32 v236, v236, v41\n\t" \
  "v_fmac_f32 v237, v41, v41\n\t" \
  "s_add_u32 m0, %[xld], 2048\n\t" \
  "s_nop 0\n\t" \
  "buffer_load_dwordx4 v248, %[xrs], %[xso] offen lds\n\t" \
  "v_add_f32 v236, v236, v42\n\t" \
  "v_fmac_f32 v237, v42, v42\n\t" \
  "s_add_u32 m0, %[xld], 3072\n\t" \
  "s_mov_b64 exec, %[xm]\n\t" \
  "buffer_load_dwordx4 v249, %[xrs], %[xso] offen lds\n\t" \
  "s_mov_b64 exec, -1\n\t" \
  "s_add_u32 %[xld], %[xld], 15184\n\t" \
  "s_cmp_eq_u32 %[xld], %[xle]\n\t" \
  "s_cselect_b32 %[xld], %[xlb], %[xld]\n\t" \
  "v_add_f32 v236, v236, v43\n\t" \
  "v_fmac_f32 v237, v43, v43\n\t" \
  "v_add_f32 v236, v236, v44\n\t" \
  "v_fmac_f32 v237, v44, v44\n\t" \
  "v_add_f32 v236, v236, v45\n\t" \
  "v_fmac_f32 v237, v45, v45\n\t" \
  "v_add_f32 v236, v236, v46\n\t" \
  "v_fmac_f32 v237, v46, v46\n\t" \
  "v_add_f32 v236, v236, v47\n\t" \
  "v_fmac_f32 v237, v47, v47\n\t" \
  "ds_write_b64 v243, v[236:237]\n\t" \
  "s_waitcnt vmcnt(7)\n\t" \
  "s_waitcnt lgkmcnt(0)\n\t" \
  "s_barrier\n\t" \
  "s_branch .Lcnx_f\n\t" \
  ".Lcnx_fx:\n\t" \
  "s_mov_b32 m0, %[wld]\n\t" \
  "s_add_u32 %[t0], %[wso], %[wpd]\n\t" \
  "s_add_u32 %[t1], %[t0], %[wpd]\n\t" \
  "buffer_load_dwordx4 v245, %[wrs], %[wso] offen lds\n\t" \
  "buffer_load_dwordx4 v245, %[wrs], %[t0] offen offset:1024 lds\n\t" \
  "buffer_load_dwordx4 v245, %[wrs], %[t1] offen offset:2048 lds\n\t" \
  "s_add_u32 %[wso], %[wso], %[wcs]\n\t" \
  "s_cmp_eq_u32 %[wso], %[wse]\n\t" \
  "s_cselect_b32 %[wso], %[wsb], %[wso]\n\t" \
  "s_add_u32 %[wld], %[wld], %[wst]\n\t" \
  "s_cmp_eq_u32 %[wld], %[wle]\n\t" \
  "s_cselect_b32 %[wld], %[wlb], %[wld]\n\t" \
  "v_mov_b32 v0, 0\n\t" \
  "v_mov_b32 v1, 0\n\t" \
  "ds_read_b64 v[224:225], v250 offset:6144\n\t" \
  "ds_read_b64 v[226:227], v250 offset:6656\n\t" \
  "ds_read_b64 v[228:229], v250 offset:7168\n\t" \
  "s_waitcnt lgkmcnt(0)\n\t" \
  "v_pk_max_i16 v0, v0, v224\n\t" \
  "v_pk_max_u16 v1, v1, v224\n\t" \
  "v_pk_max_i16 v0, v0, v225\n\t" \
  "v_pk_max_u16 v1, v1, v225\n\t" \
  "v_pk_max_i16 v0, v0, v226\n\t" \
  "v_pk_max_u16 v1, v1, v226\n\t" \
  "v_pk_max_i16 v0, v0, v227\n\t" \
  "v_pk_max_u16 v1, v1, v227\n\t" \
  "v_pk_max_i16 v0, v0, v228\n\t" \
  "v_pk_max_u16 v1, v1, v228\n\t" \
  "v_pk_max_i16 v0, v0, v229\n\t" \
  "v_pk_max_u16 v1, v1, v229\n\t" \
  "s_waitcnt vmcnt(3)\n\t" \
  "s_barrier\n\t" \
  "s_mov_b32 m0, %[wld]\n\t" \
  "s_add_u32 %[t0], %[wso], %[wpd]\n\t" \
  "s_add_u32 %[t1], %[t0], %[wpd]\n\t" \
  "buffer_load_dwordx4 v245, %[wrs], %[wso] offen lds\n\t" \
  "buffer_load_dwordx4 v245, %[wrs], %[t0] offen offset:1024 lds\n\t" \
  "buffer_load_dwordx4 v245, %[wrs], %[t1] offen offset:2048 lds\n\t" \
  "s_add_u32 %[wso], %[wso], %[wcs]\n\t" \
  "s_cmp_eq_u32 %[wso], %[wse]\n\t" \
  "s_cselect_b32 %[wso], %[wsb], %[wso]\n\t" \
  "s_add_u32 %[wld], %[wld], %[wst]\n\t" \
  "s_cmp_eq_u32 %[wld], %[wle]\n\t" \
  "s_cselect_b32 %[wld], %[wlb], %[wld]\n\t" \
  "ds_read_b64 v[224:225], v250 offset:12288\n\t" \
  "ds_read_b64 v[226:227], v250 offset:12800\n\t" \
  "ds_read_b64 v[228:229], v250 offset:13312\n\t" \
  "s_waitcnt lgkmcnt(0)\n\t" \
  "v_pk_max_i16 v0, v0, v224\n\t" \
  "v_pk_max_u16 v1, v1, v224\n\t" \
  "v_pk_max_i16 v0, v0, v225\n\t" \
  "v_pk_max_u16 v1, v1, v225\n\t" \
  "v_pk_max_i16 v0, v0, v226\n\t" \
  "v_pk_max_u16 v1, v1, v226\n\t" \
  "v_pk_max_i16 v0, v0, v227\n\t" \
  "v_pk_max_u16 v1, v1, v227\n\t" \
  "v_pk_max_i16 v0, v0, v228\n\t" \
  "v_pk_max_u16 v1, v1, v228\n\t" \
  "v_pk_max_i16 v0, v0, v229\n\t" \
  "v_pk_max_u16 v1, v1, v229\n\t" \
  "s_waitcnt vmcnt(3)\n\t" \
  "s_barrier\n\t" \
  "s_mov_b32 m0, %[wld]\n\t" \
  "s_add_u32 %[t0], %[wso], %[wpd]\n\t" \
  "s_add_u32 %[t1], %[t0], %[wpd]\n\t" \
  "buffer_load_dwordx4 v245, %[wrs], %[wso] offen lds\n\t" \
  "buffer_load_dwordx4 v245, %[wrs], %[t0] offen offset:1024 lds\n\t" \
  "buffer_load_dwordx4 v245, %[wrs], %[t1] offen offset:2048 lds\n\t" \
  "s_add_u32 %[wso], %[wso], %[wcs]\n\t" \
  "s_cmp_eq_u32 %[wso], %[wse]\n\t" \
  "s_cselect_b32 %[wso], %[wsb], %[wso]\n\t" \
  "s_add_u32 %[wld], %[wld], %[wst]\n\t" \
  "s_cmp_eq_u32 %[wld], %[wle]\n\t" \
  "s_cselect_b32 %[wld], %[wlb], %[wld]\n\t" \
  "ds_read_b64 v[224:225], v250 offset:18432\n\t" \
  "ds_read_b64 v[226:227], v250 offset:18944\n\t" \
  "ds_read_b64 v[228:229], v250 offset:19456\n\t" \
  "s_waitcnt lgkmcnt(0)\n\t" \
  "v_pk_max_i16 v0, v0, v224\n\t" \
  "v_pk_max_u16 v1, v1, v224\n\t" \
  "v_pk_max_i16 v0, v0, v225\n\t" \
  "v_pk_max_u16 v1, v1, v225\n\t" \
  "v_pk_max_i16 v0, v0, v226\n\t" \
  "v_pk_max_u16 v1, v1, v226\n\t" \
  "v_pk_max_i16 v0, v0, v227\n\t" \
  "v_pk_max_u16 v1, v1, v227\n\t" \
  "v_pk_max_i16 v0, v0, v228\n\t" \
  "v_pk_max_u16 v1, v1, v228\n\t" \
  "v_pk_max_i16 v0, v0, v229\n\t" \
  "v_pk_max_u16 v1, v1, v229\n\t" \
  "s_waitcnt vmcnt(3)\n\t" \
  "s_barrier\n\t" \
  "s_mov_b32 m0, %[wld]\n\t" \
  "s_add_u32 %[t0], %[wso], %[wpd]\n\t" \
  "s_add_u32 %[t1], %[t0], %[wpd]\n\t" \
  "buffer_load_dwordx4 v245, %[wrs], %[wso] offen lds\n\t" \
  "buffer_load_dwordx4 v245, %[wrs], %[t0] offen offset:1024 lds\n\t" \
  "buffer_load_dwordx4 v245, %[wrs], %[t1] offen offset:2048 lds\n\t" \
  "s_add_u32 %[wso], %[wso], %[wcs]\n\t" \
  "s_cmp_eq_u32 %[wso], %[wse]\n\t" \
  "s_cselect_b32 %[wso], %[wsb], %[wso]\n\t" \
  "s_add_u32 %[wld], %[wld], %[wst]\n\t" \
  "s_cmp_eq_u32 %[wld], %[wle]\n\t" \
  "s_cselect_b32 %[wld], %[wlb], %[wld]\n\t" \
  "ds_read_b64 v[224:225], v250 offset:0\n\t" \
  "ds_read_b64 v[226:227], v250 offset:512\n\t" \
  "ds_read_b64 v[228:229], v250 offset:1024\n\t" \
  "s_waitcnt lgkmcnt(0)\n\t" \
  "v_pk_max_i16 v0, v0, v224\n\t" \
  "v_pk_max_u16 v1, v1, v224\n\t" \
  "v_pk_max_i16 v0, v0, v225\n\t" \
  "v_pk_max_u16 v1, v1, v225\n\t" \
  "v_pk_max_i16 v0, v0, v226\n\t" \
  "v_pk_max_u16 v1, v1, v226\n\t" \
  "v_pk_max_i16 v0, v0, v227\n\t" \
  "v_pk_max_u16 v1, v1, v227\n\t" \
  "v_pk_max_i16 v0, v0, v228\n\t" \
  "v_pk_max_u16 v1, v1, v228\n\t" \
  "v_pk_max_i16 v0, v0, v229\n\t" \
  "v_pk_max_u16 v1, v1, v229\n\t" \
  "s_waitcnt vmcnt(3)\n\t" \
  "s_barrier\n\t" \
  "s_mov_b32 m0, %[wld]\n\t" \
  "s_add_u32 %[t0], %[wso], %[wpd]\n\t" \
  "s_add_u32 %[t1], %[t0], %[wpd]\n\t" \
  "buffer_load_dwordx4 v245, %[wrs], %[wso] offen lds\n\t" \
  "buffer_load_dwordx4 v245, %[wrs], %[t0] offen offset:1024 lds\n\t" \
  "buffer_load_dwordx4 v245, %[wrs], %[t1] offen offset:2048 lds\n\t" \
  "s_add_u32 %[wso], %[wso], %[wcs]\n\t" \
  "s_cmp_eq_u32 %[wso], %[wse]\n\t" \
  "s_cselect_b32 %[wso], %[wsb], %[wso]\n\t" \
  "s_add_u32 %[wld], %[wld], %[wst]\n\t" \
  "s_cmp_eq_u32 %[wld], %[wle]\n\t" \
  "s_cselect_b32 %[wld], %[wlb], %[wld]\n\t" \
  "ds_read_b64 v[224:225], v250 offset:6144\n\t" \
  "ds_read_b64 v[226:227], v250 offset:6656\n\t" \
  "ds_read_b64 v[228:229], v250 offset:7168\n\t" \
  "s_waitcnt lgkmcnt(0)\n\t" \
  "v_pk_max_i16 v0, v0, v224\n\t" \
  "v_pk_max_u16 v1, v1, v224\n\t" \
  "v_pk_max_i16 v0, v0, v225\n\t" \
  "v_pk_max_u16 v1, v1, v225\n\t" \
  "v_pk_max_i16 v0, v0, v226\n\t" \
  "v_pk_max_u16 v1, v1, v226\n\t" \
  "v_pk_max_i16 v0, v0, v227\n\t" \
  "v_pk_max_u16 v1, v1, v227\n\t" \
  "v_pk_max_i16 v0, v0, v228\n\t" \
  "v_pk_max_u16 v1, v1, v228\n\t" \
  "v_pk_max_i16 v0, v0, v229\n\t" \
  "v_pk_max_u16 v1, v1, v229\n\t" \
  "s_waitcnt vmcnt(3)\n\t" \
  "s_barrier\n\t" \
  "s_mov_b32 m0, %[wld]\n\t" \
  "s_add_u32 %[t0], %[wso], %[wpd]\n\t" \
  "s_add_u32 %[t1], %[t0], %[wpd]\n\t" \
  "buffer_load_dwordx4 v245, %[wrs], %[wso] offen lds\n\t" \
  "buffer_load_dwordx4 v245, %[wrs], %[t0] offen offset:1024 lds\n\t" \
  "buffer_load_dwordx4 v245, %[wrs], %[t1] offen offset:2048 lds\n\t" \
  "s_add_u32 %[wso], %[wso], %[wcs]\n\t" \
  "s_cmp_eq_u32 %[wso], %[wse]\n\t" \
  "s_cselect_b32 %[wso], %[wsb], %[wso]\n\t" \
  "s_add_u32 %[wld], %[wld], %[wst]\n\t" \
  "s_cmp_eq_u32 %[wld], %[wle]\n\t" \
  "s_cselect_b32 %[wld], %[wlb], %[wld]\n\t" \
  "ds_read_b64 v[224:225], v250 offset:12288\n\t" \
  "ds_read_b64 v[226:227], v250 offset:12800\n\t" \
  "ds_read_b64 v[228:229], v250 offset:13312\n\t" \
  "s_waitcnt lgkmcnt(0)\n\t" \
  "v_pk_max_i16 v0, v0, v224\n\t" \
  "v_pk_max_u16 v1, v1, v224\n\t" \
  "v_pk_max_i16 v0, v0, v225\n\t" \
  "v_pk_max_u16 v1, v1, v225\n\t" \
  "v_pk_max_i16 v0, v0, v226\n\t" \
  "v_pk_max_u16 v1, v1, v226\n\t" \
  "v_pk_max_i16 v0, v0, v227\n\t" \
  "v_pk_max_u16 v1, v1, v227\n\t" \
  "v_pk_max_i16 v0, v0, v228\n\t" \
  "v_pk_max_u16 v1, v1, v228\n\t" \
  "v_pk_max_i16 v0, v0, v229\n\t" \
  "v_pk_max_u16 v1, v1, v229\n\t" \
  "s_waitcnt vmcnt(3)\n\t" \
  "s_barrier\n\t" \
  "s_mov_b32 m0, %[wld]\n\t" \
  "s_add_u32 %[t0], %[wso], %[wpd]\n\t" \
  "s_add_u32 %[t1], %[t0], %[wpd]\n\t" \
  "buffer_load_dwordx4 v245, %[wrs], %[wso] offen lds\n\t" \
  "buffer_load_dwordx4 v245, %[wrs], %[t0] offen offset:1024 lds\n\t" \
  "buffer_load_dwordx4 v245, %[wrs], %[t1] offen offset:2048 lds\n\t" \
  "s_add_u32 %[wso], %[wso], %[wcs]\n\t" \
  "s_cmp_eq_u32 %[wso], %[wse]\n\t" \
  "s_cselect_b32 %[wso], %[wsb], %[wso]\n\t" \
  "s_add_u32 %[wld], %[wld], %[wst]\n\t" \
  "s_cmp_eq_u32 %[wld], %[wle]\n\t" \
  "s_cselect_b32 %[wld], %[wlb], %[wld]\n\t" \
  "ds_read_b64 v[224:225], v250 offset:18432\n\t" \
  "ds_read_b64 v[226:227], v250 offset:18944\n\t" \
  "ds_read_b64 v[228:229], v250 offset:19456\n\t" \
  "s_waitcnt lgkmcnt(0)\n\t" \
  "v_pk_max_i16 v0, v0, v224\n\t" \
  "v_pk_max_u16 v1, v1, v224\n\t" \
  "v_pk_max_i16 v0, v0, v225\n\t" \
  "v_pk_max_u16 v1, v1, v225\n\t" \
  "v_pk_max_i16 v0, v0, v226\n\t" \
  "v_pk_max_u16 v1, v1, v226\n\t" \
  "v_pk_max_i16 v0, v0, v227\n\t" \
  "v_pk_max_u16 v1, v1, v227\n\t" \
  "v_pk_max_i16 v0, v0, v228\n\t" \
  "v_pk_max_u16 v1, v1, v228\n\t" \
  "v_pk_max_i16 v0, v0, v229\n\t" \
  "v_pk_max_u16 v1, v1, v229\n\t" \
  "s_waitcnt vmcnt(3)\n\t" \
  "s_barrier\n\t" \
  "s_mov_b32 m0, %[wld]\n\t" \
  "s_add_u32 %[t0], %[wso], %[wpd]\n\t" \
  "s_add_u32 %[t1], %[t0], %[wpd]\n\t" \
  "buffer_load_dwordx4 v245, %[wrs], %[wso] offen lds\n\t" \
  "buffer_load_dwordx4 v245, %[wrs], %[t0] offen offset:1024 lds\n\t" \
  "buffer_load_dwordx4 v245, %[wrs], %[t1] offen offset:2048 lds\n\t" \
  "s_add_u32 %[wso], %[wso], %[wcs]\n\t" \
  "s_cmp_eq_u32 %[wso], %[wse]\n\t" \
  "s_cselect_b32 %[wso], %[wsb], %[wso]\n\t" \
  "s_add_u32 %[wld], %[wld], %[wst]\n\t" \
  "s_cmp_eq_u32 %[wld], %[wle]\n\t" \
  "s_cselect_b32 %[wld], %[wlb], %[wld]\n\t" \
  "ds_read_b64 v[224:225], v250 offset:0\n\t" \
  "ds_read_b64 v[226:227], v250 offset:512\n\t" \
  "ds_read_b64 v[228:229], v250 offset:1024\n\t" \
  "s_waitcnt lgkmcnt(0)\n\t" \
  "v_pk_max_i16 v0, v0, v224\n\t" \
  "v_pk_max_u16 v1, v1, v224\n\t" \
  "v_pk_max_i16 v0, v0, v225\n\t" \
  "v_pk_max_u16 v1, v1, v225\n\t" \
  "v_pk_max_i16 v0, v0, v226\n\t" \
  "v_pk_max_u16 v1, v1, v226\n\t" \
  "v_pk_max_i16 v0, v0, v227\n\t" \
  "v_pk_max_u16 v1, v1, v227\n\t" \
  "v_pk_max_i16 v0, v0, v228\n\t" \
  "v_pk_max_u16 v1, v1, v228\n\t" \
  "v_pk_max_i16 v0, v0, v229\n\t" \
  "v_pk_max_u16 v1, v1, v229\n\t" \
  "s_waitcnt vmcnt(3)\n\t" \
  "s_barrier\n\t" \
  "s_mov_b32 m0, %[wld]\n\t" \
  "s_add_u32 %[t0], %[wso], %[wpd]\n\t" \
  "s_add_u32 %[t1], %[t0], %[wpd]\n\t" \
  "buffer_load_dwordx4 v245, %[wrs], %[wso] offen lds\n\t" \
  "buffer_load_dwordx4 v245, %[wrs], %[t0] offen offset:1024 lds\n\t" \
  "buffer_load_dwordx4 v245, %[wrs], %[t1] offen offset:2048 lds\n\t" \
  "s_add_u32 %[wso], %[wso], %[wcs]\n\t" \
  "s_cmp_eq_u32 %[wso], %[wse]\n\t" \
  "s_cselect_b32 %[wso], %[wsb], %[wso]\n\t" \
  "s_add_u32 %[wld], %[wld], %[wst]\n\t" \
  "s_cmp_eq_u32 %[wld], %[wle]\n\t" \
  "s_cselect_b32 %[wld], %[wlb], %[wld]\n\t" \
  "ds_read_b64 v[224:225], v250 offset:6144\n\t" \
  "ds_read_b64 v[226:227], v250 offset:6656\n\t" \
  "ds_read_b64 v[228:229], v250 offset:7168\n\t" \
  "s_waitcnt lgkmcnt(0)\n\t" \
  "v_pk_max_i16 v0, v0, v224\n\t" \
  "v_pk_max_u16 v1, v1, v224\n\t" \
  "v_pk_max_i16 v0, v0, v225\n\t" \
  "v_pk_max_u16 v1, v1, v225\n\t" \
  "v_pk_max_i16 v0, v0, v226\n\t" \
  "v_pk_max_u16 v1, v1, v226\n\t" \
  "v_pk_max_i16 v0, v0, v227\n\t" \
  "v_pk_max_u16 v1, v1, v227\n\t" \
  "v_pk_max_i16 v0, v0, v228\n\t" \
  "v_pk_max_u16 v1, v1, v228\n\t" \
  "v_pk_max_i16 v0, v0, v229\n\t" \
  "v_pk_max_u16 v1, v1, v229\n\t" \
  "s_waitcnt vmcnt(3)\n\t" \
  "s_barrier\n\t" \
  "s_mov_b32 m0, %[wld]\n\t" \
  "s_add_u32 %[t0], %[wso], %[wpd]\n\t" \
  "s_add_u32 %[t1], %[t0], %[wpd]\n\t" \
  "buffer_load_dwordx4 v245, %[wrs], %[wso] offen lds\n\t" \
  "buffer_load_dwordx4 v245, %[wrs], %[t0] offen offset:1024 lds\n\t" \
  "buffer_load_dwordx4 v245, %[wrs], %[t1] offen offset:2048 lds\n\t" \
  "s_add_u32 %[wso], %[wso], %[wcs]\n\t" \
  "s_cmp_eq_u32 %[wso], %[wse]\n\t" \
  "s_cselect_b32 %[wso], %[wsb], %[wso]\n\t" \
  "s_add_u32 %[wld], %[wld], %[wst]\n\t" \
  "s_cmp_eq_u32 %[wld], %[wle]\n\t" \
  "s_cselect_b32 %[wld], %[wlb], %[wld]\n\t" \
  "ds_read_b64 v[224:225], v250 offset:12288\n\t" \
  "ds_read_b64 v[226:227], v250 offset:12800\n\t" \
  "ds_read_b64 v[228:229], v250 offset:13312\n\t" \
  "s_waitcnt lgkmcnt(0)\n\t" \
  "v_pk_max_i16 v0, v0, v224\n\t" \
  "v_pk_max_u16 v1, v1, v224\n\t" \
  "v_pk_max_i16 v0, v0, v225\n\t" \
  "v_pk_max_u16 v1, v1, v225\n\t" \
  "v_pk_max_i16 v0, v0, v226\n\t" \
  "v_pk_max_u16 v1, v1, v226\n\t" \
  "v_pk_max_i16 v0, v0, v227\n\t" \
  "v_pk_max_u16 v1, v1, v227\n\t" \
  "v_pk_max_i16 v0, v0, v228\n\t" \
  "v_pk_max_u16 v1, v1, v228\n\t" \
  "v_pk_max_i16 v0, v0, v229\n\t" \
  "v_pk_max_u16 v1, v1, v229\n\t" \
  "s_waitcnt vmcnt(3)\n\t" \
  "s_barrier\n\t" \
  "s_mov_b32 m0, %[wld]\n\t" \
  "s_add_u32 %[t0], %[wso], %[wpd]\n\t" \
  "s_add_u32 %[t1], %[t0], %[wpd]\n\t" \
  "buffer_load_dwordx4 v245, %[wrs], %[wso] offen lds\n\t" \
  "buffer_load_dwordx4 v245, %[wrs], %[t0] offen offset:1024 lds\n\t" \
  "buffer_load_dwordx4 v245, %[wrs], %[t1] offen offset:2048 lds\n\t" \
  "s_add_u32 %[wso], %[wso], %[wcs]\n\t" \
  "s_cmp_eq_u32 %[wso], %[wse]\n\t" \
  "s_cselect_b32 %[wso], %[wsb], %[wso]\n\t" \
  "s_add_u32 %[wld], %[wld], %[wst]\n\t" \
  "s_cmp_eq_u32 %[wld], %[wle]\n\t" \
  "s_cselect_b32 %[wld], %[wlb], %[wld]\n\t" \
  "ds_read_b64 v[224:225], v250 offset:18432\n\t" \
  "ds_read_b64 v[226:227], v250 offset:18944\n\t" \
  "ds_read_b64 v[228:229], v250 offset:19456\n\t" \
  "s_waitcnt lgkmcnt(0)\n\t" \
  "v_pk_max_i16 v0, v0, v224\n\t" \
  "v_pk_max_u16 v1, v1, v224\n\t" \
  "v_pk_max_i16 v0, v0, v225\n\t" \
  "v_pk_max_u16 v1, v1, v225\n\t" \
  "v_pk_max_i16 v0, v0, v226\n\t" \
  "v_pk_max_u16 v1, v1, v226\n\t" \
  "v_pk_max_i16 v0, v0, v227\n\t" \
  "v_pk_max_u16 v1, v1, v227\n\t" \
  "v_pk_max_i16 v0, v0, v228\n\t" \
  "v_pk_max_u16 v1, v1, v228\n\t" \
  "v_pk_max_i16 v0, v0, v229\n\t" \
  "v_pk_max_u16 v1, v1, v229\n\t" \
  "s_waitcnt vmcnt(3)\n\t" \
  "s_barrier\n\t" \
  "s_mov_b32 m0, %[wld]\n\t" \
  "s_add_u32 %[t0], %[wso], %[wpd]\n\t" \
  "s_add_u32 %[t1], %[t0], %[wpd]\n\t" \
  "buffer_load_dwordx4 v245, %[wrs], %[wso] offen lds\n\t" \
  "buffer_load_dwordx4 v245, %[wrs], %[t0] offen offset:1024 lds\n\t" \
  "buffer_load_dwordx4 v245, %[wrs], %[t1] offen offset:2048 lds\n\t" \
  "s_add_u32 %[wso], %[wso], %[wcs]\n\t" \
  "s_cmp_eq_u32 %[wso], %[wse]\n\t" \
  "s_cselect_b32 %[wso], %[wsb], %[wso]\n\t" \
  "s_add_u32 %[wld], %[wld], %[wst]\n\t" \
  "s_cmp_eq_u32 %[wld], %[wle]\n\t" \
  "s_cselect_b32 %[wld], %[wlb], %[wld]\n\t" \
  "ds_read_b64 v[224:225], v250 offset:0\n\t" \
  "ds_read_b64 v[226:227], v250 offset:512\n\t" \
  "ds_read_b64 v[228:229], v250 offset:1024\n\t" \
  "s_waitcnt lgkmcnt(0)\n\t" \
  "v_pk_max_i16 v0, v0, v224\n\t" \
  "v_pk_max_u16 v1, v1, v224\n\t" \
  "v_pk_max_i16 v0, v0, v225\n\t" \
  "v_pk_max_u16 v1, v1, v225\n\t" \
  "v_pk_max_i16 v0, v0, v226\n\t" \
  "v_pk_max_u16 v1, v1, v226\n\t" \
  "v_pk_max_i16 v0, v0, v227\n\t" \
  "v_pk_max_u16 v1, v1, v227\n\t" \
  "v_pk_max_i16 v0, v0, v228\n\t" \
  "v_pk_max_u16 v1, v1, v228\n\t" \
  "v_pk_max_i16 v0, v0, v229\n\t" \
  "v_pk_max_u16 v1, v1, v229\n\t" \
  "s_waitcnt vmcnt(3)\n\t" \
  "s_barrier\n\t" \
  ""
#define MS_ASM \
  "s_mul_i32 %[xso], %[gy], 0x18000\n\t" \
  "s_cmp_lt_u32 %[gy], 0x200\n\t" \
  "s_cselect_b32 %[xso], %[xso], 0x40000000\n\t" \
  "s_add_u32 %[gy], %[gy], %[dgy]\n\t" \
  "s_cmp_eq_u32 %[gy], %[gye]\n\t" \
  "s_cselect_b32 %[gy], 0x100000, %[gy]\n\t" \
  "s_add_u32 m0, %[xld], 0\n\t" \
  "s_nop 0\n\t" \
  "buffer_load_dwordx4 v246, %[xrs], %[xso] offen nt lds\n\t" \
  "s_add_u32 m0, %[xld], 1024\n\t" \
  "s_nop 0\n\t" \
  "buffer_load_dwordx4 v247, %[xrs], %[xso] offen nt lds\n\t" \
  "s_add_u32 m0, %[xld], 2048\n\t" \
  "s_nop 0\n\t" \
  "buffer_load_dwordx4 v248, %[xrs], %[xso] offen nt lds\n\t" \
  "s_add_u32 m0, %[xld], 3072\n\t" \
  "s_mov_b64 exec, %[xm]\n\t" \
  "buffer_load_dwordx4 v249, %[xrs], %[xso] offen nt lds\n\t" \
  "s_mov_b64 exec, -1\n\t" \
  "s_add_u32 %[xld], %[xld], 15184\n\t" \
  "s_cmp_eq_u32 %[xld], %[xle]\n\t" \
  "s_cselect_b32 %[xld], %[xlb], %[xld]\n\t" \
  "s_mul_i32 %[xso], %[gy], 0x18000\n\t" \
  "s_cmp_lt_u32 %[gy], 0x200\n\t" \
  "s_cselect_b32 %[xso], %[xso], 0x40000000\n\t" \
  "s_add_u32 %[gy], %[gy], %[dgy]\n\t" \
  "s_cmp_eq_u32 %[gy], %[gye]\n\t" \
  "s_cselect_b32 %[gy], 0x100000, %[gy]\n\t" \
  "s_add_u32 m0, %[xld], 0\n\t" \
  "s_nop 0\n\t" \
  "buffer_load_dwordx4 v246, %[xrs], %[xso] offen nt lds\n\t" \
  "s_add_u32 m0, %[xld], 1024\n\t" \
  "s_nop 0\n\t" \
  "buffer_load_dwordx4 v247, %[xrs], %[xso] offen nt lds\n\t" \
  "s_add_u32 m0, %[xld], 2048\n\t" \
  "s_nop 0\n\t" \
  "buffer_load_dwordx4 v248, %[xrs], %[xso] offen nt lds\n\t" \
  "s_add_u32 m0, %[xld], 3072\n\t" \
  "s_mov_b64 exec, %[xm]\n\t" \
  "buffer_load_dwordx4 v249, %[xrs], %[xso] offen nt lds\n\t" \
  "s_mov_b64 exec, -1\n\t" \
  "s_add_u32 %[xld], %[xld], 15184\n\t" \
  "s_cmp_eq_u32 %[xld], %[xle]\n\t" \
  "s_cselect_b32 %[xld], %[xlb], %[xld]\n\t" \
  "s_waitcnt vmcnt(8)\n\t" \
  "s_barrier\n\t" \
  "s_mul_i32 %[xso], %[gy], 0x18000\n\t" \
  "s_cmp_lt_u32 %[gy], 0x200\n\t" \
  "s_cselect_b32 %[xso], %[xso], 0x40000000\n\t" \
  "s_add_u32 %[gy], %[gy], %[dgy]\n\t" \
  "s_cmp_eq_u32 %[gy], %[gye]\n\t" \
  "s_cselect_b32 %[gy], 0x100000, %[gy]\n\t" \
  "s_add_u32 m0, %[xld], 0\n\t" \
  "s_nop 0\n\t" \
  "buffer_load_dwordx4 v246, %[xrs], %[xso] offen nt lds\n\t" \
  "s_add_u32 m0, %[xld], 1024\n\t" \
  "s_nop 0\n\t" \
  "buffer_load_dwordx4 v247, %[xrs], %[xso] offen nt lds\n\t" \
  "s_add_u32 m0, %[xld], 2048\n\t" \
  "s_nop 0\n\t" \
  "buffer_load_dwordx4 v248, %[xrs], %[xso] offen nt lds\n\t" \
  "s_add_u32 m0, %[xld], 3072\n\t" \
  "s_mov_b64 exec, %[xm]\n\t" \
  "buffer_load_dwordx4 v249, %[xrs], %[xso] offen nt lds\n\t" \
  "s_mov_b64 exec, -1\n\t" \
  "s_add_u32 %[xld], %[xld], 15184\n\t" \
  "s_cmp_eq_u32 %[xld], %[xle]\n\t" \
  "s_cselect_b32 %[xld], %[xlb], %[xld]\n\t" \
  "s_mul_i32 %[xso], %[gy], 0x18000\n\t" \
  "s_cmp_lt_u32 %[gy], 0x200\n\t" \
  "s_cselect_b32 %[xso], %[xso], 0x40000000\n\t" \
  "s_add_u32 %[gy], %[gy], %[dgy]\n\t" \
  "s_cmp_eq_u32 %[gy], %[gye]\n\t" \
  "s_cselect_b32 %[gy], 0x100000, %[gy]\n\t" \
  "s_add_u32 m0, %[xld], 0\n\t" \
  "s_nop 0\n\t" \
  "buffer_load_dwordx4 v246, %[xrs], %[xso] offen nt lds\n\t" \
  "s_add_u32 m0, %[xld], 1024\n\t" \
  "s_nop 0\n\t" \
  "buffer_load_dwordx4 v247, %[xrs], %[xso] offen nt lds\n\t" \
  "s_add_u32 m0, %[xld], 2048\n\t" \
  "s_nop 0\n\t" \
  "buffer_load_dwordx4 v248, %[xrs], %[xso] offen nt lds\n\t" \
  "s_add_u32 m0, %[xld], 3072\n\t" \
  "s_mov_b64 exec, %[xm]\n\t" \
  "buffer_load_dwordx4 v249, %[xrs], %[xso] offen nt lds\n\t" \
  "s_mov_b64 exec, -1\n\t" \
  "s_add_u32 %[xld], %[xld], 15184\n\t" \
  "s_cmp_eq_u32 %[xld], %[xle]\n\t" \
  "s_cselect_b32 %[xld], %[xlb], %[xld]\n\t" \
  "s_mov_b32 m0, %[wld]\n\t" \
  "s_add_u32 %[t0], %[wso], 0x2c00\n\t" \
  "s_add_u32 %[t1], %[t0], 0x2c00\n\t" \
  "buffer_load_dwordx4 v245, %[wrs], %[wso] offen lds\n\t" \
  "buffer_load_dwordx4 v245, %[wrs], %[t0] offen offset:1024 lds\n\t" \
  "buffer_load_dwordx4 v245, %[wrs], %[t1] offen offset:2048 lds\n\t" \
  "s_mul_i32 %[xso], %[gy], 0x18000\n\t" \
  "s_cmp_lt_u32 %[gy], 0x200\n\t" \
  "s_cselect_b32 %[xso], %[xso], 0x40000000\n\t" \
  "s_add_u32 %[gy], %[gy], %[dgy]\n\t" \
  "s_cmp_eq_u32 %[gy], %[gye]\n\t" \
  "s_cselect_b32 %[gy], 0x100000, %[gy]\n\t" \
  "s_add_u32 m0, %[xld], 0\n\t" \
  "s_nop 0\n\t" \
  "buffer_load_dwordx4 v246, %[xrs], %[xso] offen nt lds\n\t" \
  "s_add_u32 m0, %[xld], 1024\n\t" \
  "s_nop 0\n\t" \
  "buffer_load_dwordx4 v247, %[xrs], %[xso] offen nt lds\n\t" \
  "s_add_u32 m0, %[xld], 2048\n\t" \
  "s_nop 0\n\t" \
  "buffer_load_dwordx4 v248, %[xrs], %[xso] offen nt lds\n\t" \
  "s_add_u32 m0, %[xld], 3072\n\t" \
  "s_mov_b64 exec, %[xm]\n\t" \
  "buffer_load_dwordx4 v249, %[xrs], %[xso] offen nt lds\n\t" \
  "s_mov_b64 exec, -1\n\t" \
  "s_add_u32 %[xld], %[xld], 15184\n\t" \
  "s_cmp_eq_u32 %[xld], %[xle]\n\t" \
  "s_cselect_b32 %[xld], %[xlb], %[xld]\n\t" \
  "s_waitcnt vmcnt(19)\n\t" \
  "s_barrier\n\t" \
  "s_mul_i32 %[xso], %[gy], 0x18000\n\t" \
  "s_cmp_lt_u32 %[gy], 0x200\n\t" \
  "s_cselect_b32 %[xso], %[xso], 0x40000000\n\t" \
  "s_add_u32 %[gy], %[gy], %[dgy]\n\t" \
  "s_cmp_eq_u32 %[gy], %[gye]\n\t" \
  "s_cselect_b32 %[gy], 0x100000, %[gy]\n\t" \
  "s_add_u32 m0, %[xld], 0\n\t" \
  "s_nop 0\n\t" \
  "buffer_load_dwordx4 v246, %[xrs], %[xso] offen nt lds\n\t" \
  "s_add_u32 m0, %[xld], 1024\n\t" \
  "s_nop 0\n\t" \
  "buffer_load_dwordx4 v247, %[xrs], %[xso] offen nt lds\n\t" \
  "s_add_u32 m0, %[xld], 2048\n\t" \
  "s_nop 0\n\t" \
  "buffer_load_dwordx4 v248, %[xrs], %[xso] offen nt lds\n\t" \
  "s_add_u32 m0, %[xld], 3072\n\t" \
  "s_mov_b64 exec, %[xm]\n\t" \
  "buffer_load_dwordx4 v249, %[xrs], %[xso] offen nt lds\n\t" \
  "s_mov_b64 exec, -1\n\t" \
  "s_add_u32 %[xld], %[xld], 15184\n\t" \
  "s_cmp_eq_u32 %[xld], %[xle]\n\t" \
  "s_cselect_b32 %[xld], %[xlb], %[xld]\n\t" \
  "s_waitcnt vmcnt(19)\n\t" \
  "s_barrier\n\t" \
  "s_waitcnt vmcnt(15)\n\t" \
  "s_barrier\n\t" \
  "s_waitcnt vmcnt(11)\n\t" \
  "s_barrier\n\t" \
  "s_waitcnt vmcnt(4)\n\t" \
  "s_barrier\n\t" \
  ""
#define M_CLOB "v0", "v1", "v2", "v3", "v4", "v5", "v6", "v7", "v8", "v9", "v10", "v11", "v12", "v13", "v14", "v15", "v16", "v17", "v18", "v19", "v20", "v21", "v22", "v23", "v24", "v25", "v26", "v27", "v28", "v29", "v30", "v31", "v32", "v33", "v34", "v35", "v36", "v37", "v38", "v39", "v40", "v41", "v42", "v43", "v44", "v45", "v46", "v47", "v48", "v49", "v50", "v51", "v52", "v53", "v54", "v55", "v56", "v57", "v58", "v59", "v60", "v61", "v62", "v63", "v64", "v65", "v66", "v67", "v68", "v69", "v70", "v71", "v72", "v73", "v74", "v75", "v76", "v77", "v78", "v79", "v80", "v81", "v82", "v83", "v84", "v85", "v86", "v87", "v88", "v89", "v90", "v91", "v92", "v93", "v94", "v95", "v96", "v97", "v98", "v99", "v100", "v101", "v102", "v103", "v104", "v105", "v106", "v107", "v108", "v109", "v110", "v111", "v112", "v113", "v114", "v115", "v116", "v117", "v118", "v119", "v120", "v121", "v122", "v123", "v124", "v125", "v126", "v127", "v128", "v129", "v130", "v131", "v132", "v133", "v134", "v135", "v136", "v137", "v138", "v139", "v140", "v141", "v142", "v143", "v144", "v145", "v146", "v147", "v148", "v149", "v150", "v151", "v152", "v153", "v154", "v155", "v156", "v157", "v158", "v159", "v160", "v161", "v162", "v163", "v164", "v165", "v166", "v167", "v168", "v169", "v170", "v171", "v172", "v173", "v174", "v175", "v176", "v177", "v178", "v179", "v180", "v181", "v182", "v183", "v184", "v185", "v186", "v187", "v188", "v189", "v190", "v191", "v192", "v193", "v194", "v195", "v196", "v197", "v198", "v199", "v200", "v201", "v202", "v203", "v204", "v205", "v206", "v207", "v208", "v209", "v210", "v211", "v212", "v213", "v214", "v215", "v216", "v217", "v218", "v219", "v220", "v221", "v222", "v223", "v224", "v225", "v226", "v227", "v228", "v229", "v230", "v231", "v232", "v233", "v234", "v235", "v236", "v237", "v238", "v239", "v240", "v241", "v242", "v243", "v244", "v245"
#define F_CLOB "v2", "v3", "v4", "v5", "v6", "v7", "v8", "v9", "v10", "v11", "v12", "v13", "v14", "v15", "v16", "v17", "v18", "v19", "v20", "v21", "v22", "v23", "v24", "v25", "v26", "v27", "v28", "v29", "v30", "v31", "v32", "v33", "v34", "v35", "v36", "v37", "v38", "v39", "v40", "v41", "v42", "v43", "v44", "v45", "v46", "v47", "v48", "v49", "v50", "v51", "v52", "v53", "v54", "v55", "v56", "v57", "v58", "v59", "v60", "v61", "v62", "v63", "v64", "v65", "v66", "v67", "v68", "v69", "v70", "v71", "v72", "v73", "v74", "v75", "v76", "v77", "v78", "v79", "v80", "v81", "v82", "v83", "v84", "v85", "v86", "v87", "v88", "v89", "v90", "v91", "v92", "v93", "v94", "v95", "v96", "v97", "v98", "v99", "v100", "v101", "v102", "v103", "v104", "v105", "v106", "v107", "v108", "v109", "v110", "v111", "v112", "v113", "v114", "v115", "v116", "v117", "v118", "v119", "v120", "v121", "v122", "v123", "v124", "v125", "v126", "v127", "v128", "v129", "v130", "v131", "v132", "v133", "v134", "v135", "v136", "v137", "v138", "v139", "v140", "v141", "v142", "v143", "v144", "v145", "v146", "v147", "v148", "v149", "v150", "v151", "v152", "v153", "v154", "v155", "v156", "v157", "v158", "v159", "v160", "v161", "v162", "v163", "v164", "v165", "v166", "v167", "v168", "v169", "v170", "v171", "v172", "v173", "v174", "v175", "v176", "v177", "v178", "v179", "v180", "v181", "v182", "v183", "v184", "v185", "v186", "v187", "v188", "v189", "v190", "v191", "v192", "v193", "v194", "v195", "v196", "v197", "v198", "v199", "v200", "v201", "v202", "v203", "v204", "v205", "v206", "v207", "v208", "v209", "v210", "v211", "v212", "v213", "v214", "v215", "v216", "v217", "v218", "v219", "v220", "v221", "v222", "v223", "v224", "v225", "v226", "v227", "v228", "v229", "v230", "v231", "v232", "v233", "v234", "v235", "v236", "v237", "v238", "v239", "v252", "v253"


__device__ __forceinline__ unsigned lds_addr(const char* p) { return (unsigned)(uintptr_t)(lds_ptr_t)p; }
__device__ __forceinline__ u32x4 make_rsrc(const void* p, unsigned bytes) {
    const uint64_t a = (uint64_t)(uintptr_t)p;
    u32x4 r = {(unsigned)a, (unsigned)(a >> 32) & 0xffffu, bytes, 0x00020000u};
    return r;
}
__device__ __forceinline__ void dma16(u32x4 rsrc, unsigned voff, unsigned lds_dst) {
    asm volatile("s_mov_b32 m0, %1\n\ts_nop 0\n\tbuffer_load_dwordx4 %0, %2, 0 offen lds"
                 :: "v"(voff), "s"(lds_dst), "s"(rsrc) : "memory");
}
__device__ __forceinline__ void warm(u32x4 rsrc, unsigned voff, unsigned soff, unsigned lds_dst) {
    asm volatile("s_mov_b32 m0, %1\n\ts_nop 0\n\tbuffer_load_dword %0, %2, %3 offen lds"
                 :: "v"(voff), "s"(lds_dst), "s"(rsrc), "s"(soff) : "memory");
}
__device__ __forceinline__ int lane_id() {
    int v;
    asm volatile("v_mbcnt_lo_u32_b32 %0, -1, 0\n\tv_mbcnt_hi_u32_b32 %0, -1, %0" : "=v"(v));
    return v;
}
__device__ __forceinline__ void bar() { asm volatile("s_waitcnt lgkmcnt(0)\n\ts_barrier" ::: "memory"); }
__device__ __forceinline__ float bf2f(unsigned short h) { return __builtin_bit_cast(float, (unsigned)h << 16); }
__device__ __forceinline__ unsigned short f2bf(float f) { return __builtin_bit_cast(unsigned short, (__bf16)f); }
__device__ __forceinline__ float gelu_f(float v) {
    const float e = __builtin_amdgcn_exp2f(v * __builtin_fmaf(v * v, -0.10294324f, -2.3022082f));
    return v * __builtin_amdgcn_rcpf(1.0f + e);
}

__device__ __forceinline__ void plain_block(const unsigned short* x, const unsigned short* dw, const unsigned short* w1,
                                            const unsigned short* w2, unsigned short* out, char* smem, int x0, int y0, int tid) {
    float* zs = (float*)smem;
    float* gs = zs + 64 * CH;
#pragma unroll 1
    for (int u = 0; u < 16; ++u) {
#pragma unroll 1
        for (int i = tid; i < 64 * CH; i += 512) {
            const int p = i / CH, c = i - p * CH, y = y0 + 2 * u + (p >> 5), xx = x0 + (p & 31);
            float a = 0.f;
#pragma unroll 1
            for (int t = 0; t < 49; ++t) {
                const int ky = t / 7, yy = y + ky - 3, xj = xx + (t - 7 * ky) - 3;
                if ((unsigned)yy < (unsigned)IMG && (unsigned)xj < (unsigned)IMG)
                    a = __builtin_fmaf(bf2f(x[(yy * IMG + xj) * CH + c]), bf2f(dw[t * CH + c]), a);
            }
            zs[i] = a;
        }
        __syncthreads();
        if (tid < 64) {
            float m = 0.f, v = 0.f;
#pragma unroll 1
            for (int c = 0; c < CH; ++c) m += zs[tid * CH + c];
            m *= 1.0f / CH;
#pragma unroll 1
            for (int c = 0; c < CH; ++c) { const float d = zs[tid * CH + c] - m; v = __builtin_fmaf(d, d, v); }
            const float r = __builtin_amdgcn_rsqf(v * (1.0f / CH) + 1e-5f);
#pragma unroll 1
            for (int c = 0; c < CH; ++c) zs[tid * CH + c] = bf2f(f2bf((zs[tid * CH + c] - m) * r));
        }
        __syncthreads();
#pragma unroll 1
        for (int i = tid; i < 64 * HID; i += 512) {
            const int p = i / HID, n = i - p * HID;
            float a = 0.f;
#pragma unroll 1
            for (int c = 0; c < CH; ++c) a = __builtin_fmaf(zs[p * CH + c], bf2f(w1[c * HID + n]), a);
            gs[i] = bf2f(f2bf(gelu_f(a)));
        }
        __syncthreads();
#pragma unroll 1
        for (int i = tid; i < 64 * CH; i += 512) {
            const int p = i / CH, c = i - p * CH, o = ((y0 + 2 * u + (p >> 5)) * IMG + x0 + (p & 31)) * CH + c;
            float a = bf2f(x[o]);
#pragma unroll 1
            for (int n = 0; n < HID; ++n) a = __builtin_fmaf(gs[p * HID + n], bf2f(w2[n * CH + c]), a);
            out[o] = f2bf(a);
        }
    }
}

__global__ __launch_bounds__(512, 2) void convnext_kernel(const __hip_bfloat16* __restrict__ x,
                                                          const __hip_bfloat16* __restrict__ dw,
                                                          const __hip_bfloat16* __restrict__ w1,
                                                          const __hip_bfloat16* __restrict__ w2,
                                                          __hip_bfloat16* __restrict__ out) {
    __shared__ __attribute__((aligned(16))) char smem[LDS_BYTES];
    unsigned long long pc;
    unsigned len;
    asm volatile("s_getpc_b64 %0\n.Lcnx_pc:\n\ts_mov_b32 %1, .Lcnx_end-.Lcnx_pc" : "=s"(pc), "=s"(len));
    const int tid = threadIdx.x;
    const int l = tid & 63;
    const int wid = __builtin_amdgcn_readfirstlane(tid >> 6);
    const int f = wid & 3;
    const int bid = blockIdx.x;
    const int x0 = ((bid >> 3) & 15) * 32;
    const int down = bid >> 7;
    const int y0 = (2 * (bid & 7) + down) * 32;
    const int dir = 2 * down - 1;
    const int yb = down ? y0 - 3 : y0 + 34;
    const unsigned sb = lds_addr(smem);


    const u32x4 xrs = make_rsrc(x, IMG * ROWB);
    const unsigned xlb = sb + XOFF + (f >> 1) * 7592 + (f & 1) * 4096, xle = xlb + 6 * PS;
    const unsigned gy0 = (unsigned)(yb + dir * (f >> 1)), gye = gy0 + (unsigned)(38 * dir);
    const unsigned long long xm = (f & 1) ? (1ull << 26) - 1 : ~0ull;
    auto x_lane = [&](int k) {
        const int L = 64 * (4 * (f & 1) + k) + l;
        const int pp = L / 25, s = L - pp * 25;
        const int gx = x0 - 3 + 2 * pp + (s >= 12);
        return (s < 24 && pp < 19 && (unsigned)gx < (unsigned)IMG) ? (unsigned)((x0 - 3 + 2 * pp) * (CH * 2) + s * 16) : OOB;
    };
    auto w1_lane = [&]() {
        const int pos = (l >> 1) & 15, c16 = ((pos >> 2) & 1) * 8 + (pos >> 3) * 4 + (pos & 3);
        return (unsigned)(c16 * (HID * 2) + (l & 1) * 32 + (l >> 5) * 16);
    };
#define X_IN "{v246}"(x_lane(0)), "{v247}"(x_lane(1)), "{v248}"(x_lane(2)), "{v249}"(x_lane(3)), [xrs] "s"(xrs), [xlb] "s"(xlb), \
             [xle] "s"(xle), [dgy] "s"(2 * dir), [gye] "s"(gye), [xm] "s"(xm)

    if (wid >= 4) {
        {
#pragma unroll 1
            for (int i = tid - 256; i < LUTN / 2; i += 256) {
                float gp[2], gn[2];
#pragma unroll
                for (int j = 0; j < 2; ++j) {
                    const float v = (i | j) ? __builtin_bit_cast(float, (unsigned)(2 * i + j) << 16 | 0x8000u) * (0x1p111f / DL) : 0.f;
                    const float e = __builtin_amdgcn_exp2f(v * __builtin_fmaf(v * v, -0.10294324f, -2.3022082f));
                    gp[j] = v * __builtin_amdgcn_rcpf(1.0f + e);
                    gn[j] = -gp[j] * e;
                }
                *(unsigned*)(smem + i * 4) = f2bf(gp[0]) | (unsigned)f2bf(gp[1]) << 16;
                *(unsigned*)(smem + 65536 + i * 4) = f2bf(gn[0]) | (unsigned)f2bf(gn[1]) << 16;
            }
        }
        const bool isw1 = f < 2;
        const u32x4 wrs = make_rsrc(isw1 ? w1 : w2, CH * HID * 2);
        const unsigned wv = isw1 ? w1_lane() : (unsigned)(l * 16);
        const unsigned wpd = isw1 ? 16 * HID * 2 - 1024 : 0, wcs = isw1 ? 64 : 6144;
        const unsigned wsb = (f & 1) * (isw1 ? 48 * HID * 2 : 3072), wse = wsb + 12 * wcs;
        const unsigned wst = isw1 ? 6144 : 6160;
        const unsigned wlb = sb + (isw1 ? W1OFF + (f & 1) * 3072 : W2OFF + (f & 1) * 3080), wle = wlb + (isw1 ? 4 : 3) * wst;
        unsigned wso = wsb + (isw1 ? 128 : 0), wld = wlb + 2 * wst;
        unsigned xld = xlb, xso, cs = 0, t0, t1, lc, gy = gy0 + (unsigned)(12 * dir);
        const int jx = (l >> 4) & 1, jy = l >> 5, xg = l & 15;
        unsigned cb = sb + XOFF + jx * 7592 + xg * 400 + jy * 192 + f * 48;
        const unsigned zw = sb + ZOFF + (jy * 32 + (xg >> 3) * 16 + jx * 8 + (xg & 7)) * 208 + f * 48;
        const unsigned aa = sb + W2OFF + f * 4096 + ((1 - ((l >> 3) & 1)) * 8 + 4 * jy + jx - ((l >> 2) & 1) + 1) * 48 + (l & 3) * 2;
        unsigned m0, m1;

        asm volatile(F_ASM
                     : "={v0}"(m0), "={v1}"(m1), "+{v241}"(cb), [wso] "+&s"(wso), [wld] "+&s"(wld), [xld] "+&s"(xld), [gy] "+&s"(gy),
                       [cs] "+&s"(cs), [xso] "=&s"(xso), [t0] "=&s"(t0), [t1] "=&s"(t1), [lc] "=&s"(lc)
                     : "{v240}"((l & 3) * 0x5555u), "{v242}"(zw), "{v243}"(sb + STOFF + (f >> 1) * 1024 + l * 16 + (f & 1) * 8),
                       "{v244}"(sb + STOFF + l * 16), "{v245}"(wv), "{v250}"(sb + W1OFF + f * 1536 + l * 8), "{v251}"(aa), X_IN,
                       [wrs] "s"(wrs), [wpd] "s"(wpd), [wcs] "s"(wcs), [wsb] "s"(wsb), [wse] "s"(wse), [wst] "s"(wst), [wlb] "s"(wlb),
                       [wle] "s"(wle), [ps] "s"(PS), [gl] "s"(0x1p-111f * DL), [gt] "s"(CT)
                     : "memory", "scc", F_CLOB);
        const unsigned a = max(m0 & 0xffffu, m0 >> 16), b = max(m1 & 0x7fffu, (m1 >> 16) & 0x7fffu), mx = max(a, b);
        const unsigned fl = (__builtin_amdgcn_ballot_w64(mx >= 0x4180u) ? 1u : 0u) | (__builtin_amdgcn_ballot_w64(mx >= 0x3900u) ? 2u : 0u);
        *(unsigned*)(smem + FLOFF + f * 4) = fl;
        asm volatile("s_waitcnt vmcnt(0)" ::: "memory");
        bar();

    } else {
        {
            const u32x4 drs = make_rsrc(dw, 49 * CH * 2);
#pragma unroll
            for (int k = 0; k < 4; ++k) {
                const int L = 64 * k + l, cell = L / 3, s = L - cell * 3, ky = (cell >> 3) - 1, kx = (cell & 7) - 1;
                const unsigned o = ((unsigned)ky < 7u && kx >= 0) ? (unsigned)(((down ? ky : 6 - ky) * 7 + kx) * 192 + f * 48 + s * 16) : OOB;
                dma16(drs, o, sb + W2OFF + f * 4096 + k * 1024);
            }
            if (f == 0) warm(make_rsrc((const void*)pc, len & ~3u), l < 32 ? l * 64 : OOB, (bid >> 3) * 2048, sb + PFOFF);
            if (f >= 2) warm(make_rsrc(f & 1 ? w1 : w2, CH * HID * 2), l < 36 ? l * 64 : OOB, (bid >> 3) * 2304, sb + PFOFF);
            unsigned xld = xlb, gy = gy0, xso, t0, t1;
            asm volatile(MS_ASM
                         : [xld] "+&s"(xld), [gy] "+&s"(gy), [xso] "=&s"(xso), [t0] "=&s"(t0), [t1] "=&s"(t1)
                         : "{v245}"(w1_lane()), X_IN, [wrs] "s"(make_rsrc(w1, CH * HID * 2)),
                           [wso] "s"((f & 1) * (48 * HID * 2) + (f >> 1) * 64), [wld] "s"(sb + W1OFF + f * 3072)
                         : "memory", "scc");
        }
        const int q = l >> 4, i = l & 15, r4 = i >> 2, c4 = i & 3, wy = wid >> 1, wx = wid & 1;
        const unsigned a1 = sb + W1OFF + (q >> 1) * 1024 + ((q & 1) * 4 + r4) * 32 + c4 * 8;
        const unsigned a2 = sb + W2OFF + (q & 1) * 3080 + ((q >> 1) * 8 + r4) * 192 + c4 * 16;
        const unsigned zr = sb + ZOFF + (16 * wid + i) * 208 + q * 16;
        unsigned rr = sb + XOFF + (1 + wy) * PS + (1 - wy) * 7592 + (8 * wx + (i & 7) + 1 + (i >> 3)) * 400 + (1 - (i >> 3)) * 192 + q * 16;
        const unsigned vo = (16 * wx + 2 * (i & 7) + (i >> 3)) * 192 + q * 16;
        const unsigned one = q == r4 ? 0x3f80u << (16 * (c4 & 1)) : 0u;
        const u32x2 id = {c4 < 2 ? one : 0u, c4 < 2 ? 0u : one};
        unsigned ob = (unsigned)((yb + dir * (3 + wy)) * ROWB + x0 * (CH * 2)), rs = 1 + wy, t0, lc;
        asm volatile(M_ASM
                     : "+{v252}"(rr), [ob] "+&s"(ob), [rs] "+&s"(rs), [t0] "=&s"(t0), [lc] "=&s"(lc)
                     : "{v[246:247]}"(id), "{v248}"(0xddddddddu), "{v249}"(a1), "{v250}"(a2), "{v251}"(zr), "{v253}"(vo),
                       [osrd] "s"(make_rsrc(out, IMG * ROWB)), [dob] "s"(2 * dir * ROWB), [ps] "s"(PS), [h1] "s"(GH1), [hc] "s"(GHC),
                       [kk] "s"(0x1p111f * CT / DL)
                     : "memory", "scc", M_CLOB);

    }
    asm volatile(".Lcnx_end:" ::: "memory");
    const u32x4 fl = *(const u32x4*)(smem + FLOFF);
    if (__builtin_amdgcn_readfirstlane(fl[0] | fl[1] | fl[2] | fl[3]) != 2) {
        asm volatile("s_waitcnt vmcnt(0)" ::: "memory");
        __syncthreads();
        plain_block((const unsigned short*)x, (const unsigned short*)dw, (const unsigned short*)w1, (const unsigned short*)w2,
                    (unsigned short*)out, smem, x0, y0, wid * 64 + lane_id());
    }
}

}

extern "C" void kernel_launch(void* const* d_in, const int* in_sizes, int n_in, void* d_out, int out_size,
                              void* d_ws, size_t ws_size, hipStream_t stream) {
    (void)in_sizes; (void)n_in; (void)out_size; (void)d_ws; (void)ws_size;
    const __hip_bfloat16* x = (const __hip_bfloat16*)d_in[0];
    const __hip_bfloat16* dw = (const __hip_bfloat16*)d_in[1];
    const __hip_bfloat16* w1 = (const __hip_bfloat16*)d_in[2];
    const __hip_bfloat16* w2 = (const __hip_bfloat16*)d_in[3];
    __hip_bfloat16* out = (__hip_bfloat16*)d_out;
    convnext_kernel<<<256, 512, 0, stream>>>(x, dw, w1, w2, out);
}
```

```cpp
#include <hip/hip_runtime.h>
#include <hip/hip_bf16.h>
#include <stdint.h>

namespace {

typedef __attribute__((ext_vector_type(4))) unsigned u32x4;
typedef __attribute__((ext_vector_type(2))) unsigned u32x2;
typedef __attribute__((address_space(3))) void* lds_ptr_t;

constexpr int IMG = 512;
constexpr int CH = 96;
constexpr int HID = 384;
constexpr int ROWB = IMG * CH * 2;
constexpr int LUTN = 24 * 128;
constexpr int W1OFF = LUTN * 2;
constexpr int W2OFF = W1OFF + 3 * 6144;
constexpr int DWOFF = W2OFF + 2 * 6160;
constexpr int ZOFF = W2OFF + 4 * 6160;
constexpr int STOFF = ZOFF + 12288;
constexpr int FLOFF = STOFF + 2048;
constexpr int XOFF = 65536 + LUTN * 2;
constexpr int PS = 15184;
constexpr int PFOFF = XOFF + 6 * PS;
constexpr int LDS_BYTES = 160 * 1024;
static_assert(FLOFF + 16 <= 65536 && DWOFF + 20640 <= 65536 && PFOFF + 256 <= LDS_BYTES, "LDS map");
constexpr unsigned OOB = 0x40000000u;

constexpr float DL = 1.0f + 1.0f / 362.0f;
constexpr float CT = 0.46866869f;
constexpr unsigned GH1 = 0xc4eac4eau;
constexpr unsigned GHC = 0x377a377au;

#define M_ASM \
  "s_setprio 0\n\t" \
  "s_waitcnt vmcnt(10)\n\t" \
  "s_barrier\n\t" \
  "ds_read_b128 v[96:99], v251 offset:0\n\t" \
  "ds_read_b128 v[100:103], v251 offset:4096\n\t" \
  "ds_read_b128 v[104:107], v251 offset:8192\n\t" \
  "ds_read_b64_tr_b16 v[208:209], v249 offset:0\n\t" \
  "ds_read_b64_tr_b16 v[210:211], v249 offset:256\n\t" \
  "ds_read_b64_tr_b16 v[212:213], v249 offset:2048\n\t" \
  "ds_read_b64_tr_b16 v[214:215], v249 offset:2304\n\t" \
  "ds_read_b64_tr_b16 v[216:217], v249 offset:4096\n\t" \
  "ds_read_b64_tr_b16 v[218:219], v249 offset:4352\n\t" \
  "ds_read_b64_tr_b16 v[220:221], v249 offset:512\n\t" \
  "ds_read_b64_tr_b16 v[222:223], v249 offset:768\n\t" \
  "s_waitcnt lgkmcnt(6)\n\t" \
  "v_mfma_f32_16x16x32_bf16 v[144:147], v[208:211], v[96:99], 0\n\t" \
  "ds_read_b64_tr_b16 v[208:209], v249 offset:2560\n\t" \
  "ds_read_b64_tr_b16 v[210:211], v249 offset:2816\n\t" \
  "s_waitcnt lgkmcnt(6)\n\t" \
  "v_mfma_f32_16x16x32_bf16 v[144:147], v[212:215], v[100:103], v[144:147]\n\t" \
  "ds_read_b64_tr_b16 v[212:213], v249 offset:4608\n\t" \
  "ds_read_b64_tr_b16 v[214:215], v249 offset:4864\n\t" \
  "s_waitcnt lgkmcnt(6)\n\t" \
  "v_mfma_f32_16x16x32_bf16 v[144:147], v[216:219], v[104:107], v[144:147]\n\t" \
  "s_waitcnt lgkmcnt(0)\n\t" \
  "s_waitcnt vmcnt(7)\n\t" \
  "s_barrier\n\t" \
  "ds_read_b64_tr_b16 v[216:217], v249 offset:6144\n\t" \
  "ds_read_b64_tr_b16 v[218:219], v249 offset:6400\n\t" \
  "v_mfma_f32_16x16x32_bf16 v[148:151], v[220:223], v[96:99], 0\n\t" \
  "ds_read_b64_tr_b16 v[220:221], v249 offset:8192\n\t" \
  "ds_read_b64_tr_b16 v[222:223], v249 offset:8448\n\t" \
  "s_nop 0\n\t" \
  "v_lshlrev_b32_sdwa v144, 1, v144 dst_sel:DWORD dst_unused:UNUSED_PAD src0_sel:DWORD src1_sel:WORD_1\n\t" \
  "v_mfma_f32_16x16x32_bf16 v[148:151], v[208:211], v[100:103], v[148:151]\n\t" \
  "ds_read_b64_tr_b16 v[208:209], v249 offset:10240\n\t" \
  "ds_read_b64_tr_b16 v[210:211], v249 offset:10496\n\t" \
  "ds_read_u16_d16_hi v144, v144 offset:0\n\t" \
  "v_lshlrev_b32_sdwa v145, 1, v145 dst_sel:DWORD dst_unused:UNUSED_PAD src0_sel:DWORD src1_sel:WORD_1\n\t" \
  "v_mfma_f32_16x16x32_bf16 v[148:151], v[212:215], v[104:107], v[148:151]\n\t" \
  "ds_read_b64_tr_b16 v[212:213], v249 offset:6656\n\t" \
  "ds_read_b64_tr_b16 v[214:215], v249 offset:6912\n\t" \
  "ds_read_u16_d16_hi v145, v145 offset:0\n\t" \
  "v_lshlrev_b32_sdwa v146, 1, v146 dst_sel:DWORD dst_unused:UNUSED_PAD src0_sel:DWORD src1_sel:WORD_1\n\t" \
  "v_lshlrev_b32_sdwa v147, 1, v147 dst_sel:DWORD dst_unused:UNUSED_PAD src0_sel:DWORD src1_sel:WORD_1\n\t" \
  "s_nop 2\n\t" \
  "v_lshlrev_b32_sdwa v148, 1, v148 dst_sel:DWORD dst_unused:UNUSED_PAD src0_sel:DWORD src1_sel:WORD_1\n\t" \
  "v_lshlrev_b32_sdwa v149, 1, v149 dst_sel:DWORD dst_unused:UNUSED_PAD src0_sel:DWORD src1_sel:WORD_1\n\t" \
  "ds_read_b64 v[230:231], v252 offset:0\n\t" \
  "s_waitcnt lgkmcnt(0)\n\t" \
  "v_mfma_f32_16x16x16_bf16 v[0:3], v[246:247], v[230:231], 0\n\t" \
  "ds_read_b64 v[232:233], v252 offset:8\n\t" \
  "ds_read_b64 v[234:235], v252 offset:64\n\t" \
  "ds_read_b64 v[236:237], v252 offset:72\n\t" \
  "ds_read_b64 v[242:243], v252 offset:128\n\t" \
  "ds_read_b64 v[244:245], v252 offset:136\n\t" \
  "ds_read_u16_d16_hi v146, v146 offset:0\n\t" \
  "ds_read_u16_d16_hi v147, v147 offset:0\n\t" \
  "ds_read_u16_d16_hi v148, v148 offset:0\n\t" \
  "ds_read_u16_d16_hi v149, v149 offset:0\n\t" \
  "s_waitcnt lgkmcnt(8)\n\t" \
  "v_mfma_f32_16x16x16_bf16 v[4:7], v[246:247], v[232:233], 0\n\t" \
  "v_lshlrev_b32_sdwa v150, 1, v150 dst_sel:DWORD dst_unused:UNUSED_PAD src0_sel:DWORD src1_sel:WORD_1\n\t" \
  "v_lshlrev_b32_sdwa v151, 1, v151 dst_sel:DWORD dst_unused:UNUSED_PAD src0_sel:DWORD src1_sel:WORD_1\n\t" \
  "v_mfma_f32_16x16x32_bf16 v[176:179], v[216:219], v[96:99], 0\n\t" \
  "ds_read_b64_tr_b16 v[216:217], v249 offset:8704\n\t" \
  "ds_read_b64_tr_b16 v[218:219], v249 offset:8960\n\t" \
  "ds_read_u16_d16_hi v150, v150 offset:0\n\t" \
  "ds_read_u16_d16_hi v151, v151 offset:0\n\t" \
  "v_mfma_f32_16x16x32_bf16 v[176:179], v[220:223], v[100:103], v[176:179]\n\t" \
  "ds_read_b64_tr_b16 v[220:221], v249 offset:10752\n\t" \
  "ds_read_b64_tr_b16 v[222:223], v249 offset:11008\n\t" \
  "s_waitcnt lgkmcnt(13)\n\t" \
  "v_mfma_f32_16x16x16_bf16 v[8:11], v[246:247], v[234:235], 0\n\t" \
  "s_waitcnt lgkmcnt(12)\n\t" \
  "v_mfma_f32_16x16x16_bf16 v[12:15], v[246:247], v[236:237], 0\n\t" \
  "v_mfma_f32_16x16x32_bf16 v[176:179], v[208:211], v[104:107], v[176:179]\n\t" \
  "ds_read_b64_tr_b16 v[208:209], v250 offset:0\n\t" \
  "ds_read_b64_tr_b16 v[210:211], v250 offset:768\n\t" \
  "v_mfma_f32_16x16x32_bf16 v[180:183], v[212:215], v[96:99], 0\n\t" \
  "ds_read_b64_tr_b16 v[212:213], v250 offset:8\n\t" \
  "ds_read_b64_tr_b16 v[214:215], v250 offset:776\n\t" \
  "s_waitcnt lgkmcnt(14)\n\t" \
  "v_mfma_f32_16x16x16_bf16 v[16:19], v[246:247], v[242:243], 0\n\t" \
  "s_nop 0\n\t" \
  "v_lshlrev_b32_sdwa v176, 1, v176 dst_sel:DWORD dst_unused:UNUSED_PAD src0_sel:DWORD src1_sel:WORD_1\n\t" \
  "v_lshlrev_b32_sdwa v177, 1, v177 dst_sel:DWORD dst_unused:UNUSED_PAD src0_sel:DWORD src1_sel:WORD_1\n\t" \
  "v_mfma_f32_16x16x16_bf16 v[20:23], v[246:247], v[244:245], 0\n\t" \
  "ds_read_u16_d16_hi v176, v176 offset:0\n\t" \
  "ds_read_u16_d16_hi v177, v177 offset:0\n\t" \
  "s_waitcnt lgkmcnt(10)\n\t" \
  "v_mfma_f32_16x16x32_bf16 v[180:183], v[216:219], v[100:103], v[180:183]\n\t" \
  "ds_read_b64_tr_b16 v[216:217], v250 offset:64\n\t" \
  "ds_read_b64_tr_b16 v[218:219], v250 offset:832\n\t" \
  "s_waitcnt lgkmcnt(8)\n\t" \
  "v_mfma_f32_16x16x32_bf16 v[180:183], v[220:223], v[104:107], v[180:183]\n\t" \
  "ds_read_b64_tr_b16 v[220:221], v250 offset:72\n\t" \
  "ds_read_b64_tr_b16 v[222:223], v250 offset:840\n\t" \
  "s_waitcnt lgkmcnt(8)\n\t" \
  "v_smfmac_f32_16x16x64_bf16 v[0:3], v[208:211], v[144:151], v248\n\t" \
  "ds_read_b64_tr_b16 v[208:209], v250 offset:128\n\t" \
  "ds_read_b64_tr_b16 v[210:211], v250 offset:896\n\t" \
  "v_lshlrev_b32_sdwa v178, 1, v178 dst_sel:DWORD dst_unused:UNUSED_PAD src0_sel:DWORD src1_sel:WORD_1\n\t" \
  "s_waitcnt lgkmcnt(8)\n\t" \
  "v_smfmac_f32_16x16x64_bf16 v[4:7], v[212:215], v[144:151], v248\n\t" \
  "ds_read_b64_tr_b16 v[212:213], v250 offset:136\n\t" \
  "ds_read_b64_tr_b16 v[214:215], v250 offset:904\n\t" \
  "ds_read_u16_d16_hi v178, v178 offset:0\n\t" \
  "v_lshlrev_b32_sdwa v179, 1, v179 dst_sel:DWORD dst_unused:UNUSED_PAD src0_sel:DWORD src1_sel:WORD_1\n\t" \
  "s_waitcnt lgkmcnt(7)\n\t" \
  "v_smfmac_f32_16x16x64_bf16 v[8:11], v[216:219], v[144:151], v248\n\t" \
  "s_waitcnt vmcnt(4)\n\t" \
  "s_barrier\n\t" \
  "ds_read_b64_tr_b16 v[216:217], v249 offset:12288\n\t" \
  "ds_read_b64_tr_b16 v[218:219], v249 offset:12544\n\t" \
  "ds_read_u16_d16_hi v179, v179 offset:0\n\t" \
  "s_waitcnt lgkmcnt(8)\n\t" \
  "v_smfmac_f32_16x16x64_bf16 v[12:15], v[220:223], v[144:151], v248\n\t" \
  "ds_read_b64_tr_b16 v[220:221], v249 offset:14336\n\t" \
  "ds_read_b64_tr_b16 v[222:223], v249 offset:14592\n\t" \
  "v_lshlrev_b32_sdwa v180, 1, v180 dst_sel:DWORD dst_unused:UNUSED_PAD src0_sel:DWORD src1_sel:WORD_1\n\t" \
  "s_waitcnt lgkmcnt(8)\n\t" \
  "v_smfmac_f32_16x16x64_bf16 v[16:19], v[208:211], v[144:151], v248\n\t" \
  "s_add_u32 %[rs], %[rs], 1\n\t" \
  "ds_read_b64_tr_b16 v[208:209], v249 offset:16384\n\t" \
  "ds_read_b64_tr_b16 v[210:211], v249 offset:16640\n\t" \
  "ds_read_u16_d16_hi v180, v180 offset:0\n\t" \
  "v_lshlrev_b32_sdwa v181, 1, v181 dst_sel:DWORD dst_unused:UNUSED_PAD src0_sel:DWORD src1_sel:WORD_1\n\t" \
  "s_waitcnt lgkmcnt(9)\n\t" \
  "v_smfmac_f32_16x16x64_bf16 v[20:23], v[212:215], v[144:151], v248\n\t" \
  "s_cmp_eq_u32 %[rs], 6\n\t" \
  "s_cselect_b32 %[rs], 0, %[rs]\n\t" \
  "ds_read_b64_tr_b16 v[212:213], v249 offset:12800\n\t" \
  "ds_read_b64_tr_b16 v[214:215], v249 offset:13056\n\t" \
  "ds_read_u16_d16_hi v181, v181 offset:0\n\t" \
  "s_cselect_b32 %[t0], -75920, %[ps]\n\t" \
  "v_add_u32 v252, %[t0], v252\n\t" \
  "s_waitcnt lgkmcnt(9)\n\t" \
  "v_mfma_f32_16x16x32_bf16 v[144:147], v[216:219], v[96:99], 0\n\t" \
  "ds_read_b64_tr_b16 v[216:217], v249 offset:14848\n\t" \
  "ds_read_b64_tr_b16 v[218:219], v249 offset:15104\n\t" \
  "v_lshlrev_b32_sdwa v182, 1, v182 dst_sel:DWORD dst_unused:UNUSED_PAD src0_sel:DWORD src1_sel:WORD_1\n\t" \
  "s_waitcnt lgkmcnt(8)\n\t" \
  "v_mfma_f32_16x16x32_bf16 v[144:147], v[220:223], v[100:103], v[144:147]\n\t" \
  "ds_read_b64_tr_b16 v[220:221], v249 offset:16896\n\t" \
  "ds_read_b64_tr_b16 v[222:223], v249 offset:17152\n\t" \
  "ds_read_u16_d16_hi v182, v182 offset:0\n\t" \
  "v_lshlrev_b32_sdwa v183, 1, v183 dst_sel:DWORD dst_unused:UNUSED_PAD src0_sel:DWORD src1_sel:WORD_1\n\t" \
  "s_waitcnt lgkmcnt(9)\n\t" \
  "v_mfma_f32_16x16x32_bf16 v[144:147], v[208:211], v[104:107], v[144:147]\n\t" \
  "ds_read_b64_tr_b16 v[208:209], v250 offset:6160\n\t" \
  "ds_read_b64_tr_b16 v[210:211], v250 offset:6928\n\t" \
  "ds_read_u16_d16_hi v183, v183 offset:0\n\t" \
  "s_waitcnt lgkmcnt(9)\n\t" \
  "v_mfma_f32_16x16x32_bf16 v[148:151], v[212:215], v[96:99], 0\n\t" \
  "ds_read_b64_tr_b16 v[212:213], v250 offset:6168\n\t" \
  "ds_read_b64_tr_b16 v[214:215], v250 offset:6936\n\t" \
  "s_nop 0\n\t" \
  "v_lshlrev_b32_sdwa v144, 1, v144 dst_sel:DWORD dst_unused:UNUSED_PAD src0_sel:DWORD src1_sel:WORD_1\n\t" \
  "s_waitcnt lgkmcnt(8)\n\t" \
  "v_mfma_f32_16x16x32_bf16 v[148:151], v[216:219], v[100:103], v[148:151]\n\t" \
  "ds_read_b64_tr_b16 v[216:217], v250 offset:6224\n\t" \
  "ds_read_b64_tr_b16 v[218:219], v250 offset:6992\n\t" \
  "ds_read_u16_d16_hi v144, v144 offset:0\n\t" \
  "v_lshlrev_b32_sdwa v145, 1, v145 dst_sel:DWORD dst_unused:UNUSED_PAD src0_sel:DWORD src1_sel:WORD_1\n\t" \
  "s_waitcnt lgkmcnt(9)\n\t" \
  "v_mfma_f32_16x16x32_bf16 v[148:151], v[220:223], v[104:107], v[148:151]\n\t" \
  "ds_read_b64_tr_b16 v[220:221], v250 offset:6232\n\t" \
  "ds_read_b64_tr_b16 v[222:223], v250 offset:7000\n\t" \
  "ds_read_u16_d16_hi v145, v145 offset:0\n\t" \
  "s_waitcnt lgkmcnt(8)\n\t" \
  "v_smfmac_f32_16x16x64_bf16 v[0:3], v[208:211], v[176:183], v248\n\t" \
  "ds_read_b64_tr_b16 v[208:209], v250 offset:6288\n\t" \
  "ds_read_b64_tr_b16 v[210:211], v250 offset:7056\n\t" \
  "v_lshlrev_b32_sdwa v146, 1, v146 dst_sel:DWORD dst_unused:UNUSED_PAD src0_sel:DWORD src1_sel:WORD_1\n\t" \
  "s_waitcnt lgkmcnt(8)\n\t" \
  "v_smfmac_f32_16x16x64_bf16 v[4:7], v[212:215], v[176:183], v248\n\t" \
  "ds_read_b64_tr_b16 v[212:213], v250 offset:6296\n\t" \
  "ds_read_b64_tr_b16 v[214:215], v250 offset:7064\n\t" \
  "ds_read_u16_d16_hi v146, v146 offset:0\n\t" \
  "v_lshlrev_b32_sdwa v147, 1, v147 dst_sel:DWORD dst_unused:UNUSED_PAD src0_sel:DWORD src1_sel:WORD_1\n\t" \
  "s_waitcnt lgkmcnt(9)\n\t" \
  "v_smfmac_f32_16x16x64_bf16 v[8:11], v[216:219], v[176:183], v248\n\t" \
  "s_waitcnt vmcnt(0)\n\t" \
  "s_barrier\n\t" \
  "s_setprio 2\n\t" \
  "ds_read_b64_tr_b16 v[216:217], v249 offset:0\n\t" \
  "ds_read_b64_tr_b16 v[218:219], v249 offset:256\n\t" \
  "ds_read_b128 v[108:111], v251 offset:0\n\t" \
  "ds_read_b128 v[112:115], v251 offset:4096\n\t" \
  "ds_read_b128 v[116:119], v251 offset:8192\n\t" \
  "ds_read_u16_d16_hi v147, v147 offset:0\n\t" \
  "s_waitcnt lgkmcnt(12)\n\t" \
  "v_smfmac_f32_16x16x64_bf16 v[12:15], v[220:223], v[176:183], v248\n\t" \
  "ds_read_b64_tr_b16 v[220:221], v249 offset:2048\n\t" \
  "ds_read_b64_tr_b16 v[222:223], v249 offset:2304\n\t" \
  "v_lshlrev_b32_sdwa v148, 1, v148 dst_sel:DWORD dst_unused:UNUSED_PAD src0_sel:DWORD src1_sel:WORD_1\n\t" \
  "s_waitcnt lgkmcnt(11)\n\t" \
  "v_smfmac_f32_16x16x64_bf16 v[16:19], v[208:211], v[176:183], v248\n\t" \
  "ds_read_b64_tr_b16 v[208:209], v249 offset:4096\n\t" \
  "ds_read_b64_tr_b16 v[210:211], v249 offset:4352\n\t" \
  "ds_read_u16_d16_hi v148, v148 offset:0\n\t" \
  "v_lshlrev_b32_sdwa v149, 1, v149 dst_sel:DWORD dst_unused:UNUSED_PAD src0_sel:DWORD src1_sel:WORD_1\n\t" \
  "s_waitcnt lgkmcnt(12)\n\t" \
  "v_smfmac_f32_16x16x64_bf16 v[20:23], v[212:215], v[176:183], v248\n\t" \
  "ds_read_b64_tr_b16 v[212:213], v249 offset:512\n\t" \
  "ds_read_b64_tr_b16 v[214:215], v249 offset:768\n\t" \
  "ds_read_u16_d16_hi v149, v149 offset:0\n\t" \
  "s_waitcnt lgkmcnt(12)\n\t" \
  "v_mfma_f32_16x16x32_bf16 v[176:179], v[216:219], v[96:99], 0\n\t" \
  "v_lshlrev_b32_sdwa v150, 1, v150 dst_sel:DWORD dst_unused:UNUSED_PAD src0_sel:DWORD src1_sel:WORD_1\n\t" \
  "v_lshlrev_b32_sdwa v151, 1, v151 dst_sel:DWORD dst_unused:UNUSED_PAD src0_sel:DWORD src1_sel:WORD_1\n\t" \
  "s_waitcnt lgkmcnt(11)\n\t" \
  "v_mfma_f32_16x16x32_bf16 v[184:187], v[216:219], v[108:111], 0\n\t" \
  "ds_read_b64_tr_b16 v[216:217], v249 offset:2560\n\t" \
  "ds_read_b64_tr_b16 v[218:219], v249 offset:2816\n\t" \
  "ds_read_u16_d16_hi v150, v150 offset:0\n\t" \
  "ds_read_u16_d16_hi v151, v151 offset:0\n\t" \
  "s_waitcnt lgkmcnt(10)\n\t" \
  "v_mfma_f32_16x16x32_bf16 v[176:179], v[220:223], v[100:103], v[176:179]\n\t" \
  "v_mfma_f32_16x16x32_bf16 v[184:187], v[220:223], v[112:115], v[184:187]\n\t" \
  "ds_read_b64_tr_b16 v[220:221], v249 offset:4608\n\t" \
  "ds_read_b64_tr_b16 v[222:223], v249 offset:4864\n\t" \
  "s_waitcnt lgkmcnt(10)\n\t" \
  "v_mfma_f32_16x16x32_bf16 v[176:179], v[208:211], v[104:107], v[176:179]\n\t" \
  "v_mfma_f32_16x16x32_bf16 v[184:187], v[208:211], v[116:119], v[184:187]\n\t" \
  "ds_read_b64_tr_b16 v[208:209], v250 offset:12320\n\t" \
  "ds_read_b64_tr_b16 v[210:211], v250 offset:13088\n\t" \
  "s_waitcnt lgkmcnt(9)\n\t" \
  "v_mfma_f32_16x16x32_bf16 v[180:183], v[212:215], v[96:99], 0\n\t" \
  "s_nop 2\n\t" \
  "v_lshlrev_b32_sdwa v176, 1, v176 dst_sel:DWORD dst_unused:UNUSED_PAD src0_sel:DWORD src1_sel:WORD_1\n\t" \
  "v_cvt_pk_f16_f32 v224, v184, v185\n\t" \
  "v_mfma_f32_16x16x32_bf16 v[188:191], v[212:215], v[108:111], 0\n\t" \
  "ds_read_b64_tr_b16 v[212:213], v250 offset:12328\n\t" \
  "ds_read_b64_tr_b16 v[214:215], v250 offset:13096\n\t" \
  "ds_read_u16_d16_hi v176, v176 offset:0\n\t" \
  "v_cvt_pk_f16_f32 v226, v186, v187\n\t" \
  "v_pk_fma_f16 v225, v224, v224, %[h1] neg_lo:[1,0,0] neg_hi:[1,0,0]\n\t" \
  "v_pk_fma_f16 v227, v226, v226, %[h1] neg_lo:[1,0,0] neg_hi:[1,0,0]\n\t" \
  "s_waitcnt lgkmcnt(9)\n\t" \
  "v_mfma_f32_16x16x32_bf16 v[180:183], v[216:219], v[100:103], v[180:183]\n\t" \
  "v_pk_mul_f16 v225, v224, v225\n\t" \
  "v_pk_mul_f16 v227, v226, v227\n\t" \
  "v_mfma_f32_16x16x32_bf16 v[188:191], v[216:219], v[112:115], v[188:191]\n\t" \
  "ds_read_b64_tr_b16 v[216:217], v250 offset:12384\n\t" \
  "ds_read_b64_tr_b16 v[218:219], v250 offset:13152\n\t" \
  "v_exp_f16 v224, v225\n\t" \
  "v_lshlrev_b32_sdwa v177, 1, v177 dst_sel:DWORD dst_unused:UNUSED_PAD src0_sel:DWORD src1_sel:WORD_1\n\t" \
  "v_exp_f16 v226, v227\n\t" \
  "s_waitcnt lgkmcnt(7)\n\t" \
  "v_mfma_f32_16x16x32_bf16 v[180:183], v[220:223], v[104:107], v[180:183]\n\t" \
  "ds_read_u16_d16_hi v177, v177 offset:0\n\t" \
  "v_exp_f16_sdwa v224, v225 dst_sel:WORD_1 dst_unused:UNUSED_PRESERVE src0_sel:WORD_1\n\t" \
  "v_mfma_f32_16x16x32_bf16 v[188:191], v[220:223], v[116:119], v[188:191]\n\t" \
  "ds_read_b64_tr_b16 v[220:221], v250 offset:12392\n\t" \
  "ds_read_b64_tr_b16 v[222:223], v250 offset:13160\n\t" \
  "v_exp_f16_sdwa v226, v227 dst_sel:WORD_1 dst_unused:UNUSED_PRESERVE src0_sel:WORD_1\n\t" \
  "s_waitcnt lgkmcnt(8)\n\t" \
  "v_smfmac_f32_16x16x64_bf16 v[0:3], v[208:211], v[144:151], v248\n\t" \
  "ds_read_b64_tr_b16 v[208:209], v250 offset:12448\n\t" \
  "ds_read_b64_tr_b16 v[210:211], v250 offset:13216\n\t" \
  "v_pk_fma_f16 v224, v224, %[hc], %[hc]\n\t" \
  "v_lshlrev_b32_sdwa v178, 1, v178 dst_sel:DWORD dst_unused:UNUSED_PAD src0_sel:DWORD src1_sel:WORD_1\n\t" \
  "v_pk_fma_f16 v226, v226, %[hc], %[hc]\n\t" \
  "v_rcp_f16 v225, v224\n\t" \
  "v_rcp_f16 v227, v226\n\t" \
  "s_waitcnt lgkmcnt(8)\n\t" \
  "v_smfmac_f32_16x16x64_bf16 v[4:7], v[212:215], v[144:151], v248\n\t" \
  "ds_read_b64_tr_b16 v[212:213], v250 offset:12456\n\t" \
  "ds_read_b64_tr_b16 v[214:215], v250 offset:13224\n\t" \
  "ds_read_b64 v[230:231], v252 offset:0\n\t" \
  "ds_read_b64 v[232:233], v252 offset:8\n\t" \
  "ds_read_b64 v[234:235], v252 offset:64\n\t" \
  "ds_read_u16_d16_hi v178, v178 offset:0\n\t" \
  "v_rcp_f16_sdwa v224, v224 dst_sel:DWORD dst_unused:UNUSED_PAD src0_sel:WORD_1\n\t" \
  "v_lshlrev_b32_sdwa v179, 1, v179 dst_sel:DWORD dst_unused:UNUSED_PAD src0_sel:DWORD src1_sel:WORD_1\n\t" \
  "v_rcp_f16_sdwa v226, v226 dst_sel:DWORD dst_unused:UNUSED_PAD src0_sel:WORD_1\n\t" \
  "s_waitcnt lgkmcnt(11)\n\t" \
  "v_smfmac_f32_16x16x64_bf16 v[8:11], v[216:219], v[144:151], v248\n\t" \
  "s_barrier\n\t" \
  "ds_read_b64_tr_b16 v[216:217], v249 offset:6144\n\t" \
  "ds_read_b64_tr_b16 v[218:219], v249 offset:6400\n\t" \
  "ds_read_b64 v[236:237], v252 offset:72\n\t" \
  "ds_read_b64 v[242:243], v252 offset:128\n\t" \
  "ds_read_b64 v[244:245], v252 offset:136\n\t" \
  "ds_read_u16_d16_hi v179, v179 offset:0\n\t" \
  "v_fma_mix_f32 v184, v184, v225, 0 op_sel_hi:[0,1,0]\n\t" \
  "v_fma_mix_f32 v186, v186, v227, 0 op_sel_hi:[0,1,0]\n\t" \
  "v_fma_mix_f32 v185, v185, v224, 0 op_sel_hi:[0,1,0]\n\t" \
  "v_fma_mix_f32 v187, v187, v226, 0 op_sel_hi:[0,1,0]\n\t" \
  "s_waitcnt lgkmcnt(14)\n\t" \
  "v_smfmac_f32_16x16x64_bf16 v[12:15], v[220:223], v[144:151], v248\n\t" \
  "ds_read_b64_tr_b16 v[220:221], v249 offset:8192\n\t" \
  "ds_read_b64_tr_b16 v[222:223], v249 offset:8448\n\t" \
  "v_cvt_pk_f16_f32 v224, v188, v189\n\t" \
  "v_cvt_pk_f16_f32 v226, v190, v191\n\t" \
  "v_lshlrev_b32_sdwa v180, 1, v180 dst_sel:DWORD dst_unused:UNUSED_PAD src0_sel:DWORD src1_sel:WORD_1\n\t" \
  "v_pk_fma_f16 v225, v224, v224, %[h1] neg_lo:[1,0,0] neg_hi:[1,0,0]\n\t" \
  "v_pk_fma_f16 v227, v226, v226, %[h1] neg_lo:[1,0,0] neg_hi:[1,0,0]\n\t" \
  "v_pk_mul_f16 v225, v224, v225\n\t" \
  "s_waitcnt lgkmcnt(14)\n\t" \
  "v_smfmac_f32_16x16x64_bf16 v[16:19], v[208:211], v[144:151], v248\n\t" \
  "ds_read_b64_tr_b16 v[208:209], v249 offset:10240\n\t" \
  "ds_read_b64_tr_b16 v[210:211], v249 offset:10496\n\t" \
  "ds_read_u16_d16_hi v180, v180 offset:0\n\t" \
  "v_pk_mul_f16 v227, v226, v227\n\t" \
  "v_exp_f16 v224, v225\n\t" \
  "v_exp_f16 v226, v227\n\t" \
  "v_lshlrev_b32_sdwa v181, 1, v181 dst_sel:DWORD dst_unused:UNUSED_PAD src0_sel:DWORD src1_sel:WORD_1\n\t" \
  "s_waitcnt lgkmcnt(14)\n\t" \
  "v_smfmac_f32_16x16x64_bf16 v[20:23], v[212:215], v[144:151], v248\n\t" \
  "ds_read_b64_tr_b16 v[212:213], v249 offset:6656\n\t" \
  "ds_read_b64_tr_b16 v[214:215], v249 offset:6912\n\t" \
  "ds_read_u16_d16_hi v181, v181 offset:0\n\t" \
  "v_exp_f16_sdwa v224, v225 dst_sel:WORD_1 dst_unused:UNUSED_PRESERVE src0_sel:WORD_1\n\t" \
  "v_mfma_f32_16x16x16_bf16 v[24:27], v[246:247], v[230:231], 0\n\t" \
  "v_exp_f16_sdwa v226, v227 dst_sel:WORD_1 dst_unused:UNUSED_PRESERVE src0_sel:WORD_1\n\t" \
  "v_pk_fma_f16 v224, v224, %[hc], %[hc]\n\t" \
  "s_waitcnt lgkmcnt(12)\n\t" \
  "v_mfma_f32_16x16x32_bf16 v[144:147], v[216:219], v[96:99], 0\n\t" \
  "v_pk_fma_f16 v226, v226, %[hc], %[hc]\n\t" \
  "v_lshlrev_b32_sdwa v182, 1, v182 dst_sel:DWORD dst_unused:UNUSED_PAD src0_sel:DWORD src1_sel:WORD_1\n\t" \
  "v_mfma_f32_16x16x16_bf16 v[28:31], v[246:247], v[232:233], 0\n\t" \
  "ds_read_u16_d16_hi v182, v182 offset:0\n\t" \
  "v_rcp_f16 v225, v224\n\t" \
  "v_mfma_f32_16x16x32_bf16 v[152:155], v[216:219], v[108:111], 0\n\t" \
  "ds_read_b64_tr_b16 v[216:217], v249 offset:8704\n\t" \
  "ds_read_b64_tr_b16 v[218:219], v249 offset:8960\n\t" \
  "v_rcp_f16 v227, v226\n\t" \
  "s_waitcnt lgkmcnt(9)\n\t" \
  "v_mfma_f32_16x16x32_bf16 v[144:147], v[220:223], v[100:103], v[144:147]\n\t" \
  "v_rcp_f16_sdwa v224, v224 dst_sel:DWORD dst_unused:UNUSED_PAD src0_sel:WORD_1\n\t" \
  "v_mfma_f32_16x16x32_bf16 v[152:155], v[220:223], v[112:115], v[152:155]\n\t" \
  "ds_read_b64_tr_b16 v[220:221], v249 offset:10752\n\t" \
  "ds_read_b64_tr_b16 v[222:223], v249 offset:11008\n\t" \
  "v_mfma_f32_16x16x16_bf16 v[32:35], v[246:247], v[234:235], 0\n\t" \
  "v_rcp_f16_sdwa v226, v226 dst_sel:DWORD dst_unused:UNUSED_PAD src0_sel:WORD_1\n\t" \
  "v_lshlrev_b32_sdwa v183, 1, v183 dst_sel:DWORD dst_unused:UNUSED_PAD src0_sel:DWORD src1_sel:WORD_1\n\t" \
  "v_fma_mix_f32 v188, v188, v225, 0 op_sel_hi:[0,1,0]\n\t" \
  "s_waitcnt lgkmcnt(9)\n\t" \
  "v_mfma_f32_16x16x32_bf16 v[144:147], v[208:211], v[104:107], v[144:147]\n\t" \
  "ds_read_u16_d16_hi v183, v183 offset:0\n\t" \
  "v_fma_mix_f32 v190, v190, v227, 0 op_sel_hi:[0,1,0]\n\t" \
  "v_mfma_f32_16x16x16_bf16 v[36:39], v[246:247], v[236:237], 0\n\t" \
  "v_fma_mix_f32 v189, v189, v224, 0 op_sel_hi:[0,1,0]\n\t" \
  "v_fma_mix_f32 v191, v191, v226, 0 op_sel_hi:[0,1,0]\n\t" \
  "v_mfma_f32_16x16x32_bf16 v[152:155], v[208:211], v[116:119], v[152:155]\n\t" \
  "ds_read_b64_tr_b16 v[208:209], v250 offset:18480\n\t" \
  "ds_read_b64_tr_b16 v[210:211], v250 offset:19248\n\t" \
  "s_waitcnt lgkmcnt(9)\n\t" \
  "v_mfma_f32_16x16x32_bf16 v[148:151], v[212:215], v[96:99], 0\n\t" \
  "v_lshlrev_b32_sdwa v144, 1, v144 dst_sel:DWORD dst_unused:UNUSED_PAD src0_sel:DWORD src1_sel:WORD_1\n\t" \
  "s_nop 2\n\t" \
  "v_cvt_pk_f16_f32 v224, v152, v153\n\t" \
  "v_mfma_f32_16x16x32_bf16 v[156:159], v[212:215], v[108:111], 0\n\t" \
  "ds_read_b64_tr_b16 v[212:213], v250 offset:18488\n\t" \
  "ds_read_b64_tr_b16 v[214:215], v250 offset:19256\n\t" \
  "ds_read_u16_d16_hi v144, v144 offset:0\n\t" \
  "v_mfma_f32_16x16x16_bf16 v[40:43], v[246:247], v[242:243], 0\n\t" \
  "v_cvt_pk_f16_f32 v226, v154, v155\n\t" \
  "v_pk_fma_f16 v225, v224, v224, %[h1] neg_lo:[1,0,0] neg_hi:[1,0,0]\n\t" \
  "v_pk_fma_f16 v227, v226, v226, %[h1] neg_lo:[1,0,0] neg_hi:[1,0,0]\n\t" \
  "s_waitcnt lgkmcnt(8)\n\t" \
  "v_mfma_f32_16x16x32_bf16 v[148:151], v[216:219], v[100:103], v[148:151]\n\t" \
  "v_pk_mul_f16 v225, v224, v225\n\t" \
  "v_pk_mul_f16 v227, v226, v227\n\t" \
  "v_mfma_f32_16x16x16_bf16 v[44:47], v[246:247], v[244:245], 0\n\t" \
  "v_exp_f16 v224, v225\n\t" \
  "v_mfma_f32_16x16x32_bf16 v[156:159], v[216:219], v[112:115], v[156:159]\n\t" \
  "ds_read_b64_tr_b16 v[216:217], v250 offset:18544\n\t" \
  "ds_read_b64_tr_b16 v[218:219], v250 offset:19312\n\t" \
  "v_lshlrev_b32_sdwa v145, 1, v145 dst_sel:DWORD dst_unused:UNUSED_PAD src0_sel:DWORD src1_sel:WORD_1\n\t" \
  "v_exp_f16 v226, v227\n\t" \
  "s_waitcnt lgkmcnt(8)\n\t" \
  "v_mfma_f32_16x16x32_bf16 v[148:151], v[220:223], v[104:107], v[148:151]\n\t" \
  "ds_read_u16_d16_hi v145, v145 offset:0\n\t" \
  "v_exp_f16_sdwa v224, v225 dst_sel:WORD_1 dst_unused:UNUSED_PRESERVE src0_sel:WORD_1\n\t" \
  "v_mfma_f32_16x16x32_bf16 v[156:159], v[220:223], v[116:119], v[156:159]\n\t" \
  "ds_read_b64_tr_b16 v[220:221], v250 offset:18552\n\t" \
  "ds_read_b64_tr_b16 v[222:223], v250 offset:19320\n\t" \
  "v_exp_f16_sdwa v226, v227 dst_sel:WORD_1 dst_unused:UNUSED_PRESERVE src0_sel:WORD_1\n\t" \
  "s_waitcnt lgkmcnt(8)\n\t" \
  "v_smfmac_f32_16x16x64_bf16 v[0:3], v[208:211], v[176:183], v248\n\t" \
  "v_pk_fma_f16 v224, v224, %[hc], %[hc]\n\t" \
  "v_lshlrev_b32_sdwa v146, 1, v146 dst_sel:DWORD dst_unused:UNUSED_PAD src0_sel:DWORD src1_sel:WORD_1\n\t" \
  "v_smfmac_f32_16x16x64_bf16 v[24:27], v[208:211], v[184:191], v248\n\t" \
  "ds_read_b64_tr_b16 v[208:209], v250 offset:18608\n\t" \
  "ds_read_b64_tr_b16 v[210:211], v250 offset:19376\n\t" \
  "ds_read_u16_d16_hi v146, v146 offset:0\n\t" \
  "v_pk_fma_f16 v226, v226, %[hc], %[hc]\n\t" \
  "v_rcp_f16 v225, v224\n\t" \
  "v_rcp_f16 v227, v226\n\t" \
  "s_waitcnt lgkmcnt(9)\n\t" \
  "v_smfmac_f32_16x16x64_bf16 v[4:7], v[212:215], v[176:183], v248\n\t" \
  "v_rcp_f16_sdwa v224, v224 dst_sel:DWORD dst_unused:UNUSED_PAD src0_sel:WORD_1\n\t" \
  "v_smfmac_f32_16x16x64_bf16 v[28:31], v[212:215], v[184:191], v248\n\t" \
  "ds_read_b64_tr_b16 v[212:213], v250 offset:18616\n\t" \
  "ds_read_b64_tr_b16 v[214:215], v250 offset:19384\n\t" \
  "v_lshlrev_b32_sdwa v147, 1, v147 dst_sel:DWORD dst_unused:UNUSED_PAD src0_sel:DWORD src1_sel:WORD_1\n\t" \
  "v_rcp_f16_sdwa v226, v226 dst_sel:DWORD dst_unused:UNUSED_PAD src0_sel:WORD_1\n\t" \
  "s_waitcnt lgkmcnt(8)\n\t" \
  "v_smfmac_f32_16x16x64_bf16 v[8:11], v[216:219], v[176:183], v248\n\t" \
  "ds_read_u16_d16_hi v147, v147 offset:0\n\t" \
  "v_fma_mix_f32 v152, v152, v225, 0 op_sel_hi:[0,1,0]\n\t" \
  "v_smfmac_f32_16x16x64_bf16 v[32:35], v[216:219], v[184:191], v248\n\t" \
  "s_barrier\n\t" \
  "ds_read_b64_tr_b16 v[216:217], v249 offset:12288\n\t" \
  "ds_read_b64_tr_b16 v[218:219], v249 offset:12544\n\t" \
  "v_fma_mix_f32 v154, v154, v227, 0 op_sel_hi:[0,1,0]\n\t" \
  "v_fma_mix_f32 v153, v153, v224, 0 op_sel_hi:[0,1,0]\n\t" \
  "v_fma_mix_f32 v155, v155, v226, 0 op_sel_hi:[0,1,0]\n\t" \
  "s_waitcnt lgkmcnt(8)\n\t" \
  "v_smfmac_f32_16x16x64_bf16 v[12:15], v[220:223], v[176:183], v248\n\t" \
  "v_cvt_pk_f16_f32 v224, v156, v157\n\t" \
  "v_cvt_pk_f16_f32 v226, v158, v159\n\t" \
  "v_smfmac_f32_16x16x64_bf16 v[36:39], v[220:223], v[184:191], v248\n\t" \
  "ds_read_b64_tr_b16 v[220:221], v249 offset:14336\n\t" \
  "ds_read_b64_tr_b16 v[222:223], v249 offset:14592\n\t" \
  "v_lshlrev_b32_sdwa v148, 1, v148 dst_sel:DWORD dst_unused:UNUSED_PAD src0_sel:DWORD src1_sel:WORD_1\n\t" \
  "v_pk_fma_f16 v225, v224, v224, %[h1] neg_lo:[1,0,0] neg_hi:[1,0,0]\n\t" \
  "v_pk_fma_f16 v227, v226, v226, %[h1] neg_lo:[1,0,0] neg_hi:[1,0,0]\n\t" \
  "v_pk_mul_f16 v225, v224, v225\n\t" \
  "s_waitcnt lgkmcnt(8)\n\t" \
  "v_smfmac_f32_16x16x64_bf16 v[16:19], v[208:211], v[176:183], v248\n\t" \
  "s_add_u32 %[rs], %[rs], 1\n\t" \
  "ds_read_u16_d16_hi v148, v148 offset:0\n\t" \
  "v_pk_mul_f16 v227, v226, v227\n\t" \
  "v_exp_f16 v224, v225\n\t" \
  "v_smfmac_f32_16x16x64_bf16 v[40:43], v[208:211], v[184:191], v248\n\t" \
  "s_cmp_eq_u32 %[rs], 6\n\t" \
  "ds_read_b64_tr_b16 v[208:209], v249 offset:16384\n\t" \
  "ds_read_b64_tr_b16 v[210:211], v249 offset:16640\n\t" \
  "v_exp_f16 v226, v227\n\t" \
  "v_lshlrev_b32_sdwa v149, 1, v149 dst_sel:DWORD dst_unused:UNUSED_PAD src0_sel:DWORD src1_sel:WORD_1\n\t" \
  "s_waitcnt lgkmcnt(8)\n\t" \
  "v_smfmac_f32_16x16x64_bf16 v[20:23], v[212:215], v[176:183], v248\n\t" \
  "s_cselect_b32 %[rs], 0, %[rs]\n\t" \
  "ds_read_u16_d16_hi v149, v149 offset:0\n\t" \
  "v_exp_f16_sdwa v224, v225 dst_sel:WORD_1 dst_unused:UNUSED_PRESERVE src0_sel:WORD_1\n\t" \
  "v_smfmac_f32_16x16x64_bf16 v[44:47], v[212:215], v[184:191], v248\n\t" \
  "s_cselect_b32 %[t0], -75920, %[ps]\n\t" \
  "ds_read_b64_tr_b16 v[212:213], v249 offset:12800\n\t" \
  "ds_read_b64_tr_b16 v[214:215], v249 offset:13056\n\t" \
  "v_exp_f16_sdwa v226, v227 dst_sel:WORD_1 dst_unused:UNUSED_PRESERVE src0_sel:WORD_1\n\t" \
  "v_add_u32 v252, %[t0], v252\n\t" \
  "v_pk_fma_f16 v224, v224, %[hc], %[hc]\n\t" \
  "s_waitcnt lgkmcnt(8)\n\t" \
  "v_mfma_f32_16x16x32_bf16 v[176:179], v[216:219], v[96:99], 0\n\t" \
  "v_pk_fma_f16 v226, v226, %[hc], %[hc]\n\t" \
  "v_lshlrev_b32_sdwa v150, 1, v150 dst_sel:DWORD dst_unused:UNUSED_PAD src0_sel:DWORD src1_sel:WORD_1\n\t" \
  "v_mfma_f32_16x16x32_bf16 v[184:187], v[216:219], v[108:111], 0\n\t" \
  "ds_read_b64_tr_b16 v[216:217], v249 offset:14848\n\t" \
  "ds_read_b64_tr_b16 v[218:219], v249 offset:15104\n\t" \
  "ds_read_u16_d16_hi v150, v150 offset:0\n\t" \
  "v_rcp_f16 v225, v224\n\t" \
  "v_rcp_f16 v227, v226\n\t" \
  "s_waitcnt lgkmcnt(9)\n\t" \
  "v_mfma_f32_16x16x32_bf16 v[176:179], v[220:223], v[100:103], v[176:179]\n\t" \
  "v_rcp_f16_sdwa v224, v224 dst_sel:DWORD dst_unused:UNUSED_PAD src0_sel:WORD_1\n\t" \
  "v_mfma_f32_16x16x32_bf16 v[184:187], v[220:223], v[112:115], v[184:187]\n\t" \
  "ds_read_b64_tr_b16 v[220:221], v249 offset:16896\n\t" \
  "ds_read_b64_tr_b16 v[222:223], v249 offset:17152\n\t" \
  "v_rcp_f16_sdwa v226, v226 dst_sel:DWORD dst_unused:UNUSED_PAD src0_sel:WORD_1\n\t" \
  "v_lshlrev_b32_sdwa v151, 1, v151 dst_sel:DWORD dst_unused:UNUSED_PAD src0_sel:DWORD src1_sel:WORD_1\n\t" \
  "v_fma_mix_f32 v156, v156, v225, 0 op_sel_hi:[0,1,0]\n\t" \
  "s_waitcnt lgkmcnt(8)\n\t" \
  "v_mfma_f32_16x16x32_bf16 v[176:179], v[208:211], v[104:107], v[176:179]\n\t" \
  "ds_read_u16_d16_hi v151, v151 offset:0\n\t" \
  "v_fma_mix_f32 v158, v158, v227, 0 op_sel_hi:[0,1,0]\n\t" \
  "v_mfma_f32_16x16x32_bf16 v[184:187], v[208:211], v[116:119], v[184:187]\n\t" \
  "ds_read_b64_tr_b16 v[208:209], v250 offset:0\n\t" \
  "ds_read_b64_tr_b16 v[210:211], v250 offset:768\n\t" \
  "v_fma_mix_f32 v157, v157, v224, 0 op_sel_hi:[0,1,0]\n\t" \
  "v_fma_mix_f32 v159, v159, v226, 0 op_sel_hi:[0,1,0]\n\t" \
  "s_waitcnt lgkmcnt(8)\n\t" \
  "v_mfma_f32_16x16x32_bf16 v[180:183], v[212:215], v[96:99], 0\n\t" \
  "v_lshlrev_b32_sdwa v176, 1, v176 dst_sel:DWORD dst_unused:UNUSED_PAD src0_sel:DWORD src1_sel:WORD_1\n\t" \
  "s_nop 0\n\t" \
  "v_cvt_pk_f16_f32 v224, v184, v185\n\t" \
  "v_mfma_f32_16x16x32_bf16 v[188:191], v[212:215], v[108:111], 0\n\t" \
  "ds_read_b64_tr_b16 v[212:213], v250 offset:8\n\t" \
  "ds_read_b64_tr_b16 v[214:215], v250 offset:776\n\t" \
  "ds_read_u16_d16_hi v176, v176 offset:0\n\t" \
  "v_cvt_pk_f16_f32 v226, v186, v187\n\t" \
  "v_pk_fma_f16 v225, v224, v224, %[h1] neg_lo:[1,0,0] neg_hi:[1,0,0]\n\t" \
  "v_pk_fma_f16 v227, v226, v226, %[h1] neg_lo:[1,0,0] neg_hi:[1,0,0]\n\t" \
  "s_waitcnt lgkmcnt(9)\n\t" \
  "v_mfma_f32_16x16x32_bf16 v[180:183], v[216:219], v[100:103], v[180:183]\n\t" \
  "v_pk_mul_f16 v225, v224, v225\n\t" \
  "v_pk_mul_f16 v227, v226, v227\n\t" \
  "v_mfma_f32_16x16x32_bf16 v[188:191], v[216:219], v[112:115], v[188:191]\n\t" \
  "ds_read_b64_tr_b16 v[216:217], v250 offset:64\n\t" \
  "ds_read_b64_tr_b16 v[218:219], v250 offset:832\n\t" \
  "v_exp_f16 v224, v225\n\t" \
  "v_lshlrev_b32_sdwa v177, 1, v177 dst_sel:DWORD dst_unused:UNUSED_PAD src0_sel:DWORD src1_sel:WORD_1\n\t" \
  "v_exp_f16 v226, v227\n\t" \
  "s_waitcnt lgkmcnt(8)\n\t" \
  "v_mfma_f32_16x16x32_bf16 v[180:183], v[220:223], v[104:107], v[180:183]\n\t" \
  "ds_read_u16_d16_hi v177, v177 offset:0\n\t" \
  "v_exp_f16_sdwa v224, v225 dst_sel:WORD_1 dst_unused:UNUSED_PRESERVE src0_sel:WORD_1\n\t" \
  "v_mfma_f32_16x16x32_bf16 v[188:191], v[220:223], v[116:119], v[188:191]\n\t" \
  "ds_read_b64_tr_b16 v[220:221], v250 offset:72\n\t" \
  "ds_read_b64_tr_b16 v[222:223], v250 offset:840\n\t" \
  "v_exp_f16_sdwa v226, v227 dst_sel:WORD_1 dst_unused:UNUSED_PRESERVE src0_sel:WORD_1\n\t" \
  "s_waitcnt lgkmcnt(8)\n\t" \
  "v_smfmac_f32_16x16x64_bf16 v[0:3], v[208:211], v[144:151], v248\n\t" \
  "v_pk_fma_f16 v224, v224, %[hc], %[hc]\n\t" \
  "v_lshlrev_b32_sdwa v178, 1, v178 dst_sel:DWORD dst_unused:UNUSED_PAD src0_sel:DWORD src1_sel:WORD_1\n\t" \
  "v_smfmac_f32_16x16x64_bf16 v[24:27], v[208:211], v[152:159], v248\n\t" \
  "ds_read_b64_tr_b16 v[208:209], v250 offset:128\n\t" \
  "ds_read_b64_tr_b16 v[210:211], v250 offset:896\n\t" \
  "ds_read_u16_d16_hi v178, v178 offset:0\n\t" \
  "v_pk_fma_f16 v226, v226, %[hc], %[hc]\n\t" \
  "v_rcp_f16 v225, v224\n\t" \
  "v_rcp_f16 v227, v226\n\t" \
  "s_waitcnt lgkmcnt(9)\n\t" \
  "v_smfmac_f32_16x16x64_bf16 v[4:7], v[212:215], v[144:151], v248\n\t" \
  "v_rcp_f16_sdwa v224, v224 dst_sel:DWORD dst_unused:UNUSED_PAD src0_sel:WORD_1\n\t" \
  "v_smfmac_f32_16x16x64_bf16 v[28:31], v[212:215], v[152:159], v248\n\t" \
  "ds_read_b64_tr_b16 v[212:213], v250 offset:136\n\t" \
  "ds_read_b64_tr_b16 v[214:215], v250 offset:904\n\t" \
  "v_lshlrev_b32_sdwa v179, 1, v179 dst_sel:DWORD dst_unused:UNUSED_PAD src0_sel:DWORD src1_sel:WORD_1\n\t" \
  "v_rcp_f16_sdwa v226, v226 dst_sel:DWORD dst_unused:UNUSED_PAD src0_sel:WORD_1\n\t" \
  "s_waitcnt lgkmcnt(8)\n\t" \
  "v_smfmac_f32_16x16x64_bf16 v[8:11], v[216:219], v[144:151], v248\n\t" \
  "ds_read_u16_d16_hi v179, v179 offset:0\n\t" \
  "v_fma_mix_f32 v184, v184, v225, 0 op_sel_hi:[0,1,0]\n\t" \
  "v_smfmac_f32_16x16x64_bf16 v[32:35], v[216:219], v[152:159], v248\n\t" \
  "s_barrier\n\t" \
  "ds_read_b64_tr_b16 v[216:217], v249 offset:0\n\t" \
  "ds_read_b64_tr_b16 v[218:219], v249 offset:256\n\t" \
  "ds_read_b128 v[120:123], v251 offset:0\n\t" \
  "ds_read_b128 v[124:127], v251 offset:4096\n\t" \
  "ds_read_b128 v[128:131], v251 offset:8192\n\t" \
  "v_fma_mix_f32 v186, v186, v227, 0 op_sel_hi:[0,1,0]\n\t" \
  "v_fma_mix_f32 v185, v185, v224, 0 op_sel_hi:[0,1,0]\n\t" \
  "v_fma_mix_f32 v187, v187, v226, 0 op_sel_hi:[0,1,0]\n\t" \
  "s_waitcnt lgkmcnt(11)\n\t" \
  "v_smfmac_f32_16x16x64_bf16 v[12:15], v[220:223], v[144:151], v248\n\t" \
  "v_cvt_pk_f16_f32 v224, v188, v189\n\t" \
  "v_cvt_pk_f16_f32 v226, v190, v191\n\t" \
  "v_smfmac_f32_16x16x64_bf16 v[36:39], v[220:223], v[152:159], v248\n\t" \
  "ds_read_b64_tr_b16 v[220:221], v249 offset:2048\n\t" \
  "ds_read_b64_tr_b16 v[222:223], v249 offset:2304\n\t" \
  "v_lshlrev_b32_sdwa v180, 1, v180 dst_sel:DWORD dst_unused:UNUSED_PAD src0_sel:DWORD src1_sel:WORD_1\n\t" \
  "v_pk_fma_f16 v225, v224, v224, %[h1] neg_lo:[1,0,0] neg_hi:[1,0,0]\n\t" \
  "v_pk_fma_f16 v227, v226, v226, %[h1] neg_lo:[1,0,0] neg_hi:[1,0,0]\n\t" \
  "v_pk_mul_f16 v225, v224, v225\n\t" \
  "s_waitcnt lgkmcnt(11)\n\t" \
  "v_smfmac_f32_16x16x64_bf16 v[16:19], v[208:211], v[144:151], v248\n\t" \
  "ds_read_u16_d16_hi v180, v180 offset:0\n\t" \
  "v_pk_mul_f16 v227, v226, v227\n\t" \
  "v_exp_f16 v224, v225\n\t" \
  "v_smfmac_f32_16x16x64_bf16 v[40:43], v[208:211], v[152:159], v248\n\t" \
  "ds_read_b64_tr_b16 v[208:209], v249 offset:4096\n\t" \
  "ds_read_b64_tr_b16 v[210:211], v249 offset:4352\n\t" \
  "v_exp_f16 v226, v227\n\t" \
  "v_lshlrev_b32_sdwa v181, 1, v181 dst_sel:DWORD dst_unused:UNUSED_PAD src0_sel:DWORD src1_sel:WORD_1\n\t" \
  "s_waitcnt lgkmcnt(11)\n\t" \
  "v_smfmac_f32_16x16x64_bf16 v[20:23], v[212:215], v[144:151], v248\n\t" \
  "ds_read_u16_d16_hi v181, v181 offset:0\n\t" \
  "v_exp_f16_sdwa v224, v225 dst_sel:WORD_1 dst_unused:UNUSED_PRESERVE src0_sel:WORD_1\n\t" \
  "v_smfmac_f32_16x16x64_bf16 v[44:47], v[212:215], v[152:159], v248\n\t" \
  "ds_read_b64_tr_b16 v[212:213], v249 offset:512\n\t" \
  "ds_read_b64_tr_b16 v[214:215], v249 offset:768\n\t" \
  "v_exp_f16_sdwa v226, v227 dst_sel:WORD_1 dst_unused:UNUSED_PRESERVE src0_sel:WORD_1\n\t" \
  "v_pk_fma_f16 v224, v224, %[hc], %[hc]\n\t" \
  "s_waitcnt lgkmcnt(11)\n\t" \
  "v_mfma_f32_16x16x32_bf16 v[144:147], v[216:219], v[96:99], 0\n\t" \
  "v_pk_fma_f16 v226, v226, %[hc], %[hc]\n\t" \
  "v_lshlrev_b32_sdwa v182, 1, v182 dst_sel:DWORD dst_unused:UNUSED_PAD src0_sel:DWORD src1_sel:WORD_1\n\t" \
  "v_mfma_f32_16x16x32_bf16 v[152:155], v[216:219], v[108:111], 0\n\t" \
  "s_waitcnt lgkmcnt(10)\n\t" \
  "ds_read_u16_d16_hi v182, v182 offset:0\n\t" \
  "v_rcp_f16 v225, v224\n\t" \
  "v_mfma_f32_16x16x32_bf16 v[160:163], v[216:219], v[120:123], 0\n\t" \
  "s_waitcnt lgkmcnt(7)\n\t" \
  "ds_read_b64_tr_b16 v[216:217], v249 offset:2560\n\t" \
  "v_rcp_f16 v227, v226\n\t" \
  "v_mfma_f32_16x16x32_bf16 v[144:147], v[220:223], v[100:103], v[144:147]\n\t" \
  "ds_read_b64_tr_b16 v[218:219], v249 offset:2816\n\t" \
  "v_rcp_f16_sdwa v224, v224 dst_sel:DWORD dst_unused:UNUSED_PAD src0_sel:WORD_1\n\t" \
  "v_mfma_f32_16x16x32_bf16 v[152:155], v[220:223], v[112:115], v[152:155]\n\t" \
  "v_rcp_f16_sdwa v226, v226 dst_sel:DWORD dst_unused:UNUSED_PAD src0_sel:WORD_1\n\t" \
  "v_mfma_f32_16x16x32_bf16 v[160:163], v[220:223], v[124:127], v[160:163]\n\t" \
  "s_waitcnt lgkmcnt(6)\n\t" \
  "ds_read_b64_tr_b16 v[220:221], v249 offset:4608\n\t" \
  "v_lshlrev_b32_sdwa v183, 1, v183 dst_sel:DWORD dst_unused:UNUSED_PAD src0_sel:DWORD src1_sel:WORD_1\n\t" \
  "v_fma_mix_f32 v188, v188, v225, 0 op_sel_hi:[0,1,0]\n\t" \
  "v_mfma_f32_16x16x32_bf16 v[144:147], v[208:211], v[104:107], v[144:147]\n\t" \
  "ds_read_u16_d16_hi v183, v183 offset:0\n\t" \
  "v_fma_mix_f32 v190, v190, v227, 0 op_sel_hi:[0,1,0]\n\t" \
  "v_mfma_f32_16x16x32_bf16 v[152:155], v[208:211], v[116:119], v[152:155]\n\t" \
  "ds_read_b64_tr_b16 v[222:223], v249 offset:4864\n\t" \
  "v_fma_mix_f32 v189, v189, v224, 0 op_sel_hi:[0,1,0]\n\t" \
  "v_mfma_f32_16x16x32_bf16 v[160:163], v[208:211], v[128:131], v[160:163]\n\t" \
  "s_waitcnt lgkmcnt(6)\n\t" \
  "ds_read_b64_tr_b16 v[208:209], v250 offset:6160\n\t" \
  "v_fma_mix_f32 v191, v191, v226, 0 op_sel_hi:[0,1,0]\n\t" \
  "v_mfma_f32_16x16x32_bf16 v[148:151], v[212:215], v[96:99], 0\n\t" \
  "ds_read_b64_tr_b16 v[210:211], v250 offset:6928\n\t" \
  "v_lshlrev_b32_sdwa v144, 1, v144 dst_sel:DWORD dst_unused:UNUSED_PAD src0_sel:DWORD src1_sel:WORD_1\n\t" \
  "v_mfma_f32_16x16x32_bf16 v[156:159], v[212:215], v[108:111], 0\n\t" \
  "ds_read_u16_d16_hi v144, v144 offset:0\n\t" \
  "v_cvt_pk_f16_f32 v224, v152, v153\n\t" \
  "v_cvt_pk_f16_f32 v226, v154, v155\n\t" \
  "v_mfma_f32_16x16x32_bf16 v[164:167], v[212:215], v[120:123], 0\n\t" \
  "s_waitcnt lgkmcnt(6)\n\t" \
  "ds_read_b64_tr_b16 v[212:213], v250 offset:6168\n\t" \
  "v_pk_fma_f16 v225, v224, v224, %[h1] neg_lo:[1,0,0] neg_hi:[1,0,0]\n\t" \
  "v_pk_fma_f16 v227, v226, v226, %[h1] neg_lo:[1,0,0] neg_hi:[1,0,0]\n\t" \
  "v_mfma_f32_16x16x32_bf16 v[148:151], v[216:219], v[100:103], v[148:151]\n\t" \
  "ds_read_b64_tr_b16 v[214:215], v250 offset:6936\n\t" \
  "v_lshlrev_b32_sdwa v145, 1, v145 dst_sel:DWORD dst_unused:UNUSED_PAD src0_sel:DWORD src1_sel:WORD_1\n\t" \
  "v_pk_mul_f16 v225, v224, v225\n\t" \
  "v_mfma_f32_16x16x32_bf16 v[156:159], v[216:219], v[112:115], v[156:159]\n\t" \
  "ds_read_u16_d16_hi v145, v145 offset:0\n\t" \
  "v_pk_mul_f16 v227, v226, v227\n\t" \
  "v_exp_f16 v224, v225\n\t" \
  "v_mfma_f32_16x16x32_bf16 v[164:167], v[216:219], v[124:127], v[164:167]\n\t" \
  "s_waitcnt lgkmcnt(6)\n\t" \
  "ds_read_b64_tr_b16 v[216:217], v250 offset:6224\n\t" \
  "v_lshlrev_b32_sdwa v146, 1, v146 dst_sel:DWORD dst_unused:UNUSED_PAD src0_sel:DWORD src1_sel:WORD_1\n\t" \
  "v_exp_f16 v226, v227\n\t" \
  "v_mfma_f32_16x16x32_bf16 v[148:151], v[220:223], v[104:107], v[148:151]\n\t" \
  "ds_read_u16_d16_hi v146, v146 offset:0\n\t" \
  "v_exp_f16_sdwa v224, v225 dst_sel:WORD_1 dst_unused:UNUSED_PRESERVE src0_sel:WORD_1\n\t" \
  "v_mfma_f32_16x16x32_bf16 v[156:159], v[220:223], v[116:119], v[156:159]\n\t" \
  "ds_read_b64_tr_b16 v[218:219], v250 offset:6992\n\t" \
  "v_lshlrev_b32_sdwa v147, 1, v147 dst_sel:DWORD dst_unused:UNUSED_PAD src0_sel:DWORD src1_sel:WORD_1\n\t" \
  "v_mfma_f32_16x16x32_bf16 v[164:167], v[220:223], v[128:131], v[164:167]\n\t" \
  "s_waitcnt lgkmcnt(7)\n\t" \
  "ds_read_u16_d16_hi v147, v147 offset:0\n\t" \
  "v_exp_f16_sdwa v226, v227 dst_sel:WORD_1 dst_unused:UNUSED_PRESERVE src0_sel:WORD_1\n\t" \
  "v_smfmac_f32_16x16x64_bf16 v[0:3], v[208:211], v[176:183], v248\n\t" \
  "ds_read_b64_tr_b16 v[220:221], v250 offset:6232\n\t" \
  "ds_read_b64_tr_b16 v[222:223], v250 offset:7000\n\t" \
  "v_pk_fma_f16 v224, v224, %[hc], %[hc]\n\t" \
  "v_smfmac_f32_16x16x64_bf16 v[24:27], v[208:211], v[184:191], v248\n\t" \
  "ds_read_b64_tr_b16 v[208:209], v250 offset:6288\n\t" \
  "ds_read_b64_tr_b16 v[210:211], v250 offset:7056\n\t" \
  "v_lshlrev_b32_sdwa v160, 1, v160 dst_sel:DWORD dst_unused:UNUSED_PAD src0_sel:DWORD src1_sel:WORD_1\n\t" \
  "v_pk_fma_f16 v226, v226, %[hc], %[hc]\n\t" \
  "v_rcp_f16 v225, v224\n\t" \
  "v_rcp_f16 v227, v226\n\t" \
  "s_waitcnt lgkmcnt(9)\n\t" \
  "v_smfmac_f32_16x16x64_bf16 v[4:7], v[212:215], v[176:183], v248\n\t" \
  "ds_read_b64 v[230:231], v252 offset:0\n\t" \
  "ds_read_b64 v[232:233], v252 offset:8\n\t" \
  "ds_read_b64 v[234:235], v252 offset:64\n\t" \
  "ds_read_u16_d16_hi v160, v160 offset:0\n\t" \
  "v_lshlrev_b32_sdwa v161, 1, v161 dst_sel:DWORD dst_unused:UNUSED_PAD src0_sel:DWORD src1_sel:WORD_1\n\t" \
  "v_smfmac_f32_16x16x64_bf16 v[28:31], v[212:215], v[184:191], v248\n\t" \
  "ds_read_b64_tr_b16 v[212:213], v250 offset:6296\n\t" \
  "ds_read_b64_tr_b16 v[214:215], v250 offset:7064\n\t" \
  "ds_read_b64 v[236:237], v252 offset:72\n\t" \
  "ds_read_u16_d16_hi v161, v161 offset:0\n\t" \
  "v_rcp_f16_sdwa v224, v224 dst_sel:DWORD dst_unused:UNUSED_PAD src0_sel:WORD_1\n\t" \
  "v_lshlrev_b32_sdwa v162, 1, v162 dst_sel:DWORD dst_unused:UNUSED_PAD src0_sel:DWORD src1_sel:WORD_1\n\t" \
  "v_rcp_f16_sdwa v226, v226 dst_sel:DWORD dst_unused:UNUSED_PAD src0_sel:WORD_1\n\t" \
  "s_waitcnt lgkmcnt(13)\n\t" \
  "v_smfmac_f32_16x16x64_bf16 v[8:11], v[216:219], v[176:183], v248\n\t" \
  "ds_read_b64 v[242:243], v252 offset:128\n\t" \
  "ds_read_b64 v[244:245], v252 offset:136\n\t" \
  "ds_read_u16_d16_hi v162, v162 offset:0\n\t" \
  "v_fma_mix_f32 v152, v152, v225, 0 op_sel_hi:[0,1,0]\n\t" \
  "v_smfmac_f32_16x16x64_bf16 v[32:35], v[216:219], v[184:191], v248\n\t" \
  "s_barrier\n\t" \
  "ds_read_b64_tr_b16 v[216:217], v249 offset:6144\n\t" \
  "ds_read_b64_tr_b16 v[218:219], v249 offset:6400\n\t" \
  "v_fma_mix_f32 v154, v154, v227, 0 op_sel_hi:[0,1,0]\n\t" \
  "v_lshlrev_b32_sdwa v163, 1, v163 dst_sel:DWORD dst_unused:UNUSED_PAD src0_sel:DWORD src1_sel:WORD_1\n\t" \
  "v_fma_mix_f32 v153, v153, v224, 0 op_sel_hi:[0,1,0]\n\t" \
  "v_fma_mix_f32 v155, v155, v226, 0 op_sel_hi:[0,1,0]\n\t" \
  "s_waitcnt lgkmcnt(14)\n\t" \
  "v_smfmac_f32_16x16x64_bf16 v[12:15], v[220:223], v[176:183], v248\n\t" \
  "ds_read_u16_d16_hi v163, v163 offset:0\n\t" \
  "v_cvt_pk_f16_f32 v224, v156, v157\n\t" \
  "v_cvt_pk_f16_f32 v226, v158, v159\n\t" \
  "v_smfmac_f32_16x16x64_bf16 v[36:39], v[220:223], v[184:191], v248\n\t" \
  "ds_read_b64_tr_b16 v[220:221], v249 offset:8192\n\t" \
  "ds_read_b64_tr_b16 v[222:223], v249 offset:8448\n\t" \
  "v_lshlrev_b32_sdwa v148, 1, v148 dst_sel:DWORD dst_unused:UNUSED_PAD src0_sel:DWORD src1_sel:WORD_1\n\t" \
  "v_pk_fma_f16 v225, v224, v224, %[h1] neg_lo:[1,0,0] neg_hi:[1,0,0]\n\t" \
  "v_pk_fma_f16 v227, v226, v226, %[h1] neg_lo:[1,0,0] neg_hi:[1,0,0]\n\t" \
  "v_pk_mul_f16 v225, v224, v225\n\t" \
  "s_waitcnt lgkmcnt(14)\n\t" \
  "v_smfmac_f32_16x16x64_bf16 v[16:19], v[208:211], v[176:183], v248\n\t" \
  "ds_read_u16_d16_hi v148, v148 offset:0\n\t" \
  "v_lshlrev_b32_sdwa v149, 1, v149 dst_sel:DWORD dst_unused:UNUSED_PAD src0_sel:DWORD src1_sel:WORD_1\n\t" \
  "v_pk_mul_f16 v227, v226, v227\n\t" \
  "v_exp_f16 v224, v225\n\t" \
  "v_smfmac_f32_16x16x64_bf16 v[40:43], v[208:211], v[184:191], v248\n\t" \
  "ds_read_b64_tr_b16 v[208:209], v249 offset:10240\n\t" \
  "ds_read_b64_tr_b16 v[210:211], v249 offset:10496\n\t" \
  "ds_read_u16_d16_hi v149, v149 offset:0\n\t" \
  "v_exp_f16 v226, v227\n\t" \
  "v_lshlrev_b32_sdwa v150, 1, v150 dst_sel:DWORD dst_unused:UNUSED_PAD src0_sel:DWORD src1_sel:WORD_1\n\t" \
  "s_waitcnt lgkmcnt(14)\n\t" \
  "v_smfmac_f32_16x16x64_bf16 v[20:23], v[212:215], v[176:183], v248\n\t" \
  "ds_read_u16_d16_hi v150, v150 offset:0\n\t" \
  "v_exp_f16_sdwa v224, v225 dst_sel:WORD_1 dst_unused:UNUSED_PRESERVE src0_sel:WORD_1\n\t" \
  "v_smfmac_f32_16x16x64_bf16 v[44:47], v[212:215], v[184:191], v248\n\t" \
  "ds_read_b64_tr_b16 v[212:213], v249 offset:6656\n\t" \
  "ds_read_b64_tr_b16 v[214:215], v249 offset:6912\n\t" \
  "v_exp_f16_sdwa v226, v227 dst_sel:WORD_1 dst_unused:UNUSED_PRESERVE src0_sel:WORD_1\n\t" \
  "v_mfma_f32_16x16x16_bf16 v[48:51], v[246:247], v[230:231], 0\n\t" \
  "v_lshlrev_b32_sdwa v151, 1, v151 dst_sel:DWORD dst_unused:UNUSED_PAD src0_sel:DWORD src1_sel:WORD_1\n\t" \
  "v_pk_fma_f16 v224, v224, %[hc], %[hc]\n\t" \
  "s_waitcnt lgkmcnt(10)\n\t" \
  "v_mfma_f32_16x16x32_bf16 v[176:179], v[216:219], v[96:99], 0\n\t" \
  "ds_read_u16_d16_hi v151, v151 offset:0\n\t" \
  "v_pk_fma_f16 v226, v226, %[hc], %[hc]\n\t" \
  "v_mfma_f32_16x16x32_bf16 v[184:187], v[216:219], v[108:111], 0\n\t" \
  "v_lshlrev_b32_sdwa v164, 1, v164 dst_sel:DWORD dst_unused:UNUSED_PAD src0_sel:DWORD src1_sel:WORD_1\n\t" \
  "v_rcp_f16 v225, v224\n\t" \
  "v_mfma_f32_16x16x16_bf16 v[52:55], v[246:247], v[232:233], 0\n\t" \
  "ds_read_u16_d16_hi v164, v164 offset:0\n\t" \
  "v_rcp_f16 v227, v226\n\t" \
  "v_mfma_f32_16x16x32_bf16 v[192:195], v[216:219], v[120:123], 0\n\t" \
  "s_waitcnt lgkmcnt(9)\n\t" \
  "ds_read_b64_tr_b16 v[216:217], v249 offset:8704\n\t" \
  "v_lshlrev_b32_sdwa v165, 1, v165 dst_sel:DWORD dst_unused:UNUSED_PAD src0_sel:DWORD src1_sel:WORD_1\n\t" \
  "v_mfma_f32_16x16x32_bf16 v[176:179], v[220:223], v[100:103], v[176:179]\n\t" \
  "ds_read_u16_d16_hi v165, v165 offset:0\n\t" \
  "v_rcp_f16_sdwa v224, v224 dst_sel:DWORD dst_unused:UNUSED_PAD src0_sel:WORD_1\n\t" \
  "v_mfma_f32_16x16x32_bf16 v[184:187], v[220:223], v[112:115], v[184:187]\n\t" \
  "ds_read_b64_tr_b16 v[218:219], v249 offset:8960\n\t" \
  "v_rcp_f16_sdwa v226, v226 dst_sel:DWORD dst_unused:UNUSED_PAD src0_sel:WORD_1\n\t" \
  "v_mfma_f32_16x16x32_bf16 v[192:195], v[220:223], v[124:127], v[192:195]\n\t" \
  "ds_read_b64_tr_b16 v[220:221], v249 offset:10752\n\t" \
  "v_lshlrev_b32_sdwa v166, 1, v166 dst_sel:DWORD dst_unused:UNUSED_PAD src0_sel:DWORD src1_sel:WORD_1\n\t" \
  "v_mfma_f32_16x16x16_bf16 v[56:59], v[246:247], v[234:235], 0\n\t" \
  "s_waitcnt lgkmcnt(10)\n\t" \
  "ds_read_u16_d16_hi v166, v166 offset:0\n\t" \
  "v_fma_mix_f32 v156, v156, v225, 0 op_sel_hi:[0,1,0]\n\t" \
  "v_mfma_f32_16x16x32_bf16 v[176:179], v[208:211], v[104:107], v[176:179]\n\t" \
  "ds_read_b64_tr_b16 v[222:223], v249 offset:11008\n\t" \
  "v_fma_mix_f32 v158, v158, v227, 0 op_sel_hi:[0,1,0]\n\t" \
  "v_mfma_f32_16x16x32_bf16 v[184:187], v[208:211], v[116:119], v[184:187]\n\t" \
  "v_fma_mix_f32 v157, v157, v224, 0 op_sel_hi:[0,1,0]\n\t" \
  "v_lshlrev_b32_sdwa v167, 1, v167 dst_sel:DWORD dst_unused:UNUSED_PAD src0_sel:DWORD src1_sel:WORD_1\n\t" \
  "v_mfma_f32_16x16x16_bf16 v[60:63], v[246:247], v[236:237], 0\n\t" \
  "ds_read_u16_d16_hi v167, v167 offset:0\n\t" \
  "v_fma_mix_f32 v159, v159, v226, 0 op_sel_hi:[0,1,0]\n\t" \
  "v_mfma_f32_16x16x32_bf16 v[192:195], v[208:211], v[128:131], v[192:195]\n\t" \
  "s_waitcnt lgkmcnt(9)\n\t" \
  "ds_read_b64_tr_b16 v[208:209], v250 offset:12320\n\t" \
  "v_lshlrev_b32_sdwa v176, 1, v176 dst_sel:DWORD dst_unused:UNUSED_PAD src0_sel:DWORD src1_sel:WORD_1\n\t" \
  "v_mfma_f32_16x16x32_bf16 v[180:183], v[212:215], v[96:99], 0\n\t" \
  "ds_read_u16_d16_hi v176, v176 offset:0\n\t" \
  "v_cvt_pk_f16_f32 v224, v184, v185\n\t" \
  "v_mfma_f32_16x16x32_bf16 v[188:191], v[212:215], v[108:111], 0\n\t" \
  "ds_read_b64_tr_b16 v[210:211], v250 offset:13088\n\t" \
  "v_cvt_pk_f16_f32 v226, v186, v187\n\t" \
  "v_mfma_f32_16x16x32_bf16 v[196:199], v[212:215], v[120:123], 0\n\t" \
  "ds_read_b64_tr_b16 v[212:213], v250 offset:12328\n\t" \
  "v_pk_fma_f16 v225, v224, v224, %[h1] neg_lo:[1,0,0] neg_hi:[1,0,0]\n\t" \
  "v_mfma_f32_16x16x16_bf16 v[64:67], v[246:247], v[242:243], 0\n\t" \
  "s_waitcnt lgkmcnt(8)\n\t" \
  "ds_read_b64_tr_b16 v[214:215], v250 offset:13096\n\t" \
  "v_pk_fma_f16 v227, v226, v226, %[h1] neg_lo:[1,0,0] neg_hi:[1,0,0]\n\t" \
  "v_mfma_f32_16x16x32_bf16 v[180:183], v[216:219], v[100:103], v[180:183]\n\t" \
  "v_lshlrev_b32_sdwa v177, 1, v177 dst_sel:DWORD dst_unused:UNUSED_PAD src0_sel:DWORD src1_sel:WORD_1\n\t" \
  "v_pk_mul_f16 v225, v224, v225\n\t" \
  "v_mfma_f32_16x16x32_bf16 v[188:191], v[216:219], v[112:115], v[188:191]\n\t" \
  "ds_read_u16_d16_hi v177, v177 offset:0\n\t" \
  "v_pk_mul_f16 v227, v226, v227\n\t" \
  "v_mfma_f32_16x16x16_bf16 v[68:71], v[246:247], v[244:245], 0\n\t" \
  "v_exp_f16 v224, v225\n\t" \
  "v_mfma_f32_16x16x32_bf16 v[196:199], v[216:219], v[124:127], v[196:199]\n\t" \
  "s_waitcnt lgkmcnt(7)\n\t" \
  "ds_read_b64_tr_b16 v[216:217], v250 offset:12384\n\t" \
  "v_lshlrev_b32_sdwa v178, 1, v178 dst_sel:DWORD dst_unused:UNUSED_PAD src0_sel:DWORD src1_sel:WORD_1\n\t" \
  "v_exp_f16 v226, v227\n\t" \
  "v_mfma_f32_16x16x32_bf16 v[180:183], v[220:223], v[104:107], v[180:183]\n\t" \
  "ds_read_u16_d16_hi v178, v178 offset:0\n\t" \
  "v_exp_f16_sdwa v224, v225 dst_sel:WORD_1 dst_unused:UNUSED_PRESERVE src0_sel:WORD_1\n\t" \
  "v_mfma_f32_16x16x32_bf16 v[188:191], v[220:223], v[116:119], v[188:191]\n\t" \
  "ds_read_b64_tr_b16 v[218:219], v250 offset:13152\n\t" \
  "v_lshlrev_b32_sdwa v179, 1, v179 dst_sel:DWORD dst_unused:UNUSED_PAD src0_sel:DWORD src1_sel:WORD_1\n\t" \
  "v_mfma_f32_16x16x32_bf16 v[196:199], v[220:223], v[128:131], v[196:199]\n\t" \
  "s_waitcnt lgkmcnt(6)\n\t" \
  "ds_read_u16_d16_hi v179, v179 offset:0\n\t" \
  "v_exp_f16_sdwa v226, v227 dst_sel:WORD_1 dst_unused:UNUSED_PRESERVE src0_sel:WORD_1\n\t" \
  "v_smfmac_f32_16x16x64_bf16 v[0:3], v[208:211], v[144:151], v248\n\t" \
  "ds_read_b64_tr_b16 v[220:221], v250 offset:12392\n\t" \
  "v_pk_fma_f16 v224, v224, %[hc], %[hc]\n\t" \
  "v_smfmac_f32_16x16x64_bf16 v[24:27], v[208:211], v[152:159], v248\n\t" \
  "ds_read_b64_tr_b16 v[222:223], v250 offset:13160\n\t" \
  "v_lshlrev_b32_sdwa v192, 1, v192 dst_sel:DWORD dst_unused:UNUSED_PAD src0_sel:DWORD src1_sel:WORD_1\n\t" \
  "v_pk_fma_f16 v226, v226, %[hc], %[hc]\n\t" \
  "v_smfmac_f32_16x16x64_bf16 v[48:51], v[208:211], v[160:167], v248\n\t" \
  "s_waitcnt lgkmcnt(7)\n\t" \
  "ds_read_u16_d16_hi v192, v192 offset:0\n\t" \
  "v_rcp_f16 v225, v224\n\t" \
  "v_rcp_f16 v227, v226\n\t" \
  "v_smfmac_f32_16x16x64_bf16 v[4:7], v[212:215], v[144:151], v248\n\t" \
  "ds_read_b64_tr_b16 v[208:209], v250 offset:12448\n\t" \
  "v_lshlrev_b32_sdwa v193, 1, v193 dst_sel:DWORD dst_unused:UNUSED_PAD src0_sel:DWORD src1_sel:WORD_1\n\t" \
  "v_smfmac_f32_16x16x64_bf16 v[28:31], v[212:215], v[152:159], v248\n\t" \
  "ds_read_u16_d16_hi v193, v193 offset:0\n\t" \
  "v_rcp_f16_sdwa v224, v224 dst_sel:DWORD dst_unused:UNUSED_PAD src0_sel:WORD_1\n\t" \
  "v_smfmac_f32_16x16x64_bf16 v[52:55], v[212:215], v[160:167], v248\n\t" \
  "s_waitcnt lgkmcnt(6)\n\t" \
  "ds_read_b64_tr_b16 v[210:211], v250 offset:13216\n\t" \
  "v_lshlrev_b32_sdwa v194, 1, v194 dst_sel:DWORD dst_unused:UNUSED_PAD src0_sel:DWORD src1_sel:WORD_1\n\t" \
  "v_rcp_f16_sdwa v226, v226 dst_sel:DWORD dst_unused:UNUSED_PAD src0_sel:WORD_1\n\t" \
  "v_smfmac_f32_16x16x64_bf16 v[8:11], v[216:219], v[144:151], v248\n\t" \
  "ds_read_u16_d16_hi v194, v194 offset:0\n\t" \
  "v_fma_mix_f32 v184, v184, v225, 0 op_sel_hi:[0,1,0]\n\t" \
  "v_smfmac_f32_16x16x64_bf16 v[32:35], v[216:219], v[152:159], v248\n\t" \
  "ds_read_b64_tr_b16 v[212:213], v250 offset:12456\n\t" \
  "v_fma_mix_f32 v186, v186, v227, 0 op_sel_hi:[0,1,0]\n\t" \
  "v_smfmac_f32_16x16x64_bf16 v[56:59], v[216:219], v[160:167], v248\n\t" \
  "s_waitcnt lgkmcnt(6)\n\t" \
  "ds_read_b64_tr_b16 v[214:215], v250 offset:13224\n\t" \
  "v_lshlrev_b32_sdwa v195, 1, v195 dst_sel:DWORD dst_unused:UNUSED_PAD src0_sel:DWORD src1_sel:WORD_1\n\t" \
  "v_fma_mix_f32 v185, v185, v224, 0 op_sel_hi:[0,1,0]\n\t" \
  "v_fma_mix_f32 v187, v187, v226, 0 op_sel_hi:[0,1,0]\n\t" \
  "v_smfmac_f32_16x16x64_bf16 v[12:15], v[220:223], v[144:151], v248\n\t" \
  "s_barrier\n\t" \
  "ds_read_u16_d16_hi v195, v195 offset:0\n\t" \
  "v_cvt_pk_f16_f32 v224, v188, v189\n\t" \
  "v_cvt_pk_f16_f32 v226, v190, v191\n\t" \
  "v_smfmac_f32_16x16x64_bf16 v[36:39], v[220:223], v[152:159], v248\n\t" \
  "ds_read_b64_tr_b16 v[216:217], v249 offset:12288\n\t" \
  "v_lshlrev_b32_sdwa v180, 1, v180 dst_sel:DWORD dst_unused:UNUSED_PAD src0_sel:DWORD src1_sel:WORD_1\n\t" \
  "v_pk_fma_f16 v225, v224, v224, %[h1] neg_lo:[1,0,0] neg_hi:[1,0,0]\n\t" \
  "v_smfmac_f32_16x16x64_bf16 v[60:63], v[220:223], v[160:167], v248\n\t" \
  "s_waitcnt lgkmcnt(5)\n\t" \
  "ds_read_b64_tr_b16 v[218:219], v249 offset:12544\n\t" \
  "v_pk_fma_f16 v227, v226, v226, %[h1] neg_lo:[1,0,0] neg_hi:[1,0,0]\n\t" \
  "v_pk_mul_f16 v225, v224, v225\n\t" \
  "v_smfmac_f32_16x16x64_bf16 v[16:19], v[208:211], v[144:151], v248\n\t" \
  "s_add_u32 %[rs], %[rs], 1\n\t" \
  "ds_read_u16_d16_hi v180, v180 offset:0\n\t" \
  "v_lshlrev_b32_sdwa v181, 1, v181 dst_sel:DWORD dst_unused:UNUSED_PAD src0_sel:DWORD src1_sel:WORD_1\n\t" \
  "v_pk_mul_f16 v227, v226, v227\n\t" \
  "v_exp_f16 v224, v225\n\t" \
  "v_smfmac_f32_16x16x64_bf16 v[40:43], v[208:211], v[152:159], v248\n\t" \
  "s_cmp_eq_u32 %[rs], 6\n\t" \
  "ds_read_b64_tr_b16 v[220:221], v249 offset:14336\n\t" \
  "v_exp_f16 v226, v227\n\t" \
  "v_smfmac_f32_16x16x64_bf16 v[64:67], v[208:211], v[160:167], v248\n\t" \
  "s_waitcnt lgkmcnt(5)\n\t" \
  "ds_read_b64_tr_b16 v[222:223], v249 offset:14592\n\t" \
  "v_lshlrev_b32_sdwa v182, 1, v182 dst_sel:DWORD dst_unused:UNUSED_PAD src0_sel:DWORD src1_sel:WORD_1\n\t" \
  "v_smfmac_f32_16x16x64_bf16 v[20:23], v[212:215], v[144:151], v248\n\t" \
  "s_cselect_b32 %[rs], 0, %[rs]\n\t" \
  "ds_read_u16_d16_hi v181, v181 offset:0\n\t" \
  "v_exp_f16_sdwa v224, v225 dst_sel:WORD_1 dst_unused:UNUSED_PRESERVE src0_sel:WORD_1\n\t" \
  "v_smfmac_f32_16x16x64_bf16 v[44:47], v[212:215], v[152:159], v248\n\t" \
  "s_cselect_b32 %[t0], -75920, %[ps]\n\t" \
  "ds_read_b64_tr_b16 v[208:209], v249 offset:16384\n\t" \
  "v_exp_f16_sdwa v226, v227 dst_sel:WORD_1 dst_unused:UNUSED_PRESERVE src0_sel:WORD_1\n\t" \
  "v_smfmac_f32_16x16x64_bf16 v[68:71], v[212:215], v[160:167], v248\n\t" \
  "s_waitcnt lgkmcnt(5)\n\t" \
  "ds_read_b64_tr_b16 v[210:211], v249 offset:16640\n\t" \
  "v_add_u32 v252, %[t0], v252\n\t" \
  "v_lshlrev_b32_sdwa v183, 1, v183 dst_sel:DWORD dst_unused:UNUSED_PAD src0_sel:DWORD src1_sel:WORD_1\n\t" \
  "v_pk_fma_f16 v224, v224, %[hc], %[hc]\n\t" \
  "v_mfma_f32_16x16x32_bf16 v[144:147], v[216:219], v[96:99], 0\n\t" \
  "ds_read_u16_d16_hi v182, v182 offset:0\n\t" \
  "v_pk_fma_f16 v226, v226, %[hc], %[hc]\n\t" \
  "v_mfma_f32_16x16x32_bf16 v[152:155], v[216:219], v[108:111], 0\n\t" \
  "ds_read_b64_tr_b16 v[212:213], v249 offset:12800\n\t" \
  "v_lshlrev_b32_sdwa v196, 1, v196 dst_sel:DWORD dst_unused:UNUSED_PAD src0_sel:DWORD src1_sel:WORD_1\n\t" \
  "v_rcp_f16 v225, v224\n\t" \
  "v_mfma_f32_16x16x32_bf16 v[160:163], v[216:219], v[120:123], 0\n\t" \
  "s_waitcnt lgkmcnt(5)\n\t" \
  "ds_read_b64_tr_b16 v[214:215], v249 offset:13056\n\t" \
  "v_rcp_f16 v227, v226\n\t" \
  "v_mfma_f32_16x16x32_bf16 v[144:147], v[220:223], v[100:103], v[144:147]\n\t" \
  "ds_read_u16_d16_hi v183, v183 offset:0\n\t" \
  "v_lshlrev_b32_sdwa v197, 1, v197 dst_sel:DWORD dst_unused:UNUSED_PAD src0_sel:DWORD src1_sel:WORD_1\n\t" \
  "v_rcp_f16_sdwa v224, v224 dst_sel:DWORD dst_unused:UNUSED_PAD src0_sel:WORD_1\n\t" \
  "v_mfma_f32_16x16x32_bf16 v[152:155], v[220:223], v[112:115], v[152:155]\n\t" \
  "ds_read_b64_tr_b16 v[216:217], v249 offset:14848\n\t" \
  "v_rcp_f16_sdwa v226, v226 dst_sel:DWORD dst_unused:UNUSED_PAD src0_sel:WORD_1\n\t" \
  "v_mfma_f32_16x16x32_bf16 v[160:163], v[220:223], v[124:127], v[160:163]\n\t" \
  "s_waitcnt lgkmcnt(5)\n\t" \
  "ds_read_b64_tr_b16 v[218:219], v249 offset:15104\n\t" \
  "v_lshlrev_b32_sdwa v198, 1, v198 dst_sel:DWORD dst_unused:UNUSED_PAD src0_sel:DWORD src1_sel:WORD_1\n\t" \
  "v_fma_mix_f32 v188, v188, v225, 0 op_sel_hi:[0,1,0]\n\t" \
  "v_mfma_f32_16x16x32_bf16 v[144:147], v[208:211], v[104:107], v[144:147]\n\t" \
  "ds_read_u16_d16_hi v196, v196 offset:0\n\t" \
  "v_fma_mix_f32 v190, v190, v227, 0 op_sel_hi:[0,1,0]\n\t" \
  "v_mfma_f32_16x16x32_bf16 v[152:155], v[208:211], v[116:119], v[152:155]\n\t" \
  "ds_read_b64_tr_b16 v[220:221], v249 offset:16896\n\t" \
  "v_fma_mix_f32 v189, v189, v224, 0 op_sel_hi:[0,1,0]\n\t" \
  "v_mfma_f32_16x16x32_bf16 v[160:163], v[208:211], v[128:131], v[160:163]\n\t" \
  "s_waitcnt lgkmcnt(5)\n\t" \
  "ds_read_b64_tr_b16 v[222:223], v249 offset:17152\n\t" \
  "v_lshlrev_b32_sdwa v199, 1, v199 dst_sel:DWORD dst_unused:UNUSED_PAD src0_sel:DWORD src1_sel:WORD_1\n\t" \
  "v_fma_mix_f32 v191, v191, v226, 0 op_sel_hi:[0,1,0]\n\t" \
  "v_mfma_f32_16x16x32_bf16 v[148:151], v[212:215], v[96:99], 0\n\t" \
  "ds_read_u16_d16_hi v197, v197 offset:0\n\t" \
  "v_lshlrev_b32_sdwa v144, 1, v144 dst_sel:DWORD dst_unused:UNUSED_PAD src0_sel:DWORD src1_sel:WORD_1\n\t" \
  "v_mfma_f32_16x16x32_bf16 v[156:159], v[212:215], v[108:111], 0\n\t" \
  "ds_read_b64_tr_b16 v[208:209], v250 offset:18480\n\t" \
  "v_cvt_pk_f16_f32 v224, v152, v153\n\t" \
  "v_cvt_pk_f16_f32 v226, v154, v155\n\t" \
  "v_mfma_f32_16x16x32_bf16 v[164:167], v[212:215], v[120:123], 0\n\t" \
  "s_waitcnt lgkmcnt(5)\n\t" \
  "ds_read_b64_tr_b16 v[210:211], v250 offset:19248\n\t" \
  "v_pk_fma_f16 v225, v224, v224, %[h1] neg_lo:[1,0,0] neg_hi:[1,0,0]\n\t" \
  "v_pk_fma_f16 v227, v226, v226, %[h1] neg_lo:[1,0,0] neg_hi:[1,0,0]\n\t" \
  "v_mfma_f32_16x16x32_bf16 v[148:151], v[216:219], v[100:103], v[148:151]\n\t" \
  "ds_read_u16_d16_hi v198, v198 offset:0\n\t" \
  "v_lshlrev_b32_sdwa v145, 1, v145 dst_sel:DWORD dst_unused:UNUSED_PAD src0_sel:DWORD src1_sel:WORD_1\n\t" \
  "v_pk_mul_f16 v225, v224, v225\n\t" \
  "v_mfma_f32_16x16x32_bf16 v[156:159], v[216:219], v[112:115], v[156:159]\n\t" \
  "ds_read_b64_tr_b16 v[212:213], v250 offset:18488\n\t" \
  "ds_read_u16_d16_hi v199, v199 offset:0\n\t" \
  "v_pk_mul_f16 v227, v226, v227\n\t" \
  "v_exp_f16 v224, v225\n\t" \
  "v_mfma_f32_16x16x32_bf16 v[164:167], v[216:219], v[124:127], v[164:167]\n\t" \
  "s_waitcnt lgkmcnt(6)\n\t" \
  "ds_read_b64_tr_b16 v[214:215], v250 offset:19256\n\t" \
  "v_lshlrev_b32_sdwa v146, 1, v146 dst_sel:DWORD dst_unused:UNUSED_PAD src0_sel:DWORD src1_sel:WORD_1\n\t" \
  "v_exp_f16 v226, v227\n\t" \
  "v_mfma_f32_16x16x32_bf16 v[148:151], v[220:223], v[104:107], v[148:151]\n\t" \
  "ds_read_u16_d16_hi v144, v144 offset:0\n\t" \
  "v_exp_f16_sdwa v224, v225 dst_sel:WORD_1 dst_unused:UNUSED_PRESERVE src0_sel:WORD_1\n\t" \
  "v_mfma_f32_16x16x32_bf16 v[156:159], v[220:223], v[116:119], v[156:159]\n\t" \
  "ds_read_b64_tr_b16 v[216:217], v250 offset:18544\n\t" \
  "v_lshlrev_b32_sdwa v147, 1, v147 dst_sel:DWORD dst_unused:UNUSED_PAD src0_sel:DWORD src1_sel:WORD_1\n\t" \
  "v_mfma_f32_16x16x32_bf16 v[164:167], v[220:223], v[128:131], v[164:167]\n\t" \
  "s_waitcnt lgkmcnt(6)\n\t" \
  "ds_read_b64_tr_b16 v[218:219], v250 offset:19312\n\t" \
  "v_exp_f16_sdwa v226, v227 dst_sel:WORD_1 dst_unused:UNUSED_PRESERVE src0_sel:WORD_1\n\t" \
  "v_smfmac_f32_16x16x64_bf16 v[0:3], v[208:211], v[176:183], v248\n\t" \
  "ds_read_u16_d16_hi v145, v145 offset:0\n\t" \
  "v_pk_fma_f16 v224, v224, %[hc], %[hc]\n\t" \
  "v_smfmac_f32_16x16x64_bf16 v[24:27], v[208:211], v[184:191], v248\n\t" \
  "s_waitcnt lgkmcnt(5)\n\t" \
  "ds_read_b64_tr_b16 v[220:221], v250 offset:18552\n\t" \
  "v_lshlrev_b32_sdwa v160, 1, v160 dst_sel:DWORD dst_unused:UNUSED_PAD src0_sel:DWORD src1_sel:WORD_1\n\t" \
  "v_pk_fma_f16 v226, v226, %[hc], %[hc]\n\t" \
  "v_smfmac_f32_16x16x64_bf16 v[48:51], v[208:211], v[192:199], v248\n\t" \
  "s_waitcnt lgkmcnt(5)\n\t" \
  "ds_read_b64_tr_b16 v[222:223], v250 offset:19320\n\t" \
  "v_rcp_f16 v225, v224\n\t" \
  "v_rcp_f16 v227, v226\n\t" \
  "v_smfmac_f32_16x16x64_bf16 v[4:7], v[212:215], v[176:183], v248\n\t" \
  "ds_read_u16_d16_hi v146, v146 offset:0\n\t" \
  "v_lshlrev_b32_sdwa v161, 1, v161 dst_sel:DWORD dst_unused:UNUSED_PAD src0_sel:DWORD src1_sel:WORD_1\n\t" \
  "v_smfmac_f32_16x16x64_bf16 v[28:31], v[212:215], v[184:191], v248\n\t" \
  "ds_read_b64_tr_b16 v[208:209], v250 offset:18608\n\t" \
  "v_rcp_f16_sdwa v224, v224 dst_sel:DWORD dst_unused:UNUSED_PAD src0_sel:WORD_1\n\t" \
  "v_smfmac_f32_16x16x64_bf16 v[52:55], v[212:215], v[192:199], v248\n\t" \
  "s_waitcnt lgkmcnt(5)\n\t" \
  "ds_read_b64_tr_b16 v[210:211], v250 offset:19376\n\t" \
  "v_lshlrev_b32_sdwa v162, 1, v162 dst_sel:DWORD dst_unused:UNUSED_PAD src0_sel:DWORD src1_sel:WORD_1\n\t" \
  "v_rcp_f16_sdwa v226, v226 dst_sel:DWORD dst_unused:UNUSED_PAD src0_sel:WORD_1\n\t" \
  "v_smfmac_f32_16x16x64_bf16 v[8:11], v[216:219], v[176:183], v248\n\t" \
  "ds_read_u16_d16_hi v147, v147 offset:0\n\t" \
  "v_fma_mix_f32 v152, v152, v225, 0 op_sel_hi:[0,1,0]\n\t" \
  "v_smfmac_f32_16x16x64_bf16 v[32:35], v[216:219], v[184:191], v248\n\t" \
  "ds_read_b64_tr_b16 v[212:213], v250 offset:18616\n\t" \
  "v_fma_mix_f32 v154, v154, v227, 0 op_sel_hi:[0,1,0]\n\t" \
  "v_smfmac_f32_16x16x64_bf16 v[56:59], v[216:219], v[192:199], v248\n\t" \
  "s_waitcnt lgkmcnt(5)\n\t" \
  "ds_read_b64_tr_b16 v[214:215], v250 offset:19384\n\t" \
  "s_barrier\n\t" \
  "ds_read_b128 v[132:135], v251 offset:0\n\t" \
  "v_lshlrev_b32_sdwa v163, 1, v163 dst_sel:DWORD dst_unused:UNUSED_PAD src0_sel:DWORD src1_sel:WORD_1\n\t" \
  "v_fma_mix_f32 v153, v153, v224, 0 op_sel_hi:[0,1,0]\n\t" \
  "v_fma_mix_f32 v155, v155, v226, 0 op_sel_hi:[0,1,0]\n\t" \
  "v_smfmac_f32_16x16x64_bf16 v[12:15], v[220:223], v[176:183], v248\n\t" \
  "ds_read_b64_tr_b16 v[216:217], v249 offset:0\n\t" \
  "ds_read_b128 v[136:139], v251 offset:4096\n\t" \
  "v_cvt_pk_f16_f32 v224, v156, v157\n\t" \
  "v_cvt_pk_f16_f32 v226, v158, v159\n\t" \
  "v_smfmac_f32_16x16x64_bf16 v[36:39], v[220:223], v[184:191], v248\n\t" \
  "ds_read_b64_tr_b16 v[218:219], v249 offset:256\n\t" \
  "v_lshlrev_b32_sdwa v148, 1, v148 dst_sel:DWORD dst_unused:UNUSED_PAD src0_sel:DWORD src1_sel:WORD_1\n\t" \
  "v_pk_fma_f16 v225, v224, v224, %[h1] neg_lo:[1,0,0] neg_hi:[1,0,0]\n\t" \
  "v_smfmac_f32_16x16x64_bf16 v[60:63], v[220:223], v[192:199], v248\n\t" \
  "s_waitcnt lgkmcnt(7)\n\t" \
  "ds_read_b128 v[140:143], v251 offset:8192\n\t" \
  "ds_read_b64_tr_b16 v[220:221], v249 offset:2048\n\t" \
  "v_pk_fma_f16 v227, v226, v226, %[h1] neg_lo:[1,0,0] neg_hi:[1,0,0]\n\t" \
  "v_pk_mul_f16 v225, v224, v225\n\t" \
  "v_smfmac_f32_16x16x64_bf16 v[16:19], v[208:211], v[176:183], v248\n\t" \
  "ds_read_u16_d16_hi v160, v160 offset:0\n\t" \
  "v_lshlrev_b32_sdwa v149, 1, v149 dst_sel:DWORD dst_unused:UNUSED_PAD src0_sel:DWORD src1_sel:WORD_1\n\t" \
  "v_pk_mul_f16 v227, v226, v227\n\t" \
  "v_exp_f16 v224, v225\n\t" \
  "v_smfmac_f32_16x16x64_bf16 v[40:43], v[208:211], v[184:191], v248\n\t" \
  "ds_read_b64_tr_b16 v[222:223], v249 offset:2304\n\t" \
  "v_exp_f16 v226, v227\n\t" \
  "v_smfmac_f32_16x16x64_bf16 v[64:67], v[208:211], v[192:199], v248\n\t" \
  "s_waitcnt lgkmcnt(8)\n\t" \
  "ds_read_u16_d16_hi v161, v161 offset:0\n\t" \
  "ds_read_b64_tr_b16 v[208:209], v249 offset:4096\n\t" \
  "v_lshlrev_b32_sdwa v150, 1, v150 dst_sel:DWORD dst_unused:UNUSED_PAD src0_sel:DWORD src1_sel:WORD_1\n\t" \
  "v_smfmac_f32_16x16x64_bf16 v[20:23], v[212:215], v[176:183], v248\n\t" \
  "ds_read_u16_d16_hi v162, v162 offset:0\n\t" \
  "v_exp_f16_sdwa v224, v225 dst_sel:WORD_1 dst_unused:UNUSED_PRESERVE src0_sel:WORD_1\n\t" \
  "v_smfmac_f32_16x16x64_bf16 v[44:47], v[212:215], v[184:191], v248\n\t" \
  "ds_read_b64_tr_b16 v[210:211], v249 offset:4352\n\t" \
  "v_exp_f16_sdwa v226, v227 dst_sel:WORD_1 dst_unused:UNUSED_PRESERVE src0_sel:WORD_1\n\t" \
  "v_smfmac_f32_16x16x64_bf16 v[68:71], v[212:215], v[192:199], v248\n\t" \
  "s_waitcnt lgkmcnt(8)\n\t" \
  "ds_read_u16_d16_hi v163, v163 offset:0\n\t" \
  "v_lshlrev_b32_sdwa v151, 1, v151 dst_sel:DWORD dst_unused:UNUSED_PAD src0_sel:DWORD src1_sel:WORD_1\n\t" \
  "v_pk_fma_f16 v224, v224, %[hc], %[hc]\n\t" \
  "v_mfma_f32_16x16x32_bf16 v[176:179], v[216:219], v[96:99], 0\n\t" \
  "ds_read_b64_tr_b16 v[212:213], v249 offset:512\n\t" \
  "v_pk_fma_f16 v226, v226, %[hc], %[hc]\n\t" \
  "v_mfma_f32_16x16x32_bf16 v[184:187], v[216:219], v[108:111], 0\n\t" \
  "ds_read_u16_d16_hi v148, v148 offset:0\n\t" \
  "v_lshlrev_b32_sdwa v164, 1, v164 dst_sel:DWORD dst_unused:UNUSED_PAD src0_sel:DWORD src1_sel:WORD_1\n\t" \
  "v_rcp_f16 v225, v224\n\t" \
  "v_mfma_f32_16x16x32_bf16 v[192:195], v[216:219], v[120:123], 0\n\t" \
  "ds_read_b64_tr_b16 v[214:215], v249 offset:768\n\t" \
  "v_rcp_f16 v227, v226\n\t" \
  "v_mfma_f32_16x16x32_bf16 v[200:203], v[216:219], v[132:135], 0\n\t" \
  "s_waitcnt lgkmcnt(8)\n\t" \
  "ds_read_u16_d16_hi v149, v149 offset:0\n\t" \
  "v_lshlrev_b32_sdwa v165, 1, v165 dst_sel:DWORD dst_unused:UNUSED_PAD src0_sel:DWORD src1_sel:WORD_1\n\t" \
  "v_mfma_f32_16x16x32_bf16 v[176:179], v[220:223], v[100:103], v[176:179]\n\t" \
  "ds_read_b64_tr_b16 v[216:217], v249 offset:2560\n\t" \
  "v_rcp_f16_sdwa v224, v224 dst_sel:DWORD dst_unused:UNUSED_PAD src0_sel:WORD_1\n\t" \
  "v_mfma_f32_16x16x32_bf16 v[184:187], v[220:223], v[112:115], v[184:187]\n\t" \
  "ds_read_u16_d16_hi v150, v150 offset:0\n\t" \
  "v_rcp_f16_sdwa v226, v226 dst_sel:DWORD dst_unused:UNUSED_PAD src0_sel:WORD_1\n\t" \
  "v_mfma_f32_16x16x32_bf16 v[192:195], v[220:223], v[124:127], v[192:195]\n\t" \
  "ds_read_b64_tr_b16 v[218:219], v249 offset:2816\n\t" \
  "v_lshlrev_b32_sdwa v166, 1, v166 dst_sel:DWORD dst_unused:UNUSED_PAD src0_sel:DWORD src1_sel:WORD_1\n\t" \
  "v_mfma_f32_16x16x32_bf16 v[200:203], v[220:223], v[136:139], v[200:203]\n\t" \
  "s_waitcnt lgkmcnt(8)\n\t" \
  "ds_read_u16_d16_hi v151, v151 offset:0\n\t" \
  "v_fma_mix_f32 v156, v156, v225, 0 op_sel_hi:[0,1,0]\n\t" \
  "v_mfma_f32_16x16x32_bf16 v[176:179], v[208:211], v[104:107], v[176:179]\n\t" \
  "ds_read_b64_tr_b16 v[220:221], v249 offset:4608\n\t" \
  "v_fma_mix_f32 v158, v158, v227, 0 op_sel_hi:[0,1,0]\n\t" \
  "v_mfma_f32_16x16x32_bf16 v[184:187], v[208:211], v[116:119], v[184:187]\n\t" \
  "ds_read_u16_d16_hi v164, v164 offset:0\n\t" \
  "v_fma_mix_f32 v157, v157, v224, 0 op_sel_hi:[0,1,0]\n\t" \
  "v_mfma_f32_16x16x32_bf16 v[192:195], v[208:211], v[128:131], v[192:195]\n\t" \
  "ds_read_b64_tr_b16 v[222:223], v249 offset:4864\n\t" \
  "v_lshlrev_b32_sdwa v167, 1, v167 dst_sel:DWORD dst_unused:UNUSED_PAD src0_sel:DWORD src1_sel:WORD_1\n\t" \
  "v_fma_mix_f32 v159, v159, v226, 0 op_sel_hi:[0,1,0]\n\t" \
  "v_mfma_f32_16x16x32_bf16 v[200:203], v[208:211], v[140:143], v[200:203]\n\t" \
  "s_waitcnt lgkmcnt(8)\n\t" \
  "ds_read_u16_d16_hi v165, v165 offset:0\n\t" \
  "v_lshlrev_b32_sdwa v176, 1, v176 dst_sel:DWORD dst_unused:UNUSED_PAD src0_sel:DWORD src1_sel:WORD_1\n\t" \
  "v_mfma_f32_16x16x32_bf16 v[180:183], v[212:215], v[96:99], 0\n\t" \
  "ds_read_b64_tr_b16 v[208:209], v250 offset:0\n\t" \
  "v_cvt_pk_f16_f32 v224, v184, v185\n\t" \
  "v_mfma_f32_16x16x32_bf16 v[188:191], v[212:215], v[108:111], 0\n\t" \
  "ds_read_u16_d16_hi v166, v166 offset:0\n\t" \
  "v_cvt_pk_f16_f32 v226, v186, v187\n\t" \
  "v_mfma_f32_16x16x32_bf16 v[196:199], v[212:215], v[120:123], 0\n\t" \
  "ds_read_b64_tr_b16 v[210:211], v250 offset:768\n\t" \
  "v_pk_fma_f16 v225, v224, v224, %[h1] neg_lo:[1,0,0] neg_hi:[1,0,0]\n\t" \
  "v_mfma_f32_16x16x32_bf16 v[204:207], v[212:215], v[132:135], 0\n\t" \
  "s_waitcnt lgkmcnt(8)\n\t" \
  "ds_read_u16_d16_hi v167, v167 offset:0\n\t" \
  "v_lshlrev_b32_sdwa v177, 1, v177 dst_sel:DWORD dst_unused:UNUSED_PAD src0_sel:DWORD src1_sel:WORD_1\n\t" \
  "v_pk_fma_f16 v227, v226, v226, %[h1] neg_lo:[1,0,0] neg_hi:[1,0,0]\n\t" \
  "v_mfma_f32_16x16x32_bf16 v[180:183], v[216:219], v[100:103], v[180:183]\n\t" \
  "ds_read_b64_tr_b16 v[212:213], v250 offset:8\n\t" \
  "v_pk_mul_f16 v225, v224, v225\n\t" \
  "v_mfma_f32_16x16x32_bf16 v[188:191], v[216:219], v[112:115], v[188:191]\n\t" \
  "ds_read_u16_d16_hi v176, v176 offset:0\n\t" \
  "v_lshlrev_b32_sdwa v178, 1, v178 dst_sel:DWORD dst_unused:UNUSED_PAD src0_sel:DWORD src1_sel:WORD_1\n\t" \
  "v_pk_mul_f16 v227, v226, v227\n\t" \
  "v_exp_f16 v224, v225\n\t" \
  "v_mfma_f32_16x16x32_bf16 v[196:199], v[216:219], v[124:127], v[196:199]\n\t" \
  "ds_read_b64_tr_b16 v[214:215], v250 offset:776\n\t" \
  "v_lshlrev_b32_sdwa v179, 1, v179 dst_sel:DWORD dst_unused:UNUSED_PAD src0_sel:DWORD src1_sel:WORD_1\n\t" \
  "v_mfma_f32_16x16x32_bf16 v[204:207], v[216:219], v[136:139], v[204:207]\n\t" \
  "s_waitcnt lgkmcnt(8)\n\t" \
  "ds_read_u16_d16_hi v177, v177 offset:0\n\t" \
  "v_exp_f16 v226, v227\n\t" \
  "v_mfma_f32_16x16x32_bf16 v[180:183], v[220:223], v[104:107], v[180:183]\n\t" \
  "ds_read_b64_tr_b16 v[216:217], v250 offset:64\n\t" \
  "v_lshlrev_b32_sdwa v192, 1, v192 dst_sel:DWORD dst_unused:UNUSED_PAD src0_sel:DWORD src1_sel:WORD_1\n\t" \
  "v_mfma_f32_16x16x32_bf16 v[188:191], v[220:223], v[116:119], v[188:191]\n\t" \
  "ds_read_u16_d16_hi v178, v178 offset:0\n\t" \
  "v_exp_f16_sdwa v224, v225 dst_sel:WORD_1 dst_unused:UNUSED_PRESERVE src0_sel:WORD_1\n\t" \
  "v_mfma_f32_16x16x32_bf16 v[196:199], v[220:223], v[128:131], v[196:199]\n\t" \
  "ds_read_b64_tr_b16 v[218:219], v250 offset:832\n\t" \
  "v_exp_f16_sdwa v226, v227 dst_sel:WORD_1 dst_unused:UNUSED_PRESERVE src0_sel:WORD_1\n\t" \
  "v_mfma_f32_16x16x32_bf16 v[204:207], v[220:223], v[140:143], v[204:207]\n\t" \
  "s_waitcnt lgkmcnt(8)\n\t" \
  "ds_read_u16_d16_hi v179, v179 offset:0\n\t" \
  "v_lshlrev_b32_sdwa v193, 1, v193 dst_sel:DWORD dst_unused:UNUSED_PAD src0_sel:DWORD src1_sel:WORD_1\n\t" \
  "v_smfmac_f32_16x16x64_bf16 v[0:3], v[208:211], v[144:151], v248\n\t" \
  "ds_read_b64_tr_b16 v[220:221], v250 offset:72\n\t" \
  "v_pk_fma_f16 v224, v224, %[hc], %[hc]\n\t" \
  "v_smfmac_f32_16x16x64_bf16 v[24:27], v[208:211], v[152:159], v248\n\t" \
  "s_waitcnt lgkmcnt(9)\n\t" \
  "ds_read_u16_d16_hi v192, v192 offset:0\n\t" \
  "v_lshlrev_b32_sdwa v194, 1, v194 dst_sel:DWORD dst_unused:UNUSED_PAD src0_sel:DWORD src1_sel:WORD_1\n\t" \
  "v_pk_fma_f16 v226, v226, %[hc], %[hc]\n\t" \
  "v_smfmac_f32_16x16x64_bf16 v[48:51], v[208:211], v[160:167], v248\n\t" \
  "s_waitcnt lgkmcnt(7)\n\t" \
  "ds_read_b64_tr_b16 v[222:223], v250 offset:840\n\t" \
  "v_rcp_f16 v225, v224\n\t" \
  "v_lshlrev_b32_sdwa v195, 1, v195 dst_sel:DWORD dst_unused:UNUSED_PAD src0_sel:DWORD src1_sel:WORD_1\n\t" \
  "v_rcp_f16 v227, v226\n\t" \
  "v_smfmac_f32_16x16x64_bf16 v[4:7], v[212:215], v[144:151], v248\n\t" \
  "ds_read_b64 v[230:231], v252 offset:0\n\t" \
  "ds_read_b64_tr_b16 v[208:209], v250 offset:128\n\t" \
  "v_smfmac_f32_16x16x64_bf16 v[28:31], v[212:215], v[152:159], v248\n\t" \
  "ds_read_b64 v[232:233], v252 offset:8\n\t" \
  "ds_read_b64_tr_b16 v[210:211], v250 offset:896\n\t" \
  "v_lshlrev_b32_sdwa v200, 1, v200 dst_sel:DWORD dst_unused:UNUSED_PAD src0_sel:DWORD src1_sel:WORD_1\n\t" \
  "v_rcp_f16_sdwa v224, v224 dst_sel:DWORD dst_unused:UNUSED_PAD src0_sel:WORD_1\n\t" \
  "v_smfmac_f32_16x16x64_bf16 v[52:55], v[212:215], v[160:167], v248\n\t" \
  "s_waitcnt lgkmcnt(8)\n\t" \
  "ds_read_b64 v[234:235], v252 offset:64\n\t" \
  "ds_read_b64_tr_b16 v[212:213], v250 offset:136\n\t" \
  "v_lshlrev_b32_sdwa v201, 1, v201 dst_sel:DWORD dst_unused:UNUSED_PAD src0_sel:DWORD src1_sel:WORD_1\n\t" \
  "v_rcp_f16_sdwa v226, v226 dst_sel:DWORD dst_unused:UNUSED_PAD src0_sel:WORD_1\n\t" \
  "v_smfmac_f32_16x16x64_bf16 v[8:11], v[216:219], v[144:151], v248\n\t" \
  "ds_read_b64 v[236:237], v252 offset:72\n\t" \
  "ds_read_b64_tr_b16 v[214:215], v250 offset:904\n\t" \
  "v_fma_mix_f32 v184, v184, v225, 0 op_sel_hi:[0,1,0]\n\t" \
  "v_smfmac_f32_16x16x64_bf16 v[32:35], v[216:219], v[152:159], v248\n\t" \
  "ds_read_b64 v[242:243], v252 offset:128\n\t" \
  "ds_read_b64 v[244:245], v252 offset:136\n\t" \
  "v_lshlrev_b32_sdwa v202, 1, v202 dst_sel:DWORD dst_unused:UNUSED_PAD src0_sel:DWORD src1_sel:WORD_1\n\t" \
  "v_fma_mix_f32 v186, v186, v227, 0 op_sel_hi:[0,1,0]\n\t" \
  "v_smfmac_f32_16x16x64_bf16 v[56:59], v[216:219], v[160:167], v248\n\t" \
  "s_waitcnt lgkmcnt(10)\n\t" \
  "s_barrier\n\t" \
  "ds_read_b64_tr_b16 v[216:217], v249 offset:6144\n\t" \
  "ds_read_u16_d16_hi v193, v193 offset:0\n\t" \
  "v_fma_mix_f32 v185, v185, v224, 0 op_sel_hi:[0,1,0]\n\t" \
  "v_lshlrev_b32_sdwa v203, 1, v203 dst_sel:DWORD dst_unused:UNUSED_PAD src0_sel:DWORD src1_sel:WORD_1\n\t" \
  "v_fma_mix_f32 v187, v187, v226, 0 op_sel_hi:[0,1,0]\n\t" \
  "v_smfmac_f32_16x16x64_bf16 v[12:15], v[220:223], v[144:151], v248\n\t" \
  "ds_read_b64_tr_b16 v[218:219], v249 offset:6400\n\t" \
  "ds_read_u16_d16_hi v194, v194 offset:0\n\t" \
  "v_cvt_pk_f16_f32 v224, v188, v189\n\t" \
  "v_cvt_pk_f16_f32 v226, v190, v191\n\t" \
  "v_smfmac_f32_16x16x64_bf16 v[36:39], v[220:223], v[152:159], v248\n\t" \
  "ds_read_u16_d16_hi v195, v195 offset:0\n\t" \
  "v_lshlrev_b32_sdwa v180, 1, v180 dst_sel:DWORD dst_unused:UNUSED_PAD src0_sel:DWORD src1_sel:WORD_1\n\t" \
  "v_pk_fma_f16 v225, v224, v224, %[h1] neg_lo:[1,0,0] neg_hi:[1,0,0]\n\t" \
  "v_smfmac_f32_16x16x64_bf16 v[60:63], v[220:223], v[160:167], v248\n\t" \
  "s_waitcnt lgkmcnt(11)\n\t" \
  "ds_read_b64_tr_b16 v[220:221], v249 offset:8192\n\t" \
  "ds_read_u16_d16_hi v200, v200 offset:0\n\t" \
  "v_pk_fma_f16 v227, v226, v226, %[h1] neg_lo:[1,0,0] neg_hi:[1,0,0]\n\t" \
  "v_lshlrev_b32_sdwa v181, 1, v181 dst_sel:DWORD dst_unused:UNUSED_PAD src0_sel:DWORD src1_sel:WORD_1\n\t" \
  "v_pk_mul_f16 v225, v224, v225\n\t" \
  "v_smfmac_f32_16x16x64_bf16 v[16:19], v[208:211], v[144:151], v248\n\t" \
  "ds_read_b64_tr_b16 v[222:223], v249 offset:8448\n\t" \
  "v_pk_mul_f16 v227, v226, v227\n\t" \
  "v_exp_f16 v224, v225\n\t" \
  "v_smfmac_f32_16x16x64_bf16 v[40:43], v[208:211], v[152:159], v248\n\t" \
  "ds_read_u16_d16_hi v201, v201 offset:0\n\t" \
  "v_lshlrev_b32_sdwa v182, 1, v182 dst_sel:DWORD dst_unused:UNUSED_PAD src0_sel:DWORD src1_sel:WORD_1\n\t" \
  "v_smfmac_f32_16x16x64_bf16 v[64:67], v[208:211], v[160:167], v248\n\t" \
  "s_waitcnt lgkmcnt(11)\n\t" \
  "ds_read_b64_tr_b16 v[208:209], v249 offset:10240\n\t" \
  "ds_read_u16_d16_hi v202, v202 offset:0\n\t" \
  "v_exp_f16 v226, v227\n\t" \
  "v_lshlrev_b32_sdwa v183, 1, v183 dst_sel:DWORD dst_unused:UNUSED_PAD src0_sel:DWORD src1_sel:WORD_1\n\t" \
  "v_smfmac_f32_16x16x64_bf16 v[20:23], v[212:215], v[144:151], v248\n\t" \
  "ds_read_b64_tr_b16 v[210:211], v249 offset:10496\n\t" \
  "v_exp_f16_sdwa v224, v225 dst_sel:WORD_1 dst_unused:UNUSED_PRESERVE src0_sel:WORD_1\n\t" \
  "v_smfmac_f32_16x16x64_bf16 v[44:47], v[212:215], v[152:159], v248\n\t" \
  "ds_read_u16_d16_hi v203, v203 offset:0\n\t" \
  "v_lshlrev_b32_sdwa v196, 1, v196 dst_sel:DWORD dst_unused:UNUSED_PAD src0_sel:DWORD src1_sel:WORD_1\n\t" \
  "v_exp_f16_sdwa v226, v227 dst_sel:WORD_1 dst_unused:UNUSED_PRESERVE src0_sel:WORD_1\n\t" \
  "v_smfmac_f32_16x16x64_bf16 v[68:71], v[212:215], v[160:167], v248\n\t" \
  "ds_read_b64_tr_b16 v[212:213], v249 offset:6656\n\t" \
  "ds_read_u16_d16_hi v180, v180 offset:0\n\t" \
  "v_mfma_f32_16x16x16_bf16 v[72:75], v[246:247], v[230:231], 0\n\t" \
  "s_waitcnt lgkmcnt(12)\n\t" \
  "ds_read_b64_tr_b16 v[214:215], v249 offset:6912\n\t" \
  "v_lshlrev_b32_sdwa v197, 1, v197 dst_sel:DWORD dst_unused:UNUSED_PAD src0_sel:DWORD src1_sel:WORD_1\n\t" \
  "v_pk_fma_f16 v224, v224, %[hc], %[hc]\n\t" \
  "v_mfma_f32_16x16x32_bf16 v[144:147], v[216:219], v[96:99], 0\n\t" \
  "ds_read_u16_d16_hi v181, v181 offset:0\n\t" \
  "v_pk_fma_f16 v226, v226, %[hc], %[hc]\n\t" \
  "v_mfma_f32_16x16x32_bf16 v[152:155], v[216:219], v[108:111], 0\n\t" \
  "ds_read_u16_d16_hi v182, v182 offset:0\n\t" \
  "v_lshlrev_b32_sdwa v198, 1, v198 dst_sel:DWORD dst_unused:UNUSED_PAD src0_sel:DWORD src1_sel:WORD_1\n\t" \
  "v_rcp_f16 v225, v224\n\t" \
  "v_mfma_f32_16x16x32_bf16 v[160:163], v[216:219], v[120:123], 0\n\t" \
  "ds_read_u16_d16_hi v183, v183 offset:0\n\t" \
  "v_rcp_f16 v227, v226\n\t" \
  "v_mfma_f32_16x16x16_bf16 v[76:79], v[246:247], v[232:233], 0\n\t" \
  "ds_read_u16_d16_hi v196, v196 offset:0\n\t" \
  "v_lshlrev_b32_sdwa v199, 1, v199 dst_sel:DWORD dst_unused:UNUSED_PAD src0_sel:DWORD src1_sel:WORD_1\n\t" \
  "v_mfma_f32_16x16x32_bf16 v[168:171], v[216:219], v[132:135], 0\n\t" \
  "s_waitcnt lgkmcnt(12)\n\t" \
  "ds_read_b64_tr_b16 v[216:217], v249 offset:8704\n\t" \
  "v_rcp_f16_sdwa v224, v224 dst_sel:DWORD dst_unused:UNUSED_PAD src0_sel:WORD_1\n\t" \
  "v_mfma_f32_16x16x32_bf16 v[144:147], v[220:223], v[100:103], v[144:147]\n\t" \
  "ds_read_u16_d16_hi v197, v197 offset:0\n\t" \
  "v_lshlrev_b32_sdwa v204, 1, v204 dst_sel:DWORD dst_unused:UNUSED_PAD src0_sel:DWORD src1_sel:WORD_1\n\t" \
  "v_mfma_f32_16x16x32_bf16 v[152:155], v[220:223], v[112:115], v[152:155]\n\t" \
  "ds_read_b64_tr_b16 v[218:219], v249 offset:8960\n\t" \
  "v_rcp_f16_sdwa v226, v226 dst_sel:DWORD dst_unused:UNUSED_PAD src0_sel:WORD_1\n\t" \
  "v_mfma_f32_16x16x32_bf16 v[160:163], v[220:223], v[124:127], v[160:163]\n\t" \
  "ds_read_u16_d16_hi v198, v198 offset:0\n\t" \
  "v_lshlrev_b32_sdwa v205, 1, v205 dst_sel:DWORD dst_unused:UNUSED_PAD src0_sel:DWORD src1_sel:WORD_1\n\t" \
  "v_mfma_f32_16x16x32_bf16 v[168:171], v[220:223], v[136:139], v[168:171]\n\t" \
  "ds_read_b64_tr_b16 v[220:221], v249 offset:10752\n\t" \
  "v_fma_mix_f32 v188, v188, v225, 0 op_sel_hi:[0,1,0]\n\t" \
  "v_mfma_f32_16x16x16_bf16 v[80:83], v[246:247], v[234:235], 0\n\t" \
  "s_waitcnt lgkmcnt(13)\n\t" \
  "ds_read_u16_d16_hi v199, v199 offset:0\n\t" \
  "v_fma_mix_f32 v190, v190, v227, 0 op_sel_hi:[0,1,0]\n\t" \
  "v_mfma_f32_16x16x32_bf16 v[144:147], v[208:211], v[104:107], v[144:147]\n\t" \
  "ds_read_b64_tr_b16 v[222:223], v249 offset:11008\n\t" \
  "v_lshlrev_b32_sdwa v206, 1, v206 dst_sel:DWORD dst_unused:UNUSED_PAD src0_sel:DWORD src1_sel:WORD_1\n\t" \
  "v_mfma_f32_16x16x32_bf16 v[152:155], v[208:211], v[116:119], v[152:155]\n\t" \
  "ds_read_u16_d16_hi v204, v204 offset:0\n\t" \
  "v_fma_mix_f32 v189, v189, v224, 0 op_sel_hi:[0,1,0]\n\t" \
  "v_mfma_f32_16x16x32_bf16 v[160:163], v[208:211], v[128:131], v[160:163]\n\t" \
  "ds_read_u16_d16_hi v205, v205 offset:0\n\t" \
  "v_fma_mix_f32 v191, v191, v226, 0 op_sel_hi:[0,1,0]\n\t" \
  "v_mfma_f32_16x16x16_bf16 v[84:87], v[246:247], v[236:237], 0\n\t" \
  "ds_read_u16_d16_hi v206, v206 offset:0\n\t" \
  "v_lshlrev_b32_sdwa v207, 1, v207 dst_sel:DWORD dst_unused:UNUSED_PAD src0_sel:DWORD src1_sel:WORD_1\n\t" \
  "v_mfma_f32_16x16x32_bf16 v[168:171], v[208:211], v[140:143], v[168:171]\n\t" \
  "s_waitcnt lgkmcnt(14)\n\t" \
  "ds_read_b64_tr_b16 v[208:209], v250 offset:6160\n\t" \
  "v_lshlrev_b32_sdwa v144, 1, v144 dst_sel:DWORD dst_unused:UNUSED_PAD src0_sel:DWORD src1_sel:WORD_1\n\t" \
  "v_mfma_f32_16x16x32_bf16 v[148:151], v[212:215], v[96:99], 0\n\t" \
  "ds_read_u16_d16_hi v207, v207 offset:0\n\t" \
  "v_cvt_pk_f16_f32 v224, v152, v153\n\t" \
  "v_mfma_f32_16x16x32_bf16 v[156:159], v[212:215], v[108:111], 0\n\t" \
  "ds_read_b64_tr_b16 v[210:211], v250 offset:6928\n\t" \
  "v_cvt_pk_f16_f32 v226, v154, v155\n\t" \
  "v_mfma_f32_16x16x32_bf16 v[164:167], v[212:215], v[120:123], 0\n\t" \
  "ds_read_u16_d16_hi v144, v144 offset:0\n\t" \
  "v_pk_fma_f16 v225, v224, v224, %[h1] neg_lo:[1,0,0] neg_hi:[1,0,0]\n\t" \
  "v_mfma_f32_16x16x32_bf16 v[172:175], v[212:215], v[132:135], 0\n\t" \
  "ds_read_b64_tr_b16 v[212:213], v250 offset:6168\n\t" \
  "v_lshlrev_b32_sdwa v145, 1, v145 dst_sel:DWORD dst_unused:UNUSED_PAD src0_sel:DWORD src1_sel:WORD_1\n\t" \
  "v_mfma_f32_16x16x16_bf16 v[88:91], v[246:247], v[242:243], 0\n\t" \
  "s_waitcnt lgkmcnt(12)\n\t" \
  "ds_read_u16_d16_hi v145, v145 offset:0\n\t" \
  "v_pk_fma_f16 v227, v226, v226, %[h1] neg_lo:[1,0,0] neg_hi:[1,0,0]\n\t" \
  "v_mfma_f32_16x16x32_bf16 v[148:151], v[216:219], v[100:103], v[148:151]\n\t" \
  "ds_read_b64_tr_b16 v[214:215], v250 offset:6936\n\t" \
  "v_pk_mul_f16 v225, v224, v225\n\t" \
  "v_mfma_f32_16x16x32_bf16 v[156:159], v[216:219], v[112:115], v[156:159]\n\t" \
  "v_lshlrev_b32_sdwa v146, 1, v146 dst_sel:DWORD dst_unused:UNUSED_PAD src0_sel:DWORD src1_sel:WORD_1\n\t" \
  "v_pk_mul_f16 v227, v226, v227\n\t" \
  "v_exp_f16 v224, v225\n\t" \
  "v_mfma_f32_16x16x32_bf16 v[164:167], v[216:219], v[124:127], v[164:167]\n\t" \
  "ds_read_u16_d16_hi v146, v146 offset:0\n\t" \
  "v_lshlrev_b32_sdwa v147, 1, v147 dst_sel:DWORD dst_unused:UNUSED_PAD src0_sel:DWORD src1_sel:WORD_1\n\t" \
  "v_mfma_f32_16x16x16_bf16 v[92:95], v[246:247], v[244:245], 0\n\t" \
  "ds_read_u16_d16_hi v147, v147 offset:0\n\t" \
  "v_exp_f16 v226, v227\n\t" \
  "v_mfma_f32_16x16x32_bf16 v[172:175], v[216:219], v[136:139], v[172:175]\n\t" \
  "s_waitcnt lgkmcnt(12)\n\t" \
  "ds_read_b64_tr_b16 v[216:217], v250 offset:6224\n\t" \
  "v_lshlrev_b32_sdwa v160, 1, v160 dst_sel:DWORD dst_unused:UNUSED_PAD src0_sel:DWORD src1_sel:WORD_1\n\t" \
  "v_mfma_f32_16x16x32_bf16 v[148:151], v[220:223], v[104:107], v[148:151]\n\t" \
  "ds_read_u16_d16_hi v160, v160 offset:0\n\t" \
  "v_exp_f16_sdwa v224, v225 dst_sel:WORD_1 dst_unused:UNUSED_PRESERVE src0_sel:WORD_1\n\t" \
  "v_mfma_f32_16x16x32_bf16 v[156:159], v[220:223], v[116:119], v[156:159]\n\t" \
  "ds_read_b64_tr_b16 v[218:219], v250 offset:6992\n\t" \
  "v_exp_f16_sdwa v226, v227 dst_sel:WORD_1 dst_unused:UNUSED_PRESERVE src0_sel:WORD_1\n\t" \
  "v_mfma_f32_16x16x32_bf16 v[164:167], v[220:223], v[128:131], v[164:167]\n\t" \
  "v_lshlrev_b32_sdwa v161, 1, v161 dst_sel:DWORD dst_unused:UNUSED_PAD src0_sel:DWORD src1_sel:WORD_1\n\t" \
  "v_pk_fma_f16 v224, v224, %[hc], %[hc]\n\t" \
  "v_mfma_f32_16x16x32_bf16 v[172:175], v[220:223], v[140:143], v[172:175]\n\t" \
  "s_waitcnt lgkmcnt(9)\n\t" \
  "ds_read_u16_d16_hi v161, v161 offset:0\n\t" \
  "v_lshlrev_b32_sdwa v162, 1, v162 dst_sel:DWORD dst_unused:UNUSED_PAD src0_sel:DWORD src1_sel:WORD_1\n\t" \
  "v_smfmac_f32_16x16x64_bf16 v[0:3], v[208:211], v[176:183], v248\n\t" \
  "ds_read_b64_tr_b16 v[220:221], v250 offset:6232\n\t" \
  "v_pk_fma_f16 v226, v226, %[hc], %[hc]\n\t" \
  "v_smfmac_f32_16x16x64_bf16 v[24:27], v[208:211], v[184:191], v248\n\t" \
  "ds_read_u16_d16_hi v162, v162 offset:0\n\t" \
  "v_rcp_f16 v225, v224\n\t" \
  "v_smfmac_f32_16x16x64_bf16 v[48:51], v[208:211], v[192:199], v248\n\t" \
  "ds_read_b64_tr_b16 v[222:223], v250 offset:7000\n\t" \
  "v_lshlrev_b32_sdwa v163, 1, v163 dst_sel:DWORD dst_unused:UNUSED_PAD src0_sel:DWORD src1_sel:WORD_1\n\t" \
  "v_smfmac_f32_16x16x64_bf16 v[72:75], v[208:211], v[200:207], v248\n\t" \
  "s_waitcnt lgkmcnt(9)\n\t" \
  "ds_read_u16_d16_hi v163, v163 offset:0\n\t" \
  "v_rcp_f16 v227, v226\n\t" \
  "v_smfmac_f32_16x16x64_bf16 v[4:7], v[212:215], v[176:183], v248\n\t" \
  "ds_read_b64_tr_b16 v[208:209], v250 offset:6288\n\t" \
  "v_lshlrev_b32_sdwa v168, 1, v168 dst_sel:DWORD dst_unused:UNUSED_PAD src0_sel:DWORD src1_sel:WORD_1\n\t" \
  "v_smfmac_f32_16x16x64_bf16 v[28:31], v[212:215], v[184:191], v248\n\t" \
  "ds_read_u16_d16_hi v168, v168 offset:0\n\t" \
  "v_rcp_f16_sdwa v224, v224 dst_sel:DWORD dst_unused:UNUSED_PAD src0_sel:WORD_1\n\t" \
  "v_smfmac_f32_16x16x64_bf16 v[52:55], v[212:215], v[192:199], v248\n\t" \
  "ds_read_b64_tr_b16 v[210:211], v250 offset:7056\n\t" \
  "v_lshlrev_b32_sdwa v169, 1, v169 dst_sel:DWORD dst_unused:UNUSED_PAD src0_sel:DWORD src1_sel:WORD_1\n\t" \
  "v_smfmac_f32_16x16x64_bf16 v[76:79], v[212:215], v[200:207], v248\n\t" \
  "s_waitcnt lgkmcnt(8)\n\t" \
  "ds_read_u16_d16_hi v169, v169 offset:0\n\t" \
  "v_rcp_f16_sdwa v226, v226 dst_sel:DWORD dst_unused:UNUSED_PAD src0_sel:WORD_1\n\t" \
  "v_smfmac_f32_16x16x64_bf16 v[8:11], v[216:219], v[176:183], v248\n\t" \
  "ds_read_b64_tr_b16 v[212:213], v250 offset:6296\n\t" \
  "v_fma_mix_f32 v152, v152, v225, 0 op_sel_hi:[0,1,0]\n\t" \
  "v_smfmac_f32_16x16x64_bf16 v[32:35], v[216:219], v[184:191], v248\n\t" \
  "ds_read_b64_tr_b16 v[214:215], v250 offset:7064\n\t" \
  "v_lshlrev_b32_sdwa v170, 1, v170 dst_sel:DWORD dst_unused:UNUSED_PAD src0_sel:DWORD src1_sel:WORD_1\n\t" \
  "v_fma_mix_f32 v154, v154, v227, 0 op_sel_hi:[0,1,0]\n\t" \
  "v_smfmac_f32_16x16x64_bf16 v[56:59], v[216:219], v[192:199], v248\n\t" \
  "ds_read_u16_d16_hi v170, v170 offset:0\n\t" \
  "v_fma_mix_f32 v153, v153, v224, 0 op_sel_hi:[0,1,0]\n\t" \
  "v_smfmac_f32_16x16x64_bf16 v[80:83], v[216:219], v[200:207], v248\n\t" \
  "s_waitcnt lgkmcnt(8)\n\t" \
  "s_barrier\n\t" \
  "s_mov_b32 %[lc], 3\n\t" \
  ".Lcnx_m:\n\t" \
  "ds_read_b64_tr_b16 v[216:217], v249 offset:12288\n\t" \
  "v_lshlrev_b32_sdwa v171, 1, v171 dst_sel:DWORD dst_unused:UNUSED_PAD src0_sel:DWORD src1_sel:WORD_1\n\t" \
  "v_fma_mix_f32 v155, v155, v226, 0 op_sel_hi:[0,1,0]\n\t" \
  "v_smfmac_f32_16x16x64_bf16 v[12:15], v[220:223], v[176:183], v248\n\t" \
  "ds_read_u16_d16_hi v171, v171 offset:0\n\t" \
  "v_cvt_pk_f16_f32 v224, v156, v157\n\t" \
  "v_cvt_pk_f16_f32 v226, v158, v159\n\t" \
  "v_smfmac_f32_16x16x64_bf16 v[36:39], v[220:223], v[184:191], v248\n\t" \
  "ds_read_b64_tr_b16 v[218:219], v249 offset:12544\n\t" \
  "v_lshlrev_b32_sdwa v148, 1, v148 dst_sel:DWORD dst_unused:UNUSED_PAD src0_sel:DWORD src1_sel:WORD_1\n\t" \
  "v_pk_fma_f16 v225, v224, v224, %[h1] neg_lo:[1,0,0] neg_hi:[1,0,0]\n\t" \
  "v_smfmac_f32_16x16x64_bf16 v[60:63], v[220:223], v[192:199], v248\n\t" \
  "ds_read_u16_d16_hi v148, v148 offset:0\n\t" \
  "v_pk_fma_f16 v227, v226, v226, %[h1] neg_lo:[1,0,0] neg_hi:[1,0,0]\n\t" \
  "v_smfmac_f32_16x16x64_bf16 v[84:87], v[220:223], v[200:207], v248\n\t" \
  "s_waitcnt lgkmcnt(8)\n\t" \
  "ds_read_b64_tr_b16 v[220:221], v249 offset:14336\n\t" \
  "v_lshlrev_b32_sdwa v149, 1, v149 dst_sel:DWORD dst_unused:UNUSED_PAD src0_sel:DWORD src1_sel:WORD_1\n\t" \
  "v_pk_mul_f16 v225, v224, v225\n\t" \
  "v_smfmac_f32_16x16x64_bf16 v[16:19], v[208:211], v[176:183], v248\n\t" \
  "s_add_u32 %[rs], %[rs], 1\n\t" \
  "ds_read_u16_d16_hi v149, v149 offset:0\n\t" \
  "v_pk_mul_f16 v227, v226, v227\n\t" \
  "v_exp_f16 v224, v225\n\t" \
  "v_smfmac_f32_16x16x64_bf16 v[40:43], v[208:211], v[184:191], v248\n\t" \
  "s_cmp_eq_u32 %[rs], 6\n\t" \
  "ds_read_b64_tr_b16 v[222:223], v249 offset:14592\n\t" \
  "v_lshlrev_b32_sdwa v150, 1, v150 dst_sel:DWORD dst_unused:UNUSED_PAD src0_sel:DWORD src1_sel:WORD_1\n\t" \
  "v_smfmac_f32_16x16x64_bf16 v[64:67], v[208:211], v[192:199], v248\n\t" \
  "ds_read_u16_d16_hi v150, v150 offset:0\n\t" \
  "v_exp_f16 v226, v227\n\t" \
  "v_smfmac_f32_16x16x64_bf16 v[88:91], v[208:211], v[200:207], v248\n\t" \
  "s_waitcnt lgkmcnt(9)\n\t" \
  "ds_read_b64_tr_b16 v[208:209], v249 offset:16384\n\t" \
  "v_lshlrev_b32_sdwa v151, 1, v151 dst_sel:DWORD dst_unused:UNUSED_PAD src0_sel:DWORD src1_sel:WORD_1\n\t" \
  "v_smfmac_f32_16x16x64_bf16 v[20:23], v[212:215], v[176:183], v248\n\t" \
  "s_cselect_b32 %[rs], 0, %[rs]\n\t" \
  "ds_read_u16_d16_hi v151, v151 offset:0\n\t" \
  "v_exp_f16_sdwa v224, v225 dst_sel:WORD_1 dst_unused:UNUSED_PRESERVE src0_sel:WORD_1\n\t" \
  "v_smfmac_f32_16x16x64_bf16 v[44:47], v[212:215], v[184:191], v248\n\t" \
  "s_cselect_b32 %[t0], -75920, %[ps]\n\t" \
  "ds_read_b64_tr_b16 v[210:211], v249 offset:16640\n\t" \
  "v_lshlrev_b32_sdwa v164, 1, v164 dst_sel:DWORD dst_unused:UNUSED_PAD src0_sel:DWORD src1_sel:WORD_1\n\t" \
  "v_exp_f16_sdwa v226, v227 dst_sel:WORD_1 dst_unused:UNUSED_PRESERVE src0_sel:WORD_1\n\t" \
  "v_smfmac_f32_16x16x64_bf16 v[68:71], v[212:215], v[192:199], v248\n\t" \
  "ds_read_u16_d16_hi v164, v164 offset:0\n\t" \
  "v_add_u32 v252, %[t0], v252\n\t" \
  "v_smfmac_f32_16x16x64_bf16 v[92:95], v[212:215], v[200:207], v248\n\t" \
  "s_waitcnt lgkmcnt(9)\n\t" \
  "ds_read_b64_tr_b16 v[212:213], v249 offset:12800\n\t" \
  "v_lshlrev_b32_sdwa v165, 1, v165 dst_sel:DWORD dst_unused:UNUSED_PAD src0_sel:DWORD src1_sel:WORD_1\n\t" \
  "v_pk_fma_f16 v224, v224, %[hc], %[hc]\n\t" \
  "v_mfma_f32_16x16x32_bf16 v[176:179], v[216:219], v[96:99], 0\n\t" \
  "ds_read_u16_d16_hi v165, v165 offset:0\n\t" \
  "v_pk_fma_f16 v226, v226, %[hc], %[hc]\n\t" \
  "v_mfma_f32_16x16x32_bf16 v[184:187], v[216:219], v[108:111], 0\n\t" \
  "ds_read_b64_tr_b16 v[214:215], v249 offset:13056\n\t" \
  "v_lshlrev_b32_sdwa v166, 1, v166 dst_sel:DWORD dst_unused:UNUSED_PAD src0_sel:DWORD src1_sel:WORD_1\n\t" \
  "v_rcp_f16 v225, v224\n\t" \
  "v_mfma_f32_16x16x32_bf16 v[192:195], v[216:219], v[120:123], 0\n\t" \
  "ds_read_u16_d16_hi v166, v166 offset:0\n\t" \
  "v_rcp_f16 v227, v226\n\t" \
  "v_mfma_f32_16x16x32_bf16 v[200:203], v[216:219], v[132:135], 0\n\t" \
  "s_waitcnt lgkmcnt(9)\n\t" \
  "ds_read_b64_tr_b16 v[216:217], v249 offset:14848\n\t" \
  "v_lshlrev_b32_sdwa v167, 1, v167 dst_sel:DWORD dst_unused:UNUSED_PAD src0_sel:DWORD src1_sel:WORD_1\n\t" \
  "v_mfma_f32_16x16x32_bf16 v[176:179], v[220:223], v[100:103], v[176:179]\n\t" \
  "ds_read_u16_d16_hi v167, v167 offset:0\n\t" \
  "v_rcp_f16_sdwa v224, v224 dst_sel:DWORD dst_unused:UNUSED_PAD src0_sel:WORD_1\n\t" \
  "v_mfma_f32_16x16x32_bf16 v[184:187], v[220:223], v[112:115], v[184:187]\n\t" \
  "ds_read_b64_tr_b16 v[218:219], v249 offset:15104\n\t" \
  "v_lshlrev_b32_sdwa v172, 1, v172 dst_sel:DWORD dst_unused:UNUSED_PAD src0_sel:DWORD src1_sel:WORD_1\n\t" \
  "v_mfma_f32_16x16x32_bf16 v[192:195], v[220:223], v[124:127], v[192:195]\n\t" \
  "ds_read_u16_d16_hi v172, v172 offset:0\n\t" \
  "v_rcp_f16_sdwa v226, v226 dst_sel:DWORD dst_unused:UNUSED_PAD src0_sel:WORD_1\n\t" \
  "v_mfma_f32_16x16x32_bf16 v[200:203], v[220:223], v[136:139], v[200:203]\n\t" \
  "s_waitcnt lgkmcnt(9)\n\t" \
  "ds_read_b64_tr_b16 v[220:221], v249 offset:16896\n\t" \
  "v_lshlrev_b32_sdwa v173, 1, v173 dst_sel:DWORD dst_unused:UNUSED_PAD src0_sel:DWORD src1_sel:WORD_1\n\t" \
  "v_fma_mix_f32 v156, v156, v225, 0 op_sel_hi:[0,1,0]\n\t" \
  "v_mfma_f32_16x16x32_bf16 v[176:179], v[208:211], v[104:107], v[176:179]\n\t" \
  "ds_read_u16_d16_hi v173, v173 offset:0\n\t" \
  "v_fma_mix_f32 v158, v158, v227, 0 op_sel_hi:[0,1,0]\n\t" \
  "v_mfma_f32_16x16x32_bf16 v[184:187], v[208:211], v[116:119], v[184:187]\n\t" \
  "ds_read_b64_tr_b16 v[222:223], v249 offset:17152\n\t" \
  "v_lshlrev_b32_sdwa v174, 1, v174 dst_sel:DWORD dst_unused:UNUSED_PAD src0_sel:DWORD src1_sel:WORD_1\n\t" \
  "v_fma_mix_f32 v157, v157, v224, 0 op_sel_hi:[0,1,0]\n\t" \
  "v_mfma_f32_16x16x32_bf16 v[192:195], v[208:211], v[128:131], v[192:195]\n\t" \
  "ds_read_u16_d16_hi v174, v174 offset:0\n\t" \
  "v_fma_mix_f32 v159, v159, v226, 0 op_sel_hi:[0,1,0]\n\t" \
  "v_mfma_f32_16x16x32_bf16 v[200:203], v[208:211], v[140:143], v[200:203]\n\t" \
  "s_waitcnt lgkmcnt(9)\n\t" \
  "ds_read_b64_tr_b16 v[208:209], v250 offset:12320\n\t" \
  "v_lshlrev_b32_sdwa v175, 1, v175 dst_sel:DWORD dst_unused:UNUSED_PAD src0_sel:DWORD src1_sel:WORD_1\n\t" \
  "v_mfma_f32_16x16x32_bf16 v[180:183], v[212:215], v[96:99], 0\n\t" \
  "ds_read_u16_d16_hi v175, v175 offset:0\n\t" \
  "v_lshlrev_b32_sdwa v176, 1, v176 dst_sel:DWORD dst_unused:UNUSED_PAD src0_sel:DWORD src1_sel:WORD_1\n\t" \
  "v_mfma_f32_16x16x32_bf16 v[188:191], v[212:215], v[108:111], 0\n\t" \
  "ds_read_b64_tr_b16 v[210:211], v250 offset:13088\n\t" \
  "v_cvt_pk_f16_f32 v224, v184, v185\n\t" \
  "v_cvt_pk_f16_f32 v226, v186, v187\n\t" \
  "v_mfma_f32_16x16x32_bf16 v[196:199], v[212:215], v[120:123], 0\n\t" \
  "ds_read_u16_d16_hi v176, v176 offset:0\n\t" \
  "v_pk_fma_f16 v225, v224, v224, %[h1] neg_lo:[1,0,0] neg_hi:[1,0,0]\n\t" \
  "v_mfma_f32_16x16x32_bf16 v[204:207], v[212:215], v[132:135], 0\n\t" \
  "s_waitcnt lgkmcnt(9)\n\t" \
  "ds_read_b64_tr_b16 v[212:213], v250 offset:12328\n\t" \
  "v_lshlrev_b32_sdwa v177, 1, v177 dst_sel:DWORD dst_unused:UNUSED_PAD src0_sel:DWORD src1_sel:WORD_1\n\t" \
  "v_pk_fma_f16 v227, v226, v226, %[h1] neg_lo:[1,0,0] neg_hi:[1,0,0]\n\t" \
  "v_mfma_f32_16x16x32_bf16 v[180:183], v[216:219], v[100:103], v[180:183]\n\t" \
  "ds_read_u16_d16_hi v177, v177 offset:0\n\t" \
  "v_pk_mul_f16 v225, v224, v225\n\t" \
  "v_mfma_f32_16x16x32_bf16 v[188:191], v[216:219], v[112:115], v[188:191]\n\t" \
  "ds_read_b64_tr_b16 v[214:215], v250 offset:13096\n\t" \
  "v_lshlrev_b32_sdwa v178, 1, v178 dst_sel:DWORD dst_unused:UNUSED_PAD src0_sel:DWORD src1_sel:WORD_1\n\t" \
  "v_pk_mul_f16 v227, v226, v227\n\t" \
  "v_exp_f16 v224, v225\n\t" \
  "v_mfma_f32_16x16x32_bf16 v[196:199], v[216:219], v[124:127], v[196:199]\n\t" \
  "ds_read_u16_d16_hi v178, v178 offset:0\n\t" \
  "v_lshlrev_b32_sdwa v179, 1, v179 dst_sel:DWORD dst_unused:UNUSED_PAD src0_sel:DWORD src1_sel:WORD_1\n\t" \
  "v_mfma_f32_16x16x32_bf16 v[204:207], v[216:219], v[136:139], v[204:207]\n\t" \
  "s_waitcnt lgkmcnt(9)\n\t" \
  "ds_read_b64_tr_b16 v[216:217], v250 offset:12384\n\t" \
  "v_exp_f16 v226, v227\n\t" \
  "v_mfma_f32_16x16x32_bf16 v[180:183], v[220:223], v[104:107], v[180:183]\n\t" \
  "ds_read_u16_d16_hi v179, v179 offset:0\n\t" \
  "v_lshlrev_b32_sdwa v192, 1, v192 dst_sel:DWORD dst_unused:UNUSED_PAD src0_sel:DWORD src1_sel:WORD_1\n\t" \
  "v_mfma_f32_16x16x32_bf16 v[188:191], v[220:223], v[116:119], v[188:191]\n\t" \
  "ds_read_b64_tr_b16 v[218:219], v250 offset:13152\n\t" \
  "v_exp_f16_sdwa v224, v225 dst_sel:WORD_1 dst_unused:UNUSED_PRESERVE src0_sel:WORD_1\n\t" \
  "v_mfma_f32_16x16x32_bf16 v[196:199], v[220:223], v[128:131], v[196:199]\n\t" \
  "ds_read_u16_d16_hi v192, v192 offset:0\n\t" \
  "v_exp_f16_sdwa v226, v227 dst_sel:WORD_1 dst_unused:UNUSED_PRESERVE src0_sel:WORD_1\n\t" \
  "v_mfma_f32_16x16x32_bf16 v[204:207], v[220:223], v[140:143], v[204:207]\n\t" \
  "s_waitcnt lgkmcnt(9)\n\t" \
  "ds_read_b64_tr_b16 v[220:221], v250 offset:12392\n\t" \
  "v_lshlrev_b32_sdwa v193, 1, v193 dst_sel:DWORD dst_unused:UNUSED_PAD src0_sel:DWORD src1_sel:WORD_1\n\t" \
  "v_smfmac_f32_16x16x64_bf16 v[0:3], v[208:211], v[144:151], v248\n\t" \
  "ds_read_u16_d16_hi v193, v193 offset:0\n\t" \
  "v_pk_fma_f16 v224, v224, %[hc], %[hc]\n\t" \
  "v_smfmac_f32_16x16x64_bf16 v[24:27], v[208:211], v[152:159], v248\n\t" \
  "ds_read_b64_tr_b16 v[222:223], v250 offset:13160\n\t" \
  "v_lshlrev_b32_sdwa v194, 1, v194 dst_sel:DWORD dst_unused:UNUSED_PAD src0_sel:DWORD src1_sel:WORD_1\n\t" \
  "v_pk_fma_f16 v226, v226, %[hc], %[hc]\n\t" \
  "v_smfmac_f32_16x16x64_bf16 v[48:51], v[208:211], v[160:167], v248\n\t" \
  "ds_read_u16_d16_hi v194, v194 offset:0\n\t" \
  "v_rcp_f16 v225, v224\n\t" \
  "v_smfmac_f32_16x16x64_bf16 v[72:75], v[208:211], v[168:175], v248\n\t" \
  "s_waitcnt lgkmcnt(9)\n\t" \
  "ds_read_b64_tr_b16 v[208:209], v250 offset:12448\n\t" \
  "v_lshlrev_b32_sdwa v195, 1, v195 dst_sel:DWORD dst_unused:UNUSED_PAD src0_sel:DWORD src1_sel:WORD_1\n\t" \
  "v_rcp_f16 v227, v226\n\t" \
  "v_smfmac_f32_16x16x64_bf16 v[4:7], v[212:215], v[144:151], v248\n\t" \
  "ds_read_u16_d16_hi v195, v195 offset:0\n\t" \
  "v_lshlrev_b32_sdwa v200, 1, v200 dst_sel:DWORD dst_unused:UNUSED_PAD src0_sel:DWORD src1_sel:WORD_1\n\t" \
  "v_smfmac_f32_16x16x64_bf16 v[28:31], v[212:215], v[152:159], v248\n\t" \
  "ds_read_b64_tr_b16 v[210:211], v250 offset:13216\n\t" \
  "v_rcp_f16_sdwa v224, v224 dst_sel:DWORD dst_unused:UNUSED_PAD src0_sel:WORD_1\n\t" \
  "v_smfmac_f32_16x16x64_bf16 v[52:55], v[212:215], v[160:167], v248\n\t" \
  "ds_read_u16_d16_hi v200, v200 offset:0\n\t" \
  "v_lshlrev_b32_sdwa v201, 1, v201 dst_sel:DWORD dst_unused:UNUSED_PAD src0_sel:DWORD src1_sel:WORD_1\n\t" \
  "v_smfmac_f32_16x16x64_bf16 v[76:79], v[212:215], v[168:175], v248\n\t" \
  "s_waitcnt lgkmcnt(9)\n\t" \
  "ds_read_b64_tr_b16 v[212:213], v250 offset:12456\n\t" \
  "v_rcp_f16_sdwa v226, v226 dst_sel:DWORD dst_unused:UNUSED_PAD src0_sel:WORD_1\n\t" \
  "v_smfmac_f32_16x16x64_bf16 v[8:11], v[216:219], v[144:151], v248\n\t" \
  "ds_read_u16_d16_hi v201, v201 offset:0\n\t" \
  "v_fma_mix_f32 v184, v184, v225, 0 op_sel_hi:[0,1,0]\n\t" \
  "v_smfmac_f32_16x16x64_bf16 v[32:35], v[216:219], v[152:159], v248\n\t" \
  "ds_read_b64_tr_b16 v[214:215], v250 offset:13224\n\t" \
  "v_lshlrev_b32_sdwa v202, 1, v202 dst_sel:DWORD dst_unused:UNUSED_PAD src0_sel:DWORD src1_sel:WORD_1\n\t" \
  "v_fma_mix_f32 v186, v186, v227, 0 op_sel_hi:[0,1,0]\n\t" \
  "v_smfmac_f32_16x16x64_bf16 v[56:59], v[216:219], v[160:167], v248\n\t" \
  "ds_read_u16_d16_hi v202, v202 offset:0\n\t" \
  "v_fma_mix_f32 v185, v185, v224, 0 op_sel_hi:[0,1,0]\n\t" \
  "v_smfmac_f32_16x16x64_bf16 v[80:83], v[216:219], v[168:175], v248\n\t" \
  "s_waitcnt lgkmcnt(9)\n\t" \
  "s_barrier\n\t" \
  "ds_read_b64_tr_b16 v[216:217], v249 offset:0\n\t" \
  "v_lshlrev_b32_sdwa v203, 1, v203 dst_sel:DWORD dst_unused:UNUSED_PAD src0_sel:DWORD src1_sel:WORD_1\n\t" \
  "v_fma_mix_f32 v187, v187, v226, 0 op_sel_hi:[0,1,0]\n\t" \
  "v_smfmac_f32_16x16x64_bf16 v[12:15], v[220:223], v[144:151], v248\n\t" \
  "ds_read_b128 v[96:99], v251 offset:0\n\t" \
  "v_cvt_pk_f16_f32 v224, v188, v189\n\t" \
  "v_cvt_pk_f16_f32 v226, v190, v191\n\t" \
  "v_smfmac_f32_16x16x64_bf16 v[36:39], v[220:223], v[152:159], v248\n\t" \
  "ds_read_b64_tr_b16 v[218:219], v249 offset:256\n\t" \
  "v_lshlrev_b32_sdwa v180, 1, v180 dst_sel:DWORD dst_unused:UNUSED_PAD src0_sel:DWORD src1_sel:WORD_1\n\t" \
  "v_pk_fma_f16 v225, v224, v224, %[h1] neg_lo:[1,0,0] neg_hi:[1,0,0]\n\t" \
  "v_smfmac_f32_16x16x64_bf16 v[60:63], v[220:223], v[160:167], v248\n\t" \
  "ds_read_b128 v[100:103], v251 offset:4096\n\t" \
  "v_pk_fma_f16 v227, v226, v226, %[h1] neg_lo:[1,0,0] neg_hi:[1,0,0]\n\t" \
  "v_smfmac_f32_16x16x64_bf16 v[84:87], v[220:223], v[168:175], v248\n\t" \
  "s_waitcnt lgkmcnt(9)\n\t" \
  "ds_read_b64_tr_b16 v[220:221], v249 offset:2048\n\t" \
  "v_lshlrev_b32_sdwa v181, 1, v181 dst_sel:DWORD dst_unused:UNUSED_PAD src0_sel:DWORD src1_sel:WORD_1\n\t" \
  "v_pk_mul_f16 v225, v224, v225\n\t" \
  "v_smfmac_f32_16x16x64_bf16 v[16:19], v[208:211], v[144:151], v248\n\t" \
  "ds_read_b128 v[104:107], v251 offset:8192\n\t" \
  "v_pk_mul_f16 v227, v226, v227\n\t" \
  "v_exp_f16 v224, v225\n\t" \
  "v_smfmac_f32_16x16x64_bf16 v[40:43], v[208:211], v[152:159], v248\n\t" \
  "ds_read_b64_tr_b16 v[222:223], v249 offset:2304\n\t" \
  "v_lshlrev_b32_sdwa v182, 1, v182 dst_sel:DWORD dst_unused:UNUSED_PAD src0_sel:DWORD src1_sel:WORD_1\n\t" \
  "v_smfmac_f32_16x16x64_bf16 v[64:67], v[208:211], v[160:167], v248\n\t" \
  "ds_read_u16_d16_hi v203, v203 offset:0\n\t" \
  "v_exp_f16 v226, v227\n\t" \
  "v_smfmac_f32_16x16x64_bf16 v[88:91], v[208:211], v[168:175], v248\n\t" \
  "s_waitcnt lgkmcnt(9)\n\t" \
  "ds_read_b64_tr_b16 v[208:209], v249 offset:4096\n\t" \
  "v_lshlrev_b32_sdwa v183, 1, v183 dst_sel:DWORD dst_unused:UNUSED_PAD src0_sel:DWORD src1_sel:WORD_1\n\t" \
  "v_smfmac_f32_16x16x64_bf16 v[20:23], v[212:215], v[144:151], v248\n\t" \
  "ds_read_u16_d16_hi v180, v180 offset:0\n\t" \
  "v_exp_f16_sdwa v224, v225 dst_sel:WORD_1 dst_unused:UNUSED_PRESERVE src0_sel:WORD_1\n\t" \
  "v_smfmac_f32_16x16x64_bf16 v[44:47], v[212:215], v[152:159], v248\n\t" \
  "ds_read_b64_tr_b16 v[210:211], v249 offset:4352\n\t" \
  "v_lshlrev_b32_sdwa v196, 1, v196 dst_sel:DWORD dst_unused:UNUSED_PAD src0_sel:DWORD src1_sel:WORD_1\n\t" \
  "v_exp_f16_sdwa v226, v227 dst_sel:WORD_1 dst_unused:UNUSED_PRESERVE src0_sel:WORD_1\n\t" \
  "v_smfmac_f32_16x16x64_bf16 v[68:71], v[212:215], v[160:167], v248\n\t" \
  "ds_read_u16_d16_hi v181, v181 offset:0\n\t" \
  "v_lshlrev_b32_sdwa v197, 1, v197 dst_sel:DWORD dst_unused:UNUSED_PAD src0_sel:DWORD src1_sel:WORD_1\n\t" \
  "v_smfmac_f32_16x16x64_bf16 v[92:95], v[212:215], v[168:175], v248\n\t" \
  "s_waitcnt lgkmcnt(9)\n\t" \
  "ds_read_b64_tr_b16 v[212:213], v249 offset:512\n\t" \
  "v_pk_fma_f16 v224, v224, %[hc], %[hc]\n\t" \
  "v_mfma_f32_16x16x32_bf16 v[144:147], v[216:219], v[96:99], 0\n\t" \
  "ds_read_u16_d16_hi v182, v182 offset:0\n\t" \
  "v_pk_fma_f16 v226, v226, %[hc], %[hc]\n\t" \
  "v_mfma_f32_16x16x32_bf16 v[152:155], v[216:219], v[108:111], 0\n\t" \
  "ds_read_b64_tr_b16 v[214:215], v249 offset:768\n\t" \
  "v_lshlrev_b32_sdwa v198, 1, v198 dst_sel:DWORD dst_unused:UNUSED_PAD src0_sel:DWORD src1_sel:WORD_1\n\t" \
  "v_rcp_f16 v225, v224\n\t" \
  "v_mfma_f32_16x16x32_bf16 v[160:163], v[216:219], v[120:123], 0\n\t" \
  "ds_read_u16_d16_hi v183, v183 offset:0\n\t" \
  "v_rcp_f16 v227, v226\n\t" \
  "v_mfma_f32_16x16x32_bf16 v[168:171], v[216:219], v[132:135], 0\n\t" \
  "s_waitcnt lgkmcnt(9)\n\t" \
  "ds_read_b64_tr_b16 v[216:217], v249 offset:2560\n\t" \
  "v_lshlrev_b32_sdwa v199, 1, v199 dst_sel:DWORD dst_unused:UNUSED_PAD src0_sel:DWORD src1_sel:WORD_1\n\t" \
  "v_mfma_f32_16x16x32_bf16 v[144:147], v[220:223], v[100:103], v[144:147]\n\t" \
  "ds_read_u16_d16_hi v196, v196 offset:0\n\t" \
  "v_rcp_f16_sdwa v224, v224 dst_sel:DWORD dst_unused:UNUSED_PAD src0_sel:WORD_1\n\t" \
  "v_mfma_f32_16x16x32_bf16 v[152:155], v[220:223], v[112:115], v[152:155]\n\t" \
  "ds_read_b64_tr_b16 v[218:219], v249 offset:2816\n\t" \
  "v_lshlrev_b32_sdwa v204, 1, v204 dst_sel:DWORD dst_unused:UNUSED_PAD src0_sel:DWORD src1_sel:WORD_1\n\t" \
  "v_mfma_f32_16x16x32_bf16 v[160:163], v[220:223], v[124:127], v[160:163]\n\t" \
  "ds_read_u16_d16_hi v197, v197 offset:0\n\t" \
  "v_rcp_f16_sdwa v226, v226 dst_sel:DWORD dst_unused:UNUSED_PAD src0_sel:WORD_1\n\t" \
  "v_mfma_f32_16x16x32_bf16 v[168:171], v[220:223], v[136:139], v[168:171]\n\t" \
  "s_waitcnt lgkmcnt(9)\n\t" \
  "ds_read_b64_tr_b16 v[220:221], v249 offset:4608\n\t" \
  "v_lshlrev_b32_sdwa v205, 1, v205 dst_sel:DWORD dst_unused:UNUSED_PAD src0_sel:DWORD src1_sel:WORD_1\n\t" \
  "v_fma_mix_f32 v188, v188, v225, 0 op_sel_hi:[0,1,0]\n\t" \
  "v_mfma_f32_16x16x32_bf16 v[144:147], v[208:211], v[104:107], v[144:147]\n\t" \
  "ds_read_u16_d16_hi v198, v198 offset:0\n\t" \
  "v_fma_mix_f32 v190, v190, v227, 0 op_sel_hi:[0,1,0]\n\t" \
  "v_mfma_f32_16x16x32_bf16 v[152:155], v[208:211], v[116:119], v[152:155]\n\t" \
  "ds_read_b64_tr_b16 v[222:223], v249 offset:4864\n\t" \
  "v_lshlrev_b32_sdwa v206, 1, v206 dst_sel:DWORD dst_unused:UNUSED_PAD src0_sel:DWORD src1_sel:WORD_1\n\t" \
  "v_fma_mix_f32 v189, v189, v224, 0 op_sel_hi:[0,1,0]\n\t" \
  "v_mfma_f32_16x16x32_bf16 v[160:163], v[208:211], v[128:131], v[160:163]\n\t" \
  "ds_read_u16_d16_hi v199, v199 offset:0\n\t" \
  "v_fma_mix_f32 v191, v191, v226, 0 op_sel_hi:[0,1,0]\n\t" \
  "v_mfma_f32_16x16x32_bf16 v[168:171], v[208:211], v[140:143], v[168:171]\n\t" \
  "s_waitcnt lgkmcnt(9)\n\t" \
  "ds_read_b64_tr_b16 v[208:209], v250 offset:18480\n\t" \
  "v_lshlrev_b32_sdwa v207, 1, v207 dst_sel:DWORD dst_unused:UNUSED_PAD src0_sel:DWORD src1_sel:WORD_1\n\t" \
  "v_mfma_f32_16x16x32_bf16 v[148:151], v[212:215], v[96:99], 0\n\t" \
  "ds_read_u16_d16_hi v204, v204 offset:0\n\t" \
  "v_lshlrev_b32_sdwa v144, 1, v144 dst_sel:DWORD dst_unused:UNUSED_PAD src0_sel:DWORD src1_sel:WORD_1\n\t" \
  "v_mfma_f32_16x16x32_bf16 v[156:159], v[212:215], v[108:111], 0\n\t" \
  "ds_read_b64_tr_b16 v[210:211], v250 offset:19248\n\t" \
  "ds_read_u16_d16_hi v205, v205 offset:0\n\t" \
  "v_cvt_pk_f16_f32 v224, v152, v153\n\t" \
  "v_cvt_pk_f16_f32 v226, v154, v155\n\t" \
  "v_mfma_f32_16x16x32_bf16 v[164:167], v[212:215], v[120:123], 0\n\t" \
  "ds_read_u16_d16_hi v206, v206 offset:0\n\t" \
  "v_pk_fma_f16 v225, v224, v224, %[h1] neg_lo:[1,0,0] neg_hi:[1,0,0]\n\t" \
  "v_mfma_f32_16x16x32_bf16 v[172:175], v[212:215], v[132:135], 0\n\t" \
  "s_waitcnt lgkmcnt(10)\n\t" \
  "ds_read_b64_tr_b16 v[212:213], v250 offset:18488\n\t" \
  "v_lshlrev_b32_sdwa v145, 1, v145 dst_sel:DWORD dst_unused:UNUSED_PAD src0_sel:DWORD src1_sel:WORD_1\n\t" \
  "v_pk_fma_f16 v227, v226, v226, %[h1] neg_lo:[1,0,0] neg_hi:[1,0,0]\n\t" \
  "v_mfma_f32_16x16x32_bf16 v[148:151], v[216:219], v[100:103], v[148:151]\n\t" \
  "ds_read_u16_d16_hi v207, v207 offset:0\n\t" \
  "v_pk_mul_f16 v225, v224, v225\n\t" \
  "v_mfma_f32_16x16x32_bf16 v[156:159], v[216:219], v[112:115], v[156:159]\n\t" \
  "ds_read_b64_tr_b16 v[214:215], v250 offset:19256\n\t" \
  "v_lshlrev_b32_sdwa v146, 1, v146 dst_sel:DWORD dst_unused:UNUSED_PAD src0_sel:DWORD src1_sel:WORD_1\n\t" \
  "v_pk_mul_f16 v227, v226, v227\n\t" \
  "v_exp_f16 v224, v225\n\t" \
  "v_mfma_f32_16x16x32_bf16 v[164:167], v[216:219], v[124:127], v[164:167]\n\t" \
  "ds_read_u16_d16_hi v144, v144 offset:0\n\t" \
  "v_lshlrev_b32_sdwa v147, 1, v147 dst_sel:DWORD dst_unused:UNUSED_PAD src0_sel:DWORD src1_sel:WORD_1\n\t" \
  "v_mfma_f32_16x16x32_bf16 v[172:175], v[216:219], v[136:139], v[172:175]\n\t" \
  "s_waitcnt lgkmcnt(10)\n\t" \
  "ds_read_b64_tr_b16 v[216:217], v250 offset:18544\n\t" \
  "v_exp_f16 v226, v227\n\t" \
  "v_mfma_f32_16x16x32_bf16 v[148:151], v[220:223], v[104:107], v[148:151]\n\t" \
  "ds_read_u16_d16_hi v145, v145 offset:0\n\t" \
  "v_lshlrev_b32_sdwa v160, 1, v160 dst_sel:DWORD dst_unused:UNUSED_PAD src0_sel:DWORD src1_sel:WORD_1\n\t" \
  "v_mfma_f32_16x16x32_bf16 v[156:159], v[220:223], v[116:119], v[156:159]\n\t" \
  "ds_read_b64_tr_b16 v[218:219], v250 offset:19312\n\t" \
  "v_exp_f16_sdwa v224, v225 dst_sel:WORD_1 dst_unused:UNUSED_PRESERVE src0_sel:WORD_1\n\t" \
  "v_mfma_f32_16x16x32_bf16 v[164:167], v[220:223], v[128:131], v[164:167]\n\t" \
  "ds_read_u16_d16_hi v146, v146 offset:0\n\t" \
  "v_exp_f16_sdwa v226, v227 dst_sel:WORD_1 dst_unused:UNUSED_PRESERVE src0_sel:WORD_1\n\t" \
  "v_mfma_f32_16x16x32_bf16 v[172:175], v[220:223], v[140:143], v[172:175]\n\t" \
  "s_waitcnt lgkmcnt(10)\n\t" \
  "ds_read_b64_tr_b16 v[220:221], v250 offset:18552\n\t" \
  "v_lshlrev_b32_sdwa v161, 1, v161 dst_sel:DWORD dst_unused:UNUSED_PAD src0_sel:DWORD src1_sel:WORD_1\n\t" \
  "v_smfmac_f32_16x16x64_bf16 v[0:3], v[208:211], v[176:183], v248\n\t" \
  "ds_read_u16_d16_hi v147, v147 offset:0\n\t" \
  "v_pk_fma_f16 v224, v224, %[hc], %[hc]\n\t" \
  "v_smfmac_f32_16x16x64_bf16 v[24:27], v[208:211], v[184:191], v248\n\t" \
  "ds_read_b64_tr_b16 v[222:223], v250 offset:19320\n\t" \
  "v_lshlrev_b32_sdwa v162, 1, v162 dst_sel:DWORD dst_unused:UNUSED_PAD src0_sel:DWORD src1_sel:WORD_1\n\t" \
  "v_pk_fma_f16 v226, v226, %[hc], %[hc]\n\t" \
  "v_smfmac_f32_16x16x64_bf16 v[48:51], v[208:211], v[192:199], v248\n\t" \
  "s_waitcnt lgkmcnt(9)\n\t" \
  "ds_read_u16_d16_hi v160, v160 offset:0\n\t" \
  "v_rcp_f16 v225, v224\n\t" \
  "v_smfmac_f32_16x16x64_bf16 v[72:75], v[208:211], v[200:207], v248\n\t" \
  "s_waitcnt lgkmcnt(9)\n\t" \
  "ds_read_b64_tr_b16 v[208:209], v250 offset:18608\n\t" \
  "v_lshlrev_b32_sdwa v163, 1, v163 dst_sel:DWORD dst_unused:UNUSED_PAD src0_sel:DWORD src1_sel:WORD_1\n\t" \
  "v_rcp_f16 v227, v226\n\t" \
  "v_smfmac_f32_16x16x64_bf16 v[4:7], v[212:215], v[176:183], v248\n\t" \
  "ds_read_b64 v[242:243], v252 offset:128\n\t" \
  "v_lshlrev_b32_sdwa v168, 1, v168 dst_sel:DWORD dst_unused:UNUSED_PAD src0_sel:DWORD src1_sel:WORD_1\n\t" \
  "v_smfmac_f32_16x16x64_bf16 v[28:31], v[212:215], v[184:191], v248\n\t" \
  "ds_read_b64_tr_b16 v[210:211], v250 offset:19376\n\t" \
  "v_rcp_f16_sdwa v224, v224 dst_sel:DWORD dst_unused:UNUSED_PAD src0_sel:WORD_1\n\t" \
  "v_smfmac_f32_16x16x64_bf16 v[52:55], v[212:215], v[192:199], v248\n\t" \
  "ds_read_b64 v[244:245], v252 offset:136\n\t" \
  "v_cvt_pk_bf16_f32 v230, v0, v1\n\t" \
  "v_smfmac_f32_16x16x64_bf16 v[76:79], v[212:215], v[200:207], v248\n\t" \
  "s_waitcnt lgkmcnt(9)\n\t" \
  "ds_read_b64_tr_b16 v[212:213], v250 offset:18616\n\t" \
  "v_lshlrev_b32_sdwa v169, 1, v169 dst_sel:DWORD dst_unused:UNUSED_PAD src0_sel:DWORD src1_sel:WORD_1\n\t" \
  "v_rcp_f16_sdwa v226, v226 dst_sel:DWORD dst_unused:UNUSED_PAD src0_sel:WORD_1\n\t" \
  "v_cvt_pk_bf16_f32 v231, v2, v3\n\t" \
  "v_smfmac_f32_16x16x64_bf16 v[8:11], v[216:219], v[176:183], v248\n\t" \
  "ds_read_u16_d16_hi v161, v161 offset:0\n\t" \
  "v_fma_mix_f32 v152, v152, v225, 0 op_sel_hi:[0,1,0]\n\t" \
  "v_cvt_pk_bf16_f32 v232, v4, v5\n\t" \
  "v_smfmac_f32_16x16x64_bf16 v[32:35], v[216:219], v[184:191], v248\n\t" \
  "ds_read_b64_tr_b16 v[214:215], v250 offset:19384\n\t" \
  "v_lshlrev_b32_sdwa v170, 1, v170 dst_sel:DWORD dst_unused:UNUSED_PAD src0_sel:DWORD src1_sel:WORD_1\n\t" \
  "v_fma_mix_f32 v154, v154, v227, 0 op_sel_hi:[0,1,0]\n\t" \
  "v_cvt_pk_bf16_f32 v233, v6, v7\n\t" \
  "v_smfmac_f32_16x16x64_bf16 v[56:59], v[216:219], v[192:199], v248\n\t" \
  "buffer_store_dwordx4 v[230:233], v253, %[osrd], %[ob] offen offset:0 sc1\n\t" \
  "ds_read_u16_d16_hi v162, v162 offset:0\n\t" \
  "v_fma_mix_f32 v153, v153, v224, 0 op_sel_hi:[0,1,0]\n\t" \
  "v_smfmac_f32_16x16x64_bf16 v[80:83], v[216:219], v[200:207], v248\n\t" \
  "s_waitcnt lgkmcnt(9)\n\t" \
  "s_barrier\n\t" \
  "ds_read_b64_tr_b16 v[216:217], v249 offset:6144\n\t" \
  "ds_read_b64 v[230:231], v252 offset:0\n\t" \
  "v_lshlrev_b32_sdwa v171, 1, v171 dst_sel:DWORD dst_unused:UNUSED_PAD src0_sel:DWORD src1_sel:WORD_1\n\t" \
  "v_fma_mix_f32 v155, v155, v226, 0 op_sel_hi:[0,1,0]\n\t" \
  "v_smfmac_f32_16x16x64_bf16 v[12:15], v[220:223], v[176:183], v248\n\t" \
  "ds_read_b64_tr_b16 v[218:219], v249 offset:6400\n\t" \
  "ds_read_b64 v[232:233], v252 offset:8\n\t" \
  "v_cvt_pk_f16_f32 v224, v156, v157\n\t" \
  "v_cvt_pk_f16_f32 v226, v158, v159\n\t" \
  "v_smfmac_f32_16x16x64_bf16 v[36:39], v[220:223], v[184:191], v248\n\t" \
  "ds_read_u16_d16_hi v163, v163 offset:0\n\t" \
  "v_lshlrev_b32_sdwa v148, 1, v148 dst_sel:DWORD dst_unused:UNUSED_PAD src0_sel:DWORD src1_sel:WORD_1\n\t" \
  "v_pk_fma_f16 v225, v224, v224, %[h1] neg_lo:[1,0,0] neg_hi:[1,0,0]\n\t" \
  "v_smfmac_f32_16x16x64_bf16 v[60:63], v[220:223], v[192:199], v248\n\t" \
  "ds_read_u16_d16_hi v168, v168 offset:0\n\t" \
  "v_pk_fma_f16 v227, v226, v226, %[h1] neg_lo:[1,0,0] neg_hi:[1,0,0]\n\t" \
  "v_cvt_pk_bf16_f32 v234, v8, v9\n\t" \
  "v_smfmac_f32_16x16x64_bf16 v[84:87], v[220:223], v[200:207], v248\n\t" \
  "s_waitcnt lgkmcnt(11)\n\t" \
  "ds_read_b64_tr_b16 v[220:221], v249 offset:8192\n\t" \
  "v_lshlrev_b32_sdwa v149, 1, v149 dst_sel:DWORD dst_unused:UNUSED_PAD src0_sel:DWORD src1_sel:WORD_1\n\t" \
  "v_pk_mul_f16 v225, v224, v225\n\t" \
  "v_cvt_pk_bf16_f32 v235, v10, v11\n\t" \
  "v_smfmac_f32_16x16x64_bf16 v[16:19], v[208:211], v[176:183], v248\n\t" \
  "ds_read_u16_d16_hi v169, v169 offset:0\n\t" \
  "v_pk_mul_f16 v227, v226, v227\n\t" \
  "v_exp_f16 v224, v225\n\t" \
  "v_cvt_pk_bf16_f32 v236, v12, v13\n\t" \
  "v_smfmac_f32_16x16x64_bf16 v[40:43], v[208:211], v[184:191], v248\n\t" \
  "ds_read_b64_tr_b16 v[222:223], v249 offset:8448\n\t" \
  "v_lshlrev_b32_sdwa v150, 1, v150 dst_sel:DWORD dst_unused:UNUSED_PAD src0_sel:DWORD src1_sel:WORD_1\n\t" \
  "v_cvt_pk_bf16_f32 v237, v14, v15\n\t" \
  "v_smfmac_f32_16x16x64_bf16 v[64:67], v[208:211], v[192:199], v248\n\t" \
  "buffer_store_dwordx4 v[234:237], v253, %[osrd], %[ob] offen offset:64 sc1\n\t" \
  "ds_read_u16_d16_hi v170, v170 offset:0\n\t" \
  "v_exp_f16 v226, v227\n\t" \
  "v_smfmac_f32_16x16x64_bf16 v[88:91], v[208:211], v[200:207], v248\n\t" \
  "s_waitcnt lgkmcnt(11)\n\t" \
  "ds_read_b64_tr_b16 v[208:209], v249 offset:10240\n\t" \
  "ds_read_b64 v[234:235], v252 offset:64\n\t" \
  "v_lshlrev_b32_sdwa v151, 1, v151 dst_sel:DWORD dst_unused:UNUSED_PAD src0_sel:DWORD src1_sel:WORD_1\n\t" \
  "v_smfmac_f32_16x16x64_bf16 v[20:23], v[212:215], v[176:183], v248\n\t" \
  "s_waitcnt lgkmcnt(10)\n\t" \
  "ds_read_b64_tr_b16 v[210:211], v249 offset:10496\n\t" \
  "ds_read_b64 v[236:237], v252 offset:72\n\t" \
  "v_mfma_f32_16x16x16_bf16 v[0:3], v[246:247], v[230:231], 0\n\t" \
  "ds_read_u16_d16_hi v171, v171 offset:0\n\t" \
  "v_exp_f16_sdwa v224, v225 dst_sel:WORD_1 dst_unused:UNUSED_PRESERVE src0_sel:WORD_1\n\t" \
  "v_smfmac_f32_16x16x64_bf16 v[44:47], v[212:215], v[184:191], v248\n\t" \
  "ds_read_u16_d16_hi v148, v148 offset:0\n\t" \
  "v_lshlrev_b32_sdwa v164, 1, v164 dst_sel:DWORD dst_unused:UNUSED_PAD src0_sel:DWORD src1_sel:WORD_1\n\t" \
  "v_exp_f16_sdwa v226, v227 dst_sel:WORD_1 dst_unused:UNUSED_PRESERVE src0_sel:WORD_1\n\t" \
  "v_smfmac_f32_16x16x64_bf16 v[68:71], v[212:215], v[192:199], v248\n\t" \
  "ds_read_u16_d16_hi v149, v149 offset:0\n\t" \
  "v_cvt_pk_bf16_f32 v238, v16, v17\n\t" \
  "v_smfmac_f32_16x16x64_bf16 v[92:95], v[212:215], v[200:207], v248\n\t" \
  "s_waitcnt lgkmcnt(13)\n\t" \
  "ds_read_b64_tr_b16 v[212:213], v249 offset:6656\n\t" \
  "v_lshlrev_b32_sdwa v165, 1, v165 dst_sel:DWORD dst_unused:UNUSED_PAD src0_sel:DWORD src1_sel:WORD_1\n\t" \
  "v_mfma_f32_16x16x16_bf16 v[4:7], v[246:247], v[232:233], 0\n\t" \
  "ds_read_u16_d16_hi v150, v150 offset:0\n\t" \
  "v_pk_fma_f16 v224, v224, %[hc], %[hc]\n\t" \
  "v_cvt_pk_bf16_f32 v239, v18, v19\n\t" \
  "v_mfma_f32_16x16x32_bf16 v[176:179], v[216:219], v[96:99], 0\n\t" \
  "ds_read_b64_tr_b16 v[214:215], v249 offset:6912\n\t" \
  "v_pk_fma_f16 v226, v226, %[hc], %[hc]\n\t" \
  "v_cvt_pk_bf16_f32 v240, v20, v21\n\t" \
  "v_mfma_f32_16x16x32_bf16 v[184:187], v[216:219], v[108:111], 0\n\t" \
  "ds_read_u16_d16_hi v151, v151 offset:0\n\t" \
  "v_lshlrev_b32_sdwa v166, 1, v166 dst_sel:DWORD dst_unused:UNUSED_PAD src0_sel:DWORD src1_sel:WORD_1\n\t" \
  "v_rcp_f16 v225, v224\n\t" \
  "v_cvt_pk_bf16_f32 v241, v22, v23\n\t" \
  "v_mfma_f32_16x16x32_bf16 v[192:195], v[216:219], v[120:123], 0\n\t" \
  "buffer_store_dwordx4 v[238:241], v253, %[osrd], %[ob] offen offset:128 sc1\n\t" \
  "v_rcp_f16 v227, v226\n\t" \
  "v_mfma_f32_16x16x32_bf16 v[200:203], v[216:219], v[132:135], 0\n\t" \
  "s_waitcnt lgkmcnt(12)\n\t" \
  "ds_read_b64_tr_b16 v[216:217], v249 offset:8704\n\t" \
  "v_lshlrev_b32_sdwa v167, 1, v167 dst_sel:DWORD dst_unused:UNUSED_PAD src0_sel:DWORD src1_sel:WORD_1\n\t" \
  "v_mfma_f32_16x16x32_bf16 v[176:179], v[220:223], v[100:103], v[176:179]\n\t" \
  "s_waitcnt lgkmcnt(10)\n\t" \
  "ds_read_u16_d16_hi v164, v164 offset:0\n\t" \
  "v_rcp_f16_sdwa v224, v224 dst_sel:DWORD dst_unused:UNUSED_PAD src0_sel:WORD_1\n\t" \
  "v_mfma_f32_16x16x16_bf16 v[8:11], v[246:247], v[234:235], 0\n\t" \
  "ds_read_b64_tr_b16 v[218:219], v249 offset:8960\n\t" \
  "v_lshlrev_b32_sdwa v172, 1, v172 dst_sel:DWORD dst_unused:UNUSED_PAD src0_sel:DWORD src1_sel:WORD_1\n\t" \
  "v_mfma_f32_16x16x32_bf16 v[184:187], v[220:223], v[112:115], v[184:187]\n\t" \
  "ds_read_u16_d16_hi v165, v165 offset:0\n\t" \
  "v_rcp_f16_sdwa v226, v226 dst_sel:DWORD dst_unused:UNUSED_PAD src0_sel:WORD_1\n\t" \
  "v_mfma_f32_16x16x32_bf16 v[192:195], v[220:223], v[124:127], v[192:195]\n\t" \
  "ds_read_u16_d16_hi v166, v166 offset:0\n\t" \
  "v_lshlrev_b32_sdwa v173, 1, v173 dst_sel:DWORD dst_unused:UNUSED_PAD src0_sel:DWORD src1_sel:WORD_1\n\t" \
  "v_mfma_f32_16x16x32_bf16 v[200:203], v[220:223], v[136:139], v[200:203]\n\t" \
  "s_waitcnt lgkmcnt(12)\n\t" \
  "ds_read_b64_tr_b16 v[220:221], v249 offset:10752\n\t" \
  "v_fma_mix_f32 v156, v156, v225, 0 op_sel_hi:[0,1,0]\n\t" \
  "v_mfma_f32_16x16x16_bf16 v[12:15], v[246:247], v[236:237], 0\n\t" \
  "ds_read_u16_d16_hi v167, v167 offset:0\n\t" \
  "v_fma_mix_f32 v158, v158, v227, 0 op_sel_hi:[0,1,0]\n\t" \
  "v_mfma_f32_16x16x32_bf16 v[176:179], v[208:211], v[104:107], v[176:179]\n\t" \
  "ds_read_b64_tr_b16 v[222:223], v249 offset:11008\n\t" \
  "v_lshlrev_b32_sdwa v174, 1, v174 dst_sel:DWORD dst_unused:UNUSED_PAD src0_sel:DWORD src1_sel:WORD_1\n\t" \
  "v_mfma_f32_16x16x32_bf16 v[184:187], v[208:211], v[116:119], v[184:187]\n\t" \
  "ds_read_u16_d16_hi v172, v172 offset:0\n\t" \
  "v_fma_mix_f32 v157, v157, v224, 0 op_sel_hi:[0,1,0]\n\t" \
  "v_mfma_f32_16x16x32_bf16 v[192:195], v[208:211], v[128:131], v[192:195]\n\t" \
  "ds_read_u16_d16_hi v173, v173 offset:0\n\t" \
  "v_fma_mix_f32 v159, v159, v226, 0 op_sel_hi:[0,1,0]\n\t" \
  "v_mfma_f32_16x16x32_bf16 v[200:203], v[208:211], v[140:143], v[200:203]\n\t" \
  "s_waitcnt lgkmcnt(11)\n\t" \
  "ds_read_b64_tr_b16 v[208:209], v250 offset:0\n\t" \
  "v_lshlrev_b32_sdwa v175, 1, v175 dst_sel:DWORD dst_unused:UNUSED_PAD src0_sel:DWORD src1_sel:WORD_1\n\t" \
  "v_mfma_f32_16x16x32_bf16 v[180:183], v[212:215], v[96:99], 0\n\t" \
  "ds_read_u16_d16_hi v174, v174 offset:0\n\t" \
  "v_lshlrev_b32_sdwa v176, 1, v176 dst_sel:DWORD dst_unused:UNUSED_PAD src0_sel:DWORD src1_sel:WORD_1\n\t" \
  "v_mfma_f32_16x16x16_bf16 v[16:19], v[246:247], v[242:243], 0\n\t" \
  "ds_read_b64_tr_b16 v[210:211], v250 offset:768\n\t" \
  "v_cvt_pk_f16_f32 v224, v184, v185\n\t" \
  "v_mfma_f32_16x16x32_bf16 v[188:191], v[212:215], v[108:111], 0\n\t" \
  "ds_read_u16_d16_hi v175, v175 offset:0\n\t" \
  "v_cvt_pk_f16_f32 v226, v186, v187\n\t" \
  "v_mfma_f32_16x16x32_bf16 v[196:199], v[212:215], v[120:123], 0\n\t" \
  "ds_read_u16_d16_hi v176, v176 offset:0\n\t" \
  "v_pk_fma_f16 v225, v224, v224, %[h1] neg_lo:[1,0,0] neg_hi:[1,0,0]\n\t" \
  "v_mfma_f32_16x16x32_bf16 v[204:207], v[212:215], v[132:135], 0\n\t" \
  "ds_read_b64_tr_b16 v[212:213], v250 offset:8\n\t" \
  "v_lshlrev_b32_sdwa v177, 1, v177 dst_sel:DWORD dst_unused:UNUSED_PAD src0_sel:DWORD src1_sel:WORD_1\n\t" \
  "v_mfma_f32_16x16x16_bf16 v[20:23], v[246:247], v[244:245], 0\n\t" \
  "s_waitcnt lgkmcnt(13)\n\t" \
  "ds_read_u16_d16_hi v177, v177 offset:0\n\t" \
  "v_pk_fma_f16 v227, v226, v226, %[h1] neg_lo:[1,0,0] neg_hi:[1,0,0]\n\t" \
  "v_mfma_f32_16x16x32_bf16 v[180:183], v[216:219], v[100:103], v[180:183]\n\t" \
  "ds_read_b64_tr_b16 v[214:215], v250 offset:776\n\t" \
  "v_pk_mul_f16 v225, v224, v225\n\t" \
  "v_mfma_f32_16x16x32_bf16 v[188:191], v[216:219], v[112:115], v[188:191]\n\t" \
  "v_lshlrev_b32_sdwa v178, 1, v178 dst_sel:DWORD dst_unused:UNUSED_PAD src0_sel:DWORD src1_sel:WORD_1\n\t" \
  "v_pk_mul_f16 v227, v226, v227\n\t" \
  "v_exp_f16 v224, v225\n\t" \
  "v_mfma_f32_16x16x32_bf16 v[196:199], v[216:219], v[124:127], v[196:199]\n\t" \
  "ds_read_u16_d16_hi v178, v178 offset:0\n\t" \
  "v_lshlrev_b32_sdwa v179, 1, v179 dst_sel:DWORD dst_unused:UNUSED_PAD src0_sel:DWORD src1_sel:WORD_1\n\t" \
  "v_mfma_f32_16x16x32_bf16 v[204:207], v[216:219], v[136:139], v[204:207]\n\t" \
  "s_waitcnt lgkmcnt(11)\n\t" \
  "ds_read_b64_tr_b16 v[216:217], v250 offset:64\n\t" \
  "v_exp_f16 v226, v227\n\t" \
  "v_mfma_f32_16x16x32_bf16 v[180:183], v[220:223], v[104:107], v[180:183]\n\t" \
  "ds_read_u16_d16_hi v179, v179 offset:0\n\t" \
  "v_lshlrev_b32_sdwa v192, 1, v192 dst_sel:DWORD dst_unused:UNUSED_PAD src0_sel:DWORD src1_sel:WORD_1\n\t" \
  "v_mfma_f32_16x16x32_bf16 v[188:191], v[220:223], v[116:119], v[188:191]\n\t" \
  "ds_read_b64_tr_b16 v[218:219], v250 offset:832\n\t" \
  "v_exp_f16_sdwa v224, v225 dst_sel:WORD_1 dst_unused:UNUSED_PRESERVE src0_sel:WORD_1\n\t" \
  "v_mfma_f32_16x16x32_bf16 v[196:199], v[220:223], v[128:131], v[196:199]\n\t" \
  "ds_read_u16_d16_hi v192, v192 offset:0\n\t" \
  "v_exp_f16_sdwa v226, v227 dst_sel:WORD_1 dst_unused:UNUSED_PRESERVE src0_sel:WORD_1\n\t" \
  "v_mfma_f32_16x16x32_bf16 v[204:207], v[220:223], v[140:143], v[204:207]\n\t" \
  "s_waitcnt lgkmcnt(10)\n\t" \
  "ds_read_b64_tr_b16 v[220:221], v250 offset:72\n\t" \
  "v_lshlrev_b32_sdwa v193, 1, v193 dst_sel:DWORD dst_unused:UNUSED_PAD src0_sel:DWORD src1_sel:WORD_1\n\t" \
  "v_smfmac_f32_16x16x64_bf16 v[0:3], v[208:211], v[144:151], v248\n\t" \
  "ds_read_u16_d16_hi v193, v193 offset:0\n\t" \
  "v_pk_fma_f16 v224, v224, %[hc], %[hc]\n\t" \
  "v_smfmac_f32_16x16x64_bf16 v[24:27], v[208:211], v[152:159], v248\n\t" \
  "ds_read_b64_tr_b16 v[222:223], v250 offset:840\n\t" \
  "v_lshlrev_b32_sdwa v194, 1, v194 dst_sel:DWORD dst_unused:UNUSED_PAD src0_sel:DWORD src1_sel:WORD_1\n\t" \
  "v_pk_fma_f16 v226, v226, %[hc], %[hc]\n\t" \
  "v_smfmac_f32_16x16x64_bf16 v[48:51], v[208:211], v[160:167], v248\n\t" \
  "s_waitcnt lgkmcnt(12)\n\t" \
  "ds_read_u16_d16_hi v194, v194 offset:0\n\t" \
  "v_rcp_f16 v225, v224\n\t" \
  "v_smfmac_f32_16x16x64_bf16 v[72:75], v[208:211], v[168:175], v248\n\t" \
  "s_waitcnt lgkmcnt(9)\n\t" \
  "ds_read_b64_tr_b16 v[208:209], v250 offset:128\n\t" \
  "v_lshlrev_b32_sdwa v195, 1, v195 dst_sel:DWORD dst_unused:UNUSED_PAD src0_sel:DWORD src1_sel:WORD_1\n\t" \
  "v_rcp_f16 v227, v226\n\t" \
  "v_smfmac_f32_16x16x64_bf16 v[4:7], v[212:215], v[144:151], v248\n\t" \
  "ds_read_u16_d16_hi v195, v195 offset:0\n\t" \
  "v_lshlrev_b32_sdwa v200, 1, v200 dst_sel:DWORD dst_unused:UNUSED_PAD src0_sel:DWORD src1_sel:WORD_1\n\t" \
  "v_smfmac_f32_16x16x64_bf16 v[28:31], v[212:215], v[152:159], v248\n\t" \
  "s_add_u32 %[ob], %[ob], %[dob]\n\t" \
  "ds_read_b64_tr_b16 v[210:211], v250 offset:896\n\t" \
  "v_rcp_f16_sdwa v224, v224 dst_sel:DWORD dst_unused:UNUSED_PAD src0_sel:WORD_1\n\t" \
  "v_smfmac_f32_16x16x64_bf16 v[52:55], v[212:215], v[160:167], v248\n\t" \
  "ds_read_u16_d16_hi v200, v200 offset:0\n\t" \
  "v_lshlrev_b32_sdwa v201, 1, v201 dst_sel:DWORD dst_unused:UNUSED_PAD src0_sel:DWORD src1_sel:WORD_1\n\t" \
  "v_smfmac_f32_16x16x64_bf16 v[76:79], v[212:215], v[168:175], v248\n\t" \
  "s_waitcnt lgkmcnt(9)\n\t" \
  "ds_read_b64_tr_b16 v[212:213], v250 offset:136\n\t" \
  "v_rcp_f16_sdwa v226, v226 dst_sel:DWORD dst_unused:UNUSED_PAD src0_sel:WORD_1\n\t" \
  "v_smfmac_f32_16x16x64_bf16 v[8:11], v[216:219], v[144:151], v248\n\t" \
  "ds_read_u16_d16_hi v201, v201 offset:0\n\t" \
  "v_fma_mix_f32 v184, v184, v225, 0 op_sel_hi:[0,1,0]\n\t" \
  "v_smfmac_f32_16x16x64_bf16 v[32:35], v[216:219], v[152:159], v248\n\t" \
  "ds_read_b64_tr_b16 v[214:215], v250 offset:904\n\t" \
  "v_lshlrev_b32_sdwa v202, 1, v202 dst_sel:DWORD dst_unused:UNUSED_PAD src0_sel:DWORD src1_sel:WORD_1\n\t" \
  "v_fma_mix_f32 v186, v186, v227, 0 op_sel_hi:[0,1,0]\n\t" \
  "v_smfmac_f32_16x16x64_bf16 v[56:59], v[216:219], v[160:167], v248\n\t" \
  "ds_read_u16_d16_hi v202, v202 offset:0\n\t" \
  "v_fma_mix_f32 v185, v185, v224, 0 op_sel_hi:[0,1,0]\n\t" \
  "v_smfmac_f32_16x16x64_bf16 v[80:83], v[216:219], v[168:175], v248\n\t" \
  "s_waitcnt lgkmcnt(9)\n\t" \
  "s_barrier\n\t" \
  "ds_read_b64_tr_b16 v[216:217], v249 offset:12288\n\t" \
  "v_lshlrev_b32_sdwa v203, 1, v203 dst_sel:DWORD dst_unused:UNUSED_PAD src0_sel:DWORD src1_sel:WORD_1\n\t" \
  "v_fma_mix_f32 v187, v187, v226, 0 op_sel_hi:[0,1,0]\n\t" \
  "v_smfmac_f32_16x16x64_bf16 v[12:15], v[220:223], v[144:151], v248\n\t" \
  "ds_read_u16_d16_hi v203, v203 offset:0\n\t" \
  "v_cvt_pk_f16_f32 v224, v188, v189\n\t" \
  "v_cvt_pk_f16_f32 v226, v190, v191\n\t" \
  "v_smfmac_f32_16x16x64_bf16 v[36:39], v[220:223], v[152:159], v248\n\t" \
  "ds_read_b64_tr_b16 v[218:219], v249 offset:12544\n\t" \
  "v_lshlrev_b32_sdwa v180, 1, v180 dst_sel:DWORD dst_unused:UNUSED_PAD src0_sel:DWORD src1_sel:WORD_1\n\t" \
  "v_pk_fma_f16 v225, v224, v224, %[h1] neg_lo:[1,0,0] neg_hi:[1,0,0]\n\t" \
  "v_smfmac_f32_16x16x64_bf16 v[60:63], v[220:223], v[160:167], v248\n\t" \
  "ds_read_u16_d16_hi v180, v180 offset:0\n\t" \
  "v_pk_fma_f16 v227, v226, v226, %[h1] neg_lo:[1,0,0] neg_hi:[1,0,0]\n\t" \
  "v_smfmac_f32_16x16x64_bf16 v[84:87], v[220:223], v[168:175], v248\n\t" \
  "s_waitcnt lgkmcnt(9)\n\t" \
  "ds_read_b64_tr_b16 v[220:221], v249 offset:14336\n\t" \
  "v_lshlrev_b32_sdwa v181, 1, v181 dst_sel:DWORD dst_unused:UNUSED_PAD src0_sel:DWORD src1_sel:WORD_1\n\t" \
  "v_pk_mul_f16 v225, v224, v225\n\t" \
  "v_smfmac_f32_16x16x64_bf16 v[16:19], v[208:211], v[144:151], v248\n\t" \
  "s_add_u32 %[rs], %[rs], 1\n\t" \
  "ds_read_u16_d16_hi v181, v181 offset:0\n\t" \
  "v_pk_mul_f16 v227, v226, v227\n\t" \
  "v_exp_f16 v224, v225\n\t" \
  "v_smfmac_f32_16x16x64_bf16 v[40:43], v[208:211], v[152:159], v248\n\t" \
  "s_cmp_eq_u32 %[rs], 6\n\t" \
  "ds_read_b64_tr_b16 v[222:223], v249 offset:14592\n\t" \
  "v_lshlrev_b32_sdwa v182, 1, v182 dst_sel:DWORD dst_unused:UNUSED_PAD src0_sel:DWORD src1_sel:WORD_1\n\t" \
  "v_smfmac_f32_16x16x64_bf16 v[64:67], v[208:211], v[160:167], v248\n\t" \
  "ds_read_u16_d16_hi v182, v182 offset:0\n\t" \
  "v_exp_f16 v226, v227\n\t" \
  "v_smfmac_f32_16x16x64_bf16 v[88:91], v[208:211], v[168:175], v248\n\t" \
  "s_waitcnt lgkmcnt(9)\n\t" \
  "ds_read_b64_tr_b16 v[208:209], v249 offset:16384\n\t" \
  "v_lshlrev_b32_sdwa v183, 1, v183 dst_sel:DWORD dst_unused:UNUSED_PAD src0_sel:DWORD src1_sel:WORD_1\n\t" \
  "v_smfmac_f32_16x16x64_bf16 v[20:23], v[212:215], v[144:151], v248\n\t" \
  "s_cselect_b32 %[rs], 0, %[rs]\n\t" \
  "ds_read_u16_d16_hi v183, v183 offset:0\n\t" \
  "v_exp_f16_sdwa v224, v225 dst_sel:WORD_1 dst_unused:UNUSED_PRESERVE src0_sel:WORD_1\n\t" \
  "v_smfmac_f32_16x16x64_bf16 v[44:47], v[212:215], v[152:159], v248\n\t" \
  "s_cselect_b32 %[t0], -75920, %[ps]\n\t" \
  "ds_read_b64_tr_b16 v[210:211], v249 offset:16640\n\t" \
  "v_lshlrev_b32_sdwa v196, 1, v196 dst_sel:DWORD dst_unused:UNUSED_PAD src0_sel:DWORD src1_sel:WORD_1\n\t" \
  "v_exp_f16_sdwa v226, v227 dst_sel:WORD_1 dst_unused:UNUSED_PRESERVE src0_sel:WORD_1\n\t" \
  "v_smfmac_f32_16x16x64_bf16 v[68:71], v[212:215], v[160:167], v248\n\t" \
  "ds_read_u16_d16_hi v196, v196 offset:0\n\t" \
  "v_add_u32 v252, %[t0], v252\n\t" \
  "v_smfmac_f32_16x16x64_bf16 v[92:95], v[212:215], v[168:175], v248\n\t" \
  "s_waitcnt lgkmcnt(9)\n\t" \
  "ds_read_b64_tr_b16 v[212:213], v249 offset:12800\n\t" \
  "v_lshlrev_b32_sdwa v197, 1, v197 dst_sel:DWORD dst_unused:UNUSED_PAD src0_sel:DWORD src1_sel:WORD_1\n\t" \
  "v_pk_fma_f16 v224, v224, %[hc], %[hc]\n\t" \
  "v_mfma_f32_16x16x32_bf16 v[144:147], v[216:219], v[96:99], 0\n\t" \
  "ds_read_u16_d16_hi v197, v197 offset:0\n\t" \
  "v_pk_fma_f16 v226, v226, %[hc], %[hc]\n\t" \
  "v_mfma_f32_16x16x32_bf16 v[152:155], v[216:219], v[108:111], 0\n\t" \
  "ds_read_b64_tr_b16 v[214:215], v249 offset:13056\n\t" \
  "v_lshlrev_b32_sdwa v198, 1, v198 dst_sel:DWORD dst_unused:UNUSED_PAD src0_sel:DWORD src1_sel:WORD_1\n\t" \
  "v_rcp_f16 v225, v224\n\t" \
  "v_mfma_f32_16x16x32_bf16 v[160:163], v[216:219], v[120:123], 0\n\t" \
  "ds_read_u16_d16_hi v198, v198 offset:0\n\t" \
  "v_rcp_f16 v227, v226\n\t" \
  "v_mfma_f32_16x16x32_bf16 v[168:171], v[216:219], v[132:135], 0\n\t" \
  "s_waitcnt lgkmcnt(9)\n\t" \
  "ds_read_b64_tr_b16 v[216:217], v249 offset:14848\n\t" \
  "v_lshlrev_b32_sdwa v199, 1, v199 dst_sel:DWORD dst_unused:UNUSED_PAD src0_sel:DWORD src1_sel:WORD_1\n\t" \
  "v_mfma_f32_16x16x32_bf16 v[144:147], v[220:223], v[100:103], v[144:147]\n\t" \
  "ds_read_u16_d16_hi v199, v199 offset:0\n\t" \
  "v_rcp_f16_sdwa v224, v224 dst_sel:DWORD dst_unused:UNUSED_PAD src0_sel:WORD_1\n\t" \
  "v_mfma_f32_16x16x32_bf16 v[152:155], v[220:223], v[112:115], v[152:155]\n\t" \
  "ds_read_b64_tr_b16 v[218:219], v249 offset:15104\n\t" \
  "v_lshlrev_b32_sdwa v204, 1, v204 dst_sel:DWORD dst_unused:UNUSED_PAD src0_sel:DWORD src1_sel:WORD_1\n\t" \
  "v_mfma_f32_16x16x32_bf16 v[160:163], v[220:223], v[124:127], v[160:163]\n\t" \
  "ds_read_u16_d16_hi v204, v204 offset:0\n\t" \
  "v_rcp_f16_sdwa v226, v226 dst_sel:DWORD dst_unused:UNUSED_PAD src0_sel:WORD_1\n\t" \
  "v_mfma_f32_16x16x32_bf16 v[168:171], v[220:223], v[136:139], v[168:171]\n\t" \
  "s_waitcnt lgkmcnt(9)\n\t" \
  "ds_read_b64_tr_b16 v[220:221], v249 offset:16896\n\t" \
  "v_lshlrev_b32_sdwa v205, 1, v205 dst_sel:DWORD dst_unused:UNUSED_PAD src0_sel:DWORD src1_sel:WORD_1\n\t" \
  "v_fma_mix_f32 v188, v188, v225, 0 op_sel_hi:[0,1,0]\n\t" \
  "v_mfma_f32_16x16x32_bf16 v[144:147], v[208:211], v[104:107], v[144:147]\n\t" \
  "ds_read_u16_d16_hi v205, v205 offset:0\n\t" \
  "v_fma_mix_f32 v190, v190, v227, 0 op_sel_hi:[0,1,0]\n\t" \
  "v_mfma_f32_16x16x32_bf16 v[152:155], v[208:211], v[116:119], v[152:155]\n\t" \
  "ds_read_b64_tr_b16 v[222:223], v249 offset:17152\n\t" \
  "v_lshlrev_b32_sdwa v206, 1, v206 dst_sel:DWORD dst_unused:UNUSED_PAD src0_sel:DWORD src1_sel:WORD_1\n\t" \
  "v_fma_mix_f32 v189, v189, v224, 0 op_sel_hi:[0,1,0]\n\t" \
  "v_mfma_f32_16x16x32_bf16 v[160:163], v[208:211], v[128:131], v[160:163]\n\t" \
  "ds_read_u16_d16_hi v206, v206 offset:0\n\t" \
  "v_fma_mix_f32 v191, v191, v226, 0 op_sel_hi:[0,1,0]\n\t" \
  "v_mfma_f32_16x16x32_bf16 v[168:171], v[208:211], v[140:143], v[168:171]\n\t" \
  "s_waitcnt lgkmcnt(9)\n\t" \
  "ds_read_b64_tr_b16 v[208:209], v250 offset:6160\n\t" \
  "v_lshlrev_b32_sdwa v207, 1, v207 dst_sel:DWORD dst_unused:UNUSED_PAD src0_sel:DWORD src1_sel:WORD_1\n\t" \
  "v_mfma_f32_16x16x32_bf16 v[148:151], v[212:215], v[96:99], 0\n\t" \
  "ds_read_u16_d16_hi v207, v207 offset:0\n\t" \
  "v_lshlrev_b32_sdwa v144, 1, v144 dst_sel:DWORD dst_unused:UNUSED_PAD src0_sel:DWORD src1_sel:WORD_1\n\t" \
  "v_mfma_f32_16x16x32_bf16 v[156:159], v[212:215], v[108:111], 0\n\t" \
  "ds_read_b64_tr_b16 v[210:211], v250 offset:6928\n\t" \
  "v_cvt_pk_f16_f32 v224, v152, v153\n\t" \
  "v_cvt_pk_f16_f32 v226, v154, v155\n\t" \
  "v_mfma_f32_16x16x32_bf16 v[164:167], v[212:215], v[120:123], 0\n\t" \
  "ds_read_u16_d16_hi v144, v144 offset:0\n\t" \
  "v_pk_fma_f16 v225, v224, v224, %[h1] neg_lo:[1,0,0] neg_hi:[1,0,0]\n\t" \
  "v_mfma_f32_16x16x32_bf16 v[172:175], v[212:215], v[132:135], 0\n\t" \
  "s_waitcnt lgkmcnt(9)\n\t" \
  "ds_read_b64_tr_b16 v[212:213], v250 offset:6168\n\t" \
  "v_lshlrev_b32_sdwa v145, 1, v145 dst_sel:DWORD dst_unused:UNUSED_PAD src0_sel:DWORD src1_sel:WORD_1\n\t" \
  "v_pk_fma_f16 v227, v226, v226, %[h1] neg_lo:[1,0,0] neg_hi:[1,0,0]\n\t" \
  "v_mfma_f32_16x16x32_bf16 v[148:151], v[216:219], v[100:103], v[148:151]\n\t" \
  "ds_read_u16_d16_hi v145, v145 offset:0\n\t" \
  "v_pk_mul_f16 v225, v224, v225\n\t" \
  "v_mfma_f32_16x16x32_bf16 v[156:159], v[216:219], v[112:115], v[156:159]\n\t" \
  "ds_read_b64_tr_b16 v[214:215], v250 offset:6936\n\t" \
  "v_lshlrev_b32_sdwa v146, 1, v146 dst_sel:DWORD dst_unused:UNUSED_PAD src0_sel:DWORD src1_sel:WORD_1\n\t" \
  "v_pk_mul_f16 v227, v226, v227\n\t" \
  "v_exp_f16 v224, v225\n\t" \
  "v_mfma_f32_16x16x32_bf16 v[164:167], v[216:219], v[124:127], v[164:167]\n\t" \
  "ds_read_u16_d16_hi v146, v146 offset:0\n\t" \
  "v_lshlrev_b32_sdwa v147, 1, v147 dst_sel:DWORD dst_unused:UNUSED_PAD src0_sel:DWORD src1_sel:WORD_1\n\t" \
  "v_mfma_f32_16x16x32_bf16 v[172:175], v[216:219], v[136:139], v[172:175]\n\t" \
  "s_waitcnt lgkmcnt(9)\n\t" \
  "ds_read_b64_tr_b16 v[216:217], v250 offset:6224\n\t" \
  "v_exp_f16 v226, v227\n\t" \
  "v_mfma_f32_16x16x32_bf16 v[148:151], v[220:223], v[104:107], v[148:151]\n\t" \
  "ds_read_u16_d16_hi v147, v147 offset:0\n\t" \
  "v_lshlrev_b32_sdwa v160, 1, v160 dst_sel:DWORD dst_unused:UNUSED_PAD src0_sel:DWORD src1_sel:WORD_1\n\t" \
  "v_mfma_f32_16x16x32_bf16 v[156:159], v[220:223], v[116:119], v[156:159]\n\t" \
  "ds_read_b64_tr_b16 v[218:219], v250 offset:6992\n\t" \
  "v_exp_f16_sdwa v224, v225 dst_sel:WORD_1 dst_unused:UNUSED_PRESERVE src0_sel:WORD_1\n\t" \
  "v_mfma_f32_16x16x32_bf16 v[164:167], v[220:223], v[128:131], v[164:167]\n\t" \
  "ds_read_u16_d16_hi v160, v160 offset:0\n\t" \
  "v_exp_f16_sdwa v226, v227 dst_sel:WORD_1 dst_unused:UNUSED_PRESERVE src0_sel:WORD_1\n\t" \
  "v_mfma_f32_16x16x32_bf16 v[172:175], v[220:223], v[140:143], v[172:175]\n\t" \
  "s_waitcnt lgkmcnt(9)\n\t" \
  "ds_read_b64_tr_b16 v[220:221], v250 offset:6232\n\t" \
  "v_lshlrev_b32_sdwa v161, 1, v161 dst_sel:DWORD dst_unused:UNUSED_PAD src0_sel:DWORD src1_sel:WORD_1\n\t" \
  "v_smfmac_f32_16x16x64_bf16 v[0:3], v[208:211], v[176:183], v248\n\t" \
  "ds_read_u16_d16_hi v161, v161 offset:0\n\t" \
  "v_pk_fma_f16 v224, v224, %[hc], %[hc]\n\t" \
  "v_smfmac_f32_16x16x64_bf16 v[24:27], v[208:211], v[184:191], v248\n\t" \
  "ds_read_b64_tr_b16 v[222:223], v250 offset:7000\n\t" \
  "v_lshlrev_b32_sdwa v162, 1, v162 dst_sel:DWORD dst_unused:UNUSED_PAD src0_sel:DWORD src1_sel:WORD_1\n\t" \
  "v_pk_fma_f16 v226, v226, %[hc], %[hc]\n\t" \
  "v_smfmac_f32_16x16x64_bf16 v[48:51], v[208:211], v[192:199], v248\n\t" \
  "ds_read_u16_d16_hi v162, v162 offset:0\n\t" \
  "v_rcp_f16 v225, v224\n\t" \
  "v_smfmac_f32_16x16x64_bf16 v[72:75], v[208:211], v[200:207], v248\n\t" \
  "s_waitcnt lgkmcnt(9)\n\t" \
  "ds_read_b64_tr_b16 v[208:209], v250 offset:6288\n\t" \
  "v_lshlrev_b32_sdwa v163, 1, v163 dst_sel:DWORD dst_unused:UNUSED_PAD src0_sel:DWORD src1_sel:WORD_1\n\t" \
  "v_rcp_f16 v227, v226\n\t" \
  "v_smfmac_f32_16x16x64_bf16 v[4:7], v[212:215], v[176:183], v248\n\t" \
  "ds_read_u16_d16_hi v163, v163 offset:0\n\t" \
  "v_lshlrev_b32_sdwa v168, 1, v168 dst_sel:DWORD dst_unused:UNUSED_PAD src0_sel:DWORD src1_sel:WORD_1\n\t" \
  "v_smfmac_f32_16x16x64_bf16 v[28:31], v[212:215], v[184:191], v248\n\t" \
  "ds_read_b64_tr_b16 v[210:211], v250 offset:7056\n\t" \
  "v_rcp_f16_sdwa v224, v224 dst_sel:DWORD dst_unused:UNUSED_PAD src0_sel:WORD_1\n\t" \
  "v_smfmac_f32_16x16x64_bf16 v[52:55], v[212:215], v[192:199], v248\n\t" \
  "ds_read_u16_d16_hi v168, v168 offset:0\n\t" \
  "v_lshlrev_b32_sdwa v169, 1, v169 dst_sel:DWORD dst_unused:UNUSED_PAD src0_sel:DWORD src1_sel:WORD_1\n\t" \
  "v_smfmac_f32_16x16x64_bf16 v[76:79], v[212:215], v[200:207], v248\n\t" \
  "s_waitcnt lgkmcnt(9)\n\t" \
  "ds_read_b64_tr_b16 v[212:213], v250 offset:6296\n\t" \
  "v_rcp_f16_sdwa v226, v226 dst_sel:DWORD dst_unused:UNUSED_PAD src0_sel:WORD_1\n\t" \
  "v_smfmac_f32_16x16x64_bf16 v[8:11], v[216:219], v[176:183], v248\n\t" \
  "ds_read_u16_d16_hi v169, v169 offset:0\n\t" \
  "v_fma_mix_f32 v152, v152, v225, 0 op_sel_hi:[0,1,0]\n\t" \
  "v_smfmac_f32_16x16x64_bf16 v[32:35], v[216:219], v[184:191], v248\n\t" \
  "ds_read_b64_tr_b16 v[214:215], v250 offset:7064\n\t" \
  "v_lshlrev_b32_sdwa v170, 1, v170 dst_sel:DWORD dst_unused:UNUSED_PAD src0_sel:DWORD src1_sel:WORD_1\n\t" \
  "v_fma_mix_f32 v154, v154, v227, 0 op_sel_hi:[0,1,0]\n\t" \
  "v_smfmac_f32_16x16x64_bf16 v[56:59], v[216:219], v[192:199], v248\n\t" \
  "ds_read_u16_d16_hi v170, v170 offset:0\n\t" \
  "v_fma_mix_f32 v153, v153, v224, 0 op_sel_hi:[0,1,0]\n\t" \
  "v_smfmac_f32_16x16x64_bf16 v[80:83], v[216:219], v[200:207], v248\n\t" \
  "s_waitcnt lgkmcnt(9)\n\t" \
  "s_barrier\n\t" \
  "ds_read_b64_tr_b16 v[216:217], v249 offset:0\n\t" \
  "v_lshlrev_b32_sdwa v171, 1, v171 dst_sel:DWORD dst_unused:UNUSED_PAD src0_sel:DWORD src1_sel:WORD_1\n\t" \
  "v_fma_mix_f32 v155, v155, v226, 0 op_sel_hi:[0,1,0]\n\t" \
  "v_smfmac_f32_16x16x64_bf16 v[12:15], v[220:223], v[176:183], v248\n\t" \
  "ds_read_b128 v[108:111], v251 offset:0\n\t" \
  "v_cvt_pk_f16_f32 v224, v156, v157\n\t" \
  "v_cvt_pk_f16_f32 v226, v158, v159\n\t" \
  "v_smfmac_f32_16x16x64_bf16 v[36:39], v[220:223], v[184:191], v248\n\t" \
  "ds_read_b64_tr_b16 v[218:219], v249 offset:256\n\t" \
  "v_lshlrev_b32_sdwa v148, 1, v148 dst_sel:DWORD dst_unused:UNUSED_PAD src0_sel:DWORD src1_sel:WORD_1\n\t" \
  "v_pk_fma_f16 v225, v224, v224, %[h1] neg_lo:[1,0,0] neg_hi:[1,0,0]\n\t" \
  "v_smfmac_f32_16x16x64_bf16 v[60:63], v[220:223], v[192:199], v248\n\t" \
  "ds_read_b128 v[112:115], v251 offset:4096\n\t" \
  "v_pk_fma_f16 v227, v226, v226, %[h1] neg_lo:[1,0,0] neg_hi:[1,0,0]\n\t" \
  "v_smfmac_f32_16x16x64_bf16 v[84:87], v[220:223], v[200:207], v248\n\t" \
  "s_waitcnt lgkmcnt(9)\n\t" \
  "ds_read_b64_tr_b16 v[220:221], v249 offset:2048\n\t" \
  "v_lshlrev_b32_sdwa v149, 1, v149 dst_sel:DWORD dst_unused:UNUSED_PAD src0_sel:DWORD src1_sel:WORD_1\n\t" \
  "v_pk_mul_f16 v225, v224, v225\n\t" \
  "v_smfmac_f32_16x16x64_bf16 v[16:19], v[208:211], v[176:183], v248\n\t" \
  "ds_read_b128 v[116:119], v251 offset:8192\n\t" \
  "v_pk_mul_f16 v227, v226, v227\n\t" \
  "v_exp_f16 v224, v225\n\t" \
  "v_smfmac_f32_16x16x64_bf16 v[40:43], v[208:211], v[184:191], v248\n\t" \
  "ds_read_b64_tr_b16 v[222:223], v249 offset:2304\n\t" \
  "v_lshlrev_b32_sdwa v150, 1, v150 dst_sel:DWORD dst_unused:UNUSED_PAD src0_sel:DWORD src1_sel:WORD_1\n\t" \
  "v_smfmac_f32_16x16x64_bf16 v[64:67], v[208:211], v[192:199], v248\n\t" \
  "ds_read_u16_d16_hi v171, v171 offset:0\n\t" \
  "v_exp_f16 v226, v227\n\t" \
  "v_smfmac_f32_16x16x64_bf16 v[88:91], v[208:211], v[200:207], v248\n\t" \
  "s_waitcnt lgkmcnt(9)\n\t" \
  "ds_read_b64_tr_b16 v[208:209], v249 offset:4096\n\t" \
  "v_lshlrev_b32_sdwa v151, 1, v151 dst_sel:DWORD dst_unused:UNUSED_PAD src0_sel:DWORD src1_sel:WORD_1\n\t" \
  "v_smfmac_f32_16x16x64_bf16 v[20:23], v[212:215], v[176:183], v248\n\t" \
  "ds_read_u16_d16_hi v148, v148 offset:0\n\t" \
  "v_exp_f16_sdwa v224, v225 dst_sel:WORD_1 dst_unused:UNUSED_PRESERVE src0_sel:WORD_1\n\t" \
  "v_smfmac_f32_16x16x64_bf16 v[44:47], v[212:215], v[184:191], v248\n\t" \
  "ds_read_b64_tr_b16 v[210:211], v249 offset:4352\n\t" \
  "v_lshlrev_b32_sdwa v164, 1, v164 dst_sel:DWORD dst_unused:UNUSED_PAD src0_sel:DWORD src1_sel:WORD_1\n\t" \
  "v_exp_f16_sdwa v226, v227 dst_sel:WORD_1 dst_unused:UNUSED_PRESERVE src0_sel:WORD_1\n\t" \
  "v_smfmac_f32_16x16x64_bf16 v[68:71], v[212:215], v[192:199], v248\n\t" \
  "ds_read_u16_d16_hi v149, v149 offset:0\n\t" \
  "v_lshlrev_b32_sdwa v165, 1, v165 dst_sel:DWORD dst_unused:UNUSED_PAD src0_sel:DWORD src1_sel:WORD_1\n\t" \
  "v_smfmac_f32_16x16x64_bf16 v[92:95], v[212:215], v[200:207], v248\n\t" \
  "s_waitcnt lgkmcnt(9)\n\t" \
  "ds_read_b64_tr_b16 v[212:213], v249 offset:512\n\t" \
  "v_pk_fma_f16 v224, v224, %[hc], %[hc]\n\t" \
  "v_mfma_f32_16x16x32_bf16 v[176:179], v[216:219], v[96:99], 0\n\t" \
  "ds_read_u16_d16_hi v150, v150 offset:0\n\t" \
  "v_pk_fma_f16 v226, v226, %[hc], %[hc]\n\t" \
  "v_mfma_f32_16x16x32_bf16 v[184:187], v[216:219], v[108:111], 0\n\t" \
  "ds_read_b64_tr_b16 v[214:215], v249 offset:768\n\t" \
  "v_lshlrev_b32_sdwa v166, 1, v166 dst_sel:DWORD dst_unused:UNUSED_PAD src0_sel:DWORD src1_sel:WORD_1\n\t" \
  "v_rcp_f16 v225, v224\n\t" \
  "v_mfma_f32_16x16x32_bf16 v[192:195], v[216:219], v[120:123], 0\n\t" \
  "ds_read_u16_d16_hi v151, v151 offset:0\n\t" \
  "v_rcp_f16 v227, v226\n\t" \
  "v_mfma_f32_16x16x32_bf16 v[200:203], v[216:219], v[132:135], 0\n\t" \
  "s_waitcnt lgkmcnt(9)\n\t" \
  "ds_read_b64_tr_b16 v[216:217], v249 offset:2560\n\t" \
  "v_lshlrev_b32_sdwa v167, 1, v167 dst_sel:DWORD dst_unused:UNUSED_PAD src0_sel:DWORD src1_sel:WORD_1\n\t" \
  "v_mfma_f32_16x16x32_bf16 v[176:179], v[220:223], v[100:103], v[176:179]\n\t" \
  "ds_read_u16_d16_hi v164, v164 offset:0\n\t" \
  "v_rcp_f16_sdwa v224, v224 dst_sel:DWORD dst_unused:UNUSED_PAD src0_sel:WORD_1\n\t" \
  "v_mfma_f32_16x16x32_bf16 v[184:187], v[220:223], v[112:115], v[184:187]\n\t" \
  "ds_read_b64_tr_b16 v[218:219], v249 offset:2816\n\t" \
  "v_lshlrev_b32_sdwa v172, 1, v172 dst_sel:DWORD dst_unused:UNUSED_PAD src0_sel:DWORD src1_sel:WORD_1\n\t" \
  "v_mfma_f32_16x16x32_bf16 v[192:195], v[220:223], v[124:127], v[192:195]\n\t" \
  "ds_read_u16_d16_hi v165, v165 offset:0\n\t" \
  "v_rcp_f16_sdwa v226, v226 dst_sel:DWORD dst_unused:UNUSED_PAD src0_sel:WORD_1\n\t" \
  "v_mfma_f32_16x16x32_bf16 v[200:203], v[220:223], v[136:139], v[200:203]\n\t" \
  "s_waitcnt lgkmcnt(9)\n\t" \
  "ds_read_b64_tr_b16 v[220:221], v249 offset:4608\n\t" \
  "v_lshlrev_b32_sdwa v173, 1, v173 dst_sel:DWORD dst_unused:UNUSED_PAD src0_sel:DWORD src1_sel:WORD_1\n\t" \
  "v_fma_mix_f32 v156, v156, v225, 0 op_sel_hi:[0,1,0]\n\t" \
  "v_mfma_f32_16x16x32_bf16 v[176:179], v[208:211], v[104:107], v[176:179]\n\t" \
  "ds_read_u16_d16_hi v166, v166 offset:0\n\t" \
  "v_fma_mix_f32 v158, v158, v227, 0 op_sel_hi:[0,1,0]\n\t" \
  "v_mfma_f32_16x16x32_bf16 v[184:187], v[208:211], v[116:119], v[184:187]\n\t" \
  "ds_read_b64_tr_b16 v[222:223], v249 offset:4864\n\t" \
  "v_lshlrev_b32_sdwa v174, 1, v174 dst_sel:DWORD dst_unused:UNUSED_PAD src0_sel:DWORD src1_sel:WORD_1\n\t" \
  "v_fma_mix_f32 v157, v157, v224, 0 op_sel_hi:[0,1,0]\n\t" \
  "v_mfma_f32_16x16x32_bf16 v[192:195], v[208:211], v[128:131], v[192:195]\n\t" \
  "ds_read_u16_d16_hi v167, v167 offset:0\n\t" \
  "v_fma_mix_f32 v159, v159, v226, 0 op_sel_hi:[0,1,0]\n\t" \
  "v_mfma_f32_16x16x32_bf16 v[200:203], v[208:211], v[140:143], v[200:203]\n\t" \
  "s_waitcnt lgkmcnt(9)\n\t" \
  "ds_read_b64_tr_b16 v[208:209], v250 offset:12320\n\t" \
  "v_lshlrev_b32_sdwa v175, 1, v175 dst_sel:DWORD dst_unused:UNUSED_PAD src0_sel:DWORD src1_sel:WORD_1\n\t" \
  "v_mfma_f32_16x16x32_bf16 v[180:183], v[212:215], v[96:99], 0\n\t" \
  "ds_read_u16_d16_hi v172, v172 offset:0\n\t" \
  "v_lshlrev_b32_sdwa v176, 1, v176 dst_sel:DWORD dst_unused:UNUSED_PAD src0_sel:DWORD src1_sel:WORD_1\n\t" \
  "v_mfma_f32_16x16x32_bf16 v[188:191], v[212:215], v[108:111], 0\n\t" \
  "ds_read_b64_tr_b16 v[210:211], v250 offset:13088\n\t" \
  "ds_read_u16_d16_hi v173, v173 offset:0\n\t" \
  "v_cvt_pk_f16_f32 v224, v184, v185\n\t" \
  "v_cvt_pk_f16_f32 v226, v186, v187\n\t" \
  "v_mfma_f32_16x16x32_bf16 v[196:199], v[212:215], v[120:123], 0\n\t" \
  "ds_read_u16_d16_hi v174, v174 offset:0\n\t" \
  "v_pk_fma_f16 v225, v224, v224, %[h1] neg_lo:[1,0,0] neg_hi:[1,0,0]\n\t" \
  "v_mfma_f32_16x16x32_bf16 v[204:207], v[212:215], v[132:135], 0\n\t" \
  "s_waitcnt lgkmcnt(10)\n\t" \
  "ds_read_b64_tr_b16 v[212:213], v250 offset:12328\n\t" \
  "v_lshlrev_b32_sdwa v177, 1, v177 dst_sel:DWORD dst_unused:UNUSED_PAD src0_sel:DWORD src1_sel:WORD_1\n\t" \
  "v_pk_fma_f16 v227, v226, v226, %[h1] neg_lo:[1,0,0] neg_hi:[1,0,0]\n\t" \
  "v_mfma_f32_16x16x32_bf16 v[180:183], v[216:219], v[100:103], v[180:183]\n\t" \
  "ds_read_u16_d16_hi v175, v175 offset:0\n\t" \
  "v_pk_mul_f16 v225, v224, v225\n\t" \
  "v_mfma_f32_16x16x32_bf16 v[188:191], v[216:219], v[112:115], v[188:191]\n\t" \
  "ds_read_b64_tr_b16 v[214:215], v250 offset:13096\n\t" \
  "v_lshlrev_b32_sdwa v178, 1, v178 dst_sel:DWORD dst_unused:UNUSED_PAD src0_sel:DWORD src1_sel:WORD_1\n\t" \
  "v_pk_mul_f16 v227, v226, v227\n\t" \
  "v_exp_f16 v224, v225\n\t" \
  "v_mfma_f32_16x16x32_bf16 v[196:199], v[216:219], v[124:127], v[196:199]\n\t" \
  "ds_read_u16_d16_hi v176, v176 offset:0\n\t" \
  "v_lshlrev_b32_sdwa v179, 1, v179 dst_sel:DWORD dst_unused:UNUSED_PAD src0_sel:DWORD src1_sel:WORD_1\n\t" \
  "v_mfma_f32_16x16x32_bf16 v[204:207], v[216:219], v[136:139], v[204:207]\n\t" \
  "s_waitcnt lgkmcnt(10)\n\t" \
  "ds_read_b64_tr_b16 v[216:217], v250 offset:12384\n\t" \
  "v_exp_f16 v226, v227\n\t" \
  "v_mfma_f32_16x16x32_bf16 v[180:183], v[220:223], v[104:107], v[180:183]\n\t" \
  "ds_read_u16_d16_hi v177, v177 offset:0\n\t" \
  "v_lshlrev_b32_sdwa v192, 1, v192 dst_sel:DWORD dst_unused:UNUSED_PAD src0_sel:DWORD src1_sel:WORD_1\n\t" \
  "v_mfma_f32_16x16x32_bf16 v[188:191], v[220:223], v[116:119], v[188:191]\n\t" \
  "ds_read_b64_tr_b16 v[218:219], v250 offset:13152\n\t" \
  "v_exp_f16_sdwa v224, v225 dst_sel:WORD_1 dst_unused:UNUSED_PRESERVE src0_sel:WORD_1\n\t" \
  "v_mfma_f32_16x16x32_bf16 v[196:199], v[220:223], v[128:131], v[196:199]\n\t" \
  "ds_read_u16_d16_hi v178, v178 offset:0\n\t" \
  "v_exp_f16_sdwa v226, v227 dst_sel:WORD_1 dst_unused:UNUSED_PRESERVE src0_sel:WORD_1\n\t" \
  "v_mfma_f32_16x16x32_bf16 v[204:207], v[220:223], v[140:143], v[204:207]\n\t" \
  "s_waitcnt lgkmcnt(10)\n\t" \
  "ds_read_b64_tr_b16 v[220:221], v250 offset:12392\n\t" \
  "v_lshlrev_b32_sdwa v193, 1, v193 dst_sel:DWORD dst_unused:UNUSED_PAD src0_sel:DWORD src1_sel:WORD_1\n\t" \
  "v_smfmac_f32_16x16x64_bf16 v[0:3], v[208:211], v[144:151], v248\n\t" \
  "ds_read_u16_d16_hi v179, v179 offset:0\n\t" \
  "v_pk_fma_f16 v224, v224, %[hc], %[hc]\n\t" \
  "v_smfmac_f32_16x16x64_bf16 v[24:27], v[208:211], v[152:159], v248\n\t" \
  "ds_read_b64_tr_b16 v[222:223], v250 offset:13160\n\t" \
  "v_lshlrev_b32_sdwa v194, 1, v194 dst_sel:DWORD dst_unused:UNUSED_PAD src0_sel:DWORD src1_sel:WORD_1\n\t" \
  "v_pk_fma_f16 v226, v226, %[hc], %[hc]\n\t" \
  "v_smfmac_f32_16x16x64_bf16 v[48:51], v[208:211], v[160:167], v248\n\t" \
  "s_waitcnt lgkmcnt(9)\n\t" \
  "ds_read_u16_d16_hi v192, v192 offset:0\n\t" \
  "v_rcp_f16 v225, v224\n\t" \
  "v_smfmac_f32_16x16x64_bf16 v[72:75], v[208:211], v[168:175], v248\n\t" \
  "s_waitcnt lgkmcnt(9)\n\t" \
  "ds_read_b64_tr_b16 v[208:209], v250 offset:12448\n\t" \
  "v_lshlrev_b32_sdwa v195, 1, v195 dst_sel:DWORD dst_unused:UNUSED_PAD src0_sel:DWORD src1_sel:WORD_1\n\t" \
  "v_rcp_f16 v227, v226\n\t" \
  "v_smfmac_f32_16x16x64_bf16 v[4:7], v[212:215], v[144:151], v248\n\t" \
  "ds_read_b64 v[242:243], v252 offset:128\n\t" \
  "v_lshlrev_b32_sdwa v200, 1, v200 dst_sel:DWORD dst_unused:UNUSED_PAD src0_sel:DWORD src1_sel:WORD_1\n\t" \
  "v_smfmac_f32_16x16x64_bf16 v[28:31], v[212:215], v[152:159], v248\n\t" \
  "ds_read_b64_tr_b16 v[210:211], v250 offset:13216\n\t" \
  "v_rcp_f16_sdwa v224, v224 dst_sel:DWORD dst_unused:UNUSED_PAD src0_sel:WORD_1\n\t" \
  "v_smfmac_f32_16x16x64_bf16 v[52:55], v[212:215], v[160:167], v248\n\t" \
  "ds_read_b64 v[244:245], v252 offset:136\n\t" \
  "v_lshlrev_b32_sdwa v201, 1, v201 dst_sel:DWORD dst_unused:UNUSED_PAD src0_sel:DWORD src1_sel:WORD_1\n\t" \
  "v_smfmac_f32_16x16x64_bf16 v[76:79], v[212:215], v[168:175], v248\n\t" \
  "s_waitcnt lgkmcnt(9)\n\t" \
  "ds_read_b64_tr_b16 v[212:213], v250 offset:12456\n\t" \
  "ds_read_u16_d16_hi v193, v193 offset:0\n\t" \
  "v_rcp_f16_sdwa v226, v226 dst_sel:DWORD dst_unused:UNUSED_PAD src0_sel:WORD_1\n\t" \
  "v_cvt_pk_bf16_f32 v230, v24, v25\n\t" \
  "v_smfmac_f32_16x16x64_bf16 v[8:11], v[216:219], v[144:151], v248\n\t" \
  "ds_read_b64_tr_b16 v[214:215], v250 offset:13224\n\t" \
  "v_fma_mix_f32 v184, v184, v225, 0 op_sel_hi:[0,1,0]\n\t" \
  "v_cvt_pk_bf16_f32 v231, v26, v27\n\t" \
  "v_smfmac_f32_16x16x64_bf16 v[32:35], v[216:219], v[152:159], v248\n\t" \
  "ds_read_u16_d16_hi v194, v194 offset:0\n\t" \
  "v_lshlrev_b32_sdwa v202, 1, v202 dst_sel:DWORD dst_unused:UNUSED_PAD src0_sel:DWORD src1_sel:WORD_1\n\t" \
  "v_fma_mix_f32 v186, v186, v227, 0 op_sel_hi:[0,1,0]\n\t" \
  "v_cvt_pk_bf16_f32 v232, v28, v29\n\t" \
  "v_smfmac_f32_16x16x64_bf16 v[56:59], v[216:219], v[160:167], v248\n\t" \
  "ds_read_u16_d16_hi v195, v195 offset:0\n\t" \
  "v_fma_mix_f32 v185, v185, v224, 0 op_sel_hi:[0,1,0]\n\t" \
  "v_cvt_pk_bf16_f32 v233, v30, v31\n\t" \
  "v_smfmac_f32_16x16x64_bf16 v[80:83], v[216:219], v[168:175], v248\n\t" \
  "s_waitcnt lgkmcnt(10)\n\t" \
  "s_barrier\n\t" \
  "ds_read_b64_tr_b16 v[216:217], v249 offset:6144\n\t" \
  "buffer_store_dwordx4 v[230:233], v253, %[osrd], %[ob] offen offset:0 sc1\n\t" \
  "v_lshlrev_b32_sdwa v203, 1, v203 dst_sel:DWORD dst_unused:UNUSED_PAD src0_sel:DWORD src1_sel:WORD_1\n\t" \
  "v_fma_mix_f32 v187, v187, v226, 0 op_sel_hi:[0,1,0]\n\t" \
  "v_smfmac_f32_16x16x64_bf16 v[12:15], v[220:223], v[144:151], v248\n\t" \
  "ds_read_b64_tr_b16 v[218:219], v249 offset:6400\n\t" \
  "ds_read_b64 v[230:231], v252 offset:0\n\t" \
  "v_cvt_pk_f16_f32 v224, v188, v189\n\t" \
  "v_cvt_pk_f16_f32 v226, v190, v191\n\t" \
  "v_smfmac_f32_16x16x64_bf16 v[36:39], v[220:223], v[152:159], v248\n\t" \
  "ds_read_b64 v[232:233], v252 offset:8\n\t" \
  "v_lshlrev_b32_sdwa v180, 1, v180 dst_sel:DWORD dst_unused:UNUSED_PAD src0_sel:DWORD src1_sel:WORD_1\n\t" \
  "v_pk_fma_f16 v225, v224, v224, %[h1] neg_lo:[1,0,0] neg_hi:[1,0,0]\n\t" \
  "v_smfmac_f32_16x16x64_bf16 v[60:63], v[220:223], v[160:167], v248\n\t" \
  "ds_read_u16_d16_hi v200, v200 offset:0\n\t" \
  "v_pk_fma_f16 v227, v226, v226, %[h1] neg_lo:[1,0,0] neg_hi:[1,0,0]\n\t" \
  "v_smfmac_f32_16x16x64_bf16 v[84:87], v[220:223], v[168:175], v248\n\t" \
  "s_waitcnt lgkmcnt(11)\n\t" \
  "ds_read_b64_tr_b16 v[220:221], v249 offset:8192\n\t" \
  "v_lshlrev_b32_sdwa v181, 1, v181 dst_sel:DWORD dst_unused:UNUSED_PAD src0_sel:DWORD src1_sel:WORD_1\n\t" \
  "v_pk_mul_f16 v225, v224, v225\n\t" \
  "v_cvt_pk_bf16_f32 v234, v32, v33\n\t" \
  "v_smfmac_f32_16x16x64_bf16 v[16:19], v[208:211], v[144:151], v248\n\t" \
  "ds_read_u16_d16_hi v201, v201 offset:0\n\t" \
  "v_pk_mul_f16 v227, v226, v227\n\t" \
  "v_exp_f16 v224, v225\n\t" \
  "v_cvt_pk_bf16_f32 v235, v34, v35\n\t" \
  "v_smfmac_f32_16x16x64_bf16 v[40:43], v[208:211], v[152:159], v248\n\t" \
  "ds_read_b64_tr_b16 v[222:223], v249 offset:8448\n\t" \
  "v_lshlrev_b32_sdwa v182, 1, v182 dst_sel:DWORD dst_unused:UNUSED_PAD src0_sel:DWORD src1_sel:WORD_1\n\t" \
  "v_cvt_pk_bf16_f32 v236, v36, v37\n\t" \
  "v_smfmac_f32_16x16x64_bf16 v[64:67], v[208:211], v[160:167], v248\n\t" \
  "ds_read_u16_d16_hi v202, v202 offset:0\n\t" \
  "v_exp_f16 v226, v227\n\t" \
  "v_cvt_pk_bf16_f32 v237, v38, v39\n\t" \
  "v_smfmac_f32_16x16x64_bf16 v[88:91], v[208:211], v[168:175], v248\n\t" \
  "s_waitcnt lgkmcnt(11)\n\t" \
  "ds_read_b64_tr_b16 v[208:209], v249 offset:10240\n\t" \
  "buffer_store_dwordx4 v[234:237], v253, %[osrd], %[ob] offen offset:64 sc1\n\t" \
  "v_lshlrev_b32_sdwa v183, 1, v183 dst_sel:DWORD dst_unused:UNUSED_PAD src0_sel:DWORD src1_sel:WORD_1\n\t" \
  "v_smfmac_f32_16x16x64_bf16 v[20:23], v[212:215], v[144:151], v248\n\t" \
  "ds_read_b64_tr_b16 v[210:211], v249 offset:10496\n\t" \
  "ds_read_b64 v[234:235], v252 offset:64\n\t" \
  "v_exp_f16_sdwa v224, v225 dst_sel:WORD_1 dst_unused:UNUSED_PRESERVE src0_sel:WORD_1\n\t" \
  "v_smfmac_f32_16x16x64_bf16 v[44:47], v[212:215], v[152:159], v248\n\t" \
  "s_waitcnt lgkmcnt(9)\n\t" \
  "ds_read_b64 v[236:237], v252 offset:72\n\t" \
  "v_lshlrev_b32_sdwa v196, 1, v196 dst_sel:DWORD dst_unused:UNUSED_PAD src0_sel:DWORD src1_sel:WORD_1\n\t" \
  "v_mfma_f32_16x16x16_bf16 v[24:27], v[246:247], v[230:231], 0\n\t" \
  "ds_read_u16_d16_hi v203, v203 offset:0\n\t" \
  "v_exp_f16_sdwa v226, v227 dst_sel:WORD_1 dst_unused:UNUSED_PRESERVE src0_sel:WORD_1\n\t" \
  "v_smfmac_f32_16x16x64_bf16 v[68:71], v[212:215], v[160:167], v248\n\t" \
  "ds_read_u16_d16_hi v180, v180 offset:0\n\t" \
  "v_lshlrev_b32_sdwa v197, 1, v197 dst_sel:DWORD dst_unused:UNUSED_PAD src0_sel:DWORD src1_sel:WORD_1\n\t" \
  "v_smfmac_f32_16x16x64_bf16 v[92:95], v[212:215], v[168:175], v248\n\t" \
  "ds_read_b64_tr_b16 v[212:213], v249 offset:6656\n\t" \
  "ds_read_u16_d16_hi v181, v181 offset:0\n\t" \
  "v_pk_fma_f16 v224, v224, %[hc], %[hc]\n\t" \
  "v_cvt_pk_bf16_f32 v238, v40, v41\n\t" \
  "v_mfma_f32_16x16x32_bf16 v[144:147], v[216:219], v[96:99], 0\n\t" \
  "s_waitcnt lgkmcnt(13)\n\t" \
  "ds_read_b64_tr_b16 v[214:215], v249 offset:6912\n\t" \
  "v_pk_fma_f16 v226, v226, %[hc], %[hc]\n\t" \
  "v_mfma_f32_16x16x16_bf16 v[28:31], v[246:247], v[232:233], 0\n\t" \
  "ds_read_u16_d16_hi v182, v182 offset:0\n\t" \
  "v_cvt_pk_bf16_f32 v239, v42, v43\n\t" \
  "v_mfma_f32_16x16x32_bf16 v[152:155], v[216:219], v[108:111], 0\n\t" \
  "ds_read_u16_d16_hi v183, v183 offset:0\n\t" \
  "v_lshlrev_b32_sdwa v198, 1, v198 dst_sel:DWORD dst_unused:UNUSED_PAD src0_sel:DWORD src1_sel:WORD_1\n\t" \
  "v_rcp_f16 v225, v224\n\t" \
  "v_cvt_pk_bf16_f32 v240, v44, v45\n\t" \
  "v_mfma_f32_16x16x32_bf16 v[160:163], v[216:219], v[120:123], 0\n\t" \
  "ds_read_u16_d16_hi v196, v196 offset:0\n\t" \
  "v_rcp_f16 v227, v226\n\t" \
  "v_cvt_pk_bf16_f32 v241, v46, v47\n\t" \
  "v_mfma_f32_16x16x32_bf16 v[168:171], v[216:219], v[132:135], 0\n\t" \
  "s_waitcnt lgkmcnt(13)\n\t" \
  "ds_read_b64_tr_b16 v[216:217], v249 offset:8704\n\t" \
  "v_lshlrev_b32_sdwa v199, 1, v199 dst_sel:DWORD dst_unused:UNUSED_PAD src0_sel:DWORD src1_sel:WORD_1\n\t" \
  "v_mfma_f32_16x16x32_bf16 v[144:147], v[220:223], v[100:103], v[144:147]\n\t" \
  "buffer_store_dwordx4 v[238:241], v253, %[osrd], %[ob] offen offset:128 sc1\n\t" \
  "v_rcp_f16_sdwa v224, v224 dst_sel:DWORD dst_unused:UNUSED_PAD src0_sel:WORD_1\n\t" \
  "v_mfma_f32_16x16x32_bf16 v[152:155], v[220:223], v[112:115], v[152:155]\n\t" \
  "s_waitcnt lgkmcnt(10)\n\t" \
  "ds_read_b64_tr_b16 v[218:219], v249 offset:8960\n\t" \
  "v_lshlrev_b32_sdwa v204, 1, v204 dst_sel:DWORD dst_unused:UNUSED_PAD src0_sel:DWORD src1_sel:WORD_1\n\t" \
  "v_mfma_f32_16x16x16_bf16 v[32:35], v[246:247], v[234:235], 0\n\t" \
  "ds_read_u16_d16_hi v197, v197 offset:0\n\t" \
  "v_rcp_f16_sdwa v226, v226 dst_sel:DWORD dst_unused:UNUSED_PAD src0_sel:WORD_1\n\t" \
  "v_mfma_f32_16x16x32_bf16 v[160:163], v[220:223], v[124:127], v[160:163]\n\t" \
  "ds_read_u16_d16_hi v198, v198 offset:0\n\t" \
  "v_lshlrev_b32_sdwa v205, 1, v205 dst_sel:DWORD dst_unused:UNUSED_PAD src0_sel:DWORD src1_sel:WORD_1\n\t" \
  "v_mfma_f32_16x16x32_bf16 v[168:171], v[220:223], v[136:139], v[168:171]\n\t" \
  "ds_read_b64_tr_b16 v[220:221], v249 offset:10752\n\t" \
  "v_fma_mix_f32 v188, v188, v225, 0 op_sel_hi:[0,1,0]\n\t" \
  "v_mfma_f32_16x16x32_bf16 v[144:147], v[208:211], v[104:107], v[144:147]\n\t" \
  "s_waitcnt lgkmcnt(13)\n\t" \
  "ds_read_u16_d16_hi v199, v199 offset:0\n\t" \
  "v_fma_mix_f32 v190, v190, v227, 0 op_sel_hi:[0,1,0]\n\t" \
  "v_mfma_f32_16x16x16_bf16 v[36:39], v[246:247], v[236:237], 0\n\t" \
  "ds_read_b64_tr_b16 v[222:223], v249 offset:11008\n\t" \
  "v_lshlrev_b32_sdwa v206, 1, v206 dst_sel:DWORD dst_unused:UNUSED_PAD src0_sel:DWORD src1_sel:WORD_1\n\t" \
  "v_mfma_f32_16x16x32_bf16 v[152:155], v[208:211], v[116:119], v[152:155]\n\t" \
  "ds_read_u16_d16_hi v204, v204 offset:0\n\t" \
  "v_fma_mix_f32 v189, v189, v224, 0 op_sel_hi:[0,1,0]\n\t" \
  "v_mfma_f32_16x16x32_bf16 v[160:163], v[208:211], v[128:131], v[160:163]\n\t" \
  "ds_read_u16_d16_hi v205, v205 offset:0\n\t" \
  "v_fma_mix_f32 v191, v191, v226, 0 op_sel_hi:[0,1,0]\n\t" \
  "v_mfma_f32_16x16x32_bf16 v[168:171], v[208:211], v[140:143], v[168:171]\n\t" \
  "s_waitcnt lgkmcnt(12)\n\t" \
  "ds_read_b64_tr_b16 v[208:209], v250 offset:18480\n\t" \
  "v_lshlrev_b32_sdwa v207, 1, v207 dst_sel:DWORD dst_unused:UNUSED_PAD src0_sel:DWORD src1_sel:WORD_1\n\t" \
  "v_mfma_f32_16x16x32_bf16 v[148:151], v[212:215], v[96:99], 0\n\t" \
  "ds_read_u16_d16_hi v206, v206 offset:0\n\t" \
  "v_lshlrev_b32_sdwa v144, 1, v144 dst_sel:DWORD dst_unused:UNUSED_PAD src0_sel:DWORD src1_sel:WORD_1\n\t" \
  "v_mfma_f32_16x16x32_bf16 v[156:159], v[212:215], v[108:111], 0\n\t" \
  "ds_read_b64_tr_b16 v[210:211], v250 offset:19248\n\t" \
  "v_cvt_pk_f16_f32 v224, v152, v153\n\t" \
  "v_mfma_f32_16x16x16_bf16 v[40:43], v[246:247], v[242:243], 0\n\t" \
  "ds_read_u16_d16_hi v207, v207 offset:0\n\t" \
  "v_cvt_pk_f16_f32 v226, v154, v155\n\t" \
  "v_mfma_f32_16x16x32_bf16 v[164:167], v[212:215], v[120:123], 0\n\t" \
  "ds_read_u16_d16_hi v144, v144 offset:0\n\t" \
  "v_pk_fma_f16 v225, v224, v224, %[h1] neg_lo:[1,0,0] neg_hi:[1,0,0]\n\t" \
  "v_mfma_f32_16x16x32_bf16 v[172:175], v[212:215], v[132:135], 0\n\t" \
  "s_waitcnt lgkmcnt(12)\n\t" \
  "ds_read_b64_tr_b16 v[212:213], v250 offset:18488\n\t" \
  "v_lshlrev_b32_sdwa v145, 1, v145 dst_sel:DWORD dst_unused:UNUSED_PAD src0_sel:DWORD src1_sel:WORD_1\n\t" \
  "v_pk_fma_f16 v227, v226, v226, %[h1] neg_lo:[1,0,0] neg_hi:[1,0,0]\n\t" \
  "v_mfma_f32_16x16x32_bf16 v[148:151], v[216:219], v[100:103], v[148:151]\n\t" \
  "ds_read_u16_d16_hi v145, v145 offset:0\n\t" \
  "v_pk_mul_f16 v225, v224, v225\n\t" \
  "v_mfma_f32_16x16x16_bf16 v[44:47], v[246:247], v[244:245], 0\n\t" \
  "ds_read_b64_tr_b16 v[214:215], v250 offset:19256\n\t" \
  "v_lshlrev_b32_sdwa v146, 1, v146 dst_sel:DWORD dst_unused:UNUSED_PAD src0_sel:DWORD src1_sel:WORD_1\n\t" \
  "v_mfma_f32_16x16x32_bf16 v[156:159], v[216:219], v[112:115], v[156:159]\n\t" \
  "ds_read_u16_d16_hi v146, v146 offset:0\n\t" \
  "v_pk_mul_f16 v227, v226, v227\n\t" \
  "v_exp_f16 v224, v225\n\t" \
  "v_mfma_f32_16x16x32_bf16 v[164:167], v[216:219], v[124:127], v[164:167]\n\t" \
  "v_lshlrev_b32_sdwa v147, 1, v147 dst_sel:DWORD dst_unused:UNUSED_PAD src0_sel:DWORD src1_sel:WORD_1\n\t" \
  "v_exp_f16 v226, v227\n\t" \
  "v_mfma_f32_16x16x32_bf16 v[172:175], v[216:219], v[136:139], v[172:175]\n\t" \
  "s_waitcnt lgkmcnt(11)\n\t" \
  "ds_read_b64_tr_b16 v[216:217], v250 offset:18544\n\t" \
  "v_lshlrev_b32_sdwa v160, 1, v160 dst_sel:DWORD dst_unused:UNUSED_PAD src0_sel:DWORD src1_sel:WORD_1\n\t" \
  "v_mfma_f32_16x16x32_bf16 v[148:151], v[220:223], v[104:107], v[148:151]\n\t" \
  "ds_read_u16_d16_hi v147, v147 offset:0\n\t" \
  "v_exp_f16_sdwa v224, v225 dst_sel:WORD_1 dst_unused:UNUSED_PRESERVE src0_sel:WORD_1\n\t" \
  "v_mfma_f32_16x16x32_bf16 v[156:159], v[220:223], v[116:119], v[156:159]\n\t" \
  "ds_read_b64_tr_b16 v[218:219], v250 offset:19312\n\t" \
  "v_exp_f16_sdwa v226, v227 dst_sel:WORD_1 dst_unused:UNUSED_PRESERVE src0_sel:WORD_1\n\t" \
  "v_mfma_f32_16x16x32_bf16 v[164:167], v[220:223], v[128:131], v[164:167]\n\t" \
  "ds_read_u16_d16_hi v160, v160 offset:0\n\t" \
  "v_lshlrev_b32_sdwa v161, 1, v161 dst_sel:DWORD dst_unused:UNUSED_PAD src0_sel:DWORD src1_sel:WORD_1\n\t" \
  "v_mfma_f32_16x16x32_bf16 v[172:175], v[220:223], v[140:143], v[172:175]\n\t" \
  "s_waitcnt lgkmcnt(10)\n\t" \
  "ds_read_b64_tr_b16 v[220:221], v250 offset:18552\n\t" \
  "v_pk_fma_f16 v224, v224, %[hc], %[hc]\n\t" \
  "v_smfmac_f32_16x16x64_bf16 v[0:3], v[208:211], v[176:183], v248\n\t" \
  "ds_read_u16_d16_hi v161, v161 offset:0\n\t" \
  "v_lshlrev_b32_sdwa v162, 1, v162 dst_sel:DWORD dst_unused:UNUSED_PAD src0_sel:DWORD src1_sel:WORD_1\n\t" \
  "v_smfmac_f32_16x16x64_bf16 v[24:27], v[208:211], v[184:191], v248\n\t" \
  "ds_read_b64_tr_b16 v[222:223], v250 offset:19320\n\t" \
  "v_pk_fma_f16 v226, v226, %[hc], %[hc]\n\t" \
  "v_smfmac_f32_16x16x64_bf16 v[48:51], v[208:211], v[192:199], v248\n\t" \
  "s_waitcnt lgkmcnt(12)\n\t" \
  "ds_read_u16_d16_hi v162, v162 offset:0\n\t" \
  "v_rcp_f16 v225, v224\n\t" \
  "v_smfmac_f32_16x16x64_bf16 v[72:75], v[208:211], v[200:207], v248\n\t" \
  "s_waitcnt lgkmcnt(9)\n\t" \
  "ds_read_b64_tr_b16 v[208:209], v250 offset:18608\n\t" \
  "v_lshlrev_b32_sdwa v163, 1, v163 dst_sel:DWORD dst_unused:UNUSED_PAD src0_sel:DWORD src1_sel:WORD_1\n\t" \
  "v_rcp_f16 v227, v226\n\t" \
  "v_smfmac_f32_16x16x64_bf16 v[4:7], v[212:215], v[176:183], v248\n\t" \
  "ds_read_u16_d16_hi v163, v163 offset:0\n\t" \
  "v_lshlrev_b32_sdwa v168, 1, v168 dst_sel:DWORD dst_unused:UNUSED_PAD src0_sel:DWORD src1_sel:WORD_1\n\t" \
  "v_smfmac_f32_16x16x64_bf16 v[28:31], v[212:215], v[184:191], v248\n\t" \
  "s_add_u32 %[ob], %[ob], %[dob]\n\t" \
  "ds_read_b64_tr_b16 v[210:211], v250 offset:19376\n\t" \
  "v_rcp_f16_sdwa v224, v224 dst_sel:DWORD dst_unused:UNUSED_PAD src0_sel:WORD_1\n\t" \
  "v_smfmac_f32_16x16x64_bf16 v[52:55], v[212:215], v[192:199], v248\n\t" \
  "ds_read_u16_d16_hi v168, v168 offset:0\n\t" \
  "v_lshlrev_b32_sdwa v169, 1, v169 dst_sel:DWORD dst_unused:UNUSED_PAD src0_sel:DWORD src1_sel:WORD_1\n\t" \
  "v_smfmac_f32_16x16x64_bf16 v[76:79], v[212:215], v[200:207], v248\n\t" \
  "s_waitcnt lgkmcnt(9)\n\t" \
  "ds_read_b64_tr_b16 v[212:213], v250 offset:18616\n\t" \
  "v_rcp_f16_sdwa v226, v226 dst_sel:DWORD dst_unused:UNUSED_PAD src0_sel:WORD_1\n\t" \
  "v_smfmac_f32_16x16x64_bf16 v[8:11], v[216:219], v[176:183], v248\n\t" \
  "ds_read_u16_d16_hi v169, v169 offset:0\n\t" \
  "v_fma_mix_f32 v152, v152, v225, 0 op_sel_hi:[0,1,0]\n\t" \
  "v_smfmac_f32_16x16x64_bf16 v[32:35], v[216:219], v[184:191], v248\n\t" \
  "ds_read_b64_tr_b16 v[214:215], v250 offset:19384\n\t" \
  "v_lshlrev_b32_sdwa v170, 1, v170 dst_sel:DWORD dst_unused:UNUSED_PAD src0_sel:DWORD src1_sel:WORD_1\n\t" \
  "v_fma_mix_f32 v154, v154, v227, 0 op_sel_hi:[0,1,0]\n\t" \
  "v_smfmac_f32_16x16x64_bf16 v[56:59], v[216:219], v[192:199], v248\n\t" \
  "ds_read_u16_d16_hi v170, v170 offset:0\n\t" \
  "v_fma_mix_f32 v153, v153, v224, 0 op_sel_hi:[0,1,0]\n\t" \
  "v_smfmac_f32_16x16x64_bf16 v[80:83], v[216:219], v[200:207], v248\n\t" \
  "s_waitcnt lgkmcnt(9)\n\t" \
  "s_barrier\n\t" \
  "ds_read_b64_tr_b16 v[216:217], v249 offset:12288\n\t" \
  "v_lshlrev_b32_sdwa v171, 1, v171 dst_sel:DWORD dst_unused:UNUSED_PAD src0_sel:DWORD src1_sel:WORD_1\n\t" \
  "v_fma_mix_f32 v155, v155, v226, 0 op_sel_hi:[0,1,0]\n\t" \
  "v_smfmac_f32_16x16x64_bf16 v[12:15], v[220:223], v[176:183], v248\n\t" \
  "ds_read_u16_d16_hi v171, v171 offset:0\n\t" \
  "v_cvt_pk_f16_f32 v224, v156, v157\n\t" \
  "v_cvt_pk_f16_f32 v226, v158, v159\n\t" \
  "v_smfmac_f32_16x16x64_bf16 v[36:39], v[220:223], v[184:191], v248\n\t" \
  "ds_read_b64_tr_b16 v[218:219], v249 offset:12544\n\t" \
  "v_lshlrev_b32_sdwa v148, 1, v148 dst_sel:DWORD dst_unused:UNUSED_PAD src0_sel:DWORD src1_sel:WORD_1\n\t" \
  "v_pk_fma_f16 v225, v224, v224, %[h1] neg_lo:[1,0,0] neg_hi:[1,0,0]\n\t" \
  "v_smfmac_f32_16x16x64_bf16 v[60:63], v[220:223], v[192:199], v248\n\t" \
  "ds_read_u16_d16_hi v148, v148 offset:0\n\t" \
  "v_pk_fma_f16 v227, v226, v226, %[h1] neg_lo:[1,0,0] neg_hi:[1,0,0]\n\t" \
  "v_smfmac_f32_16x16x64_bf16 v[84:87], v[220:223], v[200:207], v248\n\t" \
  "s_waitcnt lgkmcnt(9)\n\t" \
  "ds_read_b64_tr_b16 v[220:221], v249 offset:14336\n\t" \
  "v_lshlrev_b32_sdwa v149, 1, v149 dst_sel:DWORD dst_unused:UNUSED_PAD src0_sel:DWORD src1_sel:WORD_1\n\t" \
  "v_pk_mul_f16 v225, v224, v225\n\t" \
  "v_smfmac_f32_16x16x64_bf16 v[16:19], v[208:211], v[176:183], v248\n\t" \
  "s_add_u32 %[rs], %[rs], 1\n\t" \
  "ds_read_u16_d16_hi v149, v149 offset:0\n\t" \
  "v_pk_mul_f16 v227, v226, v227\n\t" \
  "v_exp_f16 v224, v225\n\t" \
  "v_smfmac_f32_16x16x64_bf16 v[40:43], v[208:211], v[184:191], v248\n\t" \
  "s_cmp_eq_u32 %[rs], 6\n\t" \
  "ds_read_b64_tr_b16 v[222:223], v249 offset:14592\n\t" \
  "v_lshlrev_b32_sdwa v150, 1, v150 dst_sel:DWORD dst_unused:UNUSED_PAD src0_sel:DWORD src1_sel:WORD_1\n\t" \
  "v_smfmac_f32_16x16x64_bf16 v[64:67], v[208:211], v[192:199], v248\n\t" \
  "ds_read_u16_d16_hi v150, v150 offset:0\n\t" \
  "v_exp_f16 v226, v227\n\t" \
  "v_smfmac_f32_16x16x64_bf16 v[88:91], v[208:211], v[200:207], v248\n\t" \
  "s_waitcnt lgkmcnt(9)\n\t" \
  "ds_read_b64_tr_b16 v[208:209], v249 offset:16384\n\t" \
  "v_lshlrev_b32_sdwa v151, 1, v151 dst_sel:DWORD dst_unused:UNUSED_PAD src0_sel:DWORD src1_sel:WORD_1\n\t" \
  "v_smfmac_f32_16x16x64_bf16 v[20:23], v[212:215], v[176:183], v248\n\t" \
  "s_cselect_b32 %[rs], 0, %[rs]\n\t" \
  "ds_read_u16_d16_hi v151, v151 offset:0\n\t" \
  "v_exp_f16_sdwa v224, v225 dst_sel:WORD_1 dst_unused:UNUSED_PRESERVE src0_sel:WORD_1\n\t" \
  "v_smfmac_f32_16x16x64_bf16 v[44:47], v[212:215], v[184:191], v248\n\t" \
  "s_cselect_b32 %[t0], -75920, %[ps]\n\t" \
  "ds_read_b64_tr_b16 v[210:211], v249 offset:16640\n\t" \
  "v_lshlrev_b32_sdwa v164, 1, v164 dst_sel:DWORD dst_unused:UNUSED_PAD src0_sel:DWORD src1_sel:WORD_1\n\t" \
  "v_exp_f16_sdwa v226, v227 dst_sel:WORD_1 dst_unused:UNUSED_PRESERVE src0_sel:WORD_1\n\t" \
  "v_smfmac_f32_16x16x64_bf16 v[68:71], v[212:215], v[192:199], v248\n\t" \
  "ds_read_u16_d16_hi v164, v164 offset:0\n\t" \
  "v_add_u32 v252, %[t0], v252\n\t" \
  "v_smfmac_f32_16x16x64_bf16 v[92:95], v[212:215], v[200:207], v248\n\t" \
  "s_waitcnt lgkmcnt(9)\n\t" \
  "ds_read_b64_tr_b16 v[212:213], v249 offset:12800\n\t" \
  "v_lshlrev_b32_sdwa v165, 1, v165 dst_sel:DWORD dst_unused:UNUSED_PAD src0_sel:DWORD src1_sel:WORD_1\n\t" \
  "v_pk_fma_f16 v224, v224, %[hc], %[hc]\n\t" \
  "v_mfma_f32_16x16x32_bf16 v[176:179], v[216:219], v[96:99], 0\n\t" \
  "ds_read_u16_d16_hi v165, v165 offset:0\n\t" \
  "v_pk_fma_f16 v226, v226, %[hc], %[hc]\n\t" \
  "v_mfma_f32_16x16x32_bf16 v[184:187], v[216:219], v[108:111], 0\n\t" \
  "ds_read_b64_tr_b16 v[214:215], v249 offset:13056\n\t" \
  "v_lshlrev_b32_sdwa v166, 1, v166 dst_sel:DWORD dst_unused:UNUSED_PAD src0_sel:DWORD src1_sel:WORD_1\n\t" \
  "v_rcp_f16 v225, v224\n\t" \
  "v_mfma_f32_16x16x32_bf16 v[192:195], v[216:219], v[120:123], 0\n\t" \
  "ds_read_u16_d16_hi v166, v166 offset:0\n\t" \
  "v_rcp_f16 v227, v226\n\t" \
  "v_mfma_f32_16x16x32_bf16 v[200:203], v[216:219], v[132:135], 0\n\t" \
  "s_waitcnt lgkmcnt(9)\n\t" \
  "ds_read_b64_tr_b16 v[216:217], v249 offset:14848\n\t" \
  "v_lshlrev_b32_sdwa v167, 1, v167 dst_sel:DWORD dst_unused:UNUSED_PAD src0_sel:DWORD src1_sel:WORD_1\n\t" \
  "v_mfma_f32_16x16x32_bf16 v[176:179], v[220:223], v[100:103], v[176:179]\n\t" \
  "ds_read_u16_d16_hi v167, v167 offset:0\n\t" \
  "v_rcp_f16_sdwa v224, v224 dst_sel:DWORD dst_unused:UNUSED_PAD src0_sel:WORD_1\n\t" \
  "v_mfma_f32_16x16x32_bf16 v[184:187], v[220:223], v[112:115], v[184:187]\n\t" \
  "ds_read_b64_tr_b16 v[218:219], v249 offset:15104\n\t" \
  "v_lshlrev_b32_sdwa v172, 1, v172 dst_sel:DWORD dst_unused:UNUSED_PAD src0_sel:DWORD src1_sel:WORD_1\n\t" \
  "v_mfma_f32_16x16x32_bf16 v[192:195], v[220:223], v[124:127], v[192:195]\n\t" \
  "ds_read_u16_d16_hi v172, v172 offset:0\n\t" \
  "v_rcp_f16_sdwa v226, v226 dst_sel:DWORD dst_unused:UNUSED_PAD src0_sel:WORD_1\n\t" \
  "v_mfma_f32_16x16x32_bf16 v[200:203], v[220:223], v[136:139], v[200:203]\n\t" \
  "s_waitcnt lgkmcnt(9)\n\t" \
  "ds_read_b64_tr_b16 v[220:221], v249 offset:16896\n\t" \
  "v_lshlrev_b32_sdwa v173, 1, v173 dst_sel:DWORD dst_unused:UNUSED_PAD src0_sel:DWORD src1_sel:WORD_1\n\t" \
  "v_fma_mix_f32 v156, v156, v225, 0 op_sel_hi:[0,1,0]\n\t" \
  "v_mfma_f32_16x16x32_bf16 v[176:179], v[208:211], v[104:107], v[176:179]\n\t" \
  "ds_read_u16_d16_hi v173, v173 offset:0\n\t" \
  "v_fma_mix_f32 v158, v158, v227, 0 op_sel_hi:[0,1,0]\n\t" \
  "v_mfma_f32_16x16x32_bf16 v[184:187], v[208:211], v[116:119], v[184:187]\n\t" \
  "ds_read_b64_tr_b16 v[222:223], v249 offset:17152\n\t" \
  "v_lshlrev_b32_sdwa v174, 1, v174 dst_sel:DWORD dst_unused:UNUSED_PAD src0_sel:DWORD src1_sel:WORD_1\n\t" \
  "v_fma_mix_f32 v157, v157, v224, 0 op_sel_hi:[0,1,0]\n\t" \
  "v_mfma_f32_16x16x32_bf16 v[192:195], v[208:211], v[128:131], v[192:195]\n\t" \
  "ds_read_u16_d16_hi v174, v174 offset:0\n\t" \
  "v_fma_mix_f32 v159, v159, v226, 0 op_sel_hi:[0,1,0]\n\t" \
  "v_mfma_f32_16x16x32_bf16 v[200:203], v[208:211], v[140:143], v[200:203]\n\t" \
  "s_waitcnt lgkmcnt(9)\n\t" \
  "ds_read_b64_tr_b16 v[208:209], v250 offset:0\n\t" \
  "v_lshlrev_b32_sdwa v175, 1, v175 dst_sel:DWORD dst_unused:UNUSED_PAD src0_sel:DWORD src1_sel:WORD_1\n\t" \
  "v_mfma_f32_16x16x32_bf16 v[180:183], v[212:215], v[96:99], 0\n\t" \
  "ds_read_u16_d16_hi v175, v175 offset:0\n\t" \
  "v_lshlrev_b32_sdwa v176, 1, v176 dst_sel:DWORD dst_unused:UNUSED_PAD src0_sel:DWORD src1_sel:WORD_1\n\t" \
  "v_mfma_f32_16x16x32_bf16 v[188:191], v[212:215], v[108:111], 0\n\t" \
  "ds_read_b64_tr_b16 v[210:211], v250 offset:768\n\t" \
  "v_cvt_pk_f16_f32 v224, v184, v185\n\t" \
  "v_cvt_pk_f16_f32 v226, v186, v187\n\t" \
  "v_mfma_f32_16x16x32_bf16 v[196:199], v[212:215], v[120:123], 0\n\t" \
  "ds_read_u16_d16_hi v176, v176 offset:0\n\t" \
  "v_pk_fma_f16 v225, v224, v224, %[h1] neg_lo:[1,0,0] neg_hi:[1,0,0]\n\t" \
  "v_mfma_f32_16x16x32_bf16 v[204:207], v[212:215], v[132:135], 0\n\t" \
  "s_waitcnt lgkmcnt(9)\n\t" \
  "ds_read_b64_tr_b16 v[212:213], v250 offset:8\n\t" \
  "v_lshlrev_b32_sdwa v177, 1, v177 dst_sel:DWORD dst_unused:UNUSED_PAD src0_sel:DWORD src1_sel:WORD_1\n\t" \
  "v_pk_fma_f16 v227, v226, v226, %[h1] neg_lo:[1,0,0] neg_hi:[1,0,0]\n\t" \
  "v_mfma_f32_16x16x32_bf16 v[180:183], v[216:219], v[100:103], v[180:183]\n\t" \
  "ds_read_u16_d16_hi v177, v177 offset:0\n\t" \
  "v_pk_mul_f16 v225, v224, v225\n\t" \
  "v_mfma_f32_16x16x32_bf16 v[188:191], v[216:219], v[112:115], v[188:191]\n\t" \
  "ds_read_b64_tr_b16 v[214:215], v250 offset:776\n\t" \
  "v_lshlrev_b32_sdwa v178, 1, v178 dst_sel:DWORD dst_unused:UNUSED_PAD src0_sel:DWORD src1_sel:WORD_1\n\t" \
  "v_pk_mul_f16 v227, v226, v227\n\t" \
  "v_exp_f16 v224, v225\n\t" \
  "v_mfma_f32_16x16x32_bf16 v[196:199], v[216:219], v[124:127], v[196:199]\n\t" \
  "ds_read_u16_d16_hi v178, v178 offset:0\n\t" \
  "v_lshlrev_b32_sdwa v179, 1, v179 dst_sel:DWORD dst_unused:UNUSED_PAD src0_sel:DWORD src1_sel:WORD_1\n\t" \
  "v_mfma_f32_16x16x32_bf16 v[204:207], v[216:219], v[136:139], v[204:207]\n\t" \
  "s_waitcnt lgkmcnt(9)\n\t" \
  "ds_read_b64_tr_b16 v[216:217], v250 offset:64\n\t" \
  "v_exp_f16 v226, v227\n\t" \
  "v_mfma_f32_16x16x32_bf16 v[180:183], v[220:223], v[104:107], v[180:183]\n\t" \
  "ds_read_u16_d16_hi v179, v179 offset:0\n\t" \
  "v_lshlrev_b32_sdwa v192, 1, v192 dst_sel:DWORD dst_unused:UNUSED_PAD src0_sel:DWORD src1_sel:WORD_1\n\t" \
  "v_mfma_f32_16x16x32_bf16 v[188:191], v[220:223], v[116:119], v[188:191]\n\t" \
  "ds_read_b64_tr_b16 v[218:219], v250 offset:832\n\t" \
  "v_exp_f16_sdwa v224, v225 dst_sel:WORD_1 dst_unused:UNUSED_PRESERVE src0_sel:WORD_1\n\t" \
  "v_mfma_f32_16x16x32_bf16 v[196:199], v[220:223], v[128:131], v[196:199]\n\t" \
  "ds_read_u16_d16_hi v192, v192 offset:0\n\t" \
  "v_exp_f16_sdwa v226, v227 dst_sel:WORD_1 dst_unused:UNUSED_PRESERVE src0_sel:WORD_1\n\t" \
  "v_mfma_f32_16x16x32_bf16 v[204:207], v[220:223], v[140:143], v[204:207]\n\t" \
  "s_waitcnt lgkmcnt(9)\n\t" \
  "ds_read_b64_tr_b16 v[220:221], v250 offset:72\n\t" \
  "v_lshlrev_b32_sdwa v193, 1, v193 dst_sel:DWORD dst_unused:UNUSED_PAD src0_sel:DWORD src1_sel:WORD_1\n\t" \
  "v_smfmac_f32_16x16x64_bf16 v[0:3], v[208:211], v[144:151], v248\n\t" \
  "ds_read_u16_d16_hi v193, v193 offset:0\n\t" \
  "v_pk_fma_f16 v224, v224, %[hc], %[hc]\n\t" \
  "v_smfmac_f32_16x16x64_bf16 v[24:27], v[208:211], v[152:159], v248\n\t" \
  "ds_read_b64_tr_b16 v[222:223], v250 offset:840\n\t" \
  "v_lshlrev_b32_sdwa v194, 1, v194 dst_sel:DWORD dst_unused:UNUSED_PAD src0_sel:DWORD src1_sel:WORD_1\n\t" \
  "v_pk_fma_f16 v226, v226, %[hc], %[hc]\n\t" \
  "v_smfmac_f32_16x16x64_bf16 v[48:51], v[208:211], v[160:167], v248\n\t" \
  "ds_read_u16_d16_hi v194, v194 offset:0\n\t" \
  "v_rcp_f16 v225, v224\n\t" \
  "v_smfmac_f32_16x16x64_bf16 v[72:75], v[208:211], v[168:175], v248\n\t" \
  "s_waitcnt lgkmcnt(9)\n\t" \
  "ds_read_b64_tr_b16 v[208:209], v250 offset:128\n\t" \
  "v_lshlrev_b32_sdwa v195, 1, v195 dst_sel:DWORD dst_unused:UNUSED_PAD src0_sel:DWORD src1_sel:WORD_1\n\t" \
  "v_rcp_f16 v227, v226\n\t" \
  "v_smfmac_f32_16x16x64_bf16 v[4:7], v[212:215], v[144:151], v248\n\t" \
  "ds_read_u16_d16_hi v195, v195 offset:0\n\t" \
  "v_lshlrev_b32_sdwa v200, 1, v200 dst_sel:DWORD dst_unused:UNUSED_PAD src0_sel:DWORD src1_sel:WORD_1\n\t" \
  "v_smfmac_f32_16x16x64_bf16 v[28:31], v[212:215], v[152:159], v248\n\t" \
  "ds_read_b64_tr_b16 v[210:211], v250 offset:896\n\t" \
  "v_rcp_f16_sdwa v224, v224 dst_sel:DWORD dst_unused:UNUSED_PAD src0_sel:WORD_1\n\t" \
  "v_smfmac_f32_16x16x64_bf16 v[52:55], v[212:215], v[160:167], v248\n\t" \
  "ds_read_u16_d16_hi v200, v200 offset:0\n\t" \
  "v_lshlrev_b32_sdwa v201, 1, v201 dst_sel:DWORD dst_unused:UNUSED_PAD src0_sel:DWORD src1_sel:WORD_1\n\t" \
  "v_smfmac_f32_16x16x64_bf16 v[76:79], v[212:215], v[168:175], v248\n\t" \
  "s_waitcnt lgkmcnt(9)\n\t" \
  "ds_read_b64_tr_b16 v[212:213], v250 offset:136\n\t" \
  "v_rcp_f16_sdwa v226, v226 dst_sel:DWORD dst_unused:UNUSED_PAD src0_sel:WORD_1\n\t" \
  "v_smfmac_f32_16x16x64_bf16 v[8:11], v[216:219], v[144:151], v248\n\t" \
  "ds_read_u16_d16_hi v201, v201 offset:0\n\t" \
  "v_fma_mix_f32 v184, v184, v225, 0 op_sel_hi:[0,1,0]\n\t" \
  "v_smfmac_f32_16x16x64_bf16 v[32:35], v[216:219], v[152:159], v248\n\t" \
  "ds_read_b64_tr_b16 v[214:215], v250 offset:904\n\t" \
  "v_lshlrev_b32_sdwa v202, 1, v202 dst_sel:DWORD dst_unused:UNUSED_PAD src0_sel:DWORD src1_sel:WORD_1\n\t" \
  "v_fma_mix_f32 v186, v186, v227, 0 op_sel_hi:[0,1,0]\n\t" \
  "v_smfmac_f32_16x16x64_bf16 v[56:59], v[216:219], v[160:167], v248\n\t" \
  "ds_read_u16_d16_hi v202, v202 offset:0\n\t" \
  "v_fma_mix_f32 v185, v185, v224, 0 op_sel_hi:[0,1,0]\n\t" \
  "v_smfmac_f32_16x16x64_bf16 v[80:83], v[216:219], v[168:175], v248\n\t" \
  "s_waitcnt lgkmcnt(9)\n\t" \
  "s_barrier\n\t" \
  "ds_read_b64_tr_b16 v[216:217], v249 offset:0\n\t" \
  "v_lshlrev_b32_sdwa v203, 1, v203 dst_sel:DWORD dst_unused:UNUSED_PAD src0_sel:DWORD src1_sel:WORD_1\n\t" \
  "v_fma_mix_f32 v187, v187, v226, 0 op_sel_hi:[0,1,0]\n\t" \
  "v_smfmac_f32_16x16x64_bf16 v[12:15], v[220:223], v[144:151], v248\n\t" \
  "ds_read_b128 v[120:123], v251 offset:0\n\t" \
  "v_cvt_pk_f16_f32 v224, v188, v189\n\t" \
  "v_cvt_pk_f16_f32 v226, v190, v191\n\t" \
  "v_smfmac_f32_16x16x64_bf16 v[36:39], v[220:223], v[152:159], v248\n\t" \
  "ds_read_b64_tr_b16 v[218:219], v249 offset:256\n\t" \
  "v_lshlrev_b32_sdwa v180, 1, v180 dst_sel:DWORD dst_unused:UNUSED_PAD src0_sel:DWORD src1_sel:WORD_1\n\t" \
  "v_pk_fma_f16 v225, v224, v224, %[h1] neg_lo:[1,0,0] neg_hi:[1,0,0]\n\t" \
  "v_smfmac_f32_16x16x64_bf16 v[60:63], v[220:223], v[160:167], v248\n\t" \
  "ds_read_b128 v[124:127], v251 offset:4096\n\t" \
  "v_pk_fma_f16 v227, v226, v226, %[h1] neg_lo:[1,0,0] neg_hi:[1,0,0]\n\t" \
  "v_smfmac_f32_16x16x64_bf16 v[84:87], v[220:223], v[168:175], v248\n\t" \
  "s_waitcnt lgkmcnt(9)\n\t" \
  "ds_read_b64_tr_b16 v[220:221], v249 offset:2048\n\t" \
  "v_lshlrev_b32_sdwa v181, 1, v181 dst_sel:DWORD dst_unused:UNUSED_PAD src0_sel:DWORD src1_sel:WORD_1\n\t" \
  "v_pk_mul_f16 v225, v224, v225\n\t" \
  "v_smfmac_f32_16x16x64_bf16 v[16:19], v[208:211], v[144:151], v248\n\t" \
  "ds_read_b128 v[128:131], v251 offset:8192\n\t" \
  "v_pk_mul_f16 v227, v226, v227\n\t" \
  "v_exp_f16 v224, v225\n\t" \
  "v_smfmac_f32_16x16x64_bf16 v[40:43], v[208:211], v[152:159], v248\n\t" \
  "ds_read_b64_tr_b16 v[222:223], v249 offset:2304\n\t" \
  "v_lshlrev_b32_sdwa v182, 1, v182 dst_sel:DWORD dst_unused:UNUSED_PAD src0_sel:DWORD src1_sel:WORD_1\n\t" \
  "v_smfmac_f32_16x16x64_bf16 v[64:67], v[208:211], v[160:167], v248\n\t" \
  "ds_read_u16_d16_hi v203, v203 offset:0\n\t" \
  "v_exp_f16 v226, v227\n\t" \
  "v_smfmac_f32_16x16x64_bf16 v[88:91], v[208:211], v[168:175], v248\n\t" \
  "s_waitcnt lgkmcnt(9)\n\t" \
  "ds_read_b64_tr_b16 v[208:209], v249 offset:4096\n\t" \
  "v_lshlrev_b32_sdwa v183, 1, v183 dst_sel:DWORD dst_unused:UNUSED_PAD src0_sel:DWORD src1_sel:WORD_1\n\t" \
  "v_smfmac_f32_16x16x64_bf16 v[20:23], v[212:215], v[144:151], v248\n\t" \
  "ds_read_u16_d16_hi v180, v180 offset:0\n\t" \
  "v_exp_f16_sdwa v224, v225 dst_sel:WORD_1 dst_unused:UNUSED_PRESERVE src0_sel:WORD_1\n\t" \
  "v_smfmac_f32_16x16x64_bf16 v[44:47], v[212:215], v[152:159], v248\n\t" \
  "ds_read_b64_tr_b16 v[210:211], v249 offset:4352\n\t" \
  "v_lshlrev_b32_sdwa v196, 1, v196 dst_sel:DWORD dst_unused:UNUSED_PAD src0_sel:DWORD src1_sel:WORD_1\n\t" \
  "v_exp_f16_sdwa v226, v227 dst_sel:WORD_1 dst_unused:UNUSED_PRESERVE src0_sel:WORD_1\n\t" \
  "v_smfmac_f32_16x16x64_bf16 v[68:71], v[212:215], v[160:167], v248\n\t" \
  "ds_read_u16_d16_hi v181, v181 offset:0\n\t" \
  "v_lshlrev_b32_sdwa v197, 1, v197 dst_sel:DWORD dst_unused:UNUSED_PAD src0_sel:DWORD src1_sel:WORD_1\n\t" \
  "v_smfmac_f32_16x16x64_bf16 v[92:95], v[212:215], v[168:175], v248\n\t" \
  "s_waitcnt lgkmcnt(9)\n\t" \
  "ds_read_b64_tr_b16 v[212:213], v249 offset:512\n\t" \
  "v_pk_fma_f16 v224, v224, %[hc], %[hc]\n\t" \
  "v_mfma_f32_16x16x32_bf16 v[144:147], v[216:219], v[96:99], 0\n\t" \
  "ds_read_u16_d16_hi v182, v182 offset:0\n\t" \
  "v_pk_fma_f16 v226, v226, %[hc], %[hc]\n\t" \
  "v_mfma_f32_16x16x32_bf16 v[152:155], v[216:219], v[108:111], 0\n\t" \
  "ds_read_b64_tr_b16 v[214:215], v249 offset:768\n\t" \
  "v_lshlrev_b32_sdwa v198, 1, v198 dst_sel:DWORD dst_unused:UNUSED_PAD src0_sel:DWORD src1_sel:WORD_1\n\t" \
  "v_rcp_f16 v225, v224\n\t" \
  "v_mfma_f32_16x16x32_bf16 v[160:163], v[216:219], v[120:123], 0\n\t" \
  "ds_read_u16_d16_hi v183, v183 offset:0\n\t" \
  "v_rcp_f16 v227, v226\n\t" \
  "v_mfma_f32_16x16x32_bf16 v[168:171], v[216:219], v[132:135], 0\n\t" \
  "s_waitcnt lgkmcnt(9)\n\t" \
  "ds_read_b64_tr_b16 v[216:217], v249 offset:2560\n\t" \
  "v_lshlrev_b32_sdwa v199, 1, v199 dst_sel:DWORD dst_unused:UNUSED_PAD src0_sel:DWORD src1_sel:WORD_1\n\t" \
  "v_mfma_f32_16x16x32_bf16 v[144:147], v[220:223], v[100:103], v[144:147]\n\t" \
  "ds_read_u16_d16_hi v196, v196 offset:0\n\t" \
  "v_rcp_f16_sdwa v224, v224 dst_sel:DWORD dst_unused:UNUSED_PAD src0_sel:WORD_1\n\t" \
  "v_mfma_f32_16x16x32_bf16 v[152:155], v[220:223], v[112:115], v[152:155]\n\t" \
  "ds_read_b64_tr_b16 v[218:219], v249 offset:2816\n\t" \
  "v_lshlrev_b32_sdwa v204, 1, v204 dst_sel:DWORD dst_unused:UNUSED_PAD src0_sel:DWORD src1_sel:WORD_1\n\t" \
  "v_mfma_f32_16x16x32_bf16 v[160:163], v[220:223], v[124:127], v[160:163]\n\t" \
  "ds_read_u16_d16_hi v197, v197 offset:0\n\t" \
  "v_rcp_f16_sdwa v226, v226 dst_sel:DWORD dst_unused:UNUSED_PAD src0_sel:WORD_1\n\t" \
  "v_mfma_f32_16x16x32_bf16 v[168:171], v[220:223], v[136:139], v[168:171]\n\t" \
  "s_waitcnt lgkmcnt(9)\n\t" \
  "ds_read_b64_tr_b16 v[220:221], v249 offset:4608\n\t" \
  "v_lshlrev_b32_sdwa v205, 1, v205 dst_sel:DWORD dst_unused:UNUSED_PAD src0_sel:DWORD src1_sel:WORD_1\n\t" \
  "v_fma_mix_f32 v188, v188, v225, 0 op_sel_hi:[0,1,0]\n\t" \
  "v_mfma_f32_16x16x32_bf16 v[144:147], v[208:211], v[104:107], v[144:147]\n\t" \
  "ds_read_u16_d16_hi v198, v198 offset:0\n\t" \
  "v_fma_mix_f32 v190, v190, v227, 0 op_sel_hi:[0,1,0]\n\t" \
  "v_mfma_f32_16x16x32_bf16 v[152:155], v[208:211], v[116:119], v[152:155]\n\t" \
  "ds_read_b64_tr_b16 v[222:223], v249 offset:4864\n\t" \
  "v_lshlrev_b32_sdwa v206, 1, v206 dst_sel:DWORD dst_unused:UNUSED_PAD src0_sel:DWORD src1_sel:WORD_1\n\t" \
  "v_fma_mix_f32 v189, v189, v224, 0 op_sel_hi:[0,1,0]\n\t" \
  "v_mfma_f32_16x16x32_bf16 v[160:163], v[208:211], v[128:131], v[160:163]\n\t" \
  "ds_read_u16_d16_hi v199, v199 offset:0\n\t" \
  "v_fma_mix_f32 v191, v191, v226, 0 op_sel_hi:[0,1,0]\n\t" \
  "v_mfma_f32_16x16x32_bf16 v[168:171], v[208:211], v[140:143], v[168:171]\n\t" \
  "s_waitcnt lgkmcnt(9)\n\t" \
  "ds_read_b64_tr_b16 v[208:209], v250 offset:6160\n\t" \
  "v_lshlrev_b32_sdwa v207, 1, v207 dst_sel:DWORD dst_unused:UNUSED_PAD src0_sel:DWORD src1_sel:WORD_1\n\t" \
  "v_mfma_f32_16x16x32_bf16 v[148:151], v[212:215], v[96:99], 0\n\t" \
  "ds_read_u16_d16_hi v204, v204 offset:0\n\t" \
  "v_lshlrev_b32_sdwa v144, 1, v144 dst_sel:DWORD dst_unused:UNUSED_PAD src0_sel:DWORD src1_sel:WORD_1\n\t" \
  "v_mfma_f32_16x16x32_bf16 v[156:159], v[212:215], v[108:111], 0\n\t" \
  "ds_read_b64_tr_b16 v[210:211], v250 offset:6928\n\t" \
  "ds_read_u16_d16_hi v205, v205 offset:0\n\t" \
  "v_cvt_pk_f16_f32 v224, v152, v153\n\t" \
  "v_cvt_pk_f16_f32 v226, v154, v155\n\t" \
  "v_mfma_f32_16x16x32_bf16 v[164:167], v[212:215], v[120:123], 0\n\t" \
  "ds_read_u16_d16_hi v206, v206 offset:0\n\t" \
  "v_pk_fma_f16 v225, v224, v224, %[h1] neg_lo:[1,0,0] neg_hi:[1,0,0]\n\t" \
  "v_mfma_f32_16x16x32_bf16 v[172:175], v[212:215], v[132:135], 0\n\t" \
  "s_waitcnt lgkmcnt(10)\n\t" \
  "ds_read_b64_tr_b16 v[212:213], v250 offset:6168\n\t" \
  "v_lshlrev_b32_sdwa v145, 1, v145 dst_sel:DWORD dst_unused:UNUSED_PAD src0_sel:DWORD src1_sel:WORD_1\n\t" \
  "v_pk_fma_f16 v227, v226, v226, %[h1] neg_lo:[1,0,0] neg_hi:[1,0,0]\n\t" \
  "v_mfma_f32_16x16x32_bf16 v[148:151], v[216:219], v[100:103], v[148:151]\n\t" \
  "ds_read_u16_d16_hi v207, v207 offset:0\n\t" \
  "v_pk_mul_f16 v225, v224, v225\n\t" \
  "v_mfma_f32_16x16x32_bf16 v[156:159], v[216:219], v[112:115], v[156:159]\n\t" \
  "ds_read_b64_tr_b16 v[214:215], v250 offset:6936\n\t" \
  "v_lshlrev_b32_sdwa v146, 1, v146 dst_sel:DWORD dst_unused:UNUSED_PAD src0_sel:DWORD src1_sel:WORD_1\n\t" \
  "v_pk_mul_f16 v227, v226, v227\n\t" \
  "v_exp_f16 v224, v225\n\t" \
  "v_mfma_f32_16x16x32_bf16 v[164:167], v[216:219], v[124:127], v[164:167]\n\t" \
  "ds_read_u16_d16_hi v144, v144 offset:0\n\t" \
  "v_lshlrev_b32_sdwa v147, 1, v147 dst_sel:DWORD dst_unused:UNUSED_PAD src0_sel:DWORD src1_sel:WORD_1\n\t" \
  "v_mfma_f32_16x16x32_bf16 v[172:175], v[216:219], v[136:139], v[172:175]\n\t" \
  "s_waitcnt lgkmcnt(10)\n\t" \
  "ds_read_b64_tr_b16 v[216:217], v250 offset:6224\n\t" \
  "v_exp_f16 v226, v227\n\t" \
  "v_mfma_f32_16x16x32_bf16 v[148:151], v[220:223], v[104:107], v[148:151]\n\t" \
  "ds_read_u16_d16_hi v145, v145 offset:0\n\t" \
  "v_lshlrev_b32_sdwa v160, 1, v160 dst_sel:DWORD dst_unused:UNUSED_PAD src0_sel:DWORD src1_sel:WORD_1\n\t" \
  "v_mfma_f32_16x16x32_bf16 v[156:159], v[220:223], v[116:119], v[156:159]\n\t" \
  "ds_read_b64_tr_b16 v[218:219], v250 offset:6992\n\t" \
  "v_exp_f16_sdwa v224, v225 dst_sel:WORD_1 dst_unused:UNUSED_PRESERVE src0_sel:WORD_1\n\t" \
  "v_mfma_f32_16x16x32_bf16 v[164:167], v[220:223], v[128:131], v[164:167]\n\t" \
  "ds_read_u16_d16_hi v146, v146 offset:0\n\t" \
  "v_exp_f16_sdwa v226, v227 dst_sel:WORD_1 dst_unused:UNUSED_PRESERVE src0_sel:WORD_1\n\t" \
  "v_mfma_f32_16x16x32_bf16 v[172:175], v[220:223], v[140:143], v[172:175]\n\t" \
  "s_waitcnt lgkmcnt(10)\n\t" \
  "ds_read_b64_tr_b16 v[220:221], v250 offset:6232\n\t" \
  "v_lshlrev_b32_sdwa v161, 1, v161 dst_sel:DWORD dst_unused:UNUSED_PAD src0_sel:DWORD src1_sel:WORD_1\n\t" \
  "v_smfmac_f32_16x16x64_bf16 v[0:3], v[208:211], v[176:183], v248\n\t" \
  "ds_read_u16_d16_hi v147, v147 offset:0\n\t" \
  "v_pk_fma_f16 v224, v224, %[hc], %[hc]\n\t" \
  "v_smfmac_f32_16x16x64_bf16 v[24:27], v[208:211], v[184:191], v248\n\t" \
  "ds_read_b64_tr_b16 v[222:223], v250 offset:7000\n\t" \
  "v_lshlrev_b32_sdwa v162, 1, v162 dst_sel:DWORD dst_unused:UNUSED_PAD src0_sel:DWORD src1_sel:WORD_1\n\t" \
  "v_pk_fma_f16 v226, v226, %[hc], %[hc]\n\t" \
  "v_smfmac_f32_16x16x64_bf16 v[48:51], v[208:211], v[192:199], v248\n\t" \
  "s_waitcnt lgkmcnt(9)\n\t" \
  "ds_read_u16_d16_hi v160, v160 offset:0\n\t" \
  "v_rcp_f16 v225, v224\n\t" \
  "v_smfmac_f32_16x16x64_bf16 v[72:75], v[208:211], v[200:207], v248\n\t" \
  "s_waitcnt lgkmcnt(9)\n\t" \
  "ds_read_b64_tr_b16 v[208:209], v250 offset:6288\n\t" \
  "v_lshlrev_b32_sdwa v163, 1, v163 dst_sel:DWORD dst_unused:UNUSED_PAD src0_sel:DWORD src1_sel:WORD_1\n\t" \
  "v_rcp_f16 v227, v226\n\t" \
  "v_smfmac_f32_16x16x64_bf16 v[4:7], v[212:215], v[176:183], v248\n\t" \
  "ds_read_b64 v[242:243], v252 offset:128\n\t" \
  "v_lshlrev_b32_sdwa v168, 1, v168 dst_sel:DWORD dst_unused:UNUSED_PAD src0_sel:DWORD src1_sel:WORD_1\n\t" \
  "v_smfmac_f32_16x16x64_bf16 v[28:31], v[212:215], v[184:191], v248\n\t" \
  "ds_read_b64_tr_b16 v[210:211], v250 offset:7056\n\t" \
  "v_rcp_f16_sdwa v224, v224 dst_sel:DWORD dst_unused:UNUSED_PAD src0_sel:WORD_1\n\t" \
  "v_smfmac_f32_16x16x64_bf16 v[52:55], v[212:215], v[192:199], v248\n\t" \
  "ds_read_b64 v[244:245], v252 offset:136\n\t" \
  "v_lshlrev_b32_sdwa v169, 1, v169 dst_sel:DWORD dst_unused:UNUSED_PAD src0_sel:DWORD src1_sel:WORD_1\n\t" \
  "v_smfmac_f32_16x16x64_bf16 v[76:79], v[212:215], v[200:207], v248\n\t" \
  "s_waitcnt lgkmcnt(9)\n\t" \
  "ds_read_b64_tr_b16 v[212:213], v250 offset:6296\n\t" \
  "ds_read_u16_d16_hi v161, v161 offset:0\n\t" \
  "v_rcp_f16_sdwa v226, v226 dst_sel:DWORD dst_unused:UNUSED_PAD src0_sel:WORD_1\n\t" \
  "v_smfmac_f32_16x16x64_bf16 v[8:11], v[216:219], v[176:183], v248\n\t" \
  "ds_read_b64_tr_b16 v[214:215], v250 offset:7064\n\t" \
  "v_fma_mix_f32 v152, v152, v225, 0 op_sel_hi:[0,1,0]\n\t" \
  "v_cvt_pk_bf16_f32 v230, v48, v49\n\t" \
  "v_smfmac_f32_16x16x64_bf16 v[32:35], v[216:219], v[184:191], v248\n\t" \
  "ds_read_u16_d16_hi v162, v162 offset:0\n\t" \
  "v_lshlrev_b32_sdwa v170, 1, v170 dst_sel:DWORD dst_unused:UNUSED_PAD src0_sel:DWORD src1_sel:WORD_1\n\t" \
  "v_fma_mix_f32 v154, v154, v227, 0 op_sel_hi:[0,1,0]\n\t" \
  "v_cvt_pk_bf16_f32 v231, v50, v51\n\t" \
  "v_smfmac_f32_16x16x64_bf16 v[56:59], v[216:219], v[192:199], v248\n\t" \
  "ds_read_u16_d16_hi v163, v163 offset:0\n\t" \
  "v_fma_mix_f32 v153, v153, v224, 0 op_sel_hi:[0,1,0]\n\t" \
  "v_cvt_pk_bf16_f32 v232, v52, v53\n\t" \
  "v_smfmac_f32_16x16x64_bf16 v[80:83], v[216:219], v[200:207], v248\n\t" \
  "s_waitcnt lgkmcnt(10)\n\t" \
  "s_barrier\n\t" \
  "ds_read_b64_tr_b16 v[216:217], v249 offset:6144\n\t" \
  "v_lshlrev_b32_sdwa v171, 1, v171 dst_sel:DWORD dst_unused:UNUSED_PAD src0_sel:DWORD src1_sel:WORD_1\n\t" \
  "v_fma_mix_f32 v155, v155, v226, 0 op_sel_hi:[0,1,0]\n\t" \
  "v_cvt_pk_bf16_f32 v233, v54, v55\n\t" \
  "v_smfmac_f32_16x16x64_bf16 v[12:15], v[220:223], v[176:183], v248\n\t" \
  "buffer_store_dwordx4 v[230:233], v253, %[osrd], %[ob] offen offset:0 sc1\n\t" \
  "ds_read_b64_tr_b16 v[218:219], v249 offset:6400\n\t" \
  "v_cvt_pk_f16_f32 v224, v156, v157\n\t" \
  "v_cvt_pk_f16_f32 v226, v158, v159\n\t" \
  "v_smfmac_f32_16x16x64_bf16 v[36:39], v[220:223], v[184:191], v248\n\t" \
  "ds_read_b64 v[230:231], v252 offset:0\n\t" \
  "v_lshlrev_b32_sdwa v148, 1, v148 dst_sel:DWORD dst_unused:UNUSED_PAD src0_sel:DWORD src1_sel:WORD_1\n\t" \
  "v_pk_fma_f16 v225, v224, v224, %[h1] neg_lo:[1,0,0] neg_hi:[1,0,0]\n\t" \
  "v_smfmac_f32_16x16x64_bf16 v[60:63], v[220:223], v[192:199], v248\n\t" \
  "ds_read_b64 v[232:233], v252 offset:8\n\t" \
  "v_pk_fma_f16 v227, v226, v226, %[h1] neg_lo:[1,0,0] neg_hi:[1,0,0]\n\t" \
  "v_smfmac_f32_16x16x64_bf16 v[84:87], v[220:223], v[200:207], v248\n\t" \
  "s_waitcnt lgkmcnt(10)\n\t" \
  "ds_read_b64_tr_b16 v[220:221], v249 offset:8192\n\t" \
  "ds_read_u16_d16_hi v168, v168 offset:0\n\t" \
  "v_lshlrev_b32_sdwa v149, 1, v149 dst_sel:DWORD dst_unused:UNUSED_PAD src0_sel:DWORD src1_sel:WORD_1\n\t" \
  "v_pk_mul_f16 v225, v224, v225\n\t" \
  "v_smfmac_f32_16x16x64_bf16 v[16:19], v[208:211], v[176:183], v248\n\t" \
  "ds_read_b64_tr_b16 v[222:223], v249 offset:8448\n\t" \
  "v_pk_mul_f16 v227, v226, v227\n\t" \
  "v_exp_f16 v224, v225\n\t" \
  "v_cvt_pk_bf16_f32 v234, v56, v57\n\t" \
  "v_smfmac_f32_16x16x64_bf16 v[40:43], v[208:211], v[184:191], v248\n\t" \
  "ds_read_u16_d16_hi v169, v169 offset:0\n\t" \
  "v_lshlrev_b32_sdwa v150, 1, v150 dst_sel:DWORD dst_unused:UNUSED_PAD src0_sel:DWORD src1_sel:WORD_1\n\t" \
  "v_cvt_pk_bf16_f32 v235, v58, v59\n\t" \
  "v_smfmac_f32_16x16x64_bf16 v[64:67], v[208:211], v[192:199], v248\n\t" \
  "ds_read_u16_d16_hi v170, v170 offset:0\n\t" \
  "v_exp_f16 v226, v227\n\t" \
  "v_cvt_pk_bf16_f32 v236, v60, v61\n\t" \
  "v_smfmac_f32_16x16x64_bf16 v[88:91], v[208:211], v[200:207], v248\n\t" \
  "s_waitcnt lgkmcnt(11)\n\t" \
  "ds_read_b64_tr_b16 v[208:209], v249 offset:10240\n\t" \
  "v_lshlrev_b32_sdwa v151, 1, v151 dst_sel:DWORD dst_unused:UNUSED_PAD src0_sel:DWORD src1_sel:WORD_1\n\t" \
  "v_cvt_pk_bf16_f32 v237, v62, v63\n\t" \
  "v_smfmac_f32_16x16x64_bf16 v[20:23], v[212:215], v[176:183], v248\n\t" \
  "buffer_store_dwordx4 v[234:237], v253, %[osrd], %[ob] offen offset:64 sc1\n\t" \
  "ds_read_b64_tr_b16 v[210:211], v249 offset:10496\n\t" \
  "v_exp_f16_sdwa v224, v225 dst_sel:WORD_1 dst_unused:UNUSED_PRESERVE src0_sel:WORD_1\n\t" \
  "v_smfmac_f32_16x16x64_bf16 v[44:47], v[212:215], v[184:191], v248\n\t" \
  "ds_read_b64 v[234:235], v252 offset:64\n\t" \
  "ds_read_b64 v[236:237], v252 offset:72\n\t" \
  "v_lshlrev_b32_sdwa v164, 1, v164 dst_sel:DWORD dst_unused:UNUSED_PAD src0_sel:DWORD src1_sel:WORD_1\n\t" \
  "v_exp_f16_sdwa v226, v227 dst_sel:WORD_1 dst_unused:UNUSED_PRESERVE src0_sel:WORD_1\n\t" \
  "v_smfmac_f32_16x16x64_bf16 v[68:71], v[212:215], v[192:199], v248\n\t" \
  "s_waitcnt lgkmcnt(10)\n\t" \
  "ds_read_u16_d16_hi v171, v171 offset:0\n\t" \
  "v_lshlrev_b32_sdwa v165, 1, v165 dst_sel:DWORD dst_unused:UNUSED_PAD src0_sel:DWORD src1_sel:WORD_1\n\t" \
  "v_mfma_f32_16x16x16_bf16 v[48:51], v[246:247], v[230:231], 0\n\t" \
  "ds_read_u16_d16_hi v148, v148 offset:0\n\t" \
  "v_pk_fma_f16 v224, v224, %[hc], %[hc]\n\t" \
  "v_smfmac_f32_16x16x64_bf16 v[92:95], v[212:215], v[200:207], v248\n\t" \
  "ds_read_b64_tr_b16 v[212:213], v249 offset:6656\n\t" \
  "ds_read_u16_d16_hi v149, v149 offset:0\n\t" \
  "v_mfma_f32_16x16x32_bf16 v[176:179], v[216:219], v[96:99], 0\n\t" \
  "ds_read_b64_tr_b16 v[214:215], v249 offset:6912\n\t" \
  "v_pk_fma_f16 v226, v226, %[hc], %[hc]\n\t" \
  "v_cvt_pk_bf16_f32 v238, v64, v65\n\t" \
  "v_mfma_f32_16x16x32_bf16 v[184:187], v[216:219], v[108:111], 0\n\t" \
  "s_waitcnt lgkmcnt(14)\n\t" \
  "ds_read_u16_d16_hi v150, v150 offset:0\n\t" \
  "v_lshlrev_b32_sdwa v166, 1, v166 dst_sel:DWORD dst_unused:UNUSED_PAD src0_sel:DWORD src1_sel:WORD_1\n\t" \
  "v_mfma_f32_16x16x16_bf16 v[52:55], v[246:247], v[232:233], 0\n\t" \
  "ds_read_u16_d16_hi v151, v151 offset:0\n\t" \
  "v_rcp_f16 v225, v224\n\t" \
  "v_cvt_pk_bf16_f32 v239, v66, v67\n\t" \
  "v_mfma_f32_16x16x32_bf16 v[192:195], v[216:219], v[120:123], 0\n\t" \
  "ds_read_u16_d16_hi v164, v164 offset:0\n\t" \
  "v_rcp_f16 v227, v226\n\t" \
  "v_cvt_pk_bf16_f32 v240, v68, v69\n\t" \
  "v_mfma_f32_16x16x32_bf16 v[200:203], v[216:219], v[132:135], 0\n\t" \
  "s_waitcnt lgkmcnt(14)\n\t" \
  "ds_read_b64_tr_b16 v[216:217], v249 offset:8704\n\t" \
  "v_lshlrev_b32_sdwa v167, 1, v167 dst_sel:DWORD dst_unused:UNUSED_PAD src0_sel:DWORD src1_sel:WORD_1\n\t" \
  "v_cvt_pk_bf16_f32 v241, v70, v71\n\t" \
  "v_mfma_f32_16x16x32_bf16 v[176:179], v[220:223], v[100:103], v[176:179]\n\t" \
  "buffer_store_dwordx4 v[238:241], v253, %[osrd], %[ob] offen offset:128 sc1\n\t" \
  "v_rcp_f16_sdwa v224, v224 dst_sel:DWORD dst_unused:UNUSED_PAD src0_sel:WORD_1\n\t" \
  "v_mfma_f32_16x16x32_bf16 v[184:187], v[220:223], v[112:115], v[184:187]\n\t" \
  "ds_read_b64_tr_b16 v[218:219], v249 offset:8960\n\t" \
  "v_lshlrev_b32_sdwa v172, 1, v172 dst_sel:DWORD dst_unused:UNUSED_PAD src0_sel:DWORD src1_sel:WORD_1\n\t" \
  "v_mfma_f32_16x16x32_bf16 v[192:195], v[220:223], v[124:127], v[192:195]\n\t" \
  "s_waitcnt lgkmcnt(11)\n\t" \
  "ds_read_u16_d16_hi v165, v165 offset:0\n\t" \
  "v_rcp_f16_sdwa v226, v226 dst_sel:DWORD dst_unused:UNUSED_PAD src0_sel:WORD_1\n\t" \
  "v_mfma_f32_16x16x16_bf16 v[56:59], v[246:247], v[234:235], 0\n\t" \
  "ds_read_u16_d16_hi v166, v166 offset:0\n\t" \
  "v_lshlrev_b32_sdwa v173, 1, v173 dst_sel:DWORD dst_unused:UNUSED_PAD src0_sel:DWORD src1_sel:WORD_1\n\t" \
  "v_mfma_f32_16x16x32_bf16 v[200:203], v[220:223], v[136:139], v[200:203]\n\t" \
  "ds_read_b64_tr_b16 v[220:221], v249 offset:10752\n\t" \
  "v_fma_mix_f32 v156, v156, v225, 0 op_sel_hi:[0,1,0]\n\t" \
  "v_mfma_f32_16x16x32_bf16 v[176:179], v[208:211], v[104:107], v[176:179]\n\t" \
  "ds_read_u16_d16_hi v167, v167 offset:0\n\t" \
  "v_fma_mix_f32 v158, v158, v227, 0 op_sel_hi:[0,1,0]\n\t" \
  "v_mfma_f32_16x16x32_bf16 v[184:187], v[208:211], v[116:119], v[184:187]\n\t" \
  "s_waitcnt lgkmcnt(14)\n\t" \
  "ds_read_b64_tr_b16 v[222:223], v249 offset:11008\n\t" \
  "v_lshlrev_b32_sdwa v174, 1, v174 dst_sel:DWORD dst_unused:UNUSED_PAD src0_sel:DWORD src1_sel:WORD_1\n\t" \
  "v_mfma_f32_16x16x16_bf16 v[60:63], v[246:247], v[236:237], 0\n\t" \
  "ds_read_u16_d16_hi v172, v172 offset:0\n\t" \
  "v_fma_mix_f32 v157, v157, v224, 0 op_sel_hi:[0,1,0]\n\t" \
  "v_mfma_f32_16x16x32_bf16 v[192:195], v[208:211], v[128:131], v[192:195]\n\t" \
  "ds_read_u16_d16_hi v173, v173 offset:0\n\t" \
  "v_fma_mix_f32 v159, v159, v226, 0 op_sel_hi:[0,1,0]\n\t" \
  "v_mfma_f32_16x16x32_bf16 v[200:203], v[208:211], v[140:143], v[200:203]\n\t" \
  "s_waitcnt lgkmcnt(12)\n\t" \
  "ds_read_b64_tr_b16 v[208:209], v250 offset:12320\n\t" \
  "v_lshlrev_b32_sdwa v175, 1, v175 dst_sel:DWORD dst_unused:UNUSED_PAD src0_sel:DWORD src1_sel:WORD_1\n\t" \
  "v_mfma_f32_16x16x32_bf16 v[180:183], v[212:215], v[96:99], 0\n\t" \
  "ds_read_u16_d16_hi v174, v174 offset:0\n\t" \
  "v_lshlrev_b32_sdwa v176, 1, v176 dst_sel:DWORD dst_unused:UNUSED_PAD src0_sel:DWORD src1_sel:WORD_1\n\t" \
  "v_mfma_f32_16x16x32_bf16 v[188:191], v[212:215], v[108:111], 0\n\t" \
  "ds_read_b64_tr_b16 v[210:211], v250 offset:13088\n\t" \
  "v_cvt_pk_f16_f32 v224, v184, v185\n\t" \
  "v_cvt_pk_f16_f32 v226, v186, v187\n\t" \
  "v_mfma_f32_16x16x32_bf16 v[196:199], v[212:215], v[120:123], 0\n\t" \
  "ds_read_u16_d16_hi v175, v175 offset:0\n\t" \
  "v_pk_fma_f16 v225, v224, v224, %[h1] neg_lo:[1,0,0] neg_hi:[1,0,0]\n\t" \
  "v_mfma_f32_16x16x16_bf16 v[64:67], v[246:247], v[242:243], 0\n\t" \
  "ds_read_u16_d16_hi v176, v176 offset:0\n\t" \
  "v_lshlrev_b32_sdwa v177, 1, v177 dst_sel:DWORD dst_unused:UNUSED_PAD src0_sel:DWORD src1_sel:WORD_1\n\t" \
  "v_mfma_f32_16x16x32_bf16 v[204:207], v[212:215], v[132:135], 0\n\t" \
  "s_waitcnt lgkmcnt(12)\n\t" \
  "ds_read_b64_tr_b16 v[212:213], v250 offset:12328\n\t" \
  "v_pk_fma_f16 v227, v226, v226, %[h1] neg_lo:[1,0,0] neg_hi:[1,0,0]\n\t" \
  "v_mfma_f32_16x16x32_bf16 v[180:183], v[216:219], v[100:103], v[180:183]\n\t" \
  "ds_read_u16_d16_hi v177, v177 offset:0\n\t" \
  "v_pk_mul_f16 v225, v224, v225\n\t" \
  "v_mfma_f32_16x16x32_bf16 v[188:191], v[216:219], v[112:115], v[188:191]\n\t" \
  "ds_read_b64_tr_b16 v[214:215], v250 offset:13096\n\t" \
  "v_lshlrev_b32_sdwa v178, 1, v178 dst_sel:DWORD dst_unused:UNUSED_PAD src0_sel:DWORD src1_sel:WORD_1\n\t" \
  "v_mfma_f32_16x16x16_bf16 v[68:71], v[246:247], v[244:245], 0\n\t" \
  "ds_read_u16_d16_hi v178, v178 offset:0\n\t" \
  "v_pk_mul_f16 v227, v226, v227\n\t" \
  "v_exp_f16 v224, v225\n\t" \
  "v_mfma_f32_16x16x32_bf16 v[196:199], v[216:219], v[124:127], v[196:199]\n\t" \
  "v_lshlrev_b32_sdwa v179, 1, v179 dst_sel:DWORD dst_unused:UNUSED_PAD src0_sel:DWORD src1_sel:WORD_1\n\t" \
  "v_exp_f16 v226, v227\n\t" \
  "v_mfma_f32_16x16x32_bf16 v[204:207], v[216:219], v[136:139], v[204:207]\n\t" \
  "s_waitcnt lgkmcnt(11)\n\t" \
  "ds_read_b64_tr_b16 v[216:217], v250 offset:12384\n\t" \
  "v_lshlrev_b32_sdwa v192, 1, v192 dst_sel:DWORD dst_unused:UNUSED_PAD src0_sel:DWORD src1_sel:WORD_1\n\t" \
  "v_mfma_f32_16x16x32_bf16 v[180:183], v[220:223], v[104:107], v[180:183]\n\t" \
  "ds_read_u16_d16_hi v179, v179 offset:0\n\t" \
  "v_exp_f16_sdwa v224, v225 dst_sel:WORD_1 dst_unused:UNUSED_PRESERVE src0_sel:WORD_1\n\t" \
  "v_mfma_f32_16x16x32_bf16 v[188:191], v[220:223], v[116:119], v[188:191]\n\t" \
  "ds_read_b64_tr_b16 v[218:219], v250 offset:13152\n\t" \
  "v_exp_f16_sdwa v226, v227 dst_sel:WORD_1 dst_unused:UNUSED_PRESERVE src0_sel:WORD_1\n\t" \
  "v_mfma_f32_16x16x32_bf16 v[196:199], v[220:223], v[128:131], v[196:199]\n\t" \
  "ds_read_u16_d16_hi v192, v192 offset:0\n\t" \
  "v_lshlrev_b32_sdwa v193, 1, v193 dst_sel:DWORD dst_unused:UNUSED_PAD src0_sel:DWORD src1_sel:WORD_1\n\t" \
  "v_mfma_f32_16x16x32_bf16 v[204:207], v[220:223], v[140:143], v[204:207]\n\t" \
  "s_waitcnt lgkmcnt(10)\n\t" \
  "ds_read_b64_tr_b16 v[220:221], v250 offset:12392\n\t" \
  "v_pk_fma_f16 v224, v224, %[hc], %[hc]\n\t" \
  "v_smfmac_f32_16x16x64_bf16 v[0:3], v[208:211], v[144:151], v248\n\t" \
  "ds_read_u16_d16_hi v193, v193 offset:0\n\t" \
  "v_lshlrev_b32_sdwa v194, 1, v194 dst_sel:DWORD dst_unused:UNUSED_PAD src0_sel:DWORD src1_sel:WORD_1\n\t" \
  "v_smfmac_f32_16x16x64_bf16 v[24:27], v[208:211], v[152:159], v248\n\t" \
  "ds_read_b64_tr_b16 v[222:223], v250 offset:13160\n\t" \
  "v_pk_fma_f16 v226, v226, %[hc], %[hc]\n\t" \
  "v_smfmac_f32_16x16x64_bf16 v[48:51], v[208:211], v[160:167], v248\n\t" \
  "s_waitcnt lgkmcnt(12)\n\t" \
  "ds_read_u16_d16_hi v194, v194 offset:0\n\t" \
  "v_rcp_f16 v225, v224\n\t" \
  "v_smfmac_f32_16x16x64_bf16 v[72:75], v[208:211], v[168:175], v248\n\t" \
  "s_waitcnt lgkmcnt(9)\n\t" \
  "ds_read_b64_tr_b16 v[208:209], v250 offset:12448\n\t" \
  "v_lshlrev_b32_sdwa v195, 1, v195 dst_sel:DWORD dst_unused:UNUSED_PAD src0_sel:DWORD src1_sel:WORD_1\n\t" \
  "v_rcp_f16 v227, v226\n\t" \
  "v_smfmac_f32_16x16x64_bf16 v[4:7], v[212:215], v[144:151], v248\n\t" \
  "ds_read_u16_d16_hi v195, v195 offset:0\n\t" \
  "v_lshlrev_b32_sdwa v200, 1, v200 dst_sel:DWORD dst_unused:UNUSED_PAD src0_sel:DWORD src1_sel:WORD_1\n\t" \
  "v_smfmac_f32_16x16x64_bf16 v[28:31], v[212:215], v[152:159], v248\n\t" \
  "s_add_u32 %[ob], %[ob], %[dob]\n\t" \
  "ds_read_b64_tr_b16 v[210:211], v250 offset:13216\n\t" \
  "v_rcp_f16_sdwa v224, v224 dst_sel:DWORD dst_unused:UNUSED_PAD src0_sel:WORD_1\n\t" \
  "v_smfmac_f32_16x16x64_bf16 v[52:55], v[212:215], v[160:167], v248\n\t" \
  "ds_read_u16_d16_hi v200, v200 offset:0\n\t" \
  "v_lshlrev_b32_sdwa v201, 1, v201 dst_sel:DWORD dst_unused:UNUSED_PAD src0_sel:DWORD src1_sel:WORD_1\n\t" \
  "v_smfmac_f32_16x16x64_bf16 v[76:79], v[212:215], v[168:175], v248\n\t" \
  "s_waitcnt lgkmcnt(9)\n\t" \
  "ds_read_b64_tr_b16 v[212:213], v250 offset:12456\n\t" \
  "v_rcp_f16_sdwa v226, v226 dst_sel:DWORD dst_unused:UNUSED_PAD src0_sel:WORD_1\n\t" \
  "v_smfmac_f32_16x16x64_bf16 v[8:11], v[216:219], v[144:151], v248\n\t" \
  "ds_read_u16_d16_hi v201, v201 offset:0\n\t" \
  "v_fma_mix_f32 v184, v184, v225, 0 op_sel_hi:[0,1,0]\n\t" \
  "v_smfmac_f32_16x16x64_bf16 v[32:35], v[216:219], v[152:159], v248\n\t" \
  "ds_read_b64_tr_b16 v[214:215], v250 offset:13224\n\t" \
  "v_lshlrev_b32_sdwa v202, 1, v202 dst_sel:DWORD dst_unused:UNUSED_PAD src0_sel:DWORD src1_sel:WORD_1\n\t" \
  "v_fma_mix_f32 v186, v186, v227, 0 op_sel_hi:[0,1,0]\n\t" \
  "v_smfmac_f32_16x16x64_bf16 v[56:59], v[216:219], v[160:167], v248\n\t" \
  "ds_read_u16_d16_hi v202, v202 offset:0\n\t" \
  "v_fma_mix_f32 v185, v185, v224, 0 op_sel_hi:[0,1,0]\n\t" \
  "v_smfmac_f32_16x16x64_bf16 v[80:83], v[216:219], v[168:175], v248\n\t" \
  "s_waitcnt lgkmcnt(9)\n\t" \
  "s_barrier\n\t" \
  "ds_read_b64_tr_b16 v[216:217], v249 offset:12288\n\t" \
  "v_lshlrev_b32_sdwa v203, 1, v203 dst_sel:DWORD dst_unused:UNUSED_PAD src0_sel:DWORD src1_sel:WORD_1\n\t" \
  "v_fma_mix_f32 v187, v187, v226, 0 op_sel_hi:[0,1,0]\n\t" \
  "v_smfmac_f32_16x16x64_bf16 v[12:15], v[220:223], v[144:151], v248\n\t" \
  "ds_read_u16_d16_hi v203, v203 offset:0\n\t" \
  "v_cvt_pk_f16_f32 v224, v188, v189\n\t" \
  "v_cvt_pk_f16_f32 v226, v190, v191\n\t" \
  "v_smfmac_f32_16x16x64_bf16 v[36:39], v[220:223], v[152:159], v248\n\t" \
  "ds_read_b64_tr_b16 v[218:219], v249 offset:12544\n\t" \
  "v_lshlrev_b32_sdwa v180, 1, v180 dst_sel:DWORD dst_unused:UNUSED_PAD src0_sel:DWORD src1_sel:WORD_1\n\t" \
  "v_pk_fma_f16 v225, v224, v224, %[h1] neg_lo:[1,0,0] neg_hi:[1,0,0]\n\t" \
  "v_smfmac_f32_16x16x64_bf16 v[60:63], v[220:223], v[160:167], v248\n\t" \
  "ds_read_u16_d16_hi v180, v180 offset:0\n\t" \
  "v_pk_fma_f16 v227, v226, v226, %[h1] neg_lo:[1,0,0] neg_hi:[1,0,0]\n\t" \
  "v_smfmac_f32_16x16x64_bf16 v[84:87], v[220:223], v[168:175], v248\n\t" \
  "s_waitcnt lgkmcnt(9)\n\t" \
  "ds_read_b64_tr_b16 v[220:221], v249 offset:14336\n\t" \
  "v_lshlrev_b32_sdwa v181, 1, v181 dst_sel:DWORD dst_unused:UNUSED_PAD src0_sel:DWORD src1_sel:WORD_1\n\t" \
  "v_pk_mul_f16 v225, v224, v225\n\t" \
  "v_smfmac_f32_16x16x64_bf16 v[16:19], v[208:211], v[144:151], v248\n\t" \
  "s_add_u32 %[rs], %[rs], 1\n\t" \
  "ds_read_u16_d16_hi v181, v181 offset:0\n\t" \
  "v_pk_mul_f16 v227, v226, v227\n\t" \
  "v_exp_f16 v224, v225\n\t" \
  "v_smfmac_f32_16x16x64_bf16 v[40:43], v[208:211], v[152:159], v248\n\t" \
  "s_cmp_eq_u32 %[rs], 6\n\t" \
  "ds_read_b64_tr_b16 v[222:223], v249 offset:14592\n\t" \
  "v_lshlrev_b32_sdwa v182, 1, v182 dst_sel:DWORD dst_unused:UNUSED_PAD src0_sel:DWORD src1_sel:WORD_1\n\t" \
  "v_smfmac_f32_16x16x64_bf16 v[64:67], v[208:211], v[160:167], v248\n\t" \
  "ds_read_u16_d16_hi v182, v182 offset:0\n\t" \
  "v_exp_f16 v226, v227\n\t" \
  "v_smfmac_f32_16x16x64_bf16 v[88:91], v[208:211], v[168:175], v248\n\t" \
  "s_waitcnt lgkmcnt(9)\n\t" \
  "ds_read_b64_tr_b16 v[208:209], v249 offset:16384\n\t" \
  "v_lshlrev_b32_sdwa v183, 1, v183 dst_sel:DWORD dst_unused:UNUSED_PAD src0_sel:DWORD src1_sel:WORD_1\n\t" \
  "v_smfmac_f32_16x16x64_bf16 v[20:23], v[212:215], v[144:151], v248\n\t" \
  "s_cselect_b32 %[rs], 0, %[rs]\n\t" \
  "ds_read_u16_d16_hi v183, v183 offset:0\n\t" \
  "v_exp_f16_sdwa v224, v225 dst_sel:WORD_1 dst_unused:UNUSED_PRESERVE src0_sel:WORD_1\n\t" \
  "v_smfmac_f32_16x16x64_bf16 v[44:47], v[212:215], v[152:159], v248\n\t" \
  "s_cselect_b32 %[t0], -75920, %[ps]\n\t" \
  "ds_read_b64_tr_b16 v[210:211], v249 offset:16640\n\t" \
  "v_lshlrev_b32_sdwa v196, 1, v196 dst_sel:DWORD dst_unused:UNUSED_PAD src0_sel:DWORD src1_sel:WORD_1\n\t" \
  "v_exp_f16_sdwa v226, v227 dst_sel:WORD_1 dst_unused:UNUSED_PRESERVE src0_sel:WORD_1\n\t" \
  "v_smfmac_f32_16x16x64_bf16 v[68:71], v[212:215], v[160:167], v248\n\t" \
  "ds_read_u16_d16_hi v196, v196 offset:0\n\t" \
  "v_add_u32 v252, %[t0], v252\n\t" \
  "v_smfmac_f32_16x16x64_bf16 v[92:95], v[212:215], v[168:175], v248\n\t" \
  "s_waitcnt lgkmcnt(9)\n\t" \
  "ds_read_b64_tr_b16 v[212:213], v249 offset:12800\n\t" \
  "v_lshlrev_b32_sdwa v197, 1, v197 dst_sel:DWORD dst_unused:UNUSED_PAD src0_sel:DWORD src1_sel:WORD_1\n\t" \
  "v_pk_fma_f16 v224, v224, %[hc], %[hc]\n\t" \
  "v_mfma_f32_16x16x32_bf16 v[144:147], v[216:219], v[96:99], 0\n\t" \
  "ds_read_u16_d16_hi v197, v197 offset:0\n\t" \
  "v_pk_fma_f16 v226, v226, %[hc], %[hc]\n\t" \
  "v_mfma_f32_16x16x32_bf16 v[152:155], v[216:219], v[108:111], 0\n\t" \
  "ds_read_b64_tr_b16 v[214:215], v249 offset:13056\n\t" \
  "v_lshlrev_b32_sdwa v198, 1, v198 dst_sel:DWORD dst_unused:UNUSED_PAD src0_sel:DWORD src1_sel:WORD_1\n\t" \
  "v_rcp_f16 v225, v224\n\t" \
  "v_mfma_f32_16x16x32_bf16 v[160:163], v[216:219], v[120:123], 0\n\t" \
  "ds_read_u16_d16_hi v198, v198 offset:0\n\t" \
  "v_rcp_f16 v227, v226\n\t" \
  "v_mfma_f32_16x16x32_bf16 v[168:171], v[216:219], v[132:135], 0\n\t" \
  "s_waitcnt lgkmcnt(9)\n\t" \
  "ds_read_b64_tr_b16 v[216:217], v249 offset:14848\n\t" \
  "v_lshlrev_b32_sdwa v199, 1, v199 dst_sel:DWORD dst_unused:UNUSED_PAD src0_sel:DWORD src1_sel:WORD_1\n\t" \
  "v_mfma_f32_16x16x32_bf16 v[144:147], v[220:223], v[100:103], v[144:147]\n\t" \
  "ds_read_u16_d16_hi v199, v199 offset:0\n\t" \
  "v_rcp_f16_sdwa v224, v224 dst_sel:DWORD dst_unused:UNUSED_PAD src0_sel:WORD_1\n\t" \
  "v_mfma_f32_16x16x32_bf16 v[152:155], v[220:223], v[112:115], v[152:155]\n\t" \
  "ds_read_b64_tr_b16 v[218:219], v249 offset:15104\n\t" \
  "v_lshlrev_b32_sdwa v204, 1, v204 dst_sel:DWORD dst_unused:UNUSED_PAD src0_sel:DWORD src1_sel:WORD_1\n\t" \
  "v_mfma_f32_16x16x32_bf16 v[160:163], v[220:223], v[124:127], v[160:163]\n\t" \
  "ds_read_u16_d16_hi v204, v204 offset:0\n\t" \
  "v_rcp_f16_sdwa v226, v226 dst_sel:DWORD dst_unused:UNUSED_PAD src0_sel:WORD_1\n\t" \
  "v_mfma_f32_16x16x32_bf16 v[168:171], v[220:223], v[136:139], v[168:171]\n\t" \
  "s_waitcnt lgkmcnt(9)\n\t" \
  "ds_read_b64_tr_b16 v[220:221], v249 offset:16896\n\t" \
  "v_lshlrev_b32_sdwa v205, 1, v205 dst_sel:DWORD dst_unused:UNUSED_PAD src0_sel:DWORD src1_sel:WORD_1\n\t" \
  "v_fma_mix_f32 v188, v188, v225, 0 op_sel_hi:[0,1,0]\n\t" \
  "v_mfma_f32_16x16x32_bf16 v[144:147], v[208:211], v[104:107], v[144:147]\n\t" \
  "ds_read_u16_d16_hi v205, v205 offset:0\n\t" \
  "v_fma_mix_f32 v190, v190, v227, 0 op_sel_hi:[0,1,0]\n\t" \
  "v_mfma_f32_16x16x32_bf16 v[152:155], v[208:211], v[116:119], v[152:155]\n\t" \
  "ds_read_b64_tr_b16 v[222:223], v249 offset:17152\n\t" \
  "v_lshlrev_b32_sdwa v206, 1, v206 dst_sel:DWORD dst_unused:UNUSED_PAD src0_sel:DWORD src1_sel:WORD_1\n\t" \
  "v_fma_mix_f32 v189, v189, v224, 0 op_sel_hi:[0,1,0]\n\t" \
  "v_mfma_f32_16x16x32_bf16 v[160:163], v[208:211], v[128:131], v[160:163]\n\t" \
  "ds_read_u16_d16_hi v206, v206 offset:0\n\t" \
  "v_fma_mix_f32 v191, v191, v226, 0 op_sel_hi:[0,1,0]\n\t" \
  "v_mfma_f32_16x16x32_bf16 v[168:171], v[208:211], v[140:143], v[168:171]\n\t" \
  "s_waitcnt lgkmcnt(9)\n\t" \
  "ds_read_b64_tr_b16 v[208:209], v250 offset:18480\n\t" \
  "v_lshlrev_b32_sdwa v207, 1, v207 dst_sel:DWORD dst_unused:UNUSED_PAD src0_sel:DWORD src1_sel:WORD_1\n\t" \
  "v_mfma_f32_16x16x32_bf16 v[148:151], v[212:215], v[96:99], 0\n\t" \
  "ds_read_u16_d16_hi v207, v207 offset:0\n\t" \
  "v_lshlrev_b32_sdwa v144, 1, v144 dst_sel:DWORD dst_unused:UNUSED_PAD src0_sel:DWORD src1_sel:WORD_1\n\t" \
  "v_mfma_f32_16x16x32_bf16 v[156:159], v[212:215], v[108:111], 0\n\t" \
  "ds_read_b64_tr_b16 v[210:211], v250 offset:19248\n\t" \
  "v_cvt_pk_f16_f32 v224, v152, v153\n\t" \
  "v_cvt_pk_f16_f32 v226, v154, v155\n\t" \
  "v_mfma_f32_16x16x32_bf16 v[164:167], v[212:215], v[120:123], 0\n\t" \
  "ds_read_u16_d16_hi v144, v144 offset:0\n\t" \
  "v_pk_fma_f16 v225, v224, v224, %[h1] neg_lo:[1,0,0] neg_hi:[1,0,0]\n\t" \
  "v_mfma_f32_16x16x32_bf16 v[172:175], v[212:215], v[132:135], 0\n\t" \
  "s_waitcnt lgkmcnt(9)\n\t" \
  "ds_read_b64_tr_b16 v[212:213], v250 offset:18488\n\t" \
  "v_lshlrev_b32_sdwa v145, 1, v145 dst_sel:DWORD dst_unused:UNUSED_PAD src0_sel:DWORD src1_sel:WORD_1\n\t" \
  "v_pk_fma_f16 v227, v226, v226, %[h1] neg_lo:[1,0,0] neg_hi:[1,0,0]\n\t" \
  "v_mfma_f32_16x16x32_bf16 v[148:151], v[216:219], v[100:103], v[148:151]\n\t" \
  "ds_read_u16_d16_hi v145, v145 offset:0\n\t" \
  "v_pk_mul_f16 v225, v224, v225\n\t" \
  "v_mfma_f32_16x16x32_bf16 v[156:159], v[216:219], v[112:115], v[156:159]\n\t" \
  "ds_read_b64_tr_b16 v[214:215], v250 offset:19256\n\t" \
  "v_lshlrev_b32_sdwa v146, 1, v146 dst_sel:DWORD dst_unused:UNUSED_PAD src0_sel:DWORD src1_sel:WORD_1\n\t" \
  "v_pk_mul_f16 v227, v226, v227\n\t" \
  "v_exp_f16 v224, v225\n\t" \
  "v_mfma_f32_16x16x32_bf16 v[164:167], v[216:219], v[124:127], v[164:167]\n\t" \
  "ds_read_u16_d16_hi v146, v146 offset:0\n\t" \
  "v_lshlrev_b32_sdwa v147, 1, v147 dst_sel:DWORD dst_unused:UNUSED_PAD src0_sel:DWORD src1_sel:WORD_1\n\t" \
  "v_mfma_f32_16x16x32_bf16 v[172:175], v[216:219], v[136:139], v[172:175]\n\t" \
  "s_waitcnt lgkmcnt(9)\n\t" \
  "ds_read_b64_tr_b16 v[216:217], v250 offset:18544\n\t" \
  "v_exp_f16 v226, v227\n\t" \
  "v_mfma_f32_16x16x32_bf16 v[148:151], v[220:223], v[104:107], v[148:151]\n\t" \
  "ds_read_u16_d16_hi v147, v147 offset:0\n\t" \
  "v_lshlrev_b32_sdwa v160, 1, v160 dst_sel:DWORD dst_unused:UNUSED_PAD src0_sel:DWORD src1_sel:WORD_1\n\t" \
  "v_mfma_f32_16x16x32_bf16 v[156:159], v[220:223], v[116:119], v[156:159]\n\t" \
  "ds_read_b64_tr_b16 v[218:219], v250 offset:19312\n\t" \
  "v_exp_f16_sdwa v224, v225 dst_sel:WORD_1 dst_unused:UNUSED_PRESERVE src0_sel:WORD_1\n\t" \
  "v_mfma_f32_16x16x32_bf16 v[164:167], v[220:223], v[128:131], v[164:167]\n\t" \
  "ds_read_u16_d16_hi v160, v160 offset:0\n\t" \
  "v_exp_f16_sdwa v226, v227 dst_sel:WORD_1 dst_unused:UNUSED_PRESERVE src0_sel:WORD_1\n\t" \
  "v_mfma_f32_16x16x32_bf16 v[172:175], v[220:223], v[140:143], v[172:175]\n\t" \
  "s_waitcnt lgkmcnt(9)\n\t" \
  "ds_read_b64_tr_b16 v[220:221], v250 offset:18552\n\t" \
  "v_lshlrev_b32_sdwa v161, 1, v161 dst_sel:DWORD dst_unused:UNUSED_PAD src0_sel:DWORD src1_sel:WORD_1\n\t" \
  "v_smfmac_f32_16x16x64_bf16 v[0:3], v[208:211], v[176:183], v248\n\t" \
  "ds_read_u16_d16_hi v161, v161 offset:0\n\t" \
  "v_pk_fma_f16 v224, v224, %[hc], %[hc]\n\t" \
  "v_smfmac_f32_16x16x64_bf16 v[24:27], v[208:211], v[184:191], v248\n\t" \
  "ds_read_b64_tr_b16 v[222:223], v250 offset:19320\n\t" \
  "v_lshlrev_b32_sdwa v162, 1, v162 dst_sel:DWORD dst_unused:UNUSED_PAD src0_sel:DWORD src1_sel:WORD_1\n\t" \
  "v_pk_fma_f16 v226, v226, %[hc], %[hc]\n\t" \
  "v_smfmac_f32_16x16x64_bf16 v[48:51], v[208:211], v[192:199], v248\n\t" \
  "ds_read_u16_d16_hi v162, v162 offset:0\n\t" \
  "v_rcp_f16 v225, v224\n\t" \
  "v_smfmac_f32_16x16x64_bf16 v[72:75], v[208:211], v[200:207], v248\n\t" \
  "s_waitcnt lgkmcnt(9)\n\t" \
  "ds_read_b64_tr_b16 v[208:209], v250 offset:18608\n\t" \
  "v_lshlrev_b32_sdwa v163, 1, v163 dst_sel:DWORD dst_unused:UNUSED_PAD src0_sel:DWORD src1_sel:WORD_1\n\t" \
  "v_rcp_f16 v227, v226\n\t" \
  "v_smfmac_f32_16x16x64_bf16 v[4:7], v[212:215], v[176:183], v248\n\t" \
  "ds_read_u16_d16_hi v163, v163 offset:0\n\t" \
  "v_lshlrev_b32_sdwa v168, 1, v168 dst_sel:DWORD dst_unused:UNUSED_PAD src0_sel:DWORD src1_sel:WORD_1\n\t" \
  "v_smfmac_f32_16x16x64_bf16 v[28:31], v[212:215], v[184:191], v248\n\t" \
  "ds_read_b64_tr_b16 v[210:211], v250 offset:19376\n\t" \
  "v_rcp_f16_sdwa v224, v224 dst_sel:DWORD dst_unused:UNUSED_PAD src0_sel:WORD_1\n\t" \
  "v_smfmac_f32_16x16x64_bf16 v[52:55], v[212:215], v[192:199], v248\n\t" \
  "ds_read_u16_d16_hi v168, v168 offset:0\n\t" \
  "v_lshlrev_b32_sdwa v169, 1, v169 dst_sel:DWORD dst_unused:UNUSED_PAD src0_sel:DWORD src1_sel:WORD_1\n\t" \
  "v_smfmac_f32_16x16x64_bf16 v[76:79], v[212:215], v[200:207], v248\n\t" \
  "s_waitcnt lgkmcnt(9)\n\t" \
  "ds_read_b64_tr_b16 v[212:213], v250 offset:18616\n\t" \
  "v_rcp_f16_sdwa v226, v226 dst_sel:DWORD dst_unused:UNUSED_PAD src0_sel:WORD_1\n\t" \
  "v_smfmac_f32_16x16x64_bf16 v[8:11], v[216:219], v[176:183], v248\n\t" \
  "ds_read_u16_d16_hi v169, v169 offset:0\n\t" \
  "v_fma_mix_f32 v152, v152, v225, 0 op_sel_hi:[0,1,0]\n\t" \
  "v_smfmac_f32_16x16x64_bf16 v[32:35], v[216:219], v[184:191], v248\n\t" \
  "ds_read_b64_tr_b16 v[214:215], v250 offset:19384\n\t" \
  "v_lshlrev_b32_sdwa v170, 1, v170 dst_sel:DWORD dst_unused:UNUSED_PAD src0_sel:DWORD src1_sel:WORD_1\n\t" \
  "v_fma_mix_f32 v154, v154, v227, 0 op_sel_hi:[0,1,0]\n\t" \
  "v_smfmac_f32_16x16x64_bf16 v[56:59], v[216:219], v[192:199], v248\n\t" \
  "ds_read_u16_d16_hi v170, v170 offset:0\n\t" \
  "v_fma_mix_f32 v153, v153, v224, 0 op_sel_hi:[0,1,0]\n\t" \
  "v_smfmac_f32_16x16x64_bf16 v[80:83], v[216:219], v[200:207], v248\n\t" \
  "s_waitcnt lgkmcnt(9)\n\t" \
  "s_barrier\n\t" \
  "ds_read_b64_tr_b16 v[216:217], v249 offset:0\n\t" \
  "v_lshlrev_b32_sdwa v171, 1, v171 dst_sel:DWORD dst_unused:UNUSED_PAD src0_sel:DWORD src1_sel:WORD_1\n\t" \
  "v_fma_mix_f32 v155, v155, v226, 0 op_sel_hi:[0,1,0]\n\t" \
  "v_smfmac_f32_16x16x64_bf16 v[12:15], v[220:223], v[176:183], v248\n\t" \
  "ds_read_b128 v[132:135], v251 offset:0\n\t" \
  "v_cvt_pk_f16_f32 v224, v156, v157\n\t" \
  "v_cvt_pk_f16_f32 v226, v158, v159\n\t" \
  "v_smfmac_f32_16x16x64_bf16 v[36:39], v[220:223], v[184:191], v248\n\t" \
  "ds_read_b64_tr_b16 v[218:219], v249 offset:256\n\t" \
  "v_lshlrev_b32_sdwa v148, 1, v148 dst_sel:DWORD dst_unused:UNUSED_PAD src0_sel:DWORD src1_sel:WORD_1\n\t" \
  "v_pk_fma_f16 v225, v224, v224, %[h1] neg_lo:[1,0,0] neg_hi:[1,0,0]\n\t" \
  "v_smfmac_f32_16x16x64_bf16 v[60:63], v[220:223], v[192:199], v248\n\t" \
  "ds_read_b128 v[136:139], v251 offset:4096\n\t" \
  "v_pk_fma_f16 v227, v226, v226, %[h1] neg_lo:[1,0,0] neg_hi:[1,0,0]\n\t" \
  "v_smfmac_f32_16x16x64_bf16 v[84:87], v[220:223], v[200:207], v248\n\t" \
  "s_waitcnt lgkmcnt(9)\n\t" \
  "ds_read_b64_tr_b16 v[220:221], v249 offset:2048\n\t" \
  "v_lshlrev_b32_sdwa v149, 1, v149 dst_sel:DWORD dst_unused:UNUSED_PAD src0_sel:DWORD src1_sel:WORD_1\n\t" \
  "v_pk_mul_f16 v225, v224, v225\n\t" \
  "v_smfmac_f32_16x16x64_bf16 v[16:19], v[208:211], v[176:183], v248\n\t" \
  "ds_read_b128 v[140:143], v251 offset:8192\n\t" \
  "v_pk_mul_f16 v227, v226, v227\n\t" \
  "v_exp_f16 v224, v225\n\t" \
  "v_smfmac_f32_16x16x64_bf16 v[40:43], v[208:211], v[184:191], v248\n\t" \
  "ds_read_b64_tr_b16 v[222:223], v249 offset:2304\n\t" \
  "v_lshlrev_b32_sdwa v150, 1, v150 dst_sel:DWORD dst_unused:UNUSED_PAD src0_sel:DWORD src1_sel:WORD_1\n\t" \
  "v_smfmac_f32_16x16x64_bf16 v[64:67], v[208:211], v[192:199], v248\n\t" \
  "ds_read_u16_d16_hi v171, v171 offset:0\n\t" \
  "v_exp_f16 v226, v227\n\t" \
  "v_smfmac_f32_16x16x64_bf16 v[88:91], v[208:211], v[200:207], v248\n\t" \
  "s_waitcnt lgkmcnt(9)\n\t" \
  "ds_read_b64_tr_b16 v[208:209], v249 offset:4096\n\t" \
  "v_lshlrev_b32_sdwa v151, 1, v151 dst_sel:DWORD dst_unused:UNUSED_PAD src0_sel:DWORD src1_sel:WORD_1\n\t" \
  "v_smfmac_f32_16x16x64_bf16 v[20:23], v[212:215], v[176:183], v248\n\t" \
  "ds_read_u16_d16_hi v148, v148 offset:0\n\t" \
  "v_exp_f16_sdwa v224, v225 dst_sel:WORD_1 dst_unused:UNUSED_PRESERVE src0_sel:WORD_1\n\t" \
  "v_smfmac_f32_16x16x64_bf16 v[44:47], v[212:215], v[184:191], v248\n\t" \
  "ds_read_b64_tr_b16 v[210:211], v249 offset:4352\n\t" \
  "v_lshlrev_b32_sdwa v164, 1, v164 dst_sel:DWORD dst_unused:UNUSED_PAD src0_sel:DWORD src1_sel:WORD_1\n\t" \
  "v_exp_f16_sdwa v226, v227 dst_sel:WORD_1 dst_unused:UNUSED_PRESERVE src0_sel:WORD_1\n\t" \
  "v_smfmac_f32_16x16x64_bf16 v[68:71], v[212:215], v[192:199], v248\n\t" \
  "ds_read_u16_d16_hi v149, v149 offset:0\n\t" \
  "v_lshlrev_b32_sdwa v165, 1, v165 dst_sel:DWORD dst_unused:UNUSED_PAD src0_sel:DWORD src1_sel:WORD_1\n\t" \
  "v_smfmac_f32_16x16x64_bf16 v[92:95], v[212:215], v[200:207], v248\n\t" \
  "s_waitcnt lgkmcnt(9)\n\t" \
  "ds_read_b64_tr_b16 v[212:213], v249 offset:512\n\t" \
  "v_pk_fma_f16 v224, v224, %[hc], %[hc]\n\t" \
  "v_mfma_f32_16x16x32_bf16 v[176:179], v[216:219], v[96:99], 0\n\t" \
  "ds_read_u16_d16_hi v150, v150 offset:0\n\t" \
  "v_pk_fma_f16 v226, v226, %[hc], %[hc]\n\t" \
  "v_mfma_f32_16x16x32_bf16 v[184:187], v[216:219], v[108:111], 0\n\t" \
  "ds_read_b64_tr_b16 v[214:215], v249 offset:768\n\t" \
  "v_lshlrev_b32_sdwa v166, 1, v166 dst_sel:DWORD dst_unused:UNUSED_PAD src0_sel:DWORD src1_sel:WORD_1\n\t" \
  "v_rcp_f16 v225, v224\n\t" \
  "v_mfma_f32_16x16x32_bf16 v[192:195], v[216:219], v[120:123], 0\n\t" \
  "ds_read_u16_d16_hi v151, v151 offset:0\n\t" \
  "v_rcp_f16 v227, v226\n\t" \
  "v_mfma_f32_16x16x32_bf16 v[200:203], v[216:219], v[132:135], 0\n\t" \
  "s_waitcnt lgkmcnt(9)\n\t" \
  "ds_read_b64_tr_b16 v[216:217], v249 offset:2560\n\t" \
  "v_lshlrev_b32_sdwa v167, 1, v167 dst_sel:DWORD dst_unused:UNUSED_PAD src0_sel:DWORD src1_sel:WORD_1\n\t" \
  "v_mfma_f32_16x16x32_bf16 v[176:179], v[220:223], v[100:103], v[176:179]\n\t" \
  "ds_read_u16_d16_hi v164, v164 offset:0\n\t" \
  "v_rcp_f16_sdwa v224, v224 dst_sel:DWORD dst_unused:UNUSED_PAD src0_sel:WORD_1\n\t" \
  "v_mfma_f32_16x16x32_bf16 v[184:187], v[220:223], v[112:115], v[184:187]\n\t" \
  "ds_read_b64_tr_b16 v[218:219], v249 offset:2816\n\t" \
  "v_lshlrev_b32_sdwa v172, 1, v172 dst_sel:DWORD dst_unused:UNUSED_PAD src0_sel:DWORD src1_sel:WORD_1\n\t" \
  "v_mfma_f32_16x16x32_bf16 v[192:195], v[220:223], v[124:127], v[192:195]\n\t" \
  "ds_read_u16_d16_hi v165, v165 offset:0\n\t" \
  "v_rcp_f16_sdwa v226, v226 dst_sel:DWORD dst_unused:UNUSED_PAD src0_sel:WORD_1\n\t" \
  "v_mfma_f32_16x16x32_bf16 v[200:203], v[220:223], v[136:139], v[200:203]\n\t" \
  "s_waitcnt lgkmcnt(9)\n\t" \
  "ds_read_b64_tr_b16 v[220:221], v249 offset:4608\n\t" \
  "v_lshlrev_b32_sdwa v173, 1, v173 dst_sel:DWORD dst_unused:UNUSED_PAD src0_sel:DWORD src1_sel:WORD_1\n\t" \
  "v_fma_mix_f32 v156, v156, v225, 0 op_sel_hi:[0,1,0]\n\t" \
  "v_mfma_f32_16x16x32_bf16 v[176:179], v[208:211], v[104:107], v[176:179]\n\t" \
  "ds_read_u16_d16_hi v166, v166 offset:0\n\t" \
  "v_fma_mix_f32 v158, v158, v227, 0 op_sel_hi:[0,1,0]\n\t" \
  "v_mfma_f32_16x16x32_bf16 v[184:187], v[208:211], v[116:119], v[184:187]\n\t" \
  "ds_read_b64_tr_b16 v[222:223], v249 offset:4864\n\t" \
  "v_lshlrev_b32_sdwa v174, 1, v174 dst_sel:DWORD dst_unused:UNUSED_PAD src0_sel:DWORD src1_sel:WORD_1\n\t" \
  "v_fma_mix_f32 v157, v157, v224, 0 op_sel_hi:[0,1,0]\n\t" \
  "v_mfma_f32_16x16x32_bf16 v[192:195], v[208:211], v[128:131], v[192:195]\n\t" \
  "ds_read_u16_d16_hi v167, v167 offset:0\n\t" \
  "v_fma_mix_f32 v159, v159, v226, 0 op_sel_hi:[0,1,0]\n\t" \
  "v_mfma_f32_16x16x32_bf16 v[200:203], v[208:211], v[140:143], v[200:203]\n\t" \
  "s_waitcnt lgkmcnt(9)\n\t" \
  "ds_read_b64_tr_b16 v[208:209], v250 offset:0\n\t" \
  "v_lshlrev_b32_sdwa v175, 1, v175 dst_sel:DWORD dst_unused:UNUSED_PAD src0_sel:DWORD src1_sel:WORD_1\n\t" \
  "v_mfma_f32_16x16x32_bf16 v[180:183], v[212:215], v[96:99], 0\n\t" \
  "ds_read_u16_d16_hi v172, v172 offset:0\n\t" \
  "v_lshlrev_b32_sdwa v176, 1, v176 dst_sel:DWORD dst_unused:UNUSED_PAD src0_sel:DWORD src1_sel:WORD_1\n\t" \
  "v_mfma_f32_16x16x32_bf16 v[188:191], v[212:215], v[108:111], 0\n\t" \
  "ds_read_b64_tr_b16 v[210:211], v250 offset:768\n\t" \
  "ds_read_u16_d16_hi v173, v173 offset:0\n\t" \
  "v_cvt_pk_f16_f32 v224, v184, v185\n\t" \
  "v_cvt_pk_f16_f32 v226, v186, v187\n\t" \
  "v_mfma_f32_16x16x32_bf16 v[196:199], v[212:215], v[120:123], 0\n\t" \
  "ds_read_u16_d16_hi v174, v174 offset:0\n\t" \
  "v_pk_fma_f16 v225, v224, v224, %[h1] neg_lo:[1,0,0] neg_hi:[1,0,0]\n\t" \
  "v_mfma_f32_16x16x32_bf16 v[204:207], v[212:215], v[132:135], 0\n\t" \
  "s_waitcnt lgkmcnt(10)\n\t" \
  "ds_read_b64_tr_b16 v[212:213], v250 offset:8\n\t" \
  "v_lshlrev_b32_sdwa v177, 1, v177 dst_sel:DWORD dst_unused:UNUSED_PAD src0_sel:DWORD src1_sel:WORD_1\n\t" \
  "v_pk_fma_f16 v227, v226, v226, %[h1] neg_lo:[1,0,0] neg_hi:[1,0,0]\n\t" \
  "v_mfma_f32_16x16x32_bf16 v[180:183], v[216:219], v[100:103], v[180:183]\n\t" \
  "ds_read_u16_d16_hi v175, v175 offset:0\n\t" \
  "v_pk_mul_f16 v225, v224, v225\n\t" \
  "v_mfma_f32_16x16x32_bf16 v[188:191], v[216:219], v[112:115], v[188:191]\n\t" \
  "ds_read_b64_tr_b16 v[214:215], v250 offset:776\n\t" \
  "v_lshlrev_b32_sdwa v178, 1, v178 dst_sel:DWORD dst_unused:UNUSED_PAD src0_sel:DWORD src1_sel:WORD_1\n\t" \
  "v_pk_mul_f16 v227, v226, v227\n\t" \
  "v_exp_f16 v224, v225\n\t" \
  "v_mfma_f32_16x16x32_bf16 v[196:199], v[216:219], v[124:127], v[196:199]\n\t" \
  "ds_read_u16_d16_hi v176, v176 offset:0\n\t" \
  "v_lshlrev_b32_sdwa v179, 1, v179 dst_sel:DWORD dst_unused:UNUSED_PAD src0_sel:DWORD src1_sel:WORD_1\n\t" \
  "v_mfma_f32_16x16x32_bf16 v[204:207], v[216:219], v[136:139], v[204:207]\n\t" \
  "s_waitcnt lgkmcnt(10)\n\t" \
  "ds_read_b64_tr_b16 v[216:217], v250 offset:64\n\t" \
  "v_exp_f16 v226, v227\n\t" \
  "v_mfma_f32_16x16x32_bf16 v[180:183], v[220:223], v[104:107], v[180:183]\n\t" \
  "ds_read_u16_d16_hi v177, v177 offset:0\n\t" \
  "v_lshlrev_b32_sdwa v192, 1, v192 dst_sel:DWORD dst_unused:UNUSED_PAD src0_sel:DWORD src1_sel:WORD_1\n\t" \
  "v_mfma_f32_16x16x32_bf16 v[188:191], v[220:223], v[116:119], v[188:191]\n\t" \
  "ds_read_b64_tr_b16 v[218:219], v250 offset:832\n\t" \
  "v_exp_f16_sdwa v224, v225 dst_sel:WORD_1 dst_unused:UNUSED_PRESERVE src0_sel:WORD_1\n\t" \
  "v_mfma_f32_16x16x32_bf16 v[196:199], v[220:223], v[128:131], v[196:199]\n\t" \
  "ds_read_u16_d16_hi v178, v178 offset:0\n\t" \
  "v_exp_f16_sdwa v226, v227 dst_sel:WORD_1 dst_unused:UNUSED_PRESERVE src0_sel:WORD_1\n\t" \
  "v_mfma_f32_16x16x32_bf16 v[204:207], v[220:223], v[140:143], v[204:207]\n\t" \
  "s_waitcnt lgkmcnt(10)\n\t" \
  "ds_read_b64_tr_b16 v[220:221], v250 offset:72\n\t" \
  "v_lshlrev_b32_sdwa v193, 1, v193 dst_sel:DWORD dst_unused:UNUSED_PAD src0_sel:DWORD src1_sel:WORD_1\n\t" \
  "v_smfmac_f32_16x16x64_bf16 v[0:3], v[208:211], v[144:151], v248\n\t" \
  "ds_read_u16_d16_hi v179, v179 offset:0\n\t" \
  "v_pk_fma_f16 v224, v224, %[hc], %[hc]\n\t" \
  "v_smfmac_f32_16x16x64_bf16 v[24:27], v[208:211], v[152:159], v248\n\t" \
  "ds_read_b64_tr_b16 v[222:223], v250 offset:840\n\t" \
  "v_lshlrev_b32_sdwa v194, 1, v194 dst_sel:DWORD dst_unused:UNUSED_PAD src0_sel:DWORD src1_sel:WORD_1\n\t" \
  "v_pk_fma_f16 v226, v226, %[hc], %[hc]\n\t" \
  "v_smfmac_f32_16x16x64_bf16 v[48:51], v[208:211], v[160:167], v248\n\t" \
  "s_waitcnt lgkmcnt(9)\n\t" \
  "ds_read_u16_d16_hi v192, v192 offset:0\n\t" \
  "v_rcp_f16 v225, v224\n\t" \
  "v_smfmac_f32_16x16x64_bf16 v[72:75], v[208:211], v[168:175], v248\n\t" \
  "s_waitcnt lgkmcnt(9)\n\t" \
  "ds_read_b64_tr_b16 v[208:209], v250 offset:128\n\t" \
  "v_lshlrev_b32_sdwa v195, 1, v195 dst_sel:DWORD dst_unused:UNUSED_PAD src0_sel:DWORD src1_sel:WORD_1\n\t" \
  "v_rcp_f16 v227, v226\n\t" \
  "v_smfmac_f32_16x16x64_bf16 v[4:7], v[212:215], v[144:151], v248\n\t" \
  "ds_read_b64 v[242:243], v252 offset:128\n\t" \
  "v_lshlrev_b32_sdwa v200, 1, v200 dst_sel:DWORD dst_unused:UNUSED_PAD src0_sel:DWORD src1_sel:WORD_1\n\t" \
  "v_smfmac_f32_16x16x64_bf16 v[28:31], v[212:215], v[152:159], v248\n\t" \
  "ds_read_b64_tr_b16 v[210:211], v250 offset:896\n\t" \
  "v_rcp_f16_sdwa v224, v224 dst_sel:DWORD dst_unused:UNUSED_PAD src0_sel:WORD_1\n\t" \
  "v_smfmac_f32_16x16x64_bf16 v[52:55], v[212:215], v[160:167], v248\n\t" \
  "ds_read_b64 v[244:245], v252 offset:136\n\t" \
  "v_lshlrev_b32_sdwa v201, 1, v201 dst_sel:DWORD dst_unused:UNUSED_PAD src0_sel:DWORD src1_sel:WORD_1\n\t" \
  "v_smfmac_f32_16x16x64_bf16 v[76:79], v[212:215], v[168:175], v248\n\t" \
  "s_waitcnt lgkmcnt(9)\n\t" \
  "ds_read_b64_tr_b16 v[212:213], v250 offset:136\n\t" \
  "ds_read_u16_d16_hi v193, v193 offset:0\n\t" \
  "v_rcp_f16_sdwa v226, v226 dst_sel:DWORD dst_unused:UNUSED_PAD src0_sel:WORD_1\n\t" \
  "v_smfmac_f32_16x16x64_bf16 v[8:11], v[216:219], v[144:151], v248\n\t" \
  "ds_read_b64_tr_b16 v[214:215], v250 offset:904\n\t" \
  "v_fma_mix_f32 v184, v184, v225, 0 op_sel_hi:[0,1,0]\n\t" \
  "v_smfmac_f32_16x16x64_bf16 v[32:35], v[216:219], v[152:159], v248\n\t" \
  "ds_read_u16_d16_hi v194, v194 offset:0\n\t" \
  "v_lshlrev_b32_sdwa v202, 1, v202 dst_sel:DWORD dst_unused:UNUSED_PAD src0_sel:DWORD src1_sel:WORD_1\n\t" \
  "v_fma_mix_f32 v186, v186, v227, 0 op_sel_hi:[0,1,0]\n\t" \
  "v_cvt_pk_bf16_f32 v230, v72, v73\n\t" \
  "v_smfmac_f32_16x16x64_bf16 v[56:59], v[216:219], v[160:167], v248\n\t" \
  "ds_read_u16_d16_hi v195, v195 offset:0\n\t" \
  "v_fma_mix_f32 v185, v185, v224, 0 op_sel_hi:[0,1,0]\n\t" \
  "v_cvt_pk_bf16_f32 v231, v74, v75\n\t" \
  "v_smfmac_f32_16x16x64_bf16 v[80:83], v[216:219], v[168:175], v248\n\t" \
  "s_waitcnt lgkmcnt(10)\n\t" \
  "s_barrier\n\t" \
  "ds_read_b64_tr_b16 v[216:217], v249 offset:6144\n\t" \
  "v_lshlrev_b32_sdwa v203, 1, v203 dst_sel:DWORD dst_unused:UNUSED_PAD src0_sel:DWORD src1_sel:WORD_1\n\t" \
  "v_fma_mix_f32 v187, v187, v226, 0 op_sel_hi:[0,1,0]\n\t" \
  "v_cvt_pk_bf16_f32 v232, v76, v77\n\t" \
  "v_smfmac_f32_16x16x64_bf16 v[12:15], v[220:223], v[144:151], v248\n\t" \
  "ds_read_u16_d16_hi v200, v200 offset:0\n\t" \
  "v_cvt_pk_f16_f32 v224, v188, v189\n\t" \
  "v_cvt_pk_f16_f32 v226, v190, v191\n\t" \
  "v_cvt_pk_bf16_f32 v233, v78, v79\n\t" \
  "v_smfmac_f32_16x16x64_bf16 v[36:39], v[220:223], v[152:159], v248\n\t" \
  "ds_read_b64_tr_b16 v[218:219], v249 offset:6400\n\t" \
  "buffer_store_dwordx4 v[230:233], v253, %[osrd], %[ob] offen offset:0 sc1\n\t" \
  "v_lshlrev_b32_sdwa v180, 1, v180 dst_sel:DWORD dst_unused:UNUSED_PAD src0_sel:DWORD src1_sel:WORD_1\n\t" \
  "v_pk_fma_f16 v225, v224, v224, %[h1] neg_lo:[1,0,0] neg_hi:[1,0,0]\n\t" \
  "v_smfmac_f32_16x16x64_bf16 v[60:63], v[220:223], v[160:167], v248\n\t" \
  "ds_read_b64 v[230:231], v252 offset:0\n\t" \
  "v_pk_fma_f16 v227, v226, v226, %[h1] neg_lo:[1,0,0] neg_hi:[1,0,0]\n\t" \
  "v_smfmac_f32_16x16x64_bf16 v[84:87], v[220:223], v[168:175], v248\n\t" \
  "s_waitcnt lgkmcnt(10)\n\t" \
  "ds_read_b64_tr_b16 v[220:221], v249 offset:8192\n\t" \
  "ds_read_b64 v[232:233], v252 offset:8\n\t" \
  "v_lshlrev_b32_sdwa v181, 1, v181 dst_sel:DWORD dst_unused:UNUSED_PAD src0_sel:DWORD src1_sel:WORD_1\n\t" \
  "v_pk_mul_f16 v225, v224, v225\n\t" \
  "v_smfmac_f32_16x16x64_bf16 v[16:19], v[208:211], v[144:151], v248\n\t" \
  "ds_read_b64_tr_b16 v[222:223], v249 offset:8448\n\t" \
  "v_pk_mul_f16 v227, v226, v227\n\t" \
  "v_exp_f16 v224, v225\n\t" \
  "v_smfmac_f32_16x16x64_bf16 v[40:43], v[208:211], v[152:159], v248\n\t" \
  "ds_read_u16_d16_hi v201, v201 offset:0\n\t" \
  "v_lshlrev_b32_sdwa v182, 1, v182 dst_sel:DWORD dst_unused:UNUSED_PAD src0_sel:DWORD src1_sel:WORD_1\n\t" \
  "v_cvt_pk_bf16_f32 v234, v80, v81\n\t" \
  "v_smfmac_f32_16x16x64_bf16 v[64:67], v[208:211], v[160:167], v248\n\t" \
  "ds_read_u16_d16_hi v202, v202 offset:0\n\t" \
  "v_exp_f16 v226, v227\n\t" \
  "v_cvt_pk_bf16_f32 v235, v82, v83\n\t" \
  "v_smfmac_f32_16x16x64_bf16 v[88:91], v[208:211], v[168:175], v248\n\t" \
  "s_waitcnt lgkmcnt(11)\n\t" \
  "ds_read_b64_tr_b16 v[208:209], v249 offset:10240\n\t" \
  "v_lshlrev_b32_sdwa v183, 1, v183 dst_sel:DWORD dst_unused:UNUSED_PAD src0_sel:DWORD src1_sel:WORD_1\n\t" \
  "v_cvt_pk_bf16_f32 v236, v84, v85\n\t" \
  "v_smfmac_f32_16x16x64_bf16 v[20:23], v[212:215], v[144:151], v248\n\t" \
  "ds_read_u16_d16_hi v203, v203 offset:0\n\t" \
  "v_exp_f16_sdwa v224, v225 dst_sel:WORD_1 dst_unused:UNUSED_PRESERVE src0_sel:WORD_1\n\t" \
  "v_cvt_pk_bf16_f32 v237, v86, v87\n\t" \
  "v_smfmac_f32_16x16x64_bf16 v[44:47], v[212:215], v[152:159], v248\n\t" \
  "ds_read_b64_tr_b16 v[210:211], v249 offset:10496\n\t" \
  "buffer_store_dwordx4 v[234:237], v253, %[osrd], %[ob] offen offset:64 sc1\n\t" \
  "v_lshlrev_b32_sdwa v196, 1, v196 dst_sel:DWORD dst_unused:UNUSED_PAD src0_sel:DWORD src1_sel:WORD_1\n\t" \
  "v_exp_f16_sdwa v226, v227 dst_sel:WORD_1 dst_unused:UNUSED_PRESERVE src0_sel:WORD_1\n\t" \
  "v_smfmac_f32_16x16x64_bf16 v[68:71], v[212:215], v[160:167], v248\n\t" \
  "ds_read_b64 v[234:235], v252 offset:64\n\t" \
  "ds_read_b64 v[236:237], v252 offset:72\n\t" \
  "v_smfmac_f32_16x16x64_bf16 v[92:95], v[212:215], v[168:175], v248\n\t" \
  "s_waitcnt lgkmcnt(10)\n\t" \
  "ds_read_b64_tr_b16 v[212:213], v249 offset:6656\n\t" \
  "ds_read_u16_d16_hi v180, v180 offset:0\n\t" \
  "v_mfma_f32_16x16x16_bf16 v[72:75], v[246:247], v[230:231], 0\n\t" \
  "ds_read_b64_tr_b16 v[214:215], v249 offset:6912\n\t" \
  "v_lshlrev_b32_sdwa v197, 1, v197 dst_sel:DWORD dst_unused:UNUSED_PAD src0_sel:DWORD src1_sel:WORD_1\n\t" \
  "v_pk_fma_f16 v224, v224, %[hc], %[hc]\n\t" \
  "v_mfma_f32_16x16x32_bf16 v[144:147], v[216:219], v[96:99], 0\n\t" \
  "ds_read_u16_d16_hi v181, v181 offset:0\n\t" \
  "v_pk_fma_f16 v226, v226, %[hc], %[hc]\n\t" \
  "v_mfma_f32_16x16x32_bf16 v[152:155], v[216:219], v[108:111], 0\n\t" \
  "ds_read_u16_d16_hi v182, v182 offset:0\n\t" \
  "v_lshlrev_b32_sdwa v198, 1, v198 dst_sel:DWORD dst_unused:UNUSED_PAD src0_sel:DWORD src1_sel:WORD_1\n\t" \
  "v_rcp_f16 v225, v224\n\t" \
  "v_cvt_pk_bf16_f32 v238, v88, v89\n\t" \
  "v_mfma_f32_16x16x32_bf16 v[160:163], v[216:219], v[120:123], 0\n\t" \
  "s_waitcnt lgkmcnt(13)\n\t" \
  "ds_read_u16_d16_hi v183, v183 offset:0\n\t" \
  "v_rcp_f16 v227, v226\n\t" \
  "v_mfma_f32_16x16x16_bf16 v[76:79], v[246:247], v[232:233], 0\n\t" \
  "ds_read_u16_d16_hi v196, v196 offset:0\n\t" \
  "v_cvt_pk_bf16_f32 v239, v90, v91\n\t" \
  "v_mfma_f32_16x16x32_bf16 v[168:171], v[216:219], v[132:135], 0\n\t" \
  "s_waitcnt lgkmcnt(14)\n\t" \
  "ds_read_b64_tr_b16 v[216:217], v249 offset:8704\n\t" \
  "v_lshlrev_b32_sdwa v199, 1, v199 dst_sel:DWORD dst_unused:UNUSED_PAD src0_sel:DWORD src1_sel:WORD_1\n\t" \
  "v_cvt_pk_bf16_f32 v240, v92, v93\n\t" \
  "v_mfma_f32_16x16x32_bf16 v[144:147], v[220:223], v[100:103], v[144:147]\n\t" \
  "ds_read_u16_d16_hi v197, v197 offset:0\n\t" \
  "v_rcp_f16_sdwa v224, v224 dst_sel:DWORD dst_unused:UNUSED_PAD src0_sel:WORD_1\n\t" \
  "v_cvt_pk_bf16_f32 v241, v94, v95\n\t" \
  "v_mfma_f32_16x16x32_bf16 v[152:155], v[220:223], v[112:115], v[152:155]\n\t" \
  "ds_read_b64_tr_b16 v[218:219], v249 offset:8960\n\t" \
  "v_lshlrev_b32_sdwa v204, 1, v204 dst_sel:DWORD dst_unused:UNUSED_PAD src0_sel:DWORD src1_sel:WORD_1\n\t" \
  "v_mfma_f32_16x16x32_bf16 v[160:163], v[220:223], v[124:127], v[160:163]\n\t" \
  "buffer_store_dwordx4 v[238:241], v253, %[osrd], %[ob] offen offset:128 sc1\n\t" \
  "v_rcp_f16_sdwa v226, v226 dst_sel:DWORD dst_unused:UNUSED_PAD src0_sel:WORD_1\n\t" \
  "v_mfma_f32_16x16x32_bf16 v[168:171], v[220:223], v[136:139], v[168:171]\n\t" \
  "s_waitcnt lgkmcnt(11)\n\t" \
  "ds_read_b64_tr_b16 v[220:221], v249 offset:10752\n\t" \
  "v_lshlrev_b32_sdwa v205, 1, v205 dst_sel:DWORD dst_unused:UNUSED_PAD src0_sel:DWORD src1_sel:WORD_1\n\t" \
  "v_mfma_f32_16x16x16_bf16 v[80:83], v[246:247], v[234:235], 0\n\t" \
  "ds_read_u16_d16_hi v198, v198 offset:0\n\t" \
  "v_fma_mix_f32 v188, v188, v225, 0 op_sel_hi:[0,1,0]\n\t" \
  "v_mfma_f32_16x16x32_bf16 v[144:147], v[208:211], v[104:107], v[144:147]\n\t" \
  "ds_read_b64_tr_b16 v[222:223], v249 offset:11008\n\t" \
  "v_fma_mix_f32 v190, v190, v227, 0 op_sel_hi:[0,1,0]\n\t" \
  "v_mfma_f32_16x16x32_bf16 v[152:155], v[208:211], v[116:119], v[152:155]\n\t" \
  "ds_read_u16_d16_hi v199, v199 offset:0\n\t" \
  "v_lshlrev_b32_sdwa v206, 1, v206 dst_sel:DWORD dst_unused:UNUSED_PAD src0_sel:DWORD src1_sel:WORD_1\n\t" \
  "v_fma_mix_f32 v189, v189, v224, 0 op_sel_hi:[0,1,0]\n\t" \
  "v_mfma_f32_16x16x32_bf16 v[160:163], v[208:211], v[128:131], v[160:163]\n\t" \
  "s_waitcnt lgkmcnt(14)\n\t" \
  "ds_read_u16_d16_hi v204, v204 offset:0\n\t" \
  "v_fma_mix_f32 v191, v191, v226, 0 op_sel_hi:[0,1,0]\n\t" \
  "v_mfma_f32_16x16x16_bf16 v[84:87], v[246:247], v[236:237], 0\n\t" \
  "ds_read_u16_d16_hi v205, v205 offset:0\n\t" \
  "v_lshlrev_b32_sdwa v207, 1, v207 dst_sel:DWORD dst_unused:UNUSED_PAD src0_sel:DWORD src1_sel:WORD_1\n\t" \
  "v_mfma_f32_16x16x32_bf16 v[168:171], v[208:211], v[140:143], v[168:171]\n\t" \
  "s_waitcnt lgkmcnt(13)\n\t" \
  "ds_read_b64_tr_b16 v[208:209], v250 offset:6160\n\t" \
  "v_lshlrev_b32_sdwa v144, 1, v144 dst_sel:DWORD dst_unused:UNUSED_PAD src0_sel:DWORD src1_sel:WORD_1\n\t" \
  "v_mfma_f32_16x16x32_bf16 v[148:151], v[212:215], v[96:99], 0\n\t" \
  "ds_read_u16_d16_hi v206, v206 offset:0\n\t" \
  "v_cvt_pk_f16_f32 v224, v152, v153\n\t" \
  "v_mfma_f32_16x16x32_bf16 v[156:159], v[212:215], v[108:111], 0\n\t" \
  "ds_read_b64_tr_b16 v[210:211], v250 offset:6928\n\t" \
  "v_cvt_pk_f16_f32 v226, v154, v155\n\t" \
  "v_mfma_f32_16x16x32_bf16 v[164:167], v[212:215], v[120:123], 0\n\t" \
  "ds_read_u16_d16_hi v207, v207 offset:0\n\t" \
  "v_pk_fma_f16 v225, v224, v224, %[h1] neg_lo:[1,0,0] neg_hi:[1,0,0]\n\t" \
  "v_mfma_f32_16x16x32_bf16 v[172:175], v[212:215], v[132:135], 0\n\t" \
  "ds_read_b64_tr_b16 v[212:213], v250 offset:6168\n\t" \
  "v_lshlrev_b32_sdwa v145, 1, v145 dst_sel:DWORD dst_unused:UNUSED_PAD src0_sel:DWORD src1_sel:WORD_1\n\t" \
  "v_mfma_f32_16x16x16_bf16 v[88:91], v[246:247], v[242:243], 0\n\t" \
  "s_waitcnt lgkmcnt(11)\n\t" \
  "ds_read_u16_d16_hi v144, v144 offset:0\n\t" \
  "v_pk_fma_f16 v227, v226, v226, %[h1] neg_lo:[1,0,0] neg_hi:[1,0,0]\n\t" \
  "v_mfma_f32_16x16x32_bf16 v[148:151], v[216:219], v[100:103], v[148:151]\n\t" \
  "ds_read_b64_tr_b16 v[214:215], v250 offset:6936\n\t" \
  "v_pk_mul_f16 v225, v224, v225\n\t" \
  "v_mfma_f32_16x16x32_bf16 v[156:159], v[216:219], v[112:115], v[156:159]\n\t" \
  "ds_read_u16_d16_hi v145, v145 offset:0\n\t" \
  "v_lshlrev_b32_sdwa v146, 1, v146 dst_sel:DWORD dst_unused:UNUSED_PAD src0_sel:DWORD src1_sel:WORD_1\n\t" \
  "v_pk_mul_f16 v227, v226, v227\n\t" \
  "v_exp_f16 v224, v225\n\t" \
  "v_mfma_f32_16x16x32_bf16 v[164:167], v[216:219], v[124:127], v[164:167]\n\t" \
  "ds_read_u16_d16_hi v146, v146 offset:0\n\t" \
  "v_lshlrev_b32_sdwa v147, 1, v147 dst_sel:DWORD dst_unused:UNUSED_PAD src0_sel:DWORD src1_sel:WORD_1\n\t" \
  "v_mfma_f32_16x16x16_bf16 v[92:95], v[246:247], v[244:245], 0\n\t" \
  "ds_read_u16_d16_hi v147, v147 offset:0\n\t" \
  "v_exp_f16 v226, v227\n\t" \
  "v_mfma_f32_16x16x32_bf16 v[172:175], v[216:219], v[136:139], v[172:175]\n\t" \
  "s_waitcnt lgkmcnt(13)\n\t" \
  "ds_read_b64_tr_b16 v[216:217], v250 offset:6224\n\t" \
  "v_lshlrev_b32_sdwa v160, 1, v160 dst_sel:DWORD dst_unused:UNUSED_PAD src0_sel:DWORD src1_sel:WORD_1\n\t" \
  "v_mfma_f32_16x16x32_bf16 v[148:151], v[220:223], v[104:107], v[148:151]\n\t" \
  "ds_read_u16_d16_hi v160, v160 offset:0\n\t" \
  "v_exp_f16_sdwa v224, v225 dst_sel:WORD_1 dst_unused:UNUSED_PRESERVE src0_sel:WORD_1\n\t" \
  "v_mfma_f32_16x16x32_bf16 v[156:159], v[220:223], v[116:119], v[156:159]\n\t" \
  "ds_read_b64_tr_b16 v[218:219], v250 offset:6992\n\t" \
  "v_exp_f16_sdwa v226, v227 dst_sel:WORD_1 dst_unused:UNUSED_PRESERVE src0_sel:WORD_1\n\t" \
  "v_mfma_f32_16x16x32_bf16 v[164:167], v[220:223], v[128:131], v[164:167]\n\t" \
  "v_lshlrev_b32_sdwa v161, 1, v161 dst_sel:DWORD dst_unused:UNUSED_PAD src0_sel:DWORD src1_sel:WORD_1\n\t" \
  "v_pk_fma_f16 v224, v224, %[hc], %[hc]\n\t" \
  "v_mfma_f32_16x16x32_bf16 v[172:175], v[220:223], v[140:143], v[172:175]\n\t" \
  "s_waitcnt lgkmcnt(10)\n\t" \
  "ds_read_u16_d16_hi v161, v161 offset:0\n\t" \
  "v_lshlrev_b32_sdwa v162, 1, v162 dst_sel:DWORD dst_unused:UNUSED_PAD src0_sel:DWORD src1_sel:WORD_1\n\t" \
  "v_smfmac_f32_16x16x64_bf16 v[0:3], v[208:211], v[176:183], v248\n\t" \
  "ds_read_b64_tr_b16 v[220:221], v250 offset:6232\n\t" \
  "v_pk_fma_f16 v226, v226, %[hc], %[hc]\n\t" \
  "v_smfmac_f32_16x16x64_bf16 v[24:27], v[208:211], v[184:191], v248\n\t" \
  "ds_read_u16_d16_hi v162, v162 offset:0\n\t" \
  "v_rcp_f16 v225, v224\n\t" \
  "v_smfmac_f32_16x16x64_bf16 v[48:51], v[208:211], v[192:199], v248\n\t" \
  "s_waitcnt lgkmcnt(12)\n\t" \
  "ds_read_b64_tr_b16 v[222:223], v250 offset:7000\n\t" \
  "v_lshlrev_b32_sdwa v163, 1, v163 dst_sel:DWORD dst_unused:UNUSED_PAD src0_sel:DWORD src1_sel:WORD_1\n\t" \
  "v_smfmac_f32_16x16x64_bf16 v[72:75], v[208:211], v[200:207], v248\n\t" \
  "s_waitcnt lgkmcnt(10)\n\t" \
  "ds_read_u16_d16_hi v163, v163 offset:0\n\t" \
  "v_rcp_f16 v227, v226\n\t" \
  "v_smfmac_f32_16x16x64_bf16 v[4:7], v[212:215], v[176:183], v248\n\t" \
  "ds_read_b64_tr_b16 v[208:209], v250 offset:6288\n\t" \
  "v_lshlrev_b32_sdwa v168, 1, v168 dst_sel:DWORD dst_unused:UNUSED_PAD src0_sel:DWORD src1_sel:WORD_1\n\t" \
  "v_smfmac_f32_16x16x64_bf16 v[28:31], v[212:215], v[184:191], v248\n\t" \
  "s_add_u32 %[ob], %[ob], %[dob]\n\t" \
  "ds_read_u16_d16_hi v168, v168 offset:0\n\t" \
  "v_rcp_f16_sdwa v224, v224 dst_sel:DWORD dst_unused:UNUSED_PAD src0_sel:WORD_1\n\t" \
  "v_smfmac_f32_16x16x64_bf16 v[52:55], v[212:215], v[192:199], v248\n\t" \
  "ds_read_b64_tr_b16 v[210:211], v250 offset:7056\n\t" \
  "v_lshlrev_b32_sdwa v169, 1, v169 dst_sel:DWORD dst_unused:UNUSED_PAD src0_sel:DWORD src1_sel:WORD_1\n\t" \
  "v_smfmac_f32_16x16x64_bf16 v[76:79], v[212:215], v[200:207], v248\n\t" \
  "s_waitcnt lgkmcnt(8)\n\t" \
  "ds_read_u16_d16_hi v169, v169 offset:0\n\t" \
  "v_rcp_f16_sdwa v226, v226 dst_sel:DWORD dst_unused:UNUSED_PAD src0_sel:WORD_1\n\t" \
  "v_smfmac_f32_16x16x64_bf16 v[8:11], v[216:219], v[176:183], v248\n\t" \
  "ds_read_b64_tr_b16 v[212:213], v250 offset:6296\n\t" \
  "v_fma_mix_f32 v152, v152, v225, 0 op_sel_hi:[0,1,0]\n\t" \
  "v_smfmac_f32_16x16x64_bf16 v[32:35], v[216:219], v[184:191], v248\n\t" \
  "ds_read_b64_tr_b16 v[214:215], v250 offset:7064\n\t" \
  "v_lshlrev_b32_sdwa v170, 1, v170 dst_sel:DWORD dst_unused:UNUSED_PAD src0_sel:DWORD src1_sel:WORD_1\n\t" \
  "v_fma_mix_f32 v154, v154, v227, 0 op_sel_hi:[0,1,0]\n\t" \
  "v_smfmac_f32_16x16x64_bf16 v[56:59], v[216:219], v[192:199], v248\n\t" \
  "ds_read_u16_d16_hi v170, v170 offset:0\n\t" \
  "v_fma_mix_f32 v153, v153, v224, 0 op_sel_hi:[0,1,0]\n\t" \
  "v_smfmac_f32_16x16x64_bf16 v[80:83], v[216:219], v[200:207], v248\n\t" \
  "s_waitcnt lgkmcnt(8)\n\t" \
  "s_barrier\n\t" \
  "s_sub_u32 %[lc], %[lc], 1\n\t" \
  "s_cmp_lg_u32 %[lc], 0\n\t" \
  "s_cbranch_scc1 .Lcnx_m\n\t" \
  "ds_read_b64_tr_b16 v[216:217], v249 offset:12288\n\t" \
  "v_lshlrev_b32_sdwa v171, 1, v171 dst_sel:DWORD dst_unused:UNUSED_PAD src0_sel:DWORD src1_sel:WORD_1\n\t" \
  "v_fma_mix_f32 v155, v155, v226, 0 op_sel_hi:[0,1,0]\n\t" \
  "v_smfmac_f32_16x16x64_bf16 v[12:15], v[220:223], v[176:183], v248\n\t" \
  "ds_read_u16_d16_hi v171, v171 offset:0\n\t" \
  "v_cvt_pk_f16_f32 v224, v156, v157\n\t" \
  "v_cvt_pk_f16_f32 v226, v158, v159\n\t" \
  "v_smfmac_f32_16x16x64_bf16 v[36:39], v[220:223], v[184:191], v248\n\t" \
  "ds_read_b64_tr_b16 v[218:219], v249 offset:12544\n\t" \
  "v_lshlrev_b32_sdwa v148, 1, v148 dst_sel:DWORD dst_unused:UNUSED_PAD src0_sel:DWORD src1_sel:WORD_1\n\t" \
  "v_pk_fma_f16 v225, v224, v224, %[h1] neg_lo:[1,0,0] neg_hi:[1,0,0]\n\t" \
  "v_smfmac_f32_16x16x64_bf16 v[60:63], v[220:223], v[192:199], v248\n\t" \
  "ds_read_u16_d16_hi v148, v148 offset:0\n\t" \
  "v_pk_fma_f16 v227, v226, v226, %[h1] neg_lo:[1,0,0] neg_hi:[1,0,0]\n\t" \
  "v_smfmac_f32_16x16x64_bf16 v[84:87], v[220:223], v[200:207], v248\n\t" \
  "s_waitcnt lgkmcnt(8)\n\t" \
  "ds_read_b64_tr_b16 v[220:221], v249 offset:14336\n\t" \
  "v_lshlrev_b32_sdwa v149, 1, v149 dst_sel:DWORD dst_unused:UNUSED_PAD src0_sel:DWORD src1_sel:WORD_1\n\t" \
  "v_pk_mul_f16 v225, v224, v225\n\t" \
  "v_smfmac_f32_16x16x64_bf16 v[16:19], v[208:211], v[176:183], v248\n\t" \
  "s_add_u32 %[rs], %[rs], 1\n\t" \
  "ds_read_u16_d16_hi v149, v149 offset:0\n\t" \
  "v_pk_mul_f16 v227, v226, v227\n\t" \
  "v_exp_f16 v224, v225\n\t" \
  "v_smfmac_f32_16x16x64_bf16 v[40:43], v[208:211], v[184:191], v248\n\t" \
  "s_cmp_eq_u32 %[rs], 6\n\t" \
  "ds_read_b64_tr_b16 v[222:223], v249 offset:14592\n\t" \
  "v_lshlrev_b32_sdwa v150, 1, v150 dst_sel:DWORD dst_unused:UNUSED_PAD src0_sel:DWORD src1_sel:WORD_1\n\t" \
  "v_smfmac_f32_16x16x64_bf16 v[64:67], v[208:211], v[192:199], v248\n\t" \
  "ds_read_u16_d16_hi v150, v150 offset:0\n\t" \
  "v_exp_f16 v226, v227\n\t" \
  "v_smfmac_f32_16x16x64_bf16 v[88:91], v[208:211], v[200:207], v248\n\t" \
  "s_waitcnt lgkmcnt(9)\n\t" \
  "ds_read_b64_tr_b16 v[208:209], v249 offset:16384\n\t" \
  "v_lshlrev_b32_sdwa v151, 1, v151 dst_sel:DWORD dst_unused:UNUSED_PAD src0_sel:DWORD src1_sel:WORD_1\n\t" \
  "v_smfmac_f32_16x16x64_bf16 v[20:23], v[212:215], v[176:183], v248\n\t" \
  "s_cselect_b32 %[rs], 0, %[rs]\n\t" \
  "ds_read_u16_d16_hi v151, v151 offset:0\n\t" \
  "v_exp_f16_sdwa v224, v225 dst_sel:WORD_1 dst_unused:UNUSED_PRESERVE src0_sel:WORD_1\n\t" \
  "v_smfmac_f32_16x16x64_bf16 v[44:47], v[212:215], v[184:191], v248\n\t" \
  "s_cselect_b32 %[t0], -75920, %[ps]\n\t" \
  "ds_read_b64_tr_b16 v[210:211], v249 offset:16640\n\t" \
  "v_lshlrev_b32_sdwa v164, 1, v164 dst_sel:DWORD dst_unused:UNUSED_PAD src0_sel:DWORD src1_sel:WORD_1\n\t" \
  "v_exp_f16_sdwa v226, v227 dst_sel:WORD_1 dst_unused:UNUSED_PRESERVE src0_sel:WORD_1\n\t" \
  "v_smfmac_f32_16x16x64_bf16 v[68:71], v[212:215], v[192:199], v248\n\t" \
  "ds_read_u16_d16_hi v164, v164 offset:0\n\t" \
  "v_add_u32 v252, %[t0], v252\n\t" \
  "v_smfmac_f32_16x16x64_bf16 v[92:95], v[212:215], v[200:207], v248\n\t" \
  "s_waitcnt lgkmcnt(9)\n\t" \
  "ds_read_b64_tr_b16 v[212:213], v249 offset:12800\n\t" \
  "v_lshlrev_b32_sdwa v165, 1, v165 dst_sel:DWORD dst_unused:UNUSED_PAD src0_sel:DWORD src1_sel:WORD_1\n\t" \
  "v_pk_fma_f16 v224, v224, %[hc], %[hc]\n\t" \
  "v_mfma_f32_16x16x32_bf16 v[176:179], v[216:219], v[96:99], 0\n\t" \
  "ds_read_u16_d16_hi v165, v165 offset:0\n\t" \
  "v_pk_fma_f16 v226, v226, %[hc], %[hc]\n\t" \
  "v_mfma_f32_16x16x32_bf16 v[184:187], v[216:219], v[108:111], 0\n\t" \
  "ds_read_b64_tr_b16 v[214:215], v249 offset:13056\n\t" \
  "v_lshlrev_b32_sdwa v166, 1, v166 dst_sel:DWORD dst_unused:UNUSED_PAD src0_sel:DWORD src1_sel:WORD_1\n\t" \
  "v_rcp_f16 v225, v224\n\t" \
  "v_mfma_f32_16x16x32_bf16 v[192:195], v[216:219], v[120:123], 0\n\t" \
  "ds_read_u16_d16_hi v166, v166 offset:0\n\t" \
  "v_rcp_f16 v227, v226\n\t" \
  "v_mfma_f32_16x16x32_bf16 v[200:203], v[216:219], v[132:135], 0\n\t" \
  "s_waitcnt lgkmcnt(9)\n\t" \
  "ds_read_b64_tr_b16 v[216:217], v249 offset:14848\n\t" \
  "v_lshlrev_b32_sdwa v167, 1, v167 dst_sel:DWORD dst_unused:UNUSED_PAD src0_sel:DWORD src1_sel:WORD_1\n\t" \
  "v_mfma_f32_16x16x32_bf16 v[176:179], v[220:223], v[100:103], v[176:179]\n\t" \
  "ds_read_u16_d16_hi v167, v167 offset:0\n\t" \
  "v_rcp_f16_sdwa v224, v224 dst_sel:DWORD dst_unused:UNUSED_PAD src0_sel:WORD_1\n\t" \
  "v_mfma_f32_16x16x32_bf16 v[184:187], v[220:223], v[112:115], v[184:187]\n\t" \
  "ds_read_b64_tr_b16 v[218:219], v249 offset:15104\n\t" \
  "v_lshlrev_b32_sdwa v172, 1, v172 dst_sel:DWORD dst_unused:UNUSED_PAD src0_sel:DWORD src1_sel:WORD_1\n\t" \
  "v_mfma_f32_16x16x32_bf16 v[192:195], v[220:223], v[124:127], v[192:195]\n\t" \
  "ds_read_u16_d16_hi v172, v172 offset:0\n\t" \
  "v_rcp_f16_sdwa v226, v226 dst_sel:DWORD dst_unused:UNUSED_PAD src0_sel:WORD_1\n\t" \
  "v_mfma_f32_16x16x32_bf16 v[200:203], v[220:223], v[136:139], v[200:203]\n\t" \
  "s_waitcnt lgkmcnt(9)\n\t" \
  "ds_read_b64_tr_b16 v[220:221], v249 offset:16896\n\t" \
  "v_lshlrev_b32_sdwa v173, 1, v173 dst_sel:DWORD dst_unused:UNUSED_PAD src0_sel:DWORD src1_sel:WORD_1\n\t" \
  "v_fma_mix_f32 v156, v156, v225, 0 op_sel_hi:[0,1,0]\n\t" \
  "v_mfma_f32_16x16x32_bf16 v[176:179], v[208:211], v[104:107], v[176:179]\n\t" \
  "ds_read_u16_d16_hi v173, v173 offset:0\n\t" \
  "v_fma_mix_f32 v158, v158, v227, 0 op_sel_hi:[0,1,0]\n\t" \
  "v_mfma_f32_16x16x32_bf16 v[184:187], v[208:211], v[116:119], v[184:187]\n\t" \
  "ds_read_b64_tr_b16 v[222:223], v249 offset:17152\n\t" \
  "v_lshlrev_b32_sdwa v174, 1, v174 dst_sel:DWORD dst_unused:UNUSED_PAD src0_sel:DWORD src1_sel:WORD_1\n\t" \
  "v_fma_mix_f32 v157, v157, v224, 0 op_sel_hi:[0,1,0]\n\t" \
  "v_mfma_f32_16x16x32_bf16 v[192:195], v[208:211], v[128:131], v[192:195]\n\t" \
  "ds_read_u16_d16_hi v174, v174 offset:0\n\t" \
  "v_fma_mix_f32 v159, v159, v226, 0 op_sel_hi:[0,1,0]\n\t" \
  "v_mfma_f32_16x16x32_bf16 v[200:203], v[208:211], v[140:143], v[200:203]\n\t" \
  "s_waitcnt lgkmcnt(9)\n\t" \
  "ds_read_b64_tr_b16 v[208:209], v250 offset:12320\n\t" \
  "v_lshlrev_b32_sdwa v175, 1, v175 dst_sel:DWORD dst_unused:UNUSED_PAD src0_sel:DWORD src1_sel:WORD_1\n\t" \
  "v_mfma_f32_16x16x32_bf16 v[180:183], v[212:215], v[96:99], 0\n\t" \
  "ds_read_u16_d16_hi v175, v175 offset:0\n\t" \
  "v_lshlrev_b32_sdwa v176, 1, v176 dst_sel:DWORD dst_unused:UNUSED_PAD src0_sel:DWORD src1_sel:WORD_1\n\t" \
  "v_mfma_f32_16x16x32_bf16 v[188:191], v[212:215], v[108:111], 0\n\t" \
  "ds_read_b64_tr_b16 v[210:211], v250 offset:13088\n\t" \
  "v_cvt_pk_f16_f32 v224, v184, v185\n\t" \
  "v_cvt_pk_f16_f32 v226, v186, v187\n\t" \
  "v_mfma_f32_16x16x32_bf16 v[196:199], v[212:215], v[120:123], 0\n\t" \
  "ds_read_u16_d16_hi v176, v176 offset:0\n\t" \
  "v_pk_fma_f16 v225, v224, v224, %[h1] neg_lo:[1,0,0] neg_hi:[1,0,0]\n\t" \
  "v_mfma_f32_16x16x32_bf16 v[204:207], v[212:215], v[132:135], 0\n\t" \
  "s_waitcnt lgkmcnt(9)\n\t" \
  "ds_read_b64_tr_b16 v[212:213], v250 offset:12328\n\t" \
  "v_lshlrev_b32_sdwa v177, 1, v177 dst_sel:DWORD dst_unused:UNUSED_PAD src0_sel:DWORD src1_sel:WORD_1\n\t" \
  "v_pk_fma_f16 v227, v226, v226, %[h1] neg_lo:[1,0,0] neg_hi:[1,0,0]\n\t" \
  "v_mfma_f32_16x16x32_bf16 v[180:183], v[216:219], v[100:103], v[180:183]\n\t" \
  "ds_read_u16_d16_hi v177, v177 offset:0\n\t" \
  "v_pk_mul_f16 v225, v224, v225\n\t" \
  "v_mfma_f32_16x16x32_bf16 v[188:191], v[216:219], v[112:115], v[188:191]\n\t" \
  "ds_read_b64_tr_b16 v[214:215], v250 offset:13096\n\t" \
  "v_lshlrev_b32_sdwa v178, 1, v178 dst_sel:DWORD dst_unused:UNUSED_PAD src0_sel:DWORD src1_sel:WORD_1\n\t" \
  "v_pk_mul_f16 v227, v226, v227\n\t" \
  "v_exp_f16 v224, v225\n\t" \
  "v_mfma_f32_16x16x32_bf16 v[196:199], v[216:219], v[124:127], v[196:199]\n\t" \
  "ds_read_u16_d16_hi v178, v178 offset:0\n\t" \
  "v_lshlrev_b32_sdwa v179, 1, v179 dst_sel:DWORD dst_unused:UNUSED_PAD src0_sel:DWORD src1_sel:WORD_1\n\t" \
  "v_mfma_f32_16x16x32_bf16 v[204:207], v[216:219], v[136:139], v[204:207]\n\t" \
  "s_waitcnt lgkmcnt(9)\n\t" \
  "ds_read_b64_tr_b16 v[216:217], v250 offset:12384\n\t" \
  "v_exp_f16 v226, v227\n\t" \
  "v_mfma_f32_16x16x32_bf16 v[180:183], v[220:223], v[104:107], v[180:183]\n\t" \
  "ds_read_u16_d16_hi v179, v179 offset:0\n\t" \
  "v_lshlrev_b32_sdwa v192, 1, v192 dst_sel:DWORD dst_unused:UNUSED_PAD src0_sel:DWORD src1_sel:WORD_1\n\t" \
  "v_mfma_f32_16x16x32_bf16 v[188:191], v[220:223], v[116:119], v[188:191]\n\t" \
  "ds_read_b64_tr_b16 v[218:219], v250 offset:13152\n\t" \
  "v_exp_f16_sdwa v224, v225 dst_sel:WORD_1 dst_unused:UNUSED_PRESERVE src0_sel:WORD_1\n\t" \
  "v_mfma_f32_16x16x32_bf16 v[196:199], v[220:223], v[128:131], v[196:199]\n\t" \
  "ds_read_u16_d16_hi v192, v192 offset:0\n\t" \
  "v_exp_f16_sdwa v226, v227 dst_sel:WORD_1 dst_unused:UNUSED_PRESERVE src0_sel:WORD_1\n\t" \
  "v_mfma_f32_16x16x32_bf16 v[204:207], v[220:223], v[140:143], v[204:207]\n\t" \
  "s_waitcnt lgkmcnt(9)\n\t" \
  "ds_read_b64_tr_b16 v[220:221], v250 offset:12392\n\t" \
  "v_lshlrev_b32_sdwa v193, 1, v193 dst_sel:DWORD dst_unused:UNUSED_PAD src0_sel:DWORD src1_sel:WORD_1\n\t" \
  "v_smfmac_f32_16x16x64_bf16 v[0:3], v[208:211], v[144:151], v248\n\t" \
  "ds_read_u16_d16_hi v193, v193 offset:0\n\t" \
  "v_pk_fma_f16 v224, v224, %[hc], %[hc]\n\t" \
  "v_smfmac_f32_16x16x64_bf16 v[24:27], v[208:211], v[152:159], v248\n\t" \
  "ds_read_b64_tr_b16 v[222:223], v250 offset:13160\n\t" \
  "v_lshlrev_b32_sdwa v194, 1, v194 dst_sel:DWORD dst_unused:UNUSED_PAD src0_sel:DWORD src1_sel:WORD_1\n\t" \
  "v_pk_fma_f16 v226, v226, %[hc], %[hc]\n\t" \
  "v_smfmac_f32_16x16x64_bf16 v[48:51], v[208:211], v[160:167], v248\n\t" \
  "ds_read_u16_d16_hi v194, v194 offset:0\n\t" \
  "v_rcp_f16 v225, v224\n\t" \
  "v_smfmac_f32_16x16x64_bf16 v[72:75], v[208:211], v[168:175], v248\n\t" \
  "s_waitcnt lgkmcnt(9)\n\t" \
  "ds_read_b64_tr_b16 v[208:209], v250 offset:12448\n\t" \
  "v_lshlrev_b32_sdwa v195, 1, v195 dst_sel:DWORD dst_unused:UNUSED_PAD src0_sel:DWORD src1_sel:WORD_1\n\t" \
  "v_rcp_f16 v227, v226\n\t" \
  "v_smfmac_f32_16x16x64_bf16 v[4:7], v[212:215], v[144:151], v248\n\t" \
  "ds_read_u16_d16_hi v195, v195 offset:0\n\t" \
  "v_lshlrev_b32_sdwa v200, 1, v200 dst_sel:DWORD dst_unused:UNUSED_PAD src0_sel:DWORD src1_sel:WORD_1\n\t" \
  "v_smfmac_f32_16x16x64_bf16 v[28:31], v[212:215], v[152:159], v248\n\t" \
  "ds_read_b64_tr_b16 v[210:211], v250 offset:13216\n\t" \
  "v_rcp_f16_sdwa v224, v224 dst_sel:DWORD dst_unused:UNUSED_PAD src0_sel:WORD_1\n\t" \
  "v_smfmac_f32_16x16x64_bf16 v[52:55], v[212:215], v[160:167], v248\n\t" \
  "ds_read_u16_d16_hi v200, v200 offset:0\n\t" \
  "v_lshlrev_b32_sdwa v201, 1, v201 dst_sel:DWORD dst_unused:UNUSED_PAD src0_sel:DWORD src1_sel:WORD_1\n\t" \
  "v_smfmac_f32_16x16x64_bf16 v[76:79], v[212:215], v[168:175], v248\n\t" \
  "s_waitcnt lgkmcnt(9)\n\t" \
  "ds_read_b64_tr_b16 v[212:213], v250 offset:12456\n\t" \
  "v_rcp_f16_sdwa v226, v226 dst_sel:DWORD dst_unused:UNUSED_PAD src0_sel:WORD_1\n\t" \
  "v_smfmac_f32_16x16x64_bf16 v[8:11], v[216:219], v[144:151], v248\n\t" \
  "ds_read_u16_d16_hi v201, v201 offset:0\n\t" \
  "v_fma_mix_f32 v184, v184, v225, 0 op_sel_hi:[0,1,0]\n\t" \
  "v_smfmac_f32_16x16x64_bf16 v[32:35], v[216:219], v[152:159], v248\n\t" \
  "ds_read_b64_tr_b16 v[214:215], v250 offset:13224\n\t" \
  "v_lshlrev_b32_sdwa v202, 1, v202 dst_sel:DWORD dst_unused:UNUSED_PAD src0_sel:DWORD src1_sel:WORD_1\n\t" \
  "v_fma_mix_f32 v186, v186, v227, 0 op_sel_hi:[0,1,0]\n\t" \
  "v_smfmac_f32_16x16x64_bf16 v[56:59], v[216:219], v[160:167], v248\n\t" \
  "ds_read_u16_d16_hi v202, v202 offset:0\n\t" \
  "v_fma_mix_f32 v185, v185, v224, 0 op_sel_hi:[0,1,0]\n\t" \
  "v_smfmac_f32_16x16x64_bf16 v[80:83], v[216:219], v[168:175], v248\n\t" \
  "s_waitcnt lgkmcnt(9)\n\t" \
  "s_barrier\n\t" \
  "ds_read_b64_tr_b16 v[216:217], v249 offset:0\n\t" \
  "v_lshlrev_b32_sdwa v203, 1, v203 dst_sel:DWORD dst_unused:UNUSED_PAD src0_sel:DWORD src1_sel:WORD_1\n\t" \
  "v_fma_mix_f32 v187, v187, v226, 0 op_sel_hi:[0,1,0]\n\t" \
  "v_smfmac_f32_16x16x64_bf16 v[12:15], v[220:223], v[144:151], v248\n\t" \
  "ds_read_u16_d16_hi v203, v203 offset:0\n\t" \
  "v_cvt_pk_f16_f32 v224, v188, v189\n\t" \
  "v_cvt_pk_f16_f32 v226, v190, v191\n\t" \
  "v_smfmac_f32_16x16x64_bf16 v[36:39], v[220:223], v[152:159], v248\n\t" \
  "ds_read_b64_tr_b16 v[218:219], v249 offset:256\n\t" \
  "v_lshlrev_b32_sdwa v180, 1, v180 dst_sel:DWORD dst_unused:UNUSED_PAD src0_sel:DWORD src1_sel:WORD_1\n\t" \
  "v_pk_fma_f16 v225, v224, v224, %[h1] neg_lo:[1,0,0] neg_hi:[1,0,0]\n\t" \
  "v_smfmac_f32_16x16x64_bf16 v[60:63], v[220:223], v[160:167], v248\n\t" \
  "ds_read_u16_d16_hi v180, v180 offset:0\n\t" \
  "v_pk_fma_f16 v227, v226, v226, %[h1] neg_lo:[1,0,0] neg_hi:[1,0,0]\n\t" \
  "v_smfmac_f32_16x16x64_bf16 v[84:87], v[220:223], v[168:175], v248\n\t" \
  "s_waitcnt lgkmcnt(9)\n\t" \
  "ds_read_b64_tr_b16 v[220:221], v249 offset:2048\n\t" \
  "v_lshlrev_b32_sdwa v181, 1, v181 dst_sel:DWORD dst_unused:UNUSED_PAD src0_sel:DWORD src1_sel:WORD_1\n\t" \
  "v_pk_mul_f16 v225, v224, v225\n\t" \
  "v_smfmac_f32_16x16x64_bf16 v[16:19], v[208:211], v[144:151], v248\n\t" \
  "ds_read_u16_d16_hi v181, v181 offset:0\n\t" \
  "v_pk_mul_f16 v227, v226, v227\n\t" \
  "v_exp_f16 v224, v225\n\t" \
  "v_smfmac_f32_16x16x64_bf16 v[40:43], v[208:211], v[152:159], v248\n\t" \
  "ds_read_b64_tr_b16 v[222:223], v249 offset:2304\n\t" \
  "v_lshlrev_b32_sdwa v182, 1, v182 dst_sel:DWORD dst_unused:UNUSED_PAD src0_sel:DWORD src1_sel:WORD_1\n\t" \
  "v_smfmac_f32_16x16x64_bf16 v[64:67], v[208:211], v[160:167], v248\n\t" \
  "ds_read_u16_d16_hi v182, v182 offset:0\n\t" \
  "v_exp_f16 v226, v227\n\t" \
  "v_smfmac_f32_16x16x64_bf16 v[88:91], v[208:211], v[168:175], v248\n\t" \
  "s_waitcnt lgkmcnt(9)\n\t" \
  "ds_read_b64_tr_b16 v[208:209], v249 offset:4096\n\t" \
  "v_lshlrev_b32_sdwa v183, 1, v183 dst_sel:DWORD dst_unused:UNUSED_PAD src0_sel:DWORD src1_sel:WORD_1\n\t" \
  "v_smfmac_f32_16x16x64_bf16 v[20:23], v[212:215], v[144:151], v248\n\t" \
  "ds_read_u16_d16_hi v183, v183 offset:0\n\t" \
  "v_exp_f16_sdwa v224, v225 dst_sel:WORD_1 dst_unused:UNUSED_PRESERVE src0_sel:WORD_1\n\t" \
  "v_smfmac_f32_16x16x64_bf16 v[44:47], v[212:215], v[152:159], v248\n\t" \
  "ds_read_b64_tr_b16 v[210:211], v249 offset:4352\n\t" \
  "v_lshlrev_b32_sdwa v196, 1, v196 dst_sel:DWORD dst_unused:UNUSED_PAD src0_sel:DWORD src1_sel:WORD_1\n\t" \
  "v_exp_f16_sdwa v226, v227 dst_sel:WORD_1 dst_unused:UNUSED_PRESERVE src0_sel:WORD_1\n\t" \
  "v_smfmac_f32_16x16x64_bf16 v[68:71], v[212:215], v[160:167], v248\n\t" \
  "ds_read_u16_d16_hi v196, v196 offset:0\n\t" \
  "v_lshlrev_b32_sdwa v197, 1, v197 dst_sel:DWORD dst_unused:UNUSED_PAD src0_sel:DWORD src1_sel:WORD_1\n\t" \
  "v_smfmac_f32_16x16x64_bf16 v[92:95], v[212:215], v[168:175], v248\n\t" \
  "s_waitcnt lgkmcnt(9)\n\t" \
  "ds_read_b64_tr_b16 v[212:213], v249 offset:512\n\t" \
  "v_pk_fma_f16 v224, v224, %[hc], %[hc]\n\t" \
  "v_pk_fma_f16 v226, v226, %[hc], %[hc]\n\t" \
  "v_mfma_f32_16x16x32_bf16 v[152:155], v[216:219], v[108:111], 0\n\t" \
  "ds_read_u16_d16_hi v197, v197 offset:0\n\t" \
  "v_lshlrev_b32_sdwa v198, 1, v198 dst_sel:DWORD dst_unused:UNUSED_PAD src0_sel:DWORD src1_sel:WORD_1\n\t" \
  "v_rcp_f16 v225, v224\n\t" \
  "v_mfma_f32_16x16x32_bf16 v[160:163], v[216:219], v[120:123], 0\n\t" \
  "ds_read_b64_tr_b16 v[214:215], v249 offset:768\n\t" \
  "v_rcp_f16 v227, v226\n\t" \
  "v_mfma_f32_16x16x32_bf16 v[168:171], v[216:219], v[132:135], 0\n\t" \
  "s_waitcnt lgkmcnt(8)\n\t" \
  "ds_read_u16_d16_hi v198, v198 offset:0\n\t" \
  "v_lshlrev_b32_sdwa v199, 1, v199 dst_sel:DWORD dst_unused:UNUSED_PAD src0_sel:DWORD src1_sel:WORD_1\n\t" \
  "v_rcp_f16_sdwa v224, v224 dst_sel:DWORD dst_unused:UNUSED_PAD src0_sel:WORD_1\n\t" \
  "v_mfma_f32_16x16x32_bf16 v[152:155], v[220:223], v[112:115], v[152:155]\n\t" \
  "ds_read_b64_tr_b16 v[216:217], v249 offset:2560\n\t" \
  "v_lshlrev_b32_sdwa v204, 1, v204 dst_sel:DWORD dst_unused:UNUSED_PAD src0_sel:DWORD src1_sel:WORD_1\n\t" \
  "v_mfma_f32_16x16x32_bf16 v[160:163], v[220:223], v[124:127], v[160:163]\n\t" \
  "ds_read_u16_d16_hi v199, v199 offset:0\n\t" \
  "v_rcp_f16_sdwa v226, v226 dst_sel:DWORD dst_unused:UNUSED_PAD src0_sel:WORD_1\n\t" \
  "v_mfma_f32_16x16x32_bf16 v[168:171], v[220:223], v[136:139], v[168:171]\n\t" \
  "s_waitcnt lgkmcnt(7)\n\t" \
  "ds_read_b64_tr_b16 v[218:219], v249 offset:2816\n\t" \
  "v_lshlrev_b32_sdwa v205, 1, v205 dst_sel:DWORD dst_unused:UNUSED_PAD src0_sel:DWORD src1_sel:WORD_1\n\t" \
  "v_fma_mix_f32 v188, v188, v225, 0 op_sel_hi:[0,1,0]\n\t" \
  "v_fma_mix_f32 v190, v190, v227, 0 op_sel_hi:[0,1,0]\n\t" \
  "v_mfma_f32_16x16x32_bf16 v[152:155], v[208:211], v[116:119], v[152:155]\n\t" \
  "ds_read_u16_d16_hi v204, v204 offset:0\n\t" \
  "v_lshlrev_b32_sdwa v206, 1, v206 dst_sel:DWORD dst_unused:UNUSED_PAD src0_sel:DWORD src1_sel:WORD_1\n\t" \
  "v_fma_mix_f32 v189, v189, v224, 0 op_sel_hi:[0,1,0]\n\t" \
  "v_mfma_f32_16x16x32_bf16 v[160:163], v[208:211], v[128:131], v[160:163]\n\t" \
  "ds_read_b64_tr_b16 v[220:221], v249 offset:4608\n\t" \
  "v_fma_mix_f32 v191, v191, v226, 0 op_sel_hi:[0,1,0]\n\t" \
  "v_mfma_f32_16x16x32_bf16 v[168:171], v[208:211], v[140:143], v[168:171]\n\t" \
  "s_waitcnt lgkmcnt(6)\n\t" \
  "ds_read_b64_tr_b16 v[222:223], v249 offset:4864\n\t" \
  "v_lshlrev_b32_sdwa v207, 1, v207 dst_sel:DWORD dst_unused:UNUSED_PAD src0_sel:DWORD src1_sel:WORD_1\n\t" \
  "v_mfma_f32_16x16x32_bf16 v[156:159], v[212:215], v[108:111], 0\n\t" \
  "ds_read_u16_d16_hi v205, v205 offset:0\n\t" \
  "v_lshlrev_b32_sdwa v160, 1, v160 dst_sel:DWORD dst_unused:UNUSED_PAD src0_sel:DWORD src1_sel:WORD_1\n\t" \
  "v_cvt_pk_f16_f32 v224, v152, v153\n\t" \
  "v_cvt_pk_f16_f32 v226, v154, v155\n\t" \
  "v_mfma_f32_16x16x32_bf16 v[164:167], v[212:215], v[120:123], 0\n\t" \
  "ds_read_b64_tr_b16 v[208:209], v250 offset:18480\n\t" \
  "v_pk_fma_f16 v225, v224, v224, %[h1] neg_lo:[1,0,0] neg_hi:[1,0,0]\n\t" \
  "v_mfma_f32_16x16x32_bf16 v[172:175], v[212:215], v[132:135], 0\n\t" \
  "s_waitcnt lgkmcnt(5)\n\t" \
  "ds_read_b64_tr_b16 v[210:211], v250 offset:19248\n\t" \
  "ds_read_u16_d16_hi v206, v206 offset:0\n\t" \
  "v_pk_fma_f16 v227, v226, v226, %[h1] neg_lo:[1,0,0] neg_hi:[1,0,0]\n\t" \
  "v_lshlrev_b32_sdwa v161, 1, v161 dst_sel:DWORD dst_unused:UNUSED_PAD src0_sel:DWORD src1_sel:WORD_1\n\t" \
  "v_pk_mul_f16 v225, v224, v225\n\t" \
  "v_mfma_f32_16x16x32_bf16 v[156:159], v[216:219], v[112:115], v[156:159]\n\t" \
  "ds_read_b64_tr_b16 v[212:213], v250 offset:18488\n\t" \
  "ds_read_u16_d16_hi v207, v207 offset:0\n\t" \
  "v_pk_mul_f16 v227, v226, v227\n\t" \
  "v_exp_f16 v224, v225\n\t" \
  "v_mfma_f32_16x16x32_bf16 v[164:167], v[216:219], v[124:127], v[164:167]\n\t" \
  "ds_read_b64_tr_b16 v[214:215], v250 offset:19256\n\t" \
  "v_lshlrev_b32_sdwa v162, 1, v162 dst_sel:DWORD dst_unused:UNUSED_PAD src0_sel:DWORD src1_sel:WORD_1\n\t" \
  "v_mfma_f32_16x16x32_bf16 v[172:175], v[216:219], v[136:139], v[172:175]\n\t" \
  "s_waitcnt lgkmcnt(7)\n\t" \
  "ds_read_u16_d16_hi v160, v160 offset:0\n\t" \
  "v_exp_f16 v226, v227\n\t" \
  "v_mfma_f32_16x16x32_bf16 v[156:159], v[220:223], v[116:119], v[156:159]\n\t" \
  "ds_read_b64_tr_b16 v[216:217], v250 offset:18544\n\t" \
  "v_exp_f16_sdwa v224, v225 dst_sel:WORD_1 dst_unused:UNUSED_PRESERVE src0_sel:WORD_1\n\t" \
  "v_mfma_f32_16x16x32_bf16 v[164:167], v[220:223], v[128:131], v[164:167]\n\t" \
  "ds_read_u16_d16_hi v161, v161 offset:0\n\t" \
  "v_lshlrev_b32_sdwa v163, 1, v163 dst_sel:DWORD dst_unused:UNUSED_PAD src0_sel:DWORD src1_sel:WORD_1\n\t" \
  "v_exp_f16_sdwa v226, v227 dst_sel:WORD_1 dst_unused:UNUSED_PRESERVE src0_sel:WORD_1\n\t" \
  "v_mfma_f32_16x16x32_bf16 v[172:175], v[220:223], v[140:143], v[172:175]\n\t" \
  "s_waitcnt lgkmcnt(7)\n\t" \
  "ds_read_b64_tr_b16 v[218:219], v250 offset:19312\n\t" \
  "v_pk_fma_f16 v224, v224, %[hc], %[hc]\n\t" \
  "v_smfmac_f32_16x16x64_bf16 v[0:3], v[208:211], v[176:183], v248\n\t" \
  "ds_read_u16_d16_hi v162, v162 offset:0\n\t" \
  "v_lshlrev_b32_sdwa v168, 1, v168 dst_sel:DWORD dst_unused:UNUSED_PAD src0_sel:DWORD src1_sel:WORD_1\n\t" \
  "v_smfmac_f32_16x16x64_bf16 v[24:27], v[208:211], v[184:191], v248\n\t" \
  "ds_read_b64_tr_b16 v[220:221], v250 offset:18552\n\t" \
  "v_pk_fma_f16 v226, v226, %[hc], %[hc]\n\t" \
  "v_smfmac_f32_16x16x64_bf16 v[48:51], v[208:211], v[192:199], v248\n\t" \
  "s_waitcnt lgkmcnt(7)\n\t" \
  "ds_read_u16_d16_hi v163, v163 offset:0\n\t" \
  "v_rcp_f16 v225, v224\n\t" \
  "v_smfmac_f32_16x16x64_bf16 v[72:75], v[208:211], v[200:207], v248\n\t" \
  "s_waitcnt lgkmcnt(7)\n\t" \
  "ds_read_b64_tr_b16 v[222:223], v250 offset:19320\n\t" \
  "v_rcp_f16 v227, v226\n\t" \
  "v_smfmac_f32_16x16x64_bf16 v[4:7], v[212:215], v[176:183], v248\n\t" \
  "ds_read_u16_d16_hi v168, v168 offset:0\n\t" \
  "v_lshlrev_b32_sdwa v169, 1, v169 dst_sel:DWORD dst_unused:UNUSED_PAD src0_sel:DWORD src1_sel:WORD_1\n\t" \
  "v_smfmac_f32_16x16x64_bf16 v[28:31], v[212:215], v[184:191], v248\n\t" \
  "ds_read_b64_tr_b16 v[208:209], v250 offset:18608\n\t" \
  "v_rcp_f16_sdwa v224, v224 dst_sel:DWORD dst_unused:UNUSED_PAD src0_sel:WORD_1\n\t" \
  "v_smfmac_f32_16x16x64_bf16 v[52:55], v[212:215], v[192:199], v248\n\t" \
  "ds_read_u16_d16_hi v169, v169 offset:0\n\t" \
  "v_cvt_pk_bf16_f32 v230, v0, v1\n\t" \
  "v_smfmac_f32_16x16x64_bf16 v[76:79], v[212:215], v[200:207], v248\n\t" \
  "s_waitcnt lgkmcnt(7)\n\t" \
  "ds_read_b64_tr_b16 v[210:211], v250 offset:19376\n\t" \
  "v_lshlrev_b32_sdwa v170, 1, v170 dst_sel:DWORD dst_unused:UNUSED_PAD src0_sel:DWORD src1_sel:WORD_1\n\t" \
  "v_rcp_f16_sdwa v226, v226 dst_sel:DWORD dst_unused:UNUSED_PAD src0_sel:WORD_1\n\t" \
  "v_cvt_pk_bf16_f32 v231, v2, v3\n\t" \
  "v_smfmac_f32_16x16x64_bf16 v[8:11], v[216:219], v[176:183], v248\n\t" \
  "ds_read_u16_d16_hi v170, v170 offset:0\n\t" \
  "v_fma_mix_f32 v152, v152, v225, 0 op_sel_hi:[0,1,0]\n\t" \
  "v_cvt_pk_bf16_f32 v232, v4, v5\n\t" \
  "v_smfmac_f32_16x16x64_bf16 v[32:35], v[216:219], v[184:191], v248\n\t" \
  "ds_read_b64_tr_b16 v[212:213], v250 offset:18616\n\t" \
  "v_fma_mix_f32 v154, v154, v227, 0 op_sel_hi:[0,1,0]\n\t" \
  "v_cvt_pk_bf16_f32 v233, v6, v7\n\t" \
  "v_smfmac_f32_16x16x64_bf16 v[56:59], v[216:219], v[192:199], v248\n\t" \
  "buffer_store_dwordx4 v[230:233], v253, %[osrd], %[ob] offen offset:0 sc1\n\t" \
  "v_lshlrev_b32_sdwa v171, 1, v171 dst_sel:DWORD dst_unused:UNUSED_PAD src0_sel:DWORD src1_sel:WORD_1\n\t" \
  "v_fma_mix_f32 v153, v153, v224, 0 op_sel_hi:[0,1,0]\n\t" \
  "v_smfmac_f32_16x16x64_bf16 v[80:83], v[216:219], v[200:207], v248\n\t" \
  "s_waitcnt lgkmcnt(6)\n\t" \
  "ds_read_b64_tr_b16 v[214:215], v250 offset:19384\n\t" \
  "v_fma_mix_f32 v155, v155, v226, 0 op_sel_hi:[0,1,0]\n\t" \
  "v_smfmac_f32_16x16x64_bf16 v[12:15], v[220:223], v[176:183], v248\n\t" \
  "s_barrier\n\t" \
  "ds_read_u16_d16_hi v171, v171 offset:0\n\t" \
  "v_cvt_pk_f16_f32 v224, v156, v157\n\t" \
  "v_cvt_pk_f16_f32 v226, v158, v159\n\t" \
  "v_smfmac_f32_16x16x64_bf16 v[36:39], v[220:223], v[184:191], v248\n\t" \
  "ds_read_b64_tr_b16 v[216:217], v249 offset:6144\n\t" \
  "v_lshlrev_b32_sdwa v164, 1, v164 dst_sel:DWORD dst_unused:UNUSED_PAD src0_sel:DWORD src1_sel:WORD_1\n\t" \
  "v_pk_fma_f16 v225, v224, v224, %[h1] neg_lo:[1,0,0] neg_hi:[1,0,0]\n\t" \
  "v_smfmac_f32_16x16x64_bf16 v[60:63], v[220:223], v[192:199], v248\n\t" \
  "ds_read_u16_d16_hi v164, v164 offset:0\n\t" \
  "v_pk_fma_f16 v227, v226, v226, %[h1] neg_lo:[1,0,0] neg_hi:[1,0,0]\n\t" \
  "v_cvt_pk_bf16_f32 v234, v8, v9\n\t" \
  "v_smfmac_f32_16x16x64_bf16 v[84:87], v[220:223], v[200:207], v248\n\t" \
  "s_waitcnt lgkmcnt(6)\n\t" \
  "ds_read_b64_tr_b16 v[218:219], v249 offset:6400\n\t" \
  "v_pk_mul_f16 v225, v224, v225\n\t" \
  "v_cvt_pk_bf16_f32 v235, v10, v11\n\t" \
  "v_smfmac_f32_16x16x64_bf16 v[16:19], v[208:211], v[176:183], v248\n\t" \
  "ds_read_b64_tr_b16 v[220:221], v249 offset:8192\n\t" \
  "v_lshlrev_b32_sdwa v165, 1, v165 dst_sel:DWORD dst_unused:UNUSED_PAD src0_sel:DWORD src1_sel:WORD_1\n\t" \
  "v_pk_mul_f16 v227, v226, v227\n\t" \
  "v_exp_f16 v224, v225\n\t" \
  "v_cvt_pk_bf16_f32 v236, v12, v13\n\t" \
  "v_smfmac_f32_16x16x64_bf16 v[40:43], v[208:211], v[184:191], v248\n\t" \
  "ds_read_u16_d16_hi v165, v165 offset:0\n\t" \
  "v_cvt_pk_bf16_f32 v237, v14, v15\n\t" \
  "v_smfmac_f32_16x16x64_bf16 v[64:67], v[208:211], v[192:199], v248\n\t" \
  "ds_read_b64_tr_b16 v[222:223], v249 offset:8448\n\t" \
  "v_exp_f16 v226, v227\n\t" \
  "v_smfmac_f32_16x16x64_bf16 v[88:91], v[208:211], v[200:207], v248\n\t" \
  "s_waitcnt lgkmcnt(7)\n\t" \
  "buffer_store_dwordx4 v[234:237], v253, %[osrd], %[ob] offen offset:64 sc1\n\t" \
  "v_lshlrev_b32_sdwa v166, 1, v166 dst_sel:DWORD dst_unused:UNUSED_PAD src0_sel:DWORD src1_sel:WORD_1\n\t" \
  "v_smfmac_f32_16x16x64_bf16 v[20:23], v[212:215], v[176:183], v248\n\t" \
  "ds_read_b64_tr_b16 v[208:209], v249 offset:10240\n\t" \
  "v_exp_f16_sdwa v224, v225 dst_sel:WORD_1 dst_unused:UNUSED_PRESERVE src0_sel:WORD_1\n\t" \
  "v_smfmac_f32_16x16x64_bf16 v[44:47], v[212:215], v[184:191], v248\n\t" \
  "ds_read_u16_d16_hi v166, v166 offset:0\n\t" \
  "v_exp_f16_sdwa v226, v227 dst_sel:WORD_1 dst_unused:UNUSED_PRESERVE src0_sel:WORD_1\n\t" \
  "v_smfmac_f32_16x16x64_bf16 v[68:71], v[212:215], v[192:199], v248\n\t" \
  "ds_read_b64_tr_b16 v[210:211], v249 offset:10496\n\t" \
  "v_lshlrev_b32_sdwa v167, 1, v167 dst_sel:DWORD dst_unused:UNUSED_PAD src0_sel:DWORD src1_sel:WORD_1\n\t" \
  "v_cvt_pk_bf16_f32 v238, v16, v17\n\t" \
  "v_smfmac_f32_16x16x64_bf16 v[92:95], v[212:215], v[200:207], v248\n\t" \
  "s_waitcnt lgkmcnt(6)\n\t" \
  "ds_read_u16_d16_hi v167, v167 offset:0\n\t" \
  "v_pk_fma_f16 v224, v224, %[hc], %[hc]\n\t" \
  "v_cvt_pk_bf16_f32 v239, v18, v19\n\t" \
  "v_pk_fma_f16 v226, v226, %[hc], %[hc]\n\t" \
  "v_cvt_pk_bf16_f32 v240, v20, v21\n\t" \
  "v_mfma_f32_16x16x32_bf16 v[184:187], v[216:219], v[108:111], 0\n\t" \
  "ds_read_b64_tr_b16 v[212:213], v249 offset:6656\n\t" \
  "v_lshlrev_b32_sdwa v172, 1, v172 dst_sel:DWORD dst_unused:UNUSED_PAD src0_sel:DWORD src1_sel:WORD_1\n\t" \
  "v_rcp_f16 v225, v224\n\t" \
  "v_cvt_pk_bf16_f32 v241, v22, v23\n\t" \
  "v_mfma_f32_16x16x32_bf16 v[192:195], v[216:219], v[120:123], 0\n\t" \
  "buffer_store_dwordx4 v[238:241], v253, %[osrd], %[ob] offen offset:128 sc1\n\t" \
  "v_rcp_f16 v227, v226\n\t" \
  "v_mfma_f32_16x16x32_bf16 v[200:203], v[216:219], v[132:135], 0\n\t" \
  "s_waitcnt lgkmcnt(5)\n\t" \
  "ds_read_b64_tr_b16 v[214:215], v249 offset:6912\n\t" \
  "v_lshlrev_b32_sdwa v173, 1, v173 dst_sel:DWORD dst_unused:UNUSED_PAD src0_sel:DWORD src1_sel:WORD_1\n\t" \
  "v_rcp_f16_sdwa v224, v224 dst_sel:DWORD dst_unused:UNUSED_PAD src0_sel:WORD_1\n\t" \
  "v_mfma_f32_16x16x32_bf16 v[184:187], v[220:223], v[112:115], v[184:187]\n\t" \
  "ds_read_u16_d16_hi v172, v172 offset:0\n\t" \
  "v_rcp_f16_sdwa v226, v226 dst_sel:DWORD dst_unused:UNUSED_PAD src0_sel:WORD_1\n\t" \
  "v_mfma_f32_16x16x32_bf16 v[192:195], v[220:223], v[124:127], v[192:195]\n\t" \
  "ds_read_b64_tr_b16 v[216:217], v249 offset:8704\n\t" \
  "v_lshlrev_b32_sdwa v174, 1, v174 dst_sel:DWORD dst_unused:UNUSED_PAD src0_sel:DWORD src1_sel:WORD_1\n\t" \
  "v_mfma_f32_16x16x32_bf16 v[200:203], v[220:223], v[136:139], v[200:203]\n\t" \
  "s_waitcnt lgkmcnt(5)\n\t" \
  "ds_read_b64_tr_b16 v[218:219], v249 offset:8960\n\t" \
  "v_fma_mix_f32 v156, v156, v225, 0 op_sel_hi:[0,1,0]\n\t" \
  "v_fma_mix_f32 v158, v158, v227, 0 op_sel_hi:[0,1,0]\n\t" \
  "v_mfma_f32_16x16x32_bf16 v[184:187], v[208:211], v[116:119], v[184:187]\n\t" \
  "ds_read_u16_d16_hi v173, v173 offset:0\n\t" \
  "v_fma_mix_f32 v157, v157, v224, 0 op_sel_hi:[0,1,0]\n\t" \
  "v_mfma_f32_16x16x32_bf16 v[192:195], v[208:211], v[128:131], v[192:195]\n\t" \
  "ds_read_b64_tr_b16 v[220:221], v249 offset:10752\n\t" \
  "v_lshlrev_b32_sdwa v175, 1, v175 dst_sel:DWORD dst_unused:UNUSED_PAD src0_sel:DWORD src1_sel:WORD_1\n\t" \
  "v_fma_mix_f32 v159, v159, v226, 0 op_sel_hi:[0,1,0]\n\t" \
  "v_mfma_f32_16x16x32_bf16 v[200:203], v[208:211], v[140:143], v[200:203]\n\t" \
  "s_waitcnt lgkmcnt(5)\n\t" \
  "ds_read_b64_tr_b16 v[222:223], v249 offset:11008\n\t" \
  "v_mfma_f32_16x16x32_bf16 v[188:191], v[212:215], v[108:111], 0\n\t" \
  "ds_read_u16_d16_hi v174, v174 offset:0\n\t" \
  "v_lshlrev_b32_sdwa v192, 1, v192 dst_sel:DWORD dst_unused:UNUSED_PAD src0_sel:DWORD src1_sel:WORD_1\n\t" \
  "v_cvt_pk_f16_f32 v224, v184, v185\n\t" \
  "v_cvt_pk_f16_f32 v226, v186, v187\n\t" \
  "v_mfma_f32_16x16x32_bf16 v[196:199], v[212:215], v[120:123], 0\n\t" \
  "ds_read_b64_tr_b16 v[208:209], v250 offset:0\n\t" \
  "v_pk_fma_f16 v225, v224, v224, %[h1] neg_lo:[1,0,0] neg_hi:[1,0,0]\n\t" \
  "v_mfma_f32_16x16x32_bf16 v[204:207], v[212:215], v[132:135], 0\n\t" \
  "s_waitcnt lgkmcnt(5)\n\t" \
  "ds_read_b64_tr_b16 v[210:211], v250 offset:768\n\t" \
  "v_pk_fma_f16 v227, v226, v226, %[h1] neg_lo:[1,0,0] neg_hi:[1,0,0]\n\t" \
  "v_lshlrev_b32_sdwa v193, 1, v193 dst_sel:DWORD dst_unused:UNUSED_PAD src0_sel:DWORD src1_sel:WORD_1\n\t" \
  "v_pk_mul_f16 v225, v224, v225\n\t" \
  "v_mfma_f32_16x16x32_bf16 v[188:191], v[216:219], v[112:115], v[188:191]\n\t" \
  "ds_read_u16_d16_hi v175, v175 offset:0\n\t" \
  "v_pk_mul_f16 v227, v226, v227\n\t" \
  "v_exp_f16 v224, v225\n\t" \
  "v_mfma_f32_16x16x32_bf16 v[196:199], v[216:219], v[124:127], v[196:199]\n\t" \
  "ds_read_b64_tr_b16 v[212:213], v250 offset:8\n\t" \
  "v_lshlrev_b32_sdwa v194, 1, v194 dst_sel:DWORD dst_unused:UNUSED_PAD src0_sel:DWORD src1_sel:WORD_1\n\t" \
  "v_mfma_f32_16x16x32_bf16 v[204:207], v[216:219], v[136:139], v[204:207]\n\t" \
  "s_waitcnt lgkmcnt(5)\n\t" \
  "ds_read_b64_tr_b16 v[214:215], v250 offset:776\n\t" \
  "v_exp_f16 v226, v227\n\t" \
  "v_mfma_f32_16x16x32_bf16 v[188:191], v[220:223], v[116:119], v[188:191]\n\t" \
  "ds_read_u16_d16_hi v192, v192 offset:0\n\t" \
  "v_exp_f16_sdwa v224, v225 dst_sel:WORD_1 dst_unused:UNUSED_PRESERVE src0_sel:WORD_1\n\t" \
  "v_mfma_f32_16x16x32_bf16 v[196:199], v[220:223], v[128:131], v[196:199]\n\t" \
  "ds_read_b64_tr_b16 v[216:217], v250 offset:64\n\t" \
  "v_lshlrev_b32_sdwa v195, 1, v195 dst_sel:DWORD dst_unused:UNUSED_PAD src0_sel:DWORD src1_sel:WORD_1\n\t" \
  "v_exp_f16_sdwa v226, v227 dst_sel:WORD_1 dst_unused:UNUSED_PRESERVE src0_sel:WORD_1\n\t" \
  "v_mfma_f32_16x16x32_bf16 v[204:207], v[220:223], v[140:143], v[204:207]\n\t" \
  "s_waitcnt lgkmcnt(5)\n\t" \
  "ds_read_b64_tr_b16 v[218:219], v250 offset:832\n\t" \
  "v_pk_fma_f16 v224, v224, %[hc], %[hc]\n\t" \
  "v_smfmac_f32_16x16x64_bf16 v[24:27], v[208:211], v[152:159], v248\n\t" \
  "ds_read_u16_d16_hi v193, v193 offset:0\n\t" \
  "v_lshlrev_b32_sdwa v200, 1, v200 dst_sel:DWORD dst_unused:UNUSED_PAD src0_sel:DWORD src1_sel:WORD_1\n\t" \
  "v_pk_fma_f16 v226, v226, %[hc], %[hc]\n\t" \
  "v_smfmac_f32_16x16x64_bf16 v[48:51], v[208:211], v[160:167], v248\n\t" \
  "s_waitcnt lgkmcnt(6)\n\t" \
  "ds_read_b64_tr_b16 v[220:221], v250 offset:72\n\t" \
  "v_rcp_f16 v225, v224\n\t" \
  "v_smfmac_f32_16x16x64_bf16 v[72:75], v[208:211], v[168:175], v248\n\t" \
  "s_waitcnt lgkmcnt(5)\n\t" \
  "ds_read_b64_tr_b16 v[222:223], v250 offset:840\n\t" \
  "v_rcp_f16 v227, v226\n\t" \
  "v_lshlrev_b32_sdwa v201, 1, v201 dst_sel:DWORD dst_unused:UNUSED_PAD src0_sel:DWORD src1_sel:WORD_1\n\t" \
  "v_smfmac_f32_16x16x64_bf16 v[28:31], v[212:215], v[152:159], v248\n\t" \
  "s_add_u32 %[ob], %[ob], %[dob]\n\t" \
  "ds_read_u16_d16_hi v194, v194 offset:0\n\t" \
  "v_rcp_f16_sdwa v224, v224 dst_sel:DWORD dst_unused:UNUSED_PAD src0_sel:WORD_1\n\t" \
  "v_smfmac_f32_16x16x64_bf16 v[52:55], v[212:215], v[160:167], v248\n\t" \
  "ds_read_b64_tr_b16 v[208:209], v250 offset:128\n\t" \
  "v_lshlrev_b32_sdwa v202, 1, v202 dst_sel:DWORD dst_unused:UNUSED_PAD src0_sel:DWORD src1_sel:WORD_1\n\t" \
  "v_smfmac_f32_16x16x64_bf16 v[76:79], v[212:215], v[168:175], v248\n\t" \
  "s_waitcnt lgkmcnt(5)\n\t" \
  "ds_read_b64_tr_b16 v[210:211], v250 offset:896\n\t" \
  "ds_read_u16_d16_hi v195, v195 offset:0\n\t" \
  "v_rcp_f16_sdwa v226, v226 dst_sel:DWORD dst_unused:UNUSED_PAD src0_sel:WORD_1\n\t" \
  "v_fma_mix_f32 v184, v184, v225, 0 op_sel_hi:[0,1,0]\n\t" \
  "v_smfmac_f32_16x16x64_bf16 v[32:35], v[216:219], v[152:159], v248\n\t" \
  "ds_read_b64_tr_b16 v[212:213], v250 offset:136\n\t" \
  "v_fma_mix_f32 v186, v186, v227, 0 op_sel_hi:[0,1,0]\n\t" \
  "v_smfmac_f32_16x16x64_bf16 v[56:59], v[216:219], v[160:167], v248\n\t" \
  "ds_read_u16_d16_hi v200, v200 offset:0\n\t" \
  "v_lshlrev_b32_sdwa v203, 1, v203 dst_sel:DWORD dst_unused:UNUSED_PAD src0_sel:DWORD src1_sel:WORD_1\n\t" \
  "v_fma_mix_f32 v185, v185, v224, 0 op_sel_hi:[0,1,0]\n\t" \
  "v_smfmac_f32_16x16x64_bf16 v[80:83], v[216:219], v[168:175], v248\n\t" \
  "s_waitcnt lgkmcnt(6)\n\t" \
  "ds_read_b64_tr_b16 v[214:215], v250 offset:904\n\t" \
  "s_barrier\n\t" \
  "ds_read_u16_d16_hi v201, v201 offset:0\n\t" \
  "v_fma_mix_f32 v187, v187, v226, 0 op_sel_hi:[0,1,0]\n\t" \
  "v_cvt_pk_f16_f32 v224, v188, v189\n\t" \
  "v_cvt_pk_f16_f32 v226, v190, v191\n\t" \
  "v_smfmac_f32_16x16x64_bf16 v[36:39], v[220:223], v[152:159], v248\n\t" \
  "ds_read_b64_tr_b16 v[216:217], v249 offset:12288\n\t" \
  "v_lshlrev_b32_sdwa v196, 1, v196 dst_sel:DWORD dst_unused:UNUSED_PAD src0_sel:DWORD src1_sel:WORD_1\n\t" \
  "v_pk_fma_f16 v225, v224, v224, %[h1] neg_lo:[1,0,0] neg_hi:[1,0,0]\n\t" \
  "v_smfmac_f32_16x16x64_bf16 v[60:63], v[220:223], v[160:167], v248\n\t" \
  "ds_read_u16_d16_hi v202, v202 offset:0\n\t" \
  "v_pk_fma_f16 v227, v226, v226, %[h1] neg_lo:[1,0,0] neg_hi:[1,0,0]\n\t" \
  "v_smfmac_f32_16x16x64_bf16 v[84:87], v[220:223], v[168:175], v248\n\t" \
  "s_waitcnt lgkmcnt(7)\n\t" \
  "ds_read_b64_tr_b16 v[218:219], v249 offset:12544\n\t" \
  "v_pk_mul_f16 v225, v224, v225\n\t" \
  "v_lshlrev_b32_sdwa v197, 1, v197 dst_sel:DWORD dst_unused:UNUSED_PAD src0_sel:DWORD src1_sel:WORD_1\n\t" \
  "v_pk_mul_f16 v227, v226, v227\n\t" \
  "v_exp_f16 v224, v225\n\t" \
  "v_smfmac_f32_16x16x64_bf16 v[40:43], v[208:211], v[152:159], v248\n\t" \
  "ds_read_u16_d16_hi v203, v203 offset:0\n\t" \
  "v_exp_f16 v226, v227\n\t" \
  "v_smfmac_f32_16x16x64_bf16 v[64:67], v[208:211], v[160:167], v248\n\t" \
  "ds_read_b64_tr_b16 v[220:221], v249 offset:14336\n\t" \
  "v_lshlrev_b32_sdwa v198, 1, v198 dst_sel:DWORD dst_unused:UNUSED_PAD src0_sel:DWORD src1_sel:WORD_1\n\t" \
  "v_smfmac_f32_16x16x64_bf16 v[88:91], v[208:211], v[168:175], v248\n\t" \
  "s_waitcnt lgkmcnt(6)\n\t" \
  "ds_read_b64_tr_b16 v[222:223], v249 offset:14592\n\t" \
  "v_exp_f16_sdwa v224, v225 dst_sel:WORD_1 dst_unused:UNUSED_PRESERVE src0_sel:WORD_1\n\t" \
  "v_smfmac_f32_16x16x64_bf16 v[44:47], v[212:215], v[152:159], v248\n\t" \
  "ds_read_u16_d16_hi v196, v196 offset:0\n\t" \
  "v_exp_f16_sdwa v226, v227 dst_sel:WORD_1 dst_unused:UNUSED_PRESERVE src0_sel:WORD_1\n\t" \
  "v_smfmac_f32_16x16x64_bf16 v[68:71], v[212:215], v[160:167], v248\n\t" \
  "ds_read_b64_tr_b16 v[208:209], v249 offset:16384\n\t" \
  "v_lshlrev_b32_sdwa v199, 1, v199 dst_sel:DWORD dst_unused:UNUSED_PAD src0_sel:DWORD src1_sel:WORD_1\n\t" \
  "v_smfmac_f32_16x16x64_bf16 v[92:95], v[212:215], v[168:175], v248\n\t" \
  "s_waitcnt lgkmcnt(5)\n\t" \
  "ds_read_b64_tr_b16 v[210:211], v249 offset:16640\n\t" \
  "v_pk_fma_f16 v224, v224, %[hc], %[hc]\n\t" \
  "v_pk_fma_f16 v226, v226, %[hc], %[hc]\n\t" \
  "v_mfma_f32_16x16x32_bf16 v[152:155], v[216:219], v[108:111], 0\n\t" \
  "ds_read_u16_d16_hi v197, v197 offset:0\n\t" \
  "v_lshlrev_b32_sdwa v204, 1, v204 dst_sel:DWORD dst_unused:UNUSED_PAD src0_sel:DWORD src1_sel:WORD_1\n\t" \
  "v_rcp_f16 v225, v224\n\t" \
  "v_mfma_f32_16x16x32_bf16 v[160:163], v[216:219], v[120:123], 0\n\t" \
  "ds_read_b64_tr_b16 v[212:213], v249 offset:12800\n\t" \
  "v_rcp_f16 v227, v226\n\t" \
  "v_mfma_f32_16x16x32_bf16 v[168:171], v[216:219], v[132:135], 0\n\t" \
  "s_waitcnt lgkmcnt(5)\n\t" \
  "ds_read_b64_tr_b16 v[214:215], v249 offset:13056\n\t" \
  "v_lshlrev_b32_sdwa v205, 1, v205 dst_sel:DWORD dst_unused:UNUSED_PAD src0_sel:DWORD src1_sel:WORD_1\n\t" \
  "v_rcp_f16_sdwa v224, v224 dst_sel:DWORD dst_unused:UNUSED_PAD src0_sel:WORD_1\n\t" \
  "v_mfma_f32_16x16x32_bf16 v[152:155], v[220:223], v[112:115], v[152:155]\n\t" \
  "ds_read_u16_d16_hi v198, v198 offset:0\n\t" \
  "v_rcp_f16_sdwa v226, v226 dst_sel:DWORD dst_unused:UNUSED_PAD src0_sel:WORD_1\n\t" \
  "v_mfma_f32_16x16x32_bf16 v[160:163], v[220:223], v[124:127], v[160:163]\n\t" \
  "ds_read_b64_tr_b16 v[216:217], v249 offset:14848\n\t" \
  "v_lshlrev_b32_sdwa v206, 1, v206 dst_sel:DWORD dst_unused:UNUSED_PAD src0_sel:DWORD src1_sel:WORD_1\n\t" \
  "v_mfma_f32_16x16x32_bf16 v[168:171], v[220:223], v[136:139], v[168:171]\n\t" \
  "s_waitcnt lgkmcnt(5)\n\t" \
  "ds_read_b64_tr_b16 v[218:219], v249 offset:15104\n\t" \
  "ds_read_u16_d16_hi v199, v199 offset:0\n\t" \
  "v_fma_mix_f32 v188, v188, v225, 0 op_sel_hi:[0,1,0]\n\t" \
  "v_fma_mix_f32 v190, v190, v227, 0 op_sel_hi:[0,1,0]\n\t" \
  "v_mfma_f32_16x16x32_bf16 v[152:155], v[208:211], v[116:119], v[152:155]\n\t" \
  "ds_read_b64_tr_b16 v[220:221], v249 offset:16896\n\t" \
  "v_fma_mix_f32 v189, v189, v224, 0 op_sel_hi:[0,1,0]\n\t" \
  "v_mfma_f32_16x16x32_bf16 v[160:163], v[208:211], v[128:131], v[160:163]\n\t" \
  "ds_read_u16_d16_hi v204, v204 offset:0\n\t" \
  "v_lshlrev_b32_sdwa v207, 1, v207 dst_sel:DWORD dst_unused:UNUSED_PAD src0_sel:DWORD src1_sel:WORD_1\n\t" \
  "v_fma_mix_f32 v191, v191, v226, 0 op_sel_hi:[0,1,0]\n\t" \
  "v_mfma_f32_16x16x32_bf16 v[168:171], v[208:211], v[140:143], v[168:171]\n\t" \
  "s_waitcnt lgkmcnt(6)\n\t" \
  "ds_read_b64_tr_b16 v[222:223], v249 offset:17152\n\t" \
  "v_mfma_f32_16x16x32_bf16 v[156:159], v[212:215], v[108:111], 0\n\t" \
  "ds_read_u16_d16_hi v205, v205 offset:0\n\t" \
  "v_lshlrev_b32_sdwa v160, 1, v160 dst_sel:DWORD dst_unused:UNUSED_PAD src0_sel:DWORD src1_sel:WORD_1\n\t" \
  "v_cvt_pk_f16_f32 v224, v152, v153\n\t" \
  "v_cvt_pk_f16_f32 v226, v154, v155\n\t" \
  "v_mfma_f32_16x16x32_bf16 v[164:167], v[212:215], v[120:123], 0\n\t" \
  "ds_read_b64_tr_b16 v[208:209], v250 offset:6160\n\t" \
  "v_pk_fma_f16 v225, v224, v224, %[h1] neg_lo:[1,0,0] neg_hi:[1,0,0]\n\t" \
  "v_mfma_f32_16x16x32_bf16 v[172:175], v[212:215], v[132:135], 0\n\t" \
  "s_waitcnt lgkmcnt(6)\n\t" \
  "ds_read_b64_tr_b16 v[210:211], v250 offset:6928\n\t" \
  "ds_read_u16_d16_hi v206, v206 offset:0\n\t" \
  "v_pk_fma_f16 v227, v226, v226, %[h1] neg_lo:[1,0,0] neg_hi:[1,0,0]\n\t" \
  "v_lshlrev_b32_sdwa v161, 1, v161 dst_sel:DWORD dst_unused:UNUSED_PAD src0_sel:DWORD src1_sel:WORD_1\n\t" \
  "v_pk_mul_f16 v225, v224, v225\n\t" \
  "v_mfma_f32_16x16x32_bf16 v[156:159], v[216:219], v[112:115], v[156:159]\n\t" \
  "ds_read_b64_tr_b16 v[212:213], v250 offset:6168\n\t" \
  "ds_read_u16_d16_hi v207, v207 offset:0\n\t" \
  "v_pk_mul_f16 v227, v226, v227\n\t" \
  "v_exp_f16 v224, v225\n\t" \
  "v_mfma_f32_16x16x32_bf16 v[164:167], v[216:219], v[124:127], v[164:167]\n\t" \
  "ds_read_b64_tr_b16 v[214:215], v250 offset:6936\n\t" \
  "v_lshlrev_b32_sdwa v162, 1, v162 dst_sel:DWORD dst_unused:UNUSED_PAD src0_sel:DWORD src1_sel:WORD_1\n\t" \
  "v_mfma_f32_16x16x32_bf16 v[172:175], v[216:219], v[136:139], v[172:175]\n\t" \
  "s_waitcnt lgkmcnt(7)\n\t" \
  "ds_read_u16_d16_hi v160, v160 offset:0\n\t" \
  "v_exp_f16 v226, v227\n\t" \
  "v_mfma_f32_16x16x32_bf16 v[156:159], v[220:223], v[116:119], v[156:159]\n\t" \
  "ds_read_b64_tr_b16 v[216:217], v250 offset:6224\n\t" \
  "v_exp_f16_sdwa v224, v225 dst_sel:WORD_1 dst_unused:UNUSED_PRESERVE src0_sel:WORD_1\n\t" \
  "v_mfma_f32_16x16x32_bf16 v[164:167], v[220:223], v[128:131], v[164:167]\n\t" \
  "ds_read_u16_d16_hi v161, v161 offset:0\n\t" \
  "v_lshlrev_b32_sdwa v163, 1, v163 dst_sel:DWORD dst_unused:UNUSED_PAD src0_sel:DWORD src1_sel:WORD_1\n\t" \
  "v_exp_f16_sdwa v226, v227 dst_sel:WORD_1 dst_unused:UNUSED_PRESERVE src0_sel:WORD_1\n\t" \
  "v_mfma_f32_16x16x32_bf16 v[172:175], v[220:223], v[140:143], v[172:175]\n\t" \
  "s_waitcnt lgkmcnt(7)\n\t" \
  "ds_read_b64_tr_b16 v[218:219], v250 offset:6992\n\t" \
  "v_pk_fma_f16 v224, v224, %[hc], %[hc]\n\t" \
  "v_smfmac_f32_16x16x64_bf16 v[24:27], v[208:211], v[184:191], v248\n\t" \
  "ds_read_b64_tr_b16 v[220:221], v250 offset:6232\n\t" \
  "ds_read_b64_tr_b16 v[222:223], v250 offset:7000\n\t" \
  "v_lshlrev_b32_sdwa v168, 1, v168 dst_sel:DWORD dst_unused:UNUSED_PAD src0_sel:DWORD src1_sel:WORD_1\n\t" \
  "v_pk_fma_f16 v226, v226, %[hc], %[hc]\n\t" \
  "v_smfmac_f32_16x16x64_bf16 v[48:51], v[208:211], v[192:199], v248\n\t" \
  "s_waitcnt lgkmcnt(7)\n\t" \
  "ds_read_b64_tr_b16 v[0:1], v250 offset:6288\n\t" \
  "ds_read_b64_tr_b16 v[2:3], v250 offset:7056\n\t" \
  "v_rcp_f16 v225, v224\n\t" \
  "v_smfmac_f32_16x16x64_bf16 v[72:75], v[208:211], v[200:207], v248\n\t" \
  "s_waitcnt lgkmcnt(8)\n\t" \
  "ds_read_b64_tr_b16 v[4:5], v250 offset:6296\n\t" \
  "ds_read_b64_tr_b16 v[6:7], v250 offset:7064\n\t" \
  "v_rcp_f16 v227, v226\n\t" \
  "v_lshlrev_b32_sdwa v169, 1, v169 dst_sel:DWORD dst_unused:UNUSED_PAD src0_sel:DWORD src1_sel:WORD_1\n\t" \
  "v_smfmac_f32_16x16x64_bf16 v[28:31], v[212:215], v[184:191], v248\n\t" \
  "v_add_u32 v249, 57344, v249\n\t" \
  "ds_read_b64_tr_b16 v[8:9], v249 offset:8192\n\t" \
  "ds_read_b64_tr_b16 v[10:11], v249 offset:8448\n\t" \
  "v_rcp_f16_sdwa v224, v224 dst_sel:DWORD dst_unused:UNUSED_PAD src0_sel:WORD_1\n\t" \
  "v_smfmac_f32_16x16x64_bf16 v[52:55], v[212:215], v[192:199], v248\n\t" \
  "ds_read_b64_tr_b16 v[12:13], v249 offset:10240\n\t" \
  "ds_read_b64_tr_b16 v[14:15], v249 offset:10496\n\t" \
  "v_smfmac_f32_16x16x64_bf16 v[76:79], v[212:215], v[200:207], v248\n\t" \
  "s_waitcnt lgkmcnt(10)\n\t" \
  "ds_read_b64_tr_b16 v[208:209], v249 offset:12288\n\t" \
  "ds_read_b64_tr_b16 v[210:211], v249 offset:12544\n\t" \
  "v_lshlrev_b32_sdwa v170, 1, v170 dst_sel:DWORD dst_unused:UNUSED_PAD src0_sel:DWORD src1_sel:WORD_1\n\t" \
  "v_rcp_f16_sdwa v226, v226 dst_sel:DWORD dst_unused:UNUSED_PAD src0_sel:WORD_1\n\t" \
  "v_fma_mix_f32 v152, v152, v225, 0 op_sel_hi:[0,1,0]\n\t" \
  "v_smfmac_f32_16x16x64_bf16 v[32:35], v[216:219], v[184:191], v248\n\t" \
  "ds_read_u16_d16_hi v162, v162 offset:0\n\t" \
  "ds_read_b64_tr_b16 v[212:213], v249 offset:8704\n\t" \
  "v_fma_mix_f32 v154, v154, v227, 0 op_sel_hi:[0,1,0]\n\t" \
  "v_smfmac_f32_16x16x64_bf16 v[56:59], v[216:219], v[192:199], v248\n\t" \
  "ds_read_u16_d16_hi v163, v163 offset:0\n\t" \
  "v_lshlrev_b32_sdwa v171, 1, v171 dst_sel:DWORD dst_unused:UNUSED_PAD src0_sel:DWORD src1_sel:WORD_1\n\t" \
  "v_fma_mix_f32 v153, v153, v224, 0 op_sel_hi:[0,1,0]\n\t" \
  "v_smfmac_f32_16x16x64_bf16 v[80:83], v[216:219], v[200:207], v248\n\t" \
  "s_waitcnt lgkmcnt(13)\n\t" \
  "ds_read_b64_tr_b16 v[214:215], v249 offset:8960\n\t" \
  "ds_read_u16_d16_hi v168, v168 offset:0\n\t" \
  "v_fma_mix_f32 v155, v155, v226, 0 op_sel_hi:[0,1,0]\n\t" \
  "v_cvt_pk_f16_f32 v224, v156, v157\n\t" \
  "v_cvt_pk_f16_f32 v226, v158, v159\n\t" \
  "v_smfmac_f32_16x16x64_bf16 v[36:39], v[220:223], v[184:191], v248\n\t" \
  "ds_read_b64_tr_b16 v[216:217], v249 offset:10752\n\t" \
  "v_lshlrev_b32_sdwa v164, 1, v164 dst_sel:DWORD dst_unused:UNUSED_PAD src0_sel:DWORD src1_sel:WORD_1\n\t" \
  "v_pk_fma_f16 v225, v224, v224, %[h1] neg_lo:[1,0,0] neg_hi:[1,0,0]\n\t" \
  "v_smfmac_f32_16x16x64_bf16 v[60:63], v[220:223], v[192:199], v248\n\t" \
  "ds_read_u16_d16_hi v169, v169 offset:0\n\t" \
  "v_pk_fma_f16 v227, v226, v226, %[h1] neg_lo:[1,0,0] neg_hi:[1,0,0]\n\t" \
  "v_smfmac_f32_16x16x64_bf16 v[84:87], v[220:223], v[200:207], v248\n\t" \
  "s_waitcnt lgkmcnt(14)\n\t" \
  "ds_read_b64_tr_b16 v[218:219], v249 offset:11008\n\t" \
  "v_pk_mul_f16 v225, v224, v225\n\t" \
  "v_lshlrev_b32_sdwa v165, 1, v165 dst_sel:DWORD dst_unused:UNUSED_PAD src0_sel:DWORD src1_sel:WORD_1\n\t" \
  "v_pk_mul_f16 v227, v226, v227\n\t" \
  "v_exp_f16 v224, v225\n\t" \
  "v_smfmac_f32_16x16x64_bf16 v[40:43], v[0:3], v[184:191], v248\n\t" \
  "ds_read_u16_d16_hi v170, v170 offset:0\n\t" \
  "v_exp_f16 v226, v227\n\t" \
  "v_smfmac_f32_16x16x64_bf16 v[64:67], v[0:3], v[192:199], v248\n\t" \
  "ds_read_b64_tr_b16 v[220:221], v249 offset:12800\n\t" \
  "v_lshlrev_b32_sdwa v166, 1, v166 dst_sel:DWORD dst_unused:UNUSED_PAD src0_sel:DWORD src1_sel:WORD_1\n\t" \
  "v_smfmac_f32_16x16x64_bf16 v[88:91], v[0:3], v[200:207], v248\n\t" \
  "s_waitcnt lgkmcnt(14)\n\t" \
  "ds_read_b64_tr_b16 v[222:223], v249 offset:13056\n\t" \
  "ds_read_u16_d16_hi v171, v171 offset:0\n\t" \
  "v_exp_f16_sdwa v224, v225 dst_sel:WORD_1 dst_unused:UNUSED_PRESERVE src0_sel:WORD_1\n\t" \
  "v_smfmac_f32_16x16x64_bf16 v[44:47], v[4:7], v[184:191], v248\n\t" \
  "ds_read_b64_tr_b16 v[0:1], v250 offset:53264\n\t" \
  "v_exp_f16_sdwa v226, v227 dst_sel:WORD_1 dst_unused:UNUSED_PRESERVE src0_sel:WORD_1\n\t" \
  "v_smfmac_f32_16x16x64_bf16 v[68:71], v[4:7], v[192:199], v248\n\t" \
  "ds_read_u16_d16_hi v164, v164 offset:0\n\t" \
  "v_lshlrev_b32_sdwa v167, 1, v167 dst_sel:DWORD dst_unused:UNUSED_PAD src0_sel:DWORD src1_sel:WORD_1\n\t" \
  "v_smfmac_f32_16x16x64_bf16 v[92:95], v[4:7], v[200:207], v248\n\t" \
  "ds_read_b64_tr_b16 v[2:3], v250 offset:54032\n\t" \
  "v_pk_fma_f16 v224, v224, %[hc], %[hc]\n\t" \
  "v_pk_fma_f16 v226, v226, %[hc], %[hc]\n\t" \
  "v_lshlrev_b32_sdwa v172, 1, v172 dst_sel:DWORD dst_unused:UNUSED_PAD src0_sel:DWORD src1_sel:WORD_1\n\t" \
  "v_rcp_f16 v225, v224\n\t" \
  "v_mfma_f32_16x16x32_bf16 v[192:195], v[8:11], v[120:123], 0\n\t" \
  "ds_read_u16_d16_hi v165, v165 offset:0\n\t" \
  "ds_read_b64_tr_b16 v[4:5], v250 offset:53272\n\t" \
  "v_rcp_f16 v227, v226\n\t" \
  "v_mfma_f32_16x16x32_bf16 v[200:203], v[8:11], v[132:135], 0\n\t" \
  "s_waitcnt lgkmcnt(14)\n\t" \
  "ds_read_b64_tr_b16 v[6:7], v250 offset:54040\n\t" \
  "ds_read_u16_d16_hi v166, v166 offset:0\n\t" \
  "v_lshlrev_b32_sdwa v173, 1, v173 dst_sel:DWORD dst_unused:UNUSED_PAD src0_sel:DWORD src1_sel:WORD_1\n\t" \
  "v_rcp_f16_sdwa v224, v224 dst_sel:DWORD dst_unused:UNUSED_PAD src0_sel:WORD_1\n\t" \
  "v_mfma_f32_16x16x32_bf16 v[192:195], v[12:15], v[124:127], v[192:195]\n\t" \
  "ds_read_b64_tr_b16 v[8:9], v250 offset:53328\n\t" \
  "ds_read_u16_d16_hi v167, v167 offset:0\n\t" \
  "v_rcp_f16_sdwa v226, v226 dst_sel:DWORD dst_unused:UNUSED_PAD src0_sel:WORD_1\n\t" \
  "v_mfma_f32_16x16x32_bf16 v[200:203], v[12:15], v[136:139], v[200:203]\n\t" \
  "ds_read_b64_tr_b16 v[10:11], v250 offset:54096\n\t" \
  "ds_read_u16_d16_hi v172, v172 offset:0\n\t" \
  "v_lshlrev_b32_sdwa v174, 1, v174 dst_sel:DWORD dst_unused:UNUSED_PAD src0_sel:DWORD src1_sel:WORD_1\n\t" \
  "v_fma_mix_f32 v156, v156, v225, 0 op_sel_hi:[0,1,0]\n\t" \
  "v_fma_mix_f32 v158, v158, v227, 0 op_sel_hi:[0,1,0]\n\t" \
  "v_fma_mix_f32 v157, v157, v224, 0 op_sel_hi:[0,1,0]\n\t" \
  "v_mfma_f32_16x16x32_bf16 v[192:195], v[208:211], v[128:131], v[192:195]\n\t" \
  "ds_read_b64_tr_b16 v[12:13], v250 offset:53336\n\t" \
  "ds_read_u16_d16_hi v173, v173 offset:0\n\t" \
  "v_lshlrev_b32_sdwa v175, 1, v175 dst_sel:DWORD dst_unused:UNUSED_PAD src0_sel:DWORD src1_sel:WORD_1\n\t" \
  "v_fma_mix_f32 v159, v159, v226, 0 op_sel_hi:[0,1,0]\n\t" \
  "v_mfma_f32_16x16x32_bf16 v[200:203], v[208:211], v[140:143], v[200:203]\n\t" \
  "s_waitcnt lgkmcnt(14)\n\t" \
  "ds_read_b64_tr_b16 v[14:15], v250 offset:54104\n\t" \
  "ds_read_u16_d16_hi v174, v174 offset:0\n\t" \
  "v_lshlrev_b32_sdwa v192, 1, v192 dst_sel:DWORD dst_unused:UNUSED_PAD src0_sel:DWORD src1_sel:WORD_1\n\t" \
  "v_mfma_f32_16x16x32_bf16 v[196:199], v[212:215], v[120:123], 0\n\t" \
  "ds_read_b64_tr_b16 v[208:209], v250 offset:53392\n\t" \
  "ds_read_u16_d16_hi v175, v175 offset:0\n\t" \
  "v_mfma_f32_16x16x32_bf16 v[204:207], v[212:215], v[132:135], 0\n\t" \
  "ds_read_b64_tr_b16 v[210:211], v250 offset:54160\n\t" \
  "ds_read_u16_d16_hi v192, v192 offset:0\n\t" \
  "v_lshlrev_b32_sdwa v193, 1, v193 dst_sel:DWORD dst_unused:UNUSED_PAD src0_sel:DWORD src1_sel:WORD_1\n\t" \
  "v_mfma_f32_16x16x32_bf16 v[196:199], v[216:219], v[124:127], v[196:199]\n\t" \
  "ds_read_b64_tr_b16 v[212:213], v250 offset:53400\n\t" \
  "ds_read_u16_d16_hi v193, v193 offset:0\n\t" \
  "v_mfma_f32_16x16x32_bf16 v[204:207], v[216:219], v[136:139], v[204:207]\n\t" \
  "ds_read_b64_tr_b16 v[214:215], v250 offset:54168\n\t" \
  "ds_read_b64_tr_b16 v[216:217], v249 offset:20512\n\t" \
  "v_lshlrev_b32_sdwa v194, 1, v194 dst_sel:DWORD dst_unused:UNUSED_PAD src0_sel:DWORD src1_sel:WORD_1\n\t" \
  "v_mfma_f32_16x16x32_bf16 v[196:199], v[220:223], v[128:131], v[196:199]\n\t" \
  "ds_read_u16_d16_hi v194, v194 offset:0\n\t" \
  "ds_read_b64_tr_b16 v[218:219], v249 offset:20768\n\t" \
  "v_lshlrev_b32_sdwa v195, 1, v195 dst_sel:DWORD dst_unused:UNUSED_PAD src0_sel:DWORD src1_sel:WORD_1\n\t" \
  "v_mfma_f32_16x16x32_bf16 v[204:207], v[220:223], v[140:143], v[204:207]\n\t" \
  "s_waitcnt lgkmcnt(14)\n\t" \
  "ds_read_u16_d16_hi v195, v195 offset:0\n\t" \
  "ds_read_b64_tr_b16 v[220:221], v249 offset:22560\n\t" \
  "v_smfmac_f32_16x16x64_bf16 v[24:27], v[0:3], v[152:159], v248\n\t" \
  "ds_read_b64_tr_b16 v[222:223], v249 offset:22816\n\t" \
  "v_lshlrev_b32_sdwa v200, 1, v200 dst_sel:DWORD dst_unused:UNUSED_PAD src0_sel:DWORD src1_sel:WORD_1\n\t" \
  "v_smfmac_f32_16x16x64_bf16 v[48:51], v[0:3], v[160:167], v248\n\t" \
  "s_waitcnt lgkmcnt(11)\n\t" \
  "ds_read_u16_d16_hi v200, v200 offset:0\n\t" \
  "v_lshlrev_b32_sdwa v201, 1, v201 dst_sel:DWORD dst_unused:UNUSED_PAD src0_sel:DWORD src1_sel:WORD_1\n\t" \
  "v_smfmac_f32_16x16x64_bf16 v[72:75], v[0:3], v[168:175], v248\n\t" \
  "ds_read_b64_tr_b16 v[0:1], v249 offset:24608\n\t" \
  "v_lshlrev_b32_sdwa v202, 1, v202 dst_sel:DWORD dst_unused:UNUSED_PAD src0_sel:DWORD src1_sel:WORD_1\n\t" \
  "v_smfmac_f32_16x16x64_bf16 v[28:31], v[4:7], v[152:159], v248\n\t" \
  "ds_read_u16_d16_hi v201, v201 offset:0\n\t" \
  "v_cvt_pk_bf16_f32 v230, v24, v25\n\t" \
  "v_smfmac_f32_16x16x64_bf16 v[52:55], v[4:7], v[160:167], v248\n\t" \
  "ds_read_b64_tr_b16 v[2:3], v249 offset:24864\n\t" \
  "v_cvt_pk_bf16_f32 v231, v26, v27\n\t" \
  "v_smfmac_f32_16x16x64_bf16 v[76:79], v[4:7], v[168:175], v248\n\t" \
  "ds_read_u16_d16_hi v202, v202 offset:0\n\t" \
  "s_nop 0\n\t" \
  "v_cvt_pk_bf16_f32 v232, v28, v29\n\t" \
  "v_smfmac_f32_16x16x64_bf16 v[32:35], v[8:11], v[152:159], v248\n\t" \
  "ds_read_b64_tr_b16 v[4:5], v249 offset:21024\n\t" \
  "v_lshlrev_b32_sdwa v203, 1, v203 dst_sel:DWORD dst_unused:UNUSED_PAD src0_sel:DWORD src1_sel:WORD_1\n\t" \
  "v_smfmac_f32_16x16x64_bf16 v[56:59], v[8:11], v[160:167], v248\n\t" \
  "ds_read_u16_d16_hi v203, v203 offset:0\n\t" \
  "v_cvt_pk_bf16_f32 v233, v30, v31\n\t" \
  "v_smfmac_f32_16x16x64_bf16 v[80:83], v[8:11], v[168:175], v248\n\t" \
  "ds_read_b64_tr_b16 v[6:7], v249 offset:21280\n\t" \
  "v_lshlrev_b32_sdwa v196, 1, v196 dst_sel:DWORD dst_unused:UNUSED_PAD src0_sel:DWORD src1_sel:WORD_1\n\t" \
  "v_smfmac_f32_16x16x64_bf16 v[36:39], v[12:15], v[152:159], v248\n\t" \
  "buffer_store_dwordx4 v[230:233], v253, %[osrd], %[ob] offen offset:0 sc1\n\t" \
  "v_cvt_pk_bf16_f32 v234, v32, v33\n\t" \
  "v_smfmac_f32_16x16x64_bf16 v[60:63], v[12:15], v[160:167], v248\n\t" \
  "ds_read_b64_tr_b16 v[8:9], v249 offset:23072\n\t" \
  "v_lshlrev_b32_sdwa v197, 1, v197 dst_sel:DWORD dst_unused:UNUSED_PAD src0_sel:DWORD src1_sel:WORD_1\n\t" \
  "v_smfmac_f32_16x16x64_bf16 v[84:87], v[12:15], v[168:175], v248\n\t" \
  "s_waitcnt lgkmcnt(14)\n\t" \
  "ds_read_b64_tr_b16 v[10:11], v249 offset:23328\n\t" \
  "v_cvt_pk_bf16_f32 v235, v34, v35\n\t" \
  "v_smfmac_f32_16x16x64_bf16 v[40:43], v[208:211], v[152:159], v248\n\t" \
  "ds_read_u16_d16_hi v196, v196 offset:0\n\t" \
  "v_cvt_pk_bf16_f32 v236, v36, v37\n\t" \
  "v_smfmac_f32_16x16x64_bf16 v[64:67], v[208:211], v[160:167], v248\n\t" \
  "ds_read_b64_tr_b16 v[12:13], v249 offset:25120\n\t" \
  "v_cvt_pk_bf16_f32 v237, v38, v39\n\t" \
  "v_smfmac_f32_16x16x64_bf16 v[88:91], v[208:211], v[168:175], v248\n\t" \
  "ds_read_b64_tr_b16 v[14:15], v249 offset:25376\n\t" \
  "v_lshlrev_b32_sdwa v198, 1, v198 dst_sel:DWORD dst_unused:UNUSED_PAD src0_sel:DWORD src1_sel:WORD_1\n\t" \
  "v_smfmac_f32_16x16x64_bf16 v[44:47], v[212:215], v[152:159], v248\n\t" \
  "buffer_store_dwordx4 v[234:237], v253, %[osrd], %[ob] offen offset:64 sc1\n\t" \
  "v_lshlrev_b32_sdwa v199, 1, v199 dst_sel:DWORD dst_unused:UNUSED_PAD src0_sel:DWORD src1_sel:WORD_1\n\t" \
  "v_smfmac_f32_16x16x64_bf16 v[68:71], v[212:215], v[160:167], v248\n\t" \
  "v_add_u32 v250, 57344, v250\n\t" \
  "ds_read_b64_tr_b16 v[208:209], v250 offset:8240\n\t" \
  "v_cvt_pk_bf16_f32 v238, v40, v41\n\t" \
  "v_smfmac_f32_16x16x64_bf16 v[92:95], v[212:215], v[168:175], v248\n\t" \
  "s_waitcnt lgkmcnt(14)\n\t" \
  "ds_read_b64_tr_b16 v[210:211], v250 offset:9008\n\t" \
  "v_cvt_pk_bf16_f32 v239, v42, v43\n\t" \
  "v_lshlrev_b32_sdwa v204, 1, v204 dst_sel:DWORD dst_unused:UNUSED_PAD src0_sel:DWORD src1_sel:WORD_1\n\t" \
  "v_cvt_pk_bf16_f32 v240, v44, v45\n\t" \
  "v_mfma_f32_16x16x32_bf16 v[160:163], v[216:219], v[120:123], 0\n\t" \
  "ds_read_u16_d16_hi v197, v197 offset:0\n\t" \
  "ds_read_b64_tr_b16 v[212:213], v250 offset:8248\n\t" \
  "v_cvt_pk_bf16_f32 v241, v46, v47\n\t" \
  "v_mfma_f32_16x16x32_bf16 v[168:171], v[216:219], v[132:135], 0\n\t" \
  "ds_read_b64_tr_b16 v[214:215], v250 offset:9016\n\t" \
  "buffer_store_dwordx4 v[238:241], v253, %[osrd], %[ob] offen offset:128 sc1\n\t" \
  "v_lshlrev_b32_sdwa v205, 1, v205 dst_sel:DWORD dst_unused:UNUSED_PAD src0_sel:DWORD src1_sel:WORD_1\n\t" \
  "v_mfma_f32_16x16x32_bf16 v[160:163], v[220:223], v[124:127], v[160:163]\n\t" \
  "ds_read_b64_tr_b16 v[216:217], v250 offset:8304\n\t" \
  "ds_read_u16_d16_hi v198, v198 offset:0\n\t" \
  "v_mfma_f32_16x16x32_bf16 v[168:171], v[220:223], v[136:139], v[168:171]\n\t" \
  "s_waitcnt lgkmcnt(14)\n\t" \
  "ds_read_b64_tr_b16 v[218:219], v250 offset:9072\n\t" \
  "ds_read_u16_d16_hi v199, v199 offset:0\n\t" \
  "v_lshlrev_b32_sdwa v206, 1, v206 dst_sel:DWORD dst_unused:UNUSED_PAD src0_sel:DWORD src1_sel:WORD_1\n\t" \
  "v_mfma_f32_16x16x32_bf16 v[160:163], v[0:3], v[128:131], v[160:163]\n\t" \
  "ds_read_b64_tr_b16 v[220:221], v250 offset:8312\n\t" \
  "ds_read_u16_d16_hi v204, v204 offset:0\n\t" \
  "v_lshlrev_b32_sdwa v207, 1, v207 dst_sel:DWORD dst_unused:UNUSED_PAD src0_sel:DWORD src1_sel:WORD_1\n\t" \
  "v_mfma_f32_16x16x32_bf16 v[168:171], v[0:3], v[140:143], v[168:171]\n\t" \
  "s_waitcnt lgkmcnt(14)\n\t" \
  "ds_read_b64_tr_b16 v[222:223], v250 offset:9080\n\t" \
  "ds_read_u16_d16_hi v205, v205 offset:0\n\t" \
  "s_nop 0\n\t" \
  "v_lshlrev_b32_sdwa v160, 1, v160 dst_sel:DWORD dst_unused:UNUSED_PAD src0_sel:DWORD src1_sel:WORD_1\n\t" \
  "v_mfma_f32_16x16x32_bf16 v[164:167], v[4:7], v[120:123], 0\n\t" \
  "ds_read_b64_tr_b16 v[0:1], v250 offset:8368\n\t" \
  "ds_read_u16_d16_hi v206, v206 offset:0\n\t" \
  "v_mfma_f32_16x16x32_bf16 v[172:175], v[4:7], v[132:135], 0\n\t" \
  "ds_read_b64_tr_b16 v[2:3], v250 offset:9136\n\t" \
  "ds_read_u16_d16_hi v207, v207 offset:0\n\t" \
  "v_lshlrev_b32_sdwa v161, 1, v161 dst_sel:DWORD dst_unused:UNUSED_PAD src0_sel:DWORD src1_sel:WORD_1\n\t" \
  "v_mfma_f32_16x16x32_bf16 v[164:167], v[8:11], v[124:127], v[164:167]\n\t" \
  "ds_read_b64_tr_b16 v[4:5], v250 offset:8376\n\t" \
  "ds_read_u16_d16_hi v160, v160 offset:0\n\t" \
  "v_mfma_f32_16x16x32_bf16 v[172:175], v[8:11], v[136:139], v[172:175]\n\t" \
  "s_waitcnt lgkmcnt(14)\n\t" \
  "ds_read_b64_tr_b16 v[6:7], v250 offset:9144\n\t" \
  "ds_read_u16_d16_hi v161, v161 offset:0\n\t" \
  "v_lshlrev_b32_sdwa v162, 1, v162 dst_sel:DWORD dst_unused:UNUSED_PAD src0_sel:DWORD src1_sel:WORD_1\n\t" \
  "v_mfma_f32_16x16x32_bf16 v[164:167], v[12:15], v[128:131], v[164:167]\n\t" \
  "ds_read_b64_tr_b16 v[8:9], v249 offset:32832\n\t" \
  "ds_read_u16_d16_hi v162, v162 offset:0\n\t" \
  "v_lshlrev_b32_sdwa v163, 1, v163 dst_sel:DWORD dst_unused:UNUSED_PAD src0_sel:DWORD src1_sel:WORD_1\n\t" \
  "v_mfma_f32_16x16x32_bf16 v[172:175], v[12:15], v[140:143], v[172:175]\n\t" \
  "s_waitcnt lgkmcnt(14)\n\t" \
  "ds_read_b64_tr_b16 v[10:11], v249 offset:33088\n\t" \
  "ds_read_u16_d16_hi v163, v163 offset:0\n\t" \
  "v_lshlrev_b32_sdwa v168, 1, v168 dst_sel:DWORD dst_unused:UNUSED_PAD src0_sel:DWORD src1_sel:WORD_1\n\t" \
  "v_smfmac_f32_16x16x64_bf16 v[48:51], v[208:211], v[192:199], v248\n\t" \
  "s_waitcnt lgkmcnt(8)\n\t" \
  "ds_read_b64_tr_b16 v[12:13], v249 offset:34880\n\t" \
  "ds_read_u16_d16_hi v168, v168 offset:0\n\t" \
  "v_smfmac_f32_16x16x64_bf16 v[72:75], v[208:211], v[200:207], v248\n\t" \
  "ds_read_b64_tr_b16 v[14:15], v249 offset:35136\n\t" \
  "ds_read_b64_tr_b16 v[208:209], v249 offset:36928\n\t" \
  "v_lshlrev_b32_sdwa v169, 1, v169 dst_sel:DWORD dst_unused:UNUSED_PAD src0_sel:DWORD src1_sel:WORD_1\n\t" \
  "v_smfmac_f32_16x16x64_bf16 v[52:55], v[212:215], v[192:199], v248\n\t" \
  "s_add_u32 %[ob], %[ob], %[dob]\n\t" \
  "ds_read_u16_d16_hi v169, v169 offset:0\n\t" \
  "ds_read_b64_tr_b16 v[210:211], v249 offset:37184\n\t" \
  "v_smfmac_f32_16x16x64_bf16 v[76:79], v[212:215], v[200:207], v248\n\t" \
  "ds_read_b64_tr_b16 v[212:213], v249 offset:33344\n\t" \
  "ds_read_b64_tr_b16 v[214:215], v249 offset:33600\n\t" \
  "v_lshlrev_b32_sdwa v170, 1, v170 dst_sel:DWORD dst_unused:UNUSED_PAD src0_sel:DWORD src1_sel:WORD_1\n\t" \
  "v_smfmac_f32_16x16x64_bf16 v[56:59], v[216:219], v[192:199], v248\n\t" \
  "ds_read_u16_d16_hi v170, v170 offset:0\n\t" \
  "v_lshlrev_b32_sdwa v171, 1, v171 dst_sel:DWORD dst_unused:UNUSED_PAD src0_sel:DWORD src1_sel:WORD_1\n\t" \
  "v_smfmac_f32_16x16x64_bf16 v[80:83], v[216:219], v[200:207], v248\n\t" \
  "ds_read_b64_tr_b16 v[216:217], v249 offset:35392\n\t" \
  "ds_read_u16_d16_hi v171, v171 offset:0\n\t" \
  "v_lshlrev_b32_sdwa v164, 1, v164 dst_sel:DWORD dst_unused:UNUSED_PAD src0_sel:DWORD src1_sel:WORD_1\n\t" \
  "v_smfmac_f32_16x16x64_bf16 v[60:63], v[220:223], v[192:199], v248\n\t" \
  "ds_read_b64_tr_b16 v[218:219], v249 offset:35648\n\t" \
  "ds_read_u16_d16_hi v164, v164 offset:0\n\t" \
  "v_smfmac_f32_16x16x64_bf16 v[84:87], v[220:223], v[200:207], v248\n\t" \
  "ds_read_b64_tr_b16 v[220:221], v249 offset:37440\n\t" \
  "ds_read_b64_tr_b16 v[222:223], v249 offset:37696\n\t" \
  "v_lshlrev_b32_sdwa v165, 1, v165 dst_sel:DWORD dst_unused:UNUSED_PAD src0_sel:DWORD src1_sel:WORD_1\n\t" \
  "v_smfmac_f32_16x16x64_bf16 v[64:67], v[0:3], v[192:199], v248\n\t" \
  "ds_read_u16_d16_hi v165, v165 offset:0\n\t" \
  "v_lshlrev_b32_sdwa v166, 1, v166 dst_sel:DWORD dst_unused:UNUSED_PAD src0_sel:DWORD src1_sel:WORD_1\n\t" \
  "v_smfmac_f32_16x16x64_bf16 v[88:91], v[0:3], v[200:207], v248\n\t" \
  "s_waitcnt lgkmcnt(14)\n\t" \
  "ds_read_b64_tr_b16 v[0:1], v250 offset:20560\n\t" \
  "ds_read_u16_d16_hi v166, v166 offset:0\n\t" \
  "v_smfmac_f32_16x16x64_bf16 v[68:71], v[4:7], v[192:199], v248\n\t" \
  "ds_read_b64_tr_b16 v[2:3], v250 offset:21328\n\t" \
  "v_lshlrev_b32_sdwa v167, 1, v167 dst_sel:DWORD dst_unused:UNUSED_PAD src0_sel:DWORD src1_sel:WORD_1\n\t" \
  "v_smfmac_f32_16x16x64_bf16 v[92:95], v[4:7], v[200:207], v248\n\t" \
  "ds_read_u16_d16_hi v167, v167 offset:0\n\t" \
  "ds_read_b64_tr_b16 v[4:5], v250 offset:20568\n\t" \
  "v_lshlrev_b32_sdwa v172, 1, v172 dst_sel:DWORD dst_unused:UNUSED_PAD src0_sel:DWORD src1_sel:WORD_1\n\t" \
  "v_mfma_f32_16x16x32_bf16 v[192:195], v[8:11], v[120:123], 0\n\t" \
  "ds_read_u16_d16_hi v172, v172 offset:0\n\t" \
  "ds_read_b64_tr_b16 v[6:7], v250 offset:21336\n\t" \
  "v_mfma_f32_16x16x32_bf16 v[200:203], v[8:11], v[132:135], 0\n\t" \
  "s_waitcnt lgkmcnt(14)\n\t" \
  "ds_read_b64_tr_b16 v[8:9], v250 offset:20624\n\t" \
  "ds_read_b64_tr_b16 v[10:11], v250 offset:21392\n\t" \
  "v_lshlrev_b32_sdwa v173, 1, v173 dst_sel:DWORD dst_unused:UNUSED_PAD src0_sel:DWORD src1_sel:WORD_1\n\t" \
  "v_mfma_f32_16x16x32_bf16 v[192:195], v[12:15], v[124:127], v[192:195]\n\t" \
  "ds_read_u16_d16_hi v173, v173 offset:0\n\t" \
  "v_lshlrev_b32_sdwa v174, 1, v174 dst_sel:DWORD dst_unused:UNUSED_PAD src0_sel:DWORD src1_sel:WORD_1\n\t" \
  "v_mfma_f32_16x16x32_bf16 v[200:203], v[12:15], v[136:139], v[200:203]\n\t" \
  "ds_read_b64_tr_b16 v[12:13], v250 offset:20632\n\t" \
  "ds_read_u16_d16_hi v174, v174 offset:0\n\t" \
  "v_mfma_f32_16x16x32_bf16 v[192:195], v[208:211], v[128:131], v[192:195]\n\t" \
  "ds_read_b64_tr_b16 v[14:15], v250 offset:21400\n\t" \
  "v_lshlrev_b32_sdwa v175, 1, v175 dst_sel:DWORD dst_unused:UNUSED_PAD src0_sel:DWORD src1_sel:WORD_1\n\t" \
  "v_mfma_f32_16x16x32_bf16 v[200:203], v[208:211], v[140:143], v[200:203]\n\t" \
  "ds_read_u16_d16_hi v175, v175 offset:0\n\t" \
  "ds_read_b64_tr_b16 v[208:209], v250 offset:20688\n\t" \
  "s_nop 2\n\t" \
  "v_lshlrev_b32_sdwa v192, 1, v192 dst_sel:DWORD dst_unused:UNUSED_PAD src0_sel:DWORD src1_sel:WORD_1\n\t" \
  "v_mfma_f32_16x16x32_bf16 v[196:199], v[212:215], v[120:123], 0\n\t" \
  "ds_read_u16_d16_hi v192, v192 offset:0\n\t" \
  "ds_read_b64_tr_b16 v[210:211], v250 offset:21456\n\t" \
  "v_mfma_f32_16x16x32_bf16 v[204:207], v[212:215], v[132:135], 0\n\t" \
  "s_waitcnt lgkmcnt(14)\n\t" \
  "ds_read_b64_tr_b16 v[212:213], v250 offset:20696\n\t" \
  "ds_read_b64_tr_b16 v[214:215], v250 offset:21464\n\t" \
  "v_lshlrev_b32_sdwa v193, 1, v193 dst_sel:DWORD dst_unused:UNUSED_PAD src0_sel:DWORD src1_sel:WORD_1\n\t" \
  "v_mfma_f32_16x16x32_bf16 v[196:199], v[216:219], v[124:127], v[196:199]\n\t" \
  "ds_read_u16_d16_hi v193, v193 offset:0\n\t" \
  "v_lshlrev_b32_sdwa v194, 1, v194 dst_sel:DWORD dst_unused:UNUSED_PAD src0_sel:DWORD src1_sel:WORD_1\n\t" \
  "v_mfma_f32_16x16x32_bf16 v[204:207], v[216:219], v[136:139], v[204:207]\n\t" \
  "ds_read_b64_tr_b16 v[216:217], v249 offset:45152\n\t" \
  "ds_read_u16_d16_hi v194, v194 offset:0\n\t" \
  "v_mfma_f32_16x16x32_bf16 v[196:199], v[220:223], v[128:131], v[196:199]\n\t" \
  "ds_read_b64_tr_b16 v[218:219], v249 offset:45408\n\t" \
  "v_lshlrev_b32_sdwa v195, 1, v195 dst_sel:DWORD dst_unused:UNUSED_PAD src0_sel:DWORD src1_sel:WORD_1\n\t" \
  "v_mfma_f32_16x16x32_bf16 v[204:207], v[220:223], v[140:143], v[204:207]\n\t" \
  "s_waitcnt lgkmcnt(14)\n\t" \
  "ds_read_u16_d16_hi v195, v195 offset:0\n\t" \
  "ds_read_b64_tr_b16 v[220:221], v249 offset:47200\n\t" \
  "v_lshlrev_b32_sdwa v200, 1, v200 dst_sel:DWORD dst_unused:UNUSED_PAD src0_sel:DWORD src1_sel:WORD_1\n\t" \
  "v_smfmac_f32_16x16x64_bf16 v[48:51], v[0:3], v[160:167], v248\n\t" \
  "s_waitcnt lgkmcnt(11)\n\t" \
  "ds_read_u16_d16_hi v200, v200 offset:0\n\t" \
  "ds_read_b64_tr_b16 v[222:223], v249 offset:47456\n\t" \
  "v_smfmac_f32_16x16x64_bf16 v[72:75], v[0:3], v[168:175], v248\n\t" \
  "ds_read_b64_tr_b16 v[0:1], v249 offset:49248\n\t" \
  "ds_read_b64_tr_b16 v[2:3], v249 offset:49504\n\t" \
  "v_lshlrev_b32_sdwa v201, 1, v201 dst_sel:DWORD dst_unused:UNUSED_PAD src0_sel:DWORD src1_sel:WORD_1\n\t" \
  "v_smfmac_f32_16x16x64_bf16 v[52:55], v[4:7], v[160:167], v248\n\t" \
  "ds_read_u16_d16_hi v201, v201 offset:0\n\t" \
  "v_lshlrev_b32_sdwa v202, 1, v202 dst_sel:DWORD dst_unused:UNUSED_PAD src0_sel:DWORD src1_sel:WORD_1\n\t" \
  "v_smfmac_f32_16x16x64_bf16 v[76:79], v[4:7], v[168:175], v248\n\t" \
  "ds_read_b64_tr_b16 v[4:5], v249 offset:45664\n\t" \
  "ds_read_u16_d16_hi v202, v202 offset:0\n\t" \
  "v_smfmac_f32_16x16x64_bf16 v[56:59], v[8:11], v[160:167], v248\n\t" \
  "ds_read_b64_tr_b16 v[6:7], v249 offset:45920\n\t" \
  "v_lshlrev_b32_sdwa v203, 1, v203 dst_sel:DWORD dst_unused:UNUSED_PAD src0_sel:DWORD src1_sel:WORD_1\n\t" \
  "v_smfmac_f32_16x16x64_bf16 v[80:83], v[8:11], v[168:175], v248\n\t" \
  "ds_read_u16_d16_hi v203, v203 offset:0\n\t" \
  "ds_read_b64_tr_b16 v[8:9], v249 offset:47712\n\t" \
  "v_lshlrev_b32_sdwa v196, 1, v196 dst_sel:DWORD dst_unused:UNUSED_PAD src0_sel:DWORD src1_sel:WORD_1\n\t" \
  "v_smfmac_f32_16x16x64_bf16 v[60:63], v[12:15], v[160:167], v248\n\t" \
  "ds_read_u16_d16_hi v196, v196 offset:0\n\t" \
  "ds_read_b64_tr_b16 v[10:11], v249 offset:47968\n\t" \
  "v_smfmac_f32_16x16x64_bf16 v[84:87], v[12:15], v[168:175], v248\n\t" \
  "s_waitcnt lgkmcnt(14)\n\t" \
  "ds_read_b64_tr_b16 v[12:13], v249 offset:49760\n\t" \
  "ds_read_b64_tr_b16 v[14:15], v249 offset:50016\n\t" \
  "v_lshlrev_b32_sdwa v197, 1, v197 dst_sel:DWORD dst_unused:UNUSED_PAD src0_sel:DWORD src1_sel:WORD_1\n\t" \
  "v_smfmac_f32_16x16x64_bf16 v[64:67], v[208:211], v[160:167], v248\n\t" \
  "ds_read_u16_d16_hi v197, v197 offset:0\n\t" \
  "v_lshlrev_b32_sdwa v198, 1, v198 dst_sel:DWORD dst_unused:UNUSED_PAD src0_sel:DWORD src1_sel:WORD_1\n\t" \
  "v_smfmac_f32_16x16x64_bf16 v[88:91], v[208:211], v[168:175], v248\n\t" \
  "ds_read_b64_tr_b16 v[208:209], v250 offset:32880\n\t" \
  "ds_read_u16_d16_hi v198, v198 offset:0\n\t" \
  "v_smfmac_f32_16x16x64_bf16 v[68:71], v[212:215], v[160:167], v248\n\t" \
  "ds_read_b64_tr_b16 v[210:211], v250 offset:33648\n\t" \
  "v_lshlrev_b32_sdwa v199, 1, v199 dst_sel:DWORD dst_unused:UNUSED_PAD src0_sel:DWORD src1_sel:WORD_1\n\t" \
  "v_smfmac_f32_16x16x64_bf16 v[92:95], v[212:215], v[168:175], v248\n\t" \
  "ds_read_u16_d16_hi v199, v199 offset:0\n\t" \
  "ds_read_b64_tr_b16 v[212:213], v250 offset:32888\n\t" \
  "v_lshlrev_b32_sdwa v204, 1, v204 dst_sel:DWORD dst_unused:UNUSED_PAD src0_sel:DWORD src1_sel:WORD_1\n\t" \
  "v_mfma_f32_16x16x32_bf16 v[168:171], v[216:219], v[132:135], 0\n\t" \
  "s_waitcnt lgkmcnt(14)\n\t" \
  "ds_read_b64_tr_b16 v[214:215], v250 offset:33656\n\t" \
  "ds_read_u16_d16_hi v204, v204 offset:0\n\t" \
  "ds_read_b64_tr_b16 v[216:217], v250 offset:32944\n\t" \
  "v_lshlrev_b32_sdwa v205, 1, v205 dst_sel:DWORD dst_unused:UNUSED_PAD src0_sel:DWORD src1_sel:WORD_1\n\t" \
  "v_mfma_f32_16x16x32_bf16 v[168:171], v[220:223], v[136:139], v[168:171]\n\t" \
  "ds_read_b64_tr_b16 v[218:219], v250 offset:33712\n\t" \
  "ds_read_u16_d16_hi v205, v205 offset:0\n\t" \
  "ds_read_b64_tr_b16 v[220:221], v250 offset:32952\n\t" \
  "v_lshlrev_b32_sdwa v206, 1, v206 dst_sel:DWORD dst_unused:UNUSED_PAD src0_sel:DWORD src1_sel:WORD_1\n\t" \
  "v_lshlrev_b32_sdwa v207, 1, v207 dst_sel:DWORD dst_unused:UNUSED_PAD src0_sel:DWORD src1_sel:WORD_1\n\t" \
  "v_mfma_f32_16x16x32_bf16 v[168:171], v[0:3], v[140:143], v[168:171]\n\t" \
  "s_waitcnt lgkmcnt(14)\n\t" \
  "ds_read_b64_tr_b16 v[222:223], v250 offset:33720\n\t" \
  "ds_read_u16_d16_hi v206, v206 offset:0\n\t" \
  "ds_read_b64_tr_b16 v[0:1], v250 offset:33008\n\t" \
  "s_nop 3\n\t" \
  "v_lshlrev_b32_sdwa v168, 1, v168 dst_sel:DWORD dst_unused:UNUSED_PAD src0_sel:DWORD src1_sel:WORD_1\n\t" \
  "v_mfma_f32_16x16x32_bf16 v[172:175], v[4:7], v[132:135], 0\n\t" \
  "ds_read_b64_tr_b16 v[2:3], v250 offset:33776\n\t" \
  "ds_read_u16_d16_hi v207, v207 offset:0\n\t" \
  "ds_read_b64_tr_b16 v[4:5], v250 offset:33016\n\t" \
  "v_mfma_f32_16x16x32_bf16 v[172:175], v[8:11], v[136:139], v[172:175]\n\t" \
  "s_waitcnt lgkmcnt(14)\n\t" \
  "ds_read_b64_tr_b16 v[6:7], v250 offset:33784\n\t" \
  "ds_read_u16_d16_hi v168, v168 offset:0\n\t" \
  "ds_read_b64_tr_b16 v[8:9], v249 offset:57472\n\t" \
  "v_lshlrev_b32_sdwa v169, 1, v169 dst_sel:DWORD dst_unused:UNUSED_PAD src0_sel:DWORD src1_sel:WORD_1\n\t" \
  "v_mfma_f32_16x16x32_bf16 v[172:175], v[12:15], v[140:143], v[172:175]\n\t" \
  "s_waitcnt lgkmcnt(14)\n\t" \
  "ds_read_b64_tr_b16 v[10:11], v249 offset:57728\n\t" \
  "ds_read_u16_d16_hi v169, v169 offset:0\n\t" \
  "ds_read_b64_tr_b16 v[12:13], v249 offset:59520\n\t" \
  "v_lshlrev_b32_sdwa v170, 1, v170 dst_sel:DWORD dst_unused:UNUSED_PAD src0_sel:DWORD src1_sel:WORD_1\n\t" \
  "v_smfmac_f32_16x16x64_bf16 v[48:51], v[208:211], v[192:199], v248\n\t" \
  "s_waitcnt lgkmcnt(7)\n\t" \
  "ds_read_u16_d16_hi v170, v170 offset:0\n\t" \
  "ds_read_b64_tr_b16 v[14:15], v249 offset:59776\n\t" \
  "v_smfmac_f32_16x16x64_bf16 v[72:75], v[208:211], v[200:207], v248\n\t" \
  "ds_read_b64_tr_b16 v[208:209], v249 offset:61568\n\t" \
  "ds_read_b64_tr_b16 v[210:211], v249 offset:61824\n\t" \
  "v_smfmac_f32_16x16x64_bf16 v[52:55], v[212:215], v[192:199], v248\n\t" \
  "v_lshlrev_b32_sdwa v171, 1, v171 dst_sel:DWORD dst_unused:UNUSED_PAD src0_sel:DWORD src1_sel:WORD_1\n\t" \
  "v_cvt_pk_bf16_f32 v230, v48, v49\n\t" \
  "v_smfmac_f32_16x16x64_bf16 v[76:79], v[212:215], v[200:207], v248\n\t" \
  "ds_read_u16_d16_hi v171, v171 offset:0\n\t" \
  "ds_read_b64_tr_b16 v[212:213], v249 offset:57984\n\t" \
  "v_cvt_pk_bf16_f32 v231, v50, v51\n\t" \
  "v_smfmac_f32_16x16x64_bf16 v[56:59], v[216:219], v[192:199], v248\n\t" \
  "ds_read_b64_tr_b16 v[214:215], v249 offset:58240\n\t" \
  "v_cvt_pk_bf16_f32 v232, v52, v53\n\t" \
  "v_smfmac_f32_16x16x64_bf16 v[80:83], v[216:219], v[200:207], v248\n\t" \
  "ds_read_b64_tr_b16 v[216:217], v249 offset:60032\n\t" \
  "ds_read_b64_tr_b16 v[218:219], v249 offset:60288\n\t" \
  "v_cvt_pk_bf16_f32 v233, v54, v55\n\t" \
  "v_lshlrev_b32_sdwa v172, 1, v172 dst_sel:DWORD dst_unused:UNUSED_PAD src0_sel:DWORD src1_sel:WORD_1\n\t" \
  "v_smfmac_f32_16x16x64_bf16 v[60:63], v[220:223], v[192:199], v248\n\t" \
  "buffer_store_dwordx4 v[230:233], v253, %[osrd], %[ob] offen offset:0 sc1\n\t" \
  "ds_read_u16_d16_hi v172, v172 offset:0\n\t" \
  "v_smfmac_f32_16x16x64_bf16 v[84:87], v[220:223], v[200:207], v248\n\t" \
  "ds_read_b64_tr_b16 v[220:221], v249 offset:62080\n\t" \
  "ds_read_b64_tr_b16 v[222:223], v249 offset:62336\n\t" \
  "v_cvt_pk_bf16_f32 v234, v56, v57\n\t" \
  "v_cvt_pk_bf16_f32 v235, v58, v59\n\t" \
  "v_smfmac_f32_16x16x64_bf16 v[64:67], v[0:3], v[192:199], v248\n\t" \
  "v_cvt_pk_bf16_f32 v236, v60, v61\n\t" \
  "v_lshlrev_b32_sdwa v173, 1, v173 dst_sel:DWORD dst_unused:UNUSED_PAD src0_sel:DWORD src1_sel:WORD_1\n\t" \
  "v_smfmac_f32_16x16x64_bf16 v[88:91], v[0:3], v[200:207], v248\n\t" \
  "s_waitcnt lgkmcnt(14)\n\t" \
  "ds_read_u16_d16_hi v173, v173 offset:0\n\t" \
  "ds_read_b64_tr_b16 v[0:1], v250 offset:45200\n\t" \
  "v_cvt_pk_bf16_f32 v237, v62, v63\n\t" \
  "v_smfmac_f32_16x16x64_bf16 v[68:71], v[4:7], v[192:199], v248\n\t" \
  "buffer_store_dwordx4 v[234:237], v253, %[osrd], %[ob] offen offset:64 sc1\n\t" \
  "ds_read_b64_tr_b16 v[2:3], v250 offset:45968\n\t" \
  "v_smfmac_f32_16x16x64_bf16 v[92:95], v[4:7], v[200:207], v248\n\t" \
  "ds_read_b64_tr_b16 v[4:5], v250 offset:45208\n\t" \
  "ds_read_b64_tr_b16 v[6:7], v250 offset:45976\n\t" \
  "v_cvt_pk_bf16_f32 v238, v64, v65\n\t" \
  "v_lshlrev_b32_sdwa v174, 1, v174 dst_sel:DWORD dst_unused:UNUSED_PAD src0_sel:DWORD src1_sel:WORD_1\n\t" \
  "v_cvt_pk_bf16_f32 v239, v66, v67\n\t" \
  "v_cvt_pk_bf16_f32 v240, v68, v69\n\t" \
  "v_mfma_f32_16x16x32_bf16 v[200:203], v[8:11], v[132:135], 0\n\t" \
  "s_waitcnt lgkmcnt(14)\n\t" \
  "ds_read_u16_d16_hi v174, v174 offset:0\n\t" \
  "ds_read_b64_tr_b16 v[8:9], v250 offset:45264\n\t" \
  "ds_read_b64_tr_b16 v[10:11], v250 offset:46032\n\t" \
  "v_cvt_pk_bf16_f32 v241, v70, v71\n\t" \
  "v_mfma_f32_16x16x32_bf16 v[200:203], v[12:15], v[136:139], v[200:203]\n\t" \
  "s_waitcnt lgkmcnt(14)\n\t" \
  "buffer_store_dwordx4 v[238:241], v253, %[osrd], %[ob] offen offset:128 sc1\n\t" \
  "ds_read_b64_tr_b16 v[12:13], v250 offset:45272\n\t" \
  "ds_read_b64_tr_b16 v[14:15], v250 offset:46040\n\t" \
  "v_lshlrev_b32_sdwa v175, 1, v175 dst_sel:DWORD dst_unused:UNUSED_PAD src0_sel:DWORD src1_sel:WORD_1\n\t" \
  "v_mfma_f32_16x16x32_bf16 v[200:203], v[208:211], v[140:143], v[200:203]\n\t" \
  "s_waitcnt lgkmcnt(14)\n\t" \
  "ds_read_u16_d16_hi v175, v175 offset:0\n\t" \
  "ds_read_b64_tr_b16 v[208:209], v250 offset:45328\n\t" \
  "ds_read_b64_tr_b16 v[210:211], v250 offset:46096\n\t" \
  "s_nop 3\n\t" \
  "v_lshlrev_b32_sdwa v200, 1, v200 dst_sel:DWORD dst_unused:UNUSED_PAD src0_sel:DWORD src1_sel:WORD_1\n\t" \
  "v_mfma_f32_16x16x32_bf16 v[204:207], v[212:215], v[132:135], 0\n\t" \
  "s_waitcnt lgkmcnt(14)\n\t" \
  "ds_read_u16_d16_hi v200, v200 offset:0\n\t" \
  "ds_read_b64_tr_b16 v[212:213], v250 offset:45336\n\t" \
  "ds_read_b64_tr_b16 v[214:215], v250 offset:46104\n\t" \
  "v_mfma_f32_16x16x32_bf16 v[204:207], v[216:219], v[136:139], v[204:207]\n\t" \
  "s_waitcnt lgkmcnt(14)\n\t" \
  "v_add_u32 v249, 61440, v249\n\t" \
  "ds_read_b64_tr_b16 v[216:217], v249 offset:8352\n\t" \
  "ds_read_b64_tr_b16 v[218:219], v249 offset:8608\n\t" \
  "v_lshlrev_b32_sdwa v201, 1, v201 dst_sel:DWORD dst_unused:UNUSED_PAD src0_sel:DWORD src1_sel:WORD_1\n\t" \
  "v_mfma_f32_16x16x32_bf16 v[204:207], v[220:223], v[140:143], v[204:207]\n\t" \
  "s_waitcnt lgkmcnt(7)\n\t" \
  "ds_read_u16_d16_hi v201, v201 offset:0\n\t" \
  "ds_read_b64_tr_b16 v[220:221], v249 offset:10400\n\t" \
  "ds_read_b64_tr_b16 v[222:223], v249 offset:10656\n\t" \
  "v_lshlrev_b32_sdwa v202, 1, v202 dst_sel:DWORD dst_unused:UNUSED_PAD src0_sel:DWORD src1_sel:WORD_1\n\t" \
  "v_smfmac_f32_16x16x64_bf16 v[72:75], v[0:3], v[168:175], v248\n\t" \
  "ds_read_u16_d16_hi v202, v202 offset:0\n\t" \
  "ds_read_b64_tr_b16 v[0:1], v249 offset:12448\n\t" \
  "ds_read_b64_tr_b16 v[2:3], v249 offset:12704\n\t" \
  "v_smfmac_f32_16x16x64_bf16 v[76:79], v[4:7], v[168:175], v248\n\t" \
  "s_add_u32 %[ob], %[ob], %[dob]\n\t" \
  "ds_read_b64_tr_b16 v[4:5], v249 offset:8864\n\t" \
  "ds_read_b64_tr_b16 v[6:7], v249 offset:9120\n\t" \
  "v_lshlrev_b32_sdwa v203, 1, v203 dst_sel:DWORD dst_unused:UNUSED_PAD src0_sel:DWORD src1_sel:WORD_1\n\t" \
  "v_smfmac_f32_16x16x64_bf16 v[80:83], v[8:11], v[168:175], v248\n\t" \
  "ds_read_u16_d16_hi v203, v203 offset:0\n\t" \
  "ds_read_b64_tr_b16 v[8:9], v249 offset:10912\n\t" \
  "ds_read_b64_tr_b16 v[10:11], v249 offset:11168\n\t" \
  "v_lshlrev_b32_sdwa v204, 1, v204 dst_sel:DWORD dst_unused:UNUSED_PAD src0_sel:DWORD src1_sel:WORD_1\n\t" \
  "v_smfmac_f32_16x16x64_bf16 v[84:87], v[12:15], v[168:175], v248\n\t" \
  "s_waitcnt lgkmcnt(14)\n\t" \
  "ds_read_u16_d16_hi v204, v204 offset:0\n\t" \
  "ds_read_b64_tr_b16 v[12:13], v249 offset:12960\n\t" \
  "ds_read_b64_tr_b16 v[14:15], v249 offset:13216\n\t" \
  "v_smfmac_f32_16x16x64_bf16 v[88:91], v[208:211], v[168:175], v248\n\t" \
  "s_waitcnt lgkmcnt(14)\n\t" \
  "ds_read_b64_tr_b16 v[208:209], v250 offset:57520\n\t" \
  "ds_read_b64_tr_b16 v[210:211], v250 offset:58288\n\t" \
  "v_lshlrev_b32_sdwa v205, 1, v205 dst_sel:DWORD dst_unused:UNUSED_PAD src0_sel:DWORD src1_sel:WORD_1\n\t" \
  "v_smfmac_f32_16x16x64_bf16 v[92:95], v[212:215], v[168:175], v248\n\t" \
  "ds_read_u16_d16_hi v205, v205 offset:0\n\t" \
  "ds_read_b64_tr_b16 v[212:213], v250 offset:57528\n\t" \
  "ds_read_b64_tr_b16 v[214:215], v250 offset:58296\n\t" \
  "v_lshlrev_b32_sdwa v206, 1, v206 dst_sel:DWORD dst_unused:UNUSED_PAD src0_sel:DWORD src1_sel:WORD_1\n\t" \
  "v_mfma_f32_16x16x32_bf16 v[168:171], v[216:219], v[132:135], 0\n\t" \
  "s_waitcnt lgkmcnt(14)\n\t" \
  "ds_read_u16_d16_hi v206, v206 offset:0\n\t" \
  "ds_read_b64_tr_b16 v[216:217], v250 offset:57584\n\t" \
  "ds_read_b64_tr_b16 v[218:219], v250 offset:58352\n\t" \
  "v_mfma_f32_16x16x32_bf16 v[168:171], v[220:223], v[136:139], v[168:171]\n\t" \
  "s_waitcnt lgkmcnt(14)\n\t" \
  "ds_read_b64_tr_b16 v[220:221], v250 offset:57592\n\t" \
  "ds_read_b64_tr_b16 v[222:223], v250 offset:58360\n\t" \
  "v_lshlrev_b32_sdwa v207, 1, v207 dst_sel:DWORD dst_unused:UNUSED_PAD src0_sel:DWORD src1_sel:WORD_1\n\t" \
  "v_mfma_f32_16x16x32_bf16 v[168:171], v[0:3], v[140:143], v[168:171]\n\t" \
  "ds_read_u16_d16_hi v207, v207 offset:0\n\t" \
  "ds_read_b64_tr_b16 v[0:1], v250 offset:57648\n\t" \
  "ds_read_b64_tr_b16 v[2:3], v250 offset:58416\n\t" \
  "s_nop 4\n\t" \
  "v_lshlrev_b32_sdwa v168, 1, v168 dst_sel:DWORD dst_unused:UNUSED_PAD src0_sel:DWORD src1_sel:WORD_1\n\t" \
  "v_mfma_f32_16x16x32_bf16 v[172:175], v[4:7], v[132:135], 0\n\t" \
  "s_waitcnt lgkmcnt(14)\n\t" \
  "ds_read_u16_d16_hi v168, v168 offset:0\n\t" \
  "ds_read_b64_tr_b16 v[4:5], v250 offset:57656\n\t" \
  "ds_read_b64_tr_b16 v[6:7], v250 offset:58424\n\t" \
  "v_mfma_f32_16x16x32_bf16 v[172:175], v[8:11], v[136:139], v[172:175]\n\t" \
  "s_waitcnt lgkmcnt(14)\n\t" \
  "v_add_u32 v250, 61440, v250\n\t" \
  "ds_read_b64_tr_b16 v[8:9], v250 offset:8400\n\t" \
  "ds_read_b64_tr_b16 v[10:11], v250 offset:9168\n\t" \
  "v_lshlrev_b32_sdwa v169, 1, v169 dst_sel:DWORD dst_unused:UNUSED_PAD src0_sel:DWORD src1_sel:WORD_1\n\t" \
  "v_mfma_f32_16x16x32_bf16 v[172:175], v[12:15], v[140:143], v[172:175]\n\t" \
  "s_waitcnt lgkmcnt(7)\n\t" \
  "ds_read_u16_d16_hi v169, v169 offset:0\n\t" \
  "ds_read_b64_tr_b16 v[12:13], v250 offset:8408\n\t" \
  "ds_read_b64_tr_b16 v[14:15], v250 offset:9176\n\t" \
  "v_lshlrev_b32_sdwa v170, 1, v170 dst_sel:DWORD dst_unused:UNUSED_PAD src0_sel:DWORD src1_sel:WORD_1\n\t" \
  "v_smfmac_f32_16x16x64_bf16 v[72:75], v[208:211], v[200:207], v248\n\t" \
  "ds_read_u16_d16_hi v170, v170 offset:0\n\t" \
  "ds_read_b64_tr_b16 v[208:209], v250 offset:8464\n\t" \
  "ds_read_b64_tr_b16 v[210:211], v250 offset:9232\n\t" \
  "v_smfmac_f32_16x16x64_bf16 v[76:79], v[212:215], v[200:207], v248\n\t" \
  "ds_read_b64_tr_b16 v[212:213], v250 offset:8472\n\t" \
  "ds_read_b64_tr_b16 v[214:215], v250 offset:9240\n\t" \
  "v_lshlrev_b32_sdwa v171, 1, v171 dst_sel:DWORD dst_unused:UNUSED_PAD src0_sel:DWORD src1_sel:WORD_1\n\t" \
  "v_smfmac_f32_16x16x64_bf16 v[80:83], v[216:219], v[200:207], v248\n\t" \
  "ds_read_u16_d16_hi v171, v171 offset:0\n\t" \
  "ds_read_b64_tr_b16 v[216:217], v250 offset:8528\n\t" \
  "ds_read_b64_tr_b16 v[218:219], v250 offset:9296\n\t" \
  "v_lshlrev_b32_sdwa v172, 1, v172 dst_sel:DWORD dst_unused:UNUSED_PAD src0_sel:DWORD src1_sel:WORD_1\n\t" \
  "v_smfmac_f32_16x16x64_bf16 v[84:87], v[220:223], v[200:207], v248\n\t" \
  "s_waitcnt lgkmcnt(14)\n\t" \
  "ds_read_u16_d16_hi v172, v172 offset:0\n\t" \
  "ds_read_b64_tr_b16 v[220:221], v250 offset:8536\n\t" \
  "ds_read_b64_tr_b16 v[222:223], v250 offset:9304\n\t" \
  "v_smfmac_f32_16x16x64_bf16 v[88:91], v[0:3], v[200:207], v248\n\t" \
  "s_waitcnt lgkmcnt(14)\n\t" \
  "v_lshlrev_b32_sdwa v173, 1, v173 dst_sel:DWORD dst_unused:UNUSED_PAD src0_sel:DWORD src1_sel:WORD_1\n\t" \
  "v_lshlrev_b32_sdwa v174, 1, v174 dst_sel:DWORD dst_unused:UNUSED_PAD src0_sel:DWORD src1_sel:WORD_1\n\t" \
  "v_smfmac_f32_16x16x64_bf16 v[92:95], v[4:7], v[200:207], v248\n\t" \
  "ds_read_u16_d16_hi v173, v173 offset:0\n\t" \
  "ds_read_u16_d16_hi v174, v174 offset:0\n\t" \
  "v_lshlrev_b32_sdwa v175, 1, v175 dst_sel:DWORD dst_unused:UNUSED_PAD src0_sel:DWORD src1_sel:WORD_1\n\t" \
  "ds_read_u16_d16_hi v175, v175 offset:0\n\t" \
  "s_waitcnt lgkmcnt(0)\n\t" \
  "v_smfmac_f32_16x16x64_bf16 v[72:75], v[8:11], v[168:175], v248\n\t" \
  "v_smfmac_f32_16x16x64_bf16 v[76:79], v[12:15], v[168:175], v248\n\t" \
  "s_nop 6\n\t" \
  "v_cvt_pk_bf16_f32 v230, v72, v73\n\t" \
  "v_cvt_pk_bf16_f32 v231, v74, v75\n\t" \
  "v_smfmac_f32_16x16x64_bf16 v[80:83], v[208:211], v[168:175], v248\n\t" \
  "v_cvt_pk_bf16_f32 v232, v76, v77\n\t" \
  "v_cvt_pk_bf16_f32 v233, v78, v79\n\t" \
  "v_smfmac_f32_16x16x64_bf16 v[84:87], v[212:215], v[168:175], v248\n\t" \
  "buffer_store_dwordx4 v[230:233], v253, %[osrd], %[ob] offen offset:0 sc1\n\t" \
  "s_nop 3\n\t" \
  "v_cvt_pk_bf16_f32 v234, v80, v81\n\t" \
  "v_cvt_pk_bf16_f32 v235, v82, v83\n\t" \
  "v_smfmac_f32_16x16x64_bf16 v[88:91], v[216:219], v[168:175], v248\n\t" \
  "v_cvt_pk_bf16_f32 v236, v84, v85\n\t" \
  "v_cvt_pk_bf16_f32 v237, v86, v87\n\t" \
  "v_smfmac_f32_16x16x64_bf16 v[92:95], v[220:223], v[168:175], v248\n\t" \
  "buffer_store_dwordx4 v[234:237], v253, %[osrd], %[ob] offen offset:64 sc1\n\t" \
  "s_nop 3\n\t" \
  "v_cvt_pk_bf16_f32 v238, v88, v89\n\t" \
  "v_cvt_pk_bf16_f32 v239, v90, v91\n\t" \
  "s_nop 0\n\t" \
  "v_cvt_pk_bf16_f32 v240, v92, v93\n\t" \
  "v_cvt_pk_bf16_f32 v241, v94, v95\n\t" \
  "buffer_store_dwordx4 v[238:241], v253, %[osrd], %[ob] offen offset:128 sc1\n\t" \
  ""
#define F_ASM \
  "s_mov_b32 %[lc], 3\n\t" \
  "s_setprio 3\n\t" \
  "s_mul_i32 %[t0], %[dgy], 7\n\t" \
  "s_add_u32 %[gy], %[gy], %[t0]\n\t" \
  "s_add_u32 %[xld], %[xlb], 15184\n\t" \
  "v_mov_b32 v252, 0\n\t" \
  "v_mov_b32 v253, 0\n\t" \
  "s_waitcnt vmcnt(0)\n\t" \
  "s_barrier\n\t" \
  "v_mfma_f32_32x32x8_bf16 v[0:15], v[252:253], v[252:253], 0\n\t" \
  "ds_read_u16 v96, v251 offset:0\n\t" \
  "ds_read_u16 v97, v251 offset:416\n\t" \
  "ds_read_u16 v98, v251 offset:2064\n\t" \
  "ds_read_u16 v99, v251 offset:2480\n\t" \
  "ds_read_u16 v112, v251 offset:8\n\t" \
  "ds_read_u16 v113, v251 offset:424\n\t" \
  "ds_read_u16 v114, v251 offset:2072\n\t" \
  "ds_read_u16 v115, v251 offset:2488\n\t" \
  "v_mfma_f32_32x32x8_bf16 v[16:31], v[252:253], v[252:253], 0\n\t" \
  "ds_read_u16 v128, v251 offset:16\n\t" \
  "ds_read_u16 v129, v251 offset:432\n\t" \
  "ds_read_u16 v130, v251 offset:2080\n\t" \
  "ds_read_u16 v131, v251 offset:2496\n\t" \
  "ds_read_u16 v144, v251 offset:24\n\t" \
  "ds_read_u16 v145, v251 offset:440\n\t" \
  "ds_read_u16 v146, v251 offset:2088\n\t" \
  "ds_read_u16 v147, v251 offset:2504\n\t" \
  "v_mfma_f32_32x32x8_bf16 v[32:47], v[252:253], v[252:253], 0\n\t" \
  "ds_read_u16 v160, v251 offset:32\n\t" \
  "ds_read_u16 v161, v251 offset:448\n\t" \
  "ds_read_u16 v162, v251 offset:2096\n\t" \
  "ds_read_u16 v163, v251 offset:2512\n\t" \
  "ds_read_u16 v176, v251 offset:40\n\t" \
  "ds_read_u16 v177, v251 offset:456\n\t" \
  "ds_read_u16 v178, v251 offset:2104\n\t" \
  "ds_read_u16 v179, v251 offset:2520\n\t" \
  "v_mfma_f32_32x32x8_bf16 v[48:63], v[252:253], v[252:253], 0\n\t" \
  "ds_read_u16 v100, v251 offset:4128\n\t" \
  "ds_read_u16 v101, v251 offset:4544\n\t" \
  "ds_read_u16 v102, v251 offset:6192\n\t" \
  "ds_read_u16 v103, v251 offset:6608\n\t" \
  "ds_read_u16 v116, v251 offset:4136\n\t" \
  "ds_read_u16 v117, v251 offset:4552\n\t" \
  "ds_read_u16 v118, v251 offset:6200\n\t" \
  "ds_read_u16 v119, v251 offset:6616\n\t" \
  "v_mfma_f32_32x32x8_bf16 v[64:79], v[252:253], v[252:253], 0\n\t" \
  "ds_read_u16 v132, v251 offset:4144\n\t" \
  "ds_read_u16 v133, v251 offset:4560\n\t" \
  "ds_read_u16 v134, v251 offset:6208\n\t" \
  "ds_read_u16 v135, v251 offset:6624\n\t" \
  "ds_read_u16 v148, v251 offset:4152\n\t" \
  "ds_read_u16 v149, v251 offset:4568\n\t" \
  "ds_read_u16 v150, v251 offset:6216\n\t" \
  "ds_read_u16 v151, v251 offset:6632\n\t" \
  "v_mfma_f32_32x32x8_bf16 v[80:95], v[252:253], v[252:253], 0\n\t" \
  "ds_read_u16 v164, v251 offset:4160\n\t" \
  "ds_read_u16 v165, v251 offset:4576\n\t" \
  "ds_read_u16 v166, v251 offset:6224\n\t" \
  "ds_read_u16 v167, v251 offset:6640\n\t" \
  "ds_read_u16 v180, v251 offset:4168\n\t" \
  "ds_read_u16 v181, v251 offset:4584\n\t" \
  "ds_read_u16 v182, v251 offset:6232\n\t" \
  "ds_read_u16 v183, v251 offset:6648\n\t" \
  "s_barrier\n\t" \
  "ds_read_b64 v[192:193], v241 offset:0\n\t" \
  "ds_read_b64 v[194:195], v241 offset:400\n\t" \
  "ds_read_b64 v[196:197], v241 offset:800\n\t" \
  "ds_read_b64 v[198:199], v241 offset:1200\n\t" \
  "ds_read_b64 v[200:201], v241 offset:8\n\t" \
  "ds_read_b64 v[202:203], v241 offset:408\n\t" \
  "ds_read_b64 v[204:205], v241 offset:808\n\t" \
  "ds_read_b64 v[206:207], v241 offset:1208\n\t" \
  "s_waitcnt lgkmcnt(4)\n\t" \
  "v_smfmac_f32_16x16x64_bf16 v[0:3], v[96:99], v[192:199], v240\n\t" \
  "ds_read_b64 v[208:209], v241 offset:16\n\t" \
  "ds_read_b64 v[210:211], v241 offset:416\n\t" \
  "ds_read_b64 v[212:213], v241 offset:816\n\t" \
  "ds_read_b64 v[214:215], v241 offset:1216\n\t" \
  "s_waitcnt lgkmcnt(4)\n\t" \
  "v_smfmac_f32_16x16x64_bf16 v[4:7], v[112:115], v[200:207], v240\n\t" \
  "ds_read_b64 v[216:217], v241 offset:24\n\t" \
  "ds_read_b64 v[218:219], v241 offset:424\n\t" \
  "ds_read_b64 v[220:221], v241 offset:824\n\t" \
  "ds_read_b64 v[222:223], v241 offset:1224\n\t" \
  "s_waitcnt lgkmcnt(4)\n\t" \
  "v_smfmac_f32_16x16x64_bf16 v[8:11], v[128:131], v[208:215], v240\n\t" \
  "ds_read_b64 v[192:193], v241 offset:32\n\t" \
  "ds_read_b64 v[194:195], v241 offset:432\n\t" \
  "ds_read_b64 v[196:197], v241 offset:832\n\t" \
  "ds_read_b64 v[198:199], v241 offset:1232\n\t" \
  "s_waitcnt lgkmcnt(4)\n\t" \
  "v_smfmac_f32_16x16x64_bf16 v[12:15], v[144:147], v[216:223], v240\n\t" \
  "ds_read_b64 v[200:201], v241 offset:40\n\t" \
  "ds_read_b64 v[202:203], v241 offset:440\n\t" \
  "ds_read_b64 v[204:205], v241 offset:840\n\t" \
  "ds_read_b64 v[206:207], v241 offset:1240\n\t" \
  "s_add_u32 %[cs], %[cs], 1\n\t" \
  "s_cmp_eq_u32 %[cs], 6\n\t" \
  "s_cselect_b32 %[cs], 0, %[cs]\n\t" \
  "s_cselect_b32 %[t0], -75920, %[ps]\n\t" \
  "v_add_u32 v241, %[t0], v241\n\t" \
  "s_barrier\n\t" \
  "s_waitcnt lgkmcnt(4)\n\t" \
  "v_smfmac_f32_16x16x64_bf16 v[16:19], v[160:163], v[192:199], v240\n\t" \
  "ds_read_b64 v[208:209], v241 offset:0\n\t" \
  "ds_read_b64 v[210:211], v241 offset:400\n\t" \
  "ds_read_b64 v[212:213], v241 offset:800\n\t" \
  "ds_read_b64 v[214:215], v241 offset:1200\n\t" \
  "s_waitcnt lgkmcnt(4)\n\t" \
  "v_smfmac_f32_16x16x64_bf16 v[20:23], v[176:179], v[200:207], v240\n\t" \
  "ds_read_b64 v[216:217], v241 offset:8\n\t" \
  "ds_read_b64 v[218:219], v241 offset:408\n\t" \
  "ds_read_b64 v[220:221], v241 offset:808\n\t" \
  "ds_read_b64 v[222:223], v241 offset:1208\n\t" \
  "s_waitcnt lgkmcnt(4)\n\t" \
  "v_smfmac_f32_16x16x64_bf16 v[0:3], v[100:103], v[208:215], v240\n\t" \
  "ds_read_b64 v[192:193], v241 offset:16\n\t" \
  "ds_read_b64 v[194:195], v241 offset:416\n\t" \
  "ds_read_b64 v[196:197], v241 offset:816\n\t" \
  "ds_read_b64 v[198:199], v241 offset:1216\n\t" \
  "ds_read_u16 v104, v251 offset:8256\n\t" \
  "v_smfmac_f32_16x16x64_bf16 v[24:27], v[96:99], v[208:215], v240\n\t" \
  "ds_read_u16 v105, v251 offset:8672\n\t" \
  "ds_read_u16 v106, v251 offset:10320\n\t" \
  "ds_read_u16 v107, v251 offset:10736\n\t" \
  "ds_read_u16 v120, v251 offset:8264\n\t" \
  "ds_read_u16 v121, v251 offset:8680\n\t" \
  "s_waitcnt lgkmcnt(10)\n\t" \
  "v_smfmac_f32_16x16x64_bf16 v[4:7], v[116:119], v[216:223], v240\n\t" \
  "ds_read_b64 v[200:201], v241 offset:24\n\t" \
  "ds_read_b64 v[202:203], v241 offset:424\n\t" \
  "ds_read_b64 v[204:205], v241 offset:824\n\t" \
  "ds_read_b64 v[206:207], v241 offset:1224\n\t" \
  "ds_read_u16 v122, v251 offset:10328\n\t" \
  "v_smfmac_f32_16x16x64_bf16 v[28:31], v[112:115], v[216:223], v240\n\t" \
  "ds_read_u16 v123, v251 offset:10744\n\t" \
  "ds_read_u16 v136, v251 offset:8272\n\t" \
  "ds_read_u16 v137, v251 offset:8688\n\t" \
  "ds_read_u16 v138, v251 offset:10336\n\t" \
  "ds_read_u16 v139, v251 offset:10752\n\t" \
  "s_waitcnt lgkmcnt(14)\n\t" \
  "v_smfmac_f32_16x16x64_bf16 v[8:11], v[132:135], v[192:199], v240\n\t" \
  "ds_read_b64 v[208:209], v241 offset:32\n\t" \
  "ds_read_b64 v[210:211], v241 offset:432\n\t" \
  "ds_read_b64 v[212:213], v241 offset:832\n\t" \
  "ds_read_b64 v[214:215], v241 offset:1232\n\t" \
  "ds_read_u16 v152, v251 offset:8280\n\t" \
  "v_smfmac_f32_16x16x64_bf16 v[32:35], v[128:131], v[192:199], v240\n\t" \
  "ds_read_u16 v153, v251 offset:8696\n\t" \
  "ds_read_u16 v154, v251 offset:10344\n\t" \
  "ds_read_u16 v155, v251 offset:10760\n\t" \
  "ds_read_u16 v168, v251 offset:8288\n\t" \
  "ds_read_u16 v169, v251 offset:8704\n\t" \
  "s_waitcnt lgkmcnt(14)\n\t" \
  "v_smfmac_f32_16x16x64_bf16 v[12:15], v[148:151], v[200:207], v240\n\t" \
  "ds_read_b64 v[216:217], v241 offset:40\n\t" \
  "ds_read_b64 v[218:219], v241 offset:440\n\t" \
  "ds_read_b64 v[220:221], v241 offset:840\n\t" \
  "ds_read_b64 v[222:223], v241 offset:1240\n\t" \
  "s_add_u32 %[cs], %[cs], 1\n\t" \
  "s_cmp_eq_u32 %[cs], 6\n\t" \
  "s_cselect_b32 %[cs], 0, %[cs]\n\t" \
  "s_cselect_b32 %[t0], -75920, %[ps]\n\t" \
  "v_add_u32 v241, %[t0], v241\n\t" \
  "ds_read_u16 v170, v251 offset:10352\n\t" \
  "v_smfmac_f32_16x16x64_bf16 v[36:39], v[144:147], v[200:207], v240\n\t" \
  "ds_read_u16 v171, v251 offset:10768\n\t" \
  "ds_read_u16 v184, v251 offset:8296\n\t" \
  "ds_read_u16 v185, v251 offset:8712\n\t" \
  "ds_read_u16 v186, v251 offset:10360\n\t" \
  "ds_read_u16 v187, v251 offset:10776\n\t" \
  "s_barrier\n\t" \
  "s_waitcnt lgkmcnt(14)\n\t" \
  "v_smfmac_f32_16x16x64_bf16 v[16:19], v[164:167], v[208:215], v240\n\t" \
  "ds_read_b64 v[192:193], v241 offset:0\n\t" \
  "ds_read_b64 v[194:195], v241 offset:400\n\t" \
  "v_smfmac_f32_16x16x64_bf16 v[40:43], v[160:163], v[208:215], v240\n\t" \
  "ds_read_b64 v[196:197], v241 offset:800\n\t" \
  "ds_read_b64 v[198:199], v241 offset:1200\n\t" \
  "s_waitcnt lgkmcnt(10)\n\t" \
  "v_smfmac_f32_16x16x64_bf16 v[20:23], v[180:183], v[216:223], v240\n\t" \
  "ds_read_b64 v[200:201], v241 offset:8\n\t" \
  "ds_read_b64 v[202:203], v241 offset:408\n\t" \
  "v_smfmac_f32_16x16x64_bf16 v[44:47], v[176:179], v[216:223], v240\n\t" \
  "ds_read_b64 v[204:205], v241 offset:808\n\t" \
  "ds_read_b64 v[206:207], v241 offset:1208\n\t" \
  "s_waitcnt lgkmcnt(4)\n\t" \
  "v_smfmac_f32_16x16x64_bf16 v[0:3], v[104:107], v[192:199], v240\n\t" \
  "ds_read_b64 v[208:209], v241 offset:16\n\t" \
  "ds_read_b64 v[210:211], v241 offset:416\n\t" \
  "ds_read_b64 v[212:213], v241 offset:816\n\t" \
  "ds_read_b64 v[214:215], v241 offset:1216\n\t" \
  "v_smfmac_f32_16x16x64_bf16 v[24:27], v[100:103], v[192:199], v240\n\t" \
  "ds_read_u16 v108, v251 offset:12384\n\t" \
  "ds_read_u16 v109, v251 offset:12800\n\t" \
  "ds_read_u16 v110, v251 offset:14448\n\t" \
  "v_smfmac_f32_16x16x64_bf16 v[48:51], v[96:99], v[192:199], v240\n\t" \
  "ds_read_u16 v111, v251 offset:14864\n\t" \
  "ds_read_u16 v124, v251 offset:12392\n\t" \
  "ds_read_u16 v125, v251 offset:12808\n\t" \
  "s_waitcnt lgkmcnt(10)\n\t" \
  "v_smfmac_f32_16x16x64_bf16 v[4:7], v[120:123], v[200:207], v240\n\t" \
  "ds_read_b64 v[216:217], v241 offset:24\n\t" \
  "ds_read_b64 v[218:219], v241 offset:424\n\t" \
  "ds_read_b64 v[220:221], v241 offset:824\n\t" \
  "ds_read_b64 v[222:223], v241 offset:1224\n\t" \
  "v_smfmac_f32_16x16x64_bf16 v[28:31], v[116:119], v[200:207], v240\n\t" \
  "ds_read_u16 v126, v251 offset:14456\n\t" \
  "ds_read_u16 v127, v251 offset:14872\n\t" \
  "ds_read_u16 v140, v251 offset:12400\n\t" \
  "v_smfmac_f32_16x16x64_bf16 v[52:55], v[112:115], v[200:207], v240\n\t" \
  "ds_read_u16 v141, v251 offset:12816\n\t" \
  "ds_read_u16 v142, v251 offset:14464\n\t" \
  "ds_read_u16 v143, v251 offset:14880\n\t" \
  "s_waitcnt lgkmcnt(14)\n\t" \
  "v_smfmac_f32_16x16x64_bf16 v[8:11], v[136:139], v[208:215], v240\n\t" \
  "ds_read_b64 v[192:193], v241 offset:32\n\t" \
  "ds_read_b64 v[194:195], v241 offset:432\n\t" \
  "ds_read_b64 v[196:197], v241 offset:832\n\t" \
  "ds_read_b64 v[198:199], v241 offset:1232\n\t" \
  "v_smfmac_f32_16x16x64_bf16 v[32:35], v[132:135], v[208:215], v240\n\t" \
  "ds_read_u16 v156, v251 offset:12408\n\t" \
  "ds_read_u16 v157, v251 offset:12824\n\t" \
  "ds_read_u16 v158, v251 offset:14472\n\t" \
  "v_smfmac_f32_16x16x64_bf16 v[56:59], v[128:131], v[208:215], v240\n\t" \
  "ds_read_u16 v159, v251 offset:14888\n\t" \
  "ds_read_u16 v172, v251 offset:12416\n\t" \
  "ds_read_u16 v173, v251 offset:12832\n\t" \
  "s_waitcnt lgkmcnt(14)\n\t" \
  "v_smfmac_f32_16x16x64_bf16 v[12:15], v[152:155], v[216:223], v240\n\t" \
  "ds_read_b64 v[200:201], v241 offset:40\n\t" \
  "ds_read_b64 v[202:203], v241 offset:440\n\t" \
  "ds_read_b64 v[204:205], v241 offset:840\n\t" \
  "ds_read_b64 v[206:207], v241 offset:1240\n\t" \
  "v_smfmac_f32_16x16x64_bf16 v[36:39], v[148:151], v[216:223], v240\n\t" \
  "s_add_u32 %[cs], %[cs], 1\n\t" \
  "s_cmp_eq_u32 %[cs], 6\n\t" \
  "s_cselect_b32 %[cs], 0, %[cs]\n\t" \
  "s_cselect_b32 %[t0], -75920, %[ps]\n\t" \
  "v_add_u32 v241, %[t0], v241\n\t" \
  "ds_read_u16 v174, v251 offset:14480\n\t" \
  "ds_read_u16 v175, v251 offset:14896\n\t" \
  "ds_read_u16 v188, v251 offset:12424\n\t" \
  "v_smfmac_f32_16x16x64_bf16 v[60:63], v[144:147], v[216:223], v240\n\t" \
  "ds_read_u16 v189, v251 offset:12840\n\t" \
  "ds_read_u16 v190, v251 offset:14488\n\t" \
  "ds_read_u16 v191, v251 offset:14904\n\t" \
  "s_waitcnt lgkmcnt(0)\n\t" \
  "s_barrier\n\t" \
  "v_smfmac_f32_16x16x64_bf16 v[16:19], v[168:171], v[192:199], v240\n\t" \
  "ds_read_b64 v[208:209], v241 offset:0\n\t" \
  "ds_read_b64 v[210:211], v241 offset:400\n\t" \
  "v_smfmac_f32_16x16x64_bf16 v[40:43], v[164:167], v[192:199], v240\n\t" \
  "ds_read_b64 v[212:213], v241 offset:800\n\t" \
  "v_smfmac_f32_16x16x64_bf16 v[64:67], v[160:163], v[192:199], v240\n\t" \
  "ds_read_b64 v[214:215], v241 offset:1200\n\t" \
  "v_smfmac_f32_16x16x64_bf16 v[20:23], v[184:187], v[200:207], v240\n\t" \
  "ds_read_b64 v[216:217], v241 offset:8\n\t" \
  "ds_read_b64 v[218:219], v241 offset:408\n\t" \
  "v_smfmac_f32_16x16x64_bf16 v[44:47], v[180:183], v[200:207], v240\n\t" \
  "ds_read_b64 v[220:221], v241 offset:808\n\t" \
  "v_smfmac_f32_16x16x64_bf16 v[68:71], v[176:179], v[200:207], v240\n\t" \
  "ds_read_b64 v[222:223], v241 offset:1208\n\t" \
  "s_waitcnt lgkmcnt(4)\n\t" \
  "v_smfmac_f32_16x16x64_bf16 v[0:3], v[108:111], v[208:215], v240\n\t" \
  "ds_read_b64 v[192:193], v241 offset:16\n\t" \
  "v_smfmac_f32_16x16x64_bf16 v[24:27], v[104:107], v[208:215], v240\n\t" \
  "ds_read_b64 v[194:195], v241 offset:416\n\t" \
  "v_smfmac_f32_16x16x64_bf16 v[48:51], v[100:103], v[208:215], v240\n\t" \
  "ds_read_b64 v[196:197], v241 offset:816\n\t" \
  "v_smfmac_f32_16x16x64_bf16 v[72:75], v[96:99], v[208:215], v240\n\t" \
  "ds_read_b64 v[198:199], v241 offset:1216\n\t" \
  "s_waitcnt lgkmcnt(4)\n\t" \
  "v_smfmac_f32_16x16x64_bf16 v[4:7], v[124:127], v[216:223], v240\n\t" \
  "ds_read_b64 v[200:201], v241 offset:24\n\t" \
  "v_mul_f32 v237, v0, v0\n\t" \
  "v_add_f32 v236, v0, v1\n\t" \
  "v_smfmac_f32_16x16x64_bf16 v[28:31], v[120:123], v[216:223], v240\n\t" \
  "ds_read_b64 v[202:203], v241 offset:424\n\t" \
  "v_fmac_f32 v237, v1, v1\n\t" \
  "v_add_f32 v236, v236, v2\n\t" \
  "v_smfmac_f32_16x16x64_bf16 v[52:55], v[116:119], v[216:223], v240\n\t" \
  "ds_read_b64 v[204:205], v241 offset:824\n\t" \
  "v_fmac_f32 v237, v2, v2\n\t" \
  "v_add_f32 v236, v236, v3\n\t" \
  "v_smfmac_f32_16x16x64_bf16 v[76:79], v[112:115], v[216:223], v240\n\t" \
  "ds_read_b64 v[206:207], v241 offset:1224\n\t" \
  "v_fmac_f32 v237, v3, v3\n\t" \
  "s_waitcnt lgkmcnt(4)\n\t" \
  "v_smfmac_f32_16x16x64_bf16 v[8:11], v[140:143], v[192:199], v240\n\t" \
  "ds_read_b64 v[208:209], v241 offset:32\n\t" \
  "v_add_f32 v236, v236, v4\n\t" \
  "v_fmac_f32 v237, v4, v4\n\t" \
  "v_smfmac_f32_16x16x64_bf16 v[32:35], v[136:139], v[192:199], v240\n\t" \
  "ds_read_b64 v[210:211], v241 offset:432\n\t" \
  "v_add_f32 v236, v236, v5\n\t" \
  "v_fmac_f32 v237, v5, v5\n\t" \
  "v_smfmac_f32_16x16x64_bf16 v[56:59], v[132:135], v[192:199], v240\n\t" \
  "ds_read_b64 v[212:213], v241 offset:832\n\t" \
  "v_add_f32 v236, v236, v6\n\t" \
  "v_fmac_f32 v237, v6, v6\n\t" \
  "v_smfmac_f32_16x16x64_bf16 v[80:83], v[128:131], v[192:199], v240\n\t" \
  "ds_read_b64 v[214:215], v241 offset:1232\n\t" \
  "v_add_f32 v236, v236, v7\n\t" \
  "v_fmac_f32 v237, v7, v7\n\t" \
  "s_waitcnt lgkmcnt(4)\n\t" \
  "v_smfmac_f32_16x16x64_bf16 v[12:15], v[156:159], v[200:207], v240\n\t" \
  "ds_read_b64 v[216:217], v241 offset:40\n\t" \
  "ds_read_b64 v[218:219], v241 offset:440\n\t" \
  "v_add_f32 v236, v236, v8\n\t" \
  "v_fmac_f32 v237, v8, v8\n\t" \
  "v_smfmac_f32_16x16x64_bf16 v[36:39], v[152:155], v[200:207], v240\n\t" \
  "ds_read_b64 v[220:221], v241 offset:840\n\t" \
  "v_add_f32 v236, v236, v9\n\t" \
  "v_fmac_f32 v237, v9, v9\n\t" \
  "v_smfmac_f32_16x16x64_bf16 v[60:63], v[148:151], v[200:207], v240\n\t" \
  "ds_read_b64 v[222:223], v241 offset:1240\n\t" \
  "v_add_f32 v236, v236, v10\n\t" \
  "v_fmac_f32 v237, v10, v10\n\t" \
  "v_smfmac_f32_16x16x64_bf16 v[84:87], v[144:147], v[200:207], v240\n\t" \
  "s_add_u32 %[cs], %[cs], 1\n\t" \
  "s_cmp_eq_u32 %[cs], 6\n\t" \
  "s_cselect_b32 %[cs], 0, %[cs]\n\t" \
  "s_cselect_b32 %[t0], -75920, %[ps]\n\t" \
  "v_add_u32 v241, %[t0], v241\n\t" \
  "v_add_f32 v236, v236, v11\n\t" \
  "v_fmac_f32 v237, v11, v11\n\t" \
  "s_waitcnt lgkmcnt(4)\n\t" \
  "v_smfmac_f32_16x16x64_bf16 v[16:19], v[172:175], v[208:215], v240\n\t" \
  "v_add_f32 v236, v236, v12\n\t" \
  "v_fmac_f32 v237, v12, v12\n\t" \
  "v_smfmac_f32_16x16x64_bf16 v[40:43], v[168:171], v[208:215], v240\n\t" \
  "v_add_f32 v236, v236, v13\n\t" \
  "v_fmac_f32 v237, v13, v13\n\t" \
  "v_smfmac_f32_16x16x64_bf16 v[64:67], v[164:167], v[208:215], v240\n\t" \
  "v_add_f32 v236, v236, v14\n\t" \
  "v_fmac_f32 v237, v14, v14\n\t" \
  "v_smfmac_f32_16x16x64_bf16 v[88:91], v[160:163], v[208:215], v240\n\t" \
  "v_add_f32 v236, v236, v15\n\t" \
  "v_fmac_f32 v237, v15, v15\n\t" \
  "s_waitcnt lgkmcnt(0)\n\t" \
  "v_smfmac_f32_16x16x64_bf16 v[20:23], v[188:191], v[216:223], v240\n\t" \
  "v_add_f32 v236, v236, v16\n\t" \
  "v_fmac_f32 v237, v16, v16\n\t" \
  "v_smfmac_f32_16x16x64_bf16 v[44:47], v[184:187], v[216:223], v240\n\t" \
  "v_add_f32 v236, v236, v17\n\t" \
  "v_fmac_f32 v237, v17, v17\n\t" \
  "v_smfmac_f32_16x16x64_bf16 v[68:71], v[180:183], v[216:223], v240\n\t" \
  "v_add_f32 v236, v236, v18\n\t" \
  "v_fmac_f32 v237, v18, v18\n\t" \
  "v_smfmac_f32_16x16x64_bf16 v[92:95], v[176:179], v[216:223], v240\n\t" \
  "v_add_f32 v236, v236, v19\n\t" \
  "v_fmac_f32 v237, v19, v19\n\t" \
  "s_nop 3\n\t" \
  "v_add_f32 v236, v236, v20\n\t" \
  "v_fmac_f32 v237, v20, v20\n\t" \
  "v_add_f32 v236, v236, v21\n\t" \
  "v_fmac_f32 v237, v21, v21\n\t" \
  "v_add_f32 v236, v236, v22\n\t" \
  "v_fmac_f32 v237, v22, v22\n\t" \
  "v_add_f32 v236, v236, v23\n\t" \
  "v_fmac_f32 v237, v23, v23\n\t" \
  "ds_write_b64 v243, v[236:237]\n\t" \
  "s_waitcnt lgkmcnt(0)\n\t" \
  "s_barrier\n\t" \
  "ds_read_b128 v[224:227], v244\n\t" \
  "ds_read_b128 v[228:231], v244 offset:1024\n\t" \
  "ds_read_b64 v[192:193], v241 offset:0\n\t" \
  "ds_read_b64 v[194:195], v241 offset:400\n\t" \
  "ds_read_b64 v[196:197], v241 offset:800\n\t" \
  "ds_read_b64 v[198:199], v241 offset:1200\n\t" \
  "ds_read_b64 v[200:201], v241 offset:8\n\t" \
  "ds_read_b64 v[202:203], v241 offset:408\n\t" \
  "ds_read_b64 v[204:205], v241 offset:808\n\t" \
  "ds_read_b64 v[206:207], v241 offset:1208\n\t" \
  "s_waitcnt lgkmcnt(8)\n\t" \
  "v_add_f32 v224, v224, v226\n\t" \
  "v_add_f32 v225, v225, v227\n\t" \
  "v_add_f32 v228, v228, v230\n\t" \
  "v_add_f32 v229, v229, v231\n\t" \
  "v_add_f32 v224, v224, v228\n\t" \
  "v_add_f32 v225, v225, v229\n\t" \
  "v_mul_f32 v224, 0x3c2aaaab, v224\n\t" \
  "v_mul_f32 v225, 0x3c2aaaab, v225\n\t" \
  "v_fma_f32 v225, -v224, v224, v225\n\t" \
  "v_max_f32 v225, 0, v225\n\t" \
  "v_add_f32 v225, 0x3727c5ac, v225\n\t" \
  "v_rsq_f32 v225, v225\n\t" \
  "s_nop 0\n\t" \
  "v_mul_f32 v238, %[gl], v225\n\t" \
  "v_mul_f32 v239, -v224, v238\n\t" \
  "v_fma_f32 v0, v0, v238, v239\n\t" \
  "v_fma_f32 v1, v1, v238, v239\n\t" \
  "v_fma_f32 v2, v2, v238, v239\n\t" \
  "v_fma_f32 v3, v3, v238, v239\n\t" \
  "v_cvt_pk_bf16_f32 v224, v0, v1\n\t" \
  "v_cvt_pk_bf16_f32 v225, v2, v3\n\t" \
  "v_fma_f32 v4, v4, v238, v239\n\t" \
  "v_fma_f32 v5, v5, v238, v239\n\t" \
  "v_fma_f32 v6, v6, v238, v239\n\t" \
  "v_fma_f32 v7, v7, v238, v239\n\t" \
  "v_cvt_pk_bf16_f32 v226, v4, v5\n\t" \
  "v_cvt_pk_bf16_f32 v227, v6, v7\n\t" \
  "ds_write_b128 v242, v[224:227] offset:0\n\t" \
  "v_fma_f32 v8, v8, v238, v239\n\t" \
  "v_fma_f32 v9, v9, v238, v239\n\t" \
  "v_fma_f32 v10, v10, v238, v239\n\t" \
  "v_fma_f32 v11, v11, v238, v239\n\t" \
  "v_cvt_pk_bf16_f32 v228, v8, v9\n\t" \
  "v_cvt_pk_bf16_f32 v229, v10, v11\n\t" \
  "v_fma_f32 v12, v12, v238, v239\n\t" \
  "v_fma_f32 v13, v13, v238, v239\n\t" \
  "v_fma_f32 v14, v14, v238, v239\n\t" \
  "v_fma_f32 v15, v15, v238, v239\n\t" \
  "v_cvt_pk_bf16_f32 v230, v12, v13\n\t" \
  "v_cvt_pk_bf16_f32 v231, v14, v15\n\t" \
  "ds_write_b128 v242, v[228:231] offset:1024\n\t" \
  "v_fma_f32 v16, v16, v238, v239\n\t" \
  "v_fma_f32 v17, v17, v238, v239\n\t" \
  "v_fma_f32 v18, v18, v238, v239\n\t" \
  "v_fma_f32 v19, v19, v238, v239\n\t" \
  "v_cvt_pk_bf16_f32 v232, v16, v17\n\t" \
  "v_cvt_pk_bf16_f32 v233, v18, v19\n\t" \
  "v_fma_f32 v20, v20, v238, v239\n\t" \
  "v_fma_f32 v21, v21, v238, v239\n\t" \
  "v_fma_f32 v22, v22, v238, v239\n\t" \
  "v_fma_f32 v23, v23, v238, v239\n\t" \
  "v_cvt_pk_bf16_f32 v234, v20, v21\n\t" \
  "v_cvt_pk_bf16_f32 v235, v22, v23\n\t" \
  "ds_write_b128 v242, v[232:235] offset:2048\n\t" \
  "v_mfma_f32_16x16x16_bf16 v[0:3], v[252:253], v[252:253], 0\n\t" \
  "v_mfma_f32_16x16x16_bf16 v[4:7], v[252:253], v[252:253], 0\n\t" \
  "v_mfma_f32_32x32x8_bf16 v[8:23], v[252:253], v[252:253], 0\n\t" \
  "s_waitcnt lgkmcnt(0)\n\t" \
  "s_barrier\n\t" \
  "v_smfmac_f32_16x16x64_bf16 v[24:27], v[108:111], v[192:199], v240\n\t" \
  "ds_read_b64 v[208:209], v241 offset:16\n\t" \
  "v_smfmac_f32_16x16x64_bf16 v[48:51], v[104:107], v[192:199], v240\n\t" \
  "ds_read_b64 v[210:211], v241 offset:416\n\t" \
  "v_smfmac_f32_16x16x64_bf16 v[72:75], v[100:103], v[192:199], v240\n\t" \
  "ds_read_b64 v[212:213], v241 offset:816\n\t" \
  "v_smfmac_f32_16x16x64_bf16 v[0:3], v[96:99], v[192:199], v240\n\t" \
  "ds_read_b64 v[214:215], v241 offset:1216\n\t" \
  "v_smfmac_f32_16x16x64_bf16 v[28:31], v[124:127], v[200:207], v240\n\t" \
  "ds_read_b64 v[216:217], v241 offset:24\n\t" \
  "v_mul_f32 v237, v24, v24\n\t" \
  "v_add_f32 v236, v24, v25\n\t" \
  "v_smfmac_f32_16x16x64_bf16 v[52:55], v[120:123], v[200:207], v240\n\t" \
  "ds_read_b64 v[218:219], v241 offset:424\n\t" \
  "v_fmac_f32 v237, v25, v25\n\t" \
  "v_add_f32 v236, v236, v26\n\t" \
  "v_smfmac_f32_16x16x64_bf16 v[76:79], v[116:119], v[200:207], v240\n\t" \
  "ds_read_b64 v[220:221], v241 offset:824\n\t" \
  "v_fmac_f32 v237, v26, v26\n\t" \
  "v_add_f32 v236, v236, v27\n\t" \
  "v_smfmac_f32_16x16x64_bf16 v[4:7], v[112:115], v[200:207], v240\n\t" \
  "ds_read_b64 v[222:223], v241 offset:1224\n\t" \
  "v_fmac_f32 v237, v27, v27\n\t" \
  "s_waitcnt lgkmcnt(4)\n\t" \
  "v_smfmac_f32_16x16x64_bf16 v[32:35], v[140:143], v[208:215], v240\n\t" \
  "ds_read_b64 v[192:193], v241 offset:32\n\t" \
  "v_add_f32 v236, v236, v28\n\t" \
  "v_fmac_f32 v237, v28, v28\n\t" \
  "v_smfmac_f32_16x16x64_bf16 v[56:59], v[136:139], v[208:215], v240\n\t" \
  "ds_read_b64 v[194:195], v241 offset:432\n\t" \
  "v_add_f32 v236, v236, v29\n\t" \
  "v_fmac_f32 v237, v29, v29\n\t" \
  "v_smfmac_f32_16x16x64_bf16 v[80:83], v[132:135], v[208:215], v240\n\t" \
  "ds_read_b64 v[196:197], v241 offset:832\n\t" \
  "v_add_f32 v236, v236, v30\n\t" \
  "v_fmac_f32 v237, v30, v30\n\t" \
  "v_smfmac_f32_16x16x64_bf16 v[8:11], v[128:131], v[208:215], v240\n\t" \
  "ds_read_b64 v[198:199], v241 offset:1232\n\t" \
  "v_add_f32 v236, v236, v31\n\t" \
  "v_fmac_f32 v237, v31, v31\n\t" \
  "s_waitcnt lgkmcnt(4)\n\t" \
  "v_smfmac_f32_16x16x64_bf16 v[36:39], v[156:159], v[216:223], v240\n\t" \
  "ds_read_b64 v[200:201], v241 offset:40\n\t" \
  "ds_read_b64 v[202:203], v241 offset:440\n\t" \
  "v_add_f32 v236, v236, v32\n\t" \
  "v_fmac_f32 v237, v32, v32\n\t" \
  "v_smfmac_f32_16x16x64_bf16 v[60:63], v[152:155], v[216:223], v240\n\t" \
  "ds_read_b64 v[204:205], v241 offset:840\n\t" \
  "v_add_f32 v236, v236, v33\n\t" \
  "v_fmac_f32 v237, v33, v33\n\t" \
  "v_smfmac_f32_16x16x64_bf16 v[84:87], v[148:151], v[216:223], v240\n\t" \
  "ds_read_b64 v[206:207], v241 offset:1240\n\t" \
  "v_add_f32 v236, v236, v34\n\t" \
  "v_fmac_f32 v237, v34, v34\n\t" \
  "v_smfmac_f32_16x16x64_bf16 v[12:15], v[144:147], v[216:223], v240\n\t" \
  "s_add_u32 %[cs], %[cs], 1\n\t" \
  "s_cmp_eq_u32 %[cs], 6\n\t" \
  "s_cselect_b32 %[cs], 0, %[cs]\n\t" \
  "s_cselect_b32 %[t0], -75920, %[ps]\n\t" \
  "v_add_u32 v241, %[t0], v241\n\t" \
  "v_add_f32 v236, v236, v35\n\t" \
  "v_fmac_f32 v237, v35, v35\n\t" \
  "s_waitcnt lgkmcnt(4)\n\t" \
  "v_smfmac_f32_16x16x64_bf16 v[40:43], v[172:175], v[192:199], v240\n\t" \
  "ds_read_b64 v[208:209], v241 offset:0\n\t" \
  "v_add_f32 v236, v236, v36\n\t" \
  "v_fmac_f32 v237, v36, v36\n\t" \
  "v_smfmac_f32_16x16x64_bf16 v[64:67], v[168:171], v[192:199], v240\n\t" \
  "ds_read_b64 v[210:211], v241 offset:400\n\t" \
  "v_add_f32 v236, v236, v37\n\t" \
  "v_fmac_f32 v237, v37, v37\n\t" \
  "v_smfmac_f32_16x16x64_bf16 v[88:91], v[164:167], v[192:199], v240\n\t" \
  "ds_read_b64 v[212:213], v241 offset:800\n\t" \
  "v_add_f32 v236, v236, v38\n\t" \
  "v_fmac_f32 v237, v38, v38\n\t" \
  "v_smfmac_f32_16x16x64_bf16 v[16:19], v[160:163], v[192:199], v240\n\t" \
  "ds_read_b64 v[214:215], v241 offset:1200\n\t" \
  "v_add_f32 v236, v236, v39\n\t" \
  "v_fmac_f32 v237, v39, v39\n\t" \
  "s_waitcnt lgkmcnt(4)\n\t" \
  "v_smfmac_f32_16x16x64_bf16 v[44:47], v[188:191], v[200:207], v240\n\t" \
  "ds_read_b64 v[216:217], v241 offset:8\n\t" \
  "v_add_f32 v236, v236, v40\n\t" \
  "v_fmac_f32 v237, v40, v40\n\t" \
  "v_smfmac_f32_16x16x64_bf16 v[68:71], v[184:187], v[200:207], v240\n\t" \
  "ds_read_b64 v[218:219], v241 offset:408\n\t" \
  "v_add_f32 v236, v236, v41\n\t" \
  "v_fmac_f32 v237, v41, v41\n\t" \
  "v_smfmac_f32_16x16x64_bf16 v[92:95], v[180:183], v[200:207], v240\n\t" \
  "ds_read_b64 v[220:221], v241 offset:808\n\t" \
  "v_add_f32 v236, v236, v42\n\t" \
  "v_fmac_f32 v237, v42, v42\n\t" \
  "v_smfmac_f32_16x16x64_bf16 v[20:23], v[176:179], v[200:207], v240\n\t" \
  "ds_read_b64 v[222:223], v241 offset:1208\n\t" \
  "v_add_f32 v236, v236, v43\n\t" \
  "v_fmac_f32 v237, v43, v43\n\t" \
  "s_nop 3\n\t" \
  "v_add_f32 v236, v236, v44\n\t" \
  "v_fmac_f32 v237, v44, v44\n\t" \
  "v_add_f32 v236, v236, v45\n\t" \
  "v_fmac_f32 v237, v45, v45\n\t" \
  "v_add_f32 v236, v236, v46\n\t" \
  "v_fmac_f32 v237, v46, v46\n\t" \
  "v_add_f32 v236, v236, v47\n\t" \
  "v_fmac_f32 v237, v47, v47\n\t" \
  "ds_write_b64 v243, v[236:237]\n\t" \
  "s_waitcnt lgkmcnt(0)\n\t" \
  "s_barrier\n\t" \
  ".Lcnx_f:\n\t" \
  "ds_read_b128 v[224:227], v244\n\t" \
  "ds_read_b128 v[228:231], v244 offset:1024\n\t" \
  "s_waitcnt lgkmcnt(0)\n\t" \
  "s_setprio 1\n\t" \
  "v_add_f32 v224, v224, v226\n\t" \
  "v_add_f32 v225, v225, v227\n\t" \
  "v_add_f32 v228, v228, v230\n\t" \
  "v_add_f32 v229, v229, v231\n\t" \
  "v_add_f32 v224, v224, v228\n\t" \
  "v_add_f32 v225, v225, v229\n\t" \
  "v_mul_f32 v224, 0x3c2aaaab, v224\n\t" \
  "v_mul_f32 v225, 0x3c2aaaab, v225\n\t" \
  "v_fma_f32 v225, -v224, v224, v225\n\t" \
  "v_max_f32 v225, 0, v225\n\t" \
  "v_add_f32 v225, 0x3727c5ac, v225\n\t" \
  "v_rsq_f32 v225, v225\n\t" \
  "s_nop 0\n\t" \
  "v_mul_f32 v238, %[gt], v225\n\t" \
  "v_mul_f32 v239, -v224, v238\n\t" \
  "v_fma_f32 v24, v24, v238, v239\n\t" \
  "v_fma_f32 v25, v25, v238, v239\n\t" \
  "v_fma_f32 v26, v26, v238, v239\n\t" \
  "v_fma_f32 v27, v27, v238, v239\n\t" \
  "v_cvt_pk_bf16_f32 v224, v24, v25\n\t" \
  "v_cvt_pk_bf16_f32 v225, v26, v27\n\t" \
  "v_fma_f32 v28, v28, v238, v239\n\t" \
  "v_fma_f32 v29, v29, v238, v239\n\t" \
  "v_fma_f32 v30, v30, v238, v239\n\t" \
  "v_fma_f32 v31, v31, v238, v239\n\t" \
  "v_cvt_pk_bf16_f32 v226, v28, v29\n\t" \
  "v_cvt_pk_bf16_f32 v227, v30, v31\n\t" \
  "s_setprio 3\n\t" \
  "v_mfma_f32_16x16x16_bf16 v[24:27], v[252:253], v[252:253], 0\n\t" \
  "ds_write_b128 v242, v[224:227] offset:0\n\t" \
  "v_fma_f32 v32, v32, v238, v239\n\t" \
  "v_mfma_f32_16x16x16_bf16 v[28:31], v[252:253], v[252:253], 0\n\t" \
  "ds_read_b64 v[192:193], v241 offset:16\n\t" \
  "v_fma_f32 v33, v33, v238, v239\n\t" \
  "v_smfmac_f32_16x16x64_bf16 v[48:51], v[108:111], v[208:215], v240\n\t" \
  "ds_read_b64 v[194:195], v241 offset:416\n\t" \
  "v_fma_f32 v34, v34, v238, v239\n\t" \
  "v_smfmac_f32_16x16x64_bf16 v[72:75], v[104:107], v[208:215], v240\n\t" \
  "ds_read_b64 v[196:197], v241 offset:816\n\t" \
  "v_fma_f32 v35, v35, v238, v239\n\t" \
  "v_smfmac_f32_16x16x64_bf16 v[0:3], v[100:103], v[208:215], v240\n\t" \
  "ds_read_b64 v[198:199], v241 offset:1216\n\t" \
  "v_cvt_pk_bf16_f32 v228, v32, v33\n\t" \
  "v_smfmac_f32_16x16x64_bf16 v[24:27], v[96:99], v[208:215], v240\n\t" \
  "ds_read_b64 v[200:201], v241 offset:24\n\t" \
  "v_cvt_pk_bf16_f32 v229, v34, v35\n\t" \
  "v_smfmac_f32_16x16x64_bf16 v[52:55], v[124:127], v[216:223], v240\n\t" \
  "ds_read_b64 v[202:203], v241 offset:424\n\t" \
  "v_fma_f32 v36, v36, v238, v239\n\t" \
  "v_smfmac_f32_16x16x64_bf16 v[76:79], v[120:123], v[216:223], v240\n\t" \
  "ds_read_b64 v[204:205], v241 offset:824\n\t" \
  "v_fma_f32 v37, v37, v238, v239\n\t" \
  "v_smfmac_f32_16x16x64_bf16 v[4:7], v[116:119], v[216:223], v240\n\t" \
  "ds_read_b64 v[206:207], v241 offset:1224\n\t" \
  "v_fma_f32 v38, v38, v238, v239\n\t" \
  "v_smfmac_f32_16x16x64_bf16 v[28:31], v[112:115], v[216:223], v240\n\t" \
  "v_fma_f32 v39, v39, v238, v239\n\t" \
  "s_setprio 1\n\t" \
  "s_setprio 3\n\t" \
  "s_mov_b32 m0, %[wld]\n\t" \
  "s_add_u32 %[t1], %[wso], %[wpd]\n\t" \
  "buffer_load_dwordx4 v245, %[wrs], %[wso] offen lds\n\t" \
  "buffer_load_dwordx4 v245, %[wrs], %[t1] offen offset:1024 lds\n\t" \
  "s_add_u32 %[t1], %[t1], %[wpd]\n\t" \
  "s_add_u32 %[wso], %[wso], %[wcs]\n\t" \
  "buffer_load_dwordx4 v245, %[wrs], %[t1] offen offset:2048 lds\n\t" \
  "s_add_u32 %[wld], %[wld], %[wst]\n\t" \
  "s_setprio 1\n\t" \
  "s_setprio 3\n\t" \
  "s_waitcnt vmcnt(3)\n\t" \
  "s_barrier\n\t" \
  "s_mov_b32 m0, %[xld]\n\t" \
  "s_cmp_lt_u32 %[gy], 0x3000000\n\t" \
  "s_cselect_b32 %[xso], %[gy], 0x40000000\n\t" \
  "buffer_load_dwordx4 v246, %[xrs], %[xso] offen offset:0 nt lds\n\t" \
  "s_add_u32 %[gy], %[gy], %[dgy]\n\t" \
  "buffer_load_dwordx4 v247, %[xrs], %[xso] offen offset:1024 nt lds\n\t" \
  "s_add_u32 %[xld], %[xld], 15184\n\t" \
  "s_cmp_eq_u32 %[xld], %[xle]\n\t" \
  "s_cselect_b32 %[xld], %[xlb], %[xld]\n\t" \
  "buffer_load_dwordx4 v248, %[xrs], %[xso] offen offset:2048 nt lds\n\t" \
  "s_mov_b64 exec, %[xm]\n\t" \
  "buffer_load_dwordx4 v249, %[xrs], %[xso] offen offset:3072 nt lds\n\t" \
  "s_mov_b64 exec, -1\n\t" \
  "s_setprio 1\n\t" \
  "v_cvt_pk_bf16_f32 v230, v36, v37\n\t" \
  "v_cvt_pk_bf16_f32 v231, v38, v39\n\t" \
  "v_fma_f32 v40, v40, v238, v239\n\t" \
  "v_fma_f32 v41, v41, v238, v239\n\t" \
  "v_fma_f32 v42, v42, v238, v239\n\t" \
  "v_fma_f32 v43, v43, v238, v239\n\t" \
  "v_cvt_pk_bf16_f32 v232, v40, v41\n\t" \
  "v_cvt_pk_bf16_f32 v233, v42, v43\n\t" \
  "v_fma_f32 v44, v44, v238, v239\n\t" \
  "v_fma_f32 v45, v45, v238, v239\n\t" \
  "v_fma_f32 v46, v46, v238, v239\n\t" \
  "v_fma_f32 v47, v47, v238, v239\n\t" \
  "v_cvt_pk_bf16_f32 v234, v44, v45\n\t" \
  "v_cvt_pk_bf16_f32 v235, v46, v47\n\t" \
  "v_mul_f32 v237, v48, v48\n\t" \
  "v_add_f32 v236, v48, v49\n\t" \
  "v_fmac_f32 v237, v49, v49\n\t" \
  "v_add_f32 v236, v236, v50\n\t" \
  "s_setprio 3\n\t" \
  "v_mfma_f32_32x32x8_bf16 v[32:47], v[252:253], v[252:253], 0\n\t" \
  "s_waitcnt lgkmcnt(4)\n\t" \
  "ds_write_b128 v242, v[228:231] offset:1024\n\t" \
  "ds_read_b64 v[208:209], v241 offset:32\n\t" \
  "v_fmac_f32 v237, v50, v50\n\t" \
  "v_smfmac_f32_16x16x64_bf16 v[56:59], v[140:143], v[192:199], v240\n\t" \
  "ds_read_b64 v[210:211], v241 offset:432\n\t" \
  "v_add_f32 v236, v236, v51\n\t" \
  "v_smfmac_f32_16x16x64_bf16 v[80:83], v[136:139], v[192:199], v240\n\t" \
  "ds_read_b64 v[212:213], v241 offset:832\n\t" \
  "v_fmac_f32 v237, v51, v51\n\t" \
  "v_smfmac_f32_16x16x64_bf16 v[8:11], v[132:135], v[192:199], v240\n\t" \
  "ds_read_b64 v[214:215], v241 offset:1232\n\t" \
  "v_add_f32 v236, v236, v52\n\t" \
  "v_smfmac_f32_16x16x64_bf16 v[32:35], v[128:131], v[192:199], v240\n\t" \
  "s_waitcnt lgkmcnt(5)\n\t" \
  "ds_write_b128 v242, v[232:235] offset:2048\n\t" \
  "v_fmac_f32 v237, v52, v52\n\t" \
  "v_smfmac_f32_16x16x64_bf16 v[60:63], v[156:159], v[200:207], v240\n\t" \
  "ds_read_b64 v[216:217], v241 offset:40\n\t" \
  "v_add_f32 v236, v236, v53\n\t" \
  "v_smfmac_f32_16x16x64_bf16 v[84:87], v[152:155], v[200:207], v240\n\t" \
  "ds_read_b64 v[218:219], v241 offset:440\n\t" \
  "v_fmac_f32 v237, v53, v53\n\t" \
  "v_smfmac_f32_16x16x64_bf16 v[12:15], v[148:151], v[200:207], v240\n\t" \
  "ds_read_b64 v[220:221], v241 offset:840\n\t" \
  "v_add_f32 v236, v236, v54\n\t" \
  "v_smfmac_f32_16x16x64_bf16 v[36:39], v[144:147], v[200:207], v240\n\t" \
  "ds_read_b64 v[222:223], v241 offset:1240\n\t" \
  "v_fmac_f32 v237, v54, v54\n\t" \
  "s_setprio 1\n\t" \
  "s_setprio 3\n\t" \
  "s_add_u32 %[cs], %[cs], 1\n\t" \
  "s_cmp_eq_u32 %[cs], 6\n\t" \
  "s_cselect_b32 %[cs], 0, %[cs]\n\t" \
  "s_cselect_b32 %[t0], -75920, %[ps]\n\t" \
  "v_add_u32 v241, %[t0], v241\n\t" \
  "s_mov_b32 m0, %[wld]\n\t" \
  "s_add_u32 %[t1], %[wso], %[wpd]\n\t" \
  "buffer_load_dwordx4 v245, %[wrs], %[wso] offen lds\n\t" \
  "buffer_load_dwordx4 v245, %[wrs], %[t1] offen offset:1024 lds\n\t" \
  "s_add_u32 %[t1], %[t1], %[wpd]\n\t" \
  "s_add_u32 %[wso], %[wso], %[wcs]\n\t" \
  "buffer_load_dwordx4 v245, %[wrs], %[t1] offen offset:2048 lds\n\t" \
  "s_add_u32 %[wld], %[wld], %[wlb]\n\t" \
  "s_waitcnt vmcnt(7)\n\t" \
  "s_waitcnt lgkmcnt(4)\n\t" \
  "s_barrier\n\t" \
  "v_smfmac_f32_16x16x64_bf16 v[64:67], v[172:175], v[208:215], v240\n\t" \
  "ds_read_b64 v[192:193], v241 offset:0\n\t" \
  "v_add_f32 v236, v236, v55\n\t" \
  "v_smfmac_f32_16x16x64_bf16 v[88:91], v[168:171], v[208:215], v240\n\t" \
  "ds_read_b64 v[194:195], v241 offset:400\n\t" \
  "v_fmac_f32 v237, v55, v55\n\t" \
  "v_smfmac_f32_16x16x64_bf16 v[16:19], v[164:167], v[208:215], v240\n\t" \
  "ds_read_b64 v[196:197], v241 offset:800\n\t" \
  "v_add_f32 v236, v236, v56\n\t" \
  "v_smfmac_f32_16x16x64_bf16 v[40:43], v[160:163], v[208:215], v240\n\t" \
  "s_waitcnt lgkmcnt(3)\n\t" \
  "ds_read_b64 v[198:199], v241 offset:1200\n\t" \
  "v_fmac_f32 v237, v56, v56\n\t" \
  "v_smfmac_f32_16x16x64_bf16 v[68:71], v[188:191], v[216:223], v240\n\t" \
  "ds_read_b64 v[200:201], v241 offset:8\n\t" \
  "v_add_f32 v236, v236, v57\n\t" \
  "v_smfmac_f32_16x16x64_bf16 v[92:95], v[184:187], v[216:223], v240\n\t" \
  "ds_read_b64 v[202:203], v241 offset:408\n\t" \
  "v_fmac_f32 v237, v57, v57\n\t" \
  "v_smfmac_f32_16x16x64_bf16 v[20:23], v[180:183], v[216:223], v240\n\t" \
  "ds_read_b64 v[204:205], v241 offset:808\n\t" \
  "v_add_f32 v236, v236, v58\n\t" \
  "v_smfmac_f32_16x16x64_bf16 v[44:47], v[176:179], v[216:223], v240\n\t" \
  "ds_read_b64 v[206:207], v241 offset:1208\n\t" \
  "v_fmac_f32 v237, v58, v58\n\t" \
  "s_setprio 1\n\t" \
  "s_setprio 3\n\t" \
  "s_mov_b32 m0, %[wld]\n\t" \
  "s_add_u32 %[t1], %[wso], %[wpd]\n\t" \
  "buffer_load_dwordx4 v245, %[wrs], %[wso] offen lds\n\t" \
  "buffer_load_dwordx4 v245, %[wrs], %[t1] offen offset:1024 lds\n\t" \
  "s_add_u32 %[t1], %[t1], %[wpd]\n\t" \
  "s_add_u32 %[wso], %[wso], %[wcs]\n\t" \
  "buffer_load_dwordx4 v245, %[wrs], %[t1] offen offset:2048 lds\n\t" \
  "s_add_u32 %[wld], %[wld], %[wla]\n\t" \
  "s_setprio 1\n\t" \
  "v_add_f32 v236, v236, v59\n\t" \
  "v_fmac_f32 v237, v59, v59\n\t" \
  "v_add_f32 v236, v236, v60\n\t" \
  "v_fmac_f32 v237, v60, v60\n\t" \
  "v_add_f32 v236, v236, v61\n\t" \
  "v_fmac_f32 v237, v61, v61\n\t" \
  "v_add_f32 v236, v236, v62\n\t" \
  "v_fmac_f32 v237, v62, v62\n\t" \
  "v_add_f32 v236, v236, v63\n\t" \
  "v_fmac_f32 v237, v63, v63\n\t" \
  "v_add_f32 v236, v236, v64\n\t" \
  "v_fmac_f32 v237, v64, v64\n\t" \
  "v_add_f32 v236, v236, v65\n\t" \
  "v_fmac_f32 v237, v65, v65\n\t" \
  "v_add_f32 v236, v236, v66\n\t" \
  "v_fmac_f32 v237, v66, v66\n\t" \
  "v_add_f32 v236, v236, v67\n\t" \
  "v_fmac_f32 v237, v67, v67\n\t" \
  "v_add_f32 v236, v236, v68\n\t" \
  "v_fmac_f32 v237, v68, v68\n\t" \
  "v_add_f32 v236, v236, v69\n\t" \
  "v_fmac_f32 v237, v69, v69\n\t" \
  "v_add_f32 v236, v236, v70\n\t" \
  "v_fmac_f32 v237, v70, v70\n\t" \
  "v_add_f32 v236, v236, v71\n\t" \
  "v_fmac_f32 v237, v71, v71\n\t" \
  "s_setprio 3\n\t" \
  "ds_write_b64 v243, v[236:237]\n\t" \
  "s_waitcnt vmcnt(3)\n\t" \
  "s_waitcnt lgkmcnt(0)\n\t" \
  "s_barrier\n\t" \
  "ds_read_b128 v[224:227], v244\n\t" \
  "ds_read_b128 v[228:231], v244 offset:1024\n\t" \
  "s_waitcnt lgkmcnt(0)\n\t" \
  "s_setprio 1\n\t" \
  "v_add_f32 v224, v224, v226\n\t" \
  "v_add_f32 v225, v225, v227\n\t" \
  "v_add_f32 v228, v228, v230\n\t" \
  "v_add_f32 v229, v229, v231\n\t" \
  "v_add_f32 v224, v224, v228\n\t" \
  "v_add_f32 v225, v225, v229\n\t" \
  "v_mul_f32 v224, 0x3c2aaaab, v224\n\t" \
  "v_mul_f32 v225, 0x3c2aaaab, v225\n\t" \
  "v_fma_f32 v225, -v224, v224, v225\n\t" \
  "v_max_f32 v225, 0, v225\n\t" \
  "v_add_f32 v225, 0x3727c5ac, v225\n\t" \
  "v_rsq_f32 v225, v225\n\t" \
  "s_nop 0\n\t" \
  "v_mul_f32 v238, %[gl], v225\n\t" \
  "v_mul_f32 v239, -v224, v238\n\t" \
  "v_fma_f32 v48, v48, v238, v239\n\t" \
  "v_fma_f32 v49, v49, v238, v239\n\t" \
  "v_fma_f32 v50, v50, v238, v239\n\t" \
  "v_fma_f32 v51, v51, v238, v239\n\t" \
  "v_cvt_pk_bf16_f32 v224, v48, v49\n\t" \
  "v_cvt_pk_bf16_f32 v225, v50, v51\n\t" \
  "v_fma_f32 v52, v52, v238, v239\n\t" \
  "v_fma_f32 v53, v53, v238, v239\n\t" \
  "v_fma_f32 v54, v54, v238, v239\n\t" \
  "v_fma_f32 v55, v55, v238, v239\n\t" \
  "v_cvt_pk_bf16_f32 v226, v52, v53\n\t" \
  "v_cvt_pk_bf16_f32 v227, v54, v55\n\t" \
  "s_setprio 3\n\t" \
  "v_mfma_f32_16x16x16_bf16 v[48:51], v[252:253], v[252:253], 0\n\t" \
  "ds_write_b128 v242, v[224:227] offset:0\n\t" \
  "v_fma_f32 v56, v56, v238, v239\n\t" \
  "v_mfma_f32_16x16x16_bf16 v[52:55], v[252:253], v[252:253], 0\n\t" \
  "ds_read_b64 v[208:209], v241 offset:16\n\t" \
  "v_fma_f32 v57, v57, v238, v239\n\t" \
  "v_smfmac_f32_16x16x64_bf16 v[72:75], v[108:111], v[192:199], v240\n\t" \
  "ds_read_b64 v[210:211], v241 offset:416\n\t" \
  "v_fma_f32 v58, v58, v238, v239\n\t" \
  "v_smfmac_f32_16x16x64_bf16 v[0:3], v[104:107], v[192:199], v240\n\t" \
  "ds_read_b64 v[212:213], v241 offset:816\n\t" \
  "v_fma_f32 v59, v59, v238, v239\n\t" \
  "v_smfmac_f32_16x16x64_bf16 v[24:27], v[100:103], v[192:199], v240\n\t" \
  "ds_read_b64 v[214:215], v241 offset:1216\n\t" \
  "v_cvt_pk_bf16_f32 v228, v56, v57\n\t" \
  "v_smfmac_f32_16x16x64_bf16 v[48:51], v[96:99], v[192:199], v240\n\t" \
  "ds_read_b64 v[216:217], v241 offset:24\n\t" \
  "v_cvt_pk_bf16_f32 v229, v58, v59\n\t" \
  "v_smfmac_f32_16x16x64_bf16 v[76:79], v[124:127], v[200:207], v240\n\t" \
  "ds_read_b64 v[218:219], v241 offset:424\n\t" \
  "v_fma_f32 v60, v60, v238, v239\n\t" \
  "v_smfmac_f32_16x16x64_bf16 v[4:7], v[120:123], v[200:207], v240\n\t" \
  "ds_read_b64 v[220:221], v241 offset:824\n\t" \
  "v_fma_f32 v61, v61, v238, v239\n\t" \
  "v_smfmac_f32_16x16x64_bf16 v[28:31], v[116:119], v[200:207], v240\n\t" \
  "ds_read_b64 v[222:223], v241 offset:1224\n\t" \
  "v_fma_f32 v62, v62, v238, v239\n\t" \
  "v_smfmac_f32_16x16x64_bf16 v[52:55], v[112:115], v[200:207], v240\n\t" \
  "v_fma_f32 v63, v63, v238, v239\n\t" \
  "s_setprio 1\n\t" \
  "s_setprio 3\n\t" \
  "s_mov_b32 m0, %[wld]\n\t" \
  "s_add_u32 %[t1], %[wso], %[wpd]\n\t" \
  "buffer_load_dwordx4 v245, %[wrs], %[wso] offen lds\n\t" \
  "buffer_load_dwordx4 v245, %[wrs], %[t1] offen offset:1024 lds\n\t" \
  "s_add_u32 %[t1], %[t1], %[wpd]\n\t" \
  "s_add_u32 %[wso], %[wso], %[wcs]\n\t" \
  "buffer_load_dwordx4 v245, %[wrs], %[t1] offen offset:2048 lds\n\t" \
  "s_add_u32 %[wld], %[wld], %[wst]\n\t" \
  "s_setprio 1\n\t" \
  "s_setprio 3\n\t" \
  "s_waitcnt vmcnt(3)\n\t" \
  "s_barrier\n\t" \
  "s_mov_b32 m0, %[xld]\n\t" \
  "s_cmp_lt_u32 %[gy], 0x3000000\n\t" \
  "s_cselect_b32 %[xso], %[gy], 0x40000000\n\t" \
  "buffer_load_dwordx4 v246, %[xrs], %[xso] offen offset:0 nt lds\n\t" \
  "s_add_u32 %[gy], %[gy], %[dgy]\n\t" \
  "buffer_load_dwordx4 v247, %[xrs], %[xso] offen offset:1024 nt lds\n\t" \
  "s_add_u32 %[xld], %[xld], 15184\n\t" \
  "s_cmp_eq_u32 %[xld], %[xle]\n\t" \
  "s_cselect_b32 %[xld], %[xlb], %[xld]\n\t" \
  "buffer_load_dwordx4 v248, %[xrs], %[xso] offen offset:2048 nt lds\n\t" \
  "s_mov_b64 exec, %[xm]\n\t" \
  "buffer_load_dwordx4 v249, %[xrs], %[xso] offen offset:3072 nt lds\n\t" \
  "s_mov_b64 exec, -1\n\t" \
  "s_setprio 1\n\t" \
  "v_cvt_pk_bf16_f32 v230, v60, v61\n\t" \
  "v_cvt_pk_bf16_f32 v231, v62, v63\n\t" \
  "v_fma_f32 v64, v64, v238, v239\n\t" \
  "v_fma_f32 v65, v65, v238, v239\n\t" \
  "v_fma_f32 v66, v66, v238, v239\n\t" \
  "v_fma_f32 v67, v67, v238, v239\n\t" \
  "v_cvt_pk_bf16_f32 v232, v64, v65\n\t" \
  "v_cvt_pk_bf16_f32 v233, v66, v67\n\t" \
  "v_fma_f32 v68, v68, v238, v239\n\t" \
  "v_fma_f32 v69, v69, v238, v239\n\t" \
  "v_fma_f32 v70, v70, v238, v239\n\t" \
  "v_fma_f32 v71, v71, v238, v239\n\t" \
  "v_cvt_pk_bf16_f32 v234, v68, v69\n\t" \
  "v_cvt_pk_bf16_f32 v235, v70, v71\n\t" \
  "v_mul_f32 v237, v72, v72\n\t" \
  "v_add_f32 v236, v72, v73\n\t" \
  "v_fmac_f32 v237, v73, v73\n\t" \
  "v_add_f32 v236, v236, v74\n\t" \
  "s_setprio 3\n\t" \
  "v_mfma_f32_32x32x8_bf16 v[56:71], v[252:253], v[252:253], 0\n\t" \
  "s_waitcnt lgkmcnt(4)\n\t" \
  "ds_write_b128 v242, v[228:231] offset:1024\n\t" \
  "ds_read_b64 v[192:193], v241 offset:32\n\t" \
  "v_fmac_f32 v237, v74, v74\n\t" \
  "v_smfmac_f32_16x16x64_bf16 v[80:83], v[140:143], v[208:215], v240\n\t" \
  "ds_read_b64 v[194:195], v241 offset:432\n\t" \
  "v_add_f32 v236, v236, v75\n\t" \
  "v_smfmac_f32_16x16x64_bf16 v[8:11], v[136:139], v[208:215], v240\n\t" \
  "ds_read_b64 v[196:197], v241 offset:832\n\t" \
  "v_fmac_f32 v237, v75, v75\n\t" \
  "v_smfmac_f32_16x16x64_bf16 v[32:35], v[132:135], v[208:215], v240\n\t" \
  "ds_read_b64 v[198:199], v241 offset:1232\n\t" \
  "v_add_f32 v236, v236, v76\n\t" \
  "v_smfmac_f32_16x16x64_bf16 v[56:59], v[128:131], v[208:215], v240\n\t" \
  "s_waitcnt lgkmcnt(5)\n\t" \
  "ds_write_b128 v242, v[232:235] offset:2048\n\t" \
  "v_fmac_f32 v237, v76, v76\n\t" \
  "v_smfmac_f32_16x16x64_bf16 v[84:87], v[156:159], v[216:223], v240\n\t" \
  "ds_read_b64 v[200:201], v241 offset:40\n\t" \
  "v_add_f32 v236, v236, v77\n\t" \
  "v_smfmac_f32_16x16x64_bf16 v[12:15], v[152:155], v[216:223], v240\n\t" \
  "ds_read_b64 v[202:203], v241 offset:440\n\t" \
  "v_fmac_f32 v237, v77, v77\n\t" \
  "v_smfmac_f32_16x16x64_bf16 v[36:39], v[148:151], v[216:223], v240\n\t" \
  "ds_read_b64 v[204:205], v241 offset:840\n\t" \
  "v_add_f32 v236, v236, v78\n\t" \
  "v_smfmac_f32_16x16x64_bf16 v[60:63], v[144:147], v[216:223], v240\n\t" \
  "ds_read_b64 v[206:207], v241 offset:1240\n\t" \
  "v_fmac_f32 v237, v78, v78\n\t" \
  "s_setprio 1\n\t" \
  "s_setprio 3\n\t" \
  "s_add_u32 %[cs], %[cs], 1\n\t" \
  "s_cmp_eq_u32 %[cs], 6\n\t" \
  "s_cselect_b32 %[cs], 0, %[cs]\n\t" \
  "s_cselect_b32 %[t0], -75920, %[ps]\n\t" \
  "v_add_u32 v241, %[t0], v241\n\t" \
  "s_mov_b32 m0, %[wld]\n\t" \
  "s_add_u32 %[t1], %[wso], %[wpd]\n\t" \
  "buffer_load_dwordx4 v245, %[wrs], %[wso] offen lds\n\t" \
  "buffer_load_dwordx4 v245, %[wrs], %[t1] offen offset:1024 lds\n\t" \
  "s_add_u32 %[t1], %[t1], %[wpd]\n\t" \
  "s_add_u32 %[wso], %[wso], %[wcs]\n\t" \
  "buffer_load_dwordx4 v245, %[wrs], %[t1] offen offset:2048 lds\n\t" \
  "s_add_u32 %[wld], %[wld], %[wst]\n\t" \
  "s_waitcnt vmcnt(7)\n\t" \
  "s_waitcnt lgkmcnt(4)\n\t" \
  "s_barrier\n\t" \
  "v_smfmac_f32_16x16x64_bf16 v[88:91], v[172:175], v[192:199], v240\n\t" \
  "ds_read_b64 v[208:209], v241 offset:0\n\t" \
  "v_add_f32 v236, v236, v79\n\t" \
  "v_smfmac_f32_16x16x64_bf16 v[16:19], v[168:171], v[192:199], v240\n\t" \
  "ds_read_b64 v[210:211], v241 offset:400\n\t" \
  "v_fmac_f32 v237, v79, v79\n\t" \
  "v_smfmac_f32_16x16x64_bf16 v[40:43], v[164:167], v[192:199], v240\n\t" \
  "ds_read_b64 v[212:213], v241 offset:800\n\t" \
  "v_add_f32 v236, v236, v80\n\t" \
  "v_smfmac_f32_16x16x64_bf16 v[64:67], v[160:163], v[192:199], v240\n\t" \
  "s_waitcnt lgkmcnt(3)\n\t" \
  "ds_read_b64 v[214:215], v241 offset:1200\n\t" \
  "v_fmac_f32 v237, v80, v80\n\t" \
  "v_smfmac_f32_16x16x64_bf16 v[92:95], v[188:191], v[200:207], v240\n\t" \
  "ds_read_b64 v[216:217], v241 offset:8\n\t" \
  "v_add_f32 v236, v236, v81\n\t" \
  "v_smfmac_f32_16x16x64_bf16 v[20:23], v[184:187], v[200:207], v240\n\t" \
  "ds_read_b64 v[218:219], v241 offset:408\n\t" \
  "v_fmac_f32 v237, v81, v81\n\t" \
  "v_smfmac_f32_16x16x64_bf16 v[44:47], v[180:183], v[200:207], v240\n\t" \
  "ds_read_b64 v[220:221], v241 offset:808\n\t" \
  "v_add_f32 v236, v236, v82\n\t" \
  "v_smfmac_f32_16x16x64_bf16 v[68:71], v[176:179], v[200:207], v240\n\t" \
  "ds_read_b64 v[222:223], v241 offset:1208\n\t" \
  "v_fmac_f32 v237, v82, v82\n\t" \
  "s_setprio 1\n\t" \
  "s_setprio 3\n\t" \
  "s_mov_b32 m0, %[wld]\n\t" \
  "s_add_u32 %[t1], %[wso], %[wpd]\n\t" \
  "buffer_load_dwordx4 v245, %[wrs], %[wso] offen lds\n\t" \
  "buffer_load_dwordx4 v245, %[wrs], %[t1] offen offset:1024 lds\n\t" \
  "s_add_u32 %[t1], %[t1], %[wpd]\n\t" \
  "s_add_u32 %[wso], %[wso], %[wcs]\n\t" \
  "buffer_load_dwordx4 v245, %[wrs], %[t1] offen offset:2048 lds\n\t" \
  "s_add_u32 %[wld], %[wld], %[wlc]\n\t" \
  "s_setprio 1\n\t" \
  "v_add_f32 v236, v236, v83\n\t" \
  "v_fmac_f32 v237, v83, v83\n\t" \
  "v_add_f32 v236, v236, v84\n\t" \
  "v_fmac_f32 v237, v84, v84\n\t" \
  "v_add_f32 v236, v236, v85\n\t" \
  "v_fmac_f32 v237, v85, v85\n\t" \
  "v_add_f32 v236, v236, v86\n\t" \
  "v_fmac_f32 v237, v86, v86\n\t" \
  "v_add_f32 v236, v236, v87\n\t" \
  "v_fmac_f32 v237, v87, v87\n\t" \
  "v_add_f32 v236, v236, v88\n\t" \
  "v_fmac_f32 v237, v88, v88\n\t" \
  "v_add_f32 v236, v236, v89\n\t" \
  "v_fmac_f32 v237, v89, v89\n\t" \
  "v_add_f32 v236, v236, v90\n\t" \
  "v_fmac_f32 v237, v90, v90\n\t" \
  "v_add_f32 v236, v236, v91\n\t" \
  "v_fmac_f32 v237, v91, v91\n\t" \
  "v_add_f32 v236, v236, v92\n\t" \
  "v_fmac_f32 v237, v92, v92\n\t" \
  "v_add_f32 v236, v236, v93\n\t" \
  "v_fmac_f32 v237, v93, v93\n\t" \
  "v_add_f32 v236, v236, v94\n\t" \
  "v_fmac_f32 v237, v94, v94\n\t" \
  "v_add_f32 v236, v236, v95\n\t" \
  "v_fmac_f32 v237, v95, v95\n\t" \
  "s_setprio 3\n\t" \
  "ds_write_b64 v243, v[236:237]\n\t" \
  "s_waitcnt vmcnt(3)\n\t" \
  "s_waitcnt lgkmcnt(0)\n\t" \
  "s_barrier\n\t" \
  "ds_read_b128 v[224:227], v244\n\t" \
  "ds_read_b128 v[228:231], v244 offset:1024\n\t" \
  "s_waitcnt lgkmcnt(0)\n\t" \
  "s_setprio 1\n\t" \
  "v_add_f32 v224, v224, v226\n\t" \
  "v_add_f32 v225, v225, v227\n\t" \
  "v_add_f32 v228, v228, v230\n\t" \
  "v_add_f32 v229, v229, v231\n\t" \
  "v_add_f32 v224, v224, v228\n\t" \
  "v_add_f32 v225, v225, v229\n\t" \
  "v_mul_f32 v224, 0x3c2aaaab, v224\n\t" \
  "v_mul_f32 v225, 0x3c2aaaab, v225\n\t" \
  "v_fma_f32 v225, -v224, v224, v225\n\t" \
  "v_max_f32 v225, 0, v225\n\t" \
  "v_add_f32 v225, 0x3727c5ac, v225\n\t" \
  "v_rsq_f32 v225, v225\n\t" \
  "s_nop 0\n\t" \
  "v_mul_f32 v238, %[gl], v225\n\t" \
  "v_mul_f32 v239, -v224, v238\n\t" \
  "v_fma_f32 v72, v72, v238, v239\n\t" \
  "v_fma_f32 v73, v73, v238, v239\n\t" \
  "v_fma_f32 v74, v74, v238, v239\n\t" \
  "v_fma_f32 v75, v75, v238, v239\n\t" \
  "v_cvt_pk_bf16_f32 v224, v72, v73\n\t" \
  "v_cvt_pk_bf16_f32 v225, v74, v75\n\t" \
  "v_fma_f32 v76, v76, v238, v239\n\t" \
  "v_fma_f32 v77, v77, v238, v239\n\t" \
  "v_fma_f32 v78, v78, v238, v239\n\t" \
  "v_fma_f32 v79, v79, v238, v239\n\t" \
  "v_cvt_pk_bf16_f32 v226, v76, v77\n\t" \
  "v_cvt_pk_bf16_f32 v227, v78, v79\n\t" \
  "s_setprio 3\n\t" \
  "v_mfma_f32_16x16x16_bf16 v[72:75], v[252:253], v[252:253], 0\n\t" \
  "ds_write_b128 v242, v[224:227] offset:0\n\t" \
  "v_fma_f32 v80, v80, v238, v239\n\t" \
  "v_mfma_f32_16x16x16_bf16 v[76:79], v[252:253], v[252:253], 0\n\t" \
  "ds_read_b64 v[192:193], v241 offset:16\n\t" \
  "v_fma_f32 v81, v81, v238, v239\n\t" \
  "v_smfmac_f32_16x16x64_bf16 v[0:3], v[108:111], v[208:215], v240\n\t" \
  "ds_read_b64 v[194:195], v241 offset:416\n\t" \
  "v_fma_f32 v82, v82, v238, v239\n\t" \
  "v_smfmac_f32_16x16x64_bf16 v[24:27], v[104:107], v[208:215], v240\n\t" \
  "ds_read_b64 v[196:197], v241 offset:816\n\t" \
  "v_fma_f32 v83, v83, v238, v239\n\t" \
  "v_smfmac_f32_16x16x64_bf16 v[48:51], v[100:103], v[208:215], v240\n\t" \
  "ds_read_b64 v[198:199], v241 offset:1216\n\t" \
  "v_cvt_pk_bf16_f32 v228, v80, v81\n\t" \
  "v_smfmac_f32_16x16x64_bf16 v[72:75], v[96:99], v[208:215], v240\n\t" \
  "ds_read_b64 v[200:201], v241 offset:24\n\t" \
  "v_cvt_pk_bf16_f32 v229, v82, v83\n\t" \
  "v_smfmac_f32_16x16x64_bf16 v[4:7], v[124:127], v[216:223], v240\n\t" \
  "ds_read_b64 v[202:203], v241 offset:424\n\t" \
  "v_fma_f32 v84, v84, v238, v239\n\t" \
  "v_smfmac_f32_16x16x64_bf16 v[28:31], v[120:123], v[216:223], v240\n\t" \
  "ds_read_b64 v[204:205], v241 offset:824\n\t" \
  "v_fma_f32 v85, v85, v238, v239\n\t" \
  "v_smfmac_f32_16x16x64_bf16 v[52:55], v[116:119], v[216:223], v240\n\t" \
  "ds_read_b64 v[206:207], v241 offset:1224\n\t" \
  "v_fma_f32 v86, v86, v238, v239\n\t" \
  "v_smfmac_f32_16x16x64_bf16 v[76:79], v[112:115], v[216:223], v240\n\t" \
  "v_fma_f32 v87, v87, v238, v239\n\t" \
  "s_setprio 1\n\t" \
  "s_setprio 3\n\t" \
  "s_mov_b32 m0, %[wld]\n\t" \
  "s_add_u32 %[t1], %[wso], %[wpd]\n\t" \
  "buffer_load_dwordx4 v245, %[wrs], %[wso] offen lds\n\t" \
  "buffer_load_dwordx4 v245, %[wrs], %[t1] offen offset:1024 lds\n\t" \
  "s_add_u32 %[t1], %[t1], %[wpd]\n\t" \
  "s_add_u32 %[wso], %[wso], %[wcs]\n\t" \
  "buffer_load_dwordx4 v245, %[wrs], %[t1] offen offset:2048 lds\n\t" \
  "s_add_u32 %[wld], %[wld], %[wst]\n\t" \
  "s_setprio 1\n\t" \
  "s_setprio 3\n\t" \
  "s_waitcnt vmcnt(3)\n\t" \
  "s_barrier\n\t" \
  "s_mov_b32 m0, %[xld]\n\t" \
  "s_cmp_lt_u32 %[gy], 0x3000000\n\t" \
  "s_cselect_b32 %[xso], %[gy], 0x40000000\n\t" \
  "buffer_load_dwordx4 v246, %[xrs], %[xso] offen offset:0 nt lds\n\t" \
  "s_add_u32 %[gy], %[gy], %[dgy]\n\t" \
  "buffer_load_dwordx4 v247, %[xrs], %[xso] offen offset:1024 nt lds\n\t" \
  "s_add_u32 %[xld], %[xld], 15184\n\t" \
  "s_cmp_eq_u32 %[xld], %[xle]\n\t" \
  "s_cselect_b32 %[xld], %[xlb], %[xld]\n\t" \
  "buffer_load_dwordx4 v248, %[xrs], %[xso] offen offset:2048 nt lds\n\t" \
  "s_mov_b64 exec, %[xm]\n\t" \
  "buffer_load_dwordx4 v249, %[xrs], %[xso] offen offset:3072 nt lds\n\t" \
  "s_mov_b64 exec, -1\n\t" \
  "s_setprio 1\n\t" \
  "v_cvt_pk_bf16_f32 v230, v84, v85\n\t" \
  "v_cvt_pk_bf16_f32 v231, v86, v87\n\t" \
  "v_fma_f32 v88, v88, v238, v239\n\t" \
  "v_fma_f32 v89, v89, v238, v239\n\t" \
  "v_fma_f32 v90, v90, v238, v239\n\t" \
  "v_fma_f32 v91, v91, v238, v239\n\t" \
  "v_cvt_pk_bf16_f32 v232, v88, v89\n\t" \
  "v_cvt_pk_bf16_f32 v233, v90, v91\n\t" \
  "v_fma_f32 v92, v92, v238, v239\n\t" \
  "v_fma_f32 v93, v93, v238, v239\n\t" \
  "v_fma_f32 v94, v94, v238, v239\n\t" \
  "v_fma_f32 v95, v95, v238, v239\n\t" \
  "v_cvt_pk_bf16_f32 v234, v92, v93\n\t" \
  "v_cvt_pk_bf16_f32 v235, v94, v95\n\t" \
  "v_mul_f32 v237, v0, v0\n\t" \
  "v_add_f32 v236, v0, v1\n\t" \
  "v_fmac_f32 v237, v1, v1\n\t" \
  "v_add_f32 v236, v236, v2\n\t" \
  "s_setprio 3\n\t" \
  "v_mfma_f32_32x32x8_bf16 v[80:95], v[252:253], v[252:253], 0\n\t" \
  "s_waitcnt lgkmcnt(4)\n\t" \
  "ds_write_b128 v242, v[228:231] offset:1024\n\t" \
  "ds_read_b64 v[208:209], v241 offset:32\n\t" \
  "v_fmac_f32 v237, v2, v2\n\t" \
  "v_smfmac_f32_16x16x64_bf16 v[8:11], v[140:143], v[192:199], v240\n\t" \
  "ds_read_b64 v[210:211], v241 offset:432\n\t" \
  "v_add_f32 v236, v236, v3\n\t" \
  "v_smfmac_f32_16x16x64_bf16 v[32:35], v[136:139], v[192:199], v240\n\t" \
  "ds_read_b64 v[212:213], v241 offset:832\n\t" \
  "v_fmac_f32 v237, v3, v3\n\t" \
  "v_smfmac_f32_16x16x64_bf16 v[56:59], v[132:135], v[192:199], v240\n\t" \
  "ds_read_b64 v[214:215], v241 offset:1232\n\t" \
  "v_add_f32 v236, v236, v4\n\t" \
  "v_smfmac_f32_16x16x64_bf16 v[80:83], v[128:131], v[192:199], v240\n\t" \
  "s_waitcnt lgkmcnt(5)\n\t" \
  "ds_write_b128 v242, v[232:235] offset:2048\n\t" \
  "v_fmac_f32 v237, v4, v4\n\t" \
  "v_smfmac_f32_16x16x64_bf16 v[12:15], v[156:159], v[200:207], v240\n\t" \
  "ds_read_b64 v[216:217], v241 offset:40\n\t" \
  "v_add_f32 v236, v236, v5\n\t" \
  "v_smfmac_f32_16x16x64_bf16 v[36:39], v[152:155], v[200:207], v240\n\t" \
  "ds_read_b64 v[218:219], v241 offset:440\n\t" \
  "v_fmac_f32 v237, v5, v5\n\t" \
  "v_smfmac_f32_16x16x64_bf16 v[60:63], v[148:151], v[200:207], v240\n\t" \
  "ds_read_b64 v[220:221], v241 offset:840\n\t" \
  "v_add_f32 v236, v236, v6\n\t" \
  "v_smfmac_f32_16x16x64_bf16 v[84:87], v[144:147], v[200:207], v240\n\t" \
  "ds_read_b64 v[222:223], v241 offset:1240\n\t" \
  "v_fmac_f32 v237, v6, v6\n\t" \
  "s_setprio 1\n\t" \
  "s_setprio 3\n\t" \
  "s_add_u32 %[cs], %[cs], 1\n\t" \
  "s_cmp_eq_u32 %[cs], 6\n\t" \
  "s_cselect_b32 %[cs], 0, %[cs]\n\t" \
  "s_cselect_b32 %[t0], -75920, %[ps]\n\t" \
  "v_add_u32 v241, %[t0], v241\n\t" \
  "s_mov_b32 m0, %[wld]\n\t" \
  "s_add_u32 %[t1], %[wso], %[wpd]\n\t" \
  "buffer_load_dwordx4 v245, %[wrs], %[wso] offen lds\n\t" \
  "buffer_load_dwordx4 v245, %[wrs], %[t1] offen offset:1024 lds\n\t" \
  "s_add_u32 %[t1], %[t1], %[wpd]\n\t" \
  "s_add_u32 %[wso], %[wso], %[wcs]\n\t" \
  "buffer_load_dwordx4 v245, %[wrs], %[t1] offen offset:2048 lds\n\t" \
  "s_add_u32 %[wld], %[wld], %[wst]\n\t" \
  "s_waitcnt vmcnt(7)\n\t" \
  "s_waitcnt lgkmcnt(4)\n\t" \
  "s_barrier\n\t" \
  "v_smfmac_f32_16x16x64_bf16 v[16:19], v[172:175], v[208:215], v240\n\t" \
  "ds_read_b64 v[192:193], v241 offset:0\n\t" \
  "v_add_f32 v236, v236, v7\n\t" \
  "v_smfmac_f32_16x16x64_bf16 v[40:43], v[168:171], v[208:215], v240\n\t" \
  "ds_read_b64 v[194:195], v241 offset:400\n\t" \
  "v_fmac_f32 v237, v7, v7\n\t" \
  "v_smfmac_f32_16x16x64_bf16 v[64:67], v[164:167], v[208:215], v240\n\t" \
  "ds_read_b64 v[196:197], v241 offset:800\n\t" \
  "v_add_f32 v236, v236, v8\n\t" \
  "v_smfmac_f32_16x16x64_bf16 v[88:91], v[160:163], v[208:215], v240\n\t" \
  "s_waitcnt lgkmcnt(3)\n\t" \
  "ds_read_b64 v[198:199], v241 offset:1200\n\t" \
  "v_fmac_f32 v237, v8, v8\n\t" \
  "v_smfmac_f32_16x16x64_bf16 v[20:23], v[188:191], v[216:223], v240\n\t" \
  "ds_read_b64 v[200:201], v241 offset:8\n\t" \
  "v_add_f32 v236, v236, v9\n\t" \
  "v_smfmac_f32_16x16x64_bf16 v[44:47], v[184:187], v[216:223], v240\n\t" \
  "ds_read_b64 v[202:203], v241 offset:408\n\t" \
  "v_fmac_f32 v237, v9, v9\n\t" \
  "v_smfmac_f32_16x16x64_bf16 v[68:71], v[180:183], v[216:223], v240\n\t" \
  "ds_read_b64 v[204:205], v241 offset:808\n\t" \
  "v_add_f32 v236, v236, v10\n\t" \
  "v_smfmac_f32_16x16x64_bf16 v[92:95], v[176:179], v[216:223], v240\n\t" \
  "ds_read_b64 v[206:207], v241 offset:1208\n\t" \
  "v_fmac_f32 v237, v10, v10\n\t" \
  "s_setprio 1\n\t" \
  "s_setprio 3\n\t" \
  "s_mov_b32 m0, %[wld]\n\t" \
  "s_add_u32 %[t1], %[wso], %[wpd]\n\t" \
  "buffer_load_dwordx4 v245, %[wrs], %[wso] offen lds\n\t" \
  "buffer_load_dwordx4 v245, %[wrs], %[t1] offen offset:1024 lds\n\t" \
  "s_add_u32 %[t1], %[t1], %[wpd]\n\t" \
  "s_add_u32 %[wso], %[wso], %[wsa]\n\t" \
  "buffer_load_dwordx4 v245, %[wrs], %[t1] offen offset:2048 lds\n\t" \
  "s_add_u32 %[wld], %[wld], %[wla]\n\t" \
  "s_setprio 1\n\t" \
  "v_add_f32 v236, v236, v11\n\t" \
  "v_fmac_f32 v237, v11, v11\n\t" \
  "v_add_f32 v236, v236, v12\n\t" \
  "v_fmac_f32 v237, v12, v12\n\t" \
  "v_add_f32 v236, v236, v13\n\t" \
  "v_fmac_f32 v237, v13, v13\n\t" \
  "v_add_f32 v236, v236, v14\n\t" \
  "v_fmac_f32 v237, v14, v14\n\t" \
  "v_add_f32 v236, v236, v15\n\t" \
  "v_fmac_f32 v237, v15, v15\n\t" \
  "v_add_f32 v236, v236, v16\n\t" \
  "v_fmac_f32 v237, v16, v16\n\t" \
  "v_add_f32 v236, v236, v17\n\t" \
  "v_fmac_f32 v237, v17, v17\n\t" \
  "v_add_f32 v236, v236, v18\n\t" \
  "v_fmac_f32 v237, v18, v18\n\t" \
  "v_add_f32 v236, v236, v19\n\t" \
  "v_fmac_f32 v237, v19, v19\n\t" \
  "v_add_f32 v236, v236, v20\n\t" \
  "v_fmac_f32 v237, v20, v20\n\t" \
  "v_add_f32 v236, v236, v21\n\t" \
  "v_fmac_f32 v237, v21, v21\n\t" \
  "v_add_f32 v236, v236, v22\n\t" \
  "v_fmac_f32 v237, v22, v22\n\t" \
  "v_add_f32 v236, v236, v23\n\t" \
  "v_fmac_f32 v237, v23, v23\n\t" \
  "s_setprio 3\n\t" \
  "ds_write_b64 v243, v[236:237]\n\t" \
  "s_waitcnt vmcnt(3)\n\t" \
  "s_waitcnt lgkmcnt(0)\n\t" \
  "s_barrier\n\t" \
  "ds_read_b128 v[224:227], v244\n\t" \
  "ds_read_b128 v[228:231], v244 offset:1024\n\t" \
  "s_waitcnt lgkmcnt(0)\n\t" \
  "s_setprio 1\n\t" \
  "v_add_f32 v224, v224, v226\n\t" \
  "v_add_f32 v225, v225, v227\n\t" \
  "v_add_f32 v228, v228, v230\n\t" \
  "v_add_f32 v229, v229, v231\n\t" \
  "v_add_f32 v224, v224, v228\n\t" \
  "v_add_f32 v225, v225, v229\n\t" \
  "v_mul_f32 v224, 0x3c2aaaab, v224\n\t" \
  "v_mul_f32 v225, 0x3c2aaaab, v225\n\t" \
  "v_fma_f32 v225, -v224, v224, v225\n\t" \
  "v_max_f32 v225, 0, v225\n\t" \
  "v_add_f32 v225, 0x3727c5ac, v225\n\t" \
  "v_rsq_f32 v225, v225\n\t" \
  "s_nop 0\n\t" \
  "v_mul_f32 v238, %[gl], v225\n\t" \
  "v_mul_f32 v239, -v224, v238\n\t" \
  "v_fma_f32 v0, v0, v238, v239\n\t" \
  "v_fma_f32 v1, v1, v238, v239\n\t" \
  "v_fma_f32 v2, v2, v238, v239\n\t" \
  "v_fma_f32 v3, v3, v238, v239\n\t" \
  "v_cvt_pk_bf16_f32 v224, v0, v1\n\t" \
  "v_cvt_pk_bf16_f32 v225, v2, v3\n\t" \
  "v_fma_f32 v4, v4, v238, v239\n\t" \
  "v_fma_f32 v5, v5, v238, v239\n\t" \
  "v_fma_f32 v6, v6, v238, v239\n\t" \
  "v_fma_f32 v7, v7, v238, v239\n\t" \
  "v_cvt_pk_bf16_f32 v226, v4, v5\n\t" \
  "v_cvt_pk_bf16_f32 v227, v6, v7\n\t" \
  "s_setprio 3\n\t" \
  "v_mfma_f32_16x16x16_bf16 v[0:3], v[252:253], v[252:253], 0\n\t" \
  "ds_write_b128 v242, v[224:227] offset:0\n\t" \
  "v_fma_f32 v8, v8, v238, v239\n\t" \
  "v_mfma_f32_16x16x16_bf16 v[4:7], v[252:253], v[252:253], 0\n\t" \
  "ds_read_b64 v[208:209], v241 offset:16\n\t" \
  "v_fma_f32 v9, v9, v238, v239\n\t" \
  "v_smfmac_f32_16x16x64_bf16 v[24:27], v[108:111], v[192:199], v240\n\t" \
  "ds_read_b64 v[210:211], v241 offset:416\n\t" \
  "v_fma_f32 v10, v10, v238, v239\n\t" \
  "v_smfmac_f32_16x16x64_bf16 v[48:51], v[104:107], v[192:199], v240\n\t" \
  "ds_read_b64 v[212:213], v241 offset:816\n\t" \
  "v_fma_f32 v11, v11, v238, v239\n\t" \
  "v_smfmac_f32_16x16x64_bf16 v[72:75], v[100:103], v[192:199], v240\n\t" \
  "ds_read_b64 v[214:215], v241 offset:1216\n\t" \
  "v_cvt_pk_bf16_f32 v228, v8, v9\n\t" \
  "v_smfmac_f32_16x16x64_bf16 v[0:3], v[96:99], v[192:199], v240\n\t" \
  "ds_read_b64 v[216:217], v241 offset:24\n\t" \
  "v_cvt_pk_bf16_f32 v229, v10, v11\n\t" \
  "v_smfmac_f32_16x16x64_bf16 v[28:31], v[124:127], v[200:207], v240\n\t" \
  "ds_read_b64 v[218:219], v241 offset:424\n\t" \
  "v_fma_f32 v12, v12, v238, v239\n\t" \
  "v_smfmac_f32_16x16x64_bf16 v[52:55], v[120:123], v[200:207], v240\n\t" \
  "ds_read_b64 v[220:221], v241 offset:824\n\t" \
  "v_fma_f32 v13, v13, v238, v239\n\t" \
  "v_smfmac_f32_16x16x64_bf16 v[76:79], v[116:119], v[200:207], v240\n\t" \
  "ds_read_b64 v[222:223], v241 offset:1224\n\t" \
  "v_fma_f32 v14, v14, v238, v239\n\t" \
  "v_smfmac_f32_16x16x64_bf16 v[4:7], v[112:115], v[200:207], v240\n\t" \
  "v_fma_f32 v15, v15, v238, v239\n\t" \
  "s_setprio 1\n\t" \
  "s_setprio 3\n\t" \
  "s_mov_b32 m0, %[wld]\n\t" \
  "s_add_u32 %[t1], %[wso], %[wpd]\n\t" \
  "buffer_load_dwordx4 v245, %[wrs], %[wso] offen lds\n\t" \
  "buffer_load_dwordx4 v245, %[wrs], %[t1] offen offset:1024 lds\n\t" \
  "s_add_u32 %[t1], %[t1], %[wpd]\n\t" \
  "s_add_u32 %[wso], %[wso], %[wsb]\n\t" \
  "buffer_load_dwordx4 v245, %[wrs], %[t1] offen offset:2048 lds\n\t" \
  "s_add_u32 %[wld], %[wld], %[wlb]\n\t" \
  "s_setprio 1\n\t" \
  "s_setprio 3\n\t" \
  "s_waitcnt vmcnt(3)\n\t" \
  "s_barrier\n\t" \
  "s_mov_b32 m0, %[xld]\n\t" \
  "s_cmp_lt_u32 %[gy], 0x3000000\n\t" \
  "s_cselect_b32 %[xso], %[gy], 0x40000000\n\t" \
  "buffer_load_dwordx4 v246, %[xrs], %[xso] offen offset:0 nt lds\n\t" \
  "s_add_u32 %[gy], %[gy], %[dgy]\n\t" \
  "buffer_load_dwordx4 v247, %[xrs], %[xso] offen offset:1024 nt lds\n\t" \
  "s_add_u32 %[xld], %[xld], 15184\n\t" \
  "s_cmp_eq_u32 %[xld], %[xle]\n\t" \
  "s_cselect_b32 %[xld], %[xlb], %[xld]\n\t" \
  "buffer_load_dwordx4 v248, %[xrs], %[xso] offen offset:2048 nt lds\n\t" \
  "s_mov_b64 exec, %[xm]\n\t" \
  "buffer_load_dwordx4 v249, %[xrs], %[xso] offen offset:3072 nt lds\n\t" \
  "s_mov_b64 exec, -1\n\t" \
  "s_setprio 1\n\t" \
  "v_cvt_pk_bf16_f32 v230, v12, v13\n\t" \
  "v_cvt_pk_bf16_f32 v231, v14, v15\n\t" \
  "v_fma_f32 v16, v16, v238, v239\n\t" \
  "v_fma_f32 v17, v17, v238, v239\n\t" \
  "v_fma_f32 v18, v18, v238, v239\n\t" \
  "v_fma_f32 v19, v19, v238, v239\n\t" \
  "v_cvt_pk_bf16_f32 v232, v16, v17\n\t" \
  "v_cvt_pk_bf16_f32 v233, v18, v19\n\t" \
  "v_fma_f32 v20, v20, v238, v239\n\t" \
  "v_fma_f32 v21, v21, v238, v239\n\t" \
  "v_fma_f32 v22, v22, v238, v239\n\t" \
  "v_fma_f32 v23, v23, v238, v239\n\t" \
  "v_cvt_pk_bf16_f32 v234, v20, v21\n\t" \
  "v_cvt_pk_bf16_f32 v235, v22, v23\n\t" \
  "v_mul_f32 v237, v24, v24\n\t" \
  "v_add_f32 v236, v24, v25\n\t" \
  "v_fmac_f32 v237, v25, v25\n\t" \
  "v_add_f32 v236, v236, v26\n\t" \
  "s_setprio 3\n\t" \
  "v_mfma_f32_32x32x8_bf16 v[8:23], v[252:253], v[252:253], 0\n\t" \
  "s_waitcnt lgkmcnt(4)\n\t" \
  "ds_write_b128 v242, v[228:231] offset:1024\n\t" \
  "ds_read_b64 v[192:193], v241 offset:32\n\t" \
  "v_fmac_f32 v237, v26, v26\n\t" \
  "v_smfmac_f32_16x16x64_bf16 v[32:35], v[140:143], v[208:215], v240\n\t" \
  "ds_read_b64 v[194:195], v241 offset:432\n\t" \
  "v_add_f32 v236, v236, v27\n\t" \
  "v_smfmac_f32_16x16x64_bf16 v[56:59], v[136:139], v[208:215], v240\n\t" \
  "ds_read_b64 v[196:197], v241 offset:832\n\t" \
  "v_fmac_f32 v237, v27, v27\n\t" \
  "v_smfmac_f32_16x16x64_bf16 v[80:83], v[132:135], v[208:215], v240\n\t" \
  "ds_read_b64 v[198:199], v241 offset:1232\n\t" \
  "v_add_f32 v236, v236, v28\n\t" \
  "v_smfmac_f32_16x16x64_bf16 v[8:11], v[128:131], v[208:215], v240\n\t" \
  "s_waitcnt lgkmcnt(5)\n\t" \
  "ds_write_b128 v242, v[232:235] offset:2048\n\t" \
  "v_fmac_f32 v237, v28, v28\n\t" \
  "v_smfmac_f32_16x16x64_bf16 v[36:39], v[156:159], v[216:223], v240\n\t" \
  "ds_read_b64 v[200:201], v241 offset:40\n\t" \
  "v_add_f32 v236, v236, v29\n\t" \
  "v_smfmac_f32_16x16x64_bf16 v[60:63], v[152:155], v[216:223], v240\n\t" \
  "ds_read_b64 v[202:203], v241 offset:440\n\t" \
  "v_fmac_f32 v237, v29, v29\n\t" \
  "v_smfmac_f32_16x16x64_bf16 v[84:87], v[148:151], v[216:223], v240\n\t" \
  "ds_read_b64 v[204:205], v241 offset:840\n\t" \
  "v_add_f32 v236, v236, v30\n\t" \
  "v_smfmac_f32_16x16x64_bf16 v[12:15], v[144:147], v[216:223], v240\n\t" \
  "ds_read_b64 v[206:207], v241 offset:1240\n\t" \
  "v_fmac_f32 v237, v30, v30\n\t" \
  "s_setprio 1\n\t" \
  "s_setprio 3\n\t" \
  "s_add_u32 %[cs], %[cs], 1\n\t" \
  "s_cmp_eq_u32 %[cs], 6\n\t" \
  "s_cselect_b32 %[cs], 0, %[cs]\n\t" \
  "s_cselect_b32 %[t0], -75920, %[ps]\n\t" \
  "v_add_u32 v241, %[t0], v241\n\t" \
  "s_mov_b32 m0, %[wld]\n\t" \
  "s_add_u32 %[t1], %[wso], %[wpd]\n\t" \
  "buffer_load_dwordx4 v245, %[wrs], %[wso] offen lds\n\t" \
  "buffer_load_dwordx4 v245, %[wrs], %[t1] offen offset:1024 lds\n\t" \
  "s_add_u32 %[t1], %[t1], %[wpd]\n\t" \
  "s_add_u32 %[wso], %[wso], %[wcs]\n\t" \
  "buffer_load_dwordx4 v245, %[wrs], %[t1] offen offset:2048 lds\n\t" \
  "s_add_u32 %[wld], %[wld], %[wst]\n\t" \
  "s_waitcnt vmcnt(7)\n\t" \
  "s_waitcnt lgkmcnt(4)\n\t" \
  "s_barrier\n\t" \
  "v_smfmac_f32_16x16x64_bf16 v[40:43], v[172:175], v[192:199], v240\n\t" \
  "ds_read_b64 v[208:209], v241 offset:0\n\t" \
  "v_add_f32 v236, v236, v31\n\t" \
  "v_smfmac_f32_16x16x64_bf16 v[64:67], v[168:171], v[192:199], v240\n\t" \
  "ds_read_b64 v[210:211], v241 offset:400\n\t" \
  "v_fmac_f32 v237, v31, v31\n\t" \
  "v_smfmac_f32_16x16x64_bf16 v[88:91], v[164:167], v[192:199], v240\n\t" \
  "ds_read_b64 v[212:213], v241 offset:800\n\t" \
  "v_add_f32 v236, v236, v32\n\t" \
  "v_smfmac_f32_16x16x64_bf16 v[16:19], v[160:163], v[192:199], v240\n\t" \
  "s_waitcnt lgkmcnt(3)\n\t" \
  "ds_read_b64 v[214:215], v241 offset:1200\n\t" \
  "v_fmac_f32 v237, v32, v32\n\t" \
  "v_smfmac_f32_16x16x64_bf16 v[44:47], v[188:191], v[200:207], v240\n\t" \
  "ds_read_b64 v[216:217], v241 offset:8\n\t" \
  "v_add_f32 v236, v236, v33\n\t" \
  "v_smfmac_f32_16x16x64_bf16 v[68:71], v[184:187], v[200:207], v240\n\t" \
  "ds_read_b64 v[218:219], v241 offset:408\n\t" \
  "v_fmac_f32 v237, v33, v33\n\t" \
  "v_smfmac_f32_16x16x64_bf16 v[92:95], v[180:183], v[200:207], v240\n\t" \
  "ds_read_b64 v[220:221], v241 offset:808\n\t" \
  "v_add_f32 v236, v236, v34\n\t" \
  "v_smfmac_f32_16x16x64_bf16 v[20:23], v[176:179], v[200:207], v240\n\t" \
  "ds_read_b64 v[222:223], v241 offset:1208\n\t" \
  "v_fmac_f32 v237, v34, v34\n\t" \
  "s_setprio 1\n\t" \
  "s_setprio 3\n\t" \
  "s_mov_b32 m0, %[wld]\n\t" \
  "s_add_u32 %[t1], %[wso], %[wpd]\n\t" \
  "buffer_load_dwordx4 v245, %[wrs], %[wso] offen lds\n\t" \
  "buffer_load_dwordx4 v245, %[wrs], %[t1] offen offset:1024 lds\n\t" \
  "s_add_u32 %[t1], %[t1], %[wpd]\n\t" \
  "s_add_u32 %[wso], %[wso], %[wcs]\n\t" \
  "buffer_load_dwordx4 v245, %[wrs], %[t1] offen offset:2048 lds\n\t" \
  "s_add_u32 %[wld], %[wld], %[wla]\n\t" \
  "s_setprio 1\n\t" \
  "v_add_f32 v236, v236, v35\n\t" \
  "v_fmac_f32 v237, v35, v35\n\t" \
  "v_add_f32 v236, v236, v36\n\t" \
  "v_fmac_f32 v237, v36, v36\n\t" \
  "v_add_f32 v236, v236, v37\n\t" \
  "v_fmac_f32 v237, v37, v37\n\t" \
  "v_add_f32 v236, v236, v38\n\t" \
  "v_fmac_f32 v237, v38, v38\n\t" \
  "v_add_f32 v236, v236, v39\n\t" \
  "v_fmac_f32 v237, v39, v39\n\t" \
  "v_add_f32 v236, v236, v40\n\t" \
  "v_fmac_f32 v237, v40, v40\n\t" \
  "v_add_f32 v236, v236, v41\n\t" \
  "v_fmac_f32 v237, v41, v41\n\t" \
  "v_add_f32 v236, v236, v42\n\t" \
  "v_fmac_f32 v237, v42, v42\n\t" \
  "v_add_f32 v236, v236, v43\n\t" \
  "v_fmac_f32 v237, v43, v43\n\t" \
  "v_add_f32 v236, v236, v44\n\t" \
  "v_fmac_f32 v237, v44, v44\n\t" \
  "v_add_f32 v236, v236, v45\n\t" \
  "v_fmac_f32 v237, v45, v45\n\t" \
  "v_add_f32 v236, v236, v46\n\t" \
  "v_fmac_f32 v237, v46, v46\n\t" \
  "v_add_f32 v236, v236, v47\n\t" \
  "v_fmac_f32 v237, v47, v47\n\t" \
  "s_setprio 3\n\t" \
  "ds_write_b64 v243, v[236:237]\n\t" \
  "s_waitcnt vmcnt(3)\n\t" \
  "s_waitcnt lgkmcnt(0)\n\t" \
  "s_barrier\n\t" \
  "s_sub_u32 %[lc], %[lc], 1\n\t" \
  "s_cmp_lg_u32 %[lc], 0\n\t" \
  "s_cbranch_scc1 .Lcnx_f\n\t" \
  "ds_read_b128 v[224:227], v244\n\t" \
  "ds_read_b128 v[228:231], v244 offset:1024\n\t" \
  "s_waitcnt lgkmcnt(0)\n\t" \
  "s_setprio 1\n\t" \
  "v_add_f32 v224, v224, v226\n\t" \
  "v_add_f32 v225, v225, v227\n\t" \
  "v_add_f32 v228, v228, v230\n\t" \
  "v_add_f32 v229, v229, v231\n\t" \
  "v_add_f32 v224, v224, v228\n\t" \
  "v_add_f32 v225, v225, v229\n\t" \
  "v_mul_f32 v224, 0x3c2aaaab, v224\n\t" \
  "v_mul_f32 v225, 0x3c2aaaab, v225\n\t" \
  "v_fma_f32 v225, -v224, v224, v225\n\t" \
  "v_max_f32 v225, 0, v225\n\t" \
  "v_add_f32 v225, 0x3727c5ac, v225\n\t" \
  "v_rsq_f32 v225, v225\n\t" \
  "s_nop 0\n\t" \
  "v_mul_f32 v238, %[gt], v225\n\t" \
  "v_mul_f32 v239, -v224, v238\n\t" \
  "v_fma_f32 v24, v24, v238, v239\n\t" \
  "v_fma_f32 v25, v25, v238, v239\n\t" \
  "v_fma_f32 v26, v26, v238, v239\n\t" \
  "v_fma_f32 v27, v27, v238, v239\n\t" \
  "v_cvt_pk_bf16_f32 v224, v24, v25\n\t" \
  "v_cvt_pk_bf16_f32 v225, v26, v27\n\t" \
  "v_fma_f32 v28, v28, v238, v239\n\t" \
  "v_fma_f32 v29, v29, v238, v239\n\t" \
  "v_fma_f32 v30, v30, v238, v239\n\t" \
  "v_fma_f32 v31, v31, v238, v239\n\t" \
  "v_cvt_pk_bf16_f32 v226, v28, v29\n\t" \
  "v_cvt_pk_bf16_f32 v227, v30, v31\n\t" \
  "s_setprio 3\n\t" \
  "v_smfmac_f32_16x16x64_bf16 v[48:51], v[108:111], v[208:215], v240\n\t" \
  "ds_write_b128 v242, v[224:227] offset:0\n\t" \
  "ds_read_b64 v[192:193], v241 offset:16\n\t" \
  "ds_read_b64 v[194:195], v241 offset:416\n\t" \
  "v_fma_f32 v32, v32, v238, v239\n\t" \
  "v_fma_f32 v33, v33, v238, v239\n\t" \
  "v_smfmac_f32_16x16x64_bf16 v[72:75], v[104:107], v[208:215], v240\n\t" \
  "ds_read_b64 v[196:197], v241 offset:816\n\t" \
  "ds_read_b64 v[198:199], v241 offset:1216\n\t" \
  "v_fma_f32 v34, v34, v238, v239\n\t" \
  "v_fma_f32 v35, v35, v238, v239\n\t" \
  "v_smfmac_f32_16x16x64_bf16 v[52:55], v[124:127], v[216:223], v240\n\t" \
  "ds_read_b64 v[200:201], v241 offset:24\n\t" \
  "ds_read_b64 v[202:203], v241 offset:424\n\t" \
  "v_cvt_pk_bf16_f32 v228, v32, v33\n\t" \
  "v_cvt_pk_bf16_f32 v229, v34, v35\n\t" \
  "v_smfmac_f32_16x16x64_bf16 v[76:79], v[120:123], v[216:223], v240\n\t" \
  "ds_read_b64 v[204:205], v241 offset:824\n\t" \
  "ds_read_b64 v[206:207], v241 offset:1224\n\t" \
  "v_fma_f32 v36, v36, v238, v239\n\t" \
  "v_fma_f32 v37, v37, v238, v239\n\t" \
  "s_setprio 1\n\t" \
  "s_setprio 3\n\t" \
  "s_mov_b32 m0, %[wld]\n\t" \
  "s_add_u32 %[t1], %[wso], %[wpd]\n\t" \
  "buffer_load_dwordx4 v245, %[wrs], %[wso] offen lds\n\t" \
  "buffer_load_dwordx4 v245, %[wrs], %[t1] offen offset:1024 lds\n\t" \
  "s_add_u32 %[t1], %[t1], %[wpd]\n\t" \
  "s_add_u32 %[wso], %[wso], %[wcs]\n\t" \
  "buffer_load_dwordx4 v245, %[wrs], %[t1] offen offset:2048 lds\n\t" \
  "s_add_u32 %[wld], %[wld], %[wst]\n\t" \
  "s_setprio 1\n\t" \
  "s_setprio 3\n\t" \
  "s_waitcnt vmcnt(3)\n\t" \
  "s_barrier\n\t" \
  "s_mov_b32 m0, %[wld]\n\t" \
  "s_add_u32 %[t1], %[wso], %[wpd]\n\t" \
  "buffer_load_dwordx4 v245, %[wrs], %[wso] offen lds\n\t" \
  "buffer_load_dwordx4 v245, %[wrs], %[t1] offen offset:1024 lds\n\t" \
  "s_add_u32 %[t1], %[t1], %[wpd]\n\t" \
  "s_add_u32 %[wso], %[wso], %[wcs]\n\t" \
  "buffer_load_dwordx4 v245, %[wrs], %[t1] offen offset:2048 lds\n\t" \
  "s_add_u32 %[wld], %[wld], %[wlb]\n\t" \
  "s_setprio 1\n\t" \
  "v_fma_f32 v38, v38, v238, v239\n\t" \
  "v_fma_f32 v39, v39, v238, v239\n\t" \
  "v_cvt_pk_bf16_f32 v230, v36, v37\n\t" \
  "v_cvt_pk_bf16_f32 v231, v38, v39\n\t" \
  "v_fma_f32 v40, v40, v238, v239\n\t" \
  "v_fma_f32 v41, v41, v238, v239\n\t" \
  "v_fma_f32 v42, v42, v238, v239\n\t" \
  "v_fma_f32 v43, v43, v238, v239\n\t" \
  "v_cvt_pk_bf16_f32 v232, v40, v41\n\t" \
  "v_cvt_pk_bf16_f32 v233, v42, v43\n\t" \
  "v_fma_f32 v44, v44, v238, v239\n\t" \
  "v_fma_f32 v45, v45, v238, v239\n\t" \
  "v_fma_f32 v46, v46, v238, v239\n\t" \
  "v_fma_f32 v47, v47, v238, v239\n\t" \
  "v_cvt_pk_bf16_f32 v234, v44, v45\n\t" \
  "v_cvt_pk_bf16_f32 v235, v46, v47\n\t" \
  "v_mul_f32 v237, v48, v48\n\t" \
  "v_add_f32 v236, v48, v49\n\t" \
  "v_fmac_f32 v237, v49, v49\n\t" \
  "v_add_f32 v236, v236, v50\n\t" \
  "s_waitcnt lgkmcnt(4)\n\t" \
  "s_setprio 3\n\t" \
  "v_smfmac_f32_16x16x64_bf16 v[56:59], v[140:143], v[192:199], v240\n\t" \
  "ds_write_b128 v242, v[228:231] offset:1024\n\t" \
  "ds_read_b64 v[208:209], v241 offset:32\n\t" \
  "ds_read_b64 v[210:211], v241 offset:432\n\t" \
  "v_fmac_f32 v237, v50, v50\n\t" \
  "v_add_f32 v236, v236, v51\n\t" \
  "v_smfmac_f32_16x16x64_bf16 v[80:83], v[136:139], v[192:199], v240\n\t" \
  "s_waitcnt lgkmcnt(3)\n\t" \
  "ds_read_b64 v[212:213], v241 offset:832\n\t" \
  "ds_read_b64 v[214:215], v241 offset:1232\n\t" \
  "ds_write_b128 v242, v[232:235] offset:2048\n\t" \
  "v_fmac_f32 v237, v51, v51\n\t" \
  "v_add_f32 v236, v236, v52\n\t" \
  "v_smfmac_f32_16x16x64_bf16 v[60:63], v[156:159], v[200:207], v240\n\t" \
  "ds_read_b64 v[216:217], v241 offset:40\n\t" \
  "ds_read_b64 v[218:219], v241 offset:440\n\t" \
  "v_fmac_f32 v237, v52, v52\n\t" \
  "v_add_f32 v236, v236, v53\n\t" \
  "v_smfmac_f32_16x16x64_bf16 v[84:87], v[152:155], v[200:207], v240\n\t" \
  "ds_read_b64 v[220:221], v241 offset:840\n\t" \
  "ds_read_b64 v[222:223], v241 offset:1240\n\t" \
  "v_fmac_f32 v237, v53, v53\n\t" \
  "v_add_f32 v236, v236, v54\n\t" \
  "s_setprio 1\n\t" \
  "s_setprio 3\n\t" \
  "s_add_u32 %[cs], %[cs], 1\n\t" \
  "s_cmp_eq_u32 %[cs], 6\n\t" \
  "s_cselect_b32 %[cs], 0, %[cs]\n\t" \
  "s_cselect_b32 %[t0], -75920, %[ps]\n\t" \
  "v_add_u32 v241, %[t0], v241\n\t" \
  "s_waitcnt vmcnt(3)\n\t" \
  "s_waitcnt lgkmcnt(4)\n\t" \
  "s_barrier\n\t" \
  "v_smfmac_f32_16x16x64_bf16 v[64:67], v[172:175], v[208:215], v240\n\t" \
  "ds_read_b64 v[192:193], v241 offset:0\n\t" \
  "ds_read_b64 v[194:195], v241 offset:400\n\t" \
  "v_fmac_f32 v237, v54, v54\n\t" \
  "v_add_f32 v236, v236, v55\n\t" \
  "v_smfmac_f32_16x16x64_bf16 v[88:91], v[168:171], v[208:215], v240\n\t" \
  "s_waitcnt lgkmcnt(2)\n\t" \
  "ds_read_b64 v[196:197], v241 offset:800\n\t" \
  "ds_read_b64 v[198:199], v241 offset:1200\n\t" \
  "v_fmac_f32 v237, v55, v55\n\t" \
  "v_add_f32 v236, v236, v56\n\t" \
  "v_smfmac_f32_16x16x64_bf16 v[68:71], v[188:191], v[216:223], v240\n\t" \
  "ds_read_b64 v[200:201], v241 offset:8\n\t" \
  "ds_read_b64 v[202:203], v241 offset:408\n\t" \
  "v_fmac_f32 v237, v56, v56\n\t" \
  "v_add_f32 v236, v236, v57\n\t" \
  "v_smfmac_f32_16x16x64_bf16 v[92:95], v[184:187], v[216:223], v240\n\t" \
  "ds_read_b64 v[204:205], v241 offset:808\n\t" \
  "ds_read_b64 v[206:207], v241 offset:1208\n\t" \
  "v_fmac_f32 v237, v57, v57\n\t" \
  "v_add_f32 v236, v236, v58\n\t" \
  "s_setprio 1\n\t" \
  "s_setprio 3\n\t" \
  "s_mov_b32 m0, %[wld]\n\t" \
  "s_add_u32 %[t1], %[wso], %[wpd]\n\t" \
  "buffer_load_dwordx4 v245, %[wrs], %[wso] offen lds\n\t" \
  "buffer_load_dwordx4 v245, %[wrs], %[t1] offen offset:1024 lds\n\t" \
  "s_add_u32 %[t1], %[t1], %[wpd]\n\t" \
  "s_add_u32 %[wso], %[wso], %[wcs]\n\t" \
  "buffer_load_dwordx4 v245, %[wrs], %[t1] offen offset:2048 lds\n\t" \
  "s_add_u32 %[wld], %[wld], %[wla]\n\t" \
  "s_setprio 1\n\t" \
  "v_fmac_f32 v237, v58, v58\n\t" \
  "v_add_f32 v236, v236, v59\n\t" \
  "v_fmac_f32 v237, v59, v59\n\t" \
  "v_add_f32 v236, v236, v60\n\t" \
  "v_fmac_f32 v237, v60, v60\n\t" \
  "v_add_f32 v236, v236, v61\n\t" \
  "v_fmac_f32 v237, v61, v61\n\t" \
  "v_add_f32 v236, v236, v62\n\t" \
  "v_fmac_f32 v237, v62, v62\n\t" \
  "v_add_f32 v236, v236, v63\n\t" \
  "v_fmac_f32 v237, v63, v63\n\t" \
  "v_add_f32 v236, v236, v64\n\t" \
  "v_fmac_f32 v237, v64, v64\n\t" \
  "v_add_f32 v236, v236, v65\n\t" \
  "v_fmac_f32 v237, v65, v65\n\t" \
  "v_add_f32 v236, v236, v66\n\t" \
  "v_fmac_f32 v237, v66, v66\n\t" \
  "v_add_f32 v236, v236, v67\n\t" \
  "v_fmac_f32 v237, v67, v67\n\t" \
  "v_add_f32 v236, v236, v68\n\t" \
  "v_fmac_f32 v237, v68, v68\n\t" \
  "v_add_f32 v236, v236, v69\n\t" \
  "v_fmac_f32 v237, v69, v69\n\t" \
  "v_add_f32 v236, v236, v70\n\t" \
  "v_fmac_f32 v237, v70, v70\n\t" \
  "v_add_f32 v236, v236, v71\n\t" \
  "v_fmac_f32 v237, v71, v71\n\t" \
  "s_setprio 3\n\t" \
  "ds_write_b64 v243, v[236:237]\n\t" \
  "s_waitcnt vmcnt(3)\n\t" \
  "s_waitcnt lgkmcnt(0)\n\t" \
  "s_barrier\n\t" \
  "ds_read_b128 v[224:227], v244\n\t" \
  "ds_read_b128 v[228:231], v244 offset:1024\n\t" \
  "s_waitcnt lgkmcnt(0)\n\t" \
  "s_setprio 1\n\t" \
  "v_add_f32 v224, v224, v226\n\t" \
  "v_add_f32 v225, v225, v227\n\t" \
  "v_add_f32 v228, v228, v230\n\t" \
  "v_add_f32 v229, v229, v231\n\t" \
  "v_add_f32 v224, v224, v228\n\t" \
  "v_add_f32 v225, v225, v229\n\t" \
  "v_mul_f32 v224, 0x3c2aaaab, v224\n\t" \
  "v_mul_f32 v225, 0x3c2aaaab, v225\n\t" \
  "v_fma_f32 v225, -v224, v224, v225\n\t" \
  "v_max_f32 v225, 0, v225\n\t" \
  "v_add_f32 v225, 0x3727c5ac, v225\n\t" \
  "v_rsq_f32 v225, v225\n\t" \
  "s_nop 0\n\t" \
  "v_mul_f32 v238, %[gl], v225\n\t" \
  "v_mul_f32 v239, -v224, v238\n\t" \
  "v_fma_f32 v48, v48, v238, v239\n\t" \
  "v_fma_f32 v49, v49, v238, v239\n\t" \
  "v_fma_f32 v50, v50, v238, v239\n\t" \
  "v_fma_f32 v51, v51, v238, v239\n\t" \
  "v_cvt_pk_bf16_f32 v224, v48, v49\n\t" \
  "v_cvt_pk_bf16_f32 v225, v50, v51\n\t" \
  "v_fma_f32 v52, v52, v238, v239\n\t" \
  "v_fma_f32 v53, v53, v238, v239\n\t" \
  "v_fma_f32 v54, v54, v238, v239\n\t" \
  "v_fma_f32 v55, v55, v238, v239\n\t" \
  "v_cvt_pk_bf16_f32 v226, v52, v53\n\t" \
  "v_cvt_pk_bf16_f32 v227, v54, v55\n\t" \
  "s_setprio 3\n\t" \
  "v_smfmac_f32_16x16x64_bf16 v[72:75], v[108:111], v[192:199], v240\n\t" \
  "ds_write_b128 v242, v[224:227] offset:0\n\t" \
  "ds_read_b64 v[208:209], v241 offset:16\n\t" \
  "ds_read_b64 v[210:211], v241 offset:416\n\t" \
  "ds_read_b64 v[212:213], v241 offset:816\n\t" \
  "ds_read_b64 v[214:215], v241 offset:1216\n\t" \
  "v_fma_f32 v56, v56, v238, v239\n\t" \
  "v_fma_f32 v57, v57, v238, v239\n\t" \
  "v_smfmac_f32_16x16x64_bf16 v[76:79], v[124:127], v[200:207], v240\n\t" \
  "ds_read_b64 v[216:217], v241 offset:24\n\t" \
  "ds_read_b64 v[218:219], v241 offset:424\n\t" \
  "ds_read_b64 v[220:221], v241 offset:824\n\t" \
  "ds_read_b64 v[222:223], v241 offset:1224\n\t" \
  "v_fma_f32 v58, v58, v238, v239\n\t" \
  "v_fma_f32 v59, v59, v238, v239\n\t" \
  "s_setprio 1\n\t" \
  "s_setprio 3\n\t" \
  "s_mov_b32 m0, %[wld]\n\t" \
  "s_add_u32 %[t1], %[wso], %[wpd]\n\t" \
  "buffer_load_dwordx4 v245, %[wrs], %[wso] offen lds\n\t" \
  "buffer_load_dwordx4 v245, %[wrs], %[t1] offen offset:1024 lds\n\t" \
  "s_add_u32 %[t1], %[t1], %[wpd]\n\t" \
  "s_add_u32 %[wso], %[wso], %[wcs]\n\t" \
  "buffer_load_dwordx4 v245, %[wrs], %[t1] offen offset:2048 lds\n\t" \
  "s_add_u32 %[wld], %[wld], %[wst]\n\t" \
  "s_setprio 1\n\t" \
  "s_setprio 3\n\t" \
  "s_waitcnt vmcnt(3)\n\t" \
  "s_barrier\n\t" \
  "s_mov_b32 m0, %[wld]\n\t" \
  "s_add_u32 %[t1], %[wso], %[wpd]\n\t" \
  "buffer_load_dwordx4 v245, %[wrs], %[wso] offen lds\n\t" \
  "buffer_load_dwordx4 v245, %[wrs], %[t1] offen offset:1024 lds\n\t" \
  "s_add_u32 %[t1], %[t1], %[wpd]\n\t" \
  "s_add_u32 %[wso], %[wso], %[wcs]\n\t" \
  "buffer_load_dwordx4 v245, %[wrs], %[t1] offen offset:2048 lds\n\t" \
  "s_add_u32 %[wld], %[wld], %[wst]\n\t" \
  "s_setprio 1\n\t" \
  "v_cvt_pk_bf16_f32 v228, v56, v57\n\t" \
  "v_cvt_pk_bf16_f32 v229, v58, v59\n\t" \
  "v_fma_f32 v60, v60, v238, v239\n\t" \
  "v_fma_f32 v61, v61, v238, v239\n\t" \
  "v_fma_f32 v62, v62, v238, v239\n\t" \
  "v_fma_f32 v63, v63, v238, v239\n\t" \
  "v_cvt_pk_bf16_f32 v230, v60, v61\n\t" \
  "v_cvt_pk_bf16_f32 v231, v62, v63\n\t" \
  "v_fma_f32 v64, v64, v238, v239\n\t" \
  "v_fma_f32 v65, v65, v238, v239\n\t" \
  "v_fma_f32 v66, v66, v238, v239\n\t" \
  "v_fma_f32 v67, v67, v238, v239\n\t" \
  "v_cvt_pk_bf16_f32 v232, v64, v65\n\t" \
  "v_cvt_pk_bf16_f32 v233, v66, v67\n\t" \
  "v_fma_f32 v68, v68, v238, v239\n\t" \
  "v_fma_f32 v69, v69, v238, v239\n\t" \
  "v_fma_f32 v70, v70, v238, v239\n\t" \
  "v_fma_f32 v71, v71, v238, v239\n\t" \
  "v_cvt_pk_bf16_f32 v234, v68, v69\n\t" \
  "v_cvt_pk_bf16_f32 v235, v70, v71\n\t" \
  "v_mul_f32 v237, v72, v72\n\t" \
  "v_add_f32 v236, v72, v73\n\t" \
  "v_fmac_f32 v237, v73, v73\n\t" \
  "v_add_f32 v236, v236, v74\n\t" \
  "s_waitcnt lgkmcnt(4)\n\t" \
  "s_setprio 3\n\t" \
  "v_smfmac_f32_16x16x64_bf16 v[80:83], v[140:143], v[208:215], v240\n\t" \
  "s_waitcnt lgkmcnt(0)\n\t" \
  "ds_write_b128 v242, v[228:231] offset:1024\n\t" \
  "ds_read_b64 v[192:193], v241 offset:32\n\t" \
  "ds_read_b64 v[194:195], v241 offset:432\n\t" \
  "ds_read_b64 v[196:197], v241 offset:832\n\t" \
  "ds_read_b64 v[198:199], v241 offset:1232\n\t" \
  "v_fmac_f32 v237, v74, v74\n\t" \
  "v_add_f32 v236, v236, v75\n\t" \
  "v_smfmac_f32_16x16x64_bf16 v[84:87], v[156:159], v[216:223], v240\n\t" \
  "ds_write_b128 v242, v[232:235] offset:2048\n\t" \
  "ds_read_b64 v[200:201], v241 offset:40\n\t" \
  "ds_read_b64 v[202:203], v241 offset:440\n\t" \
  "ds_read_b64 v[204:205], v241 offset:840\n\t" \
  "ds_read_b64 v[206:207], v241 offset:1240\n\t" \
  "v_fmac_f32 v237, v75, v75\n\t" \
  "v_add_f32 v236, v236, v76\n\t" \
  "s_setprio 1\n\t" \
  "s_setprio 3\n\t" \
  "s_add_u32 %[cs], %[cs], 1\n\t" \
  "s_cmp_eq_u32 %[cs], 6\n\t" \
  "s_cselect_b32 %[cs], 0, %[cs]\n\t" \
  "s_cselect_b32 %[t0], -75920, %[ps]\n\t" \
  "v_add_u32 v241, %[t0], v241\n\t" \
  "s_waitcnt vmcnt(3)\n\t" \
  "s_waitcnt lgkmcnt(4)\n\t" \
  "s_barrier\n\t" \
  "v_smfmac_f32_16x16x64_bf16 v[88:91], v[172:175], v[192:199], v240\n\t" \
  "s_waitcnt lgkmcnt(0)\n\t" \
  "v_fmac_f32 v237, v76, v76\n\t" \
  "v_add_f32 v236, v236, v77\n\t" \
  "v_smfmac_f32_16x16x64_bf16 v[92:95], v[188:191], v[200:207], v240\n\t" \
  "v_fmac_f32 v237, v77, v77\n\t" \
  "v_add_f32 v236, v236, v78\n\t" \
  "s_setprio 1\n\t" \
  "s_setprio 3\n\t" \
  "s_mov_b32 m0, %[wld]\n\t" \
  "s_add_u32 %[t1], %[wso], %[wpd]\n\t" \
  "buffer_load_dwordx4 v245, %[wrs], %[wso] offen lds\n\t" \
  "buffer_load_dwordx4 v245, %[wrs], %[t1] offen offset:1024 lds\n\t" \
  "s_add_u32 %[t1], %[t1], %[wpd]\n\t" \
  "s_add_u32 %[wso], %[wso], %[wcs]\n\t" \
  "buffer_load_dwordx4 v245, %[wrs], %[t1] offen offset:2048 lds\n\t" \
  "s_add_u32 %[wld], %[wld], %[wlc]\n\t" \
  "s_setprio 1\n\t" \
  "v_fmac_f32 v237, v78, v78\n\t" \
  "v_add_f32 v236, v236, v79\n\t" \
  "v_fmac_f32 v237, v79, v79\n\t" \
  "v_add_f32 v236, v236, v80\n\t" \
  "v_fmac_f32 v237, v80, v80\n\t" \
  "v_add_f32 v236, v236, v81\n\t" \
  "v_fmac_f32 v237, v81, v81\n\t" \
  "v_add_f32 v236, v236, v82\n\t" \
  "v_fmac_f32 v237, v82, v82\n\t" \
  "v_add_f32 v236, v236, v83\n\t" \
  "v_fmac_f32 v237, v83, v83\n\t" \
  "v_add_f32 v236, v236, v84\n\t" \
  "v_fmac_f32 v237, v84, v84\n\t" \
  "v_add_f32 v236, v236, v85\n\t" \
  "v_fmac_f32 v237, v85, v85\n\t" \
  "v_add_f32 v236, v236, v86\n\t" \
  "v_fmac_f32 v237, v86, v86\n\t" \
  "v_add_f32 v236, v236, v87\n\t" \
  "v_fmac_f32 v237, v87, v87\n\t" \
  "v_add_f32 v236, v236, v88\n\t" \
  "v_fmac_f32 v237, v88, v88\n\t" \
  "v_add_f32 v236, v236, v89\n\t" \
  "v_fmac_f32 v237, v89, v89\n\t" \
  "v_add_f32 v236, v236, v90\n\t" \
  "v_fmac_f32 v237, v90, v90\n\t" \
  "v_add_f32 v236, v236, v91\n\t" \
  "v_fmac_f32 v237, v91, v91\n\t" \
  "v_add_f32 v236, v236, v92\n\t" \
  "v_fmac_f32 v237, v92, v92\n\t" \
  "v_add_f32 v236, v236, v93\n\t" \
  "v_fmac_f32 v237, v93, v93\n\t" \
  "v_add_f32 v236, v236, v94\n\t" \
  "v_fmac_f32 v237, v94, v94\n\t" \
  "v_add_f32 v236, v236, v95\n\t" \
  "v_fmac_f32 v237, v95, v95\n\t" \
  "s_setprio 3\n\t" \
  "ds_write_b64 v243, v[236:237]\n\t" \
  "s_waitcnt vmcnt(3)\n\t" \
  "s_waitcnt lgkmcnt(0)\n\t" \
  "s_barrier\n\t" \
  "ds_read_b128 v[224:227], v244\n\t" \
  "ds_read_b128 v[228:231], v244 offset:1024\n\t" \
  "s_waitcnt lgkmcnt(0)\n\t" \
  "s_setprio 1\n\t" \
  "v_add_f32 v224, v224, v226\n\t" \
  "v_add_f32 v225, v225, v227\n\t" \
  "v_add_f32 v228, v228, v230\n\t" \
  "v_add_f32 v229, v229, v231\n\t" \
  "v_add_f32 v224, v224, v228\n\t" \
  "v_add_f32 v225, v225, v229\n\t" \
  "v_mul_f32 v224, 0x3c2aaaab, v224\n\t" \
  "v_mul_f32 v225, 0x3c2aaaab, v225\n\t" \
  "v_fma_f32 v225, -v224, v224, v225\n\t" \
  "v_max_f32 v225, 0, v225\n\t" \
  "v_add_f32 v225, 0x3727c5ac, v225\n\t" \
  "v_rsq_f32 v225, v225\n\t" \
  "s_nop 0\n\t" \
  "v_mul_f32 v238, %[gl], v225\n\t" \
  "v_mul_f32 v239, -v224, v238\n\t" \
  "v_fma_f32 v72, v72, v238, v239\n\t" \
  "v_fma_f32 v73, v73, v238, v239\n\t" \
  "v_fma_f32 v74, v74, v238, v239\n\t" \
  "v_fma_f32 v75, v75, v238, v239\n\t" \
  "v_cvt_pk_bf16_f32 v224, v72, v73\n\t" \
  "v_cvt_pk_bf16_f32 v225, v74, v75\n\t" \
  "v_fma_f32 v76, v76, v238, v239\n\t" \
  "v_fma_f32 v77, v77, v238, v239\n\t" \
  "v_fma_f32 v78, v78, v238, v239\n\t" \
  "v_fma_f32 v79, v79, v238, v239\n\t" \
  "v_cvt_pk_bf16_f32 v226, v76, v77\n\t" \
  "v_cvt_pk_bf16_f32 v227, v78, v79\n\t" \
  "ds_write_b128 v242, v[224:227] offset:0\n\t" \
  "s_setprio 3\n\t" \
  "s_mov_b32 m0, %[wld]\n\t" \
  "s_add_u32 %[t1], %[wso], %[wpd]\n\t" \
  "buffer_load_dwordx4 v245, %[wrs], %[wso] offen lds\n\t" \
  "buffer_load_dwordx4 v245, %[wrs], %[t1] offen offset:1024 lds\n\t" \
  "s_add_u32 %[t1], %[t1], %[wpd]\n\t" \
  "s_add_u32 %[wso], %[wso], %[wcs]\n\t" \
  "buffer_load_dwordx4 v245, %[wrs], %[t1] offen offset:2048 lds\n\t" \
  "s_add_u32 %[wld], %[wld], %[wst]\n\t" \
  "s_setprio 1\n\t" \
  "s_setprio 3\n\t" \
  "s_waitcnt vmcnt(3)\n\t" \
  "s_barrier\n\t" \
  "s_add_u32 m0, %[el], 0\n\t" \
  "s_add_u32 %[t1], %[es], %[wpd]\n\t" \
  "buffer_load_dwordx4 v245, %[wrs], %[es] offen lds\n\t" \
  "buffer_load_dwordx4 v245, %[wrs], %[t1] offen offset:1024 lds\n\t" \
  "s_add_u32 %[t1], %[t1], %[wpd]\n\t" \
  "s_add_u32 %[es], %[es], %[wcs]\n\t" \
  "buffer_load_dwordx4 v245, %[wrs], %[t1] offen offset:2048 lds\n\t" \
  "s_add_u32 m0, %[el], 12320\n\t" \
  "s_add_u32 %[t1], %[es], %[wpd]\n\t" \
  "buffer_load_dwordx4 v245, %[wrs], %[es] offen lds\n\t" \
  "buffer_load_dwordx4 v245, %[wrs], %[t1] offen offset:1024 lds\n\t" \
  "s_add_u32 %[t1], %[t1], %[wpd]\n\t" \
  "s_add_u32 %[es], %[es], %[wcs]\n\t" \
  "buffer_load_dwordx4 v245, %[wrs], %[t1] offen offset:2048 lds\n\t" \
  "s_setprio 1\n\t" \
  "v_fma_f32 v80, v80, v238, v239\n\t" \
  "v_fma_f32 v81, v81, v238, v239\n\t" \
  "v_fma_f32 v82, v82, v238, v239\n\t" \
  "v_fma_f32 v83, v83, v238, v239\n\t" \
  "v_cvt_pk_bf16_f32 v228, v80, v81\n\t" \
  "v_cvt_pk_bf16_f32 v229, v82, v83\n\t" \
  "v_fma_f32 v84, v84, v238, v239\n\t" \
  "v_fma_f32 v85, v85, v238, v239\n\t" \
  "v_fma_f32 v86, v86, v238, v239\n\t" \
  "v_fma_f32 v87, v87, v238, v239\n\t" \
  "v_cvt_pk_bf16_f32 v230, v84, v85\n\t" \
  "v_cvt_pk_bf16_f32 v231, v86, v87\n\t" \
  "v_fma_f32 v88, v88, v238, v239\n\t" \
  "v_fma_f32 v89, v89, v238, v239\n\t" \
  "v_fma_f32 v90, v90, v238, v239\n\t" \
  "v_fma_f32 v91, v91, v238, v239\n\t" \
  "v_cvt_pk_bf16_f32 v232, v88, v89\n\t" \
  "v_cvt_pk_bf16_f32 v233, v90, v91\n\t" \
  "v_fma_f32 v92, v92, v238, v239\n\t" \
  "v_fma_f32 v93, v93, v238, v239\n\t" \
  "v_fma_f32 v94, v94, v238, v239\n\t" \
  "v_fma_f32 v95, v95, v238, v239\n\t" \
  "v_cvt_pk_bf16_f32 v234, v92, v93\n\t" \
  "v_cvt_pk_bf16_f32 v235, v94, v95\n\t" \
  "ds_write_b128 v242, v[228:231] offset:1024\n\t" \
  "ds_write_b128 v242, v[232:235] offset:2048\n\t" \
  "s_setprio 3\n\t" \
  "s_add_u32 %[cs], %[cs], 1\n\t" \
  "s_cmp_eq_u32 %[cs], 6\n\t" \
  "s_cselect_b32 %[cs], 0, %[cs]\n\t" \
  "s_cselect_b32 %[t0], -75920, %[ps]\n\t" \
  "v_add_u32 v241, %[t0], v241\n\t" \
  "s_mov_b32 m0, %[wld]\n\t" \
  "s_add_u32 %[t1], %[wso], %[wpd]\n\t" \
  "buffer_load_dwordx4 v245, %[wrs], %[wso] offen lds\n\t" \
  "buffer_load_dwordx4 v245, %[wrs], %[t1] offen offset:1024 lds\n\t" \
  "s_add_u32 %[t1], %[t1], %[wpd]\n\t" \
  "s_add_u32 %[wso], %[wso], %[wcs]\n\t" \
  "buffer_load_dwordx4 v245, %[wrs], %[t1] offen offset:2048 lds\n\t" \
  "s_add_u32 %[wld], %[wld], %[wst]\n\t" \
  "s_waitcnt vmcnt(9)\n\t" \
  "s_waitcnt lgkmcnt(0)\n\t" \
  "s_barrier\n\t" \
  "s_mov_b32 m0, %[wld]\n\t" \
  "s_add_u32 %[t1], %[wso], %[wpd]\n\t" \
  "buffer_load_dwordx4 v245, %[wrs], %[wso] offen lds\n\t" \
  "buffer_load_dwordx4 v245, %[wrs], %[t1] offen offset:1024 lds\n\t" \
  "s_add_u32 %[t1], %[t1], %[wpd]\n\t" \
  "s_add_u32 %[wso], %[wso], %[wsa]\n\t" \
  "buffer_load_dwordx4 v245, %[wrs], %[t1] offen offset:2048 lds\n\t" \
  "s_add_u32 %[wld], %[wld], %[wla]\n\t" \
  "s_add_u32 m0, %[el], 24640\n\t" \
  "s_add_u32 %[t1], %[es], %[wpd]\n\t" \
  "buffer_load_dwordx4 v245, %[wrs], %[es] offen lds\n\t" \
  "buffer_load_dwordx4 v245, %[wrs], %[t1] offen offset:1024 lds\n\t" \
  "s_add_u32 %[t1], %[t1], %[wpd]\n\t" \
  "s_add_u32 %[es], %[es], %[wcs]\n\t" \
  "buffer_load_dwordx4 v245, %[wrs], %[t1] offen offset:2048 lds\n\t" \
  "s_add_u32 m0, %[el], 36960\n\t" \
  "s_add_u32 %[t1], %[es], %[wpd]\n\t" \
  "buffer_load_dwordx4 v245, %[wrs], %[es] offen lds\n\t" \
  "buffer_load_dwordx4 v245, %[wrs], %[t1] offen offset:1024 lds\n\t" \
  "s_add_u32 %[t1], %[t1], %[wpd]\n\t" \
  "s_add_u32 %[es], %[es], %[wcs]\n\t" \
  "buffer_load_dwordx4 v245, %[wrs], %[t1] offen offset:2048 lds\n\t" \
  "s_waitcnt vmcnt(9)\n\t" \
  "s_barrier\n\t" \
  "s_sleep 10\n\t" \
  "s_mov_b32 m0, %[wld]\n\t" \
  "s_add_u32 %[t1], %[wso], %[wpd]\n\t" \
  "buffer_load_dwordx4 v245, %[wrs], %[wso] offen lds\n\t" \
  "buffer_load_dwordx4 v245, %[wrs], %[t1] offen offset:1024 lds\n\t" \
  "s_add_u32 %[t1], %[t1], %[wpd]\n\t" \
  "s_add_u32 %[wso], %[wso], %[wsb]\n\t" \
  "buffer_load_dwordx4 v245, %[wrs], %[t1] offen offset:2048 lds\n\t" \
  "s_add_u32 %[wld], %[wld], %[wlb]\n\t" \
  "s_waitcnt vmcnt(9)\n\t" \
  "s_barrier\n\t" \
  "s_mov_b32 m0, %[wld]\n\t" \
  "s_add_u32 %[t1], %[wso], %[wpd]\n\t" \
  "buffer_load_dwordx4 v245, %[wrs], %[wso] offen lds\n\t" \
  "buffer_load_dwordx4 v245, %[wrs], %[t1] offen offset:1024 lds\n\t" \
  "s_add_u32 %[t1], %[t1], %[wpd]\n\t" \
  "s_add_u32 %[wso], %[wso], %[wcs]\n\t" \
  "buffer_load_dwordx4 v245, %[wrs], %[t1] offen offset:2048 lds\n\t" \
  "s_add_u32 %[wld], %[wld], %[wst]\n\t" \
  "s_add_u32 m0, %[el], 49280\n\t" \
  "s_add_u32 %[t1], %[es], %[wpd]\n\t" \
  "buffer_load_dwordx4 v245, %[wrs], %[es] offen lds\n\t" \
  "buffer_load_dwordx4 v245, %[wrs], %[t1] offen offset:1024 lds\n\t" \
  "s_add_u32 %[t1], %[t1], %[wpd]\n\t" \
  "s_add_u32 %[es], %[es], %[wcs]\n\t" \
  "buffer_load_dwordx4 v245, %[wrs], %[t1] offen offset:2048 lds\n\t" \
  "s_add_u32 m0, %[el], 61600\n\t" \
  "s_add_u32 %[t1], %[es], %[wpd]\n\t" \
  "buffer_load_dwordx4 v245, %[wrs], %[es] offen lds\n\t" \
  "buffer_load_dwordx4 v245, %[wrs], %[t1] offen offset:1024 lds\n\t" \
  "s_add_u32 %[t1], %[t1], %[wpd]\n\t" \
  "s_add_u32 %[es], %[es], %[wcs]\n\t" \
  "buffer_load_dwordx4 v245, %[wrs], %[t1] offen offset:2048 lds\n\t" \
  "s_waitcnt vmcnt(9)\n\t" \
  "s_barrier\n\t" \
  "s_mov_b32 m0, %[wld]\n\t" \
  "s_add_u32 %[t1], %[wso], %[wpd]\n\t" \
  "buffer_load_dwordx4 v245, %[wrs], %[wso] offen lds\n\t" \
  "buffer_load_dwordx4 v245, %[wrs], %[t1] offen offset:1024 lds\n\t" \
  "s_add_u32 %[t1], %[t1], %[wpd]\n\t" \
  "s_add_u32 %[wso], %[wso], %[wcs]\n\t" \
  "buffer_load_dwordx4 v245, %[wrs], %[t1] offen offset:2048 lds\n\t" \
  "s_add_u32 %[wld], %[wld], %[wla]\n\t" \
  "s_add_u32 m0, %[el], 73920\n\t" \
  "s_add_u32 %[t1], %[es], %[wpd]\n\t" \
  "buffer_load_dwordx4 v245, %[wrs], %[es] offen lds\n\t" \
  "buffer_load_dwordx4 v245, %[wrs], %[t1] offen offset:1024 lds\n\t" \
  "s_add_u32 %[t1], %[t1], %[wpd]\n\t" \
  "s_add_u32 %[es], %[es], %[wcs]\n\t" \
  "buffer_load_dwordx4 v245, %[wrs], %[t1] offen offset:2048 lds\n\t" \
  "s_waitcnt vmcnt(12)\n\t" \
  "s_barrier\n\t" \
  "s_sleep 10\n\t" \
  "s_waitcnt vmcnt(0)\n\t" \
  "s_barrier\n\t" \
  ""
#define MS_ASM0 \
  "s_mov_b32 m0, %[xld]\n\t" \
  "s_cmp_lt_u32 %[gy], 0x3000000\n\t" \
  "s_cselect_b32 %[xso], %[gy], 0x40000000\n\t" \
  "buffer_load_dwordx4 v246, %[xrs], %[xso] offen offset:0 nt lds\n\t" \
  "s_add_u32 %[gy], %[gy], %[dgy]\n\t" \
  "buffer_load_dwordx4 v247, %[xrs], %[xso] offen offset:1024 nt lds\n\t" \
  "s_add_u32 %[xld], %[xld], 15184\n\t" \
  "s_cmp_eq_u32 %[xld], %[xle]\n\t" \
  "s_cselect_b32 %[xld], %[xlb], %[xld]\n\t" \
  "buffer_load_dwordx4 v248, %[xrs], %[xso] offen offset:2048 nt lds\n\t" \
  "s_mov_b64 exec, %[xm]\n\t" \
  "buffer_load_dwordx4 v249, %[xrs], %[xso] offen offset:3072 nt lds\n\t" \
  "s_mov_b64 exec, -1\n\t" \
  "s_mov_b32 m0, %[xld]\n\t" \
  "s_cmp_lt_u32 %[gy], 0x3000000\n\t" \
  "s_cselect_b32 %[xso], %[gy], 0x40000000\n\t" \
  "buffer_load_dwordx4 v246, %[xrs], %[xso] offen offset:0 nt lds\n\t" \
  "s_add_u32 %[gy], %[gy], %[dgy]\n\t" \
  "buffer_load_dwordx4 v247, %[xrs], %[xso] offen offset:1024 nt lds\n\t" \
  "s_add_u32 %[xld], %[xld], 15184\n\t" \
  "s_cmp_eq_u32 %[xld], %[xle]\n\t" \
  "s_cselect_b32 %[xld], %[xlb], %[xld]\n\t" \
  "buffer_load_dwordx4 v248, %[xrs], %[xso] offen offset:2048 nt lds\n\t" \
  "s_mov_b64 exec, %[xm]\n\t" \
  "buffer_load_dwordx4 v249, %[xrs], %[xso] offen offset:3072 nt lds\n\t" \
  "s_mov_b64 exec, -1\n\t" \
  ""
#define MS_ASM1 \
  "v_mov_b32 v72, 0xbdd2d3e8\n\t" \
  "v_add_u32 v75, 0x10000, v94\n\t" \
  "v_mul_f32 v74, %[kl], v95\n\t" \
  "v_mul_f32 v75, %[kl], v75\n\t" \
  "v_mul_f32 v76, v74, v74\n\t" \
  "v_mul_f32 v77, v75, v75\n\t" \
  "v_fmaak_f32 v76, v76, v72, 0xc0135761\n\t" \
  "v_fmaak_f32 v77, v77, v72, 0xc0135761\n\t" \
  "v_mul_f32 v76, v74, v76\n\t" \
  "v_mul_f32 v77, v75, v77\n\t" \
  "v_exp_f32 v78, v76\n\t" \
  "v_exp_f32 v79, v77\n\t" \
  "v_add_f32 v76, 1.0, v78\n\t" \
  "v_add_f32 v77, 1.0, v79\n\t" \
  "v_rcp_f32 v76, v76\n\t" \
  "v_rcp_f32 v77, v77\n\t" \
  "v_mul_f32 v74, v74, v76\n\t" \
  "v_mul_f32 v75, v75, v77\n\t" \
  "v_mul_f32 v78, -v74, v78\n\t" \
  "v_mul_f32 v79, -v75, v79\n\t" \
  "v_cvt_pk_bf16_f32 v83, v78, v79\n\t" \
  "v_cvt_pk_bf16_f32 v82, v74, v75\n\t" \
  "v_add_u32 v94, 0x2000000, v94\n\t" \
  "ds_write_b32 v70, v82 offset:0\n\t" \
  "ds_write_b32 v71, v83 offset:0\n\t" \
  "s_barrier\n\t" \
  "s_mov_b32 m0, %[xld]\n\t" \
  "s_cmp_lt_u32 %[gy], 0x3000000\n\t" \
  "s_cselect_b32 %[xso], %[gy], 0x40000000\n\t" \
  "buffer_load_dwordx4 v246, %[xrs], %[xso] offen offset:0 nt lds\n\t" \
  "s_add_u32 %[gy], %[gy], %[dgy]\n\t" \
  "buffer_load_dwordx4 v247, %[xrs], %[xso] offen offset:1024 nt lds\n\t" \
  "s_add_u32 %[xld], %[xld], 15184\n\t" \
  "s_cmp_eq_u32 %[xld], %[xle]\n\t" \
  "s_cselect_b32 %[xld], %[xlb], %[xld]\n\t" \
  "buffer_load_dwordx4 v248, %[xrs], %[xso] offen offset:2048 nt lds\n\t" \
  "s_mov_b64 exec, %[xm]\n\t" \
  "buffer_load_dwordx4 v249, %[xrs], %[xso] offen offset:3072 nt lds\n\t" \
  "s_mov_b64 exec, -1\n\t" \
  "s_waitcnt vmcnt(8)\n\t" \
  "s_barrier\n\t" \
  "s_mov_b32 m0, %[xld]\n\t" \
  "s_cmp_lt_u32 %[gy], 0x3000000\n\t" \
  "s_cselect_b32 %[xso], %[gy], 0x40000000\n\t" \
  "buffer_load_dwordx4 v246, %[xrs], %[xso] offen offset:0 nt lds\n\t" \
  "s_add_u32 %[gy], %[gy], %[dgy]\n\t" \
  "buffer_load_dwordx4 v247, %[xrs], %[xso] offen offset:1024 nt lds\n\t" \
  "s_add_u32 %[xld], %[xld], 15184\n\t" \
  "s_cmp_eq_u32 %[xld], %[xle]\n\t" \
  "s_cselect_b32 %[xld], %[xlb], %[xld]\n\t" \
  "buffer_load_dwordx4 v248, %[xrs], %[xso] offen offset:2048 nt lds\n\t" \
  "s_mov_b64 exec, %[xm]\n\t" \
  "buffer_load_dwordx4 v249, %[xrs], %[xso] offen offset:3072 nt lds\n\t" \
  "s_mov_b64 exec, -1\n\t" \
  "s_waitcnt vmcnt(8)\n\t" \
  "s_barrier\n\t" \
  "s_mov_b32 m0, %[wld]\n\t" \
  "s_mov_b32 %[xso], %[wso]\n\t" \
  "s_add_u32 %[t0], %[xso], 0x2c00\n\t" \
  "s_add_u32 %[t1], %[t0], 0x2c00\n\t" \
  "buffer_load_dwordx4 v245, %[wrs], %[xso] offen lds\n\t" \
  "buffer_load_dwordx4 v245, %[wrs], %[t0] offen offset:1024 lds\n\t" \
  "buffer_load_dwordx4 v245, %[wrs], %[t1] offen offset:2048 lds\n\t" \
  "v_add_u32 v85, 0x10000, v94\n\t" \
  "v_mul_f32 v84, %[kl], v94\n\t" \
  "v_mul_f32 v85, %[kl], v85\n\t" \
  "v_mul_f32 v86, v84, v84\n\t" \
  "v_mul_f32 v87, v85, v85\n\t" \
  "v_fmaak_f32 v86, v86, v72, 0xc0135761\n\t" \
  "v_fmaak_f32 v87, v87, v72, 0xc0135761\n\t" \
  "v_mul_f32 v86, v84, v86\n\t" \
  "v_mul_f32 v87, v85, v87\n\t" \
  "v_exp_f32 v88, v86\n\t" \
  "v_exp_f32 v89, v87\n\t" \
  "v_add_f32 v86, 1.0, v88\n\t" \
  "v_add_f32 v87, 1.0, v89\n\t" \
  "v_rcp_f32 v86, v86\n\t" \
  "v_rcp_f32 v87, v87\n\t" \
  "v_mul_f32 v84, v84, v86\n\t" \
  "v_mul_f32 v85, v85, v87\n\t" \
  "v_mul_f32 v88, -v84, v88\n\t" \
  "v_mul_f32 v89, -v85, v89\n\t" \
  "v_cvt_pk_bf16_f32 v93, v88, v89\n\t" \
  "v_cvt_pk_bf16_f32 v92, v84, v85\n\t" \
  "v_add_u32 v94, 0x2000000, v94\n\t" \
  "ds_write_b32 v70, v92 offset:1024\n\t" \
  "ds_write_b32 v71, v93 offset:1024\n\t" \
  "s_waitcnt vmcnt(7)\n\t" \
  "s_barrier\n\t" \
  "s_mov_b32 m0, %[xld]\n\t" \
  "s_cmp_lt_u32 %[gy], 0x3000000\n\t" \
  "s_cselect_b32 %[xso], %[gy], 0x40000000\n\t" \
  "buffer_load_dwordx4 v246, %[xrs], %[xso] offen offset:0 nt lds\n\t" \
  "s_add_u32 %[gy], %[gy], %[dgy]\n\t" \
  "buffer_load_dwordx4 v247, %[xrs], %[xso] offen offset:1024 nt lds\n\t" \
  "s_add_u32 %[xld], %[xld], 15184\n\t" \
  "s_cmp_eq_u32 %[xld], %[xle]\n\t" \
  "s_cselect_b32 %[xld], %[xlb], %[xld]\n\t" \
  "buffer_load_dwordx4 v248, %[xrs], %[xso] offen offset:2048 nt lds\n\t" \
  "s_mov_b64 exec, %[xm]\n\t" \
  "buffer_load_dwordx4 v249, %[xrs], %[xso] offen offset:3072 nt lds\n\t" \
  "s_mov_b64 exec, -1\n\t" \
  "v_add_u32 v75, 0x10000, v94\n\t" \
  "v_mul_f32 v74, %[kl], v94\n\t" \
  "v_mul_f32 v75, %[kl], v75\n\t" \
  "v_mul_f32 v76, v74, v74\n\t" \
  "v_mul_f32 v77, v75, v75\n\t" \
  "v_fmaak_f32 v76, v76, v72, 0xc0135761\n\t" \
  "v_fmaak_f32 v77, v77, v72, 0xc0135761\n\t" \
  "v_mul_f32 v76, v74, v76\n\t" \
  "v_mul_f32 v77, v75, v77\n\t" \
  "v_exp_f32 v78, v76\n\t" \
  "v_exp_f32 v79, v77\n\t" \
  "v_add_f32 v76, 1.0, v78\n\t" \
  "v_add_f32 v77, 1.0, v79\n\t" \
  "v_rcp_f32 v76, v76\n\t" \
  "v_rcp_f32 v77, v77\n\t" \
  "v_mul_f32 v74, v74, v76\n\t" \
  "v_mul_f32 v75, v75, v77\n\t" \
  "v_mul_f32 v78, -v74, v78\n\t" \
  "v_mul_f32 v79, -v75, v79\n\t" \
  "v_cvt_pk_bf16_f32 v83, v78, v79\n\t" \
  "v_cvt_pk_bf16_f32 v82, v74, v75\n\t" \
  "v_add_u32 v94, 0x2000000, v94\n\t" \
  "ds_write_b32 v70, v82 offset:2048\n\t" \
  "ds_write_b32 v71, v83 offset:2048\n\t" \
  "s_waitcnt vmcnt(7)\n\t" \
  "s_barrier\n\t" \
  "s_mov_b32 m0, %[xld]\n\t" \
  "s_cmp_lt_u32 %[gy], 0x3000000\n\t" \
  "s_cselect_b32 %[xso], %[gy], 0x40000000\n\t" \
  "buffer_load_dwordx4 v246, %[xrs], %[xso] offen offset:0 nt lds\n\t" \
  "s_add_u32 %[gy], %[gy], %[dgy]\n\t" \
  "buffer_load_dwordx4 v247, %[xrs], %[xso] offen offset:1024 nt lds\n\t" \
  "s_add_u32 %[xld], %[xld], 15184\n\t" \
  "s_cmp_eq_u32 %[xld], %[xle]\n\t" \
  "s_cselect_b32 %[xld], %[xlb], %[xld]\n\t" \
  "buffer_load_dwordx4 v248, %[xrs], %[xso] offen offset:2048 nt lds\n\t" \
  "s_mov_b64 exec, %[xm]\n\t" \
  "buffer_load_dwordx4 v249, %[xrs], %[xso] offen offset:3072 nt lds\n\t" \
  "s_mov_b64 exec, -1\n\t" \
  "s_mov_b32 m0, %[w2d]\n\t" \
  "s_nop 0\n\t" \
  "buffer_load_dwordx4 v244, %[w2r], %[w2s] offen offset:0 lds\n\t" \
  "buffer_load_dwordx4 v244, %[w2r], %[w2s] offen offset:1024 lds\n\t" \
  "buffer_load_dwordx4 v244, %[w2r], %[w2s] offen offset:2048 lds\n\t" \
  "v_add_u32 v85, 0x10000, v94\n\t" \
  "v_mul_f32 v84, %[kl], v94\n\t" \
  "v_mul_f32 v85, %[kl], v85\n\t" \
  "v_mul_f32 v86, v84, v84\n\t" \
  "v_mul_f32 v87, v85, v85\n\t" \
  "v_fmaak_f32 v86, v86, v72, 0xc0135761\n\t" \
  "v_fmaak_f32 v87, v87, v72, 0xc0135761\n\t" \
  "v_mul_f32 v86, v84, v86\n\t" \
  "v_mul_f32 v87, v85, v87\n\t" \
  "v_exp_f32 v88, v86\n\t" \
  "v_exp_f32 v89, v87\n\t" \
  "v_add_f32 v86, 1.0, v88\n\t" \
  "v_add_f32 v87, 1.0, v89\n\t" \
  "v_rcp_f32 v86, v86\n\t" \
  "v_rcp_f32 v87, v87\n\t" \
  "v_mul_f32 v84, v84, v86\n\t" \
  "v_mul_f32 v85, v85, v87\n\t" \
  "v_mul_f32 v88, -v84, v88\n\t" \
  "v_mul_f32 v89, -v85, v89\n\t" \
  "v_cvt_pk_bf16_f32 v93, v88, v89\n\t" \
  "v_cvt_pk_bf16_f32 v92, v84, v85\n\t" \
  "v_add_u32 v94, 0x2000000, v94\n\t" \
  "ds_write_b32 v70, v92 offset:3072\n\t" \
  "ds_write_b32 v71, v93 offset:3072\n\t" \
  "v_add_u32 v75, 0x10000, v94\n\t" \
  "v_mul_f32 v74, %[kl], v94\n\t" \
  "v_mul_f32 v75, %[kl], v75\n\t" \
  "v_mov_b32 v83, 0x80008000\n\t" \
  "v_cvt_pk_bf16_f32 v82, v74, v75\n\t" \
  "v_add_u32 v94, 0x2000000, v94\n\t" \
  "ds_write_b32 v70, v82 offset:4096\n\t" \
  "ds_write_b32 v71, v83 offset:4096\n\t" \
  "v_add_u32 v85, 0x10000, v94\n\t" \
  "v_mul_f32 v84, %[kl], v94\n\t" \
  "v_mul_f32 v85, %[kl], v85\n\t" \
  "v_mov_b32 v93, 0x80008000\n\t" \
  "v_cvt_pk_bf16_f32 v92, v84, v85\n\t" \
  "ds_write_b32 v70, v92 offset:5120\n\t" \
  "ds_write_b32 v71, v93 offset:5120\n\t" \
  "s_waitcnt vmcnt(7)\n\t" \
  "s_barrier\n\t" \
  "s_mov_b32 m0, %[wlb]\n\t" \
  "s_mov_b32 %[xso], %[wsb]\n\t" \
  "s_add_u32 %[t0], %[xso], 0x2c00\n\t" \
  "s_add_u32 %[t1], %[t0], 0x2c00\n\t" \
  "buffer_load_dwordx4 v245, %[wrs], %[xso] offen lds\n\t" \
  "buffer_load_dwordx4 v245, %[wrs], %[t0] offen offset:1024 lds\n\t" \
  "buffer_load_dwordx4 v245, %[wrs], %[t1] offen offset:2048 lds\n\t" \
  "s_mov_b32 m0, %[xld]\n\t" \
  "s_cmp_lt_u32 %[gy], 0x3000000\n\t" \
  "s_cselect_b32 %[xso], %[gy], 0x40000000\n\t" \
  "buffer_load_dwordx4 v246, %[xrs], %[xso] offen offset:0 nt lds\n\t" \
  "s_add_u32 %[gy], %[gy], %[dgy]\n\t" \
  "buffer_load_dwordx4 v247, %[xrs], %[xso] offen offset:1024 nt lds\n\t" \
  "s_add_u32 %[xld], %[xld], 15184\n\t" \
  "s_cmp_eq_u32 %[xld], %[xle]\n\t" \
  "s_cselect_b32 %[xld], %[xlb], %[xld]\n\t" \
  "buffer_load_dwordx4 v248, %[xrs], %[xso] offen offset:2048 nt lds\n\t" \
  "s_mov_b64 exec, %[xm]\n\t" \
  "buffer_load_dwordx4 v249, %[xrs], %[xso] offen offset:3072 nt lds\n\t" \
  "s_mov_b64 exec, -1\n\t" \
  "s_waitcnt lgkmcnt(0)\n\t" \
  ""
#define M_CLOB "v0", "v1", "v2", "v3", "v4", "v5", "v6", "v7", "v8", "v9", "v10", "v11", "v12", "v13", "v14", "v15", "v16", "v17", "v18", "v19", "v20", "v21", "v22", "v23", "v24", "v25", "v26", "v27", "v28", "v29", "v30", "v31", "v32", "v33", "v34", "v35", "v36", "v37", "v38", "v39", "v40", "v41", "v42", "v43", "v44", "v45", "v46", "v47", "v48", "v49", "v50", "v51", "v52", "v53", "v54", "v55", "v56", "v57", "v58", "v59", "v60", "v61", "v62", "v63", "v64", "v65", "v66", "v67", "v68", "v69", "v70", "v71", "v72", "v73", "v74", "v75", "v76", "v77", "v78", "v79", "v80", "v81", "v82", "v83", "v84", "v85", "v86", "v87", "v88", "v89", "v90", "v91", "v92", "v93", "v94", "v95", "v96", "v97", "v98", "v99", "v100", "v101", "v102", "v103", "v104", "v105", "v106", "v107", "v108", "v109", "v110", "v111", "v112", "v113", "v114", "v115", "v116", "v117", "v118", "v119", "v120", "v121", "v122", "v123", "v124", "v125", "v126", "v127", "v128", "v129", "v130", "v131", "v132", "v133", "v134", "v135", "v136", "v137", "v138", "v139", "v140", "v141", "v142", "v143", "v144", "v145", "v146", "v147", "v148", "v149", "v150", "v151", "v152", "v153", "v154", "v155", "v156", "v157", "v158", "v159", "v160", "v161", "v162", "v163", "v164", "v165", "v166", "v167", "v168", "v169", "v170", "v171", "v172", "v173", "v174", "v175", "v176", "v177", "v178", "v179", "v180", "v181", "v182", "v183", "v184", "v185", "v186", "v187", "v188", "v189", "v190", "v191", "v192", "v193", "v194", "v195", "v196", "v197", "v198", "v199", "v200", "v201", "v202", "v203", "v204", "v205", "v206", "v207", "v208", "v209", "v210", "v211", "v212", "v213", "v214", "v215", "v216", "v217", "v218", "v219", "v220", "v221", "v222", "v223", "v224", "v225", "v226", "v227", "v228", "v229", "v230", "v231", "v232", "v233", "v234", "v235", "v236", "v237", "v238", "v239", "v240", "v241", "v242", "v243", "v244", "v245"
#define LUT_CLOB "v72", "v73", "v74", "v75", "v76", "v77", "v78", "v79", "v80", "v81", "v82", "v83", "v84", "v85", "v86", "v87", "v88", "v89", "v90", "v91", "v92", "v93"
#define F_CLOB "v0", "v1", "v2", "v3", "v4", "v5", "v6", "v7", "v8", "v9", "v10", "v11", "v12", "v13", "v14", "v15", "v16", "v17", "v18", "v19", "v20", "v21", "v22", "v23", "v24", "v25", "v26", "v27", "v28", "v29", "v30", "v31", "v32", "v33", "v34", "v35", "v36", "v37", "v38", "v39", "v40", "v41", "v42", "v43", "v44", "v45", "v46", "v47", "v48", "v49", "v50", "v51", "v52", "v53", "v54", "v55", "v56", "v57", "v58", "v59", "v60", "v61", "v62", "v63", "v64", "v65", "v66", "v67", "v68", "v69", "v70", "v71", "v72", "v73", "v74", "v75", "v76", "v77", "v78", "v79", "v80", "v81", "v82", "v83", "v84", "v85", "v86", "v87", "v88", "v89", "v90", "v91", "v92", "v93", "v94", "v95", "v96", "v97", "v98", "v99", "v100", "v101", "v102", "v103", "v104", "v105", "v106", "v107", "v108", "v109", "v110", "v111", "v112", "v113", "v114", "v115", "v116", "v117", "v118", "v119", "v120", "v121", "v122", "v123", "v124", "v125", "v126", "v127", "v128", "v129", "v130", "v131", "v132", "v133", "v134", "v135", "v136", "v137", "v138", "v139", "v140", "v141", "v142", "v143", "v144", "v145", "v146", "v147", "v148", "v149", "v150", "v151", "v152", "v153", "v154", "v155", "v156", "v157", "v158", "v159", "v160", "v161", "v162", "v163", "v164", "v165", "v166", "v167", "v168", "v169", "v170", "v171", "v172", "v173", "v174", "v175", "v176", "v177", "v178", "v179", "v180", "v181", "v182", "v183", "v184", "v185", "v186", "v187", "v188", "v189", "v190", "v191", "v192", "v193", "v194", "v195", "v196", "v197", "v198", "v199", "v200", "v201", "v202", "v203", "v204", "v205", "v206", "v207", "v208", "v209", "v210", "v211", "v212", "v213", "v214", "v215", "v216", "v217", "v218", "v219", "v220", "v221", "v222", "v223", "v224", "v225", "v226", "v227", "v228", "v229", "v230", "v231", "v232", "v233", "v234", "v235", "v236", "v237", "v238", "v239", "v252", "v253"

__device__ __forceinline__ unsigned lds_addr(const char* p) { return (unsigned)(uintptr_t)(lds_ptr_t)p; }
__device__ __forceinline__ u32x4 make_rsrc(const void* p, unsigned bytes) {
    const uint64_t a = (uint64_t)(uintptr_t)p;
    u32x4 r = {(unsigned)a, (unsigned)(a >> 32) & 0xffffu, bytes, 0x00020000u};
    return r;
}
__device__ __forceinline__ void dma16(u32x4 rsrc, unsigned voff, unsigned soff, unsigned lds_dst) {
    asm volatile("s_mov_b32 m0, %1\n\ts_nop 0\n\tbuffer_load_dwordx4 %0, %2, %3 offen lds"
                 :: "v"(voff), "s"(lds_dst), "s"(rsrc), "s"(soff) : "memory");
}
__device__ __forceinline__ void warm(u32x4 rsrc, unsigned voff, unsigned soff, unsigned lds_dst) {
    asm volatile("s_mov_b32 m0, %1\n\ts_nop 0\n\tbuffer_load_dword %0, %2, %3 offen lds"
                 :: "v"(voff), "s"(lds_dst), "s"(rsrc), "s"(soff) : "memory");
}
__device__ __forceinline__ int lane_id() {
    int v;
    asm volatile("v_mbcnt_lo_u32_b32 %0, -1, 0\n\tv_mbcnt_hi_u32_b32 %0, -1, %0" : "=v"(v));
    __builtin_assume(v >= 0 && v < 64);
    return v;
}
__device__ __forceinline__ void bar() { asm volatile("s_waitcnt lgkmcnt(0)\n\ts_barrier" ::: "memory"); }
__device__ __forceinline__ float bf2f(unsigned short h) { return __builtin_bit_cast(float, (unsigned)h << 16); }
__device__ __forceinline__ unsigned short f2bf(float f) { return __builtin_bit_cast(unsigned short, (__bf16)f); }
__device__ __forceinline__ float gelu_f(float v) {
    const float e = __builtin_amdgcn_exp2f(v * __builtin_fmaf(v * v, -0.10294324f, -2.3022082f));
    return v * __builtin_amdgcn_rcpf(1.0f + e);
}

__device__ __forceinline__ void plain_block(const unsigned short* x, const unsigned short* dw, const unsigned short* w1,
                                            const unsigned short* w2, unsigned short* out, char* smem, int x0, int y0, int tid) {
    float* zs = (float*)smem;
    float* gs = zs + 64 * CH;
#pragma unroll 1
    for (int u = 0; u < 16; ++u) {
#pragma unroll 1
        for (int i = tid; i < 64 * CH; i += 512) {
            const int p = i / CH, c = i - p * CH, y = y0 + 2 * u + (p >> 5), xx = x0 + (p & 31);
            float a = 0.f;
#pragma unroll 1
            for (int t = 0; t < 49; ++t) {
                const int ky = t / 7, yy = y + ky - 3, xj = xx + (t - 7 * ky) - 3;
                if ((unsigned)yy < (unsigned)IMG && (unsigned)xj < (unsigned)IMG)
                    a = __builtin_fmaf(bf2f(x[(yy * IMG + xj) * CH + c]), bf2f(dw[t * CH + c]), a);
            }
            zs[i] = a;
        }
        __syncthreads();
        if (tid < 64) {
            float m = 0.f, v = 0.f;
#pragma unroll 1
            for (int c = 0; c < CH; ++c) m += zs[tid * CH + c];
            m *= 1.0f / CH;
#pragma unroll 1
            for (int c = 0; c < CH; ++c) { const float d = zs[tid * CH + c] - m; v = __builtin_fmaf(d, d, v); }
            const float r = __builtin_amdgcn_rsqf(v * (1.0f / CH) + 1e-5f);
#pragma unroll 1
            for (int c = 0; c < CH; ++c) zs[tid * CH + c] = bf2f(f2bf((zs[tid * CH + c] - m) * r));
        }
        __syncthreads();
#pragma unroll 1
        for (int i = tid; i < 64 * HID; i += 512) {
            const int p = i / HID, n = i - p * HID;
            float a = 0.f;
#pragma unroll 1
            for (int c = 0; c < CH; ++c) a = __builtin_fmaf(zs[p * CH + c], bf2f(w1[c * HID + n]), a);
            gs[i] = bf2f(f2bf(gelu_f(a)));
        }
        __syncthreads();
#pragma unroll 1
        for (int i = tid; i < 64 * CH; i += 512) {
            const int p = i / CH, c = i - p * CH, o = ((y0 + 2 * u + (p >> 5)) * IMG + x0 + (p & 31)) * CH + c;
            float a = bf2f(x[o]);
#pragma unroll 1
            for (int n = 0; n < HID; ++n) a = __builtin_fmaf(gs[p * HID + n], bf2f(w2[n * CH + c]), a);
            out[o] = f2bf(a);
        }
    }
}

__global__ __launch_bounds__(512, 2) void convnext_kernel(const __hip_bfloat16* __restrict__ x,
                                                          const __hip_bfloat16* __restrict__ dw,
                                                          const __hip_bfloat16* __restrict__ w1,
                                                          const __hip_bfloat16* __restrict__ w2,
                                                          __hip_bfloat16* __restrict__ out) {
    __shared__ __attribute__((aligned(16))) char smem[LDS_BYTES];
    const int tid = threadIdx.x;
    const int l = tid & 63;
    const int wid = __builtin_amdgcn_readfirstlane(tid >> 6);
    const int f = wid & 3;
    const int bid = blockIdx.x;
    asm volatile("s_getpc_b64 s[60:61]\n"
                 ".Lcnx_pc:\n\t"
                 "s_and_b32 s61, s61, 0xffff\n\t"
                 "s_mov_b32 s62, .Lcnx_end-.Lcnx_pc\n\t"
                 "s_mov_b32 s63, 0x20000\n\t"
                 "s_mov_b32 m0, %2\n\t"
                 "s_mov_b64 exec, 15\n\t"
                 "buffer_load_dword %0, s[60:63], %1 offen lds\n\t"
                 "s_mov_b64 exec, -1"
                 :: "v"((tid & 63) * 64 + (tid >> 6) * 256), "s"((bid >> 3) * 2048), "i"(PFOFF)
                 : "memory", "scc", "s60", "s61", "s62", "s63");
    const int x0 = ((bid >> 3) & 15) * 32;
    const int down = bid >> 7;
    const int y0 = (2 * (bid & 7) + down) * 32;
    const int dir = 2 * down - 1;
    const int yb = down ? y0 - 3 : y0 + 34;
    const unsigned sb = lds_addr(smem);

    if (wid >= 4) {
        __builtin_amdgcn_s_setprio(3);
        const u32x4 drs = make_rsrc(dw, 49 * CH * 2);
        const unsigned L = 64 * (f & 1) + l, cell = (L * 79) >> 10;
        const unsigned o = (cell - 1 < 7u && L - cell * 13 != 12) ? (L - cell - 12) * 16 : OOB;
#pragma unroll
        for (int k = 0; k < 5; ++k) {
            const int ky = (f >> 1) + 2 * k - 1;
            dma16(drs, o, (unsigned)ky < 7u ? (unsigned)((down ? ky : 6 - ky) * (7 * 192)) : OOB, sb + DWOFF + (ky + 1) * 2064 + (f & 1) * 1024);
        }
        __builtin_amdgcn_s_setprio(0);
    } else __builtin_amdgcn_s_setprio(2);
    const unsigned xlb = sb + XOFF + (f >> 1) * 7592 + (f & 1) * 4096, xle = xlb + 6 * PS;
    const unsigned long long xm = (f & 1) ? (1ull << 26) - 1 : ~0ull;
    unsigned xld = xlb, gy = (unsigned)((yb + dir * (f >> 1)) * ROWB), xso, xv[4];
    auto x_lanes = [&](int l) {
#pragma unroll
        for (int k = 0; k < 4; ++k) {
            const unsigned L = 64 * (4 * (f & 1) + k) + l, pp = (L * 0xa3eu) >> 16, o = (unsigned)((x0 - 3) * (CH * 2)) + (L - pp) * 16;
            xv[k] = (L - pp * 25 != 24 && o < (unsigned)ROWB) ? o + 3072 - 1024 * k : OOB;
        }
        asm volatile("" : "+{v246}"(xv[0]), "+{v247}"(xv[1]), "+{v248}"(xv[2]), "+{v249}"(xv[3]), "+s"(xld), "+s"(gy));
    };
    auto x_rsrc = [&]() {
        const char* p = (const char*)x;
        asm volatile("" : "+s"(p));
        return make_rsrc(p - 3072, IMG * ROWB + 3072);
    };
    auto w1_lane = [&](int l) {
        const int pos = (l >> 1) & 15, c16 = ((pos >> 2) & 1) * 8 + (pos >> 3) * 4 + (pos & 3);
        return (unsigned)(c16 * (HID * 2) + (l & 1) * 32 + (l >> 5) * 16);
    };
#define X_IN [xrs] "s"(xrs), [xlb] "s"(xlb), [xle] "s"(xle), [dgy] "s"(2 * dir * ROWB), [xm] "s"(xm)

#define MS_X [xld] "+&s"(xld), [gy] "+&s"(gy), [xso] "=&s"(xso)
#define MS_XIN "{v246}"(xv[0]), "{v247}"(xv[1]), "{v248}"(xv[2]), "{v249}"(xv[3]), X_IN

    if (wid >= 4) {
        const int l = lane_id();
        x_lanes(l);
        const u32x4 xrs = x_rsrc();
        const bool isw1 = f < 2;
        const u32x4 wrs = make_rsrc(isw1 ? w1 : w2, CH * HID * 2);
        const unsigned wv = isw1 ? w1_lane(l) : (unsigned)(l * 16);
        const unsigned wpd = isw1 ? 16 * HID * 2 - 1024 : 0, wcs = isw1 ? 64 : 6144;
        const unsigned wst = isw1 ? 6144 : 6160;
        unsigned wso = isw1 ? (f & 1) * (48 * HID * 2) + 192 : (f & 1) * 3072 + 2 * 6144;
        unsigned wld = sb + (isw1 ? W1OFF + (f & 1) * 3072 : W2OFF + (f & 1) * 3080 + 2 * 6160);
        unsigned cs = 0, t0, t1, lc;
        const int jx = (l >> 4) & 1, jy = l >> 5, xg = l & 15;
        unsigned cb = sb + XOFF + jx * 7592 + xg * 400 + jy * 192 + f * 48;
        const unsigned zw = sb + ZOFF + f * 3072 + l * 16;
        const unsigned aa = sb + DWOFF + (l & 8 ? 0 : 2064) + (4 * jy + jx - ((l >> 2) & 1) + 1) * 208 + f * 48 + (l & 3) * 2;
        unsigned es = (f & 1) * (isw1 ? 48 * HID * 2 : 3072) + (isw1 ? 3 : 2) * wcs;

        asm volatile(F_ASM
                     : "+{v241}"(cb), [wso] "+&s"(wso), [wld] "+&s"(wld), [xld] "+&s"(xld), [gy] "+&s"(gy),
                       [cs] "+&s"(cs), [es] "+&s"(es), [xso] "=&s"(xso), [t0] "=&s"(t0), [t1] "=&s"(t1), [lc] "=&s"(lc)
                     : "{v240}"((l & 3) * 0x5555u), "{v242}"(zw), "{v243}"(sb + STOFF + (f >> 1) * 1024 + l * 16 + (f & 1) * 8),
                       "{v244}"(sb + STOFF + l * 16), "{v245}"(wv), "{v251}"(aa),
                       "{v246}"(xv[0]), "{v247}"(xv[1]), "{v248}"(xv[2]), "{v249}"(xv[3]), X_IN,
                       [wrs] "s"(wrs), [wpd] "s"(wpd), [wcs] "s"(wcs), [wsa] "s"(isw1 ? -11 * 64 : 6144), [wsb] "s"(isw1 ? 64 : -11 * 6144),
                       [wst] "s"(wst), [wla] "s"(isw1 ? -2 * 6144 : 6160), [wlb] "s"(isw1 ? 6144 : -3 * 6160),
                       [wlc] "s"(isw1 ? -2 * 6144 : -3 * 6160), [el] "s"(sb + XOFF + (isw1 ? (f & 1) * 3072 : 6160 + (f & 1) * 3080)), [ps] "s"(PS), [gl] "s"(0x1p-114f * DL), [gt] "s"(CT)
                     : "memory", "scc", F_CLOB);
        const u32x4* wp = (const u32x4*)w1 + f * 64 + lane_id();
        u32x4 v[18];
#pragma unroll
        for (int k = 0; k < 18; ++k) v[k] = wp[k * 256];
        unsigned mx = 0;
        asm volatile("s_setprio 1" : "+v"(mx) :: "memory");
#pragma unroll
        for (int k = 0; k < 72; ++k)
            asm("v_pk_maximum3_f16 %0, %0, %1, %1 neg_lo:[0,0,1] neg_hi:[0,0,1]" : "+v"(mx) : "v"(v[k >> 2][k & 3]));
        const unsigned m = max(mx & 0x7fffu, (mx >> 16) & 0x7fffu);
        *(unsigned*)(smem + FLOFF + f * 4) =
            (__builtin_amdgcn_ballot_w64(m >= 0x4180u) ? 1u : 0u) | (__builtin_amdgcn_ballot_w64(m >= 0x3a80u) ? 2u : 0u);

    } else {
        x_lanes(l);
        const u32x4 xrs = x_rsrc();
        asm volatile(MS_ASM0 : MS_X : MS_XIN : "memory", "scc");
        unsigned t0, t1;
        if (f >= 2) warm(make_rsrc(f & 1 ? w1 : w2, CH * HID * 2), l < 36 ? l * 64 : OOB, (bid >> 3) * 2304, sb + PFOFF);
        const int q = l >> 4, i = l & 15, r4 = i >> 2, c4 = i & 3, wy = wid >> 1, wx = wid & 1;
        unsigned a1 = sb + W1OFF + (q >> 1) * 1024 + ((q & 1) * 4 + r4) * 32 + c4 * 8;
        unsigned a2 = sb + W2OFF + (q & 1) * 3080 + ((q >> 1) * 8 + r4) * 192 + c4 * 16;
        unsigned zr = sb + ZOFF + q * 1024 + (16 * wid + i) * 16;
        unsigned rr = sb + XOFF + (1 + wy) * PS + (1 - wy) * 7592 + (i + 1 + wx) * 400 + (1 - wx) * 192 + q * 16;
        unsigned vo = (2 * i + wx) * 192 + q * 16;
        const unsigned one = q == r4 ? 0x3f80u << (16 * (c4 & 1)) : 0u;
        u32x2 id = {c4 < 2 ? one : 0u, c4 < 2 ? 0u : one};
        unsigned ob = (unsigned)((yb + dir * (3 + wy)) * ROWB + x0 * (CH * 2)), rs = 1 + wy, lc;
        unsigned tb = (unsigned)tid << 17 | 0x8000u;
        asm volatile(MS_ASM1
                     : "+{v94}"(tb), MS_X, [t0] "=&s"(t0), [t1] "=&s"(t1), "+v"(a1), "+{v250}"(a2), "+{v251}"(zr), "+{v252}"(rr),
                       "+{v253}"(vo), "+v"(id), "+s"(ob)
                     : "{v95}"(tid ? tb : 0u), "{v70}"(sb + tid * 4), "{v71}"(sb + 65536 + tid * 4), "{v244}"(l * 16), "{v245}"(w1_lane(l)), MS_XIN, [wrs] "s"(make_rsrc(w1, CH * HID * 2)),
                       [wso] "s"((f & 1) * (48 * HID * 2) + (f >> 1) * 64), [wld] "s"(sb + W1OFF + f * 3072),
                       [wsb] "s"((f & 1) * (48 * HID * 2) + 128), [wlb] "s"(sb + W1OFF + 2 * 6144 + (f & 1) * 3072),
                       [w2r] "s"(make_rsrc(w2, CH * HID * 2)), [w2s] "s"(f * 3072), [w2d] "s"(sb + W2OFF + f * 3080),
                       [kl] "s"(0x1p114f / DL)
                     : "memory", "scc", LUT_CLOB);

        asm volatile(M_ASM
                     : "+{v252}"(rr), "+{v249}"(a1), "+{v250}"(a2), [ob] "+&s"(ob), [rs] "+&s"(rs), [t0] "=&s"(t0), [lc] "=&s"(lc)
                     : "{v[246:247]}"(id), "{v248}"(0xddddddddu), "{v251}"(zr), "{v253}"(vo),
                       [osrd] "s"(make_rsrc(out, IMG * ROWB)), [dob] "s"(2 * dir * ROWB), [ps] "s"(PS), [h1] "s"(GH1), [hc] "s"(GHC),
                       [kk] "s"(0x1p114f * CT / DL)
                     : "memory", "scc", M_CLOB);

    }
    asm volatile(".Lcnx_end:" ::: "memory");
    bar();
    const u32x4 fl = *(const u32x4*)(smem + FLOFF);
    if (__builtin_amdgcn_readfirstlane(fl[0] | fl[1] | fl[2] | fl[3]) != 2) {
        asm volatile("s_waitcnt vmcnt(0)" ::: "memory");
        __syncthreads();
        plain_block((const unsigned short*)x, (const unsigned short*)dw, (const unsigned short*)w1, (const unsigned short*)w2,
                    (unsigned short*)out, smem, x0, y0, wid * 64 + lane_id());
    }
}

}

extern "C" void kernel_launch(void* const* d_in, const int* in_sizes, int n_in, void* d_out, int out_size,
                              void* d_ws, size_t ws_size, hipStream_t stream) {
    (void)in_sizes; (void)n_in; (void)out_size; (void)d_ws; (void)ws_size;
    const __hip_bfloat16* x = (const __hip_bfloat16*)d_in[0];
    const __hip_bfloat16* dw = (const __hip_bfloat16*)d_in[1];
    const __hip_bfloat16* w1 = (const __hip_bfloat16*)d_in[2];
    const __hip_bfloat16* w2 = (const __hip_bfloat16*)d_in[3];
    __hip_bfloat16* out = (__hip_bfloat16*)d_out;
    convnext_kernel<<<256, 512, 0, stream>>>(x, dw, w1, w2, out);
}
```

```cpp
#include <hip/hip_runtime.h>
#include <hip/hip_bf16.h>
#include <stdint.h>

namespace {

typedef __attribute__((ext_vector_type(4))) unsigned u32x4;
typedef __attribute__((ext_vector_type(2))) unsigned u32x2;
typedef __attribute__((address_space(3))) void* lds_ptr_t;

constexpr int IMG = 512;
constexpr int CH = 96;
constexpr int HID = 384;
constexpr int ROWB = IMG * CH * 2;
constexpr int LUTN = 24 * 128;
constexpr int W1OFF = LUTN * 2;
constexpr int W2OFF = W1OFF + 3 * 6144;
constexpr int DWOFF = W2OFF + 2 * 6160;
constexpr int ZOFF = W2OFF + 4 * 6160;
constexpr int STOFF = ZOFF + 12288;
constexpr int FLOFF = STOFF + 2048;
constexpr int XOFF = 65536 + LUTN * 2;
constexpr int PS = 15184;
constexpr int PFOFF = XOFF + 6 * PS;
constexpr int LDS_BYTES = 160 * 1024;
static_assert(FLOFF + 16 <= 65536 && DWOFF + 20640 <= 65536 && PFOFF + 256 <= LDS_BYTES, "LDS map");
constexpr unsigned OOB = 0x40000000u;

constexpr float DL = 1.0f + 1.0f / 362.0f;
constexpr float CT = 0.46866869f;
constexpr unsigned GH1 = 0xc4eac4eau;
constexpr unsigned GHC = 0x377a377au;

#define M_ASM \
  "s_setprio 0\n\t" \
  "s_waitcnt vmcnt(10)\n\t" \
  "s_barrier\n\t" \
  "ds_read_b128 v[96:99], v251 offset:0\n\t" \
  "ds_read_b128 v[100:103], v251 offset:4096\n\t" \
  "ds_read_b128 v[104:107], v251 offset:8192\n\t" \
  "ds_read_b64_tr_b16 v[208:209], v249 offset:0\n\t" \
  "ds_read_b64_tr_b16 v[210:211], v249 offset:256\n\t" \
  "ds_read_b64_tr_b16 v[212:213], v249 offset:2048\n\t" \
  "ds_read_b64_tr_b16 v[214:215], v249 offset:2304\n\t" \
  "ds_read_b64_tr_b16 v[216:217], v249 offset:4096\n\t" \
  "ds_read_b64_tr_b16 v[218:219], v249 offset:4352\n\t" \
  "ds_read_b64_tr_b16 v[220:221], v249 offset:512\n\t" \
  "ds_read_b64_tr_b16 v[222:223], v249 offset:768\n\t" \
  "s_waitcnt lgkmcnt(6)\n\t" \
  "v_mfma_f32_16x16x32_bf16 v[144:147], v[208:211], v[96:99], 0\n\t" \
  "ds_read_b64_tr_b16 v[208:209], v249 offset:2560\n\t" \
  "ds_read_b64_tr_b16 v[210:211], v249 offset:2816\n\t" \
  "s_waitcnt lgkmcnt(6)\n\t" \
  "v_mfma_f32_16x16x32_bf16 v[144:147], v[212:215], v[100:103], v[144:147]\n\t" \
  "ds_read_b64_tr_b16 v[212:213], v249 offset:4608\n\t" \
  "ds_read_b64_tr_b16 v[214:215], v249 offset:4864\n\t" \
  "s_waitcnt lgkmcnt(6)\n\t" \
  "v_mfma_f32_16x16x32_bf16 v[144:147], v[216:219], v[104:107], v[144:147]\n\t" \
  "s_waitcnt lgkmcnt(0)\n\t" \
  "s_waitcnt vmcnt(7)\n\t" \
  "s_barrier\n\t" \
  "ds_read_b64_tr_b16 v[216:217], v249 offset:6144\n\t" \
  "ds_read_b64_tr_b16 v[218:219], v249 offset:6400\n\t" \
  "v_mfma_f32_16x16x32_bf16 v[148:151], v[220:223], v[96:99], 0\n\t" \
  "ds_read_b64_tr_b16 v[220:221], v249 offset:8192\n\t" \
  "ds_read_b64_tr_b16 v[222:223], v249 offset:8448\n\t" \
  "s_nop 0\n\t" \
  "v_lshlrev_b32_sdwa v144, 1, v144 dst_sel:DWORD dst_unused:UNUSED_PAD src0_sel:DWORD src1_sel:WORD_1\n\t" \
  "v_mfma_f32_16x16x32_bf16 v[148:151], v[208:211], v[100:103], v[148:151]\n\t" \
  "ds_read_b64_tr_b16 v[208:209], v249 offset:10240\n\t" \
  "ds_read_b64_tr_b16 v[210:211], v249 offset:10496\n\t" \
  "ds_read_u16_d16_hi v144, v144 offset:0\n\t" \
  "v_lshlrev_b32_sdwa v145, 1, v145 dst_sel:DWORD dst_unused:UNUSED_PAD src0_sel:DWORD src1_sel:WORD_1\n\t" \
  "v_mfma_f32_16x16x32_bf16 v[148:151], v[212:215], v[104:107], v[148:151]\n\t" \
  "ds_read_b64_tr_b16 v[212:213], v249 offset:6656\n\t" \
  "ds_read_b64_tr_b16 v[214:215], v249 offset:6912\n\t" \
  "ds_read_u16_d16_hi v145, v145 offset:0\n\t" \
  "v_lshlrev_b32_sdwa v146, 1, v146 dst_sel:DWORD dst_unused:UNUSED_PAD src0_sel:DWORD src1_sel:WORD_1\n\t" \
  "v_lshlrev_b32_sdwa v147, 1, v147 dst_sel:DWORD dst_unused:UNUSED_PAD src0_sel:DWORD src1_sel:WORD_1\n\t" \
  "s_nop 2\n\t" \
  "v_lshlrev_b32_sdwa v148, 1, v148 dst_sel:DWORD dst_unused:UNUSED_PAD src0_sel:DWORD src1_sel:WORD_1\n\t" \
  "v_lshlrev_b32_sdwa v149, 1, v149 dst_sel:DWORD dst_unused:UNUSED_PAD src0_sel:DWORD src1_sel:WORD_1\n\t" \
  "ds_read_b64 v[230:231], v252 offset:0\n\t" \
  "s_waitcnt lgkmcnt(0)\n\t" \
  "v_mfma_f32_16x16x16_bf16 v[0:3], v[246:247], v[230:231], 0\n\t" \
  "ds_read_b64 v[232:233], v252 offset:8\n\t" \
  "ds_read_b64 v[234:235], v252 offset:64\n\t" \
  "ds_read_b64 v[236:237], v252 offset:72\n\t" \
  "ds_read_b64 v[242:243], v252 offset:128\n\t" \
  "ds_read_b64 v[244:245], v252 offset:136\n\t" \
  "ds_read_u16_d16_hi v146, v146 offset:0\n\t" \
  "ds_read_u16_d16_hi v147, v147 offset:0\n\t" \
  "ds_read_u16_d16_hi v148, v148 offset:0\n\t" \
  "ds_read_u16_d16_hi v149, v149 offset:0\n\t" \
  "s_waitcnt lgkmcnt(8)\n\t" \
  "v_mfma_f32_16x16x16_bf16 v[4:7], v[246:247], v[232:233], 0\n\t" \
  "v_lshlrev_b32_sdwa v150, 1, v150 dst_sel:DWORD dst_unused:UNUSED_PAD src0_sel:DWORD src1_sel:WORD_1\n\t" \
  "v_lshlrev_b32_sdwa v151, 1, v151 dst_sel:DWORD dst_unused:UNUSED_PAD src0_sel:DWORD src1_sel:WORD_1\n\t" \
  "v_mfma_f32_16x16x32_bf16 v[176:179], v[216:219], v[96:99], 0\n\t" \
  "ds_read_b64_tr_b16 v[216:217], v249 offset:8704\n\t" \
  "ds_read_b64_tr_b16 v[218:219], v249 offset:8960\n\t" \
  "ds_read_u16_d16_hi v150, v150 offset:0\n\t" \
  "ds_read_u16_d16_hi v151, v151 offset:0\n\t" \
  "v_mfma_f32_16x16x32_bf16 v[176:179], v[220:223], v[100:103], v[176:179]\n\t" \
  "ds_read_b64_tr_b16 v[220:221], v249 offset:10752\n\t" \
  "ds_read_b64_tr_b16 v[222:223], v249 offset:11008\n\t" \
  "s_waitcnt lgkmcnt(13)\n\t" \
  "v_mfma_f32_16x16x16_bf16 v[8:11], v[246:247], v[234:235], 0\n\t" \
  "s_waitcnt lgkmcnt(12)\n\t" \
  "v_mfma_f32_16x16x16_bf16 v[12:15], v[246:247], v[236:237], 0\n\t" \
  "v_mfma_f32_16x16x32_bf16 v[176:179], v[208:211], v[104:107], v[176:179]\n\t" \
  "ds_read_b64_tr_b16 v[208:209], v250 offset:0\n\t" \
  "ds_read_b64_tr_b16 v[210:211], v250 offset:768\n\t" \
  "v_mfma_f32_16x16x32_bf16 v[180:183], v[212:215], v[96:99], 0\n\t" \
  "ds_read_b64_tr_b16 v[212:213], v250 offset:8\n\t" \
  "ds_read_b64_tr_b16 v[214:215], v250 offset:776\n\t" \
  "s_waitcnt lgkmcnt(14)\n\t" \
  "v_mfma_f32_16x16x16_bf16 v[16:19], v[246:247], v[242:243], 0\n\t" \
  "s_nop 0\n\t" \
  "v_lshlrev_b32_sdwa v176, 1, v176 dst_sel:DWORD dst_unused:UNUSED_PAD src0_sel:DWORD src1_sel:WORD_1\n\t" \
  "v_lshlrev_b32_sdwa v177, 1, v177 dst_sel:DWORD dst_unused:UNUSED_PAD src0_sel:DWORD src1_sel:WORD_1\n\t" \
  "v_mfma_f32_16x16x16_bf16 v[20:23], v[246:247], v[244:245], 0\n\t" \
  "ds_read_u16_d16_hi v176, v176 offset:0\n\t" \
  "ds_read_u16_d16_hi v177, v177 offset:0\n\t" \
  "s_waitcnt lgkmcnt(10)\n\t" \
  "v_mfma_f32_16x16x32_bf16 v[180:183], v[216:219], v[100:103], v[180:183]\n\t" \
  "ds_read_b64_tr_b16 v[216:217], v250 offset:64\n\t" \
  "ds_read_b64_tr_b16 v[218:219], v250 offset:832\n\t" \
  "s_waitcnt lgkmcnt(8)\n\t" \
  "v_mfma_f32_16x16x32_bf16 v[180:183], v[220:223], v[104:107], v[180:183]\n\t" \
  "ds_read_b64_tr_b16 v[220:221], v250 offset:72\n\t" \
  "ds_read_b64_tr_b16 v[222:223], v250 offset:840\n\t" \
  "s_waitcnt lgkmcnt(8)\n\t" \
  "v_smfmac_f32_16x16x64_bf16 v[0:3], v[208:211], v[144:151], v248\n\t" \
  "ds_read_b64_tr_b16 v[208:209], v250 offset:128\n\t" \
  "ds_read_b64_tr_b16 v[210:211], v250 offset:896\n\t" \
  "v_lshlrev_b32_sdwa v178, 1, v178 dst_sel:DWORD dst_unused:UNUSED_PAD src0_sel:DWORD src1_sel:WORD_1\n\t" \
  "s_waitcnt lgkmcnt(8)\n\t" \
  "v_smfmac_f32_16x16x64_bf16 v[4:7], v[212:215], v[144:151], v248\n\t" \
  "ds_read_b64_tr_b16 v[212:213], v250 offset:136\n\t" \
  "ds_read_b64_tr_b16 v[214:215], v250 offset:904\n\t" \
  "ds_read_u16_d16_hi v178, v178 offset:0\n\t" \
  "v_lshlrev_b32_sdwa v179, 1, v179 dst_sel:DWORD dst_unused:UNUSED_PAD src0_sel:DWORD src1_sel:WORD_1\n\t" \
  "s_waitcnt lgkmcnt(7)\n\t" \
  "v_smfmac_f32_16x16x64_bf16 v[8:11], v[216:219], v[144:151], v248\n\t" \
  "s_waitcnt vmcnt(4)\n\t" \
  "s_barrier\n\t" \
  "ds_read_b64_tr_b16 v[216:217], v249 offset:12288\n\t" \
  "ds_read_b64_tr_b16 v[218:219], v249 offset:12544\n\t" \
  "ds_read_u16_d16_hi v179, v179 offset:0\n\t" \
  "s_waitcnt lgkmcnt(8)\n\t" \
  "v_smfmac_f32_16x16x64_bf16 v[12:15], v[220:223], v[144:151], v248\n\t" \
  "ds_read_b64_tr_b16 v[220:221], v249 offset:14336\n\t" \
  "ds_read_b64_tr_b16 v[222:223], v249 offset:14592\n\t" \
  "v_lshlrev_b32_sdwa v180, 1, v180 dst_sel:DWORD dst_unused:UNUSED_PAD src0_sel:DWORD src1_sel:WORD_1\n\t" \
  "s_waitcnt lgkmcnt(8)\n\t" \
  "v_smfmac_f32_16x16x64_bf16 v[16:19], v[208:211], v[144:151], v248\n\t" \
  "s_add_u32 %[rs], %[rs], 1\n\t" \
  "ds_read_b64_tr_b16 v[208:209], v249 offset:16384\n\t" \
  "ds_read_b64_tr_b16 v[210:211], v249 offset:16640\n\t" \
  "ds_read_u16_d16_hi v180, v180 offset:0\n\t" \
  "v_lshlrev_b32_sdwa v181, 1, v181 dst_sel:DWORD dst_unused:UNUSED_PAD src0_sel:DWORD src1_sel:WORD_1\n\t" \
  "s_waitcnt lgkmcnt(9)\n\t" \
  "v_smfmac_f32_16x16x64_bf16 v[20:23], v[212:215], v[144:151], v248\n\t" \
  "s_cmp_eq_u32 %[rs], 6\n\t" \
  "s_cselect_b32 %[rs], 0, %[rs]\n\t" \
  "ds_read_b64_tr_b16 v[212:213], v249 offset:12800\n\t" \
  "ds_read_b64_tr_b16 v[214:215], v249 offset:13056\n\t" \
  "ds_read_u16_d16_hi v181, v181 offset:0\n\t" \
  "s_cselect_b32 %[t0], -75920, %[ps]\n\t" \
  "v_add_u32 v252, %[t0], v252\n\t" \
  "s_waitcnt lgkmcnt(9)\n\t" \
  "v_mfma_f32_16x16x32_bf16 v[144:147], v[216:219], v[96:99], 0\n\t" \
  "ds_read_b64_tr_b16 v[216:217], v249 offset:14848\n\t" \
  "ds_read_b64_tr_b16 v[218:219], v249 offset:15104\n\t" \
  "v_lshlrev_b32_sdwa v182, 1, v182 dst_sel:DWORD dst_unused:UNUSED_PAD src0_sel:DWORD src1_sel:WORD_1\n\t" \
  "s_waitcnt lgkmcnt(8)\n\t" \
  "v_mfma_f32_16x16x32_bf16 v[144:147], v[220:223], v[100:103], v[144:147]\n\t" \
  "ds_read_b64_tr_b16 v[220:221], v249 offset:16896\n\t" \
  "ds_read_b64_tr_b16 v[222:223], v249 offset:17152\n\t" \
  "ds_read_u16_d16_hi v182, v182 offset:0\n\t" \
  "v_lshlrev_b32_sdwa v183, 1, v183 dst_sel:DWORD dst_unused:UNUSED_PAD src0_sel:DWORD src1_sel:WORD_1\n\t" \
  "s_waitcnt lgkmcnt(9)\n\t" \
  "v_mfma_f32_16x16x32_bf16 v[144:147], v[208:211], v[104:107], v[144:147]\n\t" \
  "ds_read_b64_tr_b16 v[208:209], v250 offset:6160\n\t" \
  "ds_read_b64_tr_b16 v[210:211], v250 offset:6928\n\t" \
  "ds_read_u16_d16_hi v183, v183 offset:0\n\t" \
  "s_waitcnt lgkmcnt(9)\n\t" \
  "v_mfma_f32_16x16x32_bf16 v[148:151], v[212:215], v[96:99], 0\n\t" \
  "ds_read_b64_tr_b16 v[212:213], v250 offset:6168\n\t" \
  "ds_read_b64_tr_b16 v[214:215], v250 offset:6936\n\t" \
  "s_nop 0\n\t" \
  "v_lshlrev_b32_sdwa v144, 1, v144 dst_sel:DWORD dst_unused:UNUSED_PAD src0_sel:DWORD src1_sel:WORD_1\n\t" \
  "s_waitcnt lgkmcnt(8)\n\t" \
  "v_mfma_f32_16x16x32_bf16 v[148:151], v[216:219], v[100:103], v[148:151]\n\t" \
  "ds_read_b64_tr_b16 v[216:217], v250 offset:6224\n\t" \
  "ds_read_b64_tr_b16 v[218:219], v250 offset:6992\n\t" \
  "ds_read_u16_d16_hi v144, v144 offset:0\n\t" \
  "v_lshlrev_b32_sdwa v145, 1, v145 dst_sel:DWORD dst_unused:UNUSED_PAD src0_sel:DWORD src1_sel:WORD_1\n\t" \
  "s_waitcnt lgkmcnt(9)\n\t" \
  "v_mfma_f32_16x16x32_bf16 v[148:151], v[220:223], v[104:107], v[148:151]\n\t" \
  "ds_read_b64_tr_b16 v[220:221], v250 offset:6232\n\t" \
  "ds_read_b64_tr_b16 v[222:223], v250 offset:7000\n\t" \
  "ds_read_u16_d16_hi v145, v145 offset:0\n\t" \
  "s_waitcnt lgkmcnt(8)\n\t" \
  "v_smfmac_f32_16x16x64_bf16 v[0:3], v[208:211], v[176:183], v248\n\t" \
  "ds_read_b64_tr_b16 v[208:209], v250 offset:6288\n\t" \
  "ds_read_b64_tr_b16 v[210:211], v250 offset:7056\n\t" \
  "v_lshlrev_b32_sdwa v146, 1, v146 dst_sel:DWORD dst_unused:UNUSED_PAD src0_sel:DWORD src1_sel:WORD_1\n\t" \
  "s_waitcnt lgkmcnt(8)\n\t" \
  "v_smfmac_f32_16x16x64_bf16 v[4:7], v[212:215], v[176:183], v248\n\t" \
  "ds_read_b64_tr_b16 v[212:213], v250 offset:6296\n\t" \
  "ds_read_b64_tr_b16 v[214:215], v250 offset:7064\n\t" \
  "ds_read_u16_d16_hi v146, v146 offset:0\n\t" \
  "v_lshlrev_b32_sdwa v147, 1, v147 dst_sel:DWORD dst_unused:UNUSED_PAD src0_sel:DWORD src1_sel:WORD_1\n\t" \
  "s_waitcnt lgkmcnt(9)\n\t" \
  "v_smfmac_f32_16x16x64_bf16 v[8:11], v[216:219], v[176:183], v248\n\t" \
  "s_waitcnt vmcnt(0)\n\t" \
  "s_barrier\n\t" \
  "s_setprio 2\n\t" \
  "ds_read_b64_tr_b16 v[216:217], v249 offset:0\n\t" \
  "ds_read_b64_tr_b16 v[218:219], v249 offset:256\n\t" \
  "ds_read_b128 v[108:111], v251 offset:0\n\t" \
  "ds_read_b128 v[112:115], v251 offset:4096\n\t" \
  "ds_read_b128 v[116:119], v251 offset:8192\n\t" \
  "ds_read_u16_d16_hi v147, v147 offset:0\n\t" \
  "s_waitcnt lgkmcnt(12)\n\t" \
  "v_smfmac_f32_16x16x64_bf16 v[12:15], v[220:223], v[176:183], v248\n\t" \
  "ds_read_b64_tr_b16 v[220:221], v249 offset:2048\n\t" \
  "ds_read_b64_tr_b16 v[222:223], v249 offset:2304\n\t" \
  "v_lshlrev_b32_sdwa v148, 1, v148 dst_sel:DWORD dst_unused:UNUSED_PAD src0_sel:DWORD src1_sel:WORD_1\n\t" \
  "s_waitcnt lgkmcnt(11)\n\t" \
  "v_smfmac_f32_16x16x64_bf16 v[16:19], v[208:211], v[176:183], v248\n\t" \
  "ds_read_b64_tr_b16 v[208:209], v249 offset:4096\n\t" \
  "ds_read_b64_tr_b16 v[210:211], v249 offset:4352\n\t" \
  "ds_read_u16_d16_hi v148, v148 offset:0\n\t" \
  "v_lshlrev_b32_sdwa v149, 1, v149 dst_sel:DWORD dst_unused:UNUSED_PAD src0_sel:DWORD src1_sel:WORD_1\n\t" \
  "s_waitcnt lgkmcnt(12)\n\t" \
  "v_smfmac_f32_16x16x64_bf16 v[20:23], v[212:215], v[176:183], v248\n\t" \
  "ds_read_b64_tr_b16 v[212:213], v249 offset:512\n\t" \
  "ds_read_b64_tr_b16 v[214:215], v249 offset:768\n\t" \
  "ds_read_u16_d16_hi v149, v149 offset:0\n\t" \
  "s_waitcnt lgkmcnt(12)\n\t" \
  "v_mfma_f32_16x16x32_bf16 v[176:179], v[216:219], v[96:99], 0\n\t" \
  "v_lshlrev_b32_sdwa v150, 1, v150 dst_sel:DWORD dst_unused:UNUSED_PAD src0_sel:DWORD src1_sel:WORD_1\n\t" \
  "v_lshlrev_b32_sdwa v151, 1, v151 dst_sel:DWORD dst_unused:UNUSED_PAD src0_sel:DWORD src1_sel:WORD_1\n\t" \
  "s_waitcnt lgkmcnt(11)\n\t" \
  "v_mfma_f32_16x16x32_bf16 v[184:187], v[216:219], v[108:111], 0\n\t" \
  "ds_read_b64_tr_b16 v[216:217], v249 offset:2560\n\t" \
  "ds_read_b64_tr_b16 v[218:219], v249 offset:2816\n\t" \
  "ds_read_u16_d16_hi v150, v150 offset:0\n\t" \
  "ds_read_u16_d16_hi v151, v151 offset:0\n\t" \
  "s_waitcnt lgkmcnt(10)\n\t" \
  "v_mfma_f32_16x16x32_bf16 v[176:179], v[220:223], v[100:103], v[176:179]\n\t" \
  "v_mfma_f32_16x16x32_bf16 v[184:187], v[220:223], v[112:115], v[184:187]\n\t" \
  "ds_read_b64_tr_b16 v[220:221], v249 offset:4608\n\t" \
  "ds_read_b64_tr_b16 v[222:223], v249 offset:4864\n\t" \
  "s_waitcnt lgkmcnt(10)\n\t" \
  "v_mfma_f32_16x16x32_bf16 v[176:179], v[208:211], v[104:107], v[176:179]\n\t" \
  "v_mfma_f32_16x16x32_bf16 v[184:187], v[208:211], v[116:119], v[184:187]\n\t" \
  "ds_read_b64_tr_b16 v[208:209], v250 offset:12320\n\t" \
  "ds_read_b64_tr_b16 v[210:211], v250 offset:13088\n\t" \
  "s_waitcnt lgkmcnt(9)\n\t" \
  "v_mfma_f32_16x16x32_bf16 v[180:183], v[212:215], v[96:99], 0\n\t" \
  "s_nop 2\n\t" \
  "v_lshlrev_b32_sdwa v176, 1, v176 dst_sel:DWORD dst_unused:UNUSED_PAD src0_sel:DWORD src1_sel:WORD_1\n\t" \
  "v_lshlrev_b32_sdwa v177, 1, v177 dst_sel:DWORD dst_unused:UNUSED_PAD src0_sel:DWORD src1_sel:WORD_1\n\t" \
  "v_mfma_f32_16x16x32_bf16 v[188:191], v[212:215], v[108:111], 0\n\t" \
  "ds_read_b64_tr_b16 v[212:213], v250 offset:12328\n\t" \
  "ds_read_b64_tr_b16 v[214:215], v250 offset:13096\n\t" \
  "ds_read_u16_d16_hi v176, v176 offset:0\n\t" \
  "ds_read_u16_d16_hi v177, v177 offset:0\n\t" \
  "s_waitcnt lgkmcnt(10)\n\t" \
  "v_mfma_f32_16x16x32_bf16 v[180:183], v[216:219], v[100:103], v[180:183]\n\t" \
  "v_lshlrev_b32_sdwa v178, 1, v178 dst_sel:DWORD dst_unused:UNUSED_PAD src0_sel:DWORD src1_sel:WORD_1\n\t" \
  "v_lshlrev_b32_sdwa v179, 1, v179 dst_sel:DWORD dst_unused:UNUSED_PAD src0_sel:DWORD src1_sel:WORD_1\n\t" \
  "v_mfma_f32_16x16x32_bf16 v[188:191], v[216:219], v[112:115], v[188:191]\n\t" \
  "ds_read_b64_tr_b16 v[216:217], v250 offset:12384\n\t" \
  "ds_read_b64_tr_b16 v[218:219], v250 offset:13152\n\t" \
  "ds_read_u16_d16_hi v178, v178 offset:0\n\t" \
  "ds_read_u16_d16_hi v179, v179 offset:0\n\t" \
  "s_waitcnt lgkmcnt(10)\n\t" \
  "v_mfma_f32_16x16x32_bf16 v[180:183], v[220:223], v[104:107], v[180:183]\n\t" \
  "v_lshlrev_b32_sdwa v184, 1, v184 dst_sel:DWORD dst_unused:UNUSED_PAD src0_sel:DWORD src1_sel:WORD_1\n\t" \
  "v_lshlrev_b32_sdwa v185, 1, v185 dst_sel:DWORD dst_unused:UNUSED_PAD src0_sel:DWORD src1_sel:WORD_1\n\t" \
  "v_mfma_f32_16x16x32_bf16 v[188:191], v[220:223], v[116:119], v[188:191]\n\t" \
  "ds_read_b64_tr_b16 v[220:221], v250 offset:12392\n\t" \
  "ds_read_b64_tr_b16 v[222:223], v250 offset:13160\n\t" \
  "ds_read_u16_d16_hi v184, v184 offset:0\n\t" \
  "ds_read_u16_d16_hi v185, v185 offset:0\n\t" \
  "s_waitcnt lgkmcnt(12)\n\t" \
  "v_smfmac_f32_16x16x64_bf16 v[0:3], v[208:211], v[144:151], v248\n\t" \
  "ds_read_b64_tr_b16 v[208:209], v250 offset:12448\n\t" \
  "ds_read_b64_tr_b16 v[210:211], v250 offset:13216\n\t" \
  "s_waitcnt lgkmcnt(12)\n\t" \
  "v_smfmac_f32_16x16x64_bf16 v[4:7], v[212:215], v[144:151], v248\n\t" \
  "ds_read_b64_tr_b16 v[212:213], v250 offset:12456\n\t" \
  "ds_read_b64_tr_b16 v[214:215], v250 offset:13224\n\t" \
  "ds_read_b64 v[230:231], v252 offset:0\n\t" \
  "ds_read_b64 v[232:233], v252 offset:8\n\t" \
  "ds_read_b64 v[234:235], v252 offset:64\n\t" \
  "v_lshlrev_b32_sdwa v186, 1, v186 dst_sel:DWORD dst_unused:UNUSED_PAD src0_sel:DWORD src1_sel:WORD_1\n\t" \
  "s_waitcnt lgkmcnt(13)\n\t" \
  "v_smfmac_f32_16x16x64_bf16 v[8:11], v[216:219], v[144:151], v248\n\t" \
  "s_barrier\n\t" \
  "ds_read_b64_tr_b16 v[216:217], v249 offset:6144\n\t" \
  "ds_read_b64_tr_b16 v[218:219], v249 offset:6400\n\t" \
  "ds_read_b64 v[236:237], v252 offset:72\n\t" \
  "ds_read_b64 v[242:243], v252 offset:128\n\t" \
  "ds_read_b64 v[244:245], v252 offset:136\n\t" \
  "ds_read_u16_d16_hi v186, v186 offset:0\n\t" \
  "v_lshlrev_b32_sdwa v187, 1, v187 dst_sel:DWORD dst_unused:UNUSED_PAD src0_sel:DWORD src1_sel:WORD_1\n\t" \
  "s_waitcnt lgkmcnt(14)\n\t" \
  "v_smfmac_f32_16x16x64_bf16 v[12:15], v[220:223], v[144:151], v248\n\t" \
  "ds_read_b64_tr_b16 v[220:221], v249 offset:8192\n\t" \
  "ds_read_b64_tr_b16 v[222:223], v249 offset:8448\n\t" \
  "ds_read_u16_d16_hi v187, v187 offset:0\n\t" \
  "v_lshlrev_b32_sdwa v180, 1, v180 dst_sel:DWORD dst_unused:UNUSED_PAD src0_sel:DWORD src1_sel:WORD_1\n\t" \
  "s_waitcnt lgkmcnt(14)\n\t" \
  "v_smfmac_f32_16x16x64_bf16 v[16:19], v[208:211], v[144:151], v248\n\t" \
  "ds_read_b64_tr_b16 v[208:209], v249 offset:10240\n\t" \
  "ds_read_b64_tr_b16 v[210:211], v249 offset:10496\n\t" \
  "ds_read_u16_d16_hi v180, v180 offset:0\n\t" \
  "v_lshlrev_b32_sdwa v181, 1, v181 dst_sel:DWORD dst_unused:UNUSED_PAD src0_sel:DWORD src1_sel:WORD_1\n\t" \
  "v_lshlrev_b32_sdwa v182, 1, v182 dst_sel:DWORD dst_unused:UNUSED_PAD src0_sel:DWORD src1_sel:WORD_1\n\t" \
  "s_waitcnt lgkmcnt(14)\n\t" \
  "v_smfmac_f32_16x16x64_bf16 v[20:23], v[212:215], v[144:151], v248\n\t" \
  "ds_read_b64_tr_b16 v[212:213], v249 offset:6656\n\t" \
  "ds_read_b64_tr_b16 v[214:215], v249 offset:6912\n\t" \
  "ds_read_u16_d16_hi v181, v181 offset:0\n\t" \
  "ds_read_u16_d16_hi v182, v182 offset:0\n\t" \
  "v_mfma_f32_16x16x16_bf16 v[24:27], v[246:247], v[230:231], 0\n\t" \
  "v_lshlrev_b32_sdwa v183, 1, v183 dst_sel:DWORD dst_unused:UNUSED_PAD src0_sel:DWORD src1_sel:WORD_1\n\t" \
  "v_lshlrev_b32_sdwa v188, 1, v188 dst_sel:DWORD dst_unused:UNUSED_PAD src0_sel:DWORD src1_sel:WORD_1\n\t" \
  "s_waitcnt lgkmcnt(14)\n\t" \
  "v_mfma_f32_16x16x32_bf16 v[144:147], v[216:219], v[96:99], 0\n\t" \
  "ds_read_u16_d16_hi v183, v183 offset:0\n\t" \
  "ds_read_u16_d16_hi v188, v188 offset:0\n\t" \
  "v_mfma_f32_16x16x16_bf16 v[28:31], v[246:247], v[232:233], 0\n\t" \
  "v_lshlrev_b32_sdwa v189, 1, v189 dst_sel:DWORD dst_unused:UNUSED_PAD src0_sel:DWORD src1_sel:WORD_1\n\t" \
  "v_lshlrev_b32_sdwa v190, 1, v190 dst_sel:DWORD dst_unused:UNUSED_PAD src0_sel:DWORD src1_sel:WORD_1\n\t" \
  "v_mfma_f32_16x16x32_bf16 v[152:155], v[216:219], v[108:111], 0\n\t" \
  "ds_read_b64_tr_b16 v[216:217], v249 offset:8704\n\t" \
  "ds_read_b64_tr_b16 v[218:219], v249 offset:8960\n\t" \
  "ds_read_u16_d16_hi v189, v189 offset:0\n\t" \
  "ds_read_u16_d16_hi v190, v190 offset:0\n\t" \
  "s_waitcnt lgkmcnt(14)\n\t" \
  "v_mfma_f32_16x16x32_bf16 v[144:147], v[220:223], v[100:103], v[144:147]\n\t" \
  "v_lshlrev_b32_sdwa v191, 1, v191 dst_sel:DWORD dst_unused:UNUSED_PAD src0_sel:DWORD src1_sel:WORD_1\n\t" \
  "v_mfma_f32_16x16x32_bf16 v[152:155], v[220:223], v[112:115], v[152:155]\n\t" \
  "ds_read_b64_tr_b16 v[220:221], v249 offset:10752\n\t" \
  "ds_read_b64_tr_b16 v[222:223], v249 offset:11008\n\t" \
  "ds_read_u16_d16_hi v191, v191 offset:0\n\t" \
  "v_mfma_f32_16x16x16_bf16 v[32:35], v[246:247], v[234:235], 0\n\t" \
  "s_waitcnt lgkmcnt(14)\n\t" \
  "v_mfma_f32_16x16x32_bf16 v[144:147], v[208:211], v[104:107], v[144:147]\n\t" \
  "v_mfma_f32_16x16x16_bf16 v[36:39], v[246:247], v[236:237], 0\n\t" \
  "v_mfma_f32_16x16x32_bf16 v[152:155], v[208:211], v[116:119], v[152:155]\n\t" \
  "ds_read_b64_tr_b16 v[208:209], v250 offset:18480\n\t" \
  "ds_read_b64_tr_b16 v[210:211], v250 offset:19248\n\t" \
  "s_waitcnt lgkmcnt(13)\n\t" \
  "v_mfma_f32_16x16x32_bf16 v[148:151], v[212:215], v[96:99], 0\n\t" \
  "s_nop 1\n\t" \
  "v_lshlrev_b32_sdwa v144, 1, v144 dst_sel:DWORD dst_unused:UNUSED_PAD src0_sel:DWORD src1_sel:WORD_1\n\t" \
  "v_lshlrev_b32_sdwa v145, 1, v145 dst_sel:DWORD dst_unused:UNUSED_PAD src0_sel:DWORD src1_sel:WORD_1\n\t" \
  "v_mfma_f32_16x16x32_bf16 v[156:159], v[212:215], v[108:111], 0\n\t" \
  "ds_read_b64_tr_b16 v[212:213], v250 offset:18488\n\t" \
  "ds_read_b64_tr_b16 v[214:215], v250 offset:19256\n\t" \
  "ds_read_u16_d16_hi v144, v144 offset:0\n\t" \
  "ds_read_u16_d16_hi v145, v145 offset:0\n\t" \
  "v_mfma_f32_16x16x16_bf16 v[40:43], v[246:247], v[242:243], 0\n\t" \
  "v_lshlrev_b32_sdwa v146, 1, v146 dst_sel:DWORD dst_unused:UNUSED_PAD src0_sel:DWORD src1_sel:WORD_1\n\t" \
  "v_lshlrev_b32_sdwa v147, 1, v147 dst_sel:DWORD dst_unused:UNUSED_PAD src0_sel:DWORD src1_sel:WORD_1\n\t" \
  "s_waitcnt lgkmcnt(11)\n\t" \
  "v_mfma_f32_16x16x32_bf16 v[148:151], v[216:219], v[100:103], v[148:151]\n\t" \
  "ds_read_u16_d16_hi v146, v146 offset:0\n\t" \
  "ds_read_u16_d16_hi v147, v147 offset:0\n\t" \
  "v_mfma_f32_16x16x16_bf16 v[44:47], v[246:247], v[244:245], 0\n\t" \
  "v_lshlrev_b32_sdwa v152, 1, v152 dst_sel:DWORD dst_unused:UNUSED_PAD src0_sel:DWORD src1_sel:WORD_1\n\t" \
  "v_lshlrev_b32_sdwa v153, 1, v153 dst_sel:DWORD dst_unused:UNUSED_PAD src0_sel:DWORD src1_sel:WORD_1\n\t" \
  "v_mfma_f32_16x16x32_bf16 v[156:159], v[216:219], v[112:115], v[156:159]\n\t" \
  "ds_read_b64_tr_b16 v[216:217], v250 offset:18544\n\t" \
  "ds_read_b64_tr_b16 v[218:219], v250 offset:19312\n\t" \
  "ds_read_u16_d16_hi v152, v152 offset:0\n\t" \
  "ds_read_u16_d16_hi v153, v153 offset:0\n\t" \
  "s_waitcnt lgkmcnt(13)\n\t" \
  "v_mfma_f32_16x16x32_bf16 v[148:151], v[220:223], v[104:107], v[148:151]\n\t" \
  "v_lshlrev_b32_sdwa v154, 1, v154 dst_sel:DWORD dst_unused:UNUSED_PAD src0_sel:DWORD src1_sel:WORD_1\n\t" \
  "v_lshlrev_b32_sdwa v155, 1, v155 dst_sel:DWORD dst_unused:UNUSED_PAD src0_sel:DWORD src1_sel:WORD_1\n\t" \
  "v_mfma_f32_16x16x32_bf16 v[156:159], v[220:223], v[116:119], v[156:159]\n\t" \
  "ds_read_b64_tr_b16 v[220:221], v250 offset:18552\n\t" \
  "ds_read_b64_tr_b16 v[222:223], v250 offset:19320\n\t" \
  "ds_read_u16_d16_hi v154, v154 offset:0\n\t" \
  "ds_read_u16_d16_hi v155, v155 offset:0\n\t" \
  "s_waitcnt lgkmcnt(14)\n\t" \
  "v_smfmac_f32_16x16x64_bf16 v[0:3], v[208:211], v[176:183], v248\n\t" \
  "v_lshlrev_b32_sdwa v148, 1, v148 dst_sel:DWORD dst_unused:UNUSED_PAD src0_sel:DWORD src1_sel:WORD_1\n\t" \
  "v_lshlrev_b32_sdwa v149, 1, v149 dst_sel:DWORD dst_unused:UNUSED_PAD src0_sel:DWORD src1_sel:WORD_1\n\t" \
  "v_smfmac_f32_16x16x64_bf16 v[24:27], v[208:211], v[184:191], v248\n\t" \
  "ds_read_b64_tr_b16 v[208:209], v250 offset:18608\n\t" \
  "ds_read_b64_tr_b16 v[210:211], v250 offset:19376\n\t" \
  "ds_read_u16_d16_hi v148, v148 offset:0\n\t" \
  "ds_read_u16_d16_hi v149, v149 offset:0\n\t" \
  "s_waitcnt lgkmcnt(14)\n\t" \
  "v_smfmac_f32_16x16x64_bf16 v[4:7], v[212:215], v[176:183], v248\n\t" \
  "v_lshlrev_b32_sdwa v150, 1, v150 dst_sel:DWORD dst_unused:UNUSED_PAD src0_sel:DWORD src1_sel:WORD_1\n\t" \
  "v_lshlrev_b32_sdwa v151, 1, v151 dst_sel:DWORD dst_unused:UNUSED_PAD src0_sel:DWORD src1_sel:WORD_1\n\t" \
  "v_smfmac_f32_16x16x64_bf16 v[28:31], v[212:215], v[184:191], v248\n\t" \
  "ds_read_b64_tr_b16 v[212:213], v250 offset:18616\n\t" \
  "ds_read_b64_tr_b16 v[214:215], v250 offset:19384\n\t" \
  "ds_read_u16_d16_hi v150, v150 offset:0\n\t" \
  "ds_read_u16_d16_hi v151, v151 offset:0\n\t" \
  "s_waitcnt lgkmcnt(14)\n\t" \
  "v_smfmac_f32_16x16x64_bf16 v[8:11], v[216:219], v[176:183], v248\n\t" \
  "v_lshlrev_b32_sdwa v156, 1, v156 dst_sel:DWORD dst_unused:UNUSED_PAD src0_sel:DWORD src1_sel:WORD_1\n\t" \
  "v_lshlrev_b32_sdwa v157, 1, v157 dst_sel:DWORD dst_unused:UNUSED_PAD src0_sel:DWORD src1_sel:WORD_1\n\t" \
  "v_smfmac_f32_16x16x64_bf16 v[32:35], v[216:219], v[184:191], v248\n\t" \
  "s_barrier\n\t" \
  "ds_read_b64_tr_b16 v[216:217], v249 offset:12288\n\t" \
  "ds_read_b64_tr_b16 v[218:219], v249 offset:12544\n\t" \
  "ds_read_u16_d16_hi v156, v156 offset:0\n\t" \
  "ds_read_u16_d16_hi v157, v157 offset:0\n\t" \
  "s_waitcnt lgkmcnt(14)\n\t" \
  "v_smfmac_f32_16x16x64_bf16 v[12:15], v[220:223], v[176:183], v248\n\t" \
  "v_lshlrev_b32_sdwa v158, 1, v158 dst_sel:DWORD dst_unused:UNUSED_PAD src0_sel:DWORD src1_sel:WORD_1\n\t" \
  "v_lshlrev_b32_sdwa v159, 1, v159 dst_sel:DWORD dst_unused:UNUSED_PAD src0_sel:DWORD src1_sel:WORD_1\n\t" \
  "v_smfmac_f32_16x16x64_bf16 v[36:39], v[220:223], v[184:191], v248\n\t" \
  "ds_read_b64_tr_b16 v[220:221], v249 offset:14336\n\t" \
  "ds_read_b64_tr_b16 v[222:223], v249 offset:14592\n\t" \
  "ds_read_u16_d16_hi v158, v158 offset:0\n\t" \
  "ds_read_u16_d16_hi v159, v159 offset:0\n\t" \
  "s_waitcnt lgkmcnt(14)\n\t" \
  "v_smfmac_f32_16x16x64_bf16 v[16:19], v[208:211], v[176:183], v248\n\t" \
  "s_add_u32 %[rs], %[rs], 1\n\t" \
  "v_smfmac_f32_16x16x64_bf16 v[40:43], v[208:211], v[184:191], v248\n\t" \
  "s_cmp_eq_u32 %[rs], 6\n\t" \
  "ds_read_b64_tr_b16 v[208:209], v249 offset:16384\n\t" \
  "ds_read_b64_tr_b16 v[210:211], v249 offset:16640\n\t" \
  "s_waitcnt lgkmcnt(12)\n\t" \
  "v_smfmac_f32_16x16x64_bf16 v[20:23], v[212:215], v[176:183], v248\n\t" \
  "s_cselect_b32 %[rs], 0, %[rs]\n\t" \
  "v_smfmac_f32_16x16x64_bf16 v[44:47], v[212:215], v[184:191], v248\n\t" \
  "s_cselect_b32 %[t0], -75920, %[ps]\n\t" \
  "ds_read_b64_tr_b16 v[212:213], v249 offset:12800\n\t" \
  "ds_read_b64_tr_b16 v[214:215], v249 offset:13056\n\t" \
  "v_add_u32 v252, %[t0], v252\n\t" \
  "s_waitcnt lgkmcnt(10)\n\t" \
  "v_mfma_f32_16x16x32_bf16 v[176:179], v[216:219], v[96:99], 0\n\t" \
  "v_mfma_f32_16x16x32_bf16 v[184:187], v[216:219], v[108:111], 0\n\t" \
  "ds_read_b64_tr_b16 v[216:217], v249 offset:14848\n\t" \
  "ds_read_b64_tr_b16 v[218:219], v249 offset:15104\n\t" \
  "s_waitcnt lgkmcnt(8)\n\t" \
  "v_mfma_f32_16x16x32_bf16 v[176:179], v[220:223], v[100:103], v[176:179]\n\t" \
  "v_mfma_f32_16x16x32_bf16 v[184:187], v[220:223], v[112:115], v[184:187]\n\t" \
  "ds_read_b64_tr_b16 v[220:221], v249 offset:16896\n\t" \
  "ds_read_b64_tr_b16 v[222:223], v249 offset:17152\n\t" \
  "s_waitcnt lgkmcnt(6)\n\t" \
  "v_mfma_f32_16x16x32_bf16 v[176:179], v[208:211], v[104:107], v[176:179]\n\t" \
  "v_mfma_f32_16x16x32_bf16 v[184:187], v[208:211], v[116:119], v[184:187]\n\t" \
  "ds_read_b64_tr_b16 v[208:209], v250 offset:0\n\t" \
  "ds_read_b64_tr_b16 v[210:211], v250 offset:768\n\t" \
  "s_waitcnt lgkmcnt(6)\n\t" \
  "v_mfma_f32_16x16x32_bf16 v[180:183], v[212:215], v[96:99], 0\n\t" \
  "s_nop 2\n\t" \
  "v_lshlrev_b32_sdwa v176, 1, v176 dst_sel:DWORD dst_unused:UNUSED_PAD src0_sel:DWORD src1_sel:WORD_1\n\t" \
  "v_lshlrev_b32_sdwa v177, 1, v177 dst_sel:DWORD dst_unused:UNUSED_PAD src0_sel:DWORD src1_sel:WORD_1\n\t" \
  "v_mfma_f32_16x16x32_bf16 v[188:191], v[212:215], v[108:111], 0\n\t" \
  "ds_read_b64_tr_b16 v[212:213], v250 offset:8\n\t" \
  "ds_read_b64_tr_b16 v[214:215], v250 offset:776\n\t" \
  "ds_read_u16_d16_hi v176, v176 offset:0\n\t" \
  "ds_read_u16_d16_hi v177, v177 offset:0\n\t" \
  "s_waitcnt lgkmcnt(8)\n\t" \
  "v_mfma_f32_16x16x32_bf16 v[180:183], v[216:219], v[100:103], v[180:183]\n\t" \
  "v_lshlrev_b32_sdwa v178, 1, v178 dst_sel:DWORD dst_unused:UNUSED_PAD src0_sel:DWORD src1_sel:WORD_1\n\t" \
  "v_lshlrev_b32_sdwa v179, 1, v179 dst_sel:DWORD dst_unused:UNUSED_PAD src0_sel:DWORD src1_sel:WORD_1\n\t" \
  "v_mfma_f32_16x16x32_bf16 v[188:191], v[216:219], v[112:115], v[188:191]\n\t" \
  "ds_read_b64_tr_b16 v[216:217], v250 offset:64\n\t" \
  "ds_read_b64_tr_b16 v[218:219], v250 offset:832\n\t" \
  "ds_read_u16_d16_hi v178, v178 offset:0\n\t" \
  "ds_read_u16_d16_hi v179, v179 offset:0\n\t" \
  "s_waitcnt lgkmcnt(10)\n\t" \
  "v_mfma_f32_16x16x32_bf16 v[180:183], v[220:223], v[104:107], v[180:183]\n\t" \
  "v_lshlrev_b32_sdwa v184, 1, v184 dst_sel:DWORD dst_unused:UNUSED_PAD src0_sel:DWORD src1_sel:WORD_1\n\t" \
  "v_lshlrev_b32_sdwa v185, 1, v185 dst_sel:DWORD dst_unused:UNUSED_PAD src0_sel:DWORD src1_sel:WORD_1\n\t" \
  "v_mfma_f32_16x16x32_bf16 v[188:191], v[220:223], v[116:119], v[188:191]\n\t" \
  "ds_read_b64_tr_b16 v[220:221], v250 offset:72\n\t" \
  "ds_read_b64_tr_b16 v[222:223], v250 offset:840\n\t" \
  "ds_read_u16_d16_hi v184, v184 offset:0\n\t" \
  "ds_read_u16_d16_hi v185, v185 offset:0\n\t" \
  "s_waitcnt lgkmcnt(12)\n\t" \
  "v_smfmac_f32_16x16x64_bf16 v[0:3], v[208:211], v[144:151], v248\n\t" \
  "v_lshlrev_b32_sdwa v186, 1, v186 dst_sel:DWORD dst_unused:UNUSED_PAD src0_sel:DWORD src1_sel:WORD_1\n\t" \
  "v_lshlrev_b32_sdwa v187, 1, v187 dst_sel:DWORD dst_unused:UNUSED_PAD src0_sel:DWORD src1_sel:WORD_1\n\t" \
  "v_smfmac_f32_16x16x64_bf16 v[24:27], v[208:211], v[152:159], v248\n\t" \
  "ds_read_b64_tr_b16 v[208:209], v250 offset:128\n\t" \
  "ds_read_b64_tr_b16 v[210:211], v250 offset:896\n\t" \
  "ds_read_u16_d16_hi v186, v186 offset:0\n\t" \
  "ds_read_u16_d16_hi v187, v187 offset:0\n\t" \
  "s_waitcnt lgkmcnt(14)\n\t" \
  "v_smfmac_f32_16x16x64_bf16 v[4:7], v[212:215], v[144:151], v248\n\t" \
  "v_lshlrev_b32_sdwa v180, 1, v180 dst_sel:DWORD dst_unused:UNUSED_PAD src0_sel:DWORD src1_sel:WORD_1\n\t" \
  "v_lshlrev_b32_sdwa v181, 1, v181 dst_sel:DWORD dst_unused:UNUSED_PAD src0_sel:DWORD src1_sel:WORD_1\n\t" \
  "v_smfmac_f32_16x16x64_bf16 v[28:31], v[212:215], v[152:159], v248\n\t" \
  "ds_read_b64_tr_b16 v[212:213], v250 offset:136\n\t" \
  "ds_read_b64_tr_b16 v[214:215], v250 offset:904\n\t" \
  "ds_read_u16_d16_hi v180, v180 offset:0\n\t" \
  "ds_read_u16_d16_hi v181, v181 offset:0\n\t" \
  "s_waitcnt lgkmcnt(14)\n\t" \
  "v_smfmac_f32_16x16x64_bf16 v[8:11], v[216:219], v[144:151], v248\n\t" \
  "v_lshlrev_b32_sdwa v182, 1, v182 dst_sel:DWORD dst_unused:UNUSED_PAD src0_sel:DWORD src1_sel:WORD_1\n\t" \
  "v_lshlrev_b32_sdwa v183, 1, v183 dst_sel:DWORD dst_unused:UNUSED_PAD src0_sel:DWORD src1_sel:WORD_1\n\t" \
  "v_smfmac_f32_16x16x64_bf16 v[32:35], v[216:219], v[152:159], v248\n\t" \
  "s_barrier\n\t" \
  "ds_read_b64_tr_b16 v[216:217], v249 offset:0\n\t" \
  "ds_read_b64_tr_b16 v[218:219], v249 offset:256\n\t" \
  "ds_read_b128 v[120:123], v251 offset:0\n\t" \
  "ds_read_b128 v[124:127], v251 offset:4096\n\t" \
  "ds_read_b128 v[128:131], v251 offset:8192\n\t" \
  "ds_read_u16_d16_hi v182, v182 offset:0\n\t" \
  "ds_read_u16_d16_hi v183, v183 offset:0\n\t" \
  "s_waitcnt lgkmcnt(14)\n\t" \
  "v_smfmac_f32_16x16x64_bf16 v[12:15], v[220:223], v[144:151], v248\n\t" \
  "v_lshlrev_b32_sdwa v188, 1, v188 dst_sel:DWORD dst_unused:UNUSED_PAD src0_sel:DWORD src1_sel:WORD_1\n\t" \
  "v_lshlrev_b32_sdwa v189, 1, v189 dst_sel:DWORD dst_unused:UNUSED_PAD src0_sel:DWORD src1_sel:WORD_1\n\t" \
  "v_smfmac_f32_16x16x64_bf16 v[36:39], v[220:223], v[152:159], v248\n\t" \
  "ds_read_b64_tr_b16 v[220:221], v249 offset:2048\n\t" \
  "ds_read_b64_tr_b16 v[222:223], v249 offset:2304\n\t" \
  "ds_read_u16_d16_hi v188, v188 offset:0\n\t" \
  "ds_read_u16_d16_hi v189, v189 offset:0\n\t" \
  "s_waitcnt lgkmcnt(14)\n\t" \
  "v_smfmac_f32_16x16x64_bf16 v[16:19], v[208:211], v[144:151], v248\n\t" \
  "v_lshlrev_b32_sdwa v190, 1, v190 dst_sel:DWORD dst_unused:UNUSED_PAD src0_sel:DWORD src1_sel:WORD_1\n\t" \
  "v_lshlrev_b32_sdwa v191, 1, v191 dst_sel:DWORD dst_unused:UNUSED_PAD src0_sel:DWORD src1_sel:WORD_1\n\t" \
  "v_smfmac_f32_16x16x64_bf16 v[40:43], v[208:211], v[152:159], v248\n\t" \
  "ds_read_b64_tr_b16 v[208:209], v249 offset:4096\n\t" \
  "ds_read_b64_tr_b16 v[210:211], v249 offset:4352\n\t" \
  "ds_read_u16_d16_hi v190, v190 offset:0\n\t" \
  "ds_read_u16_d16_hi v191, v191 offset:0\n\t" \
  "s_waitcnt lgkmcnt(14)\n\t" \
  "v_smfmac_f32_16x16x64_bf16 v[20:23], v[212:215], v[144:151], v248\n\t" \
  "v_smfmac_f32_16x16x64_bf16 v[44:47], v[212:215], v[152:159], v248\n\t" \
  "ds_read_b64_tr_b16 v[212:213], v249 offset:512\n\t" \
  "ds_read_b64_tr_b16 v[214:215], v249 offset:768\n\t" \
  "s_waitcnt lgkmcnt(14)\n\t" \
  "v_mfma_f32_16x16x32_bf16 v[144:147], v[216:219], v[96:99], 0\n\t" \
  "v_mfma_f32_16x16x32_bf16 v[152:155], v[216:219], v[108:111], 0\n\t" \
  "v_mfma_f32_16x16x32_bf16 v[160:163], v[216:219], v[120:123], 0\n\t" \
  "s_waitcnt lgkmcnt(8)\n\t" \
  "ds_read_b64_tr_b16 v[216:217], v249 offset:2560\n\t" \
  "v_mfma_f32_16x16x32_bf16 v[144:147], v[220:223], v[100:103], v[144:147]\n\t" \
  "ds_read_b64_tr_b16 v[218:219], v249 offset:2816\n\t" \
  "v_mfma_f32_16x16x32_bf16 v[152:155], v[220:223], v[112:115], v[152:155]\n\t" \
  "v_mfma_f32_16x16x32_bf16 v[160:163], v[220:223], v[124:127], v[160:163]\n\t" \
  "s_waitcnt lgkmcnt(6)\n\t" \
  "ds_read_b64_tr_b16 v[220:221], v249 offset:4608\n\t" \
  "v_mfma_f32_16x16x32_bf16 v[144:147], v[208:211], v[104:107], v[144:147]\n\t" \
  "ds_read_b64_tr_b16 v[222:223], v249 offset:4864\n\t" \
  "v_mfma_f32_16x16x32_bf16 v[152:155], v[208:211], v[116:119], v[152:155]\n\t" \
  "v_mfma_f32_16x16x32_bf16 v[160:163], v[208:211], v[128:131], v[160:163]\n\t" \
  "s_waitcnt lgkmcnt(4)\n\t" \
  "ds_read_b64_tr_b16 v[208:209], v250 offset:6160\n\t" \
  "s_nop 2\n\t" \
  "v_lshlrev_b32_sdwa v144, 1, v144 dst_sel:DWORD dst_unused:UNUSED_PAD src0_sel:DWORD src1_sel:WORD_1\n\t" \
  "v_mfma_f32_16x16x32_bf16 v[148:151], v[212:215], v[96:99], 0\n\t" \
  "ds_read_u16_d16_hi v144, v144 offset:0\n\t" \
  "v_lshlrev_b32_sdwa v145, 1, v145 dst_sel:DWORD dst_unused:UNUSED_PAD src0_sel:DWORD src1_sel:WORD_1\n\t" \
  "v_mfma_f32_16x16x32_bf16 v[156:159], v[212:215], v[108:111], 0\n\t" \
  "ds_read_b64_tr_b16 v[210:211], v250 offset:6928\n\t" \
  "v_lshlrev_b32_sdwa v146, 1, v146 dst_sel:DWORD dst_unused:UNUSED_PAD src0_sel:DWORD src1_sel:WORD_1\n\t" \
  "v_mfma_f32_16x16x32_bf16 v[164:167], v[212:215], v[120:123], 0\n\t" \
  "s_waitcnt lgkmcnt(5)\n\t" \
  "ds_read_u16_d16_hi v145, v145 offset:0\n\t" \
  "v_lshlrev_b32_sdwa v147, 1, v147 dst_sel:DWORD dst_unused:UNUSED_PAD src0_sel:DWORD src1_sel:WORD_1\n\t" \
  "v_mfma_f32_16x16x32_bf16 v[148:151], v[216:219], v[100:103], v[148:151]\n\t" \
  "ds_read_b64_tr_b16 v[212:213], v250 offset:6168\n\t" \
  "v_lshlrev_b32_sdwa v152, 1, v152 dst_sel:DWORD dst_unused:UNUSED_PAD src0_sel:DWORD src1_sel:WORD_1\n\t" \
  "v_mfma_f32_16x16x32_bf16 v[156:159], v[216:219], v[112:115], v[156:159]\n\t" \
  "ds_read_u16_d16_hi v146, v146 offset:0\n\t" \
  "v_lshlrev_b32_sdwa v153, 1, v153 dst_sel:DWORD dst_unused:UNUSED_PAD src0_sel:DWORD src1_sel:WORD_1\n\t" \
  "v_mfma_f32_16x16x32_bf16 v[164:167], v[216:219], v[124:127], v[164:167]\n\t" \
  "s_waitcnt lgkmcnt(6)\n\t" \
  "ds_read_b64_tr_b16 v[214:215], v250 offset:6936\n\t" \
  "ds_read_u16_d16_hi v147, v147 offset:0\n\t" \
  "v_mfma_f32_16x16x32_bf16 v[148:151], v[220:223], v[104:107], v[148:151]\n\t" \
  "ds_read_b64_tr_b16 v[216:217], v250 offset:6224\n\t" \
  "v_lshlrev_b32_sdwa v154, 1, v154 dst_sel:DWORD dst_unused:UNUSED_PAD src0_sel:DWORD src1_sel:WORD_1\n\t" \
  "v_mfma_f32_16x16x32_bf16 v[156:159], v[220:223], v[116:119], v[156:159]\n\t" \
  "ds_read_u16_d16_hi v152, v152 offset:0\n\t" \
  "v_lshlrev_b32_sdwa v155, 1, v155 dst_sel:DWORD dst_unused:UNUSED_PAD src0_sel:DWORD src1_sel:WORD_1\n\t" \
  "v_mfma_f32_16x16x32_bf16 v[164:167], v[220:223], v[128:131], v[164:167]\n\t" \
  "s_waitcnt lgkmcnt(7)\n\t" \
  "ds_read_b64_tr_b16 v[218:219], v250 offset:6992\n\t" \
  "ds_read_u16_d16_hi v153, v153 offset:0\n\t" \
  "v_smfmac_f32_16x16x64_bf16 v[0:3], v[208:211], v[176:183], v248\n\t" \
  "ds_read_b64_tr_b16 v[220:221], v250 offset:6232\n\t" \
  "ds_read_b64_tr_b16 v[222:223], v250 offset:7000\n\t" \
  "ds_read_u16_d16_hi v154, v154 offset:0\n\t" \
  "ds_read_u16_d16_hi v155, v155 offset:0\n\t" \
  "v_smfmac_f32_16x16x64_bf16 v[24:27], v[208:211], v[184:191], v248\n\t" \
  "ds_read_b64_tr_b16 v[208:209], v250 offset:6288\n\t" \
  "ds_read_b64_tr_b16 v[210:211], v250 offset:7056\n\t" \
  "s_waitcnt lgkmcnt(11)\n\t" \
  "v_smfmac_f32_16x16x64_bf16 v[4:7], v[212:215], v[176:183], v248\n\t" \
  "ds_read_b64 v[230:231], v252 offset:0\n\t" \
  "ds_read_b64 v[232:233], v252 offset:8\n\t" \
  "ds_read_b64 v[234:235], v252 offset:64\n\t" \
  "v_smfmac_f32_16x16x64_bf16 v[28:31], v[212:215], v[184:191], v248\n\t" \
  "ds_read_b64_tr_b16 v[212:213], v250 offset:6296\n\t" \
  "ds_read_b64_tr_b16 v[214:215], v250 offset:7064\n\t" \
  "ds_read_b64 v[236:237], v252 offset:72\n\t" \
  "v_lshlrev_b32_sdwa v160, 1, v160 dst_sel:DWORD dst_unused:UNUSED_PAD src0_sel:DWORD src1_sel:WORD_1\n\t" \
  "v_lshlrev_b32_sdwa v161, 1, v161 dst_sel:DWORD dst_unused:UNUSED_PAD src0_sel:DWORD src1_sel:WORD_1\n\t" \
  "s_waitcnt lgkmcnt(13)\n\t" \
  "v_smfmac_f32_16x16x64_bf16 v[8:11], v[216:219], v[176:183], v248\n\t" \
  "ds_read_b64 v[242:243], v252 offset:128\n\t" \
  "ds_read_b64 v[244:245], v252 offset:136\n\t" \
  "ds_read_u16_d16_hi v160, v160 offset:0\n\t" \
  "ds_read_u16_d16_hi v161, v161 offset:0\n\t" \
  "v_smfmac_f32_16x16x64_bf16 v[32:35], v[216:219], v[184:191], v248\n\t" \
  "s_barrier\n\t" \
  "ds_read_b64_tr_b16 v[216:217], v249 offset:6144\n\t" \
  "ds_read_b64_tr_b16 v[218:219], v249 offset:6400\n\t" \
  "v_lshlrev_b32_sdwa v162, 1, v162 dst_sel:DWORD dst_unused:UNUSED_PAD src0_sel:DWORD src1_sel:WORD_1\n\t" \
  "v_lshlrev_b32_sdwa v163, 1, v163 dst_sel:DWORD dst_unused:UNUSED_PAD src0_sel:DWORD src1_sel:WORD_1\n\t" \
  "s_waitcnt lgkmcnt(14)\n\t" \
  "v_smfmac_f32_16x16x64_bf16 v[12:15], v[220:223], v[176:183], v248\n\t" \
  "ds_read_u16_d16_hi v162, v162 offset:0\n\t" \
  "ds_read_u16_d16_hi v163, v163 offset:0\n\t" \
  "v_smfmac_f32_16x16x64_bf16 v[36:39], v[220:223], v[184:191], v248\n\t" \
  "ds_read_b64_tr_b16 v[220:221], v249 offset:8192\n\t" \
  "ds_read_b64_tr_b16 v[222:223], v249 offset:8448\n\t" \
  "v_lshlrev_b32_sdwa v148, 1, v148 dst_sel:DWORD dst_unused:UNUSED_PAD src0_sel:DWORD src1_sel:WORD_1\n\t" \
  "v_lshlrev_b32_sdwa v149, 1, v149 dst_sel:DWORD dst_unused:UNUSED_PAD src0_sel:DWORD src1_sel:WORD_1\n\t" \
  "s_waitcnt lgkmcnt(14)\n\t" \
  "v_smfmac_f32_16x16x64_bf16 v[16:19], v[208:211], v[176:183], v248\n\t" \
  "ds_read_u16_d16_hi v148, v148 offset:0\n\t" \
  "ds_read_u16_d16_hi v149, v149 offset:0\n\t" \
  "v_smfmac_f32_16x16x64_bf16 v[40:43], v[208:211], v[184:191], v248\n\t" \
  "ds_read_b64_tr_b16 v[208:209], v249 offset:10240\n\t" \
  "ds_read_b64_tr_b16 v[210:211], v249 offset:10496\n\t" \
  "v_lshlrev_b32_sdwa v150, 1, v150 dst_sel:DWORD dst_unused:UNUSED_PAD src0_sel:DWORD src1_sel:WORD_1\n\t" \
  "v_lshlrev_b32_sdwa v151, 1, v151 dst_sel:DWORD dst_unused:UNUSED_PAD src0_sel:DWORD src1_sel:WORD_1\n\t" \
  "s_waitcnt lgkmcnt(14)\n\t" \
  "v_smfmac_f32_16x16x64_bf16 v[20:23], v[212:215], v[176:183], v248\n\t" \
  "ds_read_u16_d16_hi v150, v150 offset:0\n\t" \
  "ds_read_u16_d16_hi v151, v151 offset:0\n\t" \
  "v_smfmac_f32_16x16x64_bf16 v[44:47], v[212:215], v[184:191], v248\n\t" \
  "ds_read_b64_tr_b16 v[212:213], v249 offset:6656\n\t" \
  "ds_read_b64_tr_b16 v[214:215], v249 offset:6912\n\t" \
  "v_lshlrev_b32_sdwa v156, 1, v156 dst_sel:DWORD dst_unused:UNUSED_PAD src0_sel:DWORD src1_sel:WORD_1\n\t" \
  "v_mfma_f32_16x16x16_bf16 v[48:51], v[246:247], v[230:231], 0\n\t" \
  "ds_read_u16_d16_hi v156, v156 offset:0\n\t" \
  "v_lshlrev_b32_sdwa v157, 1, v157 dst_sel:DWORD dst_unused:UNUSED_PAD src0_sel:DWORD src1_sel:WORD_1\n\t" \
  "s_waitcnt lgkmcnt(13)\n\t" \
  "v_mfma_f32_16x16x32_bf16 v[176:179], v[216:219], v[96:99], 0\n\t" \
  "ds_read_u16_d16_hi v157, v157 offset:0\n\t" \
  "v_lshlrev_b32_sdwa v158, 1, v158 dst_sel:DWORD dst_unused:UNUSED_PAD src0_sel:DWORD src1_sel:WORD_1\n\t" \
  "v_mfma_f32_16x16x32_bf16 v[184:187], v[216:219], v[108:111], 0\n\t" \
  "ds_read_u16_d16_hi v158, v158 offset:0\n\t" \
  "v_lshlrev_b32_sdwa v159, 1, v159 dst_sel:DWORD dst_unused:UNUSED_PAD src0_sel:DWORD src1_sel:WORD_1\n\t" \
  "v_mfma_f32_16x16x16_bf16 v[52:55], v[246:247], v[232:233], 0\n\t" \
  "ds_read_u16_d16_hi v159, v159 offset:0\n\t" \
  "v_lshlrev_b32_sdwa v164, 1, v164 dst_sel:DWORD dst_unused:UNUSED_PAD src0_sel:DWORD src1_sel:WORD_1\n\t" \
  "v_mfma_f32_16x16x32_bf16 v[192:195], v[216:219], v[120:123], 0\n\t" \
  "s_waitcnt lgkmcnt(12)\n\t" \
  "ds_read_b64_tr_b16 v[216:217], v249 offset:8704\n\t" \
  "v_lshlrev_b32_sdwa v165, 1, v165 dst_sel:DWORD dst_unused:UNUSED_PAD src0_sel:DWORD src1_sel:WORD_1\n\t" \
  "v_mfma_f32_16x16x32_bf16 v[176:179], v[220:223], v[100:103], v[176:179]\n\t" \
  "ds_read_u16_d16_hi v164, v164 offset:0\n\t" \
  "v_lshlrev_b32_sdwa v166, 1, v166 dst_sel:DWORD dst_unused:UNUSED_PAD src0_sel:DWORD src1_sel:WORD_1\n\t" \
  "v_mfma_f32_16x16x32_bf16 v[184:187], v[220:223], v[112:115], v[184:187]\n\t" \
  "ds_read_b64_tr_b16 v[218:219], v249 offset:8960\n\t" \
  "v_lshlrev_b32_sdwa v167, 1, v167 dst_sel:DWORD dst_unused:UNUSED_PAD src0_sel:DWORD src1_sel:WORD_1\n\t" \
  "v_mfma_f32_16x16x32_bf16 v[192:195], v[220:223], v[124:127], v[192:195]\n\t" \
  "ds_read_u16_d16_hi v165, v165 offset:0\n\t" \
  "ds_read_b64_tr_b16 v[220:221], v249 offset:10752\n\t" \
  "v_mfma_f32_16x16x16_bf16 v[56:59], v[246:247], v[234:235], 0\n\t" \
  "s_waitcnt lgkmcnt(13)\n\t" \
  "ds_read_u16_d16_hi v166, v166 offset:0\n\t" \
  "v_mfma_f32_16x16x32_bf16 v[176:179], v[208:211], v[104:107], v[176:179]\n\t" \
  "ds_read_b64_tr_b16 v[222:223], v249 offset:11008\n\t" \
  "v_mfma_f32_16x16x32_bf16 v[184:187], v[208:211], v[116:119], v[184:187]\n\t" \
  "ds_read_u16_d16_hi v167, v167 offset:0\n\t" \
  "v_mfma_f32_16x16x16_bf16 v[60:63], v[246:247], v[236:237], 0\n\t" \
  "v_mfma_f32_16x16x32_bf16 v[192:195], v[208:211], v[128:131], v[192:195]\n\t" \
  "s_waitcnt lgkmcnt(12)\n\t" \
  "ds_read_b64_tr_b16 v[208:209], v250 offset:12320\n\t" \
  "s_nop 0\n\t" \
  "v_lshlrev_b32_sdwa v176, 1, v176 dst_sel:DWORD dst_unused:UNUSED_PAD src0_sel:DWORD src1_sel:WORD_1\n\t" \
  "v_mfma_f32_16x16x32_bf16 v[180:183], v[212:215], v[96:99], 0\n\t" \
  "ds_read_u16_d16_hi v176, v176 offset:0\n\t" \
  "v_lshlrev_b32_sdwa v177, 1, v177 dst_sel:DWORD dst_unused:UNUSED_PAD src0_sel:DWORD src1_sel:WORD_1\n\t" \
  "v_mfma_f32_16x16x32_bf16 v[188:191], v[212:215], v[108:111], 0\n\t" \
  "ds_read_b64_tr_b16 v[210:211], v250 offset:13088\n\t" \
  "v_lshlrev_b32_sdwa v178, 1, v178 dst_sel:DWORD dst_unused:UNUSED_PAD src0_sel:DWORD src1_sel:WORD_1\n\t" \
  "v_mfma_f32_16x16x32_bf16 v[196:199], v[212:215], v[120:123], 0\n\t" \
  "ds_read_u16_d16_hi v177, v177 offset:0\n\t" \
  "v_lshlrev_b32_sdwa v179, 1, v179 dst_sel:DWORD dst_unused:UNUSED_PAD src0_sel:DWORD src1_sel:WORD_1\n\t" \
  "v_mfma_f32_16x16x16_bf16 v[64:67], v[246:247], v[242:243], 0\n\t" \
  "s_waitcnt lgkmcnt(9)\n\t" \
  "ds_read_b64_tr_b16 v[212:213], v250 offset:12328\n\t" \
  "v_lshlrev_b32_sdwa v184, 1, v184 dst_sel:DWORD dst_unused:UNUSED_PAD src0_sel:DWORD src1_sel:WORD_1\n\t" \
  "v_mfma_f32_16x16x32_bf16 v[180:183], v[216:219], v[100:103], v[180:183]\n\t" \
  "ds_read_u16_d16_hi v178, v178 offset:0\n\t" \
  "v_lshlrev_b32_sdwa v185, 1, v185 dst_sel:DWORD dst_unused:UNUSED_PAD src0_sel:DWORD src1_sel:WORD_1\n\t" \
  "v_mfma_f32_16x16x32_bf16 v[188:191], v[216:219], v[112:115], v[188:191]\n\t" \
  "ds_read_b64_tr_b16 v[214:215], v250 offset:13096\n\t" \
  "v_lshlrev_b32_sdwa v186, 1, v186 dst_sel:DWORD dst_unused:UNUSED_PAD src0_sel:DWORD src1_sel:WORD_1\n\t" \
  "v_mfma_f32_16x16x16_bf16 v[68:71], v[246:247], v[244:245], 0\n\t" \
  "ds_read_u16_d16_hi v179, v179 offset:0\n\t" \
  "v_lshlrev_b32_sdwa v187, 1, v187 dst_sel:DWORD dst_unused:UNUSED_PAD src0_sel:DWORD src1_sel:WORD_1\n\t" \
  "v_mfma_f32_16x16x32_bf16 v[196:199], v[216:219], v[124:127], v[196:199]\n\t" \
  "s_waitcnt lgkmcnt(9)\n\t" \
  "ds_read_b64_tr_b16 v[216:217], v250 offset:12384\n\t" \
  "ds_read_u16_d16_hi v184, v184 offset:0\n\t" \
  "v_mfma_f32_16x16x32_bf16 v[180:183], v[220:223], v[104:107], v[180:183]\n\t" \
  "ds_read_b64_tr_b16 v[218:219], v250 offset:13152\n\t" \
  "v_lshlrev_b32_sdwa v192, 1, v192 dst_sel:DWORD dst_unused:UNUSED_PAD src0_sel:DWORD src1_sel:WORD_1\n\t" \
  "v_mfma_f32_16x16x32_bf16 v[188:191], v[220:223], v[116:119], v[188:191]\n\t" \
  "ds_read_u16_d16_hi v185, v185 offset:0\n\t" \
  "v_lshlrev_b32_sdwa v193, 1, v193 dst_sel:DWORD dst_unused:UNUSED_PAD src0_sel:DWORD src1_sel:WORD_1\n\t" \
  "v_mfma_f32_16x16x32_bf16 v[196:199], v[220:223], v[128:131], v[196:199]\n\t" \
  "s_waitcnt lgkmcnt(9)\n\t" \
  "ds_read_b64_tr_b16 v[220:221], v250 offset:12392\n\t" \
  "ds_read_u16_d16_hi v186, v186 offset:0\n\t" \
  "v_smfmac_f32_16x16x64_bf16 v[0:3], v[208:211], v[144:151], v248\n\t" \
  "ds_read_b64_tr_b16 v[222:223], v250 offset:13160\n\t" \
  "v_lshlrev_b32_sdwa v194, 1, v194 dst_sel:DWORD dst_unused:UNUSED_PAD src0_sel:DWORD src1_sel:WORD_1\n\t" \
  "v_smfmac_f32_16x16x64_bf16 v[24:27], v[208:211], v[152:159], v248\n\t" \
  "ds_read_u16_d16_hi v187, v187 offset:0\n\t" \
  "v_lshlrev_b32_sdwa v195, 1, v195 dst_sel:DWORD dst_unused:UNUSED_PAD src0_sel:DWORD src1_sel:WORD_1\n\t" \
  "v_smfmac_f32_16x16x64_bf16 v[48:51], v[208:211], v[160:167], v248\n\t" \
  "s_waitcnt lgkmcnt(9)\n\t" \
  "ds_read_b64_tr_b16 v[208:209], v250 offset:12448\n\t" \
  "ds_read_u16_d16_hi v192, v192 offset:0\n\t" \
  "v_smfmac_f32_16x16x64_bf16 v[4:7], v[212:215], v[144:151], v248\n\t" \
  "ds_read_b64_tr_b16 v[210:211], v250 offset:13216\n\t" \
  "v_lshlrev_b32_sdwa v180, 1, v180 dst_sel:DWORD dst_unused:UNUSED_PAD src0_sel:DWORD src1_sel:WORD_1\n\t" \
  "v_smfmac_f32_16x16x64_bf16 v[28:31], v[212:215], v[152:159], v248\n\t" \
  "ds_read_u16_d16_hi v193, v193 offset:0\n\t" \
  "v_lshlrev_b32_sdwa v181, 1, v181 dst_sel:DWORD dst_unused:UNUSED_PAD src0_sel:DWORD src1_sel:WORD_1\n\t" \
  "v_smfmac_f32_16x16x64_bf16 v[52:55], v[212:215], v[160:167], v248\n\t" \
  "s_waitcnt lgkmcnt(9)\n\t" \
  "ds_read_b64_tr_b16 v[212:213], v250 offset:12456\n\t" \
  "ds_read_u16_d16_hi v194, v194 offset:0\n\t" \
  "v_smfmac_f32_16x16x64_bf16 v[8:11], v[216:219], v[144:151], v248\n\t" \
  "ds_read_b64_tr_b16 v[214:215], v250 offset:13224\n\t" \
  "v_lshlrev_b32_sdwa v182, 1, v182 dst_sel:DWORD dst_unused:UNUSED_PAD src0_sel:DWORD src1_sel:WORD_1\n\t" \
  "v_smfmac_f32_16x16x64_bf16 v[32:35], v[216:219], v[152:159], v248\n\t" \
  "ds_read_u16_d16_hi v195, v195 offset:0\n\t" \
  "v_lshlrev_b32_sdwa v183, 1, v183 dst_sel:DWORD dst_unused:UNUSED_PAD src0_sel:DWORD src1_sel:WORD_1\n\t" \
  "v_smfmac_f32_16x16x64_bf16 v[56:59], v[216:219], v[160:167], v248\n\t" \
  "s_waitcnt lgkmcnt(9)\n\t" \
  "s_barrier\n\t" \
  "ds_read_b64_tr_b16 v[216:217], v249 offset:12288\n\t" \
  "ds_read_u16_d16_hi v180, v180 offset:0\n\t" \
  "v_smfmac_f32_16x16x64_bf16 v[12:15], v[220:223], v[144:151], v248\n\t" \
  "ds_read_b64_tr_b16 v[218:219], v249 offset:12544\n\t" \
  "v_lshlrev_b32_sdwa v188, 1, v188 dst_sel:DWORD dst_unused:UNUSED_PAD src0_sel:DWORD src1_sel:WORD_1\n\t" \
  "v_smfmac_f32_16x16x64_bf16 v[36:39], v[220:223], v[152:159], v248\n\t" \
  "ds_read_u16_d16_hi v181, v181 offset:0\n\t" \
  "v_lshlrev_b32_sdwa v189, 1, v189 dst_sel:DWORD dst_unused:UNUSED_PAD src0_sel:DWORD src1_sel:WORD_1\n\t" \
  "v_smfmac_f32_16x16x64_bf16 v[60:63], v[220:223], v[160:167], v248\n\t" \
  "s_waitcnt lgkmcnt(9)\n\t" \
  "ds_read_b64_tr_b16 v[220:221], v249 offset:14336\n\t" \
  "ds_read_u16_d16_hi v182, v182 offset:0\n\t" \
  "v_smfmac_f32_16x16x64_bf16 v[16:19], v[208:211], v[144:151], v248\n\t" \
  "s_add_u32 %[rs], %[rs], 1\n\t" \
  "ds_read_b64_tr_b16 v[222:223], v249 offset:14592\n\t" \
  "v_lshlrev_b32_sdwa v190, 1, v190 dst_sel:DWORD dst_unused:UNUSED_PAD src0_sel:DWORD src1_sel:WORD_1\n\t" \
  "v_smfmac_f32_16x16x64_bf16 v[40:43], v[208:211], v[152:159], v248\n\t" \
  "s_cmp_eq_u32 %[rs], 6\n\t" \
  "ds_read_u16_d16_hi v183, v183 offset:0\n\t" \
  "v_lshlrev_b32_sdwa v191, 1, v191 dst_sel:DWORD dst_unused:UNUSED_PAD src0_sel:DWORD src1_sel:WORD_1\n\t" \
  "v_smfmac_f32_16x16x64_bf16 v[64:67], v[208:211], v[160:167], v248\n\t" \
  "s_waitcnt lgkmcnt(9)\n\t" \
  "ds_read_b64_tr_b16 v[208:209], v249 offset:16384\n\t" \
  "ds_read_u16_d16_hi v188, v188 offset:0\n\t" \
  "v_smfmac_f32_16x16x64_bf16 v[20:23], v[212:215], v[144:151], v248\n\t" \
  "s_cselect_b32 %[rs], 0, %[rs]\n\t" \
  "ds_read_b64_tr_b16 v[210:211], v249 offset:16640\n\t" \
  "v_lshlrev_b32_sdwa v196, 1, v196 dst_sel:DWORD dst_unused:UNUSED_PAD src0_sel:DWORD src1_sel:WORD_1\n\t" \
  "v_smfmac_f32_16x16x64_bf16 v[44:47], v[212:215], v[152:159], v248\n\t" \
  "s_cselect_b32 %[t0], -75920, %[ps]\n\t" \
  "ds_read_u16_d16_hi v189, v189 offset:0\n\t" \
  "v_lshlrev_b32_sdwa v197, 1, v197 dst_sel:DWORD dst_unused:UNUSED_PAD src0_sel:DWORD src1_sel:WORD_1\n\t" \
  "v_smfmac_f32_16x16x64_bf16 v[68:71], v[212:215], v[160:167], v248\n\t" \
  "s_waitcnt lgkmcnt(9)\n\t" \
  "ds_read_b64_tr_b16 v[212:213], v249 offset:12800\n\t" \
  "ds_read_u16_d16_hi v190, v190 offset:0\n\t" \
  "v_add_u32 v252, %[t0], v252\n\t" \
  "v_mfma_f32_16x16x32_bf16 v[144:147], v[216:219], v[96:99], 0\n\t" \
  "ds_read_b64_tr_b16 v[214:215], v249 offset:13056\n\t" \
  "v_lshlrev_b32_sdwa v198, 1, v198 dst_sel:DWORD dst_unused:UNUSED_PAD src0_sel:DWORD src1_sel:WORD_1\n\t" \
  "v_mfma_f32_16x16x32_bf16 v[152:155], v[216:219], v[108:111], 0\n\t" \
  "ds_read_u16_d16_hi v191, v191 offset:0\n\t" \
  "v_lshlrev_b32_sdwa v199, 1, v199 dst_sel:DWORD dst_unused:UNUSED_PAD src0_sel:DWORD src1_sel:WORD_1\n\t" \
  "v_mfma_f32_16x16x32_bf16 v[160:163], v[216:219], v[120:123], 0\n\t" \
  "s_waitcnt lgkmcnt(9)\n\t" \
  "ds_read_b64_tr_b16 v[216:217], v249 offset:14848\n\t" \
  "ds_read_u16_d16_hi v196, v196 offset:0\n\t" \
  "v_mfma_f32_16x16x32_bf16 v[144:147], v[220:223], v[100:103], v[144:147]\n\t" \
  "ds_read_b64_tr_b16 v[218:219], v249 offset:15104\n\t" \
  "v_mfma_f32_16x16x32_bf16 v[152:155], v[220:223], v[112:115], v[152:155]\n\t" \
  "ds_read_u16_d16_hi v197, v197 offset:0\n\t" \
  "v_mfma_f32_16x16x32_bf16 v[160:163], v[220:223], v[124:127], v[160:163]\n\t" \
  "s_waitcnt lgkmcnt(9)\n\t" \
  "ds_read_b64_tr_b16 v[220:221], v249 offset:16896\n\t" \
  "v_mfma_f32_16x16x32_bf16 v[144:147], v[208:211], v[104:107], v[144:147]\n\t" \
  "ds_read_u16_d16_hi v198, v198 offset:0\n\t" \
  "v_mfma_f32_16x16x32_bf16 v[152:155], v[208:211], v[116:119], v[152:155]\n\t" \
  "ds_read_b64_tr_b16 v[222:223], v249 offset:17152\n\t" \
  "v_mfma_f32_16x16x32_bf16 v[160:163], v[208:211], v[128:131], v[160:163]\n\t" \
  "s_waitcnt lgkmcnt(8)\n\t" \
  "ds_read_u16_d16_hi v199, v199 offset:0\n\t" \
  "s_nop 1\n\t" \
  "v_lshlrev_b32_sdwa v144, 1, v144 dst_sel:DWORD dst_unused:UNUSED_PAD src0_sel:DWORD src1_sel:WORD_1\n\t" \
  "v_mfma_f32_16x16x32_bf16 v[148:151], v[212:215], v[96:99], 0\n\t" \
  "ds_read_b64_tr_b16 v[208:209], v250 offset:18480\n\t" \
  "v_mul_f32 v152, %[kk], v152\n\t" \
  "v_mfma_f32_16x16x32_bf16 v[156:159], v[212:215], v[108:111], 0\n\t" \
  "ds_read_u16_d16_hi v144, v144 offset:0\n\t" \
  "v_mul_f32 v153, %[kk], v153\n\t" \
  "v_mfma_f32_16x16x32_bf16 v[164:167], v[212:215], v[120:123], 0\n\t" \
  "s_waitcnt lgkmcnt(7)\n\t" \
  "ds_read_b64_tr_b16 v[210:211], v250 offset:19248\n\t" \
  "v_lshlrev_b32_sdwa v145, 1, v145 dst_sel:DWORD dst_unused:UNUSED_PAD src0_sel:DWORD src1_sel:WORD_1\n\t" \
  "v_cvt_pk_f16_f32 v224, v152, v153\n\t" \
  "v_mfma_f32_16x16x32_bf16 v[148:151], v[216:219], v[100:103], v[148:151]\n\t" \
  "ds_read_u16_d16_hi v145, v145 offset:0\n\t" \
  "v_lshlrev_b32_sdwa v146, 1, v146 dst_sel:DWORD dst_unused:UNUSED_PAD src0_sel:DWORD src1_sel:WORD_1\n\t" \
  "v_mfma_f32_16x16x32_bf16 v[156:159], v[216:219], v[112:115], v[156:159]\n\t" \
  "ds_read_b64_tr_b16 v[212:213], v250 offset:18488\n\t" \
  "v_pk_fma_f16 v225, v224, v224, %[h1] neg_lo:[1,0,0] neg_hi:[1,0,0]\n\t" \
  "v_mfma_f32_16x16x32_bf16 v[164:167], v[216:219], v[124:127], v[164:167]\n\t" \
  "s_waitcnt lgkmcnt(6)\n\t" \
  "ds_read_b64_tr_b16 v[214:215], v250 offset:19256\n\t" \
  "v_pk_mul_f16 v225, v224, v225\n\t" \
  "v_mfma_f32_16x16x32_bf16 v[148:151], v[220:223], v[104:107], v[148:151]\n\t" \
  "ds_read_u16_d16_hi v146, v146 offset:0\n\t" \
  "v_lshlrev_b32_sdwa v147, 1, v147 dst_sel:DWORD dst_unused:UNUSED_PAD src0_sel:DWORD src1_sel:WORD_1\n\t" \
  "v_exp_f16 v224, v225\n\t" \
  "v_mfma_f32_16x16x32_bf16 v[156:159], v[220:223], v[116:119], v[156:159]\n\t" \
  "ds_read_b64_tr_b16 v[216:217], v250 offset:18544\n\t" \
  "v_lshlrev_b32_sdwa v154, 1, v154 dst_sel:DWORD dst_unused:UNUSED_PAD src0_sel:DWORD src1_sel:WORD_1\n\t" \
  "v_mfma_f32_16x16x32_bf16 v[164:167], v[220:223], v[128:131], v[164:167]\n\t" \
  "s_waitcnt lgkmcnt(5)\n\t" \
  "ds_read_b64_tr_b16 v[218:219], v250 offset:19312\n\t" \
  "ds_read_u16_d16_hi v147, v147 offset:0\n\t" \
  "v_exp_f16_sdwa v224, v225 dst_sel:WORD_1 dst_unused:UNUSED_PRESERVE src0_sel:WORD_1\n\t" \
  "v_smfmac_f32_16x16x64_bf16 v[0:3], v[208:211], v[176:183], v248\n\t" \
  "ds_read_b64_tr_b16 v[220:221], v250 offset:18552\n\t" \
  "v_lshlrev_b32_sdwa v155, 1, v155 dst_sel:DWORD dst_unused:UNUSED_PAD src0_sel:DWORD src1_sel:WORD_1\n\t" \
  "v_smfmac_f32_16x16x64_bf16 v[24:27], v[208:211], v[184:191], v248\n\t" \
  "ds_read_u16_d16_hi v154, v154 offset:0\n\t" \
  "v_pk_fma_f16 v224, v224, %[hc], %[hc]\n\t" \
  "v_smfmac_f32_16x16x64_bf16 v[48:51], v[208:211], v[192:199], v248\n\t" \
  "s_waitcnt lgkmcnt(6)\n\t" \
  "ds_read_b64_tr_b16 v[222:223], v250 offset:19320\n\t" \
  "v_lshlrev_b32_sdwa v160, 1, v160 dst_sel:DWORD dst_unused:UNUSED_PAD src0_sel:DWORD src1_sel:WORD_1\n\t" \
  "v_rcp_f16 v225, v224\n\t" \
  "v_smfmac_f32_16x16x64_bf16 v[4:7], v[212:215], v[176:183], v248\n\t" \
  "ds_read_u16_d16_hi v155, v155 offset:0\n\t" \
  "v_lshlrev_b32_sdwa v161, 1, v161 dst_sel:DWORD dst_unused:UNUSED_PAD src0_sel:DWORD src1_sel:WORD_1\n\t" \
  "v_smfmac_f32_16x16x64_bf16 v[28:31], v[212:215], v[184:191], v248\n\t" \
  "ds_read_b64_tr_b16 v[208:209], v250 offset:18608\n\t" \
  "v_rcp_f16_sdwa v224, v224 dst_sel:DWORD dst_unused:UNUSED_PAD src0_sel:WORD_1\n\t" \
  "v_smfmac_f32_16x16x64_bf16 v[52:55], v[212:215], v[192:199], v248\n\t" \
  "s_waitcnt lgkmcnt(6)\n\t" \
  "ds_read_b64_tr_b16 v[210:211], v250 offset:19376\n\t" \
  "ds_read_u16_d16_hi v160, v160 offset:0\n\t" \
  "v_smfmac_f32_16x16x64_bf16 v[8:11], v[216:219], v[176:183], v248\n\t" \
  "ds_read_b64_tr_b16 v[212:213], v250 offset:18616\n\t" \
  "v_lshlrev_b32_sdwa v162, 1, v162 dst_sel:DWORD dst_unused:UNUSED_PAD src0_sel:DWORD src1_sel:WORD_1\n\t" \
  "v_smfmac_f32_16x16x64_bf16 v[32:35], v[216:219], v[184:191], v248\n\t" \
  "ds_read_u16_d16_hi v161, v161 offset:0\n\t" \
  "v_fma_mix_f32 v152, v152, v225, 0 op_sel_hi:[0,1,0]\n\t" \
  "v_smfmac_f32_16x16x64_bf16 v[56:59], v[216:219], v[192:199], v248\n\t" \
  "s_waitcnt lgkmcnt(6)\n\t" \
  "ds_read_b64_tr_b16 v[214:215], v250 offset:19384\n\t" \
  "s_barrier\n\t" \
  "ds_read_b128 v[132:135], v251 offset:0\n\t" \
  "v_fma_mix_f32 v153, v153, v224, 0 op_sel_hi:[0,1,0]\n\t" \
  "v_lshlrev_b32_sdwa v163, 1, v163 dst_sel:DWORD dst_unused:UNUSED_PAD src0_sel:DWORD src1_sel:WORD_1\n\t" \
  "v_smfmac_f32_16x16x64_bf16 v[12:15], v[220:223], v[176:183], v248\n\t" \
  "ds_read_b64_tr_b16 v[216:217], v249 offset:0\n\t" \
  "ds_read_b128 v[136:139], v251 offset:4096\n\t" \
  "v_mul_f32 v156, %[kk], v156\n\t" \
  "v_smfmac_f32_16x16x64_bf16 v[36:39], v[220:223], v[184:191], v248\n\t" \
  "ds_read_b64_tr_b16 v[218:219], v249 offset:256\n\t" \
  "v_lshlrev_b32_sdwa v148, 1, v148 dst_sel:DWORD dst_unused:UNUSED_PAD src0_sel:DWORD src1_sel:WORD_1\n\t" \
  "v_mul_f32 v157, %[kk], v157\n\t" \
  "v_smfmac_f32_16x16x64_bf16 v[60:63], v[220:223], v[192:199], v248\n\t" \
  "s_waitcnt lgkmcnt(8)\n\t" \
  "ds_read_b128 v[140:143], v251 offset:8192\n\t" \
  "ds_read_b64_tr_b16 v[220:221], v249 offset:2048\n\t" \
  "v_cvt_pk_f16_f32 v224, v156, v157\n\t" \
  "v_lshlrev_b32_sdwa v149, 1, v149 dst_sel:DWORD dst_unused:UNUSED_PAD src0_sel:DWORD src1_sel:WORD_1\n\t" \
  "v_smfmac_f32_16x16x64_bf16 v[16:19], v[208:211], v[176:183], v248\n\t" \
  "ds_read_u16_d16_hi v162, v162 offset:0\n\t" \
  "ds_read_b64_tr_b16 v[222:223], v249 offset:2304\n\t" \
  "v_pk_fma_f16 v225, v224, v224, %[h1] neg_lo:[1,0,0] neg_hi:[1,0,0]\n\t" \
  "v_smfmac_f32_16x16x64_bf16 v[40:43], v[208:211], v[184:191], v248\n\t" \
  "ds_read_u16_d16_hi v163, v163 offset:0\n\t" \
  "v_pk_mul_f16 v225, v224, v225\n\t" \
  "v_smfmac_f32_16x16x64_bf16 v[64:67], v[208:211], v[192:199], v248\n\t" \
  "s_waitcnt lgkmcnt(9)\n\t" \
  "ds_read_b64_tr_b16 v[208:209], v249 offset:4096\n\t" \
  "v_lshlrev_b32_sdwa v150, 1, v150 dst_sel:DWORD dst_unused:UNUSED_PAD src0_sel:DWORD src1_sel:WORD_1\n\t" \
  "v_exp_f16 v224, v225\n\t" \
  "v_smfmac_f32_16x16x64_bf16 v[20:23], v[212:215], v[176:183], v248\n\t" \
  "ds_read_u16_d16_hi v148, v148 offset:0\n\t" \
  "v_lshlrev_b32_sdwa v151, 1, v151 dst_sel:DWORD dst_unused:UNUSED_PAD src0_sel:DWORD src1_sel:WORD_1\n\t" \
  "v_smfmac_f32_16x16x64_bf16 v[44:47], v[212:215], v[184:191], v248\n\t" \
  "ds_read_b64_tr_b16 v[210:211], v249 offset:4352\n\t" \
  "v_exp_f16_sdwa v224, v225 dst_sel:WORD_1 dst_unused:UNUSED_PRESERVE src0_sel:WORD_1\n\t" \
  "v_smfmac_f32_16x16x64_bf16 v[68:71], v[212:215], v[192:199], v248\n\t" \
  "s_waitcnt lgkmcnt(8)\n\t" \
  "ds_read_u16_d16_hi v149, v149 offset:0\n\t" \
  "ds_read_b64_tr_b16 v[212:213], v249 offset:512\n\t" \
  "v_lshlrev_b32_sdwa v158, 1, v158 dst_sel:DWORD dst_unused:UNUSED_PAD src0_sel:DWORD src1_sel:WORD_1\n\t" \
  "v_mfma_f32_16x16x32_bf16 v[176:179], v[216:219], v[96:99], 0\n\t" \
  "ds_read_u16_d16_hi v150, v150 offset:0\n\t" \
  "v_pk_fma_f16 v224, v224, %[hc], %[hc]\n\t" \
  "v_mfma_f32_16x16x32_bf16 v[184:187], v[216:219], v[108:111], 0\n\t" \
  "ds_read_b64_tr_b16 v[214:215], v249 offset:768\n\t" \
  "v_lshlrev_b32_sdwa v159, 1, v159 dst_sel:DWORD dst_unused:UNUSED_PAD src0_sel:DWORD src1_sel:WORD_1\n\t" \
  "v_mfma_f32_16x16x32_bf16 v[192:195], v[216:219], v[120:123], 0\n\t" \
  "ds_read_u16_d16_hi v151, v151 offset:0\n\t" \
  "v_rcp_f16 v225, v224\n\t" \
  "v_mfma_f32_16x16x32_bf16 v[200:203], v[216:219], v[132:135], 0\n\t" \
  "s_waitcnt lgkmcnt(9)\n\t" \
  "ds_read_b64_tr_b16 v[216:217], v249 offset:2560\n\t" \
  "v_lshlrev_b32_sdwa v164, 1, v164 dst_sel:DWORD dst_unused:UNUSED_PAD src0_sel:DWORD src1_sel:WORD_1\n\t" \
  "v_mfma_f32_16x16x32_bf16 v[176:179], v[220:223], v[100:103], v[176:179]\n\t" \
  "ds_read_u16_d16_hi v158, v158 offset:0\n\t" \
  "v_rcp_f16_sdwa v224, v224 dst_sel:DWORD dst_unused:UNUSED_PAD src0_sel:WORD_1\n\t" \
  "v_mfma_f32_16x16x32_bf16 v[184:187], v[220:223], v[112:115], v[184:187]\n\t" \
  "ds_read_b64_tr_b16 v[218:219], v249 offset:2816\n\t" \
  "v_lshlrev_b32_sdwa v165, 1, v165 dst_sel:DWORD dst_unused:UNUSED_PAD src0_sel:DWORD src1_sel:WORD_1\n\t" \
  "v_mfma_f32_16x16x32_bf16 v[192:195], v[220:223], v[124:127], v[192:195]\n\t" \
  "ds_read_u16_d16_hi v159, v159 offset:0\n\t" \
  "v_fma_mix_f32 v156, v156, v225, 0 op_sel_hi:[0,1,0]\n\t" \
  "v_mfma_f32_16x16x32_bf16 v[200:203], v[220:223], v[136:139], v[200:203]\n\t" \
  "s_waitcnt lgkmcnt(9)\n\t" \
  "ds_read_b64_tr_b16 v[220:221], v249 offset:4608\n\t" \
  "v_lshlrev_b32_sdwa v166, 1, v166 dst_sel:DWORD dst_unused:UNUSED_PAD src0_sel:DWORD src1_sel:WORD_1\n\t" \
  "v_mfma_f32_16x16x32_bf16 v[176:179], v[208:211], v[104:107], v[176:179]\n\t" \
  "ds_read_u16_d16_hi v164, v164 offset:0\n\t" \
  "v_fma_mix_f32 v157, v157, v224, 0 op_sel_hi:[0,1,0]\n\t" \
  "v_mfma_f32_16x16x32_bf16 v[184:187], v[208:211], v[116:119], v[184:187]\n\t" \
  "ds_read_b64_tr_b16 v[222:223], v249 offset:4864\n\t" \
  "v_lshlrev_b32_sdwa v167, 1, v167 dst_sel:DWORD dst_unused:UNUSED_PAD src0_sel:DWORD src1_sel:WORD_1\n\t" \
  "v_mfma_f32_16x16x32_bf16 v[192:195], v[208:211], v[128:131], v[192:195]\n\t" \
  "ds_read_u16_d16_hi v165, v165 offset:0\n\t" \
  "s_nop 0\n\t" \
  "v_lshlrev_b32_sdwa v176, 1, v176 dst_sel:DWORD dst_unused:UNUSED_PAD src0_sel:DWORD src1_sel:WORD_1\n\t" \
  "v_mfma_f32_16x16x32_bf16 v[200:203], v[208:211], v[140:143], v[200:203]\n\t" \
  "s_waitcnt lgkmcnt(9)\n\t" \
  "ds_read_b64_tr_b16 v[208:209], v250 offset:0\n\t" \
  "ds_read_u16_d16_hi v166, v166 offset:0\n\t" \
  "v_mfma_f32_16x16x32_bf16 v[180:183], v[212:215], v[96:99], 0\n\t" \
  "ds_read_b64_tr_b16 v[210:211], v250 offset:768\n\t" \
  "v_mul_f32 v184, %[kk], v184\n\t" \
  "v_mfma_f32_16x16x32_bf16 v[188:191], v[212:215], v[108:111], 0\n\t" \
  "ds_read_u16_d16_hi v167, v167 offset:0\n\t" \
  "v_lshlrev_b32_sdwa v177, 1, v177 dst_sel:DWORD dst_unused:UNUSED_PAD src0_sel:DWORD src1_sel:WORD_1\n\t" \
  "v_mfma_f32_16x16x32_bf16 v[196:199], v[212:215], v[120:123], 0\n\t" \
  "ds_read_u16_d16_hi v176, v176 offset:0\n\t" \
  "v_mul_f32 v185, %[kk], v185\n\t" \
  "v_mfma_f32_16x16x32_bf16 v[204:207], v[212:215], v[132:135], 0\n\t" \
  "s_waitcnt lgkmcnt(10)\n\t" \
  "ds_read_b64_tr_b16 v[212:213], v250 offset:8\n\t" \
  "v_cvt_pk_f16_f32 v224, v184, v185\n\t" \
  "v_mfma_f32_16x16x32_bf16 v[180:183], v[216:219], v[100:103], v[180:183]\n\t" \
  "ds_read_u16_d16_hi v177, v177 offset:0\n\t" \
  "v_lshlrev_b32_sdwa v178, 1, v178 dst_sel:DWORD dst_unused:UNUSED_PAD src0_sel:DWORD src1_sel:WORD_1\n\t" \
  "v_mfma_f32_16x16x32_bf16 v[188:191], v[216:219], v[112:115], v[188:191]\n\t" \
  "ds_read_b64_tr_b16 v[214:215], v250 offset:776\n\t" \
  "v_pk_fma_f16 v225, v224, v224, %[h1] neg_lo:[1,0,0] neg_hi:[1,0,0]\n\t" \
  "v_mfma_f32_16x16x32_bf16 v[196:199], v[216:219], v[124:127], v[196:199]\n\t" \
  "ds_read_u16_d16_hi v178, v178 offset:0\n\t" \
  "v_lshlrev_b32_sdwa v179, 1, v179 dst_sel:DWORD dst_unused:UNUSED_PAD src0_sel:DWORD src1_sel:WORD_1\n\t" \
  "v_pk_mul_f16 v225, v224, v225\n\t" \
  "v_mfma_f32_16x16x32_bf16 v[204:207], v[216:219], v[136:139], v[204:207]\n\t" \
  "s_waitcnt lgkmcnt(10)\n\t" \
  "ds_read_b64_tr_b16 v[216:217], v250 offset:64\n\t" \
  "v_lshlrev_b32_sdwa v186, 1, v186 dst_sel:DWORD dst_unused:UNUSED_PAD src0_sel:DWORD src1_sel:WORD_1\n\t" \
  "v_mfma_f32_16x16x32_bf16 v[180:183], v[220:223], v[104:107], v[180:183]\n\t" \
  "ds_read_u16_d16_hi v179, v179 offset:0\n\t" \
  "v_exp_f16 v224, v225\n\t" \
  "v_mfma_f32_16x16x32_bf16 v[188:191], v[220:223], v[116:119], v[188:191]\n\t" \
  "ds_read_b64_tr_b16 v[218:219], v250 offset:832\n\t" \
  "v_lshlrev_b32_sdwa v187, 1, v187 dst_sel:DWORD dst_unused:UNUSED_PAD src0_sel:DWORD src1_sel:WORD_1\n\t" \
  "v_mfma_f32_16x16x32_bf16 v[196:199], v[220:223], v[128:131], v[196:199]\n\t" \
  "ds_read_u16_d16_hi v186, v186 offset:0\n\t" \
  "v_lshlrev_b32_sdwa v192, 1, v192 dst_sel:DWORD dst_unused:UNUSED_PAD src0_sel:DWORD src1_sel:WORD_1\n\t" \
  "v_mfma_f32_16x16x32_bf16 v[204:207], v[220:223], v[140:143], v[204:207]\n\t" \
  "s_waitcnt lgkmcnt(10)\n\t" \
  "ds_read_b64_tr_b16 v[220:221], v250 offset:72\n\t" \
  "v_exp_f16_sdwa v224, v225 dst_sel:WORD_1 dst_unused:UNUSED_PRESERVE src0_sel:WORD_1\n\t" \
  "v_smfmac_f32_16x16x64_bf16 v[0:3], v[208:211], v[144:151], v248\n\t" \
  "ds_read_u16_d16_hi v187, v187 offset:0\n\t" \
  "v_lshlrev_b32_sdwa v193, 1, v193 dst_sel:DWORD dst_unused:UNUSED_PAD src0_sel:DWORD src1_sel:WORD_1\n\t" \
  "v_smfmac_f32_16x16x64_bf16 v[24:27], v[208:211], v[152:159], v248\n\t" \
  "s_waitcnt lgkmcnt(11)\n\t" \
  "ds_read_b64_tr_b16 v[222:223], v250 offset:840\n\t" \
  "v_pk_fma_f16 v224, v224, %[hc], %[hc]\n\t" \
  "v_smfmac_f32_16x16x64_bf16 v[48:51], v[208:211], v[160:167], v248\n\t" \
  "s_waitcnt lgkmcnt(8)\n\t" \
  "ds_read_b64 v[230:231], v252 offset:0\n\t" \
  "ds_read_b64_tr_b16 v[208:209], v250 offset:128\n\t" \
  "v_lshlrev_b32_sdwa v194, 1, v194 dst_sel:DWORD dst_unused:UNUSED_PAD src0_sel:DWORD src1_sel:WORD_1\n\t" \
  "v_rcp_f16 v225, v224\n\t" \
  "v_smfmac_f32_16x16x64_bf16 v[4:7], v[212:215], v[144:151], v248\n\t" \
  "ds_read_b64 v[232:233], v252 offset:8\n\t" \
  "ds_read_b64_tr_b16 v[210:211], v250 offset:896\n\t" \
  "v_lshlrev_b32_sdwa v195, 1, v195 dst_sel:DWORD dst_unused:UNUSED_PAD src0_sel:DWORD src1_sel:WORD_1\n\t" \
  "v_smfmac_f32_16x16x64_bf16 v[28:31], v[212:215], v[152:159], v248\n\t" \
  "ds_read_b64 v[234:235], v252 offset:64\n\t" \
  "ds_read_b64 v[236:237], v252 offset:72\n\t" \
  "v_smfmac_f32_16x16x64_bf16 v[52:55], v[212:215], v[160:167], v248\n\t" \
  "s_waitcnt lgkmcnt(10)\n\t" \
  "ds_read_b64_tr_b16 v[212:213], v250 offset:136\n\t" \
  "ds_read_b64 v[242:243], v252 offset:128\n\t" \
  "v_lshlrev_b32_sdwa v200, 1, v200 dst_sel:DWORD dst_unused:UNUSED_PAD src0_sel:DWORD src1_sel:WORD_1\n\t" \
  "v_rcp_f16_sdwa v224, v224 dst_sel:DWORD dst_unused:UNUSED_PAD src0_sel:WORD_1\n\t" \
  "v_smfmac_f32_16x16x64_bf16 v[8:11], v[216:219], v[144:151], v248\n\t" \
  "ds_read_b64_tr_b16 v[214:215], v250 offset:904\n\t" \
  "ds_read_b64 v[244:245], v252 offset:136\n\t" \
  "v_lshlrev_b32_sdwa v201, 1, v201 dst_sel:DWORD dst_unused:UNUSED_PAD src0_sel:DWORD src1_sel:WORD_1\n\t" \
  "v_smfmac_f32_16x16x64_bf16 v[32:35], v[216:219], v[152:159], v248\n\t" \
  "ds_read_u16_d16_hi v192, v192 offset:0\n\t" \
  "ds_read_u16_d16_hi v193, v193 offset:0\n\t" \
  "v_fma_mix_f32 v184, v184, v225, 0 op_sel_hi:[0,1,0]\n\t" \
  "v_smfmac_f32_16x16x64_bf16 v[56:59], v[216:219], v[160:167], v248\n\t" \
  "s_waitcnt lgkmcnt(12)\n\t" \
  "s_barrier\n\t" \
  "ds_read_b64_tr_b16 v[216:217], v249 offset:6144\n\t" \
  "ds_read_u16_d16_hi v194, v194 offset:0\n\t" \
  "v_lshlrev_b32_sdwa v202, 1, v202 dst_sel:DWORD dst_unused:UNUSED_PAD src0_sel:DWORD src1_sel:WORD_1\n\t" \
  "v_fma_mix_f32 v185, v185, v224, 0 op_sel_hi:[0,1,0]\n\t" \
  "v_lshlrev_b32_sdwa v203, 1, v203 dst_sel:DWORD dst_unused:UNUSED_PAD src0_sel:DWORD src1_sel:WORD_1\n\t" \
  "v_smfmac_f32_16x16x64_bf16 v[12:15], v[220:223], v[144:151], v248\n\t" \
  "ds_read_b64_tr_b16 v[218:219], v249 offset:6400\n\t" \
  "ds_read_u16_d16_hi v195, v195 offset:0\n\t" \
  "v_mul_f32 v188, %[kk], v188\n\t" \
  "v_smfmac_f32_16x16x64_bf16 v[36:39], v[220:223], v[152:159], v248\n\t" \
  "ds_read_u16_d16_hi v200, v200 offset:0\n\t" \
  "v_lshlrev_b32_sdwa v180, 1, v180 dst_sel:DWORD dst_unused:UNUSED_PAD src0_sel:DWORD src1_sel:WORD_1\n\t" \
  "v_mul_f32 v189, %[kk], v189\n\t" \
  "v_smfmac_f32_16x16x64_bf16 v[60:63], v[220:223], v[160:167], v248\n\t" \
  "s_waitcnt lgkmcnt(13)\n\t" \
  "ds_read_b64_tr_b16 v[220:221], v249 offset:8192\n\t" \
  "ds_read_u16_d16_hi v201, v201 offset:0\n\t" \
  "v_cvt_pk_f16_f32 v224, v188, v189\n\t" \
  "v_lshlrev_b32_sdwa v181, 1, v181 dst_sel:DWORD dst_unused:UNUSED_PAD src0_sel:DWORD src1_sel:WORD_1\n\t" \
  "v_smfmac_f32_16x16x64_bf16 v[16:19], v[208:211], v[144:151], v248\n\t" \
  "ds_read_b64_tr_b16 v[222:223], v249 offset:8448\n\t" \
  "ds_read_u16_d16_hi v202, v202 offset:0\n\t" \
  "v_pk_fma_f16 v225, v224, v224, %[h1] neg_lo:[1,0,0] neg_hi:[1,0,0]\n\t" \
  "v_smfmac_f32_16x16x64_bf16 v[40:43], v[208:211], v[152:159], v248\n\t" \
  "ds_read_u16_d16_hi v203, v203 offset:0\n\t" \
  "v_lshlrev_b32_sdwa v182, 1, v182 dst_sel:DWORD dst_unused:UNUSED_PAD src0_sel:DWORD src1_sel:WORD_1\n\t" \
  "v_pk_mul_f16 v225, v224, v225\n\t" \
  "v_smfmac_f32_16x16x64_bf16 v[64:67], v[208:211], v[160:167], v248\n\t" \
  "s_waitcnt lgkmcnt(13)\n\t" \
  "ds_read_b64_tr_b16 v[208:209], v249 offset:10240\n\t" \
  "ds_read_u16_d16_hi v180, v180 offset:0\n\t" \
  "v_lshlrev_b32_sdwa v183, 1, v183 dst_sel:DWORD dst_unused:UNUSED_PAD src0_sel:DWORD src1_sel:WORD_1\n\t" \
  "v_exp_f16 v224, v225\n\t" \
  "v_smfmac_f32_16x16x64_bf16 v[20:23], v[212:215], v[144:151], v248\n\t" \
  "ds_read_b64_tr_b16 v[210:211], v249 offset:10496\n\t" \
  "v_lshlrev_b32_sdwa v190, 1, v190 dst_sel:DWORD dst_unused:UNUSED_PAD src0_sel:DWORD src1_sel:WORD_1\n\t" \
  "v_smfmac_f32_16x16x64_bf16 v[44:47], v[212:215], v[152:159], v248\n\t" \
  "ds_read_u16_d16_hi v181, v181 offset:0\n\t" \
  "v_lshlrev_b32_sdwa v191, 1, v191 dst_sel:DWORD dst_unused:UNUSED_PAD src0_sel:DWORD src1_sel:WORD_1\n\t" \
  "v_smfmac_f32_16x16x64_bf16 v[68:71], v[212:215], v[160:167], v248\n\t" \
  "ds_read_b64_tr_b16 v[212:213], v249 offset:6656\n\t" \
  "ds_read_u16_d16_hi v182, v182 offset:0\n\t" \
  "v_exp_f16_sdwa v224, v225 dst_sel:WORD_1 dst_unused:UNUSED_PRESERVE src0_sel:WORD_1\n\t" \
  "v_mfma_f32_16x16x16_bf16 v[72:75], v[246:247], v[230:231], 0\n\t" \
  "s_waitcnt lgkmcnt(13)\n\t" \
  "ds_read_b64_tr_b16 v[214:215], v249 offset:6912\n\t" \
  "v_lshlrev_b32_sdwa v196, 1, v196 dst_sel:DWORD dst_unused:UNUSED_PAD src0_sel:DWORD src1_sel:WORD_1\n\t" \
  "v_mfma_f32_16x16x32_bf16 v[144:147], v[216:219], v[96:99], 0\n\t" \
  "ds_read_u16_d16_hi v183, v183 offset:0\n\t" \
  "v_pk_fma_f16 v224, v224, %[hc], %[hc]\n\t" \
  "v_mfma_f32_16x16x32_bf16 v[152:155], v[216:219], v[108:111], 0\n\t" \
  "ds_read_u16_d16_hi v190, v190 offset:0\n\t" \
  "v_lshlrev_b32_sdwa v197, 1, v197 dst_sel:DWORD dst_unused:UNUSED_PAD src0_sel:DWORD src1_sel:WORD_1\n\t" \
  "v_mfma_f32_16x16x32_bf16 v[160:163], v[216:219], v[120:123], 0\n\t" \
  "ds_read_u16_d16_hi v191, v191 offset:0\n\t" \
  "v_rcp_f16 v225, v224\n\t" \
  "v_mfma_f32_16x16x16_bf16 v[76:79], v[246:247], v[232:233], 0\n\t" \
  "ds_read_u16_d16_hi v196, v196 offset:0\n\t" \
  "v_lshlrev_b32_sdwa v198, 1, v198 dst_sel:DWORD dst_unused:UNUSED_PAD src0_sel:DWORD src1_sel:WORD_1\n\t" \
  "v_mfma_f32_16x16x32_bf16 v[168:171], v[216:219], v[132:135], 0\n\t" \
  "s_waitcnt lgkmcnt(13)\n\t" \
  "ds_read_b64_tr_b16 v[216:217], v249 offset:8704\n\t" \
  "v_lshlrev_b32_sdwa v199, 1, v199 dst_sel:DWORD dst_unused:UNUSED_PAD src0_sel:DWORD src1_sel:WORD_1\n\t" \
  "v_mfma_f32_16x16x32_bf16 v[144:147], v[220:223], v[100:103], v[144:147]\n\t" \
  "ds_read_u16_d16_hi v197, v197 offset:0\n\t" \
  "v_rcp_f16_sdwa v224, v224 dst_sel:DWORD dst_unused:UNUSED_PAD src0_sel:WORD_1\n\t" \
  "v_mfma_f32_16x16x32_bf16 v[152:155], v[220:223], v[112:115], v[152:155]\n\t" \
  "ds_read_b64_tr_b16 v[218:219], v249 offset:8960\n\t" \
  "v_lshlrev_b32_sdwa v204, 1, v204 dst_sel:DWORD dst_unused:UNUSED_PAD src0_sel:DWORD src1_sel:WORD_1\n\t" \
  "v_mfma_f32_16x16x32_bf16 v[160:163], v[220:223], v[124:127], v[160:163]\n\t" \
  "ds_read_u16_d16_hi v198, v198 offset:0\n\t" \
  "v_lshlrev_b32_sdwa v205, 1, v205 dst_sel:DWORD dst_unused:UNUSED_PAD src0_sel:DWORD src1_sel:WORD_1\n\t" \
  "v_mfma_f32_16x16x32_bf16 v[168:171], v[220:223], v[136:139], v[168:171]\n\t" \
  "ds_read_b64_tr_b16 v[220:221], v249 offset:10752\n\t" \
  "ds_read_u16_d16_hi v199, v199 offset:0\n\t" \
  "v_mfma_f32_16x16x16_bf16 v[80:83], v[246:247], v[234:235], 0\n\t" \
  "s_waitcnt lgkmcnt(14)\n\t" \
  "ds_read_b64_tr_b16 v[222:223], v249 offset:11008\n\t" \
  "v_fma_mix_f32 v188, v188, v225, 0 op_sel_hi:[0,1,0]\n\t" \
  "v_mfma_f32_16x16x32_bf16 v[144:147], v[208:211], v[104:107], v[144:147]\n\t" \
  "ds_read_u16_d16_hi v204, v204 offset:0\n\t" \
  "v_fma_mix_f32 v189, v189, v224, 0 op_sel_hi:[0,1,0]\n\t" \
  "v_mfma_f32_16x16x32_bf16 v[152:155], v[208:211], v[116:119], v[152:155]\n\t" \
  "ds_read_u16_d16_hi v205, v205 offset:0\n\t" \
  "v_lshlrev_b32_sdwa v206, 1, v206 dst_sel:DWORD dst_unused:UNUSED_PAD src0_sel:DWORD src1_sel:WORD_1\n\t" \
  "v_mfma_f32_16x16x32_bf16 v[160:163], v[208:211], v[128:131], v[160:163]\n\t" \
  "ds_read_u16_d16_hi v206, v206 offset:0\n\t" \
  "v_lshlrev_b32_sdwa v207, 1, v207 dst_sel:DWORD dst_unused:UNUSED_PAD src0_sel:DWORD src1_sel:WORD_1\n\t" \
  "v_mfma_f32_16x16x16_bf16 v[84:87], v[246:247], v[236:237], 0\n\t" \
  "ds_read_u16_d16_hi v207, v207 offset:0\n\t" \
  "v_lshlrev_b32_sdwa v144, 1, v144 dst_sel:DWORD dst_unused:UNUSED_PAD src0_sel:DWORD src1_sel:WORD_1\n\t" \
  "v_mfma_f32_16x16x32_bf16 v[168:171], v[208:211], v[140:143], v[168:171]\n\t" \
  "s_waitcnt lgkmcnt(14)\n\t" \
  "ds_read_b64_tr_b16 v[208:209], v250 offset:6160\n\t" \
  "v_lshlrev_b32_sdwa v145, 1, v145 dst_sel:DWORD dst_unused:UNUSED_PAD src0_sel:DWORD src1_sel:WORD_1\n\t" \
  "v_mfma_f32_16x16x32_bf16 v[148:151], v[212:215], v[96:99], 0\n\t" \
  "ds_read_u16_d16_hi v144, v144 offset:0\n\t" \
  "v_mul_f32 v152, %[kk], v152\n\t" \
  "v_mfma_f32_16x16x32_bf16 v[156:159], v[212:215], v[108:111], 0\n\t" \
  "ds_read_b64_tr_b16 v[210:211], v250 offset:6928\n\t" \
  "v_mul_f32 v153, %[kk], v153\n\t" \
  "v_mfma_f32_16x16x32_bf16 v[164:167], v[212:215], v[120:123], 0\n\t" \
  "ds_read_u16_d16_hi v145, v145 offset:0\n\t" \
  "v_cvt_pk_f16_f32 v224, v152, v153\n\t" \
  "v_mfma_f32_16x16x32_bf16 v[172:175], v[212:215], v[132:135], 0\n\t" \
  "ds_read_b64_tr_b16 v[212:213], v250 offset:6168\n\t" \
  "v_lshlrev_b32_sdwa v146, 1, v146 dst_sel:DWORD dst_unused:UNUSED_PAD src0_sel:DWORD src1_sel:WORD_1\n\t" \
  "v_mfma_f32_16x16x16_bf16 v[88:91], v[246:247], v[242:243], 0\n\t" \
  "s_waitcnt lgkmcnt(13)\n\t" \
  "ds_read_u16_d16_hi v146, v146 offset:0\n\t" \
  "v_pk_fma_f16 v225, v224, v224, %[h1] neg_lo:[1,0,0] neg_hi:[1,0,0]\n\t" \
  "v_mfma_f32_16x16x32_bf16 v[148:151], v[216:219], v[100:103], v[148:151]\n\t" \
  "ds_read_b64_tr_b16 v[214:215], v250 offset:6936\n\t" \
  "v_lshlrev_b32_sdwa v147, 1, v147 dst_sel:DWORD dst_unused:UNUSED_PAD src0_sel:DWORD src1_sel:WORD_1\n\t" \
  "v_mfma_f32_16x16x32_bf16 v[156:159], v[216:219], v[112:115], v[156:159]\n\t" \
  "ds_read_u16_d16_hi v147, v147 offset:0\n\t" \
  "v_pk_mul_f16 v225, v224, v225\n\t" \
  "v_mfma_f32_16x16x32_bf16 v[164:167], v[216:219], v[124:127], v[164:167]\n\t" \
  "v_lshlrev_b32_sdwa v154, 1, v154 dst_sel:DWORD dst_unused:UNUSED_PAD src0_sel:DWORD src1_sel:WORD_1\n\t" \
  "v_exp_f16 v224, v225\n\t" \
  "v_mfma_f32_16x16x16_bf16 v[92:95], v[246:247], v[244:245], 0\n\t" \
  "ds_read_u16_d16_hi v154, v154 offset:0\n\t" \
  "v_lshlrev_b32_sdwa v155, 1, v155 dst_sel:DWORD dst_unused:UNUSED_PAD src0_sel:DWORD src1_sel:WORD_1\n\t" \
  "v_mfma_f32_16x16x32_bf16 v[172:175], v[216:219], v[136:139], v[172:175]\n\t" \
  "s_waitcnt lgkmcnt(13)\n\t" \
  "ds_read_b64_tr_b16 v[216:217], v250 offset:6224\n\t" \
  "v_lshlrev_b32_sdwa v160, 1, v160 dst_sel:DWORD dst_unused:UNUSED_PAD src0_sel:DWORD src1_sel:WORD_1\n\t" \
  "v_mfma_f32_16x16x32_bf16 v[148:151], v[220:223], v[104:107], v[148:151]\n\t" \
  "ds_read_u16_d16_hi v155, v155 offset:0\n\t" \
  "v_exp_f16_sdwa v224, v225 dst_sel:WORD_1 dst_unused:UNUSED_PRESERVE src0_sel:WORD_1\n\t" \
  "v_mfma_f32_16x16x32_bf16 v[156:159], v[220:223], v[116:119], v[156:159]\n\t" \
  "ds_read_b64_tr_b16 v[218:219], v250 offset:6992\n\t" \
  "v_lshlrev_b32_sdwa v161, 1, v161 dst_sel:DWORD dst_unused:UNUSED_PAD src0_sel:DWORD src1_sel:WORD_1\n\t" \
  "v_mfma_f32_16x16x32_bf16 v[164:167], v[220:223], v[128:131], v[164:167]\n\t" \
  "ds_read_u16_d16_hi v160, v160 offset:0\n\t" \
  "v_pk_fma_f16 v224, v224, %[hc], %[hc]\n\t" \
  "v_mfma_f32_16x16x32_bf16 v[172:175], v[220:223], v[140:143], v[172:175]\n\t" \
  "s_waitcnt lgkmcnt(10)\n\t" \
  "ds_read_b64_tr_b16 v[220:221], v250 offset:6232\n\t" \
  "v_lshlrev_b32_sdwa v162, 1, v162 dst_sel:DWORD dst_unused:UNUSED_PAD src0_sel:DWORD src1_sel:WORD_1\n\t" \
  "v_smfmac_f32_16x16x64_bf16 v[0:3], v[208:211], v[176:183], v248\n\t" \
  "ds_read_u16_d16_hi v161, v161 offset:0\n\t" \
  "v_rcp_f16 v225, v224\n\t" \
  "v_smfmac_f32_16x16x64_bf16 v[24:27], v[208:211], v[184:191], v248\n\t" \
  "ds_read_b64_tr_b16 v[222:223], v250 offset:7000\n\t" \
  "v_lshlrev_b32_sdwa v163, 1, v163 dst_sel:DWORD dst_unused:UNUSED_PAD src0_sel:DWORD src1_sel:WORD_1\n\t" \
  "v_smfmac_f32_16x16x64_bf16 v[48:51], v[208:211], v[192:199], v248\n\t" \
  "ds_read_u16_d16_hi v162, v162 offset:0\n\t" \
  "v_lshlrev_b32_sdwa v168, 1, v168 dst_sel:DWORD dst_unused:UNUSED_PAD src0_sel:DWORD src1_sel:WORD_1\n\t" \
  "v_smfmac_f32_16x16x64_bf16 v[72:75], v[208:211], v[200:207], v248\n\t" \
  "s_waitcnt lgkmcnt(10)\n\t" \
  "ds_read_b64_tr_b16 v[208:209], v250 offset:6288\n\t" \
  "v_rcp_f16_sdwa v224, v224 dst_sel:DWORD dst_unused:UNUSED_PAD src0_sel:WORD_1\n\t" \
  "v_smfmac_f32_16x16x64_bf16 v[4:7], v[212:215], v[176:183], v248\n\t" \
  "ds_read_u16_d16_hi v163, v163 offset:0\n\t" \
  "v_lshlrev_b32_sdwa v169, 1, v169 dst_sel:DWORD dst_unused:UNUSED_PAD src0_sel:DWORD src1_sel:WORD_1\n\t" \
  "v_smfmac_f32_16x16x64_bf16 v[28:31], v[212:215], v[184:191], v248\n\t" \
  "ds_read_b64_tr_b16 v[210:211], v250 offset:7056\n\t" \
  "v_fma_mix_f32 v152, v152, v225, 0 op_sel_hi:[0,1,0]\n\t" \
  "v_smfmac_f32_16x16x64_bf16 v[52:55], v[212:215], v[192:199], v248\n\t" \
  "ds_read_u16_d16_hi v168, v168 offset:0\n\t" \
  "v_lshlrev_b32_sdwa v170, 1, v170 dst_sel:DWORD dst_unused:UNUSED_PAD src0_sel:DWORD src1_sel:WORD_1\n\t" \
  "v_smfmac_f32_16x16x64_bf16 v[76:79], v[212:215], v[200:207], v248\n\t" \
  "s_waitcnt lgkmcnt(9)\n\t" \
  "ds_read_b64_tr_b16 v[212:213], v250 offset:6296\n\t" \
  "v_fma_mix_f32 v153, v153, v224, 0 op_sel_hi:[0,1,0]\n\t" \
  "v_smfmac_f32_16x16x64_bf16 v[8:11], v[216:219], v[176:183], v248\n\t" \
  "ds_read_u16_d16_hi v169, v169 offset:0\n\t" \
  "v_lshlrev_b32_sdwa v171, 1, v171 dst_sel:DWORD dst_unused:UNUSED_PAD src0_sel:DWORD src1_sel:WORD_1\n\t" \
  "v_smfmac_f32_16x16x64_bf16 v[32:35], v[216:219], v[184:191], v248\n\t" \
  "ds_read_b64_tr_b16 v[214:215], v250 offset:7064\n\t" \
  "v_mul_f32 v156, %[kk], v156\n\t" \
  "v_smfmac_f32_16x16x64_bf16 v[56:59], v[216:219], v[192:199], v248\n\t" \
  "ds_read_u16_d16_hi v170, v170 offset:0\n\t" \
  "v_lshlrev_b32_sdwa v148, 1, v148 dst_sel:DWORD dst_unused:UNUSED_PAD src0_sel:DWORD src1_sel:WORD_1\n\t" \
  "v_smfmac_f32_16x16x64_bf16 v[80:83], v[216:219], v[200:207], v248\n\t" \
  "s_waitcnt lgkmcnt(9)\n\t" \
  "s_barrier\n\t" \
  "ds_read_b64_tr_b16 v[216:217], v249 offset:12288\n\t" \
  "v_mul_f32 v157, %[kk], v157\n\t" \
  "v_smfmac_f32_16x16x64_bf16 v[12:15], v[220:223], v[176:183], v248\n\t" \
  "ds_read_u16_d16_hi v171, v171 offset:0\n\t" \
  "v_cvt_pk_f16_f32 v224, v156, v157\n\t" \
  "v_smfmac_f32_16x16x64_bf16 v[36:39], v[220:223], v[184:191], v248\n\t" \
  "ds_read_b64_tr_b16 v[218:219], v249 offset:12544\n\t" \
  "v_lshlrev_b32_sdwa v149, 1, v149 dst_sel:DWORD dst_unused:UNUSED_PAD src0_sel:DWORD src1_sel:WORD_1\n\t" \
  "v_smfmac_f32_16x16x64_bf16 v[60:63], v[220:223], v[192:199], v248\n\t" \
  "ds_read_u16_d16_hi v148, v148 offset:0\n\t" \
  "v_pk_fma_f16 v225, v224, v224, %[h1] neg_lo:[1,0,0] neg_hi:[1,0,0]\n\t" \
  "v_smfmac_f32_16x16x64_bf16 v[84:87], v[220:223], v[200:207], v248\n\t" \
  "s_waitcnt lgkmcnt(9)\n\t" \
  "ds_read_b64_tr_b16 v[220:221], v249 offset:14336\n\t" \
  "v_lshlrev_b32_sdwa v150, 1, v150 dst_sel:DWORD dst_unused:UNUSED_PAD src0_sel:DWORD src1_sel:WORD_1\n\t" \
  "v_smfmac_f32_16x16x64_bf16 v[16:19], v[208:211], v[176:183], v248\n\t" \
  "s_add_u32 %[rs], %[rs], 1\n\t" \
  "ds_read_u16_d16_hi v149, v149 offset:0\n\t" \
  "v_pk_mul_f16 v225, v224, v225\n\t" \
  "v_smfmac_f32_16x16x64_bf16 v[40:43], v[208:211], v[184:191], v248\n\t" \
  "s_cmp_eq_u32 %[rs], 6\n\t" \
  "ds_read_b64_tr_b16 v[222:223], v249 offset:14592\n\t" \
  "v_lshlrev_b32_sdwa v151, 1, v151 dst_sel:DWORD dst_unused:UNUSED_PAD src0_sel:DWORD src1_sel:WORD_1\n\t" \
  "v_smfmac_f32_16x16x64_bf16 v[64:67], v[208:211], v[192:199], v248\n\t" \
  "ds_read_u16_d16_hi v150, v150 offset:0\n\t" \
  "v_exp_f16 v224, v225\n\t" \
  "v_smfmac_f32_16x16x64_bf16 v[88:91], v[208:211], v[200:207], v248\n\t" \
  "s_waitcnt lgkmcnt(9)\n\t" \
  "ds_read_b64_tr_b16 v[208:209], v249 offset:16384\n\t" \
  "v_lshlrev_b32_sdwa v158, 1, v158 dst_sel:DWORD dst_unused:UNUSED_PAD src0_sel:DWORD src1_sel:WORD_1\n\t" \
  "v_smfmac_f32_16x16x64_bf16 v[20:23], v[212:215], v[176:183], v248\n\t" \
  "s_cselect_b32 %[rs], 0, %[rs]\n\t" \
  "ds_read_u16_d16_hi v151, v151 offset:0\n\t" \
  "v_lshlrev_b32_sdwa v159, 1, v159 dst_sel:DWORD dst_unused:UNUSED_PAD src0_sel:DWORD src1_sel:WORD_1\n\t" \
  "v_smfmac_f32_16x16x64_bf16 v[44:47], v[212:215], v[184:191], v248\n\t" \
  "s_cselect_b32 %[t0], -75920, %[ps]\n\t" \
  "ds_read_b64_tr_b16 v[210:211], v249 offset:16640\n\t" \
  "v_exp_f16_sdwa v224, v225 dst_sel:WORD_1 dst_unused:UNUSED_PRESERVE src0_sel:WORD_1\n\t" \
  "v_smfmac_f32_16x16x64_bf16 v[68:71], v[212:215], v[192:199], v248\n\t" \
  "ds_read_u16_d16_hi v158, v158 offset:0\n\t" \
  "v_add_u32 v252, %[t0], v252\n\t" \
  "v_smfmac_f32_16x16x64_bf16 v[92:95], v[212:215], v[200:207], v248\n\t" \
  "s_waitcnt lgkmcnt(9)\n\t" \
  "ds_read_b64_tr_b16 v[212:213], v249 offset:12800\n\t" \
  "v_lshlrev_b32_sdwa v164, 1, v164 dst_sel:DWORD dst_unused:UNUSED_PAD src0_sel:DWORD src1_sel:WORD_1\n\t" \
  "v_mfma_f32_16x16x32_bf16 v[176:179], v[216:219], v[96:99], 0\n\t" \
  "ds_read_u16_d16_hi v159, v159 offset:0\n\t" \
  "v_pk_fma_f16 v224, v224, %[hc], %[hc]\n\t" \
  "v_mfma_f32_16x16x32_bf16 v[184:187], v[216:219], v[108:111], 0\n\t" \
  "ds_read_b64_tr_b16 v[214:215], v249 offset:13056\n\t" \
  "v_lshlrev_b32_sdwa v165, 1, v165 dst_sel:DWORD dst_unused:UNUSED_PAD src0_sel:DWORD src1_sel:WORD_1\n\t" \
  "v_mfma_f32_16x16x32_bf16 v[192:195], v[216:219], v[120:123], 0\n\t" \
  "ds_read_u16_d16_hi v164, v164 offset:0\n\t" \
  "v_rcp_f16 v225, v224\n\t" \
  "v_mfma_f32_16x16x32_bf16 v[200:203], v[216:219], v[132:135], 0\n\t" \
  "s_waitcnt lgkmcnt(9)\n\t" \
  "ds_read_b64_tr_b16 v[216:217], v249 offset:14848\n\t" \
  "v_lshlrev_b32_sdwa v166, 1, v166 dst_sel:DWORD dst_unused:UNUSED_PAD src0_sel:DWORD src1_sel:WORD_1\n\t" \
  "v_mfma_f32_16x16x32_bf16 v[176:179], v[220:223], v[100:103], v[176:179]\n\t" \
  "ds_read_u16_d16_hi v165, v165 offset:0\n\t" \
  "v_lshlrev_b32_sdwa v167, 1, v167 dst_sel:DWORD dst_unused:UNUSED_PAD src0_sel:DWORD src1_sel:WORD_1\n\t" \
  "v_mfma_f32_16x16x32_bf16 v[184:187], v[220:223], v[112:115], v[184:187]\n\t" \
  "ds_read_b64_tr_b16 v[218:219], v249 offset:15104\n\t" \
  "v_rcp_f16_sdwa v224, v224 dst_sel:DWORD dst_unused:UNUSED_PAD src0_sel:WORD_1\n\t" \
  "v_mfma_f32_16x16x32_bf16 v[192:195], v[220:223], v[124:127], v[192:195]\n\t" \
  "ds_read_u16_d16_hi v166, v166 offset:0\n\t" \
  "v_lshlrev_b32_sdwa v172, 1, v172 dst_sel:DWORD dst_unused:UNUSED_PAD src0_sel:DWORD src1_sel:WORD_1\n\t" \
  "v_mfma_f32_16x16x32_bf16 v[200:203], v[220:223], v[136:139], v[200:203]\n\t" \
  "s_waitcnt lgkmcnt(9)\n\t" \
  "ds_read_b64_tr_b16 v[220:221], v249 offset:16896\n\t" \
  "v_lshlrev_b32_sdwa v173, 1, v173 dst_sel:DWORD dst_unused:UNUSED_PAD src0_sel:DWORD src1_sel:WORD_1\n\t" \
  "v_mfma_f32_16x16x32_bf16 v[176:179], v[208:211], v[104:107], v[176:179]\n\t" \
  "ds_read_u16_d16_hi v167, v167 offset:0\n\t" \
  "v_fma_mix_f32 v156, v156, v225, 0 op_sel_hi:[0,1,0]\n\t" \
  "v_mfma_f32_16x16x32_bf16 v[184:187], v[208:211], v[116:119], v[184:187]\n\t" \
  "ds_read_b64_tr_b16 v[222:223], v249 offset:17152\n\t" \
  "v_fma_mix_f32 v157, v157, v224, 0 op_sel_hi:[0,1,0]\n\t" \
  "v_mfma_f32_16x16x32_bf16 v[192:195], v[208:211], v[128:131], v[192:195]\n\t" \
  "ds_read_u16_d16_hi v172, v172 offset:0\n\t" \
  "v_lshlrev_b32_sdwa v174, 1, v174 dst_sel:DWORD dst_unused:UNUSED_PAD src0_sel:DWORD src1_sel:WORD_1\n\t" \
  "v_mfma_f32_16x16x32_bf16 v[200:203], v[208:211], v[140:143], v[200:203]\n\t" \
  "s_waitcnt lgkmcnt(9)\n\t" \
  "ds_read_b64_tr_b16 v[208:209], v250 offset:12320\n\t" \
  "v_lshlrev_b32_sdwa v175, 1, v175 dst_sel:DWORD dst_unused:UNUSED_PAD src0_sel:DWORD src1_sel:WORD_1\n\t" \
  "v_mfma_f32_16x16x32_bf16 v[180:183], v[212:215], v[96:99], 0\n\t" \
  "ds_read_u16_d16_hi v173, v173 offset:0\n\t" \
  "v_lshlrev_b32_sdwa v176, 1, v176 dst_sel:DWORD dst_unused:UNUSED_PAD src0_sel:DWORD src1_sel:WORD_1\n\t" \
  "v_mfma_f32_16x16x32_bf16 v[188:191], v[212:215], v[108:111], 0\n\t" \
  "ds_read_b64_tr_b16 v[210:211], v250 offset:13088\n\t" \
  "v_mul_f32 v184, %[kk], v184\n\t" \
  "v_mfma_f32_16x16x32_bf16 v[196:199], v[212:215], v[120:123], 0\n\t" \
  "ds_read_u16_d16_hi v174, v174 offset:0\n\t" \
  "v_lshlrev_b32_sdwa v177, 1, v177 dst_sel:DWORD dst_unused:UNUSED_PAD src0_sel:DWORD src1_sel:WORD_1\n\t" \
  "v_mul_f32 v185, %[kk], v185\n\t" \
  "v_mfma_f32_16x16x32_bf16 v[204:207], v[212:215], v[132:135], 0\n\t" \
  "s_waitcnt lgkmcnt(9)\n\t" \
  "ds_read_b64_tr_b16 v[212:213], v250 offset:12328\n\t" \
  "ds_read_u16_d16_hi v175, v175 offset:0\n\t" \
  "v_cvt_pk_f16_f32 v224, v184, v185\n\t" \
  "v_mfma_f32_16x16x32_bf16 v[180:183], v[216:219], v[100:103], v[180:183]\n\t" \
  "ds_read_b64_tr_b16 v[214:215], v250 offset:13096\n\t" \
  "v_lshlrev_b32_sdwa v178, 1, v178 dst_sel:DWORD dst_unused:UNUSED_PAD src0_sel:DWORD src1_sel:WORD_1\n\t" \
  "v_mfma_f32_16x16x32_bf16 v[188:191], v[216:219], v[112:115], v[188:191]\n\t" \
  "ds_read_u16_d16_hi v176, v176 offset:0\n\t" \
  "v_pk_fma_f16 v225, v224, v224, %[h1] neg_lo:[1,0,0] neg_hi:[1,0,0]\n\t" \
  "v_mfma_f32_16x16x32_bf16 v[196:199], v[216:219], v[124:127], v[196:199]\n\t" \
  "ds_read_u16_d16_hi v177, v177 offset:0\n\t" \
  "v_lshlrev_b32_sdwa v179, 1, v179 dst_sel:DWORD dst_unused:UNUSED_PAD src0_sel:DWORD src1_sel:WORD_1\n\t" \
  "v_pk_mul_f16 v225, v224, v225\n\t" \
  "v_mfma_f32_16x16x32_bf16 v[204:207], v[216:219], v[136:139], v[204:207]\n\t" \
  "s_waitcnt lgkmcnt(10)\n\t" \
  "ds_read_b64_tr_b16 v[216:217], v250 offset:12384\n\t" \
  "v_lshlrev_b32_sdwa v186, 1, v186 dst_sel:DWORD dst_unused:UNUSED_PAD src0_sel:DWORD src1_sel:WORD_1\n\t" \
  "v_mfma_f32_16x16x32_bf16 v[180:183], v[220:223], v[104:107], v[180:183]\n\t" \
  "ds_read_u16_d16_hi v178, v178 offset:0\n\t" \
  "v_exp_f16 v224, v225\n\t" \
  "v_mfma_f32_16x16x32_bf16 v[188:191], v[220:223], v[116:119], v[188:191]\n\t" \
  "ds_read_b64_tr_b16 v[218:219], v250 offset:13152\n\t" \
  "v_lshlrev_b32_sdwa v187, 1, v187 dst_sel:DWORD dst_unused:UNUSED_PAD src0_sel:DWORD src1_sel:WORD_1\n\t" \
  "v_mfma_f32_16x16x32_bf16 v[196:199], v[220:223], v[128:131], v[196:199]\n\t" \
  "ds_read_u16_d16_hi v179, v179 offset:0\n\t" \
  "v_lshlrev_b32_sdwa v192, 1, v192 dst_sel:DWORD dst_unused:UNUSED_PAD src0_sel:DWORD src1_sel:WORD_1\n\t" \
  "v_mfma_f32_16x16x32_bf16 v[204:207], v[220:223], v[140:143], v[204:207]\n\t" \
  "s_waitcnt lgkmcnt(10)\n\t" \
  "ds_read_b64_tr_b16 v[220:221], v250 offset:12392\n\t" \
  "ds_read_u16_d16_hi v186, v186 offset:0\n\t" \
  "v_exp_f16_sdwa v224, v225 dst_sel:WORD_1 dst_unused:UNUSED_PRESERVE src0_sel:WORD_1\n\t" \
  "v_smfmac_f32_16x16x64_bf16 v[0:3], v[208:211], v[144:151], v248\n\t" \
  "ds_read_b64_tr_b16 v[222:223], v250 offset:13160\n\t" \
  "v_lshlrev_b32_sdwa v193, 1, v193 dst_sel:DWORD dst_unused:UNUSED_PAD src0_sel:DWORD src1_sel:WORD_1\n\t" \
  "v_smfmac_f32_16x16x64_bf16 v[24:27], v[208:211], v[152:159], v248\n\t" \
  "ds_read_u16_d16_hi v187, v187 offset:0\n\t" \
  "v_pk_fma_f16 v224, v224, %[hc], %[hc]\n\t" \
  "v_smfmac_f32_16x16x64_bf16 v[48:51], v[208:211], v[160:167], v248\n\t" \
  "s_waitcnt lgkmcnt(11)\n\t" \
  "ds_read_u16_d16_hi v192, v192 offset:0\n\t" \
  "v_lshlrev_b32_sdwa v194, 1, v194 dst_sel:DWORD dst_unused:UNUSED_PAD src0_sel:DWORD src1_sel:WORD_1\n\t" \
  "v_smfmac_f32_16x16x64_bf16 v[72:75], v[208:211], v[168:175], v248\n\t" \
  "s_waitcnt lgkmcnt(11)\n\t" \
  "ds_read_b64_tr_b16 v[208:209], v250 offset:12448\n\t" \
  "v_rcp_f16 v225, v224\n\t" \
  "v_smfmac_f32_16x16x64_bf16 v[4:7], v[212:215], v[144:151], v248\n\t" \
  "ds_read_u16_d16_hi v193, v193 offset:0\n\t" \
  "v_lshlrev_b32_sdwa v195, 1, v195 dst_sel:DWORD dst_unused:UNUSED_PAD src0_sel:DWORD src1_sel:WORD_1\n\t" \
  "v_smfmac_f32_16x16x64_bf16 v[28:31], v[212:215], v[152:159], v248\n\t" \
  "ds_read_b64_tr_b16 v[210:211], v250 offset:13216\n\t" \
  "v_lshlrev_b32_sdwa v200, 1, v200 dst_sel:DWORD dst_unused:UNUSED_PAD src0_sel:DWORD src1_sel:WORD_1\n\t" \
  "v_smfmac_f32_16x16x64_bf16 v[52:55], v[212:215], v[160:167], v248\n\t" \
  "ds_read_u16_d16_hi v194, v194 offset:0\n\t" \
  "v_rcp_f16_sdwa v224, v224 dst_sel:DWORD dst_unused:UNUSED_PAD src0_sel:WORD_1\n\t" \
  "v_smfmac_f32_16x16x64_bf16 v[76:79], v[212:215], v[168:175], v248\n\t" \
  "s_waitcnt lgkmcnt(10)\n\t" \
  "ds_read_b64_tr_b16 v[212:213], v250 offset:12456\n\t" \
  "v_lshlrev_b32_sdwa v201, 1, v201 dst_sel:DWORD dst_unused:UNUSED_PAD src0_sel:DWORD src1_sel:WORD_1\n\t" \
  "v_smfmac_f32_16x16x64_bf16 v[8:11], v[216:219], v[144:151], v248\n\t" \
  "ds_read_u16_d16_hi v195, v195 offset:0\n\t" \
  "v_fma_mix_f32 v184, v184, v225, 0 op_sel_hi:[0,1,0]\n\t" \
  "v_smfmac_f32_16x16x64_bf16 v[32:35], v[216:219], v[152:159], v248\n\t" \
  "ds_read_b64_tr_b16 v[214:215], v250 offset:13224\n\t" \
  "v_lshlrev_b32_sdwa v202, 1, v202 dst_sel:DWORD dst_unused:UNUSED_PAD src0_sel:DWORD src1_sel:WORD_1\n\t" \
  "v_smfmac_f32_16x16x64_bf16 v[56:59], v[216:219], v[160:167], v248\n\t" \
  "ds_read_u16_d16_hi v200, v200 offset:0\n\t" \
  "v_fma_mix_f32 v185, v185, v224, 0 op_sel_hi:[0,1,0]\n\t" \
  "v_smfmac_f32_16x16x64_bf16 v[80:83], v[216:219], v[168:175], v248\n\t" \
  "s_waitcnt lgkmcnt(10)\n\t" \
  "s_barrier\n\t" \
  "s_mov_b32 %[lc], 3\n\t" \
  ".Lcnx_m:\n\t" \
  "ds_read_b64_tr_b16 v[216:217], v249 offset:0\n\t" \
  "ds_read_b128 v[96:99], v251 offset:0\n\t" \
  "v_lshlrev_b32_sdwa v203, 1, v203 dst_sel:DWORD dst_unused:UNUSED_PAD src0_sel:DWORD src1_sel:WORD_1\n\t" \
  "v_smfmac_f32_16x16x64_bf16 v[12:15], v[220:223], v[144:151], v248\n\t" \
  "ds_read_b64_tr_b16 v[218:219], v249 offset:256\n\t" \
  "ds_read_b128 v[100:103], v251 offset:4096\n\t" \
  "v_mul_f32 v188, %[kk], v188\n\t" \
  "v_smfmac_f32_16x16x64_bf16 v[36:39], v[220:223], v[152:159], v248\n\t" \
  "ds_read_b128 v[104:107], v251 offset:8192\n\t" \
  "v_lshlrev_b32_sdwa v180, 1, v180 dst_sel:DWORD dst_unused:UNUSED_PAD src0_sel:DWORD src1_sel:WORD_1\n\t" \
  "v_mul_f32 v189, %[kk], v189\n\t" \
  "v_smfmac_f32_16x16x64_bf16 v[60:63], v[220:223], v[160:167], v248\n\t" \
  "ds_read_u16_d16_hi v201, v201 offset:0\n\t" \
  "v_cvt_pk_f16_f32 v224, v188, v189\n\t" \
  "v_smfmac_f32_16x16x64_bf16 v[84:87], v[220:223], v[168:175], v248\n\t" \
  "s_waitcnt lgkmcnt(11)\n\t" \
  "ds_read_b64_tr_b16 v[220:221], v249 offset:2048\n\t" \
  "ds_read_u16_d16_hi v202, v202 offset:0\n\t" \
  "v_lshlrev_b32_sdwa v181, 1, v181 dst_sel:DWORD dst_unused:UNUSED_PAD src0_sel:DWORD src1_sel:WORD_1\n\t" \
  "v_smfmac_f32_16x16x64_bf16 v[16:19], v[208:211], v[144:151], v248\n\t" \
  "ds_read_b64_tr_b16 v[222:223], v249 offset:2304\n\t" \
  "v_pk_fma_f16 v225, v224, v224, %[h1] neg_lo:[1,0,0] neg_hi:[1,0,0]\n\t" \
  "v_smfmac_f32_16x16x64_bf16 v[40:43], v[208:211], v[152:159], v248\n\t" \
  "ds_read_u16_d16_hi v203, v203 offset:0\n\t" \
  "v_lshlrev_b32_sdwa v182, 1, v182 dst_sel:DWORD dst_unused:UNUSED_PAD src0_sel:DWORD src1_sel:WORD_1\n\t" \
  "v_pk_mul_f16 v225, v224, v225\n\t" \
  "v_smfmac_f32_16x16x64_bf16 v[64:67], v[208:211], v[160:167], v248\n\t" \
  "ds_read_u16_d16_hi v180, v180 offset:0\n\t" \
  "v_lshlrev_b32_sdwa v183, 1, v183 dst_sel:DWORD dst_unused:UNUSED_PAD src0_sel:DWORD src1_sel:WORD_1\n\t" \
  "v_smfmac_f32_16x16x64_bf16 v[88:91], v[208:211], v[168:175], v248\n\t" \
  "s_waitcnt lgkmcnt(12)\n\t" \
  "ds_read_b64_tr_b16 v[208:209], v249 offset:4096\n\t" \
  "ds_read_u16_d16_hi v181, v181 offset:0\n\t" \
  "v_exp_f16 v224, v225\n\t" \
  "v_smfmac_f32_16x16x64_bf16 v[20:23], v[212:215], v[144:151], v248\n\t" \
  "ds_read_b64_tr_b16 v[210:211], v249 offset:4352\n\t" \
  "v_lshlrev_b32_sdwa v190, 1, v190 dst_sel:DWORD dst_unused:UNUSED_PAD src0_sel:DWORD src1_sel:WORD_1\n\t" \
  "v_smfmac_f32_16x16x64_bf16 v[44:47], v[212:215], v[152:159], v248\n\t" \
  "ds_read_u16_d16_hi v182, v182 offset:0\n\t" \
  "v_lshlrev_b32_sdwa v191, 1, v191 dst_sel:DWORD dst_unused:UNUSED_PAD src0_sel:DWORD src1_sel:WORD_1\n\t" \
  "v_smfmac_f32_16x16x64_bf16 v[68:71], v[212:215], v[160:167], v248\n\t" \
  "ds_read_u16_d16_hi v183, v183 offset:0\n\t" \
  "v_exp_f16_sdwa v224, v225 dst_sel:WORD_1 dst_unused:UNUSED_PRESERVE src0_sel:WORD_1\n\t" \
  "v_smfmac_f32_16x16x64_bf16 v[92:95], v[212:215], v[168:175], v248\n\t" \
  "s_waitcnt lgkmcnt(13)\n\t" \
  "ds_read_b64_tr_b16 v[212:213], v249 offset:512\n\t" \
  "v_lshlrev_b32_sdwa v196, 1, v196 dst_sel:DWORD dst_unused:UNUSED_PAD src0_sel:DWORD src1_sel:WORD_1\n\t" \
  "v_mfma_f32_16x16x32_bf16 v[144:147], v[216:219], v[96:99], 0\n\t" \
  "ds_read_u16_d16_hi v190, v190 offset:0\n\t" \
  "v_pk_fma_f16 v224, v224, %[hc], %[hc]\n\t" \
  "v_mfma_f32_16x16x32_bf16 v[152:155], v[216:219], v[108:111], 0\n\t" \
  "ds_read_b64_tr_b16 v[214:215], v249 offset:768\n\t" \
  "v_lshlrev_b32_sdwa v197, 1, v197 dst_sel:DWORD dst_unused:UNUSED_PAD src0_sel:DWORD src1_sel:WORD_1\n\t" \
  "v_mfma_f32_16x16x32_bf16 v[160:163], v[216:219], v[120:123], 0\n\t" \
  "ds_read_u16_d16_hi v191, v191 offset:0\n\t" \
  "v_rcp_f16 v225, v224\n\t" \
  "v_mfma_f32_16x16x32_bf16 v[168:171], v[216:219], v[132:135], 0\n\t" \
  "s_waitcnt lgkmcnt(11)\n\t" \
  "ds_read_b64_tr_b16 v[216:217], v249 offset:2560\n\t" \
  "v_lshlrev_b32_sdwa v198, 1, v198 dst_sel:DWORD dst_unused:UNUSED_PAD src0_sel:DWORD src1_sel:WORD_1\n\t" \
  "v_mfma_f32_16x16x32_bf16 v[144:147], v[220:223], v[100:103], v[144:147]\n\t" \
  "ds_read_u16_d16_hi v196, v196 offset:0\n\t" \
  "v_lshlrev_b32_sdwa v199, 1, v199 dst_sel:DWORD dst_unused:UNUSED_PAD src0_sel:DWORD src1_sel:WORD_1\n\t" \
  "v_mfma_f32_16x16x32_bf16 v[152:155], v[220:223], v[112:115], v[152:155]\n\t" \
  "ds_read_b64_tr_b16 v[218:219], v249 offset:2816\n\t" \
  "v_rcp_f16_sdwa v224, v224 dst_sel:DWORD dst_unused:UNUSED_PAD src0_sel:WORD_1\n\t" \
  "v_mfma_f32_16x16x32_bf16 v[160:163], v[220:223], v[124:127], v[160:163]\n\t" \
  "ds_read_u16_d16_hi v197, v197 offset:0\n\t" \
  "v_lshlrev_b32_sdwa v204, 1, v204 dst_sel:DWORD dst_unused:UNUSED_PAD src0_sel:DWORD src1_sel:WORD_1\n\t" \
  "v_mfma_f32_16x16x32_bf16 v[168:171], v[220:223], v[136:139], v[168:171]\n\t" \
  "s_waitcnt lgkmcnt(10)\n\t" \
  "ds_read_b64_tr_b16 v[220:221], v249 offset:4608\n\t" \
  "ds_read_u16_d16_hi v198, v198 offset:0\n\t" \
  "v_mfma_f32_16x16x32_bf16 v[144:147], v[208:211], v[104:107], v[144:147]\n\t" \
  "ds_read_b64_tr_b16 v[222:223], v249 offset:4864\n\t" \
  "v_lshlrev_b32_sdwa v205, 1, v205 dst_sel:DWORD dst_unused:UNUSED_PAD src0_sel:DWORD src1_sel:WORD_1\n\t" \
  "v_fma_mix_f32 v188, v188, v225, 0 op_sel_hi:[0,1,0]\n\t" \
  "v_mfma_f32_16x16x32_bf16 v[152:155], v[208:211], v[116:119], v[152:155]\n\t" \
  "ds_read_u16_d16_hi v199, v199 offset:0\n\t" \
  "v_fma_mix_f32 v189, v189, v224, 0 op_sel_hi:[0,1,0]\n\t" \
  "v_mfma_f32_16x16x32_bf16 v[160:163], v[208:211], v[128:131], v[160:163]\n\t" \
  "ds_read_u16_d16_hi v204, v204 offset:0\n\t" \
  "v_lshlrev_b32_sdwa v206, 1, v206 dst_sel:DWORD dst_unused:UNUSED_PAD src0_sel:DWORD src1_sel:WORD_1\n\t" \
  "v_mfma_f32_16x16x32_bf16 v[168:171], v[208:211], v[140:143], v[168:171]\n\t" \
  "s_waitcnt lgkmcnt(10)\n\t" \
  "ds_read_b64_tr_b16 v[208:209], v250 offset:18480\n\t" \
  "v_lshlrev_b32_sdwa v207, 1, v207 dst_sel:DWORD dst_unused:UNUSED_PAD src0_sel:DWORD src1_sel:WORD_1\n\t" \
  "v_mfma_f32_16x16x32_bf16 v[148:151], v[212:215], v[96:99], 0\n\t" \
  "ds_read_u16_d16_hi v205, v205 offset:0\n\t" \
  "v_lshlrev_b32_sdwa v144, 1, v144 dst_sel:DWORD dst_unused:UNUSED_PAD src0_sel:DWORD src1_sel:WORD_1\n\t" \
  "v_mfma_f32_16x16x32_bf16 v[156:159], v[212:215], v[108:111], 0\n\t" \
  "ds_read_b64_tr_b16 v[210:211], v250 offset:19248\n\t" \
  "v_mul_f32 v152, %[kk], v152\n\t" \
  "v_mfma_f32_16x16x32_bf16 v[164:167], v[212:215], v[120:123], 0\n\t" \
  "ds_read_u16_d16_hi v206, v206 offset:0\n\t" \
  "v_lshlrev_b32_sdwa v145, 1, v145 dst_sel:DWORD dst_unused:UNUSED_PAD src0_sel:DWORD src1_sel:WORD_1\n\t" \
  "v_mul_f32 v153, %[kk], v153\n\t" \
  "v_mfma_f32_16x16x32_bf16 v[172:175], v[212:215], v[132:135], 0\n\t" \
  "s_waitcnt lgkmcnt(10)\n\t" \
  "ds_read_b64_tr_b16 v[212:213], v250 offset:18488\n\t" \
  "ds_read_u16_d16_hi v207, v207 offset:0\n\t" \
  "v_cvt_pk_f16_f32 v224, v152, v153\n\t" \
  "v_mfma_f32_16x16x32_bf16 v[148:151], v[216:219], v[100:103], v[148:151]\n\t" \
  "ds_read_b64_tr_b16 v[214:215], v250 offset:19256\n\t" \
  "v_lshlrev_b32_sdwa v146, 1, v146 dst_sel:DWORD dst_unused:UNUSED_PAD src0_sel:DWORD src1_sel:WORD_1\n\t" \
  "v_mfma_f32_16x16x32_bf16 v[156:159], v[216:219], v[112:115], v[156:159]\n\t" \
  "ds_read_u16_d16_hi v144, v144 offset:0\n\t" \
  "v_pk_fma_f16 v225, v224, v224, %[h1] neg_lo:[1,0,0] neg_hi:[1,0,0]\n\t" \
  "v_mfma_f32_16x16x32_bf16 v[164:167], v[216:219], v[124:127], v[164:167]\n\t" \
  "ds_read_u16_d16_hi v145, v145 offset:0\n\t" \
  "v_lshlrev_b32_sdwa v147, 1, v147 dst_sel:DWORD dst_unused:UNUSED_PAD src0_sel:DWORD src1_sel:WORD_1\n\t" \
  "v_pk_mul_f16 v225, v224, v225\n\t" \
  "v_mfma_f32_16x16x32_bf16 v[172:175], v[216:219], v[136:139], v[172:175]\n\t" \
  "s_waitcnt lgkmcnt(11)\n\t" \
  "ds_read_b64_tr_b16 v[216:217], v250 offset:18544\n\t" \
  "v_lshlrev_b32_sdwa v154, 1, v154 dst_sel:DWORD dst_unused:UNUSED_PAD src0_sel:DWORD src1_sel:WORD_1\n\t" \
  "v_mfma_f32_16x16x32_bf16 v[148:151], v[220:223], v[104:107], v[148:151]\n\t" \
  "ds_read_u16_d16_hi v146, v146 offset:0\n\t" \
  "v_exp_f16 v224, v225\n\t" \
  "v_mfma_f32_16x16x32_bf16 v[156:159], v[220:223], v[116:119], v[156:159]\n\t" \
  "ds_read_b64_tr_b16 v[218:219], v250 offset:19312\n\t" \
  "v_lshlrev_b32_sdwa v155, 1, v155 dst_sel:DWORD dst_unused:UNUSED_PAD src0_sel:DWORD src1_sel:WORD_1\n\t" \
  "v_mfma_f32_16x16x32_bf16 v[164:167], v[220:223], v[128:131], v[164:167]\n\t" \
  "ds_read_u16_d16_hi v147, v147 offset:0\n\t" \
  "v_lshlrev_b32_sdwa v160, 1, v160 dst_sel:DWORD dst_unused:UNUSED_PAD src0_sel:DWORD src1_sel:WORD_1\n\t" \
  "v_mfma_f32_16x16x32_bf16 v[172:175], v[220:223], v[140:143], v[172:175]\n\t" \
  "s_waitcnt lgkmcnt(10)\n\t" \
  "ds_read_b64_tr_b16 v[220:221], v250 offset:18552\n\t" \
  "ds_read_u16_d16_hi v154, v154 offset:0\n\t" \
  "v_exp_f16_sdwa v224, v225 dst_sel:WORD_1 dst_unused:UNUSED_PRESERVE src0_sel:WORD_1\n\t" \
  "v_smfmac_f32_16x16x64_bf16 v[0:3], v[208:211], v[176:183], v248\n\t" \
  "ds_read_b64_tr_b16 v[222:223], v250 offset:19320\n\t" \
  "v_lshlrev_b32_sdwa v161, 1, v161 dst_sel:DWORD dst_unused:UNUSED_PAD src0_sel:DWORD src1_sel:WORD_1\n\t" \
  "v_smfmac_f32_16x16x64_bf16 v[24:27], v[208:211], v[184:191], v248\n\t" \
  "ds_read_u16_d16_hi v155, v155 offset:0\n\t" \
  "v_pk_fma_f16 v224, v224, %[hc], %[hc]\n\t" \
  "v_smfmac_f32_16x16x64_bf16 v[48:51], v[208:211], v[192:199], v248\n\t" \
  "s_waitcnt lgkmcnt(11)\n\t" \
  "ds_read_u16_d16_hi v160, v160 offset:0\n\t" \
  "v_lshlrev_b32_sdwa v162, 1, v162 dst_sel:DWORD dst_unused:UNUSED_PAD src0_sel:DWORD src1_sel:WORD_1\n\t" \
  "v_smfmac_f32_16x16x64_bf16 v[72:75], v[208:211], v[200:207], v248\n\t" \
  "s_waitcnt lgkmcnt(11)\n\t" \
  "ds_read_b64_tr_b16 v[208:209], v250 offset:18608\n\t" \
  "v_rcp_f16 v225, v224\n\t" \
  "v_smfmac_f32_16x16x64_bf16 v[4:7], v[212:215], v[176:183], v248\n\t" \
  "ds_read_b64 v[242:243], v252 offset:128\n\t" \
  "v_lshlrev_b32_sdwa v163, 1, v163 dst_sel:DWORD dst_unused:UNUSED_PAD src0_sel:DWORD src1_sel:WORD_1\n\t" \
  "v_smfmac_f32_16x16x64_bf16 v[28:31], v[212:215], v[184:191], v248\n\t" \
  "ds_read_b64_tr_b16 v[210:211], v250 offset:19376\n\t" \
  "ds_read_b64 v[244:245], v252 offset:136\n\t" \
  "v_smfmac_f32_16x16x64_bf16 v[52:55], v[212:215], v[192:199], v248\n\t" \
  "ds_read_u16_d16_hi v161, v161 offset:0\n\t" \
  "v_lshlrev_b32_sdwa v168, 1, v168 dst_sel:DWORD dst_unused:UNUSED_PAD src0_sel:DWORD src1_sel:WORD_1\n\t" \
  "v_rcp_f16_sdwa v224, v224 dst_sel:DWORD dst_unused:UNUSED_PAD src0_sel:WORD_1\n\t" \
  "v_cvt_pk_bf16_f32 v230, v0, v1\n\t" \
  "v_smfmac_f32_16x16x64_bf16 v[76:79], v[212:215], v[200:207], v248\n\t" \
  "s_waitcnt lgkmcnt(11)\n\t" \
  "ds_read_b64_tr_b16 v[212:213], v250 offset:18616\n\t" \
  "ds_read_u16_d16_hi v162, v162 offset:0\n\t" \
  "v_cvt_pk_bf16_f32 v231, v2, v3\n\t" \
  "v_smfmac_f32_16x16x64_bf16 v[8:11], v[216:219], v[176:183], v248\n\t" \
  "ds_read_b64_tr_b16 v[214:215], v250 offset:19384\n\t" \
  "v_lshlrev_b32_sdwa v169, 1, v169 dst_sel:DWORD dst_unused:UNUSED_PAD src0_sel:DWORD src1_sel:WORD_1\n\t" \
  "v_cvt_pk_bf16_f32 v232, v4, v5\n\t" \
  "v_smfmac_f32_16x16x64_bf16 v[32:35], v[216:219], v[184:191], v248\n\t" \
  "ds_read_u16_d16_hi v163, v163 offset:0\n\t" \
  "v_fma_mix_f32 v152, v152, v225, 0 op_sel_hi:[0,1,0]\n\t" \
  "v_cvt_pk_bf16_f32 v233, v6, v7\n\t" \
  "v_smfmac_f32_16x16x64_bf16 v[56:59], v[216:219], v[192:199], v248\n\t" \
  "buffer_store_dwordx4 v[230:233], v253, %[osrd], %[ob] offen offset:0 sc1\n\t" \
  "ds_read_u16_d16_hi v168, v168 offset:0\n\t" \
  "v_lshlrev_b32_sdwa v170, 1, v170 dst_sel:DWORD dst_unused:UNUSED_PAD src0_sel:DWORD src1_sel:WORD_1\n\t" \
  "v_fma_mix_f32 v153, v153, v224, 0 op_sel_hi:[0,1,0]\n\t" \
  "v_smfmac_f32_16x16x64_bf16 v[80:83], v[216:219], v[200:207], v248\n\t" \
  "s_waitcnt lgkmcnt(12)\n\t" \
  "s_barrier\n\t" \
  "ds_read_b64_tr_b16 v[216:217], v249 offset:6144\n\t" \
  "ds_read_b64 v[230:231], v252 offset:0\n\t" \
  "v_lshlrev_b32_sdwa v171, 1, v171 dst_sel:DWORD dst_unused:UNUSED_PAD src0_sel:DWORD src1_sel:WORD_1\n\t" \
  "v_smfmac_f32_16x16x64_bf16 v[12:15], v[220:223], v[176:183], v248\n\t" \
  "ds_read_b64_tr_b16 v[218:219], v249 offset:6400\n\t" \
  "ds_read_b64 v[232:233], v252 offset:8\n\t" \
  "v_mul_f32 v156, %[kk], v156\n\t" \
  "v_smfmac_f32_16x16x64_bf16 v[36:39], v[220:223], v[184:191], v248\n\t" \
  "ds_read_u16_d16_hi v169, v169 offset:0\n\t" \
  "v_lshlrev_b32_sdwa v148, 1, v148 dst_sel:DWORD dst_unused:UNUSED_PAD src0_sel:DWORD src1_sel:WORD_1\n\t" \
  "v_mul_f32 v157, %[kk], v157\n\t" \
  "v_smfmac_f32_16x16x64_bf16 v[60:63], v[220:223], v[192:199], v248\n\t" \
  "ds_read_u16_d16_hi v170, v170 offset:0\n\t" \
  "v_cvt_pk_f16_f32 v224, v156, v157\n\t" \
  "v_cvt_pk_bf16_f32 v234, v8, v9\n\t" \
  "v_smfmac_f32_16x16x64_bf16 v[84:87], v[220:223], v[200:207], v248\n\t" \
  "s_waitcnt lgkmcnt(13)\n\t" \
  "ds_read_b64_tr_b16 v[220:221], v249 offset:8192\n\t" \
  "v_lshlrev_b32_sdwa v149, 1, v149 dst_sel:DWORD dst_unused:UNUSED_PAD src0_sel:DWORD src1_sel:WORD_1\n\t" \
  "v_cvt_pk_bf16_f32 v235, v10, v11\n\t" \
  "v_smfmac_f32_16x16x64_bf16 v[16:19], v[208:211], v[176:183], v248\n\t" \
  "ds_read_u16_d16_hi v171, v171 offset:0\n\t" \
  "v_pk_fma_f16 v225, v224, v224, %[h1] neg_lo:[1,0,0] neg_hi:[1,0,0]\n\t" \
  "v_cvt_pk_bf16_f32 v236, v12, v13\n\t" \
  "v_smfmac_f32_16x16x64_bf16 v[40:43], v[208:211], v[184:191], v248\n\t" \
  "ds_read_b64_tr_b16 v[222:223], v249 offset:8448\n\t" \
  "v_lshlrev_b32_sdwa v150, 1, v150 dst_sel:DWORD dst_unused:UNUSED_PAD src0_sel:DWORD src1_sel:WORD_1\n\t" \
  "v_pk_mul_f16 v225, v224, v225\n\t" \
  "v_cvt_pk_bf16_f32 v237, v14, v15\n\t" \
  "v_smfmac_f32_16x16x64_bf16 v[64:67], v[208:211], v[192:199], v248\n\t" \
  "buffer_store_dwordx4 v[234:237], v253, %[osrd], %[ob] offen offset:64 sc1\n\t" \
  "ds_read_u16_d16_hi v148, v148 offset:0\n\t" \
  "v_lshlrev_b32_sdwa v151, 1, v151 dst_sel:DWORD dst_unused:UNUSED_PAD src0_sel:DWORD src1_sel:WORD_1\n\t" \
  "v_smfmac_f32_16x16x64_bf16 v[88:91], v[208:211], v[200:207], v248\n\t" \
  "s_waitcnt lgkmcnt(12)\n\t" \
  "ds_read_b64_tr_b16 v[208:209], v249 offset:10240\n\t" \
  "ds_read_b64 v[234:235], v252 offset:64\n\t" \
  "v_exp_f16 v224, v225\n\t" \
  "v_smfmac_f32_16x16x64_bf16 v[20:23], v[212:215], v[176:183], v248\n\t" \
  "s_waitcnt lgkmcnt(10)\n\t" \
  "ds_read_b64_tr_b16 v[210:211], v249 offset:10496\n\t" \
  "ds_read_b64 v[236:237], v252 offset:72\n\t" \
  "v_mfma_f32_16x16x16_bf16 v[0:3], v[246:247], v[230:231], 0\n\t" \
  "ds_read_u16_d16_hi v149, v149 offset:0\n\t" \
  "v_lshlrev_b32_sdwa v158, 1, v158 dst_sel:DWORD dst_unused:UNUSED_PAD src0_sel:DWORD src1_sel:WORD_1\n\t" \
  "v_smfmac_f32_16x16x64_bf16 v[44:47], v[212:215], v[184:191], v248\n\t" \
  "ds_read_u16_d16_hi v150, v150 offset:0\n\t" \
  "v_lshlrev_b32_sdwa v159, 1, v159 dst_sel:DWORD dst_unused:UNUSED_PAD src0_sel:DWORD src1_sel:WORD_1\n\t" \
  "v_smfmac_f32_16x16x64_bf16 v[68:71], v[212:215], v[192:199], v248\n\t" \
  "ds_read_u16_d16_hi v151, v151 offset:0\n\t" \
  "v_exp_f16_sdwa v224, v225 dst_sel:WORD_1 dst_unused:UNUSED_PRESERVE src0_sel:WORD_1\n\t" \
  "v_cvt_pk_bf16_f32 v238, v16, v17\n\t" \
  "v_smfmac_f32_16x16x64_bf16 v[92:95], v[212:215], v[200:207], v248\n\t" \
  "s_waitcnt lgkmcnt(13)\n\t" \
  "ds_read_b64_tr_b16 v[212:213], v249 offset:6656\n\t" \
  "v_cvt_pk_bf16_f32 v239, v18, v19\n\t" \
  "v_mfma_f32_16x16x16_bf16 v[4:7], v[246:247], v[232:233], 0\n\t" \
  "ds_read_u16_d16_hi v158, v158 offset:0\n\t" \
  "v_lshlrev_b32_sdwa v164, 1, v164 dst_sel:DWORD dst_unused:UNUSED_PAD src0_sel:DWORD src1_sel:WORD_1\n\t" \
  "v_mfma_f32_16x16x32_bf16 v[176:179], v[216:219], v[96:99], 0\n\t" \
  "ds_read_b64_tr_b16 v[214:215], v249 offset:6912\n\t" \
  "v_pk_fma_f16 v224, v224, %[hc], %[hc]\n\t" \
  "v_cvt_pk_bf16_f32 v240, v20, v21\n\t" \
  "v_mfma_f32_16x16x32_bf16 v[184:187], v[216:219], v[108:111], 0\n\t" \
  "ds_read_u16_d16_hi v159, v159 offset:0\n\t" \
  "v_lshlrev_b32_sdwa v165, 1, v165 dst_sel:DWORD dst_unused:UNUSED_PAD src0_sel:DWORD src1_sel:WORD_1\n\t" \
  "v_cvt_pk_bf16_f32 v241, v22, v23\n\t" \
  "v_mfma_f32_16x16x32_bf16 v[192:195], v[216:219], v[120:123], 0\n\t" \
  "buffer_store_dwordx4 v[238:241], v253, %[osrd], %[ob] offen offset:128 sc1\n\t" \
  "v_rcp_f16 v225, v224\n\t" \
  "v_mfma_f32_16x16x32_bf16 v[200:203], v[216:219], v[132:135], 0\n\t" \
  "s_waitcnt lgkmcnt(12)\n\t" \
  "ds_read_b64_tr_b16 v[216:217], v249 offset:8704\n\t" \
  "v_lshlrev_b32_sdwa v166, 1, v166 dst_sel:DWORD dst_unused:UNUSED_PAD src0_sel:DWORD src1_sel:WORD_1\n\t" \
  "v_mfma_f32_16x16x32_bf16 v[176:179], v[220:223], v[100:103], v[176:179]\n\t" \
  "s_waitcnt lgkmcnt(10)\n\t" \
  "ds_read_u16_d16_hi v164, v164 offset:0\n\t" \
  "v_lshlrev_b32_sdwa v167, 1, v167 dst_sel:DWORD dst_unused:UNUSED_PAD src0_sel:DWORD src1_sel:WORD_1\n\t" \
  "v_mfma_f32_16x16x16_bf16 v[8:11], v[246:247], v[234:235], 0\n\t" \
  "ds_read_b64_tr_b16 v[218:219], v249 offset:8960\n\t" \
  "v_rcp_f16_sdwa v224, v224 dst_sel:DWORD dst_unused:UNUSED_PAD src0_sel:WORD_1\n\t" \
  "v_mfma_f32_16x16x32_bf16 v[184:187], v[220:223], v[112:115], v[184:187]\n\t" \
  "ds_read_u16_d16_hi v165, v165 offset:0\n\t" \
  "v_lshlrev_b32_sdwa v172, 1, v172 dst_sel:DWORD dst_unused:UNUSED_PAD src0_sel:DWORD src1_sel:WORD_1\n\t" \
  "v_mfma_f32_16x16x32_bf16 v[192:195], v[220:223], v[124:127], v[192:195]\n\t" \
  "ds_read_u16_d16_hi v166, v166 offset:0\n\t" \
  "v_lshlrev_b32_sdwa v173, 1, v173 dst_sel:DWORD dst_unused:UNUSED_PAD src0_sel:DWORD src1_sel:WORD_1\n\t" \
  "v_mfma_f32_16x16x32_bf16 v[200:203], v[220:223], v[136:139], v[200:203]\n\t" \
  "s_waitcnt lgkmcnt(12)\n\t" \
  "ds_read_b64_tr_b16 v[220:221], v249 offset:10752\n\t" \
  "ds_read_u16_d16_hi v167, v167 offset:0\n\t" \
  "v_mfma_f32_16x16x16_bf16 v[12:15], v[246:247], v[236:237], 0\n\t" \
  "ds_read_b64_tr_b16 v[222:223], v249 offset:11008\n\t" \
  "v_fma_mix_f32 v156, v156, v225, 0 op_sel_hi:[0,1,0]\n\t" \
  "v_mfma_f32_16x16x32_bf16 v[176:179], v[208:211], v[104:107], v[176:179]\n\t" \
  "ds_read_u16_d16_hi v172, v172 offset:0\n\t" \
  "v_fma_mix_f32 v157, v157, v224, 0 op_sel_hi:[0,1,0]\n\t" \
  "v_mfma_f32_16x16x32_bf16 v[184:187], v[208:211], v[116:119], v[184:187]\n\t" \
  "ds_read_u16_d16_hi v173, v173 offset:0\n\t" \
  "v_lshlrev_b32_sdwa v174, 1, v174 dst_sel:DWORD dst_unused:UNUSED_PAD src0_sel:DWORD src1_sel:WORD_1\n\t" \
  "v_mfma_f32_16x16x32_bf16 v[192:195], v[208:211], v[128:131], v[192:195]\n\t" \
  "ds_read_u16_d16_hi v174, v174 offset:0\n\t" \
  "v_lshlrev_b32_sdwa v175, 1, v175 dst_sel:DWORD dst_unused:UNUSED_PAD src0_sel:DWORD src1_sel:WORD_1\n\t" \
  "v_mfma_f32_16x16x32_bf16 v[200:203], v[208:211], v[140:143], v[200:203]\n\t" \
  "s_waitcnt lgkmcnt(12)\n\t" \
  "ds_read_b64_tr_b16 v[208:209], v250 offset:0\n\t" \
  "v_lshlrev_b32_sdwa v176, 1, v176 dst_sel:DWORD dst_unused:UNUSED_PAD src0_sel:DWORD src1_sel:WORD_1\n\t" \
  "v_mfma_f32_16x16x32_bf16 v[180:183], v[212:215], v[96:99], 0\n\t" \
  "ds_read_u16_d16_hi v175, v175 offset:0\n\t" \
  "v_mul_f32 v184, %[kk], v184\n\t" \
  "v_mfma_f32_16x16x16_bf16 v[16:19], v[246:247], v[242:243], 0\n\t" \
  "ds_read_b64_tr_b16 v[210:211], v250 offset:768\n\t" \
  "v_lshlrev_b32_sdwa v177, 1, v177 dst_sel:DWORD dst_unused:UNUSED_PAD src0_sel:DWORD src1_sel:WORD_1\n\t" \
  "v_mfma_f32_16x16x32_bf16 v[188:191], v[212:215], v[108:111], 0\n\t" \
  "ds_read_u16_d16_hi v176, v176 offset:0\n\t" \
  "v_mul_f32 v185, %[kk], v185\n\t" \
  "v_mfma_f32_16x16x32_bf16 v[196:199], v[212:215], v[120:123], 0\n\t" \
  "ds_read_u16_d16_hi v177, v177 offset:0\n\t" \
  "v_cvt_pk_f16_f32 v224, v184, v185\n\t" \
  "v_mfma_f32_16x16x32_bf16 v[204:207], v[212:215], v[132:135], 0\n\t" \
  "ds_read_b64_tr_b16 v[212:213], v250 offset:8\n\t" \
  "v_lshlrev_b32_sdwa v178, 1, v178 dst_sel:DWORD dst_unused:UNUSED_PAD src0_sel:DWORD src1_sel:WORD_1\n\t" \
  "v_mfma_f32_16x16x16_bf16 v[20:23], v[246:247], v[244:245], 0\n\t" \
  "s_waitcnt lgkmcnt(14)\n\t" \
  "ds_read_u16_d16_hi v178, v178 offset:0\n\t" \
  "v_pk_fma_f16 v225, v224, v224, %[h1] neg_lo:[1,0,0] neg_hi:[1,0,0]\n\t" \
  "v_mfma_f32_16x16x32_bf16 v[180:183], v[216:219], v[100:103], v[180:183]\n\t" \
  "ds_read_b64_tr_b16 v[214:215], v250 offset:776\n\t" \
  "v_lshlrev_b32_sdwa v179, 1, v179 dst_sel:DWORD dst_unused:UNUSED_PAD src0_sel:DWORD src1_sel:WORD_1\n\t" \
  "v_mfma_f32_16x16x32_bf16 v[188:191], v[216:219], v[112:115], v[188:191]\n\t" \
  "ds_read_u16_d16_hi v179, v179 offset:0\n\t" \
  "v_pk_mul_f16 v225, v224, v225\n\t" \
  "v_mfma_f32_16x16x32_bf16 v[196:199], v[216:219], v[124:127], v[196:199]\n\t" \
  "v_lshlrev_b32_sdwa v186, 1, v186 dst_sel:DWORD dst_unused:UNUSED_PAD src0_sel:DWORD src1_sel:WORD_1\n\t" \
  "v_exp_f16 v224, v225\n\t" \
  "v_mfma_f32_16x16x32_bf16 v[204:207], v[216:219], v[136:139], v[204:207]\n\t" \
  "s_waitcnt lgkmcnt(12)\n\t" \
  "ds_read_b64_tr_b16 v[216:217], v250 offset:64\n\t" \
  "v_lshlrev_b32_sdwa v187, 1, v187 dst_sel:DWORD dst_unused:UNUSED_PAD src0_sel:DWORD src1_sel:WORD_1\n\t" \
  "v_mfma_f32_16x16x32_bf16 v[180:183], v[220:223], v[104:107], v[180:183]\n\t" \
  "ds_read_u16_d16_hi v186, v186 offset:0\n\t" \
  "v_lshlrev_b32_sdwa v192, 1, v192 dst_sel:DWORD dst_unused:UNUSED_PAD src0_sel:DWORD src1_sel:WORD_1\n\t" \
  "v_mfma_f32_16x16x32_bf16 v[188:191], v[220:223], v[116:119], v[188:191]\n\t" \
  "ds_read_b64_tr_b16 v[218:219], v250 offset:832\n\t" \
  "v_exp_f16_sdwa v224, v225 dst_sel:WORD_1 dst_unused:UNUSED_PRESERVE src0_sel:WORD_1\n\t" \
  "v_mfma_f32_16x16x32_bf16 v[196:199], v[220:223], v[128:131], v[196:199]\n\t" \
  "ds_read_u16_d16_hi v187, v187 offset:0\n\t" \
  "v_lshlrev_b32_sdwa v193, 1, v193 dst_sel:DWORD dst_unused:UNUSED_PAD src0_sel:DWORD src1_sel:WORD_1\n\t" \
  "v_mfma_f32_16x16x32_bf16 v[204:207], v[220:223], v[140:143], v[204:207]\n\t" \
  "s_waitcnt lgkmcnt(10)\n\t" \
  "ds_read_b64_tr_b16 v[220:221], v250 offset:72\n\t" \
  "v_pk_fma_f16 v224, v224, %[hc], %[hc]\n\t" \
  "v_smfmac_f32_16x16x64_bf16 v[0:3], v[208:211], v[144:151], v248\n\t" \
  "ds_read_u16_d16_hi v192, v192 offset:0\n\t" \
  "v_lshlrev_b32_sdwa v194, 1, v194 dst_sel:DWORD dst_unused:UNUSED_PAD src0_sel:DWORD src1_sel:WORD_1\n\t" \
  "v_smfmac_f32_16x16x64_bf16 v[24:27], v[208:211], v[152:159], v248\n\t" \
  "ds_read_b64_tr_b16 v[222:223], v250 offset:840\n\t" \
  "v_rcp_f16 v225, v224\n\t" \
  "v_smfmac_f32_16x16x64_bf16 v[48:51], v[208:211], v[160:167], v248\n\t" \
  "ds_read_u16_d16_hi v193, v193 offset:0\n\t" \
  "v_lshlrev_b32_sdwa v195, 1, v195 dst_sel:DWORD dst_unused:UNUSED_PAD src0_sel:DWORD src1_sel:WORD_1\n\t" \
  "v_smfmac_f32_16x16x64_bf16 v[72:75], v[208:211], v[168:175], v248\n\t" \
  "s_waitcnt lgkmcnt(9)\n\t" \
  "ds_read_b64_tr_b16 v[208:209], v250 offset:128\n\t" \
  "v_lshlrev_b32_sdwa v200, 1, v200 dst_sel:DWORD dst_unused:UNUSED_PAD src0_sel:DWORD src1_sel:WORD_1\n\t" \
  "v_smfmac_f32_16x16x64_bf16 v[4:7], v[212:215], v[144:151], v248\n\t" \
  "ds_read_u16_d16_hi v194, v194 offset:0\n\t" \
  "v_rcp_f16_sdwa v224, v224 dst_sel:DWORD dst_unused:UNUSED_PAD src0_sel:WORD_1\n\t" \
  "v_smfmac_f32_16x16x64_bf16 v[28:31], v[212:215], v[152:159], v248\n\t" \
  "s_add_u32 %[ob], %[ob], %[dob]\n\t" \
  "ds_read_b64_tr_b16 v[210:211], v250 offset:896\n\t" \
  "v_lshlrev_b32_sdwa v201, 1, v201 dst_sel:DWORD dst_unused:UNUSED_PAD src0_sel:DWORD src1_sel:WORD_1\n\t" \
  "v_smfmac_f32_16x16x64_bf16 v[52:55], v[212:215], v[160:167], v248\n\t" \
  "ds_read_u16_d16_hi v195, v195 offset:0\n\t" \
  "v_fma_mix_f32 v184, v184, v225, 0 op_sel_hi:[0,1,0]\n\t" \
  "v_smfmac_f32_16x16x64_bf16 v[76:79], v[212:215], v[168:175], v248\n\t" \
  "s_waitcnt lgkmcnt(9)\n\t" \
  "ds_read_b64_tr_b16 v[212:213], v250 offset:136\n\t" \
  "v_lshlrev_b32_sdwa v202, 1, v202 dst_sel:DWORD dst_unused:UNUSED_PAD src0_sel:DWORD src1_sel:WORD_1\n\t" \
  "v_smfmac_f32_16x16x64_bf16 v[8:11], v[216:219], v[144:151], v248\n\t" \
  "ds_read_u16_d16_hi v200, v200 offset:0\n\t" \
  "v_fma_mix_f32 v185, v185, v224, 0 op_sel_hi:[0,1,0]\n\t" \
  "v_smfmac_f32_16x16x64_bf16 v[32:35], v[216:219], v[152:159], v248\n\t" \
  "ds_read_b64_tr_b16 v[214:215], v250 offset:904\n\t" \
  "v_lshlrev_b32_sdwa v203, 1, v203 dst_sel:DWORD dst_unused:UNUSED_PAD src0_sel:DWORD src1_sel:WORD_1\n\t" \
  "v_smfmac_f32_16x16x64_bf16 v[56:59], v[216:219], v[160:167], v248\n\t" \
  "ds_read_u16_d16_hi v201, v201 offset:0\n\t" \
  "v_mul_f32 v188, %[kk], v188\n\t" \
  "v_smfmac_f32_16x16x64_bf16 v[80:83], v[216:219], v[168:175], v248\n\t" \
  "s_waitcnt lgkmcnt(9)\n\t" \
  "s_barrier\n\t" \
  "ds_read_b64_tr_b16 v[216:217], v249 offset:12288\n\t" \
  "v_lshlrev_b32_sdwa v180, 1, v180 dst_sel:DWORD dst_unused:UNUSED_PAD src0_sel:DWORD src1_sel:WORD_1\n\t" \
  "v_smfmac_f32_16x16x64_bf16 v[12:15], v[220:223], v[144:151], v248\n\t" \
  "ds_read_u16_d16_hi v202, v202 offset:0\n\t" \
  "v_mul_f32 v189, %[kk], v189\n\t" \
  "v_smfmac_f32_16x16x64_bf16 v[36:39], v[220:223], v[152:159], v248\n\t" \
  "ds_read_b64_tr_b16 v[218:219], v249 offset:12544\n\t" \
  "v_cvt_pk_f16_f32 v224, v188, v189\n\t" \
  "v_smfmac_f32_16x16x64_bf16 v[60:63], v[220:223], v[160:167], v248\n\t" \
  "ds_read_u16_d16_hi v203, v203 offset:0\n\t" \
  "v_lshlrev_b32_sdwa v181, 1, v181 dst_sel:DWORD dst_unused:UNUSED_PAD src0_sel:DWORD src1_sel:WORD_1\n\t" \
  "v_smfmac_f32_16x16x64_bf16 v[84:87], v[220:223], v[168:175], v248\n\t" \
  "s_waitcnt lgkmcnt(9)\n\t" \
  "ds_read_b64_tr_b16 v[220:221], v249 offset:14336\n\t" \
  "v_pk_fma_f16 v225, v224, v224, %[h1] neg_lo:[1,0,0] neg_hi:[1,0,0]\n\t" \
  "v_smfmac_f32_16x16x64_bf16 v[16:19], v[208:211], v[144:151], v248\n\t" \
  "s_add_u32 %[rs], %[rs], 1\n\t" \
  "ds_read_u16_d16_hi v180, v180 offset:0\n\t" \
  "v_lshlrev_b32_sdwa v182, 1, v182 dst_sel:DWORD dst_unused:UNUSED_PAD src0_sel:DWORD src1_sel:WORD_1\n\t" \
  "v_smfmac_f32_16x16x64_bf16 v[40:43], v[208:211], v[152:159], v248\n\t" \
  "s_cmp_eq_u32 %[rs], 6\n\t" \
  "ds_read_b64_tr_b16 v[222:223], v249 offset:14592\n\t" \
  "v_pk_mul_f16 v225, v224, v225\n\t" \
  "v_smfmac_f32_16x16x64_bf16 v[64:67], v[208:211], v[160:167], v248\n\t" \
  "ds_read_u16_d16_hi v181, v181 offset:0\n\t" \
  "v_lshlrev_b32_sdwa v183, 1, v183 dst_sel:DWORD dst_unused:UNUSED_PAD src0_sel:DWORD src1_sel:WORD_1\n\t" \
  "v_smfmac_f32_16x16x64_bf16 v[88:91], v[208:211], v[168:175], v248\n\t" \
  "s_waitcnt lgkmcnt(9)\n\t" \
  "ds_read_b64_tr_b16 v[208:209], v249 offset:16384\n\t" \
  "v_exp_f16 v224, v225\n\t" \
  "v_smfmac_f32_16x16x64_bf16 v[20:23], v[212:215], v[144:151], v248\n\t" \
  "s_cselect_b32 %[rs], 0, %[rs]\n\t" \
  "ds_read_u16_d16_hi v182, v182 offset:0\n\t" \
  "v_lshlrev_b32_sdwa v190, 1, v190 dst_sel:DWORD dst_unused:UNUSED_PAD src0_sel:DWORD src1_sel:WORD_1\n\t" \
  "v_smfmac_f32_16x16x64_bf16 v[44:47], v[212:215], v[152:159], v248\n\t" \
  "s_cselect_b32 %[t0], -75920, %[ps]\n\t" \
  "ds_read_b64_tr_b16 v[210:211], v249 offset:16640\n\t" \
  "v_lshlrev_b32_sdwa v191, 1, v191 dst_sel:DWORD dst_unused:UNUSED_PAD src0_sel:DWORD src1_sel:WORD_1\n\t" \
  "v_smfmac_f32_16x16x64_bf16 v[68:71], v[212:215], v[160:167], v248\n\t" \
  "ds_read_u16_d16_hi v183, v183 offset:0\n\t" \
  "v_add_u32 v252, %[t0], v252\n\t" \
  "v_exp_f16_sdwa v224, v225 dst_sel:WORD_1 dst_unused:UNUSED_PRESERVE src0_sel:WORD_1\n\t" \
  "v_smfmac_f32_16x16x64_bf16 v[92:95], v[212:215], v[168:175], v248\n\t" \
  "s_waitcnt lgkmcnt(9)\n\t" \
  "ds_read_b64_tr_b16 v[212:213], v249 offset:12800\n\t" \
  "v_lshlrev_b32_sdwa v196, 1, v196 dst_sel:DWORD dst_unused:UNUSED_PAD src0_sel:DWORD src1_sel:WORD_1\n\t" \
  "v_mfma_f32_16x16x32_bf16 v[144:147], v[216:219], v[96:99], 0\n\t" \
  "ds_read_u16_d16_hi v190, v190 offset:0\n\t" \
  "v_pk_fma_f16 v224, v224, %[hc], %[hc]\n\t" \
  "v_mfma_f32_16x16x32_bf16 v[152:155], v[216:219], v[108:111], 0\n\t" \
  "ds_read_b64_tr_b16 v[214:215], v249 offset:13056\n\t" \
  "v_lshlrev_b32_sdwa v197, 1, v197 dst_sel:DWORD dst_unused:UNUSED_PAD src0_sel:DWORD src1_sel:WORD_1\n\t" \
  "v_mfma_f32_16x16x32_bf16 v[160:163], v[216:219], v[120:123], 0\n\t" \
  "ds_read_u16_d16_hi v191, v191 offset:0\n\t" \
  "v_rcp_f16 v225, v224\n\t" \
  "v_mfma_f32_16x16x32_bf16 v[168:171], v[216:219], v[132:135], 0\n\t" \
  "s_waitcnt lgkmcnt(9)\n\t" \
  "ds_read_b64_tr_b16 v[216:217], v249 offset:14848\n\t" \
  "v_lshlrev_b32_sdwa v198, 1, v198 dst_sel:DWORD dst_unused:UNUSED_PAD src0_sel:DWORD src1_sel:WORD_1\n\t" \
  "v_mfma_f32_16x16x32_bf16 v[144:147], v[220:223], v[100:103], v[144:147]\n\t" \
  "ds_read_u16_d16_hi v196, v196 offset:0\n\t" \
  "v_lshlrev_b32_sdwa v199, 1, v199 dst_sel:DWORD dst_unused:UNUSED_PAD src0_sel:DWORD src1_sel:WORD_1\n\t" \
  "v_mfma_f32_16x16x32_bf16 v[152:155], v[220:223], v[112:115], v[152:155]\n\t" \
  "ds_read_b64_tr_b16 v[218:219], v249 offset:15104\n\t" \
  "v_rcp_f16_sdwa v224, v224 dst_sel:DWORD dst_unused:UNUSED_PAD src0_sel:WORD_1\n\t" \
  "v_mfma_f32_16x16x32_bf16 v[160:163], v[220:223], v[124:127], v[160:163]\n\t" \
  "ds_read_u16_d16_hi v197, v197 offset:0\n\t" \
  "v_lshlrev_b32_sdwa v204, 1, v204 dst_sel:DWORD dst_unused:UNUSED_PAD src0_sel:DWORD src1_sel:WORD_1\n\t" \
  "v_mfma_f32_16x16x32_bf16 v[168:171], v[220:223], v[136:139], v[168:171]\n\t" \
  "s_waitcnt lgkmcnt(9)\n\t" \
  "ds_read_b64_tr_b16 v[220:221], v249 offset:16896\n\t" \
  "ds_read_u16_d16_hi v198, v198 offset:0\n\t" \
  "v_mfma_f32_16x16x32_bf16 v[144:147], v[208:211], v[104:107], v[144:147]\n\t" \
  "ds_read_b64_tr_b16 v[222:223], v249 offset:17152\n\t" \
  "v_lshlrev_b32_sdwa v205, 1, v205 dst_sel:DWORD dst_unused:UNUSED_PAD src0_sel:DWORD src1_sel:WORD_1\n\t" \
  "v_fma_mix_f32 v188, v188, v225, 0 op_sel_hi:[0,1,0]\n\t" \
  "v_mfma_f32_16x16x32_bf16 v[152:155], v[208:211], v[116:119], v[152:155]\n\t" \
  "ds_read_u16_d16_hi v199, v199 offset:0\n\t" \
  "v_fma_mix_f32 v189, v189, v224, 0 op_sel_hi:[0,1,0]\n\t" \
  "v_mfma_f32_16x16x32_bf16 v[160:163], v[208:211], v[128:131], v[160:163]\n\t" \
  "ds_read_u16_d16_hi v204, v204 offset:0\n\t" \
  "v_lshlrev_b32_sdwa v206, 1, v206 dst_sel:DWORD dst_unused:UNUSED_PAD src0_sel:DWORD src1_sel:WORD_1\n\t" \
  "v_mfma_f32_16x16x32_bf16 v[168:171], v[208:211], v[140:143], v[168:171]\n\t" \
  "s_waitcnt lgkmcnt(10)\n\t" \
  "ds_read_b64_tr_b16 v[208:209], v250 offset:6160\n\t" \
  "v_lshlrev_b32_sdwa v207, 1, v207 dst_sel:DWORD dst_unused:UNUSED_PAD src0_sel:DWORD src1_sel:WORD_1\n\t" \
  "v_mfma_f32_16x16x32_bf16 v[148:151], v[212:215], v[96:99], 0\n\t" \
  "ds_read_u16_d16_hi v205, v205 offset:0\n\t" \
  "v_lshlrev_b32_sdwa v144, 1, v144 dst_sel:DWORD dst_unused:UNUSED_PAD src0_sel:DWORD src1_sel:WORD_1\n\t" \
  "v_mfma_f32_16x16x32_bf16 v[156:159], v[212:215], v[108:111], 0\n\t" \
  "ds_read_b64_tr_b16 v[210:211], v250 offset:6928\n\t" \
  "v_mul_f32 v152, %[kk], v152\n\t" \
  "v_mfma_f32_16x16x32_bf16 v[164:167], v[212:215], v[120:123], 0\n\t" \
  "ds_read_u16_d16_hi v206, v206 offset:0\n\t" \
  "v_lshlrev_b32_sdwa v145, 1, v145 dst_sel:DWORD dst_unused:UNUSED_PAD src0_sel:DWORD src1_sel:WORD_1\n\t" \
  "v_mul_f32 v153, %[kk], v153\n\t" \
  "v_mfma_f32_16x16x32_bf16 v[172:175], v[212:215], v[132:135], 0\n\t" \
  "s_waitcnt lgkmcnt(10)\n\t" \
  "ds_read_b64_tr_b16 v[212:213], v250 offset:6168\n\t" \
  "ds_read_u16_d16_hi v207, v207 offset:0\n\t" \
  "v_cvt_pk_f16_f32 v224, v152, v153\n\t" \
  "v_mfma_f32_16x16x32_bf16 v[148:151], v[216:219], v[100:103], v[148:151]\n\t" \
  "ds_read_b64_tr_b16 v[214:215], v250 offset:6936\n\t" \
  "v_lshlrev_b32_sdwa v146, 1, v146 dst_sel:DWORD dst_unused:UNUSED_PAD src0_sel:DWORD src1_sel:WORD_1\n\t" \
  "v_mfma_f32_16x16x32_bf16 v[156:159], v[216:219], v[112:115], v[156:159]\n\t" \
  "ds_read_u16_d16_hi v144, v144 offset:0\n\t" \
  "v_pk_fma_f16 v225, v224, v224, %[h1] neg_lo:[1,0,0] neg_hi:[1,0,0]\n\t" \
  "v_mfma_f32_16x16x32_bf16 v[164:167], v[216:219], v[124:127], v[164:167]\n\t" \
  "ds_read_u16_d16_hi v145, v145 offset:0\n\t" \
  "v_lshlrev_b32_sdwa v147, 1, v147 dst_sel:DWORD dst_unused:UNUSED_PAD src0_sel:DWORD src1_sel:WORD_1\n\t" \
  "v_pk_mul_f16 v225, v224, v225\n\t" \
  "v_mfma_f32_16x16x32_bf16 v[172:175], v[216:219], v[136:139], v[172:175]\n\t" \
  "s_waitcnt lgkmcnt(11)\n\t" \
  "ds_read_b64_tr_b16 v[216:217], v250 offset:6224\n\t" \
  "v_lshlrev_b32_sdwa v154, 1, v154 dst_sel:DWORD dst_unused:UNUSED_PAD src0_sel:DWORD src1_sel:WORD_1\n\t" \
  "v_mfma_f32_16x16x32_bf16 v[148:151], v[220:223], v[104:107], v[148:151]\n\t" \
  "ds_read_u16_d16_hi v146, v146 offset:0\n\t" \
  "v_exp_f16 v224, v225\n\t" \
  "v_mfma_f32_16x16x32_bf16 v[156:159], v[220:223], v[116:119], v[156:159]\n\t" \
  "ds_read_b64_tr_b16 v[218:219], v250 offset:6992\n\t" \
  "v_lshlrev_b32_sdwa v155, 1, v155 dst_sel:DWORD dst_unused:UNUSED_PAD src0_sel:DWORD src1_sel:WORD_1\n\t" \
  "v_mfma_f32_16x16x32_bf16 v[164:167], v[220:223], v[128:131], v[164:167]\n\t" \
  "ds_read_u16_d16_hi v147, v147 offset:0\n\t" \
  "v_lshlrev_b32_sdwa v160, 1, v160 dst_sel:DWORD dst_unused:UNUSED_PAD src0_sel:DWORD src1_sel:WORD_1\n\t" \
  "v_mfma_f32_16x16x32_bf16 v[172:175], v[220:223], v[140:143], v[172:175]\n\t" \
  "s_waitcnt lgkmcnt(10)\n\t" \
  "ds_read_b64_tr_b16 v[220:221], v250 offset:6232\n\t" \
  "ds_read_u16_d16_hi v154, v154 offset:0\n\t" \
  "v_exp_f16_sdwa v224, v225 dst_sel:WORD_1 dst_unused:UNUSED_PRESERVE src0_sel:WORD_1\n\t" \
  "v_smfmac_f32_16x16x64_bf16 v[0:3], v[208:211], v[176:183], v248\n\t" \
  "ds_read_b64_tr_b16 v[222:223], v250 offset:7000\n\t" \
  "v_lshlrev_b32_sdwa v161, 1, v161 dst_sel:DWORD dst_unused:UNUSED_PAD src0_sel:DWORD src1_sel:WORD_1\n\t" \
  "v_smfmac_f32_16x16x64_bf16 v[24:27], v[208:211], v[184:191], v248\n\t" \
  "ds_read_u16_d16_hi v155, v155 offset:0\n\t" \
  "v_pk_fma_f16 v224, v224, %[hc], %[hc]\n\t" \
  "v_smfmac_f32_16x16x64_bf16 v[48:51], v[208:211], v[192:199], v248\n\t" \
  "s_waitcnt lgkmcnt(11)\n\t" \
  "ds_read_u16_d16_hi v160, v160 offset:0\n\t" \
  "v_lshlrev_b32_sdwa v162, 1, v162 dst_sel:DWORD dst_unused:UNUSED_PAD src0_sel:DWORD src1_sel:WORD_1\n\t" \
  "v_smfmac_f32_16x16x64_bf16 v[72:75], v[208:211], v[200:207], v248\n\t" \
  "s_waitcnt lgkmcnt(11)\n\t" \
  "ds_read_b64_tr_b16 v[208:209], v250 offset:6288\n\t" \
  "v_rcp_f16 v225, v224\n\t" \
  "v_smfmac_f32_16x16x64_bf16 v[4:7], v[212:215], v[176:183], v248\n\t" \
  "ds_read_u16_d16_hi v161, v161 offset:0\n\t" \
  "v_lshlrev_b32_sdwa v163, 1, v163 dst_sel:DWORD dst_unused:UNUSED_PAD src0_sel:DWORD src1_sel:WORD_1\n\t" \
  "v_smfmac_f32_16x16x64_bf16 v[28:31], v[212:215], v[184:191], v248\n\t" \
  "ds_read_b64_tr_b16 v[210:211], v250 offset:7056\n\t" \
  "v_lshlrev_b32_sdwa v168, 1, v168 dst_sel:DWORD dst_unused:UNUSED_PAD src0_sel:DWORD src1_sel:WORD_1\n\t" \
  "v_smfmac_f32_16x16x64_bf16 v[52:55], v[212:215], v[192:199], v248\n\t" \
  "ds_read_u16_d16_hi v162, v162 offset:0\n\t" \
  "v_rcp_f16_sdwa v224, v224 dst_sel:DWORD dst_unused:UNUSED_PAD src0_sel:WORD_1\n\t" \
  "v_smfmac_f32_16x16x64_bf16 v[76:79], v[212:215], v[200:207], v248\n\t" \
  "s_waitcnt lgkmcnt(10)\n\t" \
  "ds_read_b64_tr_b16 v[212:213], v250 offset:6296\n\t" \
  "v_lshlrev_b32_sdwa v169, 1, v169 dst_sel:DWORD dst_unused:UNUSED_PAD src0_sel:DWORD src1_sel:WORD_1\n\t" \
  "v_smfmac_f32_16x16x64_bf16 v[8:11], v[216:219], v[176:183], v248\n\t" \
  "ds_read_u16_d16_hi v163, v163 offset:0\n\t" \
  "v_fma_mix_f32 v152, v152, v225, 0 op_sel_hi:[0,1,0]\n\t" \
  "v_smfmac_f32_16x16x64_bf16 v[32:35], v[216:219], v[184:191], v248\n\t" \
  "ds_read_b64_tr_b16 v[214:215], v250 offset:7064\n\t" \
  "v_lshlrev_b32_sdwa v170, 1, v170 dst_sel:DWORD dst_unused:UNUSED_PAD src0_sel:DWORD src1_sel:WORD_1\n\t" \
  "v_smfmac_f32_16x16x64_bf16 v[56:59], v[216:219], v[192:199], v248\n\t" \
  "ds_read_u16_d16_hi v168, v168 offset:0\n\t" \
  "v_fma_mix_f32 v153, v153, v224, 0 op_sel_hi:[0,1,0]\n\t" \
  "v_smfmac_f32_16x16x64_bf16 v[80:83], v[216:219], v[200:207], v248\n\t" \
  "s_waitcnt lgkmcnt(10)\n\t" \
  "s_barrier\n\t" \
  "ds_read_b64_tr_b16 v[216:217], v249 offset:0\n\t" \
  "ds_read_b128 v[108:111], v251 offset:0\n\t" \
  "v_lshlrev_b32_sdwa v171, 1, v171 dst_sel:DWORD dst_unused:UNUSED_PAD src0_sel:DWORD src1_sel:WORD_1\n\t" \
  "v_smfmac_f32_16x16x64_bf16 v[12:15], v[220:223], v[176:183], v248\n\t" \
  "ds_read_b64_tr_b16 v[218:219], v249 offset:256\n\t" \
  "ds_read_b128 v[112:115], v251 offset:4096\n\t" \
  "v_mul_f32 v156, %[kk], v156\n\t" \
  "v_smfmac_f32_16x16x64_bf16 v[36:39], v[220:223], v[184:191], v248\n\t" \
  "ds_read_b128 v[116:119], v251 offset:8192\n\t" \
  "v_lshlrev_b32_sdwa v148, 1, v148 dst_sel:DWORD dst_unused:UNUSED_PAD src0_sel:DWORD src1_sel:WORD_1\n\t" \
  "v_mul_f32 v157, %[kk], v157\n\t" \
  "v_smfmac_f32_16x16x64_bf16 v[60:63], v[220:223], v[192:199], v248\n\t" \
  "ds_read_u16_d16_hi v169, v169 offset:0\n\t" \
  "v_cvt_pk_f16_f32 v224, v156, v157\n\t" \
  "v_smfmac_f32_16x16x64_bf16 v[84:87], v[220:223], v[200:207], v248\n\t" \
  "s_waitcnt lgkmcnt(11)\n\t" \
  "ds_read_b64_tr_b16 v[220:221], v249 offset:2048\n\t" \
  "ds_read_u16_d16_hi v170, v170 offset:0\n\t" \
  "v_lshlrev_b32_sdwa v149, 1, v149 dst_sel:DWORD dst_unused:UNUSED_PAD src0_sel:DWORD src1_sel:WORD_1\n\t" \
  "v_smfmac_f32_16x16x64_bf16 v[16:19], v[208:211], v[176:183], v248\n\t" \
  "ds_read_b64_tr_b16 v[222:223], v249 offset:2304\n\t" \
  "v_pk_fma_f16 v225, v224, v224, %[h1] neg_lo:[1,0,0] neg_hi:[1,0,0]\n\t" \
  "v_smfmac_f32_16x16x64_bf16 v[40:43], v[208:211], v[184:191], v248\n\t" \
  "ds_read_u16_d16_hi v171, v171 offset:0\n\t" \
  "v_lshlrev_b32_sdwa v150, 1, v150 dst_sel:DWORD dst_unused:UNUSED_PAD src0_sel:DWORD src1_sel:WORD_1\n\t" \
  "v_pk_mul_f16 v225, v224, v225\n\t" \
  "v_smfmac_f32_16x16x64_bf16 v[64:67], v[208:211], v[192:199], v248\n\t" \
  "ds_read_u16_d16_hi v148, v148 offset:0\n\t" \
  "v_lshlrev_b32_sdwa v151, 1, v151 dst_sel:DWORD dst_unused:UNUSED_PAD src0_sel:DWORD src1_sel:WORD_1\n\t" \
  "v_smfmac_f32_16x16x64_bf16 v[88:91], v[208:211], v[200:207], v248\n\t" \
  "s_waitcnt lgkmcnt(12)\n\t" \
  "ds_read_b64_tr_b16 v[208:209], v249 offset:4096\n\t" \
  "ds_read_u16_d16_hi v149, v149 offset:0\n\t" \
  "v_exp_f16 v224, v225\n\t" \
  "v_smfmac_f32_16x16x64_bf16 v[20:23], v[212:215], v[176:183], v248\n\t" \
  "ds_read_b64_tr_b16 v[210:211], v249 offset:4352\n\t" \
  "v_lshlrev_b32_sdwa v158, 1, v158 dst_sel:DWORD dst_unused:UNUSED_PAD src0_sel:DWORD src1_sel:WORD_1\n\t" \
  "v_smfmac_f32_16x16x64_bf16 v[44:47], v[212:215], v[184:191], v248\n\t" \
  "ds_read_u16_d16_hi v150, v150 offset:0\n\t" \
  "v_lshlrev_b32_sdwa v159, 1, v159 dst_sel:DWORD dst_unused:UNUSED_PAD src0_sel:DWORD src1_sel:WORD_1\n\t" \
  "v_smfmac_f32_16x16x64_bf16 v[68:71], v[212:215], v[192:199], v248\n\t" \
  "ds_read_u16_d16_hi v151, v151 offset:0\n\t" \
  "v_exp_f16_sdwa v224, v225 dst_sel:WORD_1 dst_unused:UNUSED_PRESERVE src0_sel:WORD_1\n\t" \
  "v_smfmac_f32_16x16x64_bf16 v[92:95], v[212:215], v[200:207], v248\n\t" \
  "s_waitcnt lgkmcnt(13)\n\t" \
  "ds_read_b64_tr_b16 v[212:213], v249 offset:512\n\t" \
  "v_lshlrev_b32_sdwa v164, 1, v164 dst_sel:DWORD dst_unused:UNUSED_PAD src0_sel:DWORD src1_sel:WORD_1\n\t" \
  "v_mfma_f32_16x16x32_bf16 v[176:179], v[216:219], v[96:99], 0\n\t" \
  "ds_read_u16_d16_hi v158, v158 offset:0\n\t" \
  "v_pk_fma_f16 v224, v224, %[hc], %[hc]\n\t" \
  "v_mfma_f32_16x16x32_bf16 v[184:187], v[216:219], v[108:111], 0\n\t" \
  "ds_read_b64_tr_b16 v[214:215], v249 offset:768\n\t" \
  "v_lshlrev_b32_sdwa v165, 1, v165 dst_sel:DWORD dst_unused:UNUSED_PAD src0_sel:DWORD src1_sel:WORD_1\n\t" \
  "v_mfma_f32_16x16x32_bf16 v[192:195], v[216:219], v[120:123], 0\n\t" \
  "ds_read_u16_d16_hi v159, v159 offset:0\n\t" \
  "v_rcp_f16 v225, v224\n\t" \
  "v_mfma_f32_16x16x32_bf16 v[200:203], v[216:219], v[132:135], 0\n\t" \
  "s_waitcnt lgkmcnt(11)\n\t" \
  "ds_read_b64_tr_b16 v[216:217], v249 offset:2560\n\t" \
  "v_lshlrev_b32_sdwa v166, 1, v166 dst_sel:DWORD dst_unused:UNUSED_PAD src0_sel:DWORD src1_sel:WORD_1\n\t" \
  "v_mfma_f32_16x16x32_bf16 v[176:179], v[220:223], v[100:103], v[176:179]\n\t" \
  "ds_read_u16_d16_hi v164, v164 offset:0\n\t" \
  "v_lshlrev_b32_sdwa v167, 1, v167 dst_sel:DWORD dst_unused:UNUSED_PAD src0_sel:DWORD src1_sel:WORD_1\n\t" \
  "v_mfma_f32_16x16x32_bf16 v[184:187], v[220:223], v[112:115], v[184:187]\n\t" \
  "ds_read_b64_tr_b16 v[218:219], v249 offset:2816\n\t" \
  "v_rcp_f16_sdwa v224, v224 dst_sel:DWORD dst_unused:UNUSED_PAD src0_sel:WORD_1\n\t" \
  "v_mfma_f32_16x16x32_bf16 v[192:195], v[220:223], v[124:127], v[192:195]\n\t" \
  "ds_read_u16_d16_hi v165, v165 offset:0\n\t" \
  "v_lshlrev_b32_sdwa v172, 1, v172 dst_sel:DWORD dst_unused:UNUSED_PAD src0_sel:DWORD src1_sel:WORD_1\n\t" \
  "v_mfma_f32_16x16x32_bf16 v[200:203], v[220:223], v[136:139], v[200:203]\n\t" \
  "s_waitcnt lgkmcnt(10)\n\t" \
  "ds_read_b64_tr_b16 v[220:221], v249 offset:4608\n\t" \
  "ds_read_u16_d16_hi v166, v166 offset:0\n\t" \
  "v_mfma_f32_16x16x32_bf16 v[176:179], v[208:211], v[104:107], v[176:179]\n\t" \
  "ds_read_b64_tr_b16 v[222:223], v249 offset:4864\n\t" \
  "v_lshlrev_b32_sdwa v173, 1, v173 dst_sel:DWORD dst_unused:UNUSED_PAD src0_sel:DWORD src1_sel:WORD_1\n\t" \
  "v_fma_mix_f32 v156, v156, v225, 0 op_sel_hi:[0,1,0]\n\t" \
  "v_mfma_f32_16x16x32_bf16 v[184:187], v[208:211], v[116:119], v[184:187]\n\t" \
  "ds_read_u16_d16_hi v167, v167 offset:0\n\t" \
  "v_fma_mix_f32 v157, v157, v224, 0 op_sel_hi:[0,1,0]\n\t" \
  "v_mfma_f32_16x16x32_bf16 v[192:195], v[208:211], v[128:131], v[192:195]\n\t" \
  "ds_read_u16_d16_hi v172, v172 offset:0\n\t" \
  "v_lshlrev_b32_sdwa v174, 1, v174 dst_sel:DWORD dst_unused:UNUSED_PAD src0_sel:DWORD src1_sel:WORD_1\n\t" \
  "v_mfma_f32_16x16x32_bf16 v[200:203], v[208:211], v[140:143], v[200:203]\n\t" \
  "s_waitcnt lgkmcnt(10)\n\t" \
  "ds_read_b64_tr_b16 v[208:209], v250 offset:12320\n\t" \
  "v_lshlrev_b32_sdwa v175, 1, v175 dst_sel:DWORD dst_unused:UNUSED_PAD src0_sel:DWORD src1_sel:WORD_1\n\t" \
  "v_mfma_f32_16x16x32_bf16 v[180:183], v[212:215], v[96:99], 0\n\t" \
  "ds_read_u16_d16_hi v173, v173 offset:0\n\t" \
  "v_lshlrev_b32_sdwa v176, 1, v176 dst_sel:DWORD dst_unused:UNUSED_PAD src0_sel:DWORD src1_sel:WORD_1\n\t" \
  "v_mfma_f32_16x16x32_bf16 v[188:191], v[212:215], v[108:111], 0\n\t" \
  "ds_read_b64_tr_b16 v[210:211], v250 offset:13088\n\t" \
  "v_mul_f32 v184, %[kk], v184\n\t" \
  "v_mfma_f32_16x16x32_bf16 v[196:199], v[212:215], v[120:123], 0\n\t" \
  "ds_read_u16_d16_hi v174, v174 offset:0\n\t" \
  "v_lshlrev_b32_sdwa v177, 1, v177 dst_sel:DWORD dst_unused:UNUSED_PAD src0_sel:DWORD src1_sel:WORD_1\n\t" \
  "v_mul_f32 v185, %[kk], v185\n\t" \
  "v_mfma_f32_16x16x32_bf16 v[204:207], v[212:215], v[132:135], 0\n\t" \
  "s_waitcnt lgkmcnt(10)\n\t" \
  "ds_read_b64_tr_b16 v[212:213], v250 offset:12328\n\t" \
  "ds_read_u16_d16_hi v175, v175 offset:0\n\t" \
  "v_cvt_pk_f16_f32 v224, v184, v185\n\t" \
  "v_mfma_f32_16x16x32_bf16 v[180:183], v[216:219], v[100:103], v[180:183]\n\t" \
  "ds_read_b64_tr_b16 v[214:215], v250 offset:13096\n\t" \
  "v_lshlrev_b32_sdwa v178, 1, v178 dst_sel:DWORD dst_unused:UNUSED_PAD src0_sel:DWORD src1_sel:WORD_1\n\t" \
  "v_mfma_f32_16x16x32_bf16 v[188:191], v[216:219], v[112:115], v[188:191]\n\t" \
  "ds_read_u16_d16_hi v176, v176 offset:0\n\t" \
  "v_pk_fma_f16 v225, v224, v224, %[h1] neg_lo:[1,0,0] neg_hi:[1,0,0]\n\t" \
  "v_mfma_f32_16x16x32_bf16 v[196:199], v[216:219], v[124:127], v[196:199]\n\t" \
  "ds_read_u16_d16_hi v177, v177 offset:0\n\t" \
  "v_lshlrev_b32_sdwa v179, 1, v179 dst_sel:DWORD dst_unused:UNUSED_PAD src0_sel:DWORD src1_sel:WORD_1\n\t" \
  "v_pk_mul_f16 v225, v224, v225\n\t" \
  "v_mfma_f32_16x16x32_bf16 v[204:207], v[216:219], v[136:139], v[204:207]\n\t" \
  "s_waitcnt lgkmcnt(11)\n\t" \
  "ds_read_b64_tr_b16 v[216:217], v250 offset:12384\n\t" \
  "v_lshlrev_b32_sdwa v186, 1, v186 dst_sel:DWORD dst_unused:UNUSED_PAD src0_sel:DWORD src1_sel:WORD_1\n\t" \
  "v_mfma_f32_16x16x32_bf16 v[180:183], v[220:223], v[104:107], v[180:183]\n\t" \
  "ds_read_u16_d16_hi v178, v178 offset:0\n\t" \
  "v_exp_f16 v224, v225\n\t" \
  "v_mfma_f32_16x16x32_bf16 v[188:191], v[220:223], v[116:119], v[188:191]\n\t" \
  "ds_read_b64_tr_b16 v[218:219], v250 offset:13152\n\t" \
  "v_lshlrev_b32_sdwa v187, 1, v187 dst_sel:DWORD dst_unused:UNUSED_PAD src0_sel:DWORD src1_sel:WORD_1\n\t" \
  "v_mfma_f32_16x16x32_bf16 v[196:199], v[220:223], v[128:131], v[196:199]\n\t" \
  "ds_read_u16_d16_hi v179, v179 offset:0\n\t" \
  "v_lshlrev_b32_sdwa v192, 1, v192 dst_sel:DWORD dst_unused:UNUSED_PAD src0_sel:DWORD src1_sel:WORD_1\n\t" \
  "v_mfma_f32_16x16x32_bf16 v[204:207], v[220:223], v[140:143], v[204:207]\n\t" \
  "s_waitcnt lgkmcnt(10)\n\t" \
  "ds_read_b64_tr_b16 v[220:221], v250 offset:12392\n\t" \
  "ds_read_u16_d16_hi v186, v186 offset:0\n\t" \
  "v_exp_f16_sdwa v224, v225 dst_sel:WORD_1 dst_unused:UNUSED_PRESERVE src0_sel:WORD_1\n\t" \
  "v_smfmac_f32_16x16x64_bf16 v[0:3], v[208:211], v[144:151], v248\n\t" \
  "ds_read_b64_tr_b16 v[222:223], v250 offset:13160\n\t" \
  "v_lshlrev_b32_sdwa v193, 1, v193 dst_sel:DWORD dst_unused:UNUSED_PAD src0_sel:DWORD src1_sel:WORD_1\n\t" \
  "v_smfmac_f32_16x16x64_bf16 v[24:27], v[208:211], v[152:159], v248\n\t" \
  "ds_read_u16_d16_hi v187, v187 offset:0\n\t" \
  "v_pk_fma_f16 v224, v224, %[hc], %[hc]\n\t" \
  "v_smfmac_f32_16x16x64_bf16 v[48:51], v[208:211], v[160:167], v248\n\t" \
  "s_waitcnt lgkmcnt(11)\n\t" \
  "ds_read_u16_d16_hi v192, v192 offset:0\n\t" \
  "v_lshlrev_b32_sdwa v194, 1, v194 dst_sel:DWORD dst_unused:UNUSED_PAD src0_sel:DWORD src1_sel:WORD_1\n\t" \
  "v_smfmac_f32_16x16x64_bf16 v[72:75], v[208:211], v[168:175], v248\n\t" \
  "s_waitcnt lgkmcnt(11)\n\t" \
  "ds_read_b64_tr_b16 v[208:209], v250 offset:12448\n\t" \
  "v_rcp_f16 v225, v224\n\t" \
  "v_smfmac_f32_16x16x64_bf16 v[4:7], v[212:215], v[144:151], v248\n\t" \
  "ds_read_b64 v[242:243], v252 offset:128\n\t" \
  "v_lshlrev_b32_sdwa v195, 1, v195 dst_sel:DWORD dst_unused:UNUSED_PAD src0_sel:DWORD src1_sel:WORD_1\n\t" \
  "v_smfmac_f32_16x16x64_bf16 v[28:31], v[212:215], v[152:159], v248\n\t" \
  "ds_read_b64_tr_b16 v[210:211], v250 offset:13216\n\t" \
  "ds_read_b64 v[244:245], v252 offset:136\n\t" \
  "v_smfmac_f32_16x16x64_bf16 v[52:55], v[212:215], v[160:167], v248\n\t" \
  "ds_read_u16_d16_hi v193, v193 offset:0\n\t" \
  "v_lshlrev_b32_sdwa v200, 1, v200 dst_sel:DWORD dst_unused:UNUSED_PAD src0_sel:DWORD src1_sel:WORD_1\n\t" \
  "v_rcp_f16_sdwa v224, v224 dst_sel:DWORD dst_unused:UNUSED_PAD src0_sel:WORD_1\n\t" \
  "v_smfmac_f32_16x16x64_bf16 v[76:79], v[212:215], v[168:175], v248\n\t" \
  "s_waitcnt lgkmcnt(11)\n\t" \
  "ds_read_b64_tr_b16 v[212:213], v250 offset:12456\n\t" \
  "ds_read_u16_d16_hi v194, v194 offset:0\n\t" \
  "v_cvt_pk_bf16_f32 v230, v24, v25\n\t" \
  "v_smfmac_f32_16x16x64_bf16 v[8:11], v[216:219], v[144:151], v248\n\t" \
  "ds_read_b64_tr_b16 v[214:215], v250 offset:13224\n\t" \
  "v_lshlrev_b32_sdwa v201, 1, v201 dst_sel:DWORD dst_unused:UNUSED_PAD src0_sel:DWORD src1_sel:WORD_1\n\t" \
  "v_cvt_pk_bf16_f32 v231, v26, v27\n\t" \
  "v_smfmac_f32_16x16x64_bf16 v[32:35], v[216:219], v[152:159], v248\n\t" \
  "ds_read_u16_d16_hi v195, v195 offset:0\n\t" \
  "v_fma_mix_f32 v184, v184, v225, 0 op_sel_hi:[0,1,0]\n\t" \
  "v_cvt_pk_bf16_f32 v232, v28, v29\n\t" \
  "v_smfmac_f32_16x16x64_bf16 v[56:59], v[216:219], v[160:167], v248\n\t" \
  "ds_read_u16_d16_hi v200, v200 offset:0\n\t" \
  "v_lshlrev_b32_sdwa v202, 1, v202 dst_sel:DWORD dst_unused:UNUSED_PAD src0_sel:DWORD src1_sel:WORD_1\n\t" \
  "v_fma_mix_f32 v185, v185, v224, 0 op_sel_hi:[0,1,0]\n\t" \
  "v_cvt_pk_bf16_f32 v233, v30, v31\n\t" \
  "v_smfmac_f32_16x16x64_bf16 v[80:83], v[216:219], v[168:175], v248\n\t" \
  "s_waitcnt lgkmcnt(12)\n\t" \
  "s_barrier\n\t" \
  "ds_read_b64_tr_b16 v[216:217], v249 offset:6144\n\t" \
  "buffer_store_dwordx4 v[230:233], v253, %[osrd], %[ob] offen offset:0 sc1\n\t" \
  "v_lshlrev_b32_sdwa v203, 1, v203 dst_sel:DWORD dst_unused:UNUSED_PAD src0_sel:DWORD src1_sel:WORD_1\n\t" \
  "v_smfmac_f32_16x16x64_bf16 v[12:15], v[220:223], v[144:151], v248\n\t" \
  "ds_read_b64_tr_b16 v[218:219], v249 offset:6400\n\t" \
  "ds_read_b64 v[230:231], v252 offset:0\n\t" \
  "v_mul_f32 v188, %[kk], v188\n\t" \
  "v_smfmac_f32_16x16x64_bf16 v[36:39], v[220:223], v[152:159], v248\n\t" \
  "ds_read_b64 v[232:233], v252 offset:8\n\t" \
  "v_lshlrev_b32_sdwa v180, 1, v180 dst_sel:DWORD dst_unused:UNUSED_PAD src0_sel:DWORD src1_sel:WORD_1\n\t" \
  "v_mul_f32 v189, %[kk], v189\n\t" \
  "v_smfmac_f32_16x16x64_bf16 v[60:63], v[220:223], v[160:167], v248\n\t" \
  "ds_read_u16_d16_hi v201, v201 offset:0\n\t" \
  "v_cvt_pk_f16_f32 v224, v188, v189\n\t" \
  "v_smfmac_f32_16x16x64_bf16 v[84:87], v[220:223], v[168:175], v248\n\t" \
  "s_waitcnt lgkmcnt(12)\n\t" \
  "ds_read_b64_tr_b16 v[220:221], v249 offset:8192\n\t" \
  "ds_read_u16_d16_hi v202, v202 offset:0\n\t" \
  "v_lshlrev_b32_sdwa v181, 1, v181 dst_sel:DWORD dst_unused:UNUSED_PAD src0_sel:DWORD src1_sel:WORD_1\n\t" \
  "v_cvt_pk_bf16_f32 v234, v32, v33\n\t" \
  "v_smfmac_f32_16x16x64_bf16 v[16:19], v[208:211], v[144:151], v248\n\t" \
  "ds_read_b64_tr_b16 v[222:223], v249 offset:8448\n\t" \
  "v_pk_fma_f16 v225, v224, v224, %[h1] neg_lo:[1,0,0] neg_hi:[1,0,0]\n\t" \
  "v_cvt_pk_bf16_f32 v235, v34, v35\n\t" \
  "v_smfmac_f32_16x16x64_bf16 v[40:43], v[208:211], v[152:159], v248\n\t" \
  "ds_read_u16_d16_hi v203, v203 offset:0\n\t" \
  "v_lshlrev_b32_sdwa v182, 1, v182 dst_sel:DWORD dst_unused:UNUSED_PAD src0_sel:DWORD src1_sel:WORD_1\n\t" \
  "v_pk_mul_f16 v225, v224, v225\n\t" \
  "v_cvt_pk_bf16_f32 v236, v36, v37\n\t" \
  "v_smfmac_f32_16x16x64_bf16 v[64:67], v[208:211], v[160:167], v248\n\t" \
  "ds_read_u16_d16_hi v180, v180 offset:0\n\t" \
  "v_lshlrev_b32_sdwa v183, 1, v183 dst_sel:DWORD dst_unused:UNUSED_PAD src0_sel:DWORD src1_sel:WORD_1\n\t" \
  "v_cvt_pk_bf16_f32 v237, v38, v39\n\t" \
  "v_smfmac_f32_16x16x64_bf16 v[88:91], v[208:211], v[168:175], v248\n\t" \
  "s_waitcnt lgkmcnt(12)\n\t" \
  "ds_read_b64_tr_b16 v[208:209], v249 offset:10240\n\t" \
  "buffer_store_dwordx4 v[234:237], v253, %[osrd], %[ob] offen offset:64 sc1\n\t" \
  "v_exp_f16 v224, v225\n\t" \
  "v_smfmac_f32_16x16x64_bf16 v[20:23], v[212:215], v[144:151], v248\n\t" \
  "ds_read_b64_tr_b16 v[210:211], v249 offset:10496\n\t" \
  "ds_read_b64 v[234:235], v252 offset:64\n\t" \
  "v_lshlrev_b32_sdwa v190, 1, v190 dst_sel:DWORD dst_unused:UNUSED_PAD src0_sel:DWORD src1_sel:WORD_1\n\t" \
  "v_smfmac_f32_16x16x64_bf16 v[44:47], v[212:215], v[152:159], v248\n\t" \
  "s_waitcnt lgkmcnt(10)\n\t" \
  "ds_read_b64 v[236:237], v252 offset:72\n\t" \
  "ds_read_u16_d16_hi v181, v181 offset:0\n\t" \
  "v_mfma_f32_16x16x16_bf16 v[24:27], v[246:247], v[230:231], 0\n\t" \
  "ds_read_u16_d16_hi v182, v182 offset:0\n\t" \
  "v_lshlrev_b32_sdwa v191, 1, v191 dst_sel:DWORD dst_unused:UNUSED_PAD src0_sel:DWORD src1_sel:WORD_1\n\t" \
  "v_smfmac_f32_16x16x64_bf16 v[68:71], v[212:215], v[160:167], v248\n\t" \
  "ds_read_u16_d16_hi v183, v183 offset:0\n\t" \
  "v_exp_f16_sdwa v224, v225 dst_sel:WORD_1 dst_unused:UNUSED_PRESERVE src0_sel:WORD_1\n\t" \
  "v_smfmac_f32_16x16x64_bf16 v[92:95], v[212:215], v[168:175], v248\n\t" \
  "ds_read_b64_tr_b16 v[212:213], v249 offset:6656\n\t" \
  "v_cvt_pk_bf16_f32 v238, v40, v41\n\t" \
  "v_mfma_f32_16x16x32_bf16 v[144:147], v[216:219], v[96:99], 0\n\t" \
  "s_waitcnt lgkmcnt(14)\n\t" \
  "ds_read_u16_d16_hi v190, v190 offset:0\n\t" \
  "v_lshlrev_b32_sdwa v196, 1, v196 dst_sel:DWORD dst_unused:UNUSED_PAD src0_sel:DWORD src1_sel:WORD_1\n\t" \
  "v_mfma_f32_16x16x16_bf16 v[28:31], v[246:247], v[232:233], 0\n\t" \
  "ds_read_b64_tr_b16 v[214:215], v249 offset:6912\n\t" \
  "v_pk_fma_f16 v224, v224, %[hc], %[hc]\n\t" \
  "v_cvt_pk_bf16_f32 v239, v42, v43\n\t" \
  "v_mfma_f32_16x16x32_bf16 v[152:155], v[216:219], v[108:111], 0\n\t" \
  "ds_read_u16_d16_hi v191, v191 offset:0\n\t" \
  "v_lshlrev_b32_sdwa v197, 1, v197 dst_sel:DWORD dst_unused:UNUSED_PAD src0_sel:DWORD src1_sel:WORD_1\n\t" \
  "v_cvt_pk_bf16_f32 v240, v44, v45\n\t" \
  "v_mfma_f32_16x16x32_bf16 v[160:163], v[216:219], v[120:123], 0\n\t" \
  "ds_read_u16_d16_hi v196, v196 offset:0\n\t" \
  "v_rcp_f16 v225, v224\n\t" \
  "v_cvt_pk_bf16_f32 v241, v46, v47\n\t" \
  "v_mfma_f32_16x16x32_bf16 v[168:171], v[216:219], v[132:135], 0\n\t" \
  "s_waitcnt lgkmcnt(14)\n\t" \
  "ds_read_b64_tr_b16 v[216:217], v249 offset:8704\n\t" \
  "v_lshlrev_b32_sdwa v198, 1, v198 dst_sel:DWORD dst_unused:UNUSED_PAD src0_sel:DWORD src1_sel:WORD_1\n\t" \
  "v_mfma_f32_16x16x32_bf16 v[144:147], v[220:223], v[100:103], v[144:147]\n\t" \
  "buffer_store_dwordx4 v[238:241], v253, %[osrd], %[ob] offen offset:128 sc1\n\t" \
  "v_lshlrev_b32_sdwa v199, 1, v199 dst_sel:DWORD dst_unused:UNUSED_PAD src0_sel:DWORD src1_sel:WORD_1\n\t" \
  "v_mfma_f32_16x16x32_bf16 v[152:155], v[220:223], v[112:115], v[152:155]\n\t" \
  "s_waitcnt lgkmcnt(10)\n\t" \
  "ds_read_b64_tr_b16 v[218:219], v249 offset:8960\n\t" \
  "v_rcp_f16_sdwa v224, v224 dst_sel:DWORD dst_unused:UNUSED_PAD src0_sel:WORD_1\n\t" \
  "v_mfma_f32_16x16x16_bf16 v[32:35], v[246:247], v[234:235], 0\n\t" \
  "ds_read_u16_d16_hi v197, v197 offset:0\n\t" \
  "v_lshlrev_b32_sdwa v204, 1, v204 dst_sel:DWORD dst_unused:UNUSED_PAD src0_sel:DWORD src1_sel:WORD_1\n\t" \
  "v_mfma_f32_16x16x32_bf16 v[160:163], v[220:223], v[124:127], v[160:163]\n\t" \
  "ds_read_u16_d16_hi v198, v198 offset:0\n\t" \
  "v_lshlrev_b32_sdwa v205, 1, v205 dst_sel:DWORD dst_unused:UNUSED_PAD src0_sel:DWORD src1_sel:WORD_1\n\t" \
  "v_mfma_f32_16x16x32_bf16 v[168:171], v[220:223], v[136:139], v[168:171]\n\t" \
  "ds_read_b64_tr_b16 v[220:221], v249 offset:10752\n\t" \
  "ds_read_u16_d16_hi v199, v199 offset:0\n\t" \
  "v_mfma_f32_16x16x32_bf16 v[144:147], v[208:211], v[104:107], v[144:147]\n\t" \
  "s_waitcnt lgkmcnt(14)\n\t" \
  "ds_read_b64_tr_b16 v[222:223], v249 offset:11008\n\t" \
  "v_fma_mix_f32 v188, v188, v225, 0 op_sel_hi:[0,1,0]\n\t" \
  "v_mfma_f32_16x16x16_bf16 v[36:39], v[246:247], v[236:237], 0\n\t" \
  "ds_read_u16_d16_hi v204, v204 offset:0\n\t" \
  "v_fma_mix_f32 v189, v189, v224, 0 op_sel_hi:[0,1,0]\n\t" \
  "v_mfma_f32_16x16x32_bf16 v[152:155], v[208:211], v[116:119], v[152:155]\n\t" \
  "ds_read_u16_d16_hi v205, v205 offset:0\n\t" \
  "v_lshlrev_b32_sdwa v206, 1, v206 dst_sel:DWORD dst_unused:UNUSED_PAD src0_sel:DWORD src1_sel:WORD_1\n\t" \
  "v_mfma_f32_16x16x32_bf16 v[160:163], v[208:211], v[128:131], v[160:163]\n\t" \
  "ds_read_u16_d16_hi v206, v206 offset:0\n\t" \
  "v_lshlrev_b32_sdwa v207, 1, v207 dst_sel:DWORD dst_unused:UNUSED_PAD src0_sel:DWORD src1_sel:WORD_1\n\t" \
  "v_mfma_f32_16x16x32_bf16 v[168:171], v[208:211], v[140:143], v[168:171]\n\t" \
  "s_waitcnt lgkmcnt(12)\n\t" \
  "ds_read_b64_tr_b16 v[208:209], v250 offset:18480\n\t" \
  "v_lshlrev_b32_sdwa v144, 1, v144 dst_sel:DWORD dst_unused:UNUSED_PAD src0_sel:DWORD src1_sel:WORD_1\n\t" \
  "v_mfma_f32_16x16x32_bf16 v[148:151], v[212:215], v[96:99], 0\n\t" \
  "ds_read_u16_d16_hi v207, v207 offset:0\n\t" \
  "v_mul_f32 v152, %[kk], v152\n\t" \
  "v_mfma_f32_16x16x32_bf16 v[156:159], v[212:215], v[108:111], 0\n\t" \
  "ds_read_b64_tr_b16 v[210:211], v250 offset:19248\n\t" \
  "v_lshlrev_b32_sdwa v145, 1, v145 dst_sel:DWORD dst_unused:UNUSED_PAD src0_sel:DWORD src1_sel:WORD_1\n\t" \
  "v_mfma_f32_16x16x16_bf16 v[40:43], v[246:247], v[242:243], 0\n\t" \
  "ds_read_u16_d16_hi v144, v144 offset:0\n\t" \
  "v_mul_f32 v153, %[kk], v153\n\t" \
  "v_mfma_f32_16x16x32_bf16 v[164:167], v[212:215], v[120:123], 0\n\t" \
  "ds_read_u16_d16_hi v145, v145 offset:0\n\t" \
  "v_cvt_pk_f16_f32 v224, v152, v153\n\t" \
  "v_mfma_f32_16x16x32_bf16 v[172:175], v[212:215], v[132:135], 0\n\t" \
  "s_waitcnt lgkmcnt(13)\n\t" \
  "ds_read_b64_tr_b16 v[212:213], v250 offset:18488\n\t" \
  "v_lshlrev_b32_sdwa v146, 1, v146 dst_sel:DWORD dst_unused:UNUSED_PAD src0_sel:DWORD src1_sel:WORD_1\n\t" \
  "v_mfma_f32_16x16x32_bf16 v[148:151], v[216:219], v[100:103], v[148:151]\n\t" \
  "ds_read_u16_d16_hi v146, v146 offset:0\n\t" \
  "v_pk_fma_f16 v225, v224, v224, %[h1] neg_lo:[1,0,0] neg_hi:[1,0,0]\n\t" \
  "v_mfma_f32_16x16x16_bf16 v[44:47], v[246:247], v[244:245], 0\n\t" \
  "ds_read_b64_tr_b16 v[214:215], v250 offset:19256\n\t" \
  "v_lshlrev_b32_sdwa v147, 1, v147 dst_sel:DWORD dst_unused:UNUSED_PAD src0_sel:DWORD src1_sel:WORD_1\n\t" \
  "v_mfma_f32_16x16x32_bf16 v[156:159], v[216:219], v[112:115], v[156:159]\n\t" \
  "ds_read_u16_d16_hi v147, v147 offset:0\n\t" \
  "v_pk_mul_f16 v225, v224, v225\n\t" \
  "v_mfma_f32_16x16x32_bf16 v[164:167], v[216:219], v[124:127], v[164:167]\n\t" \
  "v_lshlrev_b32_sdwa v154, 1, v154 dst_sel:DWORD dst_unused:UNUSED_PAD src0_sel:DWORD src1_sel:WORD_1\n\t" \
  "v_exp_f16 v224, v225\n\t" \
  "v_mfma_f32_16x16x32_bf16 v[172:175], v[216:219], v[136:139], v[172:175]\n\t" \
  "s_waitcnt lgkmcnt(12)\n\t" \
  "ds_read_b64_tr_b16 v[216:217], v250 offset:18544\n\t" \
  "v_lshlrev_b32_sdwa v155, 1, v155 dst_sel:DWORD dst_unused:UNUSED_PAD src0_sel:DWORD src1_sel:WORD_1\n\t" \
  "v_mfma_f32_16x16x32_bf16 v[148:151], v[220:223], v[104:107], v[148:151]\n\t" \
  "ds_read_u16_d16_hi v154, v154 offset:0\n\t" \
  "v_lshlrev_b32_sdwa v160, 1, v160 dst_sel:DWORD dst_unused:UNUSED_PAD src0_sel:DWORD src1_sel:WORD_1\n\t" \
  "v_mfma_f32_16x16x32_bf16 v[156:159], v[220:223], v[116:119], v[156:159]\n\t" \
  "ds_read_b64_tr_b16 v[218:219], v250 offset:19312\n\t" \
  "v_exp_f16_sdwa v224, v225 dst_sel:WORD_1 dst_unused:UNUSED_PRESERVE src0_sel:WORD_1\n\t" \
  "v_mfma_f32_16x16x32_bf16 v[164:167], v[220:223], v[128:131], v[164:167]\n\t" \
  "ds_read_u16_d16_hi v155, v155 offset:0\n\t" \
  "v_lshlrev_b32_sdwa v161, 1, v161 dst_sel:DWORD dst_unused:UNUSED_PAD src0_sel:DWORD src1_sel:WORD_1\n\t" \
  "v_mfma_f32_16x16x32_bf16 v[172:175], v[220:223], v[140:143], v[172:175]\n\t" \
  "s_waitcnt lgkmcnt(10)\n\t" \
  "ds_read_b64_tr_b16 v[220:221], v250 offset:18552\n\t" \
  "v_pk_fma_f16 v224, v224, %[hc], %[hc]\n\t" \
  "v_smfmac_f32_16x16x64_bf16 v[0:3], v[208:211], v[176:183], v248\n\t" \
  "ds_read_u16_d16_hi v160, v160 offset:0\n\t" \
  "v_lshlrev_b32_sdwa v162, 1, v162 dst_sel:DWORD dst_unused:UNUSED_PAD src0_sel:DWORD src1_sel:WORD_1\n\t" \
  "v_smfmac_f32_16x16x64_bf16 v[24:27], v[208:211], v[184:191], v248\n\t" \
  "ds_read_b64_tr_b16 v[222:223], v250 offset:19320\n\t" \
  "v_rcp_f16 v225, v224\n\t" \
  "v_smfmac_f32_16x16x64_bf16 v[48:51], v[208:211], v[192:199], v248\n\t" \
  "ds_read_u16_d16_hi v161, v161 offset:0\n\t" \
  "v_lshlrev_b32_sdwa v163, 1, v163 dst_sel:DWORD dst_unused:UNUSED_PAD src0_sel:DWORD src1_sel:WORD_1\n\t" \
  "v_smfmac_f32_16x16x64_bf16 v[72:75], v[208:211], v[200:207], v248\n\t" \
  "s_waitcnt lgkmcnt(9)\n\t" \
  "ds_read_b64_tr_b16 v[208:209], v250 offset:18608\n\t" \
  "v_lshlrev_b32_sdwa v168, 1, v168 dst_sel:DWORD dst_unused:UNUSED_PAD src0_sel:DWORD src1_sel:WORD_1\n\t" \
  "v_smfmac_f32_16x16x64_bf16 v[4:7], v[212:215], v[176:183], v248\n\t" \
  "ds_read_u16_d16_hi v162, v162 offset:0\n\t" \
  "v_rcp_f16_sdwa v224, v224 dst_sel:DWORD dst_unused:UNUSED_PAD src0_sel:WORD_1\n\t" \
  "v_smfmac_f32_16x16x64_bf16 v[28:31], v[212:215], v[184:191], v248\n\t" \
  "s_add_u32 %[ob], %[ob], %[dob]\n\t" \
  "ds_read_b64_tr_b16 v[210:211], v250 offset:19376\n\t" \
  "v_lshlrev_b32_sdwa v169, 1, v169 dst_sel:DWORD dst_unused:UNUSED_PAD src0_sel:DWORD src1_sel:WORD_1\n\t" \
  "v_smfmac_f32_16x16x64_bf16 v[52:55], v[212:215], v[192:199], v248\n\t" \
  "ds_read_u16_d16_hi v163, v163 offset:0\n\t" \
  "v_fma_mix_f32 v152, v152, v225, 0 op_sel_hi:[0,1,0]\n\t" \
  "v_smfmac_f32_16x16x64_bf16 v[76:79], v[212:215], v[200:207], v248\n\t" \
  "s_waitcnt lgkmcnt(9)\n\t" \
  "ds_read_b64_tr_b16 v[212:213], v250 offset:18616\n\t" \
  "v_lshlrev_b32_sdwa v170, 1, v170 dst_sel:DWORD dst_unused:UNUSED_PAD src0_sel:DWORD src1_sel:WORD_1\n\t" \
  "v_smfmac_f32_16x16x64_bf16 v[8:11], v[216:219], v[176:183], v248\n\t" \
  "ds_read_u16_d16_hi v168, v168 offset:0\n\t" \
  "v_fma_mix_f32 v153, v153, v224, 0 op_sel_hi:[0,1,0]\n\t" \
  "v_smfmac_f32_16x16x64_bf16 v[32:35], v[216:219], v[184:191], v248\n\t" \
  "ds_read_b64_tr_b16 v[214:215], v250 offset:19384\n\t" \
  "v_lshlrev_b32_sdwa v171, 1, v171 dst_sel:DWORD dst_unused:UNUSED_PAD src0_sel:DWORD src1_sel:WORD_1\n\t" \
  "v_smfmac_f32_16x16x64_bf16 v[56:59], v[216:219], v[192:199], v248\n\t" \
  "ds_read_u16_d16_hi v169, v169 offset:0\n\t" \
  "v_mul_f32 v156, %[kk], v156\n\t" \
  "v_smfmac_f32_16x16x64_bf16 v[80:83], v[216:219], v[200:207], v248\n\t" \
  "s_waitcnt lgkmcnt(9)\n\t" \
  "s_barrier\n\t" \
  "ds_read_b64_tr_b16 v[216:217], v249 offset:12288\n\t" \
  "v_lshlrev_b32_sdwa v148, 1, v148 dst_sel:DWORD dst_unused:UNUSED_PAD src0_sel:DWORD src1_sel:WORD_1\n\t" \
  "v_smfmac_f32_16x16x64_bf16 v[12:15], v[220:223], v[176:183], v248\n\t" \
  "ds_read_u16_d16_hi v170, v170 offset:0\n\t" \
  "v_mul_f32 v157, %[kk], v157\n\t" \
  "v_smfmac_f32_16x16x64_bf16 v[36:39], v[220:223], v[184:191], v248\n\t" \
  "ds_read_b64_tr_b16 v[218:219], v249 offset:12544\n\t" \
  "v_cvt_pk_f16_f32 v224, v156, v157\n\t" \
  "v_smfmac_f32_16x16x64_bf16 v[60:63], v[220:223], v[192:199], v248\n\t" \
  "ds_read_u16_d16_hi v171, v171 offset:0\n\t" \
  "v_lshlrev_b32_sdwa v149, 1, v149 dst_sel:DWORD dst_unused:UNUSED_PAD src0_sel:DWORD src1_sel:WORD_1\n\t" \
  "v_smfmac_f32_16x16x64_bf16 v[84:87], v[220:223], v[200:207], v248\n\t" \
  "s_waitcnt lgkmcnt(9)\n\t" \
  "ds_read_b64_tr_b16 v[220:221], v249 offset:14336\n\t" \
  "v_pk_fma_f16 v225, v224, v224, %[h1] neg_lo:[1,0,0] neg_hi:[1,0,0]\n\t" \
  "v_smfmac_f32_16x16x64_bf16 v[16:19], v[208:211], v[176:183], v248\n\t" \
  "s_add_u32 %[rs], %[rs], 1\n\t" \
  "ds_read_u16_d16_hi v148, v148 offset:0\n\t" \
  "v_lshlrev_b32_sdwa v150, 1, v150 dst_sel:DWORD dst_unused:UNUSED_PAD src0_sel:DWORD src1_sel:WORD_1\n\t" \
  "v_smfmac_f32_16x16x64_bf16 v[40:43], v[208:211], v[184:191], v248\n\t" \
  "s_cmp_eq_u32 %[rs], 6\n\t" \
  "ds_read_b64_tr_b16 v[222:223], v249 offset:14592\n\t" \
  "v_pk_mul_f16 v225, v224, v225\n\t" \
  "v_smfmac_f32_16x16x64_bf16 v[64:67], v[208:211], v[192:199], v248\n\t" \
  "ds_read_u16_d16_hi v149, v149 offset:0\n\t" \
  "v_lshlrev_b32_sdwa v151, 1, v151 dst_sel:DWORD dst_unused:UNUSED_PAD src0_sel:DWORD src1_sel:WORD_1\n\t" \
  "v_smfmac_f32_16x16x64_bf16 v[88:91], v[208:211], v[200:207], v248\n\t" \
  "s_waitcnt lgkmcnt(9)\n\t" \
  "ds_read_b64_tr_b16 v[208:209], v249 offset:16384\n\t" \
  "v_exp_f16 v224, v225\n\t" \
  "v_smfmac_f32_16x16x64_bf16 v[20:23], v[212:215], v[176:183], v248\n\t" \
  "s_cselect_b32 %[rs], 0, %[rs]\n\t" \
  "ds_read_u16_d16_hi v150, v150 offset:0\n\t" \
  "v_lshlrev_b32_sdwa v158, 1, v158 dst_sel:DWORD dst_unused:UNUSED_PAD src0_sel:DWORD src1_sel:WORD_1\n\t" \
  "v_smfmac_f32_16x16x64_bf16 v[44:47], v[212:215], v[184:191], v248\n\t" \
  "s_cselect_b32 %[t0], -75920, %[ps]\n\t" \
  "ds_read_b64_tr_b16 v[210:211], v249 offset:16640\n\t" \
  "v_lshlrev_b32_sdwa v159, 1, v159 dst_sel:DWORD dst_unused:UNUSED_PAD src0_sel:DWORD src1_sel:WORD_1\n\t" \
  "v_smfmac_f32_16x16x64_bf16 v[68:71], v[212:215], v[192:199], v248\n\t" \
  "ds_read_u16_d16_hi v151, v151 offset:0\n\t" \
  "v_add_u32 v252, %[t0], v252\n\t" \
  "v_exp_f16_sdwa v224, v225 dst_sel:WORD_1 dst_unused:UNUSED_PRESERVE src0_sel:WORD_1\n\t" \
  "v_smfmac_f32_16x16x64_bf16 v[92:95], v[212:215], v[200:207], v248\n\t" \
  "s_waitcnt lgkmcnt(9)\n\t" \
  "ds_read_b64_tr_b16 v[212:213], v249 offset:12800\n\t" \
  "v_lshlrev_b32_sdwa v164, 1, v164 dst_sel:DWORD dst_unused:UNUSED_PAD src0_sel:DWORD src1_sel:WORD_1\n\t" \
  "v_mfma_f32_16x16x32_bf16 v[176:179], v[216:219], v[96:99], 0\n\t" \
  "ds_read_u16_d16_hi v158, v158 offset:0\n\t" \
  "v_pk_fma_f16 v224, v224, %[hc], %[hc]\n\t" \
  "v_mfma_f32_16x16x32_bf16 v[184:187], v[216:219], v[108:111], 0\n\t" \
  "ds_read_b64_tr_b16 v[214:215], v249 offset:13056\n\t" \
  "v_lshlrev_b32_sdwa v165, 1, v165 dst_sel:DWORD dst_unused:UNUSED_PAD src0_sel:DWORD src1_sel:WORD_1\n\t" \
  "v_mfma_f32_16x16x32_bf16 v[192:195], v[216:219], v[120:123], 0\n\t" \
  "ds_read_u16_d16_hi v159, v159 offset:0\n\t" \
  "v_rcp_f16 v225, v224\n\t" \
  "v_mfma_f32_16x16x32_bf16 v[200:203], v[216:219], v[132:135], 0\n\t" \
  "s_waitcnt lgkmcnt(9)\n\t" \
  "ds_read_b64_tr_b16 v[216:217], v249 offset:14848\n\t" \
  "v_lshlrev_b32_sdwa v166, 1, v166 dst_sel:DWORD dst_unused:UNUSED_PAD src0_sel:DWORD src1_sel:WORD_1\n\t" \
  "v_mfma_f32_16x16x32_bf16 v[176:179], v[220:223], v[100:103], v[176:179]\n\t" \
  "ds_read_u16_d16_hi v164, v164 offset:0\n\t" \
  "v_lshlrev_b32_sdwa v167, 1, v167 dst_sel:DWORD dst_unused:UNUSED_PAD src0_sel:DWORD src1_sel:WORD_1\n\t" \
  "v_mfma_f32_16x16x32_bf16 v[184:187], v[220:223], v[112:115], v[184:187]\n\t" \
  "ds_read_b64_tr_b16 v[218:219], v249 offset:15104\n\t" \
  "v_rcp_f16_sdwa v224, v224 dst_sel:DWORD dst_unused:UNUSED_PAD src0_sel:WORD_1\n\t" \
  "v_mfma_f32_16x16x32_bf16 v[192:195], v[220:223], v[124:127], v[192:195]\n\t" \
  "ds_read_u16_d16_hi v165, v165 offset:0\n\t" \
  "v_lshlrev_b32_sdwa v172, 1, v172 dst_sel:DWORD dst_unused:UNUSED_PAD src0_sel:DWORD src1_sel:WORD_1\n\t" \
  "v_mfma_f32_16x16x32_bf16 v[200:203], v[220:223], v[136:139], v[200:203]\n\t" \
  "s_waitcnt lgkmcnt(9)\n\t" \
  "ds_read_b64_tr_b16 v[220:221], v249 offset:16896\n\t" \
  "ds_read_u16_d16_hi v166, v166 offset:0\n\t" \
  "v_mfma_f32_16x16x32_bf16 v[176:179], v[208:211], v[104:107], v[176:179]\n\t" \
  "ds_read_b64_tr_b16 v[222:223], v249 offset:17152\n\t" \
  "v_lshlrev_b32_sdwa v173, 1, v173 dst_sel:DWORD dst_unused:UNUSED_PAD src0_sel:DWORD src1_sel:WORD_1\n\t" \
  "v_fma_mix_f32 v156, v156, v225, 0 op_sel_hi:[0,1,0]\n\t" \
  "v_mfma_f32_16x16x32_bf16 v[184:187], v[208:211], v[116:119], v[184:187]\n\t" \
  "ds_read_u16_d16_hi v167, v167 offset:0\n\t" \
  "v_fma_mix_f32 v157, v157, v224, 0 op_sel_hi:[0,1,0]\n\t" \
  "v_mfma_f32_16x16x32_bf16 v[192:195], v[208:211], v[128:131], v[192:195]\n\t" \
  "ds_read_u16_d16_hi v172, v172 offset:0\n\t" \
  "v_lshlrev_b32_sdwa v174, 1, v174 dst_sel:DWORD dst_unused:UNUSED_PAD src0_sel:DWORD src1_sel:WORD_1\n\t" \
  "v_mfma_f32_16x16x32_bf16 v[200:203], v[208:211], v[140:143], v[200:203]\n\t" \
  "s_waitcnt lgkmcnt(10)\n\t" \
  "ds_read_b64_tr_b16 v[208:209], v250 offset:0\n\t" \
  "v_lshlrev_b32_sdwa v175, 1, v175 dst_sel:DWORD dst_unused:UNUSED_PAD src0_sel:DWORD src1_sel:WORD_1\n\t" \
  "v_mfma_f32_16x16x32_bf16 v[180:183], v[212:215], v[96:99], 0\n\t" \
  "ds_read_u16_d16_hi v173, v173 offset:0\n\t" \
  "v_lshlrev_b32_sdwa v176, 1, v176 dst_sel:DWORD dst_unused:UNUSED_PAD src0_sel:DWORD src1_sel:WORD_1\n\t" \
  "v_mfma_f32_16x16x32_bf16 v[188:191], v[212:215], v[108:111], 0\n\t" \
  "ds_read_b64_tr_b16 v[210:211], v250 offset:768\n\t" \
  "v_mul_f32 v184, %[kk], v184\n\t" \
  "v_mfma_f32_16x16x32_bf16 v[196:199], v[212:215], v[120:123], 0\n\t" \
  "ds_read_u16_d16_hi v174, v174 offset:0\n\t" \
  "v_lshlrev_b32_sdwa v177, 1, v177 dst_sel:DWORD dst_unused:UNUSED_PAD src0_sel:DWORD src1_sel:WORD_1\n\t" \
  "v_mul_f32 v185, %[kk], v185\n\t" \
  "v_mfma_f32_16x16x32_bf16 v[204:207], v[212:215], v[132:135], 0\n\t" \
  "s_waitcnt lgkmcnt(10)\n\t" \
  "ds_read_b64_tr_b16 v[212:213], v250 offset:8\n\t" \
  "ds_read_u16_d16_hi v175, v175 offset:0\n\t" \
  "v_cvt_pk_f16_f32 v224, v184, v185\n\t" \
  "v_mfma_f32_16x16x32_bf16 v[180:183], v[216:219], v[100:103], v[180:183]\n\t" \
  "ds_read_b64_tr_b16 v[214:215], v250 offset:776\n\t" \
  "v_lshlrev_b32_sdwa v178, 1, v178 dst_sel:DWORD dst_unused:UNUSED_PAD src0_sel:DWORD src1_sel:WORD_1\n\t" \
  "v_mfma_f32_16x16x32_bf16 v[188:191], v[216:219], v[112:115], v[188:191]\n\t" \
  "ds_read_u16_d16_hi v176, v176 offset:0\n\t" \
  "v_pk_fma_f16 v225, v224, v224, %[h1] neg_lo:[1,0,0] neg_hi:[1,0,0]\n\t" \
  "v_mfma_f32_16x16x32_bf16 v[196:199], v[216:219], v[124:127], v[196:199]\n\t" \
  "ds_read_u16_d16_hi v177, v177 offset:0\n\t" \
  "v_lshlrev_b32_sdwa v179, 1, v179 dst_sel:DWORD dst_unused:UNUSED_PAD src0_sel:DWORD src1_sel:WORD_1\n\t" \
  "v_pk_mul_f16 v225, v224, v225\n\t" \
  "v_mfma_f32_16x16x32_bf16 v[204:207], v[216:219], v[136:139], v[204:207]\n\t" \
  "s_waitcnt lgkmcnt(11)\n\t" \
  "ds_read_b64_tr_b16 v[216:217], v250 offset:64\n\t" \
  "v_lshlrev_b32_sdwa v186, 1, v186 dst_sel:DWORD dst_unused:UNUSED_PAD src0_sel:DWORD src1_sel:WORD_1\n\t" \
  "v_mfma_f32_16x16x32_bf16 v[180:183], v[220:223], v[104:107], v[180:183]\n\t" \
  "ds_read_u16_d16_hi v178, v178 offset:0\n\t" \
  "v_exp_f16 v224, v225\n\t" \
  "v_mfma_f32_16x16x32_bf16 v[188:191], v[220:223], v[116:119], v[188:191]\n\t" \
  "ds_read_b64_tr_b16 v[218:219], v250 offset:832\n\t" \
  "v_lshlrev_b32_sdwa v187, 1, v187 dst_sel:DWORD dst_unused:UNUSED_PAD src0_sel:DWORD src1_sel:WORD_1\n\t" \
  "v_mfma_f32_16x16x32_bf16 v[196:199], v[220:223], v[128:131], v[196:199]\n\t" \
  "ds_read_u16_d16_hi v179, v179 offset:0\n\t" \
  "v_lshlrev_b32_sdwa v192, 1, v192 dst_sel:DWORD dst_unused:UNUSED_PAD src0_sel:DWORD src1_sel:WORD_1\n\t" \
  "v_mfma_f32_16x16x32_bf16 v[204:207], v[220:223], v[140:143], v[204:207]\n\t" \
  "s_waitcnt lgkmcnt(10)\n\t" \
  "ds_read_b64_tr_b16 v[220:221], v250 offset:72\n\t" \
  "ds_read_u16_d16_hi v186, v186 offset:0\n\t" \
  "v_exp_f16_sdwa v224, v225 dst_sel:WORD_1 dst_unused:UNUSED_PRESERVE src0_sel:WORD_1\n\t" \
  "v_smfmac_f32_16x16x64_bf16 v[0:3], v[208:211], v[144:151], v248\n\t" \
  "ds_read_b64_tr_b16 v[222:223], v250 offset:840\n\t" \
  "v_lshlrev_b32_sdwa v193, 1, v193 dst_sel:DWORD dst_unused:UNUSED_PAD src0_sel:DWORD src1_sel:WORD_1\n\t" \
  "v_smfmac_f32_16x16x64_bf16 v[24:27], v[208:211], v[152:159], v248\n\t" \
  "ds_read_u16_d16_hi v187, v187 offset:0\n\t" \
  "v_pk_fma_f16 v224, v224, %[hc], %[hc]\n\t" \
  "v_smfmac_f32_16x16x64_bf16 v[48:51], v[208:211], v[160:167], v248\n\t" \
  "s_waitcnt lgkmcnt(11)\n\t" \
  "ds_read_u16_d16_hi v192, v192 offset:0\n\t" \
  "v_lshlrev_b32_sdwa v194, 1, v194 dst_sel:DWORD dst_unused:UNUSED_PAD src0_sel:DWORD src1_sel:WORD_1\n\t" \
  "v_smfmac_f32_16x16x64_bf16 v[72:75], v[208:211], v[168:175], v248\n\t" \
  "s_waitcnt lgkmcnt(11)\n\t" \
  "ds_read_b64_tr_b16 v[208:209], v250 offset:128\n\t" \
  "v_rcp_f16 v225, v224\n\t" \
  "v_smfmac_f32_16x16x64_bf16 v[4:7], v[212:215], v[144:151], v248\n\t" \
  "ds_read_u16_d16_hi v193, v193 offset:0\n\t" \
  "v_lshlrev_b32_sdwa v195, 1, v195 dst_sel:DWORD dst_unused:UNUSED_PAD src0_sel:DWORD src1_sel:WORD_1\n\t" \
  "v_smfmac_f32_16x16x64_bf16 v[28:31], v[212:215], v[152:159], v248\n\t" \
  "ds_read_b64_tr_b16 v[210:211], v250 offset:896\n\t" \
  "v_lshlrev_b32_sdwa v200, 1, v200 dst_sel:DWORD dst_unused:UNUSED_PAD src0_sel:DWORD src1_sel:WORD_1\n\t" \
  "v_smfmac_f32_16x16x64_bf16 v[52:55], v[212:215], v[160:167], v248\n\t" \
  "ds_read_u16_d16_hi v194, v194 offset:0\n\t" \
  "v_rcp_f16_sdwa v224, v224 dst_sel:DWORD dst_unused:UNUSED_PAD src0_sel:WORD_1\n\t" \
  "v_smfmac_f32_16x16x64_bf16 v[76:79], v[212:215], v[168:175], v248\n\t" \
  "s_waitcnt lgkmcnt(10)\n\t" \
  "ds_read_b64_tr_b16 v[212:213], v250 offset:136\n\t" \
  "v_lshlrev_b32_sdwa v201, 1, v201 dst_sel:DWORD dst_unused:UNUSED_PAD src0_sel:DWORD src1_sel:WORD_1\n\t" \
  "v_smfmac_f32_16x16x64_bf16 v[8:11], v[216:219], v[144:151], v248\n\t" \
  "ds_read_u16_d16_hi v195, v195 offset:0\n\t" \
  "v_fma_mix_f32 v184, v184, v225, 0 op_sel_hi:[0,1,0]\n\t" \
  "v_smfmac_f32_16x16x64_bf16 v[32:35], v[216:219], v[152:159], v248\n\t" \
  "ds_read_b64_tr_b16 v[214:215], v250 offset:904\n\t" \
  "v_lshlrev_b32_sdwa v202, 1, v202 dst_sel:DWORD dst_unused:UNUSED_PAD src0_sel:DWORD src1_sel:WORD_1\n\t" \
  "v_smfmac_f32_16x16x64_bf16 v[56:59], v[216:219], v[160:167], v248\n\t" \
  "ds_read_u16_d16_hi v200, v200 offset:0\n\t" \
  "v_fma_mix_f32 v185, v185, v224, 0 op_sel_hi:[0,1,0]\n\t" \
  "v_smfmac_f32_16x16x64_bf16 v[80:83], v[216:219], v[168:175], v248\n\t" \
  "s_waitcnt lgkmcnt(10)\n\t" \
  "s_barrier\n\t" \
  "ds_read_b64_tr_b16 v[216:217], v249 offset:0\n\t" \
  "ds_read_b128 v[120:123], v251 offset:0\n\t" \
  "v_lshlrev_b32_sdwa v203, 1, v203 dst_sel:DWORD dst_unused:UNUSED_PAD src0_sel:DWORD src1_sel:WORD_1\n\t" \
  "v_smfmac_f32_16x16x64_bf16 v[12:15], v[220:223], v[144:151], v248\n\t" \
  "ds_read_b64_tr_b16 v[218:219], v249 offset:256\n\t" \
  "ds_read_b128 v[124:127], v251 offset:4096\n\t" \
  "v_mul_f32 v188, %[kk], v188\n\t" \
  "v_smfmac_f32_16x16x64_bf16 v[36:39], v[220:223], v[152:159], v248\n\t" \
  "ds_read_b128 v[128:131], v251 offset:8192\n\t" \
  "v_lshlrev_b32_sdwa v180, 1, v180 dst_sel:DWORD dst_unused:UNUSED_PAD src0_sel:DWORD src1_sel:WORD_1\n\t" \
  "v_mul_f32 v189, %[kk], v189\n\t" \
  "v_smfmac_f32_16x16x64_bf16 v[60:63], v[220:223], v[160:167], v248\n\t" \
  "ds_read_u16_d16_hi v201, v201 offset:0\n\t" \
  "v_cvt_pk_f16_f32 v224, v188, v189\n\t" \
  "v_smfmac_f32_16x16x64_bf16 v[84:87], v[220:223], v[168:175], v248\n\t" \
  "s_waitcnt lgkmcnt(11)\n\t" \
  "ds_read_b64_tr_b16 v[220:221], v249 offset:2048\n\t" \
  "ds_read_u16_d16_hi v202, v202 offset:0\n\t" \
  "v_lshlrev_b32_sdwa v181, 1, v181 dst_sel:DWORD dst_unused:UNUSED_PAD src0_sel:DWORD src1_sel:WORD_1\n\t" \
  "v_smfmac_f32_16x16x64_bf16 v[16:19], v[208:211], v[144:151], v248\n\t" \
  "ds_read_b64_tr_b16 v[222:223], v249 offset:2304\n\t" \
  "v_pk_fma_f16 v225, v224, v224, %[h1] neg_lo:[1,0,0] neg_hi:[1,0,0]\n\t" \
  "v_smfmac_f32_16x16x64_bf16 v[40:43], v[208:211], v[152:159], v248\n\t" \
  "ds_read_u16_d16_hi v203, v203 offset:0\n\t" \
  "v_lshlrev_b32_sdwa v182, 1, v182 dst_sel:DWORD dst_unused:UNUSED_PAD src0_sel:DWORD src1_sel:WORD_1\n\t" \
  "v_pk_mul_f16 v225, v224, v225\n\t" \
  "v_smfmac_f32_16x16x64_bf16 v[64:67], v[208:211], v[160:167], v248\n\t" \
  "ds_read_u16_d16_hi v180, v180 offset:0\n\t" \
  "v_lshlrev_b32_sdwa v183, 1, v183 dst_sel:DWORD dst_unused:UNUSED_PAD src0_sel:DWORD src1_sel:WORD_1\n\t" \
  "v_smfmac_f32_16x16x64_bf16 v[88:91], v[208:211], v[168:175], v248\n\t" \
  "s_waitcnt lgkmcnt(12)\n\t" \
  "ds_read_b64_tr_b16 v[208:209], v249 offset:4096\n\t" \
  "ds_read_u16_d16_hi v181, v181 offset:0\n\t" \
  "v_exp_f16 v224, v225\n\t" \
  "v_smfmac_f32_16x16x64_bf16 v[20:23], v[212:215], v[144:151], v248\n\t" \
  "ds_read_b64_tr_b16 v[210:211], v249 offset:4352\n\t" \
  "v_lshlrev_b32_sdwa v190, 1, v190 dst_sel:DWORD dst_unused:UNUSED_PAD src0_sel:DWORD src1_sel:WORD_1\n\t" \
  "v_smfmac_f32_16x16x64_bf16 v[44:47], v[212:215], v[152:159], v248\n\t" \
  "ds_read_u16_d16_hi v182, v182 offset:0\n\t" \
  "v_lshlrev_b32_sdwa v191, 1, v191 dst_sel:DWORD dst_unused:UNUSED_PAD src0_sel:DWORD src1_sel:WORD_1\n\t" \
  "v_smfmac_f32_16x16x64_bf16 v[68:71], v[212:215], v[160:167], v248\n\t" \
  "ds_read_u16_d16_hi v183, v183 offset:0\n\t" \
  "v_exp_f16_sdwa v224, v225 dst_sel:WORD_1 dst_unused:UNUSED_PRESERVE src0_sel:WORD_1\n\t" \
  "v_smfmac_f32_16x16x64_bf16 v[92:95], v[212:215], v[168:175], v248\n\t" \
  "s_waitcnt lgkmcnt(13)\n\t" \
  "ds_read_b64_tr_b16 v[212:213], v249 offset:512\n\t" \
  "v_lshlrev_b32_sdwa v196, 1, v196 dst_sel:DWORD dst_unused:UNUSED_PAD src0_sel:DWORD src1_sel:WORD_1\n\t" \
  "v_mfma_f32_16x16x32_bf16 v[144:147], v[216:219], v[96:99], 0\n\t" \
  "ds_read_u16_d16_hi v190, v190 offset:0\n\t" \
  "v_pk_fma_f16 v224, v224, %[hc], %[hc]\n\t" \
  "v_mfma_f32_16x16x32_bf16 v[152:155], v[216:219], v[108:111], 0\n\t" \
  "ds_read_b64_tr_b16 v[214:215], v249 offset:768\n\t" \
  "v_lshlrev_b32_sdwa v197, 1, v197 dst_sel:DWORD dst_unused:UNUSED_PAD src0_sel:DWORD src1_sel:WORD_1\n\t" \
  "v_mfma_f32_16x16x32_bf16 v[160:163], v[216:219], v[120:123], 0\n\t" \
  "ds_read_u16_d16_hi v191, v191 offset:0\n\t" \
  "v_rcp_f16 v225, v224\n\t" \
  "v_mfma_f32_16x16x32_bf16 v[168:171], v[216:219], v[132:135], 0\n\t" \
  "s_waitcnt lgkmcnt(11)\n\t" \
  "ds_read_b64_tr_b16 v[216:217], v249 offset:2560\n\t" \
  "v_lshlrev_b32_sdwa v198, 1, v198 dst_sel:DWORD dst_unused:UNUSED_PAD src0_sel:DWORD src1_sel:WORD_1\n\t" \
  "v_mfma_f32_16x16x32_bf16 v[144:147], v[220:223], v[100:103], v[144:147]\n\t" \
  "ds_read_u16_d16_hi v196, v196 offset:0\n\t" \
  "v_lshlrev_b32_sdwa v199, 1, v199 dst_sel:DWORD dst_unused:UNUSED_PAD src0_sel:DWORD src1_sel:WORD_1\n\t" \
  "v_mfma_f32_16x16x32_bf16 v[152:155], v[220:223], v[112:115], v[152:155]\n\t" \
  "ds_read_b64_tr_b16 v[218:219], v249 offset:2816\n\t" \
  "v_rcp_f16_sdwa v224, v224 dst_sel:DWORD dst_unused:UNUSED_PAD src0_sel:WORD_1\n\t" \
  "v_mfma_f32_16x16x32_bf16 v[160:163], v[220:223], v[124:127], v[160:163]\n\t" \
  "ds_read_u16_d16_hi v197, v197 offset:0\n\t" \
  "v_lshlrev_b32_sdwa v204, 1, v204 dst_sel:DWORD dst_unused:UNUSED_PAD src0_sel:DWORD src1_sel:WORD_1\n\t" \
  "v_mfma_f32_16x16x32_bf16 v[168:171], v[220:223], v[136:139], v[168:171]\n\t" \
  "s_waitcnt lgkmcnt(10)\n\t" \
  "ds_read_b64_tr_b16 v[220:221], v249 offset:4608\n\t" \
  "ds_read_u16_d16_hi v198, v198 offset:0\n\t" \
  "v_mfma_f32_16x16x32_bf16 v[144:147], v[208:211], v[104:107], v[144:147]\n\t" \
  "ds_read_b64_tr_b16 v[222:223], v249 offset:4864\n\t" \
  "v_lshlrev_b32_sdwa v205, 1, v205 dst_sel:DWORD dst_unused:UNUSED_PAD src0_sel:DWORD src1_sel:WORD_1\n\t" \
  "v_fma_mix_f32 v188, v188, v225, 0 op_sel_hi:[0,1,0]\n\t" \
  "v_mfma_f32_16x16x32_bf16 v[152:155], v[208:211], v[116:119], v[152:155]\n\t" \
  "ds_read_u16_d16_hi v199, v199 offset:0\n\t" \
  "v_fma_mix_f32 v189, v189, v224, 0 op_sel_hi:[0,1,0]\n\t" \
  "v_mfma_f32_16x16x32_bf16 v[160:163], v[208:211], v[128:131], v[160:163]\n\t" \
  "ds_read_u16_d16_hi v204, v204 offset:0\n\t" \
  "v_lshlrev_b32_sdwa v206, 1, v206 dst_sel:DWORD dst_unused:UNUSED_PAD src0_sel:DWORD src1_sel:WORD_1\n\t" \
  "v_mfma_f32_16x16x32_bf16 v[168:171], v[208:211], v[140:143], v[168:171]\n\t" \
  "s_waitcnt lgkmcnt(10)\n\t" \
  "ds_read_b64_tr_b16 v[208:209], v250 offset:6160\n\t" \
  "v_lshlrev_b32_sdwa v207, 1, v207 dst_sel:DWORD dst_unused:UNUSED_PAD src0_sel:DWORD src1_sel:WORD_1\n\t" \
  "v_mfma_f32_16x16x32_bf16 v[148:151], v[212:215], v[96:99], 0\n\t" \
  "ds_read_u16_d16_hi v205, v205 offset:0\n\t" \
  "v_lshlrev_b32_sdwa v144, 1, v144 dst_sel:DWORD dst_unused:UNUSED_PAD src0_sel:DWORD src1_sel:WORD_1\n\t" \
  "v_mfma_f32_16x16x32_bf16 v[156:159], v[212:215], v[108:111], 0\n\t" \
  "ds_read_b64_tr_b16 v[210:211], v250 offset:6928\n\t" \
  "v_mul_f32 v152, %[kk], v152\n\t" \
  "v_mfma_f32_16x16x32_bf16 v[164:167], v[212:215], v[120:123], 0\n\t" \
  "ds_read_u16_d16_hi v206, v206 offset:0\n\t" \
  "v_lshlrev_b32_sdwa v145, 1, v145 dst_sel:DWORD dst_unused:UNUSED_PAD src0_sel:DWORD src1_sel:WORD_1\n\t" \
  "v_mul_f32 v153, %[kk], v153\n\t" \
  "v_mfma_f32_16x16x32_bf16 v[172:175], v[212:215], v[132:135], 0\n\t" \
  "s_waitcnt lgkmcnt(10)\n\t" \
  "ds_read_b64_tr_b16 v[212:213], v250 offset:6168\n\t" \
  "ds_read_u16_d16_hi v207, v207 offset:0\n\t" \
  "v_cvt_pk_f16_f32 v224, v152, v153\n\t" \
  "v_mfma_f32_16x16x32_bf16 v[148:151], v[216:219], v[100:103], v[148:151]\n\t" \
  "ds_read_b64_tr_b16 v[214:215], v250 offset:6936\n\t" \
  "v_lshlrev_b32_sdwa v146, 1, v146 dst_sel:DWORD dst_unused:UNUSED_PAD src0_sel:DWORD src1_sel:WORD_1\n\t" \
  "v_mfma_f32_16x16x32_bf16 v[156:159], v[216:219], v[112:115], v[156:159]\n\t" \
  "ds_read_u16_d16_hi v144, v144 offset:0\n\t" \
  "v_pk_fma_f16 v225, v224, v224, %[h1] neg_lo:[1,0,0] neg_hi:[1,0,0]\n\t" \
  "v_mfma_f32_16x16x32_bf16 v[164:167], v[216:219], v[124:127], v[164:167]\n\t" \
  "ds_read_u16_d16_hi v145, v145 offset:0\n\t" \
  "v_lshlrev_b32_sdwa v147, 1, v147 dst_sel:DWORD dst_unused:UNUSED_PAD src0_sel:DWORD src1_sel:WORD_1\n\t" \
  "v_pk_mul_f16 v225, v224, v225\n\t" \
  "v_mfma_f32_16x16x32_bf16 v[172:175], v[216:219], v[136:139], v[172:175]\n\t" \
  "s_waitcnt lgkmcnt(11)\n\t" \
  "ds_read_b64_tr_b16 v[216:217], v250 offset:6224\n\t" \
  "v_lshlrev_b32_sdwa v154, 1, v154 dst_sel:DWORD dst_unused:UNUSED_PAD src0_sel:DWORD src1_sel:WORD_1\n\t" \
  "v_mfma_f32_16x16x32_bf16 v[148:151], v[220:223], v[104:107], v[148:151]\n\t" \
  "ds_read_u16_d16_hi v146, v146 offset:0\n\t" \
  "v_exp_f16 v224, v225\n\t" \
  "v_mfma_f32_16x16x32_bf16 v[156:159], v[220:223], v[116:119], v[156:159]\n\t" \
  "ds_read_b64_tr_b16 v[218:219], v250 offset:6992\n\t" \
  "v_lshlrev_b32_sdwa v155, 1, v155 dst_sel:DWORD dst_unused:UNUSED_PAD src0_sel:DWORD src1_sel:WORD_1\n\t" \
  "v_mfma_f32_16x16x32_bf16 v[164:167], v[220:223], v[128:131], v[164:167]\n\t" \
  "ds_read_u16_d16_hi v147, v147 offset:0\n\t" \
  "v_lshlrev_b32_sdwa v160, 1, v160 dst_sel:DWORD dst_unused:UNUSED_PAD src0_sel:DWORD src1_sel:WORD_1\n\t" \
  "v_mfma_f32_16x16x32_bf16 v[172:175], v[220:223], v[140:143], v[172:175]\n\t" \
  "s_waitcnt lgkmcnt(10)\n\t" \
  "ds_read_b64_tr_b16 v[220:221], v250 offset:6232\n\t" \
  "ds_read_u16_d16_hi v154, v154 offset:0\n\t" \
  "v_exp_f16_sdwa v224, v225 dst_sel:WORD_1 dst_unused:UNUSED_PRESERVE src0_sel:WORD_1\n\t" \
  "v_smfmac_f32_16x16x64_bf16 v[0:3], v[208:211], v[176:183], v248\n\t" \
  "ds_read_b64_tr_b16 v[222:223], v250 offset:7000\n\t" \
  "v_lshlrev_b32_sdwa v161, 1, v161 dst_sel:DWORD dst_unused:UNUSED_PAD src0_sel:DWORD src1_sel:WORD_1\n\t" \
  "v_smfmac_f32_16x16x64_bf16 v[24:27], v[208:211], v[184:191], v248\n\t" \
  "ds_read_u16_d16_hi v155, v155 offset:0\n\t" \
  "v_pk_fma_f16 v224, v224, %[hc], %[hc]\n\t" \
  "v_smfmac_f32_16x16x64_bf16 v[48:51], v[208:211], v[192:199], v248\n\t" \
  "s_waitcnt lgkmcnt(11)\n\t" \
  "ds_read_u16_d16_hi v160, v160 offset:0\n\t" \
  "v_lshlrev_b32_sdwa v162, 1, v162 dst_sel:DWORD dst_unused:UNUSED_PAD src0_sel:DWORD src1_sel:WORD_1\n\t" \
  "v_smfmac_f32_16x16x64_bf16 v[72:75], v[208:211], v[200:207], v248\n\t" \
  "s_waitcnt lgkmcnt(11)\n\t" \
  "ds_read_b64_tr_b16 v[208:209], v250 offset:6288\n\t" \
  "v_rcp_f16 v225, v224\n\t" \
  "v_smfmac_f32_16x16x64_bf16 v[4:7], v[212:215], v[176:183], v248\n\t" \
  "ds_read_b64 v[242:243], v252 offset:128\n\t" \
  "v_lshlrev_b32_sdwa v163, 1, v163 dst_sel:DWORD dst_unused:UNUSED_PAD src0_sel:DWORD src1_sel:WORD_1\n\t" \
  "v_smfmac_f32_16x16x64_bf16 v[28:31], v[212:215], v[184:191], v248\n\t" \
  "ds_read_b64_tr_b16 v[210:211], v250 offset:7056\n\t" \
  "ds_read_b64 v[244:245], v252 offset:136\n\t" \
  "v_smfmac_f32_16x16x64_bf16 v[52:55], v[212:215], v[192:199], v248\n\t" \
  "ds_read_u16_d16_hi v161, v161 offset:0\n\t" \
  "v_lshlrev_b32_sdwa v168, 1, v168 dst_sel:DWORD dst_unused:UNUSED_PAD src0_sel:DWORD src1_sel:WORD_1\n\t" \
  "v_rcp_f16_sdwa v224, v224 dst_sel:DWORD dst_unused:UNUSED_PAD src0_sel:WORD_1\n\t" \
  "v_smfmac_f32_16x16x64_bf16 v[76:79], v[212:215], v[200:207], v248\n\t" \
  "s_waitcnt lgkmcnt(11)\n\t" \
  "ds_read_b64_tr_b16 v[212:213], v250 offset:6296\n\t" \
  "ds_read_u16_d16_hi v162, v162 offset:0\n\t" \
  "v_smfmac_f32_16x16x64_bf16 v[8:11], v[216:219], v[176:183], v248\n\t" \
  "ds_read_b64_tr_b16 v[214:215], v250 offset:7064\n\t" \
  "v_lshlrev_b32_sdwa v169, 1, v169 dst_sel:DWORD dst_unused:UNUSED_PAD src0_sel:DWORD src1_sel:WORD_1\n\t" \
  "v_cvt_pk_bf16_f32 v230, v48, v49\n\t" \
  "v_smfmac_f32_16x16x64_bf16 v[32:35], v[216:219], v[184:191], v248\n\t" \
  "ds_read_u16_d16_hi v163, v163 offset:0\n\t" \
  "v_fma_mix_f32 v152, v152, v225, 0 op_sel_hi:[0,1,0]\n\t" \
  "v_cvt_pk_bf16_f32 v231, v50, v51\n\t" \
  "v_smfmac_f32_16x16x64_bf16 v[56:59], v[216:219], v[192:199], v248\n\t" \
  "ds_read_u16_d16_hi v168, v168 offset:0\n\t" \
  "v_lshlrev_b32_sdwa v170, 1, v170 dst_sel:DWORD dst_unused:UNUSED_PAD src0_sel:DWORD src1_sel:WORD_1\n\t" \
  "v_fma_mix_f32 v153, v153, v224, 0 op_sel_hi:[0,1,0]\n\t" \
  "v_cvt_pk_bf16_f32 v232, v52, v53\n\t" \
  "v_smfmac_f32_16x16x64_bf16 v[80:83], v[216:219], v[200:207], v248\n\t" \
  "s_waitcnt lgkmcnt(12)\n\t" \
  "s_barrier\n\t" \
  "ds_read_b64_tr_b16 v[216:217], v249 offset:6144\n\t" \
  "v_lshlrev_b32_sdwa v171, 1, v171 dst_sel:DWORD dst_unused:UNUSED_PAD src0_sel:DWORD src1_sel:WORD_1\n\t" \
  "v_cvt_pk_bf16_f32 v233, v54, v55\n\t" \
  "v_smfmac_f32_16x16x64_bf16 v[12:15], v[220:223], v[176:183], v248\n\t" \
  "buffer_store_dwordx4 v[230:233], v253, %[osrd], %[ob] offen offset:0 sc1\n\t" \
  "ds_read_b64_tr_b16 v[218:219], v249 offset:6400\n\t" \
  "v_mul_f32 v156, %[kk], v156\n\t" \
  "v_smfmac_f32_16x16x64_bf16 v[36:39], v[220:223], v[184:191], v248\n\t" \
  "ds_read_b64 v[230:231], v252 offset:0\n\t" \
  "ds_read_b64 v[232:233], v252 offset:8\n\t" \
  "v_lshlrev_b32_sdwa v148, 1, v148 dst_sel:DWORD dst_unused:UNUSED_PAD src0_sel:DWORD src1_sel:WORD_1\n\t" \
  "v_mul_f32 v157, %[kk], v157\n\t" \
  "v_smfmac_f32_16x16x64_bf16 v[60:63], v[220:223], v[192:199], v248\n\t" \
  "ds_read_u16_d16_hi v169, v169 offset:0\n\t" \
  "v_cvt_pk_f16_f32 v224, v156, v157\n\t" \
  "v_smfmac_f32_16x16x64_bf16 v[84:87], v[220:223], v[200:207], v248\n\t" \
  "s_waitcnt lgkmcnt(12)\n\t" \
  "ds_read_b64_tr_b16 v[220:221], v249 offset:8192\n\t" \
  "ds_read_u16_d16_hi v170, v170 offset:0\n\t" \
  "v_lshlrev_b32_sdwa v149, 1, v149 dst_sel:DWORD dst_unused:UNUSED_PAD src0_sel:DWORD src1_sel:WORD_1\n\t" \
  "v_smfmac_f32_16x16x64_bf16 v[16:19], v[208:211], v[176:183], v248\n\t" \
  "ds_read_b64_tr_b16 v[222:223], v249 offset:8448\n\t" \
  "v_pk_fma_f16 v225, v224, v224, %[h1] neg_lo:[1,0,0] neg_hi:[1,0,0]\n\t" \
  "v_cvt_pk_bf16_f32 v234, v56, v57\n\t" \
  "v_smfmac_f32_16x16x64_bf16 v[40:43], v[208:211], v[184:191], v248\n\t" \
  "ds_read_u16_d16_hi v171, v171 offset:0\n\t" \
  "v_lshlrev_b32_sdwa v150, 1, v150 dst_sel:DWORD dst_unused:UNUSED_PAD src0_sel:DWORD src1_sel:WORD_1\n\t" \
  "v_pk_mul_f16 v225, v224, v225\n\t" \
  "v_cvt_pk_bf16_f32 v235, v58, v59\n\t" \
  "v_smfmac_f32_16x16x64_bf16 v[64:67], v[208:211], v[192:199], v248\n\t" \
  "ds_read_u16_d16_hi v148, v148 offset:0\n\t" \
  "v_lshlrev_b32_sdwa v151, 1, v151 dst_sel:DWORD dst_unused:UNUSED_PAD src0_sel:DWORD src1_sel:WORD_1\n\t" \
  "v_cvt_pk_bf16_f32 v236, v60, v61\n\t" \
  "v_smfmac_f32_16x16x64_bf16 v[88:91], v[208:211], v[200:207], v248\n\t" \
  "s_waitcnt lgkmcnt(12)\n\t" \
  "ds_read_b64_tr_b16 v[208:209], v249 offset:10240\n\t" \
  "ds_read_u16_d16_hi v149, v149 offset:0\n\t" \
  "v_exp_f16 v224, v225\n\t" \
  "v_cvt_pk_bf16_f32 v237, v62, v63\n\t" \
  "v_smfmac_f32_16x16x64_bf16 v[20:23], v[212:215], v[176:183], v248\n\t" \
  "ds_read_b64_tr_b16 v[210:211], v249 offset:10496\n\t" \
  "buffer_store_dwordx4 v[234:237], v253, %[osrd], %[ob] offen offset:64 sc1\n\t" \
  "v_lshlrev_b32_sdwa v158, 1, v158 dst_sel:DWORD dst_unused:UNUSED_PAD src0_sel:DWORD src1_sel:WORD_1\n\t" \
  "v_smfmac_f32_16x16x64_bf16 v[44:47], v[212:215], v[184:191], v248\n\t" \
  "ds_read_b64 v[234:235], v252 offset:64\n\t" \
  "ds_read_b64 v[236:237], v252 offset:72\n\t" \
  "v_smfmac_f32_16x16x64_bf16 v[68:71], v[212:215], v[192:199], v248\n\t" \
  "s_waitcnt lgkmcnt(12)\n\t" \
  "ds_read_u16_d16_hi v150, v150 offset:0\n\t" \
  "v_lshlrev_b32_sdwa v159, 1, v159 dst_sel:DWORD dst_unused:UNUSED_PAD src0_sel:DWORD src1_sel:WORD_1\n\t" \
  "v_mfma_f32_16x16x16_bf16 v[48:51], v[246:247], v[230:231], 0\n\t" \
  "ds_read_u16_d16_hi v151, v151 offset:0\n\t" \
  "v_exp_f16_sdwa v224, v225 dst_sel:WORD_1 dst_unused:UNUSED_PRESERVE src0_sel:WORD_1\n\t" \
  "v_smfmac_f32_16x16x64_bf16 v[92:95], v[212:215], v[200:207], v248\n\t" \
  "ds_read_b64_tr_b16 v[212:213], v249 offset:6656\n\t" \
  "v_lshlrev_b32_sdwa v164, 1, v164 dst_sel:DWORD dst_unused:UNUSED_PAD src0_sel:DWORD src1_sel:WORD_1\n\t" \
  "v_mfma_f32_16x16x32_bf16 v[176:179], v[216:219], v[96:99], 0\n\t" \
  "ds_read_u16_d16_hi v158, v158 offset:0\n\t" \
  "v_pk_fma_f16 v224, v224, %[hc], %[hc]\n\t" \
  "v_cvt_pk_bf16_f32 v238, v64, v65\n\t" \
  "v_mfma_f32_16x16x32_bf16 v[184:187], v[216:219], v[108:111], 0\n\t" \
  "s_waitcnt lgkmcnt(14)\n\t" \
  "ds_read_b64_tr_b16 v[214:215], v249 offset:6912\n\t" \
  "v_lshlrev_b32_sdwa v165, 1, v165 dst_sel:DWORD dst_unused:UNUSED_PAD src0_sel:DWORD src1_sel:WORD_1\n\t" \
  "v_mfma_f32_16x16x16_bf16 v[52:55], v[246:247], v[232:233], 0\n\t" \
  "ds_read_u16_d16_hi v159, v159 offset:0\n\t" \
  "v_cvt_pk_bf16_f32 v239, v66, v67\n\t" \
  "v_mfma_f32_16x16x32_bf16 v[192:195], v[216:219], v[120:123], 0\n\t" \
  "ds_read_u16_d16_hi v164, v164 offset:0\n\t" \
  "v_rcp_f16 v225, v224\n\t" \
  "v_cvt_pk_bf16_f32 v240, v68, v69\n\t" \
  "v_mfma_f32_16x16x32_bf16 v[200:203], v[216:219], v[132:135], 0\n\t" \
  "s_waitcnt lgkmcnt(14)\n\t" \
  "ds_read_b64_tr_b16 v[216:217], v249 offset:8704\n\t" \
  "v_lshlrev_b32_sdwa v166, 1, v166 dst_sel:DWORD dst_unused:UNUSED_PAD src0_sel:DWORD src1_sel:WORD_1\n\t" \
  "v_cvt_pk_bf16_f32 v241, v70, v71\n\t" \
  "v_mfma_f32_16x16x32_bf16 v[176:179], v[220:223], v[100:103], v[176:179]\n\t" \
  "buffer_store_dwordx4 v[238:241], v253, %[osrd], %[ob] offen offset:128 sc1\n\t" \
  "v_lshlrev_b32_sdwa v167, 1, v167 dst_sel:DWORD dst_unused:UNUSED_PAD src0_sel:DWORD src1_sel:WORD_1\n\t" \
  "v_mfma_f32_16x16x32_bf16 v[184:187], v[220:223], v[112:115], v[184:187]\n\t" \
  "ds_read_b64_tr_b16 v[218:219], v249 offset:8960\n\t" \
  "v_rcp_f16_sdwa v224, v224 dst_sel:DWORD dst_unused:UNUSED_PAD src0_sel:WORD_1\n\t" \
  "v_mfma_f32_16x16x32_bf16 v[192:195], v[220:223], v[124:127], v[192:195]\n\t" \
  "s_waitcnt lgkmcnt(10)\n\t" \
  "ds_read_u16_d16_hi v165, v165 offset:0\n\t" \
  "v_lshlrev_b32_sdwa v172, 1, v172 dst_sel:DWORD dst_unused:UNUSED_PAD src0_sel:DWORD src1_sel:WORD_1\n\t" \
  "v_mfma_f32_16x16x16_bf16 v[56:59], v[246:247], v[234:235], 0\n\t" \
  "ds_read_u16_d16_hi v166, v166 offset:0\n\t" \
  "v_lshlrev_b32_sdwa v173, 1, v173 dst_sel:DWORD dst_unused:UNUSED_PAD src0_sel:DWORD src1_sel:WORD_1\n\t" \
  "v_mfma_f32_16x16x32_bf16 v[200:203], v[220:223], v[136:139], v[200:203]\n\t" \
  "ds_read_b64_tr_b16 v[220:221], v249 offset:10752\n\t" \
  "ds_read_u16_d16_hi v167, v167 offset:0\n\t" \
  "v_mfma_f32_16x16x32_bf16 v[176:179], v[208:211], v[104:107], v[176:179]\n\t" \
  "ds_read_b64_tr_b16 v[222:223], v249 offset:11008\n\t" \
  "v_fma_mix_f32 v156, v156, v225, 0 op_sel_hi:[0,1,0]\n\t" \
  "v_mfma_f32_16x16x32_bf16 v[184:187], v[208:211], v[116:119], v[184:187]\n\t" \
  "s_waitcnt lgkmcnt(14)\n\t" \
  "ds_read_u16_d16_hi v172, v172 offset:0\n\t" \
  "v_fma_mix_f32 v157, v157, v224, 0 op_sel_hi:[0,1,0]\n\t" \
  "v_mfma_f32_16x16x16_bf16 v[60:63], v[246:247], v[236:237], 0\n\t" \
  "ds_read_u16_d16_hi v173, v173 offset:0\n\t" \
  "v_lshlrev_b32_sdwa v174, 1, v174 dst_sel:DWORD dst_unused:UNUSED_PAD src0_sel:DWORD src1_sel:WORD_1\n\t" \
  "v_mfma_f32_16x16x32_bf16 v[192:195], v[208:211], v[128:131], v[192:195]\n\t" \
  "ds_read_u16_d16_hi v174, v174 offset:0\n\t" \
  "v_lshlrev_b32_sdwa v175, 1, v175 dst_sel:DWORD dst_unused:UNUSED_PAD src0_sel:DWORD src1_sel:WORD_1\n\t" \
  "v_mfma_f32_16x16x32_bf16 v[200:203], v[208:211], v[140:143], v[200:203]\n\t" \
  "s_waitcnt lgkmcnt(12)\n\t" \
  "ds_read_b64_tr_b16 v[208:209], v250 offset:12320\n\t" \
  "v_lshlrev_b32_sdwa v176, 1, v176 dst_sel:DWORD dst_unused:UNUSED_PAD src0_sel:DWORD src1_sel:WORD_1\n\t" \
  "v_mfma_f32_16x16x32_bf16 v[180:183], v[212:215], v[96:99], 0\n\t" \
  "ds_read_u16_d16_hi v175, v175 offset:0\n\t" \
  "v_mul_f32 v184, %[kk], v184\n\t" \
  "v_mfma_f32_16x16x32_bf16 v[188:191], v[212:215], v[108:111], 0\n\t" \
  "ds_read_b64_tr_b16 v[210:211], v250 offset:13088\n\t" \
  "v_lshlrev_b32_sdwa v177, 1, v177 dst_sel:DWORD dst_unused:UNUSED_PAD src0_sel:DWORD src1_sel:WORD_1\n\t" \
  "v_mfma_f32_16x16x32_bf16 v[196:199], v[212:215], v[120:123], 0\n\t" \
  "ds_read_u16_d16_hi v176, v176 offset:0\n\t" \
  "v_mul_f32 v185, %[kk], v185\n\t" \
  "v_mfma_f32_16x16x16_bf16 v[64:67], v[246:247], v[242:243], 0\n\t" \
  "ds_read_u16_d16_hi v177, v177 offset:0\n\t" \
  "v_cvt_pk_f16_f32 v224, v184, v185\n\t" \
  "v_mfma_f32_16x16x32_bf16 v[204:207], v[212:215], v[132:135], 0\n\t" \
  "s_waitcnt lgkmcnt(13)\n\t" \
  "ds_read_b64_tr_b16 v[212:213], v250 offset:12328\n\t" \
  "v_lshlrev_b32_sdwa v178, 1, v178 dst_sel:DWORD dst_unused:UNUSED_PAD src0_sel:DWORD src1_sel:WORD_1\n\t" \
  "v_mfma_f32_16x16x32_bf16 v[180:183], v[216:219], v[100:103], v[180:183]\n\t" \
  "ds_read_u16_d16_hi v178, v178 offset:0\n\t" \
  "v_pk_fma_f16 v225, v224, v224, %[h1] neg_lo:[1,0,0] neg_hi:[1,0,0]\n\t" \
  "v_mfma_f32_16x16x32_bf16 v[188:191], v[216:219], v[112:115], v[188:191]\n\t" \
  "ds_read_b64_tr_b16 v[214:215], v250 offset:13096\n\t" \
  "v_lshlrev_b32_sdwa v179, 1, v179 dst_sel:DWORD dst_unused:UNUSED_PAD src0_sel:DWORD src1_sel:WORD_1\n\t" \
  "v_mfma_f32_16x16x16_bf16 v[68:71], v[246:247], v[244:245], 0\n\t" \
  "ds_read_u16_d16_hi v179, v179 offset:0\n\t" \
  "v_pk_mul_f16 v225, v224, v225\n\t" \
  "v_mfma_f32_16x16x32_bf16 v[196:199], v[216:219], v[124:127], v[196:199]\n\t" \
  "v_lshlrev_b32_sdwa v186, 1, v186 dst_sel:DWORD dst_unused:UNUSED_PAD src0_sel:DWORD src1_sel:WORD_1\n\t" \
  "v_exp_f16 v224, v225\n\t" \
  "v_mfma_f32_16x16x32_bf16 v[204:207], v[216:219], v[136:139], v[204:207]\n\t" \
  "s_waitcnt lgkmcnt(12)\n\t" \
  "ds_read_b64_tr_b16 v[216:217], v250 offset:12384\n\t" \
  "v_lshlrev_b32_sdwa v187, 1, v187 dst_sel:DWORD dst_unused:UNUSED_PAD src0_sel:DWORD src1_sel:WORD_1\n\t" \
  "v_mfma_f32_16x16x32_bf16 v[180:183], v[220:223], v[104:107], v[180:183]\n\t" \
  "ds_read_u16_d16_hi v186, v186 offset:0\n\t" \
  "v_lshlrev_b32_sdwa v192, 1, v192 dst_sel:DWORD dst_unused:UNUSED_PAD src0_sel:DWORD src1_sel:WORD_1\n\t" \
  "v_mfma_f32_16x16x32_bf16 v[188:191], v[220:223], v[116:119], v[188:191]\n\t" \
  "ds_read_b64_tr_b16 v[218:219], v250 offset:13152\n\t" \
  "v_exp_f16_sdwa v224, v225 dst_sel:WORD_1 dst_unused:UNUSED_PRESERVE src0_sel:WORD_1\n\t" \
  "v_mfma_f32_16x16x32_bf16 v[196:199], v[220:223], v[128:131], v[196:199]\n\t" \
  "ds_read_u16_d16_hi v187, v187 offset:0\n\t" \
  "v_lshlrev_b32_sdwa v193, 1, v193 dst_sel:DWORD dst_unused:UNUSED_PAD src0_sel:DWORD src1_sel:WORD_1\n\t" \
  "v_mfma_f32_16x16x32_bf16 v[204:207], v[220:223], v[140:143], v[204:207]\n\t" \
  "s_waitcnt lgkmcnt(10)\n\t" \
  "ds_read_b64_tr_b16 v[220:221], v250 offset:12392\n\t" \
  "v_pk_fma_f16 v224, v224, %[hc], %[hc]\n\t" \
  "v_smfmac_f32_16x16x64_bf16 v[0:3], v[208:211], v[144:151], v248\n\t" \
  "ds_read_u16_d16_hi v192, v192 offset:0\n\t" \
  "v_lshlrev_b32_sdwa v194, 1, v194 dst_sel:DWORD dst_unused:UNUSED_PAD src0_sel:DWORD src1_sel:WORD_1\n\t" \
  "v_smfmac_f32_16x16x64_bf16 v[24:27], v[208:211], v[152:159], v248\n\t" \
  "ds_read_b64_tr_b16 v[222:223], v250 offset:13160\n\t" \
  "v_rcp_f16 v225, v224\n\t" \
  "v_smfmac_f32_16x16x64_bf16 v[48:51], v[208:211], v[160:167], v248\n\t" \
  "ds_read_u16_d16_hi v193, v193 offset:0\n\t" \
  "v_lshlrev_b32_sdwa v195, 1, v195 dst_sel:DWORD dst_unused:UNUSED_PAD src0_sel:DWORD src1_sel:WORD_1\n\t" \
  "v_smfmac_f32_16x16x64_bf16 v[72:75], v[208:211], v[168:175], v248\n\t" \
  "s_waitcnt lgkmcnt(9)\n\t" \
  "ds_read_b64_tr_b16 v[208:209], v250 offset:12448\n\t" \
  "v_lshlrev_b32_sdwa v200, 1, v200 dst_sel:DWORD dst_unused:UNUSED_PAD src0_sel:DWORD src1_sel:WORD_1\n\t" \
  "v_smfmac_f32_16x16x64_bf16 v[4:7], v[212:215], v[144:151], v248\n\t" \
  "ds_read_u16_d16_hi v194, v194 offset:0\n\t" \
  "v_rcp_f16_sdwa v224, v224 dst_sel:DWORD dst_unused:UNUSED_PAD src0_sel:WORD_1\n\t" \
  "v_smfmac_f32_16x16x64_bf16 v[28:31], v[212:215], v[152:159], v248\n\t" \
  "s_add_u32 %[ob], %[ob], %[dob]\n\t" \
  "ds_read_b64_tr_b16 v[210:211], v250 offset:13216\n\t" \
  "v_lshlrev_b32_sdwa v201, 1, v201 dst_sel:DWORD dst_unused:UNUSED_PAD src0_sel:DWORD src1_sel:WORD_1\n\t" \
  "v_smfmac_f32_16x16x64_bf16 v[52:55], v[212:215], v[160:167], v248\n\t" \
  "ds_read_u16_d16_hi v195, v195 offset:0\n\t" \
  "v_fma_mix_f32 v184, v184, v225, 0 op_sel_hi:[0,1,0]\n\t" \
  "v_smfmac_f32_16x16x64_bf16 v[76:79], v[212:215], v[168:175], v248\n\t" \
  "s_waitcnt lgkmcnt(9)\n\t" \
  "ds_read_b64_tr_b16 v[212:213], v250 offset:12456\n\t" \
  "v_lshlrev_b32_sdwa v202, 1, v202 dst_sel:DWORD dst_unused:UNUSED_PAD src0_sel:DWORD src1_sel:WORD_1\n\t" \
  "v_smfmac_f32_16x16x64_bf16 v[8:11], v[216:219], v[144:151], v248\n\t" \
  "ds_read_u16_d16_hi v200, v200 offset:0\n\t" \
  "v_fma_mix_f32 v185, v185, v224, 0 op_sel_hi:[0,1,0]\n\t" \
  "v_smfmac_f32_16x16x64_bf16 v[32:35], v[216:219], v[152:159], v248\n\t" \
  "ds_read_b64_tr_b16 v[214:215], v250 offset:13224\n\t" \
  "v_lshlrev_b32_sdwa v203, 1, v203 dst_sel:DWORD dst_unused:UNUSED_PAD src0_sel:DWORD src1_sel:WORD_1\n\t" \
  "v_smfmac_f32_16x16x64_bf16 v[56:59], v[216:219], v[160:167], v248\n\t" \
  "ds_read_u16_d16_hi v201, v201 offset:0\n\t" \
  "v_mul_f32 v188, %[kk], v188\n\t" \
  "v_smfmac_f32_16x16x64_bf16 v[80:83], v[216:219], v[168:175], v248\n\t" \
  "s_waitcnt lgkmcnt(9)\n\t" \
  "s_barrier\n\t" \
  "ds_read_b64_tr_b16 v[216:217], v249 offset:12288\n\t" \
  "v_lshlrev_b32_sdwa v180, 1, v180 dst_sel:DWORD dst_unused:UNUSED_PAD src0_sel:DWORD src1_sel:WORD_1\n\t" \
  "v_smfmac_f32_16x16x64_bf16 v[12:15], v[220:223], v[144:151], v248\n\t" \
  "ds_read_u16_d16_hi v202, v202 offset:0\n\t" \
  "v_mul_f32 v189, %[kk], v189\n\t" \
  "v_smfmac_f32_16x16x64_bf16 v[36:39], v[220:223], v[152:159], v248\n\t" \
  "ds_read_b64_tr_b16 v[218:219], v249 offset:12544\n\t" \
  "v_cvt_pk_f16_f32 v224, v188, v189\n\t" \
  "v_smfmac_f32_16x16x64_bf16 v[60:63], v[220:223], v[160:167], v248\n\t" \
  "ds_read_u16_d16_hi v203, v203 offset:0\n\t" \
  "v_lshlrev_b32_sdwa v181, 1, v181 dst_sel:DWORD dst_unused:UNUSED_PAD src0_sel:DWORD src1_sel:WORD_1\n\t" \
  "v_smfmac_f32_16x16x64_bf16 v[84:87], v[220:223], v[168:175], v248\n\t" \
  "s_waitcnt lgkmcnt(9)\n\t" \
  "ds_read_b64_tr_b16 v[220:221], v249 offset:14336\n\t" \
  "v_pk_fma_f16 v225, v224, v224, %[h1] neg_lo:[1,0,0] neg_hi:[1,0,0]\n\t" \
  "v_smfmac_f32_16x16x64_bf16 v[16:19], v[208:211], v[144:151], v248\n\t" \
  "s_add_u32 %[rs], %[rs], 1\n\t" \
  "ds_read_u16_d16_hi v180, v180 offset:0\n\t" \
  "v_lshlrev_b32_sdwa v182, 1, v182 dst_sel:DWORD dst_unused:UNUSED_PAD src0_sel:DWORD src1_sel:WORD_1\n\t" \
  "v_smfmac_f32_16x16x64_bf16 v[40:43], v[208:211], v[152:159], v248\n\t" \
  "s_cmp_eq_u32 %[rs], 6\n\t" \
  "ds_read_b64_tr_b16 v[222:223], v249 offset:14592\n\t" \
  "v_pk_mul_f16 v225, v224, v225\n\t" \
  "v_smfmac_f32_16x16x64_bf16 v[64:67], v[208:211], v[160:167], v248\n\t" \
  "ds_read_u16_d16_hi v181, v181 offset:0\n\t" \
  "v_lshlrev_b32_sdwa v183, 1, v183 dst_sel:DWORD dst_unused:UNUSED_PAD src0_sel:DWORD src1_sel:WORD_1\n\t" \
  "v_smfmac_f32_16x16x64_bf16 v[88:91], v[208:211], v[168:175], v248\n\t" \
  "s_waitcnt lgkmcnt(9)\n\t" \
  "ds_read_b64_tr_b16 v[208:209], v249 offset:16384\n\t" \
  "v_exp_f16 v224, v225\n\t" \
  "v_smfmac_f32_16x16x64_bf16 v[20:23], v[212:215], v[144:151], v248\n\t" \
  "s_cselect_b32 %[rs], 0, %[rs]\n\t" \
  "ds_read_u16_d16_hi v182, v182 offset:0\n\t" \
  "v_lshlrev_b32_sdwa v190, 1, v190 dst_sel:DWORD dst_unused:UNUSED_PAD src0_sel:DWORD src1_sel:WORD_1\n\t" \
  "v_smfmac_f32_16x16x64_bf16 v[44:47], v[212:215], v[152:159], v248\n\t" \
  "s_cselect_b32 %[t0], -75920, %[ps]\n\t" \
  "ds_read_b64_tr_b16 v[210:211], v249 offset:16640\n\t" \
  "v_lshlrev_b32_sdwa v191, 1, v191 dst_sel:DWORD dst_unused:UNUSED_PAD src0_sel:DWORD src1_sel:WORD_1\n\t" \
  "v_smfmac_f32_16x16x64_bf16 v[68:71], v[212:215], v[160:167], v248\n\t" \
  "ds_read_u16_d16_hi v183, v183 offset:0\n\t" \
  "v_add_u32 v252, %[t0], v252\n\t" \
  "v_exp_f16_sdwa v224, v225 dst_sel:WORD_1 dst_unused:UNUSED_PRESERVE src0_sel:WORD_1\n\t" \
  "v_smfmac_f32_16x16x64_bf16 v[92:95], v[212:215], v[168:175], v248\n\t" \
  "s_waitcnt lgkmcnt(9)\n\t" \
  "ds_read_b64_tr_b16 v[212:213], v249 offset:12800\n\t" \
  "v_lshlrev_b32_sdwa v196, 1, v196 dst_sel:DWORD dst_unused:UNUSED_PAD src0_sel:DWORD src1_sel:WORD_1\n\t" \
  "v_mfma_f32_16x16x32_bf16 v[144:147], v[216:219], v[96:99], 0\n\t" \
  "ds_read_u16_d16_hi v190, v190 offset:0\n\t" \
  "v_pk_fma_f16 v224, v224, %[hc], %[hc]\n\t" \
  "v_mfma_f32_16x16x32_bf16 v[152:155], v[216:219], v[108:111], 0\n\t" \
  "ds_read_b64_tr_b16 v[214:215], v249 offset:13056\n\t" \
  "v_lshlrev_b32_sdwa v197, 1, v197 dst_sel:DWORD dst_unused:UNUSED_PAD src0_sel:DWORD src1_sel:WORD_1\n\t" \
  "v_mfma_f32_16x16x32_bf16 v[160:163], v[216:219], v[120:123], 0\n\t" \
  "ds_read_u16_d16_hi v191, v191 offset:0\n\t" \
  "v_rcp_f16 v225, v224\n\t" \
  "v_mfma_f32_16x16x32_bf16 v[168:171], v[216:219], v[132:135], 0\n\t" \
  "s_waitcnt lgkmcnt(9)\n\t" \
  "ds_read_b64_tr_b16 v[216:217], v249 offset:14848\n\t" \
  "v_lshlrev_b32_sdwa v198, 1, v198 dst_sel:DWORD dst_unused:UNUSED_PAD src0_sel:DWORD src1_sel:WORD_1\n\t" \
  "v_mfma_f32_16x16x32_bf16 v[144:147], v[220:223], v[100:103], v[144:147]\n\t" \
  "ds_read_u16_d16_hi v196, v196 offset:0\n\t" \
  "v_lshlrev_b32_sdwa v199, 1, v199 dst_sel:DWORD dst_unused:UNUSED_PAD src0_sel:DWORD src1_sel:WORD_1\n\t" \
  "v_mfma_f32_16x16x32_bf16 v[152:155], v[220:223], v[112:115], v[152:155]\n\t" \
  "ds_read_b64_tr_b16 v[218:219], v249 offset:15104\n\t" \
  "v_rcp_f16_sdwa v224, v224 dst_sel:DWORD dst_unused:UNUSED_PAD src0_sel:WORD_1\n\t" \
  "v_mfma_f32_16x16x32_bf16 v[160:163], v[220:223], v[124:127], v[160:163]\n\t" \
  "ds_read_u16_d16_hi v197, v197 offset:0\n\t" \
  "v_lshlrev_b32_sdwa v204, 1, v204 dst_sel:DWORD dst_unused:UNUSED_PAD src0_sel:DWORD src1_sel:WORD_1\n\t" \
  "v_mfma_f32_16x16x32_bf16 v[168:171], v[220:223], v[136:139], v[168:171]\n\t" \
  "s_waitcnt lgkmcnt(9)\n\t" \
  "ds_read_b64_tr_b16 v[220:221], v249 offset:16896\n\t" \
  "ds_read_u16_d16_hi v198, v198 offset:0\n\t" \
  "v_mfma_f32_16x16x32_bf16 v[144:147], v[208:211], v[104:107], v[144:147]\n\t" \
  "ds_read_b64_tr_b16 v[222:223], v249 offset:17152\n\t" \
  "v_lshlrev_b32_sdwa v205, 1, v205 dst_sel:DWORD dst_unused:UNUSED_PAD src0_sel:DWORD src1_sel:WORD_1\n\t" \
  "v_fma_mix_f32 v188, v188, v225, 0 op_sel_hi:[0,1,0]\n\t" \
  "v_mfma_f32_16x16x32_bf16 v[152:155], v[208:211], v[116:119], v[152:155]\n\t" \
  "ds_read_u16_d16_hi v199, v199 offset:0\n\t" \
  "v_fma_mix_f32 v189, v189, v224, 0 op_sel_hi:[0,1,0]\n\t" \
  "v_mfma_f32_16x16x32_bf16 v[160:163], v[208:211], v[128:131], v[160:163]\n\t" \
  "ds_read_u16_d16_hi v204, v204 offset:0\n\t" \
  "v_lshlrev_b32_sdwa v206, 1, v206 dst_sel:DWORD dst_unused:UNUSED_PAD src0_sel:DWORD src1_sel:WORD_1\n\t" \
  "v_mfma_f32_16x16x32_bf16 v[168:171], v[208:211], v[140:143], v[168:171]\n\t" \
  "s_waitcnt lgkmcnt(10)\n\t" \
  "ds_read_b64_tr_b16 v[208:209], v250 offset:18480\n\t" \
  "v_lshlrev_b32_sdwa v207, 1, v207 dst_sel:DWORD dst_unused:UNUSED_PAD src0_sel:DWORD src1_sel:WORD_1\n\t" \
  "v_mfma_f32_16x16x32_bf16 v[148:151], v[212:215], v[96:99], 0\n\t" \
  "ds_read_u16_d16_hi v205, v205 offset:0\n\t" \
  "v_lshlrev_b32_sdwa v144, 1, v144 dst_sel:DWORD dst_unused:UNUSED_PAD src0_sel:DWORD src1_sel:WORD_1\n\t" \
  "v_mfma_f32_16x16x32_bf16 v[156:159], v[212:215], v[108:111], 0\n\t" \
  "ds_read_b64_tr_b16 v[210:211], v250 offset:19248\n\t" \
  "v_mul_f32 v152, %[kk], v152\n\t" \
  "v_mfma_f32_16x16x32_bf16 v[164:167], v[212:215], v[120:123], 0\n\t" \
  "ds_read_u16_d16_hi v206, v206 offset:0\n\t" \
  "v_lshlrev_b32_sdwa v145, 1, v145 dst_sel:DWORD dst_unused:UNUSED_PAD src0_sel:DWORD src1_sel:WORD_1\n\t" \
  "v_mul_f32 v153, %[kk], v153\n\t" \
  "v_mfma_f32_16x16x32_bf16 v[172:175], v[212:215], v[132:135], 0\n\t" \
  "s_waitcnt lgkmcnt(10)\n\t" \
  "ds_read_b64_tr_b16 v[212:213], v250 offset:18488\n\t" \
  "ds_read_u16_d16_hi v207, v207 offset:0\n\t" \
  "v_cvt_pk_f16_f32 v224, v152, v153\n\t" \
  "v_mfma_f32_16x16x32_bf16 v[148:151], v[216:219], v[100:103], v[148:151]\n\t" \
  "ds_read_b64_tr_b16 v[214:215], v250 offset:19256\n\t" \
  "v_lshlrev_b32_sdwa v146, 1, v146 dst_sel:DWORD dst_unused:UNUSED_PAD src0_sel:DWORD src1_sel:WORD_1\n\t" \
  "v_mfma_f32_16x16x32_bf16 v[156:159], v[216:219], v[112:115], v[156:159]\n\t" \
  "ds_read_u16_d16_hi v144, v144 offset:0\n\t" \
  "v_pk_fma_f16 v225, v224, v224, %[h1] neg_lo:[1,0,0] neg_hi:[1,0,0]\n\t" \
  "v_mfma_f32_16x16x32_bf16 v[164:167], v[216:219], v[124:127], v[164:167]\n\t" \
  "ds_read_u16_d16_hi v145, v145 offset:0\n\t" \
  "v_lshlrev_b32_sdwa v147, 1, v147 dst_sel:DWORD dst_unused:UNUSED_PAD src0_sel:DWORD src1_sel:WORD_1\n\t" \
  "v_pk_mul_f16 v225, v224, v225\n\t" \
  "v_mfma_f32_16x16x32_bf16 v[172:175], v[216:219], v[136:139], v[172:175]\n\t" \
  "s_waitcnt lgkmcnt(11)\n\t" \
  "ds_read_b64_tr_b16 v[216:217], v250 offset:18544\n\t" \
  "v_lshlrev_b32_sdwa v154, 1, v154 dst_sel:DWORD dst_unused:UNUSED_PAD src0_sel:DWORD src1_sel:WORD_1\n\t" \
  "v_mfma_f32_16x16x32_bf16 v[148:151], v[220:223], v[104:107], v[148:151]\n\t" \
  "ds_read_u16_d16_hi v146, v146 offset:0\n\t" \
  "v_exp_f16 v224, v225\n\t" \
  "v_mfma_f32_16x16x32_bf16 v[156:159], v[220:223], v[116:119], v[156:159]\n\t" \
  "ds_read_b64_tr_b16 v[218:219], v250 offset:19312\n\t" \
  "v_lshlrev_b32_sdwa v155, 1, v155 dst_sel:DWORD dst_unused:UNUSED_PAD src0_sel:DWORD src1_sel:WORD_1\n\t" \
  "v_mfma_f32_16x16x32_bf16 v[164:167], v[220:223], v[128:131], v[164:167]\n\t" \
  "ds_read_u16_d16_hi v147, v147 offset:0\n\t" \
  "v_lshlrev_b32_sdwa v160, 1, v160 dst_sel:DWORD dst_unused:UNUSED_PAD src0_sel:DWORD src1_sel:WORD_1\n\t" \
  "v_mfma_f32_16x16x32_bf16 v[172:175], v[220:223], v[140:143], v[172:175]\n\t" \
  "s_waitcnt lgkmcnt(10)\n\t" \
  "ds_read_b64_tr_b16 v[220:221], v250 offset:18552\n\t" \
  "ds_read_u16_d16_hi v154, v154 offset:0\n\t" \
  "v_exp_f16_sdwa v224, v225 dst_sel:WORD_1 dst_unused:UNUSED_PRESERVE src0_sel:WORD_1\n\t" \
  "v_smfmac_f32_16x16x64_bf16 v[0:3], v[208:211], v[176:183], v248\n\t" \
  "ds_read_b64_tr_b16 v[222:223], v250 offset:19320\n\t" \
  "v_lshlrev_b32_sdwa v161, 1, v161 dst_sel:DWORD dst_unused:UNUSED_PAD src0_sel:DWORD src1_sel:WORD_1\n\t" \
  "v_smfmac_f32_16x16x64_bf16 v[24:27], v[208:211], v[184:191], v248\n\t" \
  "ds_read_u16_d16_hi v155, v155 offset:0\n\t" \
  "v_pk_fma_f16 v224, v224, %[hc], %[hc]\n\t" \
  "v_smfmac_f32_16x16x64_bf16 v[48:51], v[208:211], v[192:199], v248\n\t" \
  "s_waitcnt lgkmcnt(11)\n\t" \
  "ds_read_u16_d16_hi v160, v160 offset:0\n\t" \
  "v_lshlrev_b32_sdwa v162, 1, v162 dst_sel:DWORD dst_unused:UNUSED_PAD src0_sel:DWORD src1_sel:WORD_1\n\t" \
  "v_smfmac_f32_16x16x64_bf16 v[72:75], v[208:211], v[200:207], v248\n\t" \
  "s_waitcnt lgkmcnt(11)\n\t" \
  "ds_read_b64_tr_b16 v[208:209], v250 offset:18608\n\t" \
  "v_rcp_f16 v225, v224\n\t" \
  "v_smfmac_f32_16x16x64_bf16 v[4:7], v[212:215], v[176:183], v248\n\t" \
  "ds_read_u16_d16_hi v161, v161 offset:0\n\t" \
  "v_lshlrev_b32_sdwa v163, 1, v163 dst_sel:DWORD dst_unused:UNUSED_PAD src0_sel:DWORD src1_sel:WORD_1\n\t" \
  "v_smfmac_f32_16x16x64_bf16 v[28:31], v[212:215], v[184:191], v248\n\t" \
  "ds_read_b64_tr_b16 v[210:211], v250 offset:19376\n\t" \
  "v_lshlrev_b32_sdwa v168, 1, v168 dst_sel:DWORD dst_unused:UNUSED_PAD src0_sel:DWORD src1_sel:WORD_1\n\t" \
  "v_smfmac_f32_16x16x64_bf16 v[52:55], v[212:215], v[192:199], v248\n\t" \
  "ds_read_u16_d16_hi v162, v162 offset:0\n\t" \
  "v_rcp_f16_sdwa v224, v224 dst_sel:DWORD dst_unused:UNUSED_PAD src0_sel:WORD_1\n\t" \
  "v_smfmac_f32_16x16x64_bf16 v[76:79], v[212:215], v[200:207], v248\n\t" \
  "s_waitcnt lgkmcnt(10)\n\t" \
  "ds_read_b64_tr_b16 v[212:213], v250 offset:18616\n\t" \
  "v_lshlrev_b32_sdwa v169, 1, v169 dst_sel:DWORD dst_unused:UNUSED_PAD src0_sel:DWORD src1_sel:WORD_1\n\t" \
  "v_smfmac_f32_16x16x64_bf16 v[8:11], v[216:219], v[176:183], v248\n\t" \
  "ds_read_u16_d16_hi v163, v163 offset:0\n\t" \
  "v_fma_mix_f32 v152, v152, v225, 0 op_sel_hi:[0,1,0]\n\t" \
  "v_smfmac_f32_16x16x64_bf16 v[32:35], v[216:219], v[184:191], v248\n\t" \
  "ds_read_b64_tr_b16 v[214:215], v250 offset:19384\n\t" \
  "v_lshlrev_b32_sdwa v170, 1, v170 dst_sel:DWORD dst_unused:UNUSED_PAD src0_sel:DWORD src1_sel:WORD_1\n\t" \
  "v_smfmac_f32_16x16x64_bf16 v[56:59], v[216:219], v[192:199], v248\n\t" \
  "ds_read_u16_d16_hi v168, v168 offset:0\n\t" \
  "v_fma_mix_f32 v153, v153, v224, 0 op_sel_hi:[0,1,0]\n\t" \
  "v_smfmac_f32_16x16x64_bf16 v[80:83], v[216:219], v[200:207], v248\n\t" \
  "s_waitcnt lgkmcnt(10)\n\t" \
  "s_barrier\n\t" \
  "ds_read_b64_tr_b16 v[216:217], v249 offset:0\n\t" \
  "ds_read_b128 v[132:135], v251 offset:0\n\t" \
  "v_lshlrev_b32_sdwa v171, 1, v171 dst_sel:DWORD dst_unused:UNUSED_PAD src0_sel:DWORD src1_sel:WORD_1\n\t" \
  "v_smfmac_f32_16x16x64_bf16 v[12:15], v[220:223], v[176:183], v248\n\t" \
  "ds_read_b64_tr_b16 v[218:219], v249 offset:256\n\t" \
  "ds_read_b128 v[136:139], v251 offset:4096\n\t" \
  "v_mul_f32 v156, %[kk], v156\n\t" \
  "v_smfmac_f32_16x16x64_bf16 v[36:39], v[220:223], v[184:191], v248\n\t" \
  "ds_read_b128 v[140:143], v251 offset:8192\n\t" \
  "v_lshlrev_b32_sdwa v148, 1, v148 dst_sel:DWORD dst_unused:UNUSED_PAD src0_sel:DWORD src1_sel:WORD_1\n\t" \
  "v_mul_f32 v157, %[kk], v157\n\t" \
  "v_smfmac_f32_16x16x64_bf16 v[60:63], v[220:223], v[192:199], v248\n\t" \
  "ds_read_u16_d16_hi v169, v169 offset:0\n\t" \
  "v_cvt_pk_f16_f32 v224, v156, v157\n\t" \
  "v_smfmac_f32_16x16x64_bf16 v[84:87], v[220:223], v[200:207], v248\n\t" \
  "s_waitcnt lgkmcnt(11)\n\t" \
  "ds_read_b64_tr_b16 v[220:221], v249 offset:2048\n\t" \
  "ds_read_u16_d16_hi v170, v170 offset:0\n\t" \
  "v_lshlrev_b32_sdwa v149, 1, v149 dst_sel:DWORD dst_unused:UNUSED_PAD src0_sel:DWORD src1_sel:WORD_1\n\t" \
  "v_smfmac_f32_16x16x64_bf16 v[16:19], v[208:211], v[176:183], v248\n\t" \
  "ds_read_b64_tr_b16 v[222:223], v249 offset:2304\n\t" \
  "v_pk_fma_f16 v225, v224, v224, %[h1] neg_lo:[1,0,0] neg_hi:[1,0,0]\n\t" \
  "v_smfmac_f32_16x16x64_bf16 v[40:43], v[208:211], v[184:191], v248\n\t" \
  "ds_read_u16_d16_hi v171, v171 offset:0\n\t" \
  "v_lshlrev_b32_sdwa v150, 1, v150 dst_sel:DWORD dst_unused:UNUSED_PAD src0_sel:DWORD src1_sel:WORD_1\n\t" \
  "v_pk_mul_f16 v225, v224, v225\n\t" \
  "v_smfmac_f32_16x16x64_bf16 v[64:67], v[208:211], v[192:199], v248\n\t" \
  "ds_read_u16_d16_hi v148, v148 offset:0\n\t" \
  "v_lshlrev_b32_sdwa v151, 1, v151 dst_sel:DWORD dst_unused:UNUSED_PAD src0_sel:DWORD src1_sel:WORD_1\n\t" \
  "v_smfmac_f32_16x16x64_bf16 v[88:91], v[208:211], v[200:207], v248\n\t" \
  "s_waitcnt lgkmcnt(12)\n\t" \
  "ds_read_b64_tr_b16 v[208:209], v249 offset:4096\n\t" \
  "ds_read_u16_d16_hi v149, v149 offset:0\n\t" \
  "v_exp_f16 v224, v225\n\t" \
  "v_smfmac_f32_16x16x64_bf16 v[20:23], v[212:215], v[176:183], v248\n\t" \
  "ds_read_b64_tr_b16 v[210:211], v249 offset:4352\n\t" \
  "v_lshlrev_b32_sdwa v158, 1, v158 dst_sel:DWORD dst_unused:UNUSED_PAD src0_sel:DWORD src1_sel:WORD_1\n\t" \
  "v_smfmac_f32_16x16x64_bf16 v[44:47], v[212:215], v[184:191], v248\n\t" \
  "ds_read_u16_d16_hi v150, v150 offset:0\n\t" \
  "v_lshlrev_b32_sdwa v159, 1, v159 dst_sel:DWORD dst_unused:UNUSED_PAD src0_sel:DWORD src1_sel:WORD_1\n\t" \
  "v_smfmac_f32_16x16x64_bf16 v[68:71], v[212:215], v[192:199], v248\n\t" \
  "ds_read_u16_d16_hi v151, v151 offset:0\n\t" \
  "v_exp_f16_sdwa v224, v225 dst_sel:WORD_1 dst_unused:UNUSED_PRESERVE src0_sel:WORD_1\n\t" \
  "v_smfmac_f32_16x16x64_bf16 v[92:95], v[212:215], v[200:207], v248\n\t" \
  "s_waitcnt lgkmcnt(13)\n\t" \
  "ds_read_b64_tr_b16 v[212:213], v249 offset:512\n\t" \
  "v_lshlrev_b32_sdwa v164, 1, v164 dst_sel:DWORD dst_unused:UNUSED_PAD src0_sel:DWORD src1_sel:WORD_1\n\t" \
  "v_mfma_f32_16x16x32_bf16 v[176:179], v[216:219], v[96:99], 0\n\t" \
  "ds_read_u16_d16_hi v158, v158 offset:0\n\t" \
  "v_pk_fma_f16 v224, v224, %[hc], %[hc]\n\t" \
  "v_mfma_f32_16x16x32_bf16 v[184:187], v[216:219], v[108:111], 0\n\t" \
  "ds_read_b64_tr_b16 v[214:215], v249 offset:768\n\t" \
  "v_lshlrev_b32_sdwa v165, 1, v165 dst_sel:DWORD dst_unused:UNUSED_PAD src0_sel:DWORD src1_sel:WORD_1\n\t" \
  "v_mfma_f32_16x16x32_bf16 v[192:195], v[216:219], v[120:123], 0\n\t" \
  "ds_read_u16_d16_hi v159, v159 offset:0\n\t" \
  "v_rcp_f16 v225, v224\n\t" \
  "v_mfma_f32_16x16x32_bf16 v[200:203], v[216:219], v[132:135], 0\n\t" \
  "s_waitcnt lgkmcnt(11)\n\t" \
  "ds_read_b64_tr_b16 v[216:217], v249 offset:2560\n\t" \
  "v_lshlrev_b32_sdwa v166, 1, v166 dst_sel:DWORD dst_unused:UNUSED_PAD src0_sel:DWORD src1_sel:WORD_1\n\t" \
  "v_mfma_f32_16x16x32_bf16 v[176:179], v[220:223], v[100:103], v[176:179]\n\t" \
  "ds_read_u16_d16_hi v164, v164 offset:0\n\t" \
  "v_lshlrev_b32_sdwa v167, 1, v167 dst_sel:DWORD dst_unused:UNUSED_PAD src0_sel:DWORD src1_sel:WORD_1\n\t" \
  "v_mfma_f32_16x16x32_bf16 v[184:187], v[220:223], v[112:115], v[184:187]\n\t" \
  "ds_read_b64_tr_b16 v[218:219], v249 offset:2816\n\t" \
  "v_rcp_f16_sdwa v224, v224 dst_sel:DWORD dst_unused:UNUSED_PAD src0_sel:WORD_1\n\t" \
  "v_mfma_f32_16x16x32_bf16 v[192:195], v[220:223], v[124:127], v[192:195]\n\t" \
  "ds_read_u16_d16_hi v165, v165 offset:0\n\t" \
  "v_lshlrev_b32_sdwa v172, 1, v172 dst_sel:DWORD dst_unused:UNUSED_PAD src0_sel:DWORD src1_sel:WORD_1\n\t" \
  "v_mfma_f32_16x16x32_bf16 v[200:203], v[220:223], v[136:139], v[200:203]\n\t" \
  "s_waitcnt lgkmcnt(10)\n\t" \
  "ds_read_b64_tr_b16 v[220:221], v249 offset:4608\n\t" \
  "ds_read_u16_d16_hi v166, v166 offset:0\n\t" \
  "v_mfma_f32_16x16x32_bf16 v[176:179], v[208:211], v[104:107], v[176:179]\n\t" \
  "ds_read_b64_tr_b16 v[222:223], v249 offset:4864\n\t" \
  "v_lshlrev_b32_sdwa v173, 1, v173 dst_sel:DWORD dst_unused:UNUSED_PAD src0_sel:DWORD src1_sel:WORD_1\n\t" \
  "v_fma_mix_f32 v156, v156, v225, 0 op_sel_hi:[0,1,0]\n\t" \
  "v_mfma_f32_16x16x32_bf16 v[184:187], v[208:211], v[116:119], v[184:187]\n\t" \
  "ds_read_u16_d16_hi v167, v167 offset:0\n\t" \
  "v_fma_mix_f32 v157, v157, v224, 0 op_sel_hi:[0,1,0]\n\t" \
  "v_mfma_f32_16x16x32_bf16 v[192:195], v[208:211], v[128:131], v[192:195]\n\t" \
  "ds_read_u16_d16_hi v172, v172 offset:0\n\t" \
  "v_lshlrev_b32_sdwa v174, 1, v174 dst_sel:DWORD dst_unused:UNUSED_PAD src0_sel:DWORD src1_sel:WORD_1\n\t" \
  "v_mfma_f32_16x16x32_bf16 v[200:203], v[208:211], v[140:143], v[200:203]\n\t" \
  "s_waitcnt lgkmcnt(10)\n\t" \
  "ds_read_b64_tr_b16 v[208:209], v250 offset:0\n\t" \
  "v_lshlrev_b32_sdwa v175, 1, v175 dst_sel:DWORD dst_unused:UNUSED_PAD src0_sel:DWORD src1_sel:WORD_1\n\t" \
  "v_mfma_f32_16x16x32_bf16 v[180:183], v[212:215], v[96:99], 0\n\t" \
  "ds_read_u16_d16_hi v173, v173 offset:0\n\t" \
  "v_lshlrev_b32_sdwa v176, 1, v176 dst_sel:DWORD dst_unused:UNUSED_PAD src0_sel:DWORD src1_sel:WORD_1\n\t" \
  "v_mfma_f32_16x16x32_bf16 v[188:191], v[212:215], v[108:111], 0\n\t" \
  "ds_read_b64_tr_b16 v[210:211], v250 offset:768\n\t" \
  "v_mul_f32 v184, %[kk], v184\n\t" \
  "v_mfma_f32_16x16x32_bf16 v[196:199], v[212:215], v[120:123], 0\n\t" \
  "ds_read_u16_d16_hi v174, v174 offset:0\n\t" \
  "v_lshlrev_b32_sdwa v177, 1, v177 dst_sel:DWORD dst_unused:UNUSED_PAD src0_sel:DWORD src1_sel:WORD_1\n\t" \
  "v_mul_f32 v185, %[kk], v185\n\t" \
  "v_mfma_f32_16x16x32_bf16 v[204:207], v[212:215], v[132:135], 0\n\t" \
  "s_waitcnt lgkmcnt(10)\n\t" \
  "ds_read_b64_tr_b16 v[212:213], v250 offset:8\n\t" \
  "ds_read_u16_d16_hi v175, v175 offset:0\n\t" \
  "v_cvt_pk_f16_f32 v224, v184, v185\n\t" \
  "v_mfma_f32_16x16x32_bf16 v[180:183], v[216:219], v[100:103], v[180:183]\n\t" \
  "ds_read_b64_tr_b16 v[214:215], v250 offset:776\n\t" \
  "v_lshlrev_b32_sdwa v178, 1, v178 dst_sel:DWORD dst_unused:UNUSED_PAD src0_sel:DWORD src1_sel:WORD_1\n\t" \
  "v_mfma_f32_16x16x32_bf16 v[188:191], v[216:219], v[112:115], v[188:191]\n\t" \
  "ds_read_u16_d16_hi v176, v176 offset:0\n\t" \
  "v_pk_fma_f16 v225, v224, v224, %[h1] neg_lo:[1,0,0] neg_hi:[1,0,0]\n\t" \
  "v_mfma_f32_16x16x32_bf16 v[196:199], v[216:219], v[124:127], v[196:199]\n\t" \
  "ds_read_u16_d16_hi v177, v177 offset:0\n\t" \
  "v_lshlrev_b32_sdwa v179, 1, v179 dst_sel:DWORD dst_unused:UNUSED_PAD src0_sel:DWORD src1_sel:WORD_1\n\t" \
  "v_pk_mul_f16 v225, v224, v225\n\t" \
  "v_mfma_f32_16x16x32_bf16 v[204:207], v[216:219], v[136:139], v[204:207]\n\t" \
  "s_waitcnt lgkmcnt(11)\n\t" \
  "ds_read_b64_tr_b16 v[216:217], v250 offset:64\n\t" \
  "v_lshlrev_b32_sdwa v186, 1, v186 dst_sel:DWORD dst_unused:UNUSED_PAD src0_sel:DWORD src1_sel:WORD_1\n\t" \
  "v_mfma_f32_16x16x32_bf16 v[180:183], v[220:223], v[104:107], v[180:183]\n\t" \
  "ds_read_u16_d16_hi v178, v178 offset:0\n\t" \
  "v_exp_f16 v224, v225\n\t" \
  "v_mfma_f32_16x16x32_bf16 v[188:191], v[220:223], v[116:119], v[188:191]\n\t" \
  "ds_read_b64_tr_b16 v[218:219], v250 offset:832\n\t" \
  "v_lshlrev_b32_sdwa v187, 1, v187 dst_sel:DWORD dst_unused:UNUSED_PAD src0_sel:DWORD src1_sel:WORD_1\n\t" \
  "v_mfma_f32_16x16x32_bf16 v[196:199], v[220:223], v[128:131], v[196:199]\n\t" \
  "ds_read_u16_d16_hi v179, v179 offset:0\n\t" \
  "v_lshlrev_b32_sdwa v192, 1, v192 dst_sel:DWORD dst_unused:UNUSED_PAD src0_sel:DWORD src1_sel:WORD_1\n\t" \
  "v_mfma_f32_16x16x32_bf16 v[204:207], v[220:223], v[140:143], v[204:207]\n\t" \
  "s_waitcnt lgkmcnt(10)\n\t" \
  "ds_read_b64_tr_b16 v[220:221], v250 offset:72\n\t" \
  "ds_read_u16_d16_hi v186, v186 offset:0\n\t" \
  "v_exp_f16_sdwa v224, v225 dst_sel:WORD_1 dst_unused:UNUSED_PRESERVE src0_sel:WORD_1\n\t" \
  "v_smfmac_f32_16x16x64_bf16 v[0:3], v[208:211], v[144:151], v248\n\t" \
  "ds_read_b64_tr_b16 v[222:223], v250 offset:840\n\t" \
  "v_lshlrev_b32_sdwa v193, 1, v193 dst_sel:DWORD dst_unused:UNUSED_PAD src0_sel:DWORD src1_sel:WORD_1\n\t" \
  "v_smfmac_f32_16x16x64_bf16 v[24:27], v[208:211], v[152:159], v248\n\t" \
  "ds_read_u16_d16_hi v187, v187 offset:0\n\t" \
  "v_pk_fma_f16 v224, v224, %[hc], %[hc]\n\t" \
  "v_smfmac_f32_16x16x64_bf16 v[48:51], v[208:211], v[160:167], v248\n\t" \
  "s_waitcnt lgkmcnt(11)\n\t" \
  "ds_read_u16_d16_hi v192, v192 offset:0\n\t" \
  "v_lshlrev_b32_sdwa v194, 1, v194 dst_sel:DWORD dst_unused:UNUSED_PAD src0_sel:DWORD src1_sel:WORD_1\n\t" \
  "v_smfmac_f32_16x16x64_bf16 v[72:75], v[208:211], v[168:175], v248\n\t" \
  "s_waitcnt lgkmcnt(11)\n\t" \
  "ds_read_b64_tr_b16 v[208:209], v250 offset:128\n\t" \
  "v_rcp_f16 v225, v224\n\t" \
  "v_smfmac_f32_16x16x64_bf16 v[4:7], v[212:215], v[144:151], v248\n\t" \
  "ds_read_b64 v[242:243], v252 offset:128\n\t" \
  "v_lshlrev_b32_sdwa v195, 1, v195 dst_sel:DWORD dst_unused:UNUSED_PAD src0_sel:DWORD src1_sel:WORD_1\n\t" \
  "v_smfmac_f32_16x16x64_bf16 v[28:31], v[212:215], v[152:159], v248\n\t" \
  "ds_read_b64_tr_b16 v[210:211], v250 offset:896\n\t" \
  "ds_read_b64 v[244:245], v252 offset:136\n\t" \
  "v_smfmac_f32_16x16x64_bf16 v[52:55], v[212:215], v[160:167], v248\n\t" \
  "ds_read_u16_d16_hi v193, v193 offset:0\n\t" \
  "v_lshlrev_b32_sdwa v200, 1, v200 dst_sel:DWORD dst_unused:UNUSED_PAD src0_sel:DWORD src1_sel:WORD_1\n\t" \
  "v_rcp_f16_sdwa v224, v224 dst_sel:DWORD dst_unused:UNUSED_PAD src0_sel:WORD_1\n\t" \
  "v_smfmac_f32_16x16x64_bf16 v[76:79], v[212:215], v[168:175], v248\n\t" \
  "s_waitcnt lgkmcnt(11)\n\t" \
  "ds_read_b64_tr_b16 v[212:213], v250 offset:136\n\t" \
  "ds_read_u16_d16_hi v194, v194 offset:0\n\t" \
  "v_smfmac_f32_16x16x64_bf16 v[8:11], v[216:219], v[144:151], v248\n\t" \
  "ds_read_b64_tr_b16 v[214:215], v250 offset:904\n\t" \
  "v_lshlrev_b32_sdwa v201, 1, v201 dst_sel:DWORD dst_unused:UNUSED_PAD src0_sel:DWORD src1_sel:WORD_1\n\t" \
  "v_smfmac_f32_16x16x64_bf16 v[32:35], v[216:219], v[152:159], v248\n\t" \
  "ds_read_u16_d16_hi v195, v195 offset:0\n\t" \
  "v_fma_mix_f32 v184, v184, v225, 0 op_sel_hi:[0,1,0]\n\t" \
  "v_cvt_pk_bf16_f32 v230, v72, v73\n\t" \
  "v_smfmac_f32_16x16x64_bf16 v[56:59], v[216:219], v[160:167], v248\n\t" \
  "ds_read_u16_d16_hi v200, v200 offset:0\n\t" \
  "v_lshlrev_b32_sdwa v202, 1, v202 dst_sel:DWORD dst_unused:UNUSED_PAD src0_sel:DWORD src1_sel:WORD_1\n\t" \
  "v_fma_mix_f32 v185, v185, v224, 0 op_sel_hi:[0,1,0]\n\t" \
  "v_cvt_pk_bf16_f32 v231, v74, v75\n\t" \
  "v_smfmac_f32_16x16x64_bf16 v[80:83], v[216:219], v[168:175], v248\n\t" \
  "s_waitcnt lgkmcnt(12)\n\t" \
  "s_barrier\n\t" \
  "ds_read_b64_tr_b16 v[216:217], v249 offset:6144\n\t" \
  "v_lshlrev_b32_sdwa v203, 1, v203 dst_sel:DWORD dst_unused:UNUSED_PAD src0_sel:DWORD src1_sel:WORD_1\n\t" \
  "v_cvt_pk_bf16_f32 v232, v76, v77\n\t" \
  "v_smfmac_f32_16x16x64_bf16 v[12:15], v[220:223], v[144:151], v248\n\t" \
  "ds_read_u16_d16_hi v201, v201 offset:0\n\t" \
  "v_mul_f32 v188, %[kk], v188\n\t" \
  "v_cvt_pk_bf16_f32 v233, v78, v79\n\t" \
  "v_smfmac_f32_16x16x64_bf16 v[36:39], v[220:223], v[152:159], v248\n\t" \
  "ds_read_b64_tr_b16 v[218:219], v249 offset:6400\n\t" \
  "buffer_store_dwordx4 v[230:233], v253, %[osrd], %[ob] offen offset:0 sc1\n\t" \
  "v_lshlrev_b32_sdwa v180, 1, v180 dst_sel:DWORD dst_unused:UNUSED_PAD src0_sel:DWORD src1_sel:WORD_1\n\t" \
  "v_mul_f32 v189, %[kk], v189\n\t" \
  "v_smfmac_f32_16x16x64_bf16 v[60:63], v[220:223], v[160:167], v248\n\t" \
  "ds_read_b64 v[230:231], v252 offset:0\n\t" \
  "ds_read_b64 v[232:233], v252 offset:8\n\t" \
  "v_cvt_pk_f16_f32 v224, v188, v189\n\t" \
  "v_smfmac_f32_16x16x64_bf16 v[84:87], v[220:223], v[168:175], v248\n\t" \
  "s_waitcnt lgkmcnt(12)\n\t" \
  "ds_read_b64_tr_b16 v[220:221], v249 offset:8192\n\t" \
  "ds_read_u16_d16_hi v202, v202 offset:0\n\t" \
  "v_lshlrev_b32_sdwa v181, 1, v181 dst_sel:DWORD dst_unused:UNUSED_PAD src0_sel:DWORD src1_sel:WORD_1\n\t" \
  "v_smfmac_f32_16x16x64_bf16 v[16:19], v[208:211], v[144:151], v248\n\t" \
  "ds_read_b64_tr_b16 v[222:223], v249 offset:8448\n\t" \
  "v_pk_fma_f16 v225, v224, v224, %[h1] neg_lo:[1,0,0] neg_hi:[1,0,0]\n\t" \
  "v_smfmac_f32_16x16x64_bf16 v[40:43], v[208:211], v[152:159], v248\n\t" \
  "ds_read_u16_d16_hi v203, v203 offset:0\n\t" \
  "v_lshlrev_b32_sdwa v182, 1, v182 dst_sel:DWORD dst_unused:UNUSED_PAD src0_sel:DWORD src1_sel:WORD_1\n\t" \
  "v_pk_mul_f16 v225, v224, v225\n\t" \
  "v_cvt_pk_bf16_f32 v234, v80, v81\n\t" \
  "v_smfmac_f32_16x16x64_bf16 v[64:67], v[208:211], v[160:167], v248\n\t" \
  "ds_read_u16_d16_hi v180, v180 offset:0\n\t" \
  "v_lshlrev_b32_sdwa v183, 1, v183 dst_sel:DWORD dst_unused:UNUSED_PAD src0_sel:DWORD src1_sel:WORD_1\n\t" \
  "v_cvt_pk_bf16_f32 v235, v82, v83\n\t" \
  "v_smfmac_f32_16x16x64_bf16 v[88:91], v[208:211], v[168:175], v248\n\t" \
  "s_waitcnt lgkmcnt(12)\n\t" \
  "ds_read_b64_tr_b16 v[208:209], v249 offset:10240\n\t" \
  "ds_read_u16_d16_hi v181, v181 offset:0\n\t" \
  "v_exp_f16 v224, v225\n\t" \
  "v_cvt_pk_bf16_f32 v236, v84, v85\n\t" \
  "v_smfmac_f32_16x16x64_bf16 v[20:23], v[212:215], v[144:151], v248\n\t" \
  "ds_read_b64_tr_b16 v[210:211], v249 offset:10496\n\t" \
  "v_lshlrev_b32_sdwa v190, 1, v190 dst_sel:DWORD dst_unused:UNUSED_PAD src0_sel:DWORD src1_sel:WORD_1\n\t" \
  "v_cvt_pk_bf16_f32 v237, v86, v87\n\t" \
  "v_smfmac_f32_16x16x64_bf16 v[44:47], v[212:215], v[152:159], v248\n\t" \
  "buffer_store_dwordx4 v[234:237], v253, %[osrd], %[ob] offen offset:64 sc1\n\t" \
  "ds_read_u16_d16_hi v182, v182 offset:0\n\t" \
  "v_smfmac_f32_16x16x64_bf16 v[68:71], v[212:215], v[160:167], v248\n\t" \
  "ds_read_b64 v[234:235], v252 offset:64\n\t" \
  "v_lshlrev_b32_sdwa v191, 1, v191 dst_sel:DWORD dst_unused:UNUSED_PAD src0_sel:DWORD src1_sel:WORD_1\n\t" \
  "v_exp_f16_sdwa v224, v225 dst_sel:WORD_1 dst_unused:UNUSED_PRESERVE src0_sel:WORD_1\n\t" \
  "v_smfmac_f32_16x16x64_bf16 v[92:95], v[212:215], v[168:175], v248\n\t" \
  "s_waitcnt lgkmcnt(11)\n\t" \
  "ds_read_b64_tr_b16 v[212:213], v249 offset:6656\n\t" \
  "ds_read_b64 v[236:237], v252 offset:72\n\t" \
  "v_mfma_f32_16x16x16_bf16 v[72:75], v[246:247], v[230:231], 0\n\t" \
  "ds_read_b64_tr_b16 v[214:215], v249 offset:6912\n\t" \
  "v_lshlrev_b32_sdwa v196, 1, v196 dst_sel:DWORD dst_unused:UNUSED_PAD src0_sel:DWORD src1_sel:WORD_1\n\t" \
  "v_mfma_f32_16x16x32_bf16 v[144:147], v[216:219], v[96:99], 0\n\t" \
  "ds_read_u16_d16_hi v183, v183 offset:0\n\t" \
  "v_pk_fma_f16 v224, v224, %[hc], %[hc]\n\t" \
  "v_mfma_f32_16x16x32_bf16 v[152:155], v[216:219], v[108:111], 0\n\t" \
  "ds_read_u16_d16_hi v190, v190 offset:0\n\t" \
  "v_lshlrev_b32_sdwa v197, 1, v197 dst_sel:DWORD dst_unused:UNUSED_PAD src0_sel:DWORD src1_sel:WORD_1\n\t" \
  "v_cvt_pk_bf16_f32 v238, v88, v89\n\t" \
  "v_mfma_f32_16x16x32_bf16 v[160:163], v[216:219], v[120:123], 0\n\t" \
  "s_waitcnt lgkmcnt(14)\n\t" \
  "ds_read_u16_d16_hi v191, v191 offset:0\n\t" \
  "v_rcp_f16 v225, v224\n\t" \
  "v_mfma_f32_16x16x16_bf16 v[76:79], v[246:247], v[232:233], 0\n\t" \
  "ds_read_u16_d16_hi v196, v196 offset:0\n\t" \
  "v_cvt_pk_bf16_f32 v239, v90, v91\n\t" \
  "v_mfma_f32_16x16x32_bf16 v[168:171], v[216:219], v[132:135], 0\n\t" \
  "s_waitcnt lgkmcnt(14)\n\t" \
  "ds_read_b64_tr_b16 v[216:217], v249 offset:8704\n\t" \
  "v_lshlrev_b32_sdwa v198, 1, v198 dst_sel:DWORD dst_unused:UNUSED_PAD src0_sel:DWORD src1_sel:WORD_1\n\t" \
  "v_cvt_pk_bf16_f32 v240, v92, v93\n\t" \
  "v_mfma_f32_16x16x32_bf16 v[144:147], v[220:223], v[100:103], v[144:147]\n\t" \
  "ds_read_u16_d16_hi v197, v197 offset:0\n\t" \
  "v_cvt_pk_bf16_f32 v241, v94, v95\n\t" \
  "v_mfma_f32_16x16x32_bf16 v[152:155], v[220:223], v[112:115], v[152:155]\n\t" \
  "ds_read_b64_tr_b16 v[218:219], v249 offset:8960\n\t" \
  "v_lshlrev_b32_sdwa v199, 1, v199 dst_sel:DWORD dst_unused:UNUSED_PAD src0_sel:DWORD src1_sel:WORD_1\n\t" \
  "v_rcp_f16_sdwa v224, v224 dst_sel:DWORD dst_unused:UNUSED_PAD src0_sel:WORD_1\n\t" \
  "v_mfma_f32_16x16x32_bf16 v[160:163], v[220:223], v[124:127], v[160:163]\n\t" \
  "buffer_store_dwordx4 v[238:241], v253, %[osrd], %[ob] offen offset:128 sc1\n\t" \
  "v_lshlrev_b32_sdwa v204, 1, v204 dst_sel:DWORD dst_unused:UNUSED_PAD src0_sel:DWORD src1_sel:WORD_1\n\t" \
  "v_mfma_f32_16x16x32_bf16 v[168:171], v[220:223], v[136:139], v[168:171]\n\t" \
  "s_waitcnt lgkmcnt(10)\n\t" \
  "ds_read_b64_tr_b16 v[220:221], v249 offset:10752\n\t" \
  "ds_read_u16_d16_hi v198, v198 offset:0\n\t" \
  "v_mfma_f32_16x16x16_bf16 v[80:83], v[246:247], v[234:235], 0\n\t" \
  "ds_read_b64_tr_b16 v[222:223], v249 offset:11008\n\t" \
  "v_lshlrev_b32_sdwa v205, 1, v205 dst_sel:DWORD dst_unused:UNUSED_PAD src0_sel:DWORD src1_sel:WORD_1\n\t" \
  "v_mfma_f32_16x16x32_bf16 v[144:147], v[208:211], v[104:107], v[144:147]\n\t" \
  "ds_read_u16_d16_hi v199, v199 offset:0\n\t" \
  "v_fma_mix_f32 v188, v188, v225, 0 op_sel_hi:[0,1,0]\n\t" \
  "v_mfma_f32_16x16x32_bf16 v[152:155], v[208:211], v[116:119], v[152:155]\n\t" \
  "ds_read_u16_d16_hi v204, v204 offset:0\n\t" \
  "v_fma_mix_f32 v189, v189, v224, 0 op_sel_hi:[0,1,0]\n\t" \
  "v_mfma_f32_16x16x32_bf16 v[160:163], v[208:211], v[128:131], v[160:163]\n\t" \
  "s_waitcnt lgkmcnt(13)\n\t" \
  "ds_read_u16_d16_hi v205, v205 offset:0\n\t" \
  "v_lshlrev_b32_sdwa v206, 1, v206 dst_sel:DWORD dst_unused:UNUSED_PAD src0_sel:DWORD src1_sel:WORD_1\n\t" \
  "v_mfma_f32_16x16x16_bf16 v[84:87], v[246:247], v[236:237], 0\n\t" \
  "ds_read_u16_d16_hi v206, v206 offset:0\n\t" \
  "v_lshlrev_b32_sdwa v207, 1, v207 dst_sel:DWORD dst_unused:UNUSED_PAD src0_sel:DWORD src1_sel:WORD_1\n\t" \
  "v_mfma_f32_16x16x32_bf16 v[168:171], v[208:211], v[140:143], v[168:171]\n\t" \
  "s_waitcnt lgkmcnt(14)\n\t" \
  "ds_read_b64_tr_b16 v[208:209], v250 offset:6160\n\t" \
  "v_lshlrev_b32_sdwa v144, 1, v144 dst_sel:DWORD dst_unused:UNUSED_PAD src0_sel:DWORD src1_sel:WORD_1\n\t" \
  "v_mfma_f32_16x16x32_bf16 v[148:151], v[212:215], v[96:99], 0\n\t" \
  "ds_read_u16_d16_hi v207, v207 offset:0\n\t" \
  "v_mul_f32 v152, %[kk], v152\n\t" \
  "v_mfma_f32_16x16x32_bf16 v[156:159], v[212:215], v[108:111], 0\n\t" \
  "ds_read_b64_tr_b16 v[210:211], v250 offset:6928\n\t" \
  "v_lshlrev_b32_sdwa v145, 1, v145 dst_sel:DWORD dst_unused:UNUSED_PAD src0_sel:DWORD src1_sel:WORD_1\n\t" \
  "v_mfma_f32_16x16x32_bf16 v[164:167], v[212:215], v[120:123], 0\n\t" \
  "ds_read_u16_d16_hi v144, v144 offset:0\n\t" \
  "v_mul_f32 v153, %[kk], v153\n\t" \
  "v_mfma_f32_16x16x32_bf16 v[172:175], v[212:215], v[132:135], 0\n\t" \
  "ds_read_b64_tr_b16 v[212:213], v250 offset:6168\n\t" \
  "v_cvt_pk_f16_f32 v224, v152, v153\n\t" \
  "v_mfma_f32_16x16x16_bf16 v[88:91], v[246:247], v[242:243], 0\n\t" \
  "s_waitcnt lgkmcnt(12)\n\t" \
  "ds_read_u16_d16_hi v145, v145 offset:0\n\t" \
  "v_lshlrev_b32_sdwa v146, 1, v146 dst_sel:DWORD dst_unused:UNUSED_PAD src0_sel:DWORD src1_sel:WORD_1\n\t" \
  "v_mfma_f32_16x16x32_bf16 v[148:151], v[216:219], v[100:103], v[148:151]\n\t" \
  "ds_read_b64_tr_b16 v[214:215], v250 offset:6936\n\t" \
  "v_pk_fma_f16 v225, v224, v224, %[h1] neg_lo:[1,0,0] neg_hi:[1,0,0]\n\t" \
  "v_mfma_f32_16x16x32_bf16 v[156:159], v[216:219], v[112:115], v[156:159]\n\t" \
  "ds_read_u16_d16_hi v146, v146 offset:0\n\t" \
  "v_lshlrev_b32_sdwa v147, 1, v147 dst_sel:DWORD dst_unused:UNUSED_PAD src0_sel:DWORD src1_sel:WORD_1\n\t" \
  "v_mfma_f32_16x16x32_bf16 v[164:167], v[216:219], v[124:127], v[164:167]\n\t" \
  "ds_read_u16_d16_hi v147, v147 offset:0\n\t" \
  "v_pk_mul_f16 v225, v224, v225\n\t" \
  "v_mfma_f32_16x16x16_bf16 v[92:95], v[246:247], v[244:245], 0\n\t" \
  "v_lshlrev_b32_sdwa v154, 1, v154 dst_sel:DWORD dst_unused:UNUSED_PAD src0_sel:DWORD src1_sel:WORD_1\n\t" \
  "v_exp_f16 v224, v225\n\t" \
  "v_mfma_f32_16x16x32_bf16 v[172:175], v[216:219], v[136:139], v[172:175]\n\t" \
  "s_waitcnt lgkmcnt(13)\n\t" \
  "ds_read_b64_tr_b16 v[216:217], v250 offset:6224\n\t" \
  "v_lshlrev_b32_sdwa v155, 1, v155 dst_sel:DWORD dst_unused:UNUSED_PAD src0_sel:DWORD src1_sel:WORD_1\n\t" \
  "v_mfma_f32_16x16x32_bf16 v[148:151], v[220:223], v[104:107], v[148:151]\n\t" \
  "ds_read_u16_d16_hi v154, v154 offset:0\n\t" \
  "v_lshlrev_b32_sdwa v160, 1, v160 dst_sel:DWORD dst_unused:UNUSED_PAD src0_sel:DWORD src1_sel:WORD_1\n\t" \
  "v_mfma_f32_16x16x32_bf16 v[156:159], v[220:223], v[116:119], v[156:159]\n\t" \
  "ds_read_b64_tr_b16 v[218:219], v250 offset:6992\n\t" \
  "v_exp_f16_sdwa v224, v225 dst_sel:WORD_1 dst_unused:UNUSED_PRESERVE src0_sel:WORD_1\n\t" \
  "v_mfma_f32_16x16x32_bf16 v[164:167], v[220:223], v[128:131], v[164:167]\n\t" \
  "ds_read_u16_d16_hi v155, v155 offset:0\n\t" \
  "v_lshlrev_b32_sdwa v161, 1, v161 dst_sel:DWORD dst_unused:UNUSED_PAD src0_sel:DWORD src1_sel:WORD_1\n\t" \
  "v_mfma_f32_16x16x32_bf16 v[172:175], v[220:223], v[140:143], v[172:175]\n\t" \
  "s_waitcnt lgkmcnt(10)\n\t" \
  "ds_read_b64_tr_b16 v[220:221], v250 offset:6232\n\t" \
  "v_pk_fma_f16 v224, v224, %[hc], %[hc]\n\t" \
  "v_smfmac_f32_16x16x64_bf16 v[0:3], v[208:211], v[176:183], v248\n\t" \
  "ds_read_u16_d16_hi v160, v160 offset:0\n\t" \
  "v_lshlrev_b32_sdwa v162, 1, v162 dst_sel:DWORD dst_unused:UNUSED_PAD src0_sel:DWORD src1_sel:WORD_1\n\t" \
  "v_smfmac_f32_16x16x64_bf16 v[24:27], v[208:211], v[184:191], v248\n\t" \
  "ds_read_b64_tr_b16 v[222:223], v250 offset:7000\n\t" \
  "v_rcp_f16 v225, v224\n\t" \
  "v_smfmac_f32_16x16x64_bf16 v[48:51], v[208:211], v[192:199], v248\n\t" \
  "ds_read_u16_d16_hi v161, v161 offset:0\n\t" \
  "v_lshlrev_b32_sdwa v163, 1, v163 dst_sel:DWORD dst_unused:UNUSED_PAD src0_sel:DWORD src1_sel:WORD_1\n\t" \
  "v_smfmac_f32_16x16x64_bf16 v[72:75], v[208:211], v[200:207], v248\n\t" \
  "s_waitcnt lgkmcnt(10)\n\t" \
  "ds_read_b64_tr_b16 v[208:209], v250 offset:6288\n\t" \
  "v_lshlrev_b32_sdwa v168, 1, v168 dst_sel:DWORD dst_unused:UNUSED_PAD src0_sel:DWORD src1_sel:WORD_1\n\t" \
  "v_smfmac_f32_16x16x64_bf16 v[4:7], v[212:215], v[176:183], v248\n\t" \
  "ds_read_u16_d16_hi v162, v162 offset:0\n\t" \
  "v_rcp_f16_sdwa v224, v224 dst_sel:DWORD dst_unused:UNUSED_PAD src0_sel:WORD_1\n\t" \
  "v_smfmac_f32_16x16x64_bf16 v[28:31], v[212:215], v[184:191], v248\n\t" \
  "s_add_u32 %[ob], %[ob], %[dob]\n\t" \
  "ds_read_b64_tr_b16 v[210:211], v250 offset:7056\n\t" \
  "v_lshlrev_b32_sdwa v169, 1, v169 dst_sel:DWORD dst_unused:UNUSED_PAD src0_sel:DWORD src1_sel:WORD_1\n\t" \
  "v_smfmac_f32_16x16x64_bf16 v[52:55], v[212:215], v[192:199], v248\n\t" \
  "ds_read_u16_d16_hi v163, v163 offset:0\n\t" \
  "v_fma_mix_f32 v152, v152, v225, 0 op_sel_hi:[0,1,0]\n\t" \
  "v_smfmac_f32_16x16x64_bf16 v[76:79], v[212:215], v[200:207], v248\n\t" \
  "s_waitcnt lgkmcnt(9)\n\t" \
  "ds_read_b64_tr_b16 v[212:213], v250 offset:6296\n\t" \
  "v_lshlrev_b32_sdwa v170, 1, v170 dst_sel:DWORD dst_unused:UNUSED_PAD src0_sel:DWORD src1_sel:WORD_1\n\t" \
  "v_smfmac_f32_16x16x64_bf16 v[8:11], v[216:219], v[176:183], v248\n\t" \
  "ds_read_u16_d16_hi v168, v168 offset:0\n\t" \
  "v_fma_mix_f32 v153, v153, v224, 0 op_sel_hi:[0,1,0]\n\t" \
  "v_smfmac_f32_16x16x64_bf16 v[32:35], v[216:219], v[184:191], v248\n\t" \
  "ds_read_b64_tr_b16 v[214:215], v250 offset:7064\n\t" \
  "v_lshlrev_b32_sdwa v171, 1, v171 dst_sel:DWORD dst_unused:UNUSED_PAD src0_sel:DWORD src1_sel:WORD_1\n\t" \
  "v_smfmac_f32_16x16x64_bf16 v[56:59], v[216:219], v[192:199], v248\n\t" \
  "ds_read_u16_d16_hi v169, v169 offset:0\n\t" \
  "v_mul_f32 v156, %[kk], v156\n\t" \
  "v_smfmac_f32_16x16x64_bf16 v[80:83], v[216:219], v[200:207], v248\n\t" \
  "s_waitcnt lgkmcnt(9)\n\t" \
  "s_barrier\n\t" \
  "s_sub_u32 %[lc], %[lc], 1\n\t" \
  "s_cmp_eq_u32 %[lc], 0\n\t" \
  "s_cbranch_scc1 .Lcnx_mx\n\t" \
  "ds_read_b64_tr_b16 v[216:217], v249 offset:12288\n\t" \
  "v_lshlrev_b32_sdwa v148, 1, v148 dst_sel:DWORD dst_unused:UNUSED_PAD src0_sel:DWORD src1_sel:WORD_1\n\t" \
  "v_smfmac_f32_16x16x64_bf16 v[12:15], v[220:223], v[176:183], v248\n\t" \
  "ds_read_u16_d16_hi v170, v170 offset:0\n\t" \
  "v_mul_f32 v157, %[kk], v157\n\t" \
  "v_smfmac_f32_16x16x64_bf16 v[36:39], v[220:223], v[184:191], v248\n\t" \
  "ds_read_b64_tr_b16 v[218:219], v249 offset:12544\n\t" \
  "v_cvt_pk_f16_f32 v224, v156, v157\n\t" \
  "v_smfmac_f32_16x16x64_bf16 v[60:63], v[220:223], v[192:199], v248\n\t" \
  "ds_read_u16_d16_hi v171, v171 offset:0\n\t" \
  "v_lshlrev_b32_sdwa v149, 1, v149 dst_sel:DWORD dst_unused:UNUSED_PAD src0_sel:DWORD src1_sel:WORD_1\n\t" \
  "v_smfmac_f32_16x16x64_bf16 v[84:87], v[220:223], v[200:207], v248\n\t" \
  "s_waitcnt lgkmcnt(9)\n\t" \
  "ds_read_b64_tr_b16 v[220:221], v249 offset:14336\n\t" \
  "v_pk_fma_f16 v225, v224, v224, %[h1] neg_lo:[1,0,0] neg_hi:[1,0,0]\n\t" \
  "v_smfmac_f32_16x16x64_bf16 v[16:19], v[208:211], v[176:183], v248\n\t" \
  "s_add_u32 %[rs], %[rs], 1\n\t" \
  "ds_read_u16_d16_hi v148, v148 offset:0\n\t" \
  "v_lshlrev_b32_sdwa v150, 1, v150 dst_sel:DWORD dst_unused:UNUSED_PAD src0_sel:DWORD src1_sel:WORD_1\n\t" \
  "v_smfmac_f32_16x16x64_bf16 v[40:43], v[208:211], v[184:191], v248\n\t" \
  "s_cmp_eq_u32 %[rs], 6\n\t" \
  "ds_read_b64_tr_b16 v[222:223], v249 offset:14592\n\t" \
  "v_pk_mul_f16 v225, v224, v225\n\t" \
  "v_smfmac_f32_16x16x64_bf16 v[64:67], v[208:211], v[192:199], v248\n\t" \
  "ds_read_u16_d16_hi v149, v149 offset:0\n\t" \
  "v_lshlrev_b32_sdwa v151, 1, v151 dst_sel:DWORD dst_unused:UNUSED_PAD src0_sel:DWORD src1_sel:WORD_1\n\t" \
  "v_smfmac_f32_16x16x64_bf16 v[88:91], v[208:211], v[200:207], v248\n\t" \
  "s_waitcnt lgkmcnt(9)\n\t" \
  "ds_read_b64_tr_b16 v[208:209], v249 offset:16384\n\t" \
  "v_exp_f16 v224, v225\n\t" \
  "v_smfmac_f32_16x16x64_bf16 v[20:23], v[212:215], v[176:183], v248\n\t" \
  "s_cselect_b32 %[rs], 0, %[rs]\n\t" \
  "ds_read_u16_d16_hi v150, v150 offset:0\n\t" \
  "v_lshlrev_b32_sdwa v158, 1, v158 dst_sel:DWORD dst_unused:UNUSED_PAD src0_sel:DWORD src1_sel:WORD_1\n\t" \
  "v_smfmac_f32_16x16x64_bf16 v[44:47], v[212:215], v[184:191], v248\n\t" \
  "s_cselect_b32 %[t0], -75920, %[ps]\n\t" \
  "ds_read_b64_tr_b16 v[210:211], v249 offset:16640\n\t" \
  "v_lshlrev_b32_sdwa v159, 1, v159 dst_sel:DWORD dst_unused:UNUSED_PAD src0_sel:DWORD src1_sel:WORD_1\n\t" \
  "v_smfmac_f32_16x16x64_bf16 v[68:71], v[212:215], v[192:199], v248\n\t" \
  "ds_read_u16_d16_hi v151, v151 offset:0\n\t" \
  "v_add_u32 v252, %[t0], v252\n\t" \
  "v_exp_f16_sdwa v224, v225 dst_sel:WORD_1 dst_unused:UNUSED_PRESERVE src0_sel:WORD_1\n\t" \
  "v_smfmac_f32_16x16x64_bf16 v[92:95], v[212:215], v[200:207], v248\n\t" \
  "s_waitcnt lgkmcnt(9)\n\t" \
  "ds_read_b64_tr_b16 v[212:213], v249 offset:12800\n\t" \
  "v_lshlrev_b32_sdwa v164, 1, v164 dst_sel:DWORD dst_unused:UNUSED_PAD src0_sel:DWORD src1_sel:WORD_1\n\t" \
  "v_mfma_f32_16x16x32_bf16 v[176:179], v[216:219], v[96:99], 0\n\t" \
  "ds_read_u16_d16_hi v158, v158 offset:0\n\t" \
  "v_pk_fma_f16 v224, v224, %[hc], %[hc]\n\t" \
  "v_mfma_f32_16x16x32_bf16 v[184:187], v[216:219], v[108:111], 0\n\t" \
  "ds_read_b64_tr_b16 v[214:215], v249 offset:13056\n\t" \
  "v_lshlrev_b32_sdwa v165, 1, v165 dst_sel:DWORD dst_unused:UNUSED_PAD src0_sel:DWORD src1_sel:WORD_1\n\t" \
  "v_mfma_f32_16x16x32_bf16 v[192:195], v[216:219], v[120:123], 0\n\t" \
  "ds_read_u16_d16_hi v159, v159 offset:0\n\t" \
  "v_rcp_f16 v225, v224\n\t" \
  "v_mfma_f32_16x16x32_bf16 v[200:203], v[216:219], v[132:135], 0\n\t" \
  "s_waitcnt lgkmcnt(9)\n\t" \
  "ds_read_b64_tr_b16 v[216:217], v249 offset:14848\n\t" \
  "v_lshlrev_b32_sdwa v166, 1, v166 dst_sel:DWORD dst_unused:UNUSED_PAD src0_sel:DWORD src1_sel:WORD_1\n\t" \
  "v_mfma_f32_16x16x32_bf16 v[176:179], v[220:223], v[100:103], v[176:179]\n\t" \
  "ds_read_u16_d16_hi v164, v164 offset:0\n\t" \
  "v_lshlrev_b32_sdwa v167, 1, v167 dst_sel:DWORD dst_unused:UNUSED_PAD src0_sel:DWORD src1_sel:WORD_1\n\t" \
  "v_mfma_f32_16x16x32_bf16 v[184:187], v[220:223], v[112:115], v[184:187]\n\t" \
  "ds_read_b64_tr_b16 v[218:219], v249 offset:15104\n\t" \
  "v_rcp_f16_sdwa v224, v224 dst_sel:DWORD dst_unused:UNUSED_PAD src0_sel:WORD_1\n\t" \
  "v_mfma_f32_16x16x32_bf16 v[192:195], v[220:223], v[124:127], v[192:195]\n\t" \
  "ds_read_u16_d16_hi v165, v165 offset:0\n\t" \
  "v_lshlrev_b32_sdwa v172, 1, v172 dst_sel:DWORD dst_unused:UNUSED_PAD src0_sel:DWORD src1_sel:WORD_1\n\t" \
  "v_mfma_f32_16x16x32_bf16 v[200:203], v[220:223], v[136:139], v[200:203]\n\t" \
  "s_waitcnt lgkmcnt(9)\n\t" \
  "ds_read_b64_tr_b16 v[220:221], v249 offset:16896\n\t" \
  "ds_read_u16_d16_hi v166, v166 offset:0\n\t" \
  "v_mfma_f32_16x16x32_bf16 v[176:179], v[208:211], v[104:107], v[176:179]\n\t" \
  "ds_read_b64_tr_b16 v[222:223], v249 offset:17152\n\t" \
  "v_lshlrev_b32_sdwa v173, 1, v173 dst_sel:DWORD dst_unused:UNUSED_PAD src0_sel:DWORD src1_sel:WORD_1\n\t" \
  "v_fma_mix_f32 v156, v156, v225, 0 op_sel_hi:[0,1,0]\n\t" \
  "v_mfma_f32_16x16x32_bf16 v[184:187], v[208:211], v[116:119], v[184:187]\n\t" \
  "ds_read_u16_d16_hi v167, v167 offset:0\n\t" \
  "v_fma_mix_f32 v157, v157, v224, 0 op_sel_hi:[0,1,0]\n\t" \
  "v_mfma_f32_16x16x32_bf16 v[192:195], v[208:211], v[128:131], v[192:195]\n\t" \
  "ds_read_u16_d16_hi v172, v172 offset:0\n\t" \
  "v_lshlrev_b32_sdwa v174, 1, v174 dst_sel:DWORD dst_unused:UNUSED_PAD src0_sel:DWORD src1_sel:WORD_1\n\t" \
  "v_mfma_f32_16x16x32_bf16 v[200:203], v[208:211], v[140:143], v[200:203]\n\t" \
  "s_waitcnt lgkmcnt(10)\n\t" \
  "ds_read_b64_tr_b16 v[208:209], v250 offset:12320\n\t" \
  "v_lshlrev_b32_sdwa v175, 1, v175 dst_sel:DWORD dst_unused:UNUSED_PAD src0_sel:DWORD src1_sel:WORD_1\n\t" \
  "v_mfma_f32_16x16x32_bf16 v[180:183], v[212:215], v[96:99], 0\n\t" \
  "ds_read_u16_d16_hi v173, v173 offset:0\n\t" \
  "v_lshlrev_b32_sdwa v176, 1, v176 dst_sel:DWORD dst_unused:UNUSED_PAD src0_sel:DWORD src1_sel:WORD_1\n\t" \
  "v_mfma_f32_16x16x32_bf16 v[188:191], v[212:215], v[108:111], 0\n\t" \
  "ds_read_b64_tr_b16 v[210:211], v250 offset:13088\n\t" \
  "v_mul_f32 v184, %[kk], v184\n\t" \
  "v_mfma_f32_16x16x32_bf16 v[196:199], v[212:215], v[120:123], 0\n\t" \
  "ds_read_u16_d16_hi v174, v174 offset:0\n\t" \
  "v_lshlrev_b32_sdwa v177, 1, v177 dst_sel:DWORD dst_unused:UNUSED_PAD src0_sel:DWORD src1_sel:WORD_1\n\t" \
  "v_mul_f32 v185, %[kk], v185\n\t" \
  "v_mfma_f32_16x16x32_bf16 v[204:207], v[212:215], v[132:135], 0\n\t" \
  "s_waitcnt lgkmcnt(10)\n\t" \
  "ds_read_b64_tr_b16 v[212:213], v250 offset:12328\n\t" \
  "ds_read_u16_d16_hi v175, v175 offset:0\n\t" \
  "v_cvt_pk_f16_f32 v224, v184, v185\n\t" \
  "v_mfma_f32_16x16x32_bf16 v[180:183], v[216:219], v[100:103], v[180:183]\n\t" \
  "ds_read_b64_tr_b16 v[214:215], v250 offset:13096\n\t" \
  "v_lshlrev_b32_sdwa v178, 1, v178 dst_sel:DWORD dst_unused:UNUSED_PAD src0_sel:DWORD src1_sel:WORD_1\n\t" \
  "v_mfma_f32_16x16x32_bf16 v[188:191], v[216:219], v[112:115], v[188:191]\n\t" \
  "ds_read_u16_d16_hi v176, v176 offset:0\n\t" \
  "v_pk_fma_f16 v225, v224, v224, %[h1] neg_lo:[1,0,0] neg_hi:[1,0,0]\n\t" \
  "v_mfma_f32_16x16x32_bf16 v[196:199], v[216:219], v[124:127], v[196:199]\n\t" \
  "ds_read_u16_d16_hi v177, v177 offset:0\n\t" \
  "v_lshlrev_b32_sdwa v179, 1, v179 dst_sel:DWORD dst_unused:UNUSED_PAD src0_sel:DWORD src1_sel:WORD_1\n\t" \
  "v_pk_mul_f16 v225, v224, v225\n\t" \
  "v_mfma_f32_16x16x32_bf16 v[204:207], v[216:219], v[136:139], v[204:207]\n\t" \
  "s_waitcnt lgkmcnt(11)\n\t" \
  "ds_read_b64_tr_b16 v[216:217], v250 offset:12384\n\t" \
  "v_lshlrev_b32_sdwa v186, 1, v186 dst_sel:DWORD dst_unused:UNUSED_PAD src0_sel:DWORD src1_sel:WORD_1\n\t" \
  "v_mfma_f32_16x16x32_bf16 v[180:183], v[220:223], v[104:107], v[180:183]\n\t" \
  "ds_read_u16_d16_hi v178, v178 offset:0\n\t" \
  "v_exp_f16 v224, v225\n\t" \
  "v_mfma_f32_16x16x32_bf16 v[188:191], v[220:223], v[116:119], v[188:191]\n\t" \
  "ds_read_b64_tr_b16 v[218:219], v250 offset:13152\n\t" \
  "v_lshlrev_b32_sdwa v187, 1, v187 dst_sel:DWORD dst_unused:UNUSED_PAD src0_sel:DWORD src1_sel:WORD_1\n\t" \
  "v_mfma_f32_16x16x32_bf16 v[196:199], v[220:223], v[128:131], v[196:199]\n\t" \
  "ds_read_u16_d16_hi v179, v179 offset:0\n\t" \
  "v_lshlrev_b32_sdwa v192, 1, v192 dst_sel:DWORD dst_unused:UNUSED_PAD src0_sel:DWORD src1_sel:WORD_1\n\t" \
  "v_mfma_f32_16x16x32_bf16 v[204:207], v[220:223], v[140:143], v[204:207]\n\t" \
  "s_waitcnt lgkmcnt(10)\n\t" \
  "ds_read_b64_tr_b16 v[220:221], v250 offset:12392\n\t" \
  "ds_read_u16_d16_hi v186, v186 offset:0\n\t" \
  "v_exp_f16_sdwa v224, v225 dst_sel:WORD_1 dst_unused:UNUSED_PRESERVE src0_sel:WORD_1\n\t" \
  "v_smfmac_f32_16x16x64_bf16 v[0:3], v[208:211], v[144:151], v248\n\t" \
  "ds_read_b64_tr_b16 v[222:223], v250 offset:13160\n\t" \
  "v_lshlrev_b32_sdwa v193, 1, v193 dst_sel:DWORD dst_unused:UNUSED_PAD src0_sel:DWORD src1_sel:WORD_1\n\t" \
  "v_smfmac_f32_16x16x64_bf16 v[24:27], v[208:211], v[152:159], v248\n\t" \
  "ds_read_u16_d16_hi v187, v187 offset:0\n\t" \
  "v_pk_fma_f16 v224, v224, %[hc], %[hc]\n\t" \
  "v_smfmac_f32_16x16x64_bf16 v[48:51], v[208:211], v[160:167], v248\n\t" \
  "s_waitcnt lgkmcnt(11)\n\t" \
  "ds_read_u16_d16_hi v192, v192 offset:0\n\t" \
  "v_lshlrev_b32_sdwa v194, 1, v194 dst_sel:DWORD dst_unused:UNUSED_PAD src0_sel:DWORD src1_sel:WORD_1\n\t" \
  "v_smfmac_f32_16x16x64_bf16 v[72:75], v[208:211], v[168:175], v248\n\t" \
  "s_waitcnt lgkmcnt(11)\n\t" \
  "ds_read_b64_tr_b16 v[208:209], v250 offset:12448\n\t" \
  "v_rcp_f16 v225, v224\n\t" \
  "v_smfmac_f32_16x16x64_bf16 v[4:7], v[212:215], v[144:151], v248\n\t" \
  "ds_read_u16_d16_hi v193, v193 offset:0\n\t" \
  "v_lshlrev_b32_sdwa v195, 1, v195 dst_sel:DWORD dst_unused:UNUSED_PAD src0_sel:DWORD src1_sel:WORD_1\n\t" \
  "v_smfmac_f32_16x16x64_bf16 v[28:31], v[212:215], v[152:159], v248\n\t" \
  "ds_read_b64_tr_b16 v[210:211], v250 offset:13216\n\t" \
  "v_lshlrev_b32_sdwa v200, 1, v200 dst_sel:DWORD dst_unused:UNUSED_PAD src0_sel:DWORD src1_sel:WORD_1\n\t" \
  "v_smfmac_f32_16x16x64_bf16 v[52:55], v[212:215], v[160:167], v248\n\t" \
  "ds_read_u16_d16_hi v194, v194 offset:0\n\t" \
  "v_rcp_f16_sdwa v224, v224 dst_sel:DWORD dst_unused:UNUSED_PAD src0_sel:WORD_1\n\t" \
  "v_smfmac_f32_16x16x64_bf16 v[76:79], v[212:215], v[168:175], v248\n\t" \
  "s_waitcnt lgkmcnt(10)\n\t" \
  "ds_read_b64_tr_b16 v[212:213], v250 offset:12456\n\t" \
  "v_lshlrev_b32_sdwa v201, 1, v201 dst_sel:DWORD dst_unused:UNUSED_PAD src0_sel:DWORD src1_sel:WORD_1\n\t" \
  "v_smfmac_f32_16x16x64_bf16 v[8:11], v[216:219], v[144:151], v248\n\t" \
  "ds_read_u16_d16_hi v195, v195 offset:0\n\t" \
  "v_fma_mix_f32 v184, v184, v225, 0 op_sel_hi:[0,1,0]\n\t" \
  "v_smfmac_f32_16x16x64_bf16 v[32:35], v[216:219], v[152:159], v248\n\t" \
  "ds_read_b64_tr_b16 v[214:215], v250 offset:13224\n\t" \
  "v_lshlrev_b32_sdwa v202, 1, v202 dst_sel:DWORD dst_unused:UNUSED_PAD src0_sel:DWORD src1_sel:WORD_1\n\t" \
  "v_smfmac_f32_16x16x64_bf16 v[56:59], v[216:219], v[160:167], v248\n\t" \
  "ds_read_u16_d16_hi v200, v200 offset:0\n\t" \
  "v_fma_mix_f32 v185, v185, v224, 0 op_sel_hi:[0,1,0]\n\t" \
  "v_smfmac_f32_16x16x64_bf16 v[80:83], v[216:219], v[168:175], v248\n\t" \
  "s_waitcnt lgkmcnt(10)\n\t" \
  "s_barrier\n\t" \
  "s_branch .Lcnx_m\n\t" \
  ".Lcnx_mx:\n\t" \
  "ds_read_b64_tr_b16 v[216:217], v249 offset:12288\n\t" \
  "v_lshlrev_b32_sdwa v148, 1, v148 dst_sel:DWORD dst_unused:UNUSED_PAD src0_sel:DWORD src1_sel:WORD_1\n\t" \
  "v_smfmac_f32_16x16x64_bf16 v[12:15], v[220:223], v[176:183], v248\n\t" \
  "ds_read_u16_d16_hi v170, v170 offset:0\n\t" \
  "v_mul_f32 v157, %[kk], v157\n\t" \
  "v_smfmac_f32_16x16x64_bf16 v[36:39], v[220:223], v[184:191], v248\n\t" \
  "ds_read_b64_tr_b16 v[218:219], v249 offset:12544\n\t" \
  "v_cvt_pk_f16_f32 v224, v156, v157\n\t" \
  "v_smfmac_f32_16x16x64_bf16 v[60:63], v[220:223], v[192:199], v248\n\t" \
  "ds_read_u16_d16_hi v171, v171 offset:0\n\t" \
  "v_lshlrev_b32_sdwa v149, 1, v149 dst_sel:DWORD dst_unused:UNUSED_PAD src0_sel:DWORD src1_sel:WORD_1\n\t" \
  "v_smfmac_f32_16x16x64_bf16 v[84:87], v[220:223], v[200:207], v248\n\t" \
  "s_waitcnt lgkmcnt(9)\n\t" \
  "ds_read_b64_tr_b16 v[220:221], v249 offset:14336\n\t" \
  "v_pk_fma_f16 v225, v224, v224, %[h1] neg_lo:[1,0,0] neg_hi:[1,0,0]\n\t" \
  "v_smfmac_f32_16x16x64_bf16 v[16:19], v[208:211], v[176:183], v248\n\t" \
  "s_add_u32 %[rs], %[rs], 1\n\t" \
  "ds_read_u16_d16_hi v148, v148 offset:0\n\t" \
  "v_lshlrev_b32_sdwa v150, 1, v150 dst_sel:DWORD dst_unused:UNUSED_PAD src0_sel:DWORD src1_sel:WORD_1\n\t" \
  "v_smfmac_f32_16x16x64_bf16 v[40:43], v[208:211], v[184:191], v248\n\t" \
  "s_cmp_eq_u32 %[rs], 6\n\t" \
  "ds_read_b64_tr_b16 v[222:223], v249 offset:14592\n\t" \
  "v_pk_mul_f16 v225, v224, v225\n\t" \
  "v_smfmac_f32_16x16x64_bf16 v[64:67], v[208:211], v[192:199], v248\n\t" \
  "ds_read_u16_d16_hi v149, v149 offset:0\n\t" \
  "v_lshlrev_b32_sdwa v151, 1, v151 dst_sel:DWORD dst_unused:UNUSED_PAD src0_sel:DWORD src1_sel:WORD_1\n\t" \
  "v_smfmac_f32_16x16x64_bf16 v[88:91], v[208:211], v[200:207], v248\n\t" \
  "s_waitcnt lgkmcnt(9)\n\t" \
  "ds_read_b64_tr_b16 v[208:209], v249 offset:16384\n\t" \
  "v_exp_f16 v224, v225\n\t" \
  "v_smfmac_f32_16x16x64_bf16 v[20:23], v[212:215], v[176:183], v248\n\t" \
  "s_cselect_b32 %[rs], 0, %[rs]\n\t" \
  "ds_read_u16_d16_hi v150, v150 offset:0\n\t" \
  "v_lshlrev_b32_sdwa v158, 1, v158 dst_sel:DWORD dst_unused:UNUSED_PAD src0_sel:DWORD src1_sel:WORD_1\n\t" \
  "v_smfmac_f32_16x16x64_bf16 v[44:47], v[212:215], v[184:191], v248\n\t" \
  "s_cselect_b32 %[t0], -75920, %[ps]\n\t" \
  "ds_read_b64_tr_b16 v[210:211], v249 offset:16640\n\t" \
  "v_lshlrev_b32_sdwa v159, 1, v159 dst_sel:DWORD dst_unused:UNUSED_PAD src0_sel:DWORD src1_sel:WORD_1\n\t" \
  "v_smfmac_f32_16x16x64_bf16 v[68:71], v[212:215], v[192:199], v248\n\t" \
  "ds_read_u16_d16_hi v151, v151 offset:0\n\t" \
  "v_add_u32 v252, %[t0], v252\n\t" \
  "v_exp_f16_sdwa v224, v225 dst_sel:WORD_1 dst_unused:UNUSED_PRESERVE src0_sel:WORD_1\n\t" \
  "v_smfmac_f32_16x16x64_bf16 v[92:95], v[212:215], v[200:207], v248\n\t" \
  "s_waitcnt lgkmcnt(9)\n\t" \
  "ds_read_b64_tr_b16 v[212:213], v249 offset:12800\n\t" \
  "v_lshlrev_b32_sdwa v164, 1, v164 dst_sel:DWORD dst_unused:UNUSED_PAD src0_sel:DWORD src1_sel:WORD_1\n\t" \
  "v_mfma_f32_16x16x32_bf16 v[176:179], v[216:219], v[96:99], 0\n\t" \
  "ds_read_u16_d16_hi v158, v158 offset:0\n\t" \
  "v_pk_fma_f16 v224, v224, %[hc], %[hc]\n\t" \
  "v_mfma_f32_16x16x32_bf16 v[184:187], v[216:219], v[108:111], 0\n\t" \
  "ds_read_b64_tr_b16 v[214:215], v249 offset:13056\n\t" \
  "v_lshlrev_b32_sdwa v165, 1, v165 dst_sel:DWORD dst_unused:UNUSED_PAD src0_sel:DWORD src1_sel:WORD_1\n\t" \
  "v_mfma_f32_16x16x32_bf16 v[192:195], v[216:219], v[120:123], 0\n\t" \
  "ds_read_u16_d16_hi v159, v159 offset:0\n\t" \
  "v_rcp_f16 v225, v224\n\t" \
  "v_mfma_f32_16x16x32_bf16 v[200:203], v[216:219], v[132:135], 0\n\t" \
  "s_waitcnt lgkmcnt(9)\n\t" \
  "ds_read_b64_tr_b16 v[216:217], v249 offset:14848\n\t" \
  "v_lshlrev_b32_sdwa v166, 1, v166 dst_sel:DWORD dst_unused:UNUSED_PAD src0_sel:DWORD src1_sel:WORD_1\n\t" \
  "v_mfma_f32_16x16x32_bf16 v[176:179], v[220:223], v[100:103], v[176:179]\n\t" \
  "ds_read_u16_d16_hi v164, v164 offset:0\n\t" \
  "v_lshlrev_b32_sdwa v167, 1, v167 dst_sel:DWORD dst_unused:UNUSED_PAD src0_sel:DWORD src1_sel:WORD_1\n\t" \
  "v_mfma_f32_16x16x32_bf16 v[184:187], v[220:223], v[112:115], v[184:187]\n\t" \
  "ds_read_b64_tr_b16 v[218:219], v249 offset:15104\n\t" \
  "v_rcp_f16_sdwa v224, v224 dst_sel:DWORD dst_unused:UNUSED_PAD src0_sel:WORD_1\n\t" \
  "v_mfma_f32_16x16x32_bf16 v[192:195], v[220:223], v[124:127], v[192:195]\n\t" \
  "ds_read_u16_d16_hi v165, v165 offset:0\n\t" \
  "v_lshlrev_b32_sdwa v172, 1, v172 dst_sel:DWORD dst_unused:UNUSED_PAD src0_sel:DWORD src1_sel:WORD_1\n\t" \
  "v_mfma_f32_16x16x32_bf16 v[200:203], v[220:223], v[136:139], v[200:203]\n\t" \
  "s_waitcnt lgkmcnt(9)\n\t" \
  "ds_read_b64_tr_b16 v[220:221], v249 offset:16896\n\t" \
  "ds_read_u16_d16_hi v166, v166 offset:0\n\t" \
  "v_mfma_f32_16x16x32_bf16 v[176:179], v[208:211], v[104:107], v[176:179]\n\t" \
  "ds_read_b64_tr_b16 v[222:223], v249 offset:17152\n\t" \
  "v_lshlrev_b32_sdwa v173, 1, v173 dst_sel:DWORD dst_unused:UNUSED_PAD src0_sel:DWORD src1_sel:WORD_1\n\t" \
  "v_fma_mix_f32 v156, v156, v225, 0 op_sel_hi:[0,1,0]\n\t" \
  "v_mfma_f32_16x16x32_bf16 v[184:187], v[208:211], v[116:119], v[184:187]\n\t" \
  "ds_read_u16_d16_hi v167, v167 offset:0\n\t" \
  "v_fma_mix_f32 v157, v157, v224, 0 op_sel_hi:[0,1,0]\n\t" \
  "v_mfma_f32_16x16x32_bf16 v[192:195], v[208:211], v[128:131], v[192:195]\n\t" \
  "ds_read_u16_d16_hi v172, v172 offset:0\n\t" \
  "v_lshlrev_b32_sdwa v174, 1, v174 dst_sel:DWORD dst_unused:UNUSED_PAD src0_sel:DWORD src1_sel:WORD_1\n\t" \
  "v_mfma_f32_16x16x32_bf16 v[200:203], v[208:211], v[140:143], v[200:203]\n\t" \
  "s_waitcnt lgkmcnt(10)\n\t" \
  "ds_read_b64_tr_b16 v[208:209], v250 offset:12320\n\t" \
  "v_lshlrev_b32_sdwa v175, 1, v175 dst_sel:DWORD dst_unused:UNUSED_PAD src0_sel:DWORD src1_sel:WORD_1\n\t" \
  "v_mfma_f32_16x16x32_bf16 v[180:183], v[212:215], v[96:99], 0\n\t" \
  "ds_read_u16_d16_hi v173, v173 offset:0\n\t" \
  "v_lshlrev_b32_sdwa v176, 1, v176 dst_sel:DWORD dst_unused:UNUSED_PAD src0_sel:DWORD src1_sel:WORD_1\n\t" \
  "v_mfma_f32_16x16x32_bf16 v[188:191], v[212:215], v[108:111], 0\n\t" \
  "ds_read_b64_tr_b16 v[210:211], v250 offset:13088\n\t" \
  "v_mul_f32 v184, %[kk], v184\n\t" \
  "v_mfma_f32_16x16x32_bf16 v[196:199], v[212:215], v[120:123], 0\n\t" \
  "ds_read_u16_d16_hi v174, v174 offset:0\n\t" \
  "v_lshlrev_b32_sdwa v177, 1, v177 dst_sel:DWORD dst_unused:UNUSED_PAD src0_sel:DWORD src1_sel:WORD_1\n\t" \
  "v_mul_f32 v185, %[kk], v185\n\t" \
  "v_mfma_f32_16x16x32_bf16 v[204:207], v[212:215], v[132:135], 0\n\t" \
  "s_waitcnt lgkmcnt(10)\n\t" \
  "ds_read_b64_tr_b16 v[212:213], v250 offset:12328\n\t" \
  "ds_read_u16_d16_hi v175, v175 offset:0\n\t" \
  "v_cvt_pk_f16_f32 v224, v184, v185\n\t" \
  "v_mfma_f32_16x16x32_bf16 v[180:183], v[216:219], v[100:103], v[180:183]\n\t" \
  "ds_read_b64_tr_b16 v[214:215], v250 offset:13096\n\t" \
  "v_lshlrev_b32_sdwa v178, 1, v178 dst_sel:DWORD dst_unused:UNUSED_PAD src0_sel:DWORD src1_sel:WORD_1\n\t" \
  "v_mfma_f32_16x16x32_bf16 v[188:191], v[216:219], v[112:115], v[188:191]\n\t" \
  "ds_read_u16_d16_hi v176, v176 offset:0\n\t" \
  "v_pk_fma_f16 v225, v224, v224, %[h1] neg_lo:[1,0,0] neg_hi:[1,0,0]\n\t" \
  "v_mfma_f32_16x16x32_bf16 v[196:199], v[216:219], v[124:127], v[196:199]\n\t" \
  "ds_read_u16_d16_hi v177, v177 offset:0\n\t" \
  "v_lshlrev_b32_sdwa v179, 1, v179 dst_sel:DWORD dst_unused:UNUSED_PAD src0_sel:DWORD src1_sel:WORD_1\n\t" \
  "v_pk_mul_f16 v225, v224, v225\n\t" \
  "v_mfma_f32_16x16x32_bf16 v[204:207], v[216:219], v[136:139], v[204:207]\n\t" \
  "s_waitcnt lgkmcnt(11)\n\t" \
  "ds_read_b64_tr_b16 v[216:217], v250 offset:12384\n\t" \
  "v_lshlrev_b32_sdwa v186, 1, v186 dst_sel:DWORD dst_unused:UNUSED_PAD src0_sel:DWORD src1_sel:WORD_1\n\t" \
  "v_mfma_f32_16x16x32_bf16 v[180:183], v[220:223], v[104:107], v[180:183]\n\t" \
  "ds_read_u16_d16_hi v178, v178 offset:0\n\t" \
  "v_exp_f16 v224, v225\n\t" \
  "v_mfma_f32_16x16x32_bf16 v[188:191], v[220:223], v[116:119], v[188:191]\n\t" \
  "ds_read_b64_tr_b16 v[218:219], v250 offset:13152\n\t" \
  "v_lshlrev_b32_sdwa v187, 1, v187 dst_sel:DWORD dst_unused:UNUSED_PAD src0_sel:DWORD src1_sel:WORD_1\n\t" \
  "v_mfma_f32_16x16x32_bf16 v[196:199], v[220:223], v[128:131], v[196:199]\n\t" \
  "ds_read_u16_d16_hi v179, v179 offset:0\n\t" \
  "v_lshlrev_b32_sdwa v192, 1, v192 dst_sel:DWORD dst_unused:UNUSED_PAD src0_sel:DWORD src1_sel:WORD_1\n\t" \
  "v_mfma_f32_16x16x32_bf16 v[204:207], v[220:223], v[140:143], v[204:207]\n\t" \
  "s_waitcnt lgkmcnt(10)\n\t" \
  "ds_read_b64_tr_b16 v[220:221], v250 offset:12392\n\t" \
  "ds_read_u16_d16_hi v186, v186 offset:0\n\t" \
  "v_exp_f16_sdwa v224, v225 dst_sel:WORD_1 dst_unused:UNUSED_PRESERVE src0_sel:WORD_1\n\t" \
  "v_smfmac_f32_16x16x64_bf16 v[0:3], v[208:211], v[144:151], v248\n\t" \
  "ds_read_b64_tr_b16 v[222:223], v250 offset:13160\n\t" \
  "v_lshlrev_b32_sdwa v193, 1, v193 dst_sel:DWORD dst_unused:UNUSED_PAD src0_sel:DWORD src1_sel:WORD_1\n\t" \
  "v_smfmac_f32_16x16x64_bf16 v[24:27], v[208:211], v[152:159], v248\n\t" \
  "ds_read_u16_d16_hi v187, v187 offset:0\n\t" \
  "v_pk_fma_f16 v224, v224, %[hc], %[hc]\n\t" \
  "v_smfmac_f32_16x16x64_bf16 v[48:51], v[208:211], v[160:167], v248\n\t" \
  "s_waitcnt lgkmcnt(11)\n\t" \
  "ds_read_u16_d16_hi v192, v192 offset:0\n\t" \
  "v_lshlrev_b32_sdwa v194, 1, v194 dst_sel:DWORD dst_unused:UNUSED_PAD src0_sel:DWORD src1_sel:WORD_1\n\t" \
  "v_smfmac_f32_16x16x64_bf16 v[72:75], v[208:211], v[168:175], v248\n\t" \
  "s_waitcnt lgkmcnt(11)\n\t" \
  "ds_read_b64_tr_b16 v[208:209], v250 offset:12448\n\t" \
  "v_rcp_f16 v225, v224\n\t" \
  "v_smfmac_f32_16x16x64_bf16 v[4:7], v[212:215], v[144:151], v248\n\t" \
  "ds_read_u16_d16_hi v193, v193 offset:0\n\t" \
  "v_lshlrev_b32_sdwa v195, 1, v195 dst_sel:DWORD dst_unused:UNUSED_PAD src0_sel:DWORD src1_sel:WORD_1\n\t" \
  "v_smfmac_f32_16x16x64_bf16 v[28:31], v[212:215], v[152:159], v248\n\t" \
  "ds_read_b64_tr_b16 v[210:211], v250 offset:13216\n\t" \
  "v_lshlrev_b32_sdwa v200, 1, v200 dst_sel:DWORD dst_unused:UNUSED_PAD src0_sel:DWORD src1_sel:WORD_1\n\t" \
  "v_smfmac_f32_16x16x64_bf16 v[52:55], v[212:215], v[160:167], v248\n\t" \
  "ds_read_u16_d16_hi v194, v194 offset:0\n\t" \
  "v_rcp_f16_sdwa v224, v224 dst_sel:DWORD dst_unused:UNUSED_PAD src0_sel:WORD_1\n\t" \
  "v_smfmac_f32_16x16x64_bf16 v[76:79], v[212:215], v[168:175], v248\n\t" \
  "s_waitcnt lgkmcnt(10)\n\t" \
  "ds_read_b64_tr_b16 v[212:213], v250 offset:12456\n\t" \
  "v_lshlrev_b32_sdwa v201, 1, v201 dst_sel:DWORD dst_unused:UNUSED_PAD src0_sel:DWORD src1_sel:WORD_1\n\t" \
  "v_smfmac_f32_16x16x64_bf16 v[8:11], v[216:219], v[144:151], v248\n\t" \
  "ds_read_u16_d16_hi v195, v195 offset:0\n\t" \
  "v_fma_mix_f32 v184, v184, v225, 0 op_sel_hi:[0,1,0]\n\t" \
  "v_smfmac_f32_16x16x64_bf16 v[32:35], v[216:219], v[152:159], v248\n\t" \
  "ds_read_b64_tr_b16 v[214:215], v250 offset:13224\n\t" \
  "v_lshlrev_b32_sdwa v202, 1, v202 dst_sel:DWORD dst_unused:UNUSED_PAD src0_sel:DWORD src1_sel:WORD_1\n\t" \
  "v_smfmac_f32_16x16x64_bf16 v[56:59], v[216:219], v[160:167], v248\n\t" \
  "ds_read_u16_d16_hi v200, v200 offset:0\n\t" \
  "v_fma_mix_f32 v185, v185, v224, 0 op_sel_hi:[0,1,0]\n\t" \
  "v_smfmac_f32_16x16x64_bf16 v[80:83], v[216:219], v[168:175], v248\n\t" \
  "s_waitcnt lgkmcnt(10)\n\t" \
  "s_barrier\n\t" \
  "ds_read_b64_tr_b16 v[216:217], v249 offset:0\n\t" \
  "v_lshlrev_b32_sdwa v203, 1, v203 dst_sel:DWORD dst_unused:UNUSED_PAD src0_sel:DWORD src1_sel:WORD_1\n\t" \
  "v_smfmac_f32_16x16x64_bf16 v[12:15], v[220:223], v[144:151], v248\n\t" \
  "ds_read_u16_d16_hi v201, v201 offset:0\n\t" \
  "v_mul_f32 v188, %[kk], v188\n\t" \
  "v_smfmac_f32_16x16x64_bf16 v[36:39], v[220:223], v[152:159], v248\n\t" \
  "ds_read_b64_tr_b16 v[218:219], v249 offset:256\n\t" \
  "v_lshlrev_b32_sdwa v180, 1, v180 dst_sel:DWORD dst_unused:UNUSED_PAD src0_sel:DWORD src1_sel:WORD_1\n\t" \
  "v_mul_f32 v189, %[kk], v189\n\t" \
  "v_smfmac_f32_16x16x64_bf16 v[60:63], v[220:223], v[160:167], v248\n\t" \
  "ds_read_u16_d16_hi v202, v202 offset:0\n\t" \
  "v_cvt_pk_f16_f32 v224, v188, v189\n\t" \
  "v_smfmac_f32_16x16x64_bf16 v[84:87], v[220:223], v[168:175], v248\n\t" \
  "s_waitcnt lgkmcnt(9)\n\t" \
  "ds_read_b64_tr_b16 v[220:221], v249 offset:2048\n\t" \
  "v_lshlrev_b32_sdwa v181, 1, v181 dst_sel:DWORD dst_unused:UNUSED_PAD src0_sel:DWORD src1_sel:WORD_1\n\t" \
  "v_smfmac_f32_16x16x64_bf16 v[16:19], v[208:211], v[144:151], v248\n\t" \
  "ds_read_u16_d16_hi v203, v203 offset:0\n\t" \
  "v_pk_fma_f16 v225, v224, v224, %[h1] neg_lo:[1,0,0] neg_hi:[1,0,0]\n\t" \
  "v_smfmac_f32_16x16x64_bf16 v[40:43], v[208:211], v[152:159], v248\n\t" \
  "ds_read_b64_tr_b16 v[222:223], v249 offset:2304\n\t" \
  "v_lshlrev_b32_sdwa v182, 1, v182 dst_sel:DWORD dst_unused:UNUSED_PAD src0_sel:DWORD src1_sel:WORD_1\n\t" \
  "v_pk_mul_f16 v225, v224, v225\n\t" \
  "v_smfmac_f32_16x16x64_bf16 v[64:67], v[208:211], v[160:167], v248\n\t" \
  "ds_read_u16_d16_hi v180, v180 offset:0\n\t" \
  "v_lshlrev_b32_sdwa v183, 1, v183 dst_sel:DWORD dst_unused:UNUSED_PAD src0_sel:DWORD src1_sel:WORD_1\n\t" \
  "v_smfmac_f32_16x16x64_bf16 v[88:91], v[208:211], v[168:175], v248\n\t" \
  "s_waitcnt lgkmcnt(9)\n\t" \
  "ds_read_b64_tr_b16 v[208:209], v249 offset:4096\n\t" \
  "ds_read_u16_d16_hi v181, v181 offset:0\n\t" \
  "v_exp_f16 v224, v225\n\t" \
  "v_smfmac_f32_16x16x64_bf16 v[20:23], v[212:215], v[144:151], v248\n\t" \
  "ds_read_b64_tr_b16 v[210:211], v249 offset:4352\n\t" \
  "v_lshlrev_b32_sdwa v190, 1, v190 dst_sel:DWORD dst_unused:UNUSED_PAD src0_sel:DWORD src1_sel:WORD_1\n\t" \
  "v_smfmac_f32_16x16x64_bf16 v[44:47], v[212:215], v[152:159], v248\n\t" \
  "ds_read_u16_d16_hi v182, v182 offset:0\n\t" \
  "v_lshlrev_b32_sdwa v191, 1, v191 dst_sel:DWORD dst_unused:UNUSED_PAD src0_sel:DWORD src1_sel:WORD_1\n\t" \
  "v_smfmac_f32_16x16x64_bf16 v[68:71], v[212:215], v[160:167], v248\n\t" \
  "ds_read_u16_d16_hi v183, v183 offset:0\n\t" \
  "v_exp_f16_sdwa v224, v225 dst_sel:WORD_1 dst_unused:UNUSED_PRESERVE src0_sel:WORD_1\n\t" \
  "v_smfmac_f32_16x16x64_bf16 v[92:95], v[212:215], v[168:175], v248\n\t" \
  "s_waitcnt lgkmcnt(10)\n\t" \
  "ds_read_b64_tr_b16 v[212:213], v249 offset:512\n\t" \
  "v_lshlrev_b32_sdwa v196, 1, v196 dst_sel:DWORD dst_unused:UNUSED_PAD src0_sel:DWORD src1_sel:WORD_1\n\t" \
  "v_pk_fma_f16 v224, v224, %[hc], %[hc]\n\t" \
  "v_mfma_f32_16x16x32_bf16 v[152:155], v[216:219], v[108:111], 0\n\t" \
  "ds_read_u16_d16_hi v190, v190 offset:0\n\t" \
  "v_lshlrev_b32_sdwa v197, 1, v197 dst_sel:DWORD dst_unused:UNUSED_PAD src0_sel:DWORD src1_sel:WORD_1\n\t" \
  "v_mfma_f32_16x16x32_bf16 v[160:163], v[216:219], v[120:123], 0\n\t" \
  "ds_read_b64_tr_b16 v[214:215], v249 offset:768\n\t" \
  "v_rcp_f16 v225, v224\n\t" \
  "v_mfma_f32_16x16x32_bf16 v[168:171], v[216:219], v[132:135], 0\n\t" \
  "s_waitcnt lgkmcnt(9)\n\t" \
  "ds_read_u16_d16_hi v191, v191 offset:0\n\t" \
  "ds_read_b64_tr_b16 v[216:217], v249 offset:2560\n\t" \
  "v_lshlrev_b32_sdwa v198, 1, v198 dst_sel:DWORD dst_unused:UNUSED_PAD src0_sel:DWORD src1_sel:WORD_1\n\t" \
  "v_mfma_f32_16x16x32_bf16 v[152:155], v[220:223], v[112:115], v[152:155]\n\t" \
  "ds_read_u16_d16_hi v196, v196 offset:0\n\t" \
  "v_lshlrev_b32_sdwa v199, 1, v199 dst_sel:DWORD dst_unused:UNUSED_PAD src0_sel:DWORD src1_sel:WORD_1\n\t" \
  "v_rcp_f16_sdwa v224, v224 dst_sel:DWORD dst_unused:UNUSED_PAD src0_sel:WORD_1\n\t" \
  "v_mfma_f32_16x16x32_bf16 v[160:163], v[220:223], v[124:127], v[160:163]\n\t" \
  "ds_read_b64_tr_b16 v[218:219], v249 offset:2816\n\t" \
  "v_lshlrev_b32_sdwa v204, 1, v204 dst_sel:DWORD dst_unused:UNUSED_PAD src0_sel:DWORD src1_sel:WORD_1\n\t" \
  "v_mfma_f32_16x16x32_bf16 v[168:171], v[220:223], v[136:139], v[168:171]\n\t" \
  "s_waitcnt lgkmcnt(9)\n\t" \
  "ds_read_u16_d16_hi v197, v197 offset:0\n\t" \
  "ds_read_b64_tr_b16 v[220:221], v249 offset:4608\n\t" \
  "v_lshlrev_b32_sdwa v205, 1, v205 dst_sel:DWORD dst_unused:UNUSED_PAD src0_sel:DWORD src1_sel:WORD_1\n\t" \
  "v_fma_mix_f32 v188, v188, v225, 0 op_sel_hi:[0,1,0]\n\t" \
  "v_mfma_f32_16x16x32_bf16 v[152:155], v[208:211], v[116:119], v[152:155]\n\t" \
  "ds_read_u16_d16_hi v198, v198 offset:0\n\t" \
  "ds_read_b64_tr_b16 v[222:223], v249 offset:4864\n\t" \
  "v_fma_mix_f32 v189, v189, v224, 0 op_sel_hi:[0,1,0]\n\t" \
  "v_mfma_f32_16x16x32_bf16 v[160:163], v[208:211], v[128:131], v[160:163]\n\t" \
  "ds_read_u16_d16_hi v199, v199 offset:0\n\t" \
  "v_lshlrev_b32_sdwa v206, 1, v206 dst_sel:DWORD dst_unused:UNUSED_PAD src0_sel:DWORD src1_sel:WORD_1\n\t" \
  "v_mfma_f32_16x16x32_bf16 v[168:171], v[208:211], v[140:143], v[168:171]\n\t" \
  "s_waitcnt lgkmcnt(9)\n\t" \
  "ds_read_b64_tr_b16 v[208:209], v250 offset:18480\n\t" \
  "ds_read_u16_d16_hi v204, v204 offset:0\n\t" \
  "v_lshlrev_b32_sdwa v207, 1, v207 dst_sel:DWORD dst_unused:UNUSED_PAD src0_sel:DWORD src1_sel:WORD_1\n\t" \
  "v_mfma_f32_16x16x32_bf16 v[156:159], v[212:215], v[108:111], 0\n\t" \
  "ds_read_b64_tr_b16 v[210:211], v250 offset:19248\n\t" \
  "ds_read_u16_d16_hi v205, v205 offset:0\n\t" \
  "v_lshlrev_b32_sdwa v154, 1, v154 dst_sel:DWORD dst_unused:UNUSED_PAD src0_sel:DWORD src1_sel:WORD_1\n\t" \
  "v_mul_f32 v152, %[kk], v152\n\t" \
  "v_mfma_f32_16x16x32_bf16 v[164:167], v[212:215], v[120:123], 0\n\t" \
  "ds_read_u16_d16_hi v206, v206 offset:0\n\t" \
  "v_mul_f32 v153, %[kk], v153\n\t" \
  "v_mfma_f32_16x16x32_bf16 v[172:175], v[212:215], v[132:135], 0\n\t" \
  "s_waitcnt lgkmcnt(10)\n\t" \
  "ds_read_b64_tr_b16 v[212:213], v250 offset:18488\n\t" \
  "v_lshlrev_b32_sdwa v155, 1, v155 dst_sel:DWORD dst_unused:UNUSED_PAD src0_sel:DWORD src1_sel:WORD_1\n\t" \
  "v_cvt_pk_f16_f32 v224, v152, v153\n\t" \
  "v_mfma_f32_16x16x32_bf16 v[156:159], v[216:219], v[112:115], v[156:159]\n\t" \
  "ds_read_u16_d16_hi v207, v207 offset:0\n\t" \
  "v_lshlrev_b32_sdwa v160, 1, v160 dst_sel:DWORD dst_unused:UNUSED_PAD src0_sel:DWORD src1_sel:WORD_1\n\t" \
  "v_pk_fma_f16 v225, v224, v224, %[h1] neg_lo:[1,0,0] neg_hi:[1,0,0]\n\t" \
  "v_mfma_f32_16x16x32_bf16 v[164:167], v[216:219], v[124:127], v[164:167]\n\t" \
  "ds_read_b64_tr_b16 v[214:215], v250 offset:19256\n\t" \
  "v_pk_mul_f16 v225, v224, v225\n\t" \
  "v_mfma_f32_16x16x32_bf16 v[172:175], v[216:219], v[136:139], v[172:175]\n\t" \
  "s_waitcnt lgkmcnt(9)\n\t" \
  "ds_read_u16_d16_hi v154, v154 offset:0\n\t" \
  "ds_read_b64_tr_b16 v[216:217], v250 offset:18544\n\t" \
  "v_lshlrev_b32_sdwa v161, 1, v161 dst_sel:DWORD dst_unused:UNUSED_PAD src0_sel:DWORD src1_sel:WORD_1\n\t" \
  "v_exp_f16 v224, v225\n\t" \
  "v_mfma_f32_16x16x32_bf16 v[156:159], v[220:223], v[116:119], v[156:159]\n\t" \
  "ds_read_u16_d16_hi v155, v155 offset:0\n\t" \
  "v_lshlrev_b32_sdwa v162, 1, v162 dst_sel:DWORD dst_unused:UNUSED_PAD src0_sel:DWORD src1_sel:WORD_1\n\t" \
  "v_mfma_f32_16x16x32_bf16 v[164:167], v[220:223], v[128:131], v[164:167]\n\t" \
  "ds_read_b64_tr_b16 v[218:219], v250 offset:19312\n\t" \
  "v_exp_f16_sdwa v224, v225 dst_sel:WORD_1 dst_unused:UNUSED_PRESERVE src0_sel:WORD_1\n\t" \
  "v_mfma_f32_16x16x32_bf16 v[172:175], v[220:223], v[140:143], v[172:175]\n\t" \
  "s_waitcnt lgkmcnt(9)\n\t" \
  "ds_read_u16_d16_hi v160, v160 offset:0\n\t" \
  "ds_read_b64_tr_b16 v[220:221], v250 offset:18552\n\t" \
  "v_smfmac_f32_16x16x64_bf16 v[0:3], v[208:211], v[176:183], v248\n\t" \
  "ds_read_u16_d16_hi v161, v161 offset:0\n\t" \
  "v_lshlrev_b32_sdwa v163, 1, v163 dst_sel:DWORD dst_unused:UNUSED_PAD src0_sel:DWORD src1_sel:WORD_1\n\t" \
  "v_smfmac_f32_16x16x64_bf16 v[24:27], v[208:211], v[184:191], v248\n\t" \
  "ds_read_b64_tr_b16 v[222:223], v250 offset:19320\n\t" \
  "v_pk_fma_f16 v224, v224, %[hc], %[hc]\n\t" \
  "v_smfmac_f32_16x16x64_bf16 v[48:51], v[208:211], v[192:199], v248\n\t" \
  "s_waitcnt lgkmcnt(9)\n\t" \
  "ds_read_u16_d16_hi v162, v162 offset:0\n\t" \
  "v_lshlrev_b32_sdwa v168, 1, v168 dst_sel:DWORD dst_unused:UNUSED_PAD src0_sel:DWORD src1_sel:WORD_1\n\t" \
  "v_smfmac_f32_16x16x64_bf16 v[72:75], v[208:211], v[200:207], v248\n\t" \
  "s_waitcnt lgkmcnt(9)\n\t" \
  "ds_read_b64_tr_b16 v[208:209], v250 offset:18608\n\t" \
  "v_rcp_f16 v225, v224\n\t" \
  "v_smfmac_f32_16x16x64_bf16 v[4:7], v[212:215], v[176:183], v248\n\t" \
  "ds_read_u16_d16_hi v163, v163 offset:0\n\t" \
  "v_lshlrev_b32_sdwa v169, 1, v169 dst_sel:DWORD dst_unused:UNUSED_PAD src0_sel:DWORD src1_sel:WORD_1\n\t" \
  "v_smfmac_f32_16x16x64_bf16 v[28:31], v[212:215], v[184:191], v248\n\t" \
  "ds_read_b64_tr_b16 v[210:211], v250 offset:19376\n\t" \
  "v_rcp_f16_sdwa v224, v224 dst_sel:DWORD dst_unused:UNUSED_PAD src0_sel:WORD_1\n\t" \
  "v_smfmac_f32_16x16x64_bf16 v[52:55], v[212:215], v[192:199], v248\n\t" \
  "ds_read_u16_d16_hi v168, v168 offset:0\n\t" \
  "v_cvt_pk_bf16_f32 v230, v0, v1\n\t" \
  "v_smfmac_f32_16x16x64_bf16 v[76:79], v[212:215], v[200:207], v248\n\t" \
  "s_waitcnt lgkmcnt(9)\n\t" \
  "ds_read_b64_tr_b16 v[212:213], v250 offset:18616\n\t" \
  "v_cvt_pk_bf16_f32 v231, v2, v3\n\t" \
  "v_smfmac_f32_16x16x64_bf16 v[8:11], v[216:219], v[176:183], v248\n\t" \
  "ds_read_u16_d16_hi v169, v169 offset:0\n\t" \
  "v_lshlrev_b32_sdwa v170, 1, v170 dst_sel:DWORD dst_unused:UNUSED_PAD src0_sel:DWORD src1_sel:WORD_1\n\t" \
  "v_cvt_pk_bf16_f32 v232, v4, v5\n\t" \
  "v_smfmac_f32_16x16x64_bf16 v[32:35], v[216:219], v[184:191], v248\n\t" \
  "ds_read_b64_tr_b16 v[214:215], v250 offset:19384\n\t" \
  "v_fma_mix_f32 v152, v152, v225, 0 op_sel_hi:[0,1,0]\n\t" \
  "v_cvt_pk_bf16_f32 v233, v6, v7\n\t" \
  "v_smfmac_f32_16x16x64_bf16 v[56:59], v[216:219], v[192:199], v248\n\t" \
  "buffer_store_dwordx4 v[230:233], v253, %[osrd], %[ob] offen offset:0 sc1\n\t" \
  "v_fma_mix_f32 v153, v153, v224, 0 op_sel_hi:[0,1,0]\n\t" \
  "v_smfmac_f32_16x16x64_bf16 v[80:83], v[216:219], v[200:207], v248\n\t" \
  "s_waitcnt lgkmcnt(8)\n\t" \
  "s_barrier\n\t" \
  "ds_read_b64_tr_b16 v[216:217], v249 offset:6144\n\t" \
  "v_lshlrev_b32_sdwa v171, 1, v171 dst_sel:DWORD dst_unused:UNUSED_PAD src0_sel:DWORD src1_sel:WORD_1\n\t" \
  "v_smfmac_f32_16x16x64_bf16 v[12:15], v[220:223], v[176:183], v248\n\t" \
  "ds_read_u16_d16_hi v170, v170 offset:0\n\t" \
  "v_mul_f32 v156, %[kk], v156\n\t" \
  "v_smfmac_f32_16x16x64_bf16 v[36:39], v[220:223], v[184:191], v248\n\t" \
  "ds_read_b64_tr_b16 v[218:219], v249 offset:6400\n\t" \
  "v_lshlrev_b32_sdwa v158, 1, v158 dst_sel:DWORD dst_unused:UNUSED_PAD src0_sel:DWORD src1_sel:WORD_1\n\t" \
  "v_mul_f32 v157, %[kk], v157\n\t" \
  "v_smfmac_f32_16x16x64_bf16 v[60:63], v[220:223], v[192:199], v248\n\t" \
  "ds_read_u16_d16_hi v171, v171 offset:0\n\t" \
  "v_cvt_pk_f16_f32 v224, v156, v157\n\t" \
  "v_cvt_pk_bf16_f32 v234, v8, v9\n\t" \
  "v_smfmac_f32_16x16x64_bf16 v[84:87], v[220:223], v[200:207], v248\n\t" \
  "s_waitcnt lgkmcnt(8)\n\t" \
  "ds_read_b64_tr_b16 v[220:221], v249 offset:8192\n\t" \
  "v_lshlrev_b32_sdwa v159, 1, v159 dst_sel:DWORD dst_unused:UNUSED_PAD src0_sel:DWORD src1_sel:WORD_1\n\t" \
  "v_cvt_pk_bf16_f32 v235, v10, v11\n\t" \
  "v_smfmac_f32_16x16x64_bf16 v[16:19], v[208:211], v[176:183], v248\n\t" \
  "ds_read_u16_d16_hi v158, v158 offset:0\n\t" \
  "v_pk_fma_f16 v225, v224, v224, %[h1] neg_lo:[1,0,0] neg_hi:[1,0,0]\n\t" \
  "v_cvt_pk_bf16_f32 v236, v12, v13\n\t" \
  "v_smfmac_f32_16x16x64_bf16 v[40:43], v[208:211], v[184:191], v248\n\t" \
  "ds_read_b64_tr_b16 v[222:223], v249 offset:8448\n\t" \
  "v_pk_mul_f16 v225, v224, v225\n\t" \
  "v_cvt_pk_bf16_f32 v237, v14, v15\n\t" \
  "v_smfmac_f32_16x16x64_bf16 v[64:67], v[208:211], v[192:199], v248\n\t" \
  "buffer_store_dwordx4 v[234:237], v253, %[osrd], %[ob] offen offset:64 sc1\n\t" \
  "v_lshlrev_b32_sdwa v164, 1, v164 dst_sel:DWORD dst_unused:UNUSED_PAD src0_sel:DWORD src1_sel:WORD_1\n\t" \
  "v_smfmac_f32_16x16x64_bf16 v[88:91], v[208:211], v[200:207], v248\n\t" \
  "s_waitcnt lgkmcnt(7)\n\t" \
  "ds_read_b64_tr_b16 v[208:209], v249 offset:10240\n\t" \
  "v_exp_f16 v224, v225\n\t" \
  "v_smfmac_f32_16x16x64_bf16 v[20:23], v[212:215], v[176:183], v248\n\t" \
  "ds_read_u16_d16_hi v159, v159 offset:0\n\t" \
  "v_lshlrev_b32_sdwa v165, 1, v165 dst_sel:DWORD dst_unused:UNUSED_PAD src0_sel:DWORD src1_sel:WORD_1\n\t" \
  "v_smfmac_f32_16x16x64_bf16 v[44:47], v[212:215], v[184:191], v248\n\t" \
  "ds_read_b64_tr_b16 v[210:211], v249 offset:10496\n\t" \
  "v_exp_f16_sdwa v224, v225 dst_sel:WORD_1 dst_unused:UNUSED_PRESERVE src0_sel:WORD_1\n\t" \
  "v_smfmac_f32_16x16x64_bf16 v[68:71], v[212:215], v[192:199], v248\n\t" \
  "ds_read_u16_d16_hi v164, v164 offset:0\n\t" \
  "v_cvt_pk_bf16_f32 v238, v16, v17\n\t" \
  "v_smfmac_f32_16x16x64_bf16 v[92:95], v[212:215], v[200:207], v248\n\t" \
  "s_waitcnt lgkmcnt(8)\n\t" \
  "ds_read_b64_tr_b16 v[212:213], v249 offset:6656\n\t" \
  "v_lshlrev_b32_sdwa v166, 1, v166 dst_sel:DWORD dst_unused:UNUSED_PAD src0_sel:DWORD src1_sel:WORD_1\n\t" \
  "v_cvt_pk_bf16_f32 v239, v18, v19\n\t" \
  "v_pk_fma_f16 v224, v224, %[hc], %[hc]\n\t" \
  "v_cvt_pk_bf16_f32 v240, v20, v21\n\t" \
  "v_mfma_f32_16x16x32_bf16 v[184:187], v[216:219], v[108:111], 0\n\t" \
  "ds_read_u16_d16_hi v165, v165 offset:0\n\t" \
  "v_lshlrev_b32_sdwa v167, 1, v167 dst_sel:DWORD dst_unused:UNUSED_PAD src0_sel:DWORD src1_sel:WORD_1\n\t" \
  "v_cvt_pk_bf16_f32 v241, v22, v23\n\t" \
  "v_mfma_f32_16x16x32_bf16 v[192:195], v[216:219], v[120:123], 0\n\t" \
  "ds_read_b64_tr_b16 v[214:215], v249 offset:6912\n\t" \
  "v_rcp_f16 v225, v224\n\t" \
  "v_mfma_f32_16x16x32_bf16 v[200:203], v[216:219], v[132:135], 0\n\t" \
  "s_waitcnt lgkmcnt(7)\n\t" \
  "buffer_store_dwordx4 v[238:241], v253, %[osrd], %[ob] offen offset:128 sc1\n\t" \
  "ds_read_b64_tr_b16 v[216:217], v249 offset:8704\n\t" \
  "v_lshlrev_b32_sdwa v172, 1, v172 dst_sel:DWORD dst_unused:UNUSED_PAD src0_sel:DWORD src1_sel:WORD_1\n\t" \
  "v_mfma_f32_16x16x32_bf16 v[184:187], v[220:223], v[112:115], v[184:187]\n\t" \
  "ds_read_u16_d16_hi v166, v166 offset:0\n\t" \
  "v_rcp_f16_sdwa v224, v224 dst_sel:DWORD dst_unused:UNUSED_PAD src0_sel:WORD_1\n\t" \
  "v_mfma_f32_16x16x32_bf16 v[192:195], v[220:223], v[124:127], v[192:195]\n\t" \
  "ds_read_b64_tr_b16 v[218:219], v249 offset:8960\n\t" \
  "v_lshlrev_b32_sdwa v173, 1, v173 dst_sel:DWORD dst_unused:UNUSED_PAD src0_sel:DWORD src1_sel:WORD_1\n\t" \
  "v_mfma_f32_16x16x32_bf16 v[200:203], v[220:223], v[136:139], v[200:203]\n\t" \
  "s_waitcnt lgkmcnt(7)\n\t" \
  "ds_read_u16_d16_hi v167, v167 offset:0\n\t" \
  "ds_read_b64_tr_b16 v[220:221], v249 offset:10752\n\t" \
  "v_fma_mix_f32 v156, v156, v225, 0 op_sel_hi:[0,1,0]\n\t" \
  "v_mfma_f32_16x16x32_bf16 v[184:187], v[208:211], v[116:119], v[184:187]\n\t" \
  "ds_read_u16_d16_hi v172, v172 offset:0\n\t" \
  "v_lshlrev_b32_sdwa v174, 1, v174 dst_sel:DWORD dst_unused:UNUSED_PAD src0_sel:DWORD src1_sel:WORD_1\n\t" \
  "v_fma_mix_f32 v157, v157, v224, 0 op_sel_hi:[0,1,0]\n\t" \
  "v_mfma_f32_16x16x32_bf16 v[192:195], v[208:211], v[128:131], v[192:195]\n\t" \
  "ds_read_b64_tr_b16 v[222:223], v249 offset:11008\n\t" \
  "v_lshlrev_b32_sdwa v175, 1, v175 dst_sel:DWORD dst_unused:UNUSED_PAD src0_sel:DWORD src1_sel:WORD_1\n\t" \
  "v_mfma_f32_16x16x32_bf16 v[200:203], v[208:211], v[140:143], v[200:203]\n\t" \
  "s_waitcnt lgkmcnt(7)\n\t" \
  "ds_read_u16_d16_hi v173, v173 offset:0\n\t" \
  "ds_read_b64_tr_b16 v[208:209], v250 offset:0\n\t" \
  "v_mfma_f32_16x16x32_bf16 v[188:191], v[212:215], v[108:111], 0\n\t" \
  "ds_read_u16_d16_hi v174, v174 offset:0\n\t" \
  "v_lshlrev_b32_sdwa v186, 1, v186 dst_sel:DWORD dst_unused:UNUSED_PAD src0_sel:DWORD src1_sel:WORD_1\n\t" \
  "v_mul_f32 v184, %[kk], v184\n\t" \
  "v_mfma_f32_16x16x32_bf16 v[196:199], v[212:215], v[120:123], 0\n\t" \
  "ds_read_b64_tr_b16 v[210:211], v250 offset:768\n\t" \
  "v_mul_f32 v185, %[kk], v185\n\t" \
  "v_mfma_f32_16x16x32_bf16 v[204:207], v[212:215], v[132:135], 0\n\t" \
  "s_waitcnt lgkmcnt(8)\n\t" \
  "ds_read_u16_d16_hi v175, v175 offset:0\n\t" \
  "v_lshlrev_b32_sdwa v187, 1, v187 dst_sel:DWORD dst_unused:UNUSED_PAD src0_sel:DWORD src1_sel:WORD_1\n\t" \
  "v_cvt_pk_f16_f32 v224, v184, v185\n\t" \
  "v_mfma_f32_16x16x32_bf16 v[188:191], v[216:219], v[112:115], v[188:191]\n\t" \
  "ds_read_b64_tr_b16 v[212:213], v250 offset:8\n\t" \
  "v_lshlrev_b32_sdwa v192, 1, v192 dst_sel:DWORD dst_unused:UNUSED_PAD src0_sel:DWORD src1_sel:WORD_1\n\t" \
  "v_pk_fma_f16 v225, v224, v224, %[h1] neg_lo:[1,0,0] neg_hi:[1,0,0]\n\t" \
  "v_mfma_f32_16x16x32_bf16 v[196:199], v[216:219], v[124:127], v[196:199]\n\t" \
  "ds_read_u16_d16_hi v186, v186 offset:0\n\t" \
  "v_pk_mul_f16 v225, v224, v225\n\t" \
  "v_mfma_f32_16x16x32_bf16 v[204:207], v[216:219], v[136:139], v[204:207]\n\t" \
  "s_waitcnt lgkmcnt(7)\n\t" \
  "ds_read_b64_tr_b16 v[214:215], v250 offset:776\n\t" \
  "ds_read_u16_d16_hi v187, v187 offset:0\n\t" \
  "v_lshlrev_b32_sdwa v193, 1, v193 dst_sel:DWORD dst_unused:UNUSED_PAD src0_sel:DWORD src1_sel:WORD_1\n\t" \
  "v_exp_f16 v224, v225\n\t" \
  "v_mfma_f32_16x16x32_bf16 v[188:191], v[220:223], v[116:119], v[188:191]\n\t" \
  "ds_read_b64_tr_b16 v[216:217], v250 offset:64\n\t" \
  "v_lshlrev_b32_sdwa v194, 1, v194 dst_sel:DWORD dst_unused:UNUSED_PAD src0_sel:DWORD src1_sel:WORD_1\n\t" \
  "v_mfma_f32_16x16x32_bf16 v[196:199], v[220:223], v[128:131], v[196:199]\n\t" \
  "ds_read_u16_d16_hi v192, v192 offset:0\n\t" \
  "v_exp_f16_sdwa v224, v225 dst_sel:WORD_1 dst_unused:UNUSED_PRESERVE src0_sel:WORD_1\n\t" \
  "v_mfma_f32_16x16x32_bf16 v[204:207], v[220:223], v[140:143], v[204:207]\n\t" \
  "s_waitcnt lgkmcnt(7)\n\t" \
  "ds_read_b64_tr_b16 v[218:219], v250 offset:832\n\t" \
  "ds_read_u16_d16_hi v193, v193 offset:0\n\t" \
  "v_smfmac_f32_16x16x64_bf16 v[24:27], v[208:211], v[152:159], v248\n\t" \
  "ds_read_b64_tr_b16 v[220:221], v250 offset:72\n\t" \
  "v_lshlrev_b32_sdwa v195, 1, v195 dst_sel:DWORD dst_unused:UNUSED_PAD src0_sel:DWORD src1_sel:WORD_1\n\t" \
  "v_pk_fma_f16 v224, v224, %[hc], %[hc]\n\t" \
  "v_smfmac_f32_16x16x64_bf16 v[48:51], v[208:211], v[160:167], v248\n\t" \
  "s_waitcnt lgkmcnt(9)\n\t" \
  "ds_read_u16_d16_hi v194, v194 offset:0\n\t" \
  "v_lshlrev_b32_sdwa v200, 1, v200 dst_sel:DWORD dst_unused:UNUSED_PAD src0_sel:DWORD src1_sel:WORD_1\n\t" \
  "v_smfmac_f32_16x16x64_bf16 v[72:75], v[208:211], v[168:175], v248\n\t" \
  "s_waitcnt lgkmcnt(7)\n\t" \
  "ds_read_b64_tr_b16 v[222:223], v250 offset:840\n\t" \
  "ds_read_u16_d16_hi v195, v195 offset:0\n\t" \
  "v_rcp_f16 v225, v224\n\t" \
  "v_smfmac_f32_16x16x64_bf16 v[28:31], v[212:215], v[152:159], v248\n\t" \
  "s_add_u32 %[ob], %[ob], %[dob]\n\t" \
  "ds_read_b64_tr_b16 v[208:209], v250 offset:128\n\t" \
  "v_lshlrev_b32_sdwa v201, 1, v201 dst_sel:DWORD dst_unused:UNUSED_PAD src0_sel:DWORD src1_sel:WORD_1\n\t" \
  "v_smfmac_f32_16x16x64_bf16 v[52:55], v[212:215], v[160:167], v248\n\t" \
  "ds_read_u16_d16_hi v200, v200 offset:0\n\t" \
  "v_rcp_f16_sdwa v224, v224 dst_sel:DWORD dst_unused:UNUSED_PAD src0_sel:WORD_1\n\t" \
  "v_smfmac_f32_16x16x64_bf16 v[76:79], v[212:215], v[168:175], v248\n\t" \
  "s_waitcnt lgkmcnt(7)\n\t" \
  "ds_read_b64_tr_b16 v[210:211], v250 offset:896\n\t" \
  "v_lshlrev_b32_sdwa v202, 1, v202 dst_sel:DWORD dst_unused:UNUSED_PAD src0_sel:DWORD src1_sel:WORD_1\n\t" \
  "v_smfmac_f32_16x16x64_bf16 v[32:35], v[216:219], v[152:159], v248\n\t" \
  "ds_read_u16_d16_hi v201, v201 offset:0\n\t" \
  "v_fma_mix_f32 v184, v184, v225, 0 op_sel_hi:[0,1,0]\n\t" \
  "v_smfmac_f32_16x16x64_bf16 v[56:59], v[216:219], v[160:167], v248\n\t" \
  "ds_read_b64_tr_b16 v[212:213], v250 offset:136\n\t" \
  "v_fma_mix_f32 v185, v185, v224, 0 op_sel_hi:[0,1,0]\n\t" \
  "v_smfmac_f32_16x16x64_bf16 v[80:83], v[216:219], v[168:175], v248\n\t" \
  "s_waitcnt lgkmcnt(6)\n\t" \
  "ds_read_b64_tr_b16 v[214:215], v250 offset:904\n\t" \
  "s_barrier\n\t" \
  "ds_read_u16_d16_hi v202, v202 offset:0\n\t" \
  "v_lshlrev_b32_sdwa v203, 1, v203 dst_sel:DWORD dst_unused:UNUSED_PAD src0_sel:DWORD src1_sel:WORD_1\n\t" \
  "v_mul_f32 v188, %[kk], v188\n\t" \
  "v_smfmac_f32_16x16x64_bf16 v[36:39], v[220:223], v[152:159], v248\n\t" \
  "ds_read_b64_tr_b16 v[216:217], v249 offset:12288\n\t" \
  "v_lshlrev_b32_sdwa v190, 1, v190 dst_sel:DWORD dst_unused:UNUSED_PAD src0_sel:DWORD src1_sel:WORD_1\n\t" \
  "v_mul_f32 v189, %[kk], v189\n\t" \
  "v_smfmac_f32_16x16x64_bf16 v[60:63], v[220:223], v[160:167], v248\n\t" \
  "ds_read_u16_d16_hi v203, v203 offset:0\n\t" \
  "v_cvt_pk_f16_f32 v224, v188, v189\n\t" \
  "v_smfmac_f32_16x16x64_bf16 v[84:87], v[220:223], v[168:175], v248\n\t" \
  "s_waitcnt lgkmcnt(6)\n\t" \
  "ds_read_b64_tr_b16 v[218:219], v249 offset:12544\n\t" \
  "v_lshlrev_b32_sdwa v191, 1, v191 dst_sel:DWORD dst_unused:UNUSED_PAD src0_sel:DWORD src1_sel:WORD_1\n\t" \
  "v_pk_fma_f16 v225, v224, v224, %[h1] neg_lo:[1,0,0] neg_hi:[1,0,0]\n\t" \
  "v_smfmac_f32_16x16x64_bf16 v[40:43], v[208:211], v[152:159], v248\n\t" \
  "ds_read_u16_d16_hi v190, v190 offset:0\n\t" \
  "v_pk_mul_f16 v225, v224, v225\n\t" \
  "v_smfmac_f32_16x16x64_bf16 v[64:67], v[208:211], v[160:167], v248\n\t" \
  "ds_read_b64_tr_b16 v[220:221], v249 offset:14336\n\t" \
  "v_lshlrev_b32_sdwa v196, 1, v196 dst_sel:DWORD dst_unused:UNUSED_PAD src0_sel:DWORD src1_sel:WORD_1\n\t" \
  "v_smfmac_f32_16x16x64_bf16 v[88:91], v[208:211], v[168:175], v248\n\t" \
  "s_waitcnt lgkmcnt(6)\n\t" \
  "ds_read_b64_tr_b16 v[222:223], v249 offset:14592\n\t" \
  "ds_read_u16_d16_hi v191, v191 offset:0\n\t" \
  "v_exp_f16 v224, v225\n\t" \
  "v_lshlrev_b32_sdwa v197, 1, v197 dst_sel:DWORD dst_unused:UNUSED_PAD src0_sel:DWORD src1_sel:WORD_1\n\t" \
  "v_smfmac_f32_16x16x64_bf16 v[44:47], v[212:215], v[152:159], v248\n\t" \
  "ds_read_b64_tr_b16 v[208:209], v249 offset:16384\n\t" \
  "v_exp_f16_sdwa v224, v225 dst_sel:WORD_1 dst_unused:UNUSED_PRESERVE src0_sel:WORD_1\n\t" \
  "v_smfmac_f32_16x16x64_bf16 v[68:71], v[212:215], v[160:167], v248\n\t" \
  "ds_read_u16_d16_hi v196, v196 offset:0\n\t" \
  "v_lshlrev_b32_sdwa v198, 1, v198 dst_sel:DWORD dst_unused:UNUSED_PAD src0_sel:DWORD src1_sel:WORD_1\n\t" \
  "v_smfmac_f32_16x16x64_bf16 v[92:95], v[212:215], v[168:175], v248\n\t" \
  "s_waitcnt lgkmcnt(6)\n\t" \
  "ds_read_b64_tr_b16 v[210:211], v249 offset:16640\n\t" \
  "ds_read_u16_d16_hi v197, v197 offset:0\n\t" \
  "v_pk_fma_f16 v224, v224, %[hc], %[hc]\n\t" \
  "v_mfma_f32_16x16x32_bf16 v[152:155], v[216:219], v[108:111], 0\n\t" \
  "ds_read_b64_tr_b16 v[212:213], v249 offset:12800\n\t" \
  "v_lshlrev_b32_sdwa v199, 1, v199 dst_sel:DWORD dst_unused:UNUSED_PAD src0_sel:DWORD src1_sel:WORD_1\n\t" \
  "v_mfma_f32_16x16x32_bf16 v[160:163], v[216:219], v[120:123], 0\n\t" \
  "ds_read_u16_d16_hi v198, v198 offset:0\n\t" \
  "v_rcp_f16 v225, v224\n\t" \
  "v_mfma_f32_16x16x32_bf16 v[168:171], v[216:219], v[132:135], 0\n\t" \
  "s_waitcnt lgkmcnt(7)\n\t" \
  "ds_read_b64_tr_b16 v[214:215], v249 offset:13056\n\t" \
  "v_lshlrev_b32_sdwa v204, 1, v204 dst_sel:DWORD dst_unused:UNUSED_PAD src0_sel:DWORD src1_sel:WORD_1\n\t" \
  "v_mfma_f32_16x16x32_bf16 v[152:155], v[220:223], v[112:115], v[152:155]\n\t" \
  "ds_read_u16_d16_hi v199, v199 offset:0\n\t" \
  "v_rcp_f16_sdwa v224, v224 dst_sel:DWORD dst_unused:UNUSED_PAD src0_sel:WORD_1\n\t" \
  "v_mfma_f32_16x16x32_bf16 v[160:163], v[220:223], v[124:127], v[160:163]\n\t" \
  "ds_read_b64_tr_b16 v[216:217], v249 offset:14848\n\t" \
  "v_lshlrev_b32_sdwa v205, 1, v205 dst_sel:DWORD dst_unused:UNUSED_PAD src0_sel:DWORD src1_sel:WORD_1\n\t" \
  "v_mfma_f32_16x16x32_bf16 v[168:171], v[220:223], v[136:139], v[168:171]\n\t" \
  "s_waitcnt lgkmcnt(6)\n\t" \
  "ds_read_b64_tr_b16 v[218:219], v249 offset:15104\n\t" \
  "ds_read_u16_d16_hi v204, v204 offset:0\n\t" \
  "v_fma_mix_f32 v188, v188, v225, 0 op_sel_hi:[0,1,0]\n\t" \
  "v_mfma_f32_16x16x32_bf16 v[152:155], v[208:211], v[116:119], v[152:155]\n\t" \
  "ds_read_b64_tr_b16 v[220:221], v249 offset:16896\n\t" \
  "v_lshlrev_b32_sdwa v206, 1, v206 dst_sel:DWORD dst_unused:UNUSED_PAD src0_sel:DWORD src1_sel:WORD_1\n\t" \
  "v_fma_mix_f32 v189, v189, v224, 0 op_sel_hi:[0,1,0]\n\t" \
  "v_mfma_f32_16x16x32_bf16 v[160:163], v[208:211], v[128:131], v[160:163]\n\t" \
  "ds_read_u16_d16_hi v205, v205 offset:0\n\t" \
  "v_lshlrev_b32_sdwa v207, 1, v207 dst_sel:DWORD dst_unused:UNUSED_PAD src0_sel:DWORD src1_sel:WORD_1\n\t" \
  "v_mfma_f32_16x16x32_bf16 v[168:171], v[208:211], v[140:143], v[168:171]\n\t" \
  "s_waitcnt lgkmcnt(6)\n\t" \
  "ds_read_b64_tr_b16 v[222:223], v249 offset:17152\n\t" \
  "ds_read_u16_d16_hi v206, v206 offset:0\n\t" \
  "v_mfma_f32_16x16x32_bf16 v[156:159], v[212:215], v[108:111], 0\n\t" \
  "ds_read_b64_tr_b16 v[208:209], v250 offset:6160\n\t" \
  "v_lshlrev_b32_sdwa v154, 1, v154 dst_sel:DWORD dst_unused:UNUSED_PAD src0_sel:DWORD src1_sel:WORD_1\n\t" \
  "v_mul_f32 v152, %[kk], v152\n\t" \
  "v_mfma_f32_16x16x32_bf16 v[164:167], v[212:215], v[120:123], 0\n\t" \
  "ds_read_u16_d16_hi v207, v207 offset:0\n\t" \
  "v_mul_f32 v153, %[kk], v153\n\t" \
  "v_mfma_f32_16x16x32_bf16 v[172:175], v[212:215], v[132:135], 0\n\t" \
  "s_waitcnt lgkmcnt(7)\n\t" \
  "ds_read_b64_tr_b16 v[210:211], v250 offset:6928\n\t" \
  "v_lshlrev_b32_sdwa v155, 1, v155 dst_sel:DWORD dst_unused:UNUSED_PAD src0_sel:DWORD src1_sel:WORD_1\n\t" \
  "v_cvt_pk_f16_f32 v224, v152, v153\n\t" \
  "v_mfma_f32_16x16x32_bf16 v[156:159], v[216:219], v[112:115], v[156:159]\n\t" \
  "ds_read_u16_d16_hi v154, v154 offset:0\n\t" \
  "v_lshlrev_b32_sdwa v160, 1, v160 dst_sel:DWORD dst_unused:UNUSED_PAD src0_sel:DWORD src1_sel:WORD_1\n\t" \
  "v_pk_fma_f16 v225, v224, v224, %[h1] neg_lo:[1,0,0] neg_hi:[1,0,0]\n\t" \
  "v_mfma_f32_16x16x32_bf16 v[164:167], v[216:219], v[124:127], v[164:167]\n\t" \
  "ds_read_b64_tr_b16 v[212:213], v250 offset:6168\n\t" \
  "v_pk_mul_f16 v225, v224, v225\n\t" \
  "v_mfma_f32_16x16x32_bf16 v[172:175], v[216:219], v[136:139], v[172:175]\n\t" \
  "s_waitcnt lgkmcnt(6)\n\t" \
  "ds_read_b64_tr_b16 v[214:215], v250 offset:6936\n\t" \
  "ds_read_u16_d16_hi v155, v155 offset:0\n\t" \
  "v_lshlrev_b32_sdwa v161, 1, v161 dst_sel:DWORD dst_unused:UNUSED_PAD src0_sel:DWORD src1_sel:WORD_1\n\t" \
  "v_exp_f16 v224, v225\n\t" \
  "v_mfma_f32_16x16x32_bf16 v[156:159], v[220:223], v[116:119], v[156:159]\n\t" \
  "ds_read_b64_tr_b16 v[216:217], v250 offset:6224\n\t" \
  "v_lshlrev_b32_sdwa v162, 1, v162 dst_sel:DWORD dst_unused:UNUSED_PAD src0_sel:DWORD src1_sel:WORD_1\n\t" \
  "v_mfma_f32_16x16x32_bf16 v[164:167], v[220:223], v[128:131], v[164:167]\n\t" \
  "ds_read_u16_d16_hi v160, v160 offset:0\n\t" \
  "v_exp_f16_sdwa v224, v225 dst_sel:WORD_1 dst_unused:UNUSED_PRESERVE src0_sel:WORD_1\n\t" \
  "v_mfma_f32_16x16x32_bf16 v[172:175], v[220:223], v[140:143], v[172:175]\n\t" \
  "s_waitcnt lgkmcnt(6)\n\t" \
  "ds_read_b64_tr_b16 v[218:219], v250 offset:6992\n\t" \
  "ds_read_u16_d16_hi v161, v161 offset:0\n\t" \
  "v_smfmac_f32_16x16x64_bf16 v[24:27], v[208:211], v[184:191], v248\n\t" \
  "ds_read_b64_tr_b16 v[220:221], v250 offset:6232\n\t" \
  "ds_read_b64_tr_b16 v[222:223], v250 offset:7000\n\t" \
  "v_lshlrev_b32_sdwa v163, 1, v163 dst_sel:DWORD dst_unused:UNUSED_PAD src0_sel:DWORD src1_sel:WORD_1\n\t" \
  "v_pk_fma_f16 v224, v224, %[hc], %[hc]\n\t" \
  "v_smfmac_f32_16x16x64_bf16 v[48:51], v[208:211], v[192:199], v248\n\t" \
  "ds_read_b64_tr_b16 v[0:1], v250 offset:6288\n\t" \
  "ds_read_b64_tr_b16 v[2:3], v250 offset:7056\n\t" \
  "v_smfmac_f32_16x16x64_bf16 v[72:75], v[208:211], v[200:207], v248\n\t" \
  "s_waitcnt lgkmcnt(9)\n\t" \
  "ds_read_b64_tr_b16 v[4:5], v250 offset:6296\n\t" \
  "ds_read_b64_tr_b16 v[6:7], v250 offset:7064\n\t" \
  "v_lshlrev_b32_sdwa v168, 1, v168 dst_sel:DWORD dst_unused:UNUSED_PAD src0_sel:DWORD src1_sel:WORD_1\n\t" \
  "v_rcp_f16 v225, v224\n\t" \
  "v_smfmac_f32_16x16x64_bf16 v[28:31], v[212:215], v[184:191], v248\n\t" \
  "v_add_u32 v249, 57344, v249\n\t" \
  "ds_read_b64_tr_b16 v[8:9], v249 offset:8192\n\t" \
  "ds_read_b64_tr_b16 v[10:11], v249 offset:8448\n\t" \
  "v_smfmac_f32_16x16x64_bf16 v[52:55], v[212:215], v[192:199], v248\n\t" \
  "ds_read_b64_tr_b16 v[12:13], v249 offset:10240\n\t" \
  "ds_read_b64_tr_b16 v[14:15], v249 offset:10496\n\t" \
  "v_lshlrev_b32_sdwa v169, 1, v169 dst_sel:DWORD dst_unused:UNUSED_PAD src0_sel:DWORD src1_sel:WORD_1\n\t" \
  "v_rcp_f16_sdwa v224, v224 dst_sel:DWORD dst_unused:UNUSED_PAD src0_sel:WORD_1\n\t" \
  "v_smfmac_f32_16x16x64_bf16 v[76:79], v[212:215], v[200:207], v248\n\t" \
  "s_waitcnt lgkmcnt(11)\n\t" \
  "ds_read_b64_tr_b16 v[208:209], v249 offset:12288\n\t" \
  "ds_read_b64_tr_b16 v[210:211], v249 offset:12544\n\t" \
  "v_lshlrev_b32_sdwa v170, 1, v170 dst_sel:DWORD dst_unused:UNUSED_PAD src0_sel:DWORD src1_sel:WORD_1\n\t" \
  "v_smfmac_f32_16x16x64_bf16 v[32:35], v[216:219], v[184:191], v248\n\t" \
  "ds_read_u16_d16_hi v162, v162 offset:0\n\t" \
  "ds_read_b64_tr_b16 v[212:213], v249 offset:8704\n\t" \
  "v_fma_mix_f32 v152, v152, v225, 0 op_sel_hi:[0,1,0]\n\t" \
  "v_smfmac_f32_16x16x64_bf16 v[56:59], v[216:219], v[192:199], v248\n\t" \
  "ds_read_u16_d16_hi v163, v163 offset:0\n\t" \
  "ds_read_b64_tr_b16 v[214:215], v249 offset:8960\n\t" \
  "v_fma_mix_f32 v153, v153, v224, 0 op_sel_hi:[0,1,0]\n\t" \
  "v_smfmac_f32_16x16x64_bf16 v[80:83], v[216:219], v[200:207], v248\n\t" \
  "s_waitcnt lgkmcnt(14)\n\t" \
  "ds_read_u16_d16_hi v168, v168 offset:0\n\t" \
  "ds_read_b64_tr_b16 v[216:217], v249 offset:10752\n\t" \
  "v_lshlrev_b32_sdwa v171, 1, v171 dst_sel:DWORD dst_unused:UNUSED_PAD src0_sel:DWORD src1_sel:WORD_1\n\t" \
  "v_mul_f32 v156, %[kk], v156\n\t" \
  "v_smfmac_f32_16x16x64_bf16 v[36:39], v[220:223], v[184:191], v248\n\t" \
  "ds_read_u16_d16_hi v169, v169 offset:0\n\t" \
  "v_lshlrev_b32_sdwa v158, 1, v158 dst_sel:DWORD dst_unused:UNUSED_PAD src0_sel:DWORD src1_sel:WORD_1\n\t" \
  "v_mul_f32 v157, %[kk], v157\n\t" \
  "v_smfmac_f32_16x16x64_bf16 v[60:63], v[220:223], v[192:199], v248\n\t" \
  "ds_read_b64_tr_b16 v[218:219], v249 offset:11008\n\t" \
  "v_cvt_pk_f16_f32 v224, v156, v157\n\t" \
  "v_smfmac_f32_16x16x64_bf16 v[84:87], v[220:223], v[200:207], v248\n\t" \
  "s_waitcnt lgkmcnt(14)\n\t" \
  "ds_read_u16_d16_hi v170, v170 offset:0\n\t" \
  "ds_read_b64_tr_b16 v[220:221], v249 offset:12800\n\t" \
  "v_lshlrev_b32_sdwa v159, 1, v159 dst_sel:DWORD dst_unused:UNUSED_PAD src0_sel:DWORD src1_sel:WORD_1\n\t" \
  "v_pk_fma_f16 v225, v224, v224, %[h1] neg_lo:[1,0,0] neg_hi:[1,0,0]\n\t" \
  "v_smfmac_f32_16x16x64_bf16 v[40:43], v[0:3], v[184:191], v248\n\t" \
  "ds_read_u16_d16_hi v171, v171 offset:0\n\t" \
  "v_pk_mul_f16 v225, v224, v225\n\t" \
  "v_smfmac_f32_16x16x64_bf16 v[64:67], v[0:3], v[192:199], v248\n\t" \
  "ds_read_b64_tr_b16 v[222:223], v249 offset:13056\n\t" \
  "v_lshlrev_b32_sdwa v164, 1, v164 dst_sel:DWORD dst_unused:UNUSED_PAD src0_sel:DWORD src1_sel:WORD_1\n\t" \
  "v_smfmac_f32_16x16x64_bf16 v[88:91], v[0:3], v[200:207], v248\n\t" \
  "ds_read_u16_d16_hi v158, v158 offset:0\n\t" \
  "ds_read_b64_tr_b16 v[0:1], v250 offset:53264\n\t" \
  "v_exp_f16 v224, v225\n\t" \
  "v_lshlrev_b32_sdwa v165, 1, v165 dst_sel:DWORD dst_unused:UNUSED_PAD src0_sel:DWORD src1_sel:WORD_1\n\t" \
  "v_smfmac_f32_16x16x64_bf16 v[44:47], v[4:7], v[184:191], v248\n\t" \
  "ds_read_u16_d16_hi v159, v159 offset:0\n\t" \
  "v_exp_f16_sdwa v224, v225 dst_sel:WORD_1 dst_unused:UNUSED_PRESERVE src0_sel:WORD_1\n\t" \
  "v_smfmac_f32_16x16x64_bf16 v[68:71], v[4:7], v[192:199], v248\n\t" \
  "ds_read_b64_tr_b16 v[2:3], v250 offset:54032\n\t" \
  "v_lshlrev_b32_sdwa v166, 1, v166 dst_sel:DWORD dst_unused:UNUSED_PAD src0_sel:DWORD src1_sel:WORD_1\n\t" \
  "v_smfmac_f32_16x16x64_bf16 v[92:95], v[4:7], v[200:207], v248\n\t" \
  "s_waitcnt lgkmcnt(14)\n\t" \
  "ds_read_u16_d16_hi v164, v164 offset:0\n\t" \
  "ds_read_b64_tr_b16 v[4:5], v250 offset:53272\n\t" \
  "v_pk_fma_f16 v224, v224, %[hc], %[hc]\n\t" \
  "v_lshlrev_b32_sdwa v167, 1, v167 dst_sel:DWORD dst_unused:UNUSED_PAD src0_sel:DWORD src1_sel:WORD_1\n\t" \
  "v_mfma_f32_16x16x32_bf16 v[192:195], v[8:11], v[120:123], 0\n\t" \
  "ds_read_u16_d16_hi v165, v165 offset:0\n\t" \
  "ds_read_b64_tr_b16 v[6:7], v250 offset:54040\n\t" \
  "v_rcp_f16 v225, v224\n\t" \
  "v_mfma_f32_16x16x32_bf16 v[200:203], v[8:11], v[132:135], 0\n\t" \
  "ds_read_u16_d16_hi v166, v166 offset:0\n\t" \
  "ds_read_b64_tr_b16 v[8:9], v250 offset:53328\n\t" \
  "v_lshlrev_b32_sdwa v172, 1, v172 dst_sel:DWORD dst_unused:UNUSED_PAD src0_sel:DWORD src1_sel:WORD_1\n\t" \
  "v_rcp_f16_sdwa v224, v224 dst_sel:DWORD dst_unused:UNUSED_PAD src0_sel:WORD_1\n\t" \
  "v_mfma_f32_16x16x32_bf16 v[192:195], v[12:15], v[124:127], v[192:195]\n\t" \
  "ds_read_u16_d16_hi v167, v167 offset:0\n\t" \
  "ds_read_b64_tr_b16 v[10:11], v250 offset:54096\n\t" \
  "v_lshlrev_b32_sdwa v173, 1, v173 dst_sel:DWORD dst_unused:UNUSED_PAD src0_sel:DWORD src1_sel:WORD_1\n\t" \
  "v_mfma_f32_16x16x32_bf16 v[200:203], v[12:15], v[136:139], v[200:203]\n\t" \
  "ds_read_u16_d16_hi v172, v172 offset:0\n\t" \
  "ds_read_b64_tr_b16 v[12:13], v250 offset:53336\n\t" \
  "v_fma_mix_f32 v156, v156, v225, 0 op_sel_hi:[0,1,0]\n\t" \
  "v_lshlrev_b32_sdwa v174, 1, v174 dst_sel:DWORD dst_unused:UNUSED_PAD src0_sel:DWORD src1_sel:WORD_1\n\t" \
  "v_fma_mix_f32 v157, v157, v224, 0 op_sel_hi:[0,1,0]\n\t" \
  "v_mfma_f32_16x16x32_bf16 v[192:195], v[208:211], v[128:131], v[192:195]\n\t" \
  "ds_read_u16_d16_hi v173, v173 offset:0\n\t" \
  "ds_read_b64_tr_b16 v[14:15], v250 offset:54104\n\t" \
  "v_mfma_f32_16x16x32_bf16 v[200:203], v[208:211], v[140:143], v[200:203]\n\t" \
  "s_waitcnt lgkmcnt(14)\n\t" \
  "ds_read_u16_d16_hi v174, v174 offset:0\n\t" \
  "ds_read_b64_tr_b16 v[208:209], v250 offset:53392\n\t" \
  "v_lshlrev_b32_sdwa v175, 1, v175 dst_sel:DWORD dst_unused:UNUSED_PAD src0_sel:DWORD src1_sel:WORD_1\n\t" \
  "s_nop 0\n\t" \
  "v_lshlrev_b32_sdwa v192, 1, v192 dst_sel:DWORD dst_unused:UNUSED_PAD src0_sel:DWORD src1_sel:WORD_1\n\t" \
  "v_mfma_f32_16x16x32_bf16 v[196:199], v[212:215], v[120:123], 0\n\t" \
  "ds_read_u16_d16_hi v175, v175 offset:0\n\t" \
  "ds_read_b64_tr_b16 v[210:211], v250 offset:54160\n\t" \
  "v_mfma_f32_16x16x32_bf16 v[204:207], v[212:215], v[132:135], 0\n\t" \
  "ds_read_u16_d16_hi v192, v192 offset:0\n\t" \
  "ds_read_b64_tr_b16 v[212:213], v250 offset:53400\n\t" \
  "v_lshlrev_b32_sdwa v193, 1, v193 dst_sel:DWORD dst_unused:UNUSED_PAD src0_sel:DWORD src1_sel:WORD_1\n\t" \
  "v_mfma_f32_16x16x32_bf16 v[196:199], v[216:219], v[124:127], v[196:199]\n\t" \
  "ds_read_u16_d16_hi v193, v193 offset:0\n\t" \
  "ds_read_b64_tr_b16 v[214:215], v250 offset:54168\n\t" \
  "v_mfma_f32_16x16x32_bf16 v[204:207], v[216:219], v[136:139], v[204:207]\n\t" \
  "ds_read_b64_tr_b16 v[216:217], v249 offset:20512\n\t" \
  "ds_read_b64_tr_b16 v[218:219], v249 offset:20768\n\t" \
  "v_lshlrev_b32_sdwa v194, 1, v194 dst_sel:DWORD dst_unused:UNUSED_PAD src0_sel:DWORD src1_sel:WORD_1\n\t" \
  "v_mfma_f32_16x16x32_bf16 v[196:199], v[220:223], v[128:131], v[196:199]\n\t" \
  "ds_read_u16_d16_hi v194, v194 offset:0\n\t" \
  "v_lshlrev_b32_sdwa v195, 1, v195 dst_sel:DWORD dst_unused:UNUSED_PAD src0_sel:DWORD src1_sel:WORD_1\n\t" \
  "v_mfma_f32_16x16x32_bf16 v[204:207], v[220:223], v[140:143], v[204:207]\n\t" \
  "s_waitcnt lgkmcnt(14)\n\t" \
  "ds_read_b64_tr_b16 v[220:221], v249 offset:22560\n\t" \
  "ds_read_u16_d16_hi v195, v195 offset:0\n\t" \
  "v_smfmac_f32_16x16x64_bf16 v[24:27], v[0:3], v[152:159], v248\n\t" \
  "ds_read_b64_tr_b16 v[222:223], v249 offset:22816\n\t" \
  "v_lshlrev_b32_sdwa v200, 1, v200 dst_sel:DWORD dst_unused:UNUSED_PAD src0_sel:DWORD src1_sel:WORD_1\n\t" \
  "v_smfmac_f32_16x16x64_bf16 v[48:51], v[0:3], v[160:167], v248\n\t" \
  "s_waitcnt lgkmcnt(11)\n\t" \
  "ds_read_u16_d16_hi v200, v200 offset:0\n\t" \
  "v_lshlrev_b32_sdwa v201, 1, v201 dst_sel:DWORD dst_unused:UNUSED_PAD src0_sel:DWORD src1_sel:WORD_1\n\t" \
  "v_smfmac_f32_16x16x64_bf16 v[72:75], v[0:3], v[168:175], v248\n\t" \
  "ds_read_b64_tr_b16 v[0:1], v249 offset:24608\n\t" \
  "v_lshlrev_b32_sdwa v202, 1, v202 dst_sel:DWORD dst_unused:UNUSED_PAD src0_sel:DWORD src1_sel:WORD_1\n\t" \
  "v_smfmac_f32_16x16x64_bf16 v[28:31], v[4:7], v[152:159], v248\n\t" \
  "ds_read_u16_d16_hi v201, v201 offset:0\n\t" \
  "v_cvt_pk_bf16_f32 v230, v24, v25\n\t" \
  "v_smfmac_f32_16x16x64_bf16 v[52:55], v[4:7], v[160:167], v248\n\t" \
  "ds_read_b64_tr_b16 v[2:3], v249 offset:24864\n\t" \
  "v_cvt_pk_bf16_f32 v231, v26, v27\n\t" \
  "v_smfmac_f32_16x16x64_bf16 v[76:79], v[4:7], v[168:175], v248\n\t" \
  "ds_read_u16_d16_hi v202, v202 offset:0\n\t" \
  "s_nop 0\n\t" \
  "v_cvt_pk_bf16_f32 v232, v28, v29\n\t" \
  "v_smfmac_f32_16x16x64_bf16 v[32:35], v[8:11], v[152:159], v248\n\t" \
  "ds_read_b64_tr_b16 v[4:5], v249 offset:21024\n\t" \
  "v_lshlrev_b32_sdwa v203, 1, v203 dst_sel:DWORD dst_unused:UNUSED_PAD src0_sel:DWORD src1_sel:WORD_1\n\t" \
  "v_smfmac_f32_16x16x64_bf16 v[56:59], v[8:11], v[160:167], v248\n\t" \
  "ds_read_u16_d16_hi v203, v203 offset:0\n\t" \
  "v_cvt_pk_bf16_f32 v233, v30, v31\n\t" \
  "v_smfmac_f32_16x16x64_bf16 v[80:83], v[8:11], v[168:175], v248\n\t" \
  "ds_read_b64_tr_b16 v[6:7], v249 offset:21280\n\t" \
  "v_lshlrev_b32_sdwa v196, 1, v196 dst_sel:DWORD dst_unused:UNUSED_PAD src0_sel:DWORD src1_sel:WORD_1\n\t" \
  "v_smfmac_f32_16x16x64_bf16 v[36:39], v[12:15], v[152:159], v248\n\t" \
  "buffer_store_dwordx4 v[230:233], v253, %[osrd], %[ob] offen offset:0 sc1\n\t" \
  "v_cvt_pk_bf16_f32 v234, v32, v33\n\t" \
  "v_smfmac_f32_16x16x64_bf16 v[60:63], v[12:15], v[160:167], v248\n\t" \
  "ds_read_b64_tr_b16 v[8:9], v249 offset:23072\n\t" \
  "v_lshlrev_b32_sdwa v197, 1, v197 dst_sel:DWORD dst_unused:UNUSED_PAD src0_sel:DWORD src1_sel:WORD_1\n\t" \
  "v_smfmac_f32_16x16x64_bf16 v[84:87], v[12:15], v[168:175], v248\n\t" \
  "s_waitcnt lgkmcnt(14)\n\t" \
  "ds_read_b64_tr_b16 v[10:11], v249 offset:23328\n\t" \
  "ds_read_u16_d16_hi v196, v196 offset:0\n\t" \
  "v_cvt_pk_bf16_f32 v235, v34, v35\n\t" \
  "v_smfmac_f32_16x16x64_bf16 v[40:43], v[208:211], v[152:159], v248\n\t" \
  "ds_read_b64_tr_b16 v[12:13], v249 offset:25120\n\t" \
  "v_cvt_pk_bf16_f32 v236, v36, v37\n\t" \
  "v_smfmac_f32_16x16x64_bf16 v[64:67], v[208:211], v[160:167], v248\n\t" \
  "ds_read_u16_d16_hi v197, v197 offset:0\n\t" \
  "v_cvt_pk_bf16_f32 v237, v38, v39\n\t" \
  "v_smfmac_f32_16x16x64_bf16 v[88:91], v[208:211], v[168:175], v248\n\t" \
  "ds_read_b64_tr_b16 v[14:15], v249 offset:25376\n\t" \
  "v_lshlrev_b32_sdwa v198, 1, v198 dst_sel:DWORD dst_unused:UNUSED_PAD src0_sel:DWORD src1_sel:WORD_1\n\t" \
  "v_smfmac_f32_16x16x64_bf16 v[44:47], v[212:215], v[152:159], v248\n\t" \
  "buffer_store_dwordx4 v[234:237], v253, %[osrd], %[ob] offen offset:64 sc1\n\t" \
  "v_lshlrev_b32_sdwa v199, 1, v199 dst_sel:DWORD dst_unused:UNUSED_PAD src0_sel:DWORD src1_sel:WORD_1\n\t" \
  "v_smfmac_f32_16x16x64_bf16 v[68:71], v[212:215], v[160:167], v248\n\t" \
  "v_add_u32 v250, 57344, v250\n\t" \
  "ds_read_b64_tr_b16 v[208:209], v250 offset:8240\n\t" \
  "v_cvt_pk_bf16_f32 v238, v40, v41\n\t" \
  "v_smfmac_f32_16x16x64_bf16 v[92:95], v[212:215], v[168:175], v248\n\t" \
  "s_waitcnt lgkmcnt(14)\n\t" \
  "ds_read_b64_tr_b16 v[210:211], v250 offset:9008\n\t" \
  "ds_read_u16_d16_hi v198, v198 offset:0\n\t" \
  "v_cvt_pk_bf16_f32 v239, v42, v43\n\t" \
  "v_lshlrev_b32_sdwa v204, 1, v204 dst_sel:DWORD dst_unused:UNUSED_PAD src0_sel:DWORD src1_sel:WORD_1\n\t" \
  "v_cvt_pk_bf16_f32 v240, v44, v45\n\t" \
  "v_mfma_f32_16x16x32_bf16 v[160:163], v[216:219], v[120:123], 0\n\t" \
  "ds_read_b64_tr_b16 v[212:213], v250 offset:8248\n\t" \
  "ds_read_u16_d16_hi v199, v199 offset:0\n\t" \
  "v_cvt_pk_bf16_f32 v241, v46, v47\n\t" \
  "v_mfma_f32_16x16x32_bf16 v[168:171], v[216:219], v[132:135], 0\n\t" \
  "ds_read_b64_tr_b16 v[214:215], v250 offset:9016\n\t" \
  "buffer_store_dwordx4 v[238:241], v253, %[osrd], %[ob] offen offset:128 sc1\n\t" \
  "v_lshlrev_b32_sdwa v205, 1, v205 dst_sel:DWORD dst_unused:UNUSED_PAD src0_sel:DWORD src1_sel:WORD_1\n\t" \
  "v_mfma_f32_16x16x32_bf16 v[160:163], v[220:223], v[124:127], v[160:163]\n\t" \
  "ds_read_b64_tr_b16 v[216:217], v250 offset:8304\n\t" \
  "ds_read_u16_d16_hi v204, v204 offset:0\n\t" \
  "v_mfma_f32_16x16x32_bf16 v[168:171], v[220:223], v[136:139], v[168:171]\n\t" \
  "s_waitcnt lgkmcnt(14)\n\t" \
  "ds_read_b64_tr_b16 v[218:219], v250 offset:9072\n\t" \
  "ds_read_u16_d16_hi v205, v205 offset:0\n\t" \
  "v_lshlrev_b32_sdwa v206, 1, v206 dst_sel:DWORD dst_unused:UNUSED_PAD src0_sel:DWORD src1_sel:WORD_1\n\t" \
  "v_mfma_f32_16x16x32_bf16 v[160:163], v[0:3], v[128:131], v[160:163]\n\t" \
  "ds_read_b64_tr_b16 v[220:221], v250 offset:8312\n\t" \
  "ds_read_u16_d16_hi v206, v206 offset:0\n\t" \
  "v_lshlrev_b32_sdwa v207, 1, v207 dst_sel:DWORD dst_unused:UNUSED_PAD src0_sel:DWORD src1_sel:WORD_1\n\t" \
  "v_mfma_f32_16x16x32_bf16 v[168:171], v[0:3], v[140:143], v[168:171]\n\t" \
  "ds_read_b64_tr_b16 v[222:223], v250 offset:9080\n\t" \
  "ds_read_u16_d16_hi v207, v207 offset:0\n\t" \
  "s_nop 1\n\t" \
  "v_lshlrev_b32_sdwa v160, 1, v160 dst_sel:DWORD dst_unused:UNUSED_PAD src0_sel:DWORD src1_sel:WORD_1\n\t" \
  "v_mfma_f32_16x16x32_bf16 v[164:167], v[4:7], v[120:123], 0\n\t" \
  "ds_read_b64_tr_b16 v[0:1], v250 offset:8368\n\t" \
  "ds_read_u16_d16_hi v160, v160 offset:0\n\t" \
  "v_mfma_f32_16x16x32_bf16 v[172:175], v[4:7], v[132:135], 0\n\t" \
  "s_waitcnt lgkmcnt(14)\n\t" \
  "ds_read_b64_tr_b16 v[2:3], v250 offset:9136\n\t" \
  "ds_read_b64_tr_b16 v[4:5], v250 offset:8376\n\t" \
  "v_lshlrev_b32_sdwa v161, 1, v161 dst_sel:DWORD dst_unused:UNUSED_PAD src0_sel:DWORD src1_sel:WORD_1\n\t" \
  "v_mfma_f32_16x16x32_bf16 v[164:167], v[8:11], v[124:127], v[164:167]\n\t" \
  "ds_read_u16_d16_hi v161, v161 offset:0\n\t" \
  "ds_read_b64_tr_b16 v[6:7], v250 offset:9144\n\t" \
  "v_mfma_f32_16x16x32_bf16 v[172:175], v[8:11], v[136:139], v[172:175]\n\t" \
  "ds_read_b64_tr_b16 v[8:9], v249 offset:32832\n\t" \
  "ds_read_b64_tr_b16 v[10:11], v249 offset:33088\n\t" \
  "v_lshlrev_b32_sdwa v162, 1, v162 dst_sel:DWORD dst_unused:UNUSED_PAD src0_sel:DWORD src1_sel:WORD_1\n\t" \
  "v_mfma_f32_16x16x32_bf16 v[164:167], v[12:15], v[128:131], v[164:167]\n\t" \
  "ds_read_u16_d16_hi v162, v162 offset:0\n\t" \
  "v_lshlrev_b32_sdwa v163, 1, v163 dst_sel:DWORD dst_unused:UNUSED_PAD src0_sel:DWORD src1_sel:WORD_1\n\t" \
  "v_mfma_f32_16x16x32_bf16 v[172:175], v[12:15], v[140:143], v[172:175]\n\t" \
  "s_waitcnt lgkmcnt(14)\n\t" \
  "ds_read_b64_tr_b16 v[12:13], v249 offset:34880\n\t" \
  "ds_read_u16_d16_hi v163, v163 offset:0\n\t" \
  "v_lshlrev_b32_sdwa v168, 1, v168 dst_sel:DWORD dst_unused:UNUSED_PAD src0_sel:DWORD src1_sel:WORD_1\n\t" \
  "v_smfmac_f32_16x16x64_bf16 v[48:51], v[208:211], v[192:199], v248\n\t" \
  "s_waitcnt lgkmcnt(11)\n\t" \
  "ds_read_b64_tr_b16 v[14:15], v249 offset:35136\n\t" \
  "ds_read_u16_d16_hi v168, v168 offset:0\n\t" \
  "v_smfmac_f32_16x16x64_bf16 v[72:75], v[208:211], v[200:207], v248\n\t" \
  "ds_read_b64_tr_b16 v[208:209], v249 offset:36928\n\t" \
  "ds_read_b64_tr_b16 v[210:211], v249 offset:37184\n\t" \
  "v_lshlrev_b32_sdwa v169, 1, v169 dst_sel:DWORD dst_unused:UNUSED_PAD src0_sel:DWORD src1_sel:WORD_1\n\t" \
  "v_smfmac_f32_16x16x64_bf16 v[52:55], v[212:215], v[192:199], v248\n\t" \
  "s_add_u32 %[ob], %[ob], %[dob]\n\t" \
  "ds_read_u16_d16_hi v169, v169 offset:0\n\t" \
  "v_lshlrev_b32_sdwa v170, 1, v170 dst_sel:DWORD dst_unused:UNUSED_PAD src0_sel:DWORD src1_sel:WORD_1\n\t" \
  "v_smfmac_f32_16x16x64_bf16 v[76:79], v[212:215], v[200:207], v248\n\t" \
  "ds_read_b64_tr_b16 v[212:213], v249 offset:33344\n\t" \
  "ds_read_u16_d16_hi v170, v170 offset:0\n\t" \
  "v_smfmac_f32_16x16x64_bf16 v[56:59], v[216:219], v[192:199], v248\n\t" \
  "ds_read_b64_tr_b16 v[214:215], v249 offset:33600\n\t" \
  "v_lshlrev_b32_sdwa v171, 1, v171 dst_sel:DWORD dst_unused:UNUSED_PAD src0_sel:DWORD src1_sel:WORD_1\n\t" \
  "v_smfmac_f32_16x16x64_bf16 v[80:83], v[216:219], v[200:207], v248\n\t" \
  "ds_read_u16_d16_hi v171, v171 offset:0\n\t" \
  "ds_read_b64_tr_b16 v[216:217], v249 offset:35392\n\t" \
  "v_lshlrev_b32_sdwa v164, 1, v164 dst_sel:DWORD dst_unused:UNUSED_PAD src0_sel:DWORD src1_sel:WORD_1\n\t" \
  "v_smfmac_f32_16x16x64_bf16 v[60:63], v[220:223], v[192:199], v248\n\t" \
  "ds_read_u16_d16_hi v164, v164 offset:0\n\t" \
  "ds_read_b64_tr_b16 v[218:219], v249 offset:35648\n\t" \
  "v_smfmac_f32_16x16x64_bf16 v[84:87], v[220:223], v[200:207], v248\n\t" \
  "s_waitcnt lgkmcnt(14)\n\t" \
  "ds_read_b64_tr_b16 v[220:221], v249 offset:37440\n\t" \
  "ds_read_b64_tr_b16 v[222:223], v249 offset:37696\n\t" \
  "v_lshlrev_b32_sdwa v165, 1, v165 dst_sel:DWORD dst_unused:UNUSED_PAD src0_sel:DWORD src1_sel:WORD_1\n\t" \
  "v_smfmac_f32_16x16x64_bf16 v[64:67], v[0:3], v[192:199], v248\n\t" \
  "ds_read_u16_d16_hi v165, v165 offset:0\n\t" \
  "v_lshlrev_b32_sdwa v166, 1, v166 dst_sel:DWORD dst_unused:UNUSED_PAD src0_sel:DWORD src1_sel:WORD_1\n\t" \
  "v_smfmac_f32_16x16x64_bf16 v[88:91], v[0:3], v[200:207], v248\n\t" \
  "ds_read_b64_tr_b16 v[0:1], v250 offset:20560\n\t" \
  "ds_read_u16_d16_hi v166, v166 offset:0\n\t" \
  "v_smfmac_f32_16x16x64_bf16 v[68:71], v[4:7], v[192:199], v248\n\t" \
  "ds_read_b64_tr_b16 v[2:3], v250 offset:21328\n\t" \
  "v_lshlrev_b32_sdwa v167, 1, v167 dst_sel:DWORD dst_unused:UNUSED_PAD src0_sel:DWORD src1_sel:WORD_1\n\t" \
  "v_smfmac_f32_16x16x64_bf16 v[92:95], v[4:7], v[200:207], v248\n\t" \
  "ds_read_u16_d16_hi v167, v167 offset:0\n\t" \
  "ds_read_b64_tr_b16 v[4:5], v250 offset:20568\n\t" \
  "v_lshlrev_b32_sdwa v172, 1, v172 dst_sel:DWORD dst_unused:UNUSED_PAD src0_sel:DWORD src1_sel:WORD_1\n\t" \
  "v_mfma_f32_16x16x32_bf16 v[192:195], v[8:11], v[120:123], 0\n\t" \
  "ds_read_u16_d16_hi v172, v172 offset:0\n\t" \
  "ds_read_b64_tr_b16 v[6:7], v250 offset:21336\n\t" \
  "v_mfma_f32_16x16x32_bf16 v[200:203], v[8:11], v[132:135], 0\n\t" \
  "s_waitcnt lgkmcnt(14)\n\t" \
  "ds_read_b64_tr_b16 v[8:9], v250 offset:20624\n\t" \
  "ds_read_b64_tr_b16 v[10:11], v250 offset:21392\n\t" \
  "v_lshlrev_b32_sdwa v173, 1, v173 dst_sel:DWORD dst_unused:UNUSED_PAD src0_sel:DWORD src1_sel:WORD_1\n\t" \
  "v_mfma_f32_16x16x32_bf16 v[192:195], v[12:15], v[124:127], v[192:195]\n\t" \
  "ds_read_u16_d16_hi v173, v173 offset:0\n\t" \
  "v_lshlrev_b32_sdwa v174, 1, v174 dst_sel:DWORD dst_unused:UNUSED_PAD src0_sel:DWORD src1_sel:WORD_1\n\t" \
  "v_mfma_f32_16x16x32_bf16 v[200:203], v[12:15], v[136:139], v[200:203]\n\t" \
  "ds_read_b64_tr_b16 v[12:13], v250 offset:20632\n\t" \
  "ds_read_u16_d16_hi v174, v174 offset:0\n\t" \
  "v_mfma_f32_16x16x32_bf16 v[192:195], v[208:211], v[128:131], v[192:195]\n\t" \
  "ds_read_b64_tr_b16 v[14:15], v250 offset:21400\n\t" \
  "v_lshlrev_b32_sdwa v175, 1, v175 dst_sel:DWORD dst_unused:UNUSED_PAD src0_sel:DWORD src1_sel:WORD_1\n\t" \
  "v_mfma_f32_16x16x32_bf16 v[200:203], v[208:211], v[140:143], v[200:203]\n\t" \
  "ds_read_u16_d16_hi v175, v175 offset:0\n\t" \
  "ds_read_b64_tr_b16 v[208:209], v250 offset:20688\n\t" \
  "s_nop 2\n\t" \
  "v_lshlrev_b32_sdwa v192, 1, v192 dst_sel:DWORD dst_unused:UNUSED_PAD src0_sel:DWORD src1_sel:WORD_1\n\t" \
  "v_mfma_f32_16x16x32_bf16 v[196:199], v[212:215], v[120:123], 0\n\t" \
  "ds_read_u16_d16_hi v192, v192 offset:0\n\t" \
  "ds_read_b64_tr_b16 v[210:211], v250 offset:21456\n\t" \
  "v_mfma_f32_16x16x32_bf16 v[204:207], v[212:215], v[132:135], 0\n\t" \
  "s_waitcnt lgkmcnt(14)\n\t" \
  "ds_read_b64_tr_b16 v[212:213], v250 offset:20696\n\t" \
  "ds_read_b64_tr_b16 v[214:215], v250 offset:21464\n\t" \
  "v_lshlrev_b32_sdwa v193, 1, v193 dst_sel:DWORD dst_unused:UNUSED_PAD src0_sel:DWORD src1_sel:WORD_1\n\t" \
  "v_mfma_f32_16x16x32_bf16 v[196:199], v[216:219], v[124:127], v[196:199]\n\t" \
  "ds_read_u16_d16_hi v193, v193 offset:0\n\t" \
  "v_lshlrev_b32_sdwa v194, 1, v194 dst_sel:DWORD dst_unused:UNUSED_PAD src0_sel:DWORD src1_sel:WORD_1\n\t" \
  "v_mfma_f32_16x16x32_bf16 v[204:207], v[216:219], v[136:139], v[204:207]\n\t" \
  "ds_read_b64_tr_b16 v[216:217], v249 offset:45152\n\t" \
  "ds_read_u16_d16_hi v194, v194 offset:0\n\t" \
  "v_mfma_f32_16x16x32_bf16 v[196:199], v[220:223], v[128:131], v[196:199]\n\t" \
  "ds_read_b64_tr_b16 v[218:219], v249 offset:45408\n\t" \
  "v_lshlrev_b32_sdwa v195, 1, v195 dst_sel:DWORD dst_unused:UNUSED_PAD src0_sel:DWORD src1_sel:WORD_1\n\t" \
  "v_mfma_f32_16x16x32_bf16 v[204:207], v[220:223], v[140:143], v[204:207]\n\t" \
  "s_waitcnt lgkmcnt(14)\n\t" \
  "ds_read_u16_d16_hi v195, v195 offset:0\n\t" \
  "ds_read_b64_tr_b16 v[220:221], v249 offset:47200\n\t" \
  "v_lshlrev_b32_sdwa v200, 1, v200 dst_sel:DWORD dst_unused:UNUSED_PAD src0_sel:DWORD src1_sel:WORD_1\n\t" \
  "v_smfmac_f32_16x16x64_bf16 v[48:51], v[0:3], v[160:167], v248\n\t" \
  "s_waitcnt lgkmcnt(11)\n\t" \
  "ds_read_u16_d16_hi v200, v200 offset:0\n\t" \
  "ds_read_b64_tr_b16 v[222:223], v249 offset:47456\n\t" \
  "v_smfmac_f32_16x16x64_bf16 v[72:75], v[0:3], v[168:175], v248\n\t" \
  "ds_read_b64_tr_b16 v[0:1], v249 offset:49248\n\t" \
  "ds_read_b64_tr_b16 v[2:3], v249 offset:49504\n\t" \
  "v_lshlrev_b32_sdwa v201, 1, v201 dst_sel:DWORD dst_unused:UNUSED_PAD src0_sel:DWORD src1_sel:WORD_1\n\t" \
  "v_smfmac_f32_16x16x64_bf16 v[52:55], v[4:7], v[160:167], v248\n\t" \
  "ds_read_u16_d16_hi v201, v201 offset:0\n\t" \
  "v_lshlrev_b32_sdwa v202, 1, v202 dst_sel:DWORD dst_unused:UNUSED_PAD src0_sel:DWORD src1_sel:WORD_1\n\t" \
  "v_smfmac_f32_16x16x64_bf16 v[76:79], v[4:7], v[168:175], v248\n\t" \
  "ds_read_b64_tr_b16 v[4:5], v249 offset:45664\n\t" \
  "ds_read_u16_d16_hi v202, v202 offset:0\n\t" \
  "v_smfmac_f32_16x16x64_bf16 v[56:59], v[8:11], v[160:167], v248\n\t" \
  "ds_read_b64_tr_b16 v[6:7], v249 offset:45920\n\t" \
  "v_lshlrev_b32_sdwa v203, 1, v203 dst_sel:DWORD dst_unused:UNUSED_PAD src0_sel:DWORD src1_sel:WORD_1\n\t" \
  "v_smfmac_f32_16x16x64_bf16 v[80:83], v[8:11], v[168:175], v248\n\t" \
  "ds_read_u16_d16_hi v203, v203 offset:0\n\t" \
  "ds_read_b64_tr_b16 v[8:9], v249 offset:47712\n\t" \
  "v_lshlrev_b32_sdwa v196, 1, v196 dst_sel:DWORD dst_unused:UNUSED_PAD src0_sel:DWORD src1_sel:WORD_1\n\t" \
  "v_smfmac_f32_16x16x64_bf16 v[60:63], v[12:15], v[160:167], v248\n\t" \
  "ds_read_u16_d16_hi v196, v196 offset:0\n\t" \
  "ds_read_b64_tr_b16 v[10:11], v249 offset:47968\n\t" \
  "v_smfmac_f32_16x16x64_bf16 v[84:87], v[12:15], v[168:175], v248\n\t" \
  "s_waitcnt lgkmcnt(14)\n\t" \
  "ds_read_b64_tr_b16 v[12:13], v249 offset:49760\n\t" \
  "ds_read_b64_tr_b16 v[14:15], v249 offset:50016\n\t" \
  "v_lshlrev_b32_sdwa v197, 1, v197 dst_sel:DWORD dst_unused:UNUSED_PAD src0_sel:DWORD src1_sel:WORD_1\n\t" \
  "v_smfmac_f32_16x16x64_bf16 v[64:67], v[208:211], v[160:167], v248\n\t" \
  "ds_read_u16_d16_hi v197, v197 offset:0\n\t" \
  "v_lshlrev_b32_sdwa v198, 1, v198 dst_sel:DWORD dst_unused:UNUSED_PAD src0_sel:DWORD src1_sel:WORD_1\n\t" \
  "v_smfmac_f32_16x16x64_bf16 v[88:91], v[208:211], v[168:175], v248\n\t" \
  "ds_read_b64_tr_b16 v[208:209], v250 offset:32880\n\t" \
  "ds_read_u16_d16_hi v198, v198 offset:0\n\t" \
  "v_smfmac_f32_16x16x64_bf16 v[68:71], v[212:215], v[160:167], v248\n\t" \
  "ds_read_b64_tr_b16 v[210:211], v250 offset:33648\n\t" \
  "v_lshlrev_b32_sdwa v199, 1, v199 dst_sel:DWORD dst_unused:UNUSED_PAD src0_sel:DWORD src1_sel:WORD_1\n\t" \
  "v_smfmac_f32_16x16x64_bf16 v[92:95], v[212:215], v[168:175], v248\n\t" \
  "ds_read_u16_d16_hi v199, v199 offset:0\n\t" \
  "ds_read_b64_tr_b16 v[212:213], v250 offset:32888\n\t" \
  "v_lshlrev_b32_sdwa v204, 1, v204 dst_sel:DWORD dst_unused:UNUSED_PAD src0_sel:DWORD src1_sel:WORD_1\n\t" \
  "v_mfma_f32_16x16x32_bf16 v[168:171], v[216:219], v[132:135], 0\n\t" \
  "s_waitcnt lgkmcnt(14)\n\t" \
  "ds_read_b64_tr_b16 v[214:215], v250 offset:33656\n\t" \
  "ds_read_u16_d16_hi v204, v204 offset:0\n\t" \
  "ds_read_b64_tr_b16 v[216:217], v250 offset:32944\n\t" \
  "v_lshlrev_b32_sdwa v205, 1, v205 dst_sel:DWORD dst_unused:UNUSED_PAD src0_sel:DWORD src1_sel:WORD_1\n\t" \
  "v_mfma_f32_16x16x32_bf16 v[168:171], v[220:223], v[136:139], v[168:171]\n\t" \
  "ds_read_b64_tr_b16 v[218:219], v250 offset:33712\n\t" \
  "ds_read_u16_d16_hi v205, v205 offset:0\n\t" \
  "ds_read_b64_tr_b16 v[220:221], v250 offset:32952\n\t" \
  "v_lshlrev_b32_sdwa v206, 1, v206 dst_sel:DWORD dst_unused:UNUSED_PAD src0_sel:DWORD src1_sel:WORD_1\n\t" \
  "v_lshlrev_b32_sdwa v207, 1, v207 dst_sel:DWORD dst_unused:UNUSED_PAD src0_sel:DWORD src1_sel:WORD_1\n\t" \
  "v_mfma_f32_16x16x32_bf16 v[168:171], v[0:3], v[140:143], v[168:171]\n\t" \
  "s_waitcnt lgkmcnt(14)\n\t" \
  "ds_read_b64_tr_b16 v[222:223], v250 offset:33720\n\t" \
  "ds_read_u16_d16_hi v206, v206 offset:0\n\t" \
  "ds_read_b64_tr_b16 v[0:1], v250 offset:33008\n\t" \
  "s_nop 3\n\t" \
  "v_lshlrev_b32_sdwa v168, 1, v168 dst_sel:DWORD dst_unused:UNUSED_PAD src0_sel:DWORD src1_sel:WORD_1\n\t" \
  "v_mfma_f32_16x16x32_bf16 v[172:175], v[4:7], v[132:135], 0\n\t" \
  "ds_read_b64_tr_b16 v[2:3], v250 offset:33776\n\t" \
  "ds_read_u16_d16_hi v207, v207 offset:0\n\t" \
  "ds_read_b64_tr_b16 v[4:5], v250 offset:33016\n\t" \
  "v_mfma_f32_16x16x32_bf16 v[172:175], v[8:11], v[136:139], v[172:175]\n\t" \
  "s_waitcnt lgkmcnt(14)\n\t" \
  "ds_read_b64_tr_b16 v[6:7], v250 offset:33784\n\t" \
  "ds_read_u16_d16_hi v168, v168 offset:0\n\t" \
  "ds_read_b64_tr_b16 v[8:9], v249 offset:57472\n\t" \
  "v_lshlrev_b32_sdwa v169, 1, v169 dst_sel:DWORD dst_unused:UNUSED_PAD src0_sel:DWORD src1_sel:WORD_1\n\t" \
  "v_mfma_f32_16x16x32_bf16 v[172:175], v[12:15], v[140:143], v[172:175]\n\t" \
  "s_waitcnt lgkmcnt(14)\n\t" \
  "ds_read_b64_tr_b16 v[10:11], v249 offset:57728\n\t" \
  "ds_read_u16_d16_hi v169, v169 offset:0\n\t" \
  "ds_read_b64_tr_b16 v[12:13], v249 offset:59520\n\t" \
  "v_lshlrev_b32_sdwa v170, 1, v170 dst_sel:DWORD dst_unused:UNUSED_PAD src0_sel:DWORD src1_sel:WORD_1\n\t" \
  "v_smfmac_f32_16x16x64_bf16 v[48:51], v[208:211], v[192:199], v248\n\t" \
  "s_waitcnt lgkmcnt(7)\n\t" \
  "ds_read_u16_d16_hi v170, v170 offset:0\n\t" \
  "ds_read_b64_tr_b16 v[14:15], v249 offset:59776\n\t" \
  "v_smfmac_f32_16x16x64_bf16 v[72:75], v[208:211], v[200:207], v248\n\t" \
  "ds_read_b64_tr_b16 v[208:209], v249 offset:61568\n\t" \
  "ds_read_b64_tr_b16 v[210:211], v249 offset:61824\n\t" \
  "v_smfmac_f32_16x16x64_bf16 v[52:55], v[212:215], v[192:199], v248\n\t" \
  "v_lshlrev_b32_sdwa v171, 1, v171 dst_sel:DWORD dst_unused:UNUSED_PAD src0_sel:DWORD src1_sel:WORD_1\n\t" \
  "v_cvt_pk_bf16_f32 v230, v48, v49\n\t" \
  "v_smfmac_f32_16x16x64_bf16 v[76:79], v[212:215], v[200:207], v248\n\t" \
  "ds_read_u16_d16_hi v171, v171 offset:0\n\t" \
  "ds_read_b64_tr_b16 v[212:213], v249 offset:57984\n\t" \
  "v_cvt_pk_bf16_f32 v231, v50, v51\n\t" \
  "v_smfmac_f32_16x16x64_bf16 v[56:59], v[216:219], v[192:199], v248\n\t" \
  "ds_read_b64_tr_b16 v[214:215], v249 offset:58240\n\t" \
  "v_cvt_pk_bf16_f32 v232, v52, v53\n\t" \
  "v_smfmac_f32_16x16x64_bf16 v[80:83], v[216:219], v[200:207], v248\n\t" \
  "ds_read_b64_tr_b16 v[216:217], v249 offset:60032\n\t" \
  "ds_read_b64_tr_b16 v[218:219], v249 offset:60288\n\t" \
  "v_cvt_pk_bf16_f32 v233, v54, v55\n\t" \
  "v_lshlrev_b32_sdwa v172, 1, v172 dst_sel:DWORD dst_unused:UNUSED_PAD src0_sel:DWORD src1_sel:WORD_1\n\t" \
  "v_smfmac_f32_16x16x64_bf16 v[60:63], v[220:223], v[192:199], v248\n\t" \
  "buffer_store_dwordx4 v[230:233], v253, %[osrd], %[ob] offen offset:0 sc1\n\t" \
  "ds_read_u16_d16_hi v172, v172 offset:0\n\t" \
  "v_smfmac_f32_16x16x64_bf16 v[84:87], v[220:223], v[200:207], v248\n\t" \
  "ds_read_b64_tr_b16 v[220:221], v249 offset:62080\n\t" \
  "ds_read_b64_tr_b16 v[222:223], v249 offset:62336\n\t" \
  "v_cvt_pk_bf16_f32 v234, v56, v57\n\t" \
  "v_cvt_pk_bf16_f32 v235, v58, v59\n\t" \
  "v_smfmac_f32_16x16x64_bf16 v[64:67], v[0:3], v[192:199], v248\n\t" \
  "v_cvt_pk_bf16_f32 v236, v60, v61\n\t" \
  "v_lshlrev_b32_sdwa v173, 1, v173 dst_sel:DWORD dst_unused:UNUSED_PAD src0_sel:DWORD src1_sel:WORD_1\n\t" \
  "v_smfmac_f32_16x16x64_bf16 v[88:91], v[0:3], v[200:207], v248\n\t" \
  "s_waitcnt lgkmcnt(14)\n\t" \
  "ds_read_u16_d16_hi v173, v173 offset:0\n\t" \
  "ds_read_b64_tr_b16 v[0:1], v250 offset:45200\n\t" \
  "v_cvt_pk_bf16_f32 v237, v62, v63\n\t" \
  "v_smfmac_f32_16x16x64_bf16 v[68:71], v[4:7], v[192:199], v248\n\t" \
  "buffer_store_dwordx4 v[234:237], v253, %[osrd], %[ob] offen offset:64 sc1\n\t" \
  "ds_read_b64_tr_b16 v[2:3], v250 offset:45968\n\t" \
  "v_smfmac_f32_16x16x64_bf16 v[92:95], v[4:7], v[200:207], v248\n\t" \
  "ds_read_b64_tr_b16 v[4:5], v250 offset:45208\n\t" \
  "ds_read_b64_tr_b16 v[6:7], v250 offset:45976\n\t" \
  "v_cvt_pk_bf16_f32 v238, v64, v65\n\t" \
  "v_lshlrev_b32_sdwa v174, 1, v174 dst_sel:DWORD dst_unused:UNUSED_PAD src0_sel:DWORD src1_sel:WORD_1\n\t" \
  "v_cvt_pk_bf16_f32 v239, v66, v67\n\t" \
  "v_cvt_pk_bf16_f32 v240, v68, v69\n\t" \
  "v_mfma_f32_16x16x32_bf16 v[200:203], v[8:11], v[132:135], 0\n\t" \
  "s_waitcnt lgkmcnt(14)\n\t" \
  "ds_read_u16_d16_hi v174, v174 offset:0\n\t" \
  "ds_read_b64_tr_b16 v[8:9], v250 offset:45264\n\t" \
  "ds_read_b64_tr_b16 v[10:11], v250 offset:46032\n\t" \
  "v_cvt_pk_bf16_f32 v241, v70, v71\n\t" \
  "v_mfma_f32_16x16x32_bf16 v[200:203], v[12:15], v[136:139], v[200:203]\n\t" \
  "s_waitcnt lgkmcnt(14)\n\t" \
  "buffer_store_dwordx4 v[238:241], v253, %[osrd], %[ob] offen offset:128 sc1\n\t" \
  "ds_read_b64_tr_b16 v[12:13], v250 offset:45272\n\t" \
  "ds_read_b64_tr_b16 v[14:15], v250 offset:46040\n\t" \
  "v_lshlrev_b32_sdwa v175, 1, v175 dst_sel:DWORD dst_unused:UNUSED_PAD src0_sel:DWORD src1_sel:WORD_1\n\t" \
  "v_mfma_f32_16x16x32_bf16 v[200:203], v[208:211], v[140:143], v[200:203]\n\t" \
  "s_waitcnt lgkmcnt(14)\n\t" \
  "ds_read_u16_d16_hi v175, v175 offset:0\n\t" \
  "ds_read_b64_tr_b16 v[208:209], v250 offset:45328\n\t" \
  "ds_read_b64_tr_b16 v[210:211], v250 offset:46096\n\t" \
  "s_nop 3\n\t" \
  "v_lshlrev_b32_sdwa v200, 1, v200 dst_sel:DWORD dst_unused:UNUSED_PAD src0_sel:DWORD src1_sel:WORD_1\n\t" \
  "v_mfma_f32_16x16x32_bf16 v[204:207], v[212:215], v[132:135], 0\n\t" \
  "s_waitcnt lgkmcnt(14)\n\t" \
  "ds_read_u16_d16_hi v200, v200 offset:0\n\t" \
  "ds_read_b64_tr_b16 v[212:213], v250 offset:45336\n\t" \
  "ds_read_b64_tr_b16 v[214:215], v250 offset:46104\n\t" \
  "v_mfma_f32_16x16x32_bf16 v[204:207], v[216:219], v[136:139], v[204:207]\n\t" \
  "s_waitcnt lgkmcnt(14)\n\t" \
  "v_add_u32 v249, 61440, v249\n\t" \
  "ds_read_b64_tr_b16 v[216:217], v249 offset:8352\n\t" \
  "ds_read_b64_tr_b16 v[218:219], v249 offset:8608\n\t" \
  "v_lshlrev_b32_sdwa v201, 1, v201 dst_sel:DWORD dst_unused:UNUSED_PAD src0_sel:DWORD src1_sel:WORD_1\n\t" \
  "v_mfma_f32_16x16x32_bf16 v[204:207], v[220:223], v[140:143], v[204:207]\n\t" \
  "s_waitcnt lgkmcnt(7)\n\t" \
  "ds_read_u16_d16_hi v201, v201 offset:0\n\t" \
  "ds_read_b64_tr_b16 v[220:221], v249 offset:10400\n\t" \
  "ds_read_b64_tr_b16 v[222:223], v249 offset:10656\n\t" \
  "v_lshlrev_b32_sdwa v202, 1, v202 dst_sel:DWORD dst_unused:UNUSED_PAD src0_sel:DWORD src1_sel:WORD_1\n\t" \
  "v_smfmac_f32_16x16x64_bf16 v[72:75], v[0:3], v[168:175], v248\n\t" \
  "ds_read_u16_d16_hi v202, v202 offset:0\n\t" \
  "ds_read_b64_tr_b16 v[0:1], v249 offset:12448\n\t" \
  "ds_read_b64_tr_b16 v[2:3], v249 offset:12704\n\t" \
  "v_smfmac_f32_16x16x64_bf16 v[76:79], v[4:7], v[168:175], v248\n\t" \
  "s_add_u32 %[ob], %[ob], %[dob]\n\t" \
  "ds_read_b64_tr_b16 v[4:5], v249 offset:8864\n\t" \
  "ds_read_b64_tr_b16 v[6:7], v249 offset:9120\n\t" \
  "v_lshlrev_b32_sdwa v203, 1, v203 dst_sel:DWORD dst_unused:UNUSED_PAD src0_sel:DWORD src1_sel:WORD_1\n\t" \
  "v_smfmac_f32_16x16x64_bf16 v[80:83], v[8:11], v[168:175], v248\n\t" \
  "ds_read_u16_d16_hi v203, v203 offset:0\n\t" \
  "ds_read_b64_tr_b16 v[8:9], v249 offset:10912\n\t" \
  "ds_read_b64_tr_b16 v[10:11], v249 offset:11168\n\t" \
  "v_lshlrev_b32_sdwa v204, 1, v204 dst_sel:DWORD dst_unused:UNUSED_PAD src0_sel:DWORD src1_sel:WORD_1\n\t" \
  "v_smfmac_f32_16x16x64_bf16 v[84:87], v[12:15], v[168:175], v248\n\t" \
  "s_waitcnt lgkmcnt(14)\n\t" \
  "ds_read_u16_d16_hi v204, v204 offset:0\n\t" \
  "ds_read_b64_tr_b16 v[12:13], v249 offset:12960\n\t" \
  "ds_read_b64_tr_b16 v[14:15], v249 offset:13216\n\t" \
  "v_smfmac_f32_16x16x64_bf16 v[88:91], v[208:211], v[168:175], v248\n\t" \
  "s_waitcnt lgkmcnt(14)\n\t" \
  "ds_read_b64_tr_b16 v[208:209], v250 offset:57520\n\t" \
  "ds_read_b64_tr_b16 v[210:211], v250 offset:58288\n\t" \
  "v_lshlrev_b32_sdwa v205, 1, v205 dst_sel:DWORD dst_unused:UNUSED_PAD src0_sel:DWORD src1_sel:WORD_1\n\t" \
  "v_smfmac_f32_16x16x64_bf16 v[92:95], v[212:215], v[168:175], v248\n\t" \
  "ds_read_u16_d16_hi v205, v205 offset:0\n\t" \
  "ds_read_b64_tr_b16 v[212:213], v250 offset:57528\n\t" \
  "ds_read_b64_tr_b16 v[214:215], v250 offset:58296\n\t" \
  "v_lshlrev_b32_sdwa v206, 1, v206 dst_sel:DWORD dst_unused:UNUSED_PAD src0_sel:DWORD src1_sel:WORD_1\n\t" \
  "v_mfma_f32_16x16x32_bf16 v[168:171], v[216:219], v[132:135], 0\n\t" \
  "s_waitcnt lgkmcnt(14)\n\t" \
  "ds_read_u16_d16_hi v206, v206 offset:0\n\t" \
  "ds_read_b64_tr_b16 v[216:217], v250 offset:57584\n\t" \
  "ds_read_b64_tr_b16 v[218:219], v250 offset:58352\n\t" \
  "v_mfma_f32_16x16x32_bf16 v[168:171], v[220:223], v[136:139], v[168:171]\n\t" \
  "s_waitcnt lgkmcnt(14)\n\t" \
  "ds_read_b64_tr_b16 v[220:221], v250 offset:57592\n\t" \
  "ds_read_b64_tr_b16 v[222:223], v250 offset:58360\n\t" \
  "v_lshlrev_b32_sdwa v207, 1, v207 dst_sel:DWORD dst_unused:UNUSED_PAD src0_sel:DWORD src1_sel:WORD_1\n\t" \
  "v_mfma_f32_16x16x32_bf16 v[168:171], v[0:3], v[140:143], v[168:171]\n\t" \
  "ds_read_u16_d16_hi v207, v207 offset:0\n\t" \
  "ds_read_b64_tr_b16 v[0:1], v250 offset:57648\n\t" \
  "ds_read_b64_tr_b16 v[2:3], v250 offset:58416\n\t" \
  "s_nop 4\n\t" \
  "v_lshlrev_b32_sdwa v168, 1, v168 dst_sel:DWORD dst_unused:UNUSED_PAD src0_sel:DWORD src1_sel:WORD_1\n\t" \
  "v_mfma_f32_16x16x32_bf16 v[172:175], v[4:7], v[132:135], 0\n\t" \
  "s_waitcnt lgkmcnt(14)\n\t" \
  "ds_read_u16_d16_hi v168, v168 offset:0\n\t" \
  "ds_read_b64_tr_b16 v[4:5], v250 offset:57656\n\t" \
  "ds_read_b64_tr_b16 v[6:7], v250 offset:58424\n\t" \
  "v_mfma_f32_16x16x32_bf16 v[172:175], v[8:11], v[136:139], v[172:175]\n\t" \
  "s_waitcnt lgkmcnt(14)\n\t" \
  "v_add_u32 v250, 61440, v250\n\t" \
  "ds_read_b64_tr_b16 v[8:9], v250 offset:8400\n\t" \
  "ds_read_b64_tr_b16 v[10:11], v250 offset:9168\n\t" \
  "v_lshlrev_b32_sdwa v169, 1, v169 dst_sel:DWORD dst_unused:UNUSED_PAD src0_sel:DWORD src1_sel:WORD_1\n\t" \
  "v_mfma_f32_16x16x32_bf16 v[172:175], v[12:15], v[140:143], v[172:175]\n\t" \
  "s_waitcnt lgkmcnt(7)\n\t" \
  "ds_read_u16_d16_hi v169, v169 offset:0\n\t" \
  "ds_read_b64_tr_b16 v[12:13], v250 offset:8408\n\t" \
  "ds_read_b64_tr_b16 v[14:15], v250 offset:9176\n\t" \
  "v_lshlrev_b32_sdwa v170, 1, v170 dst_sel:DWORD dst_unused:UNUSED_PAD src0_sel:DWORD src1_sel:WORD_1\n\t" \
  "v_smfmac_f32_16x16x64_bf16 v[72:75], v[208:211], v[200:207], v248\n\t" \
  "ds_read_u16_d16_hi v170, v170 offset:0\n\t" \
  "ds_read_b64_tr_b16 v[208:209], v250 offset:8464\n\t" \
  "ds_read_b64_tr_b16 v[210:211], v250 offset:9232\n\t" \
  "v_smfmac_f32_16x16x64_bf16 v[76:79], v[212:215], v[200:207], v248\n\t" \
  "ds_read_b64_tr_b16 v[212:213], v250 offset:8472\n\t" \
  "ds_read_b64_tr_b16 v[214:215], v250 offset:9240\n\t" \
  "v_lshlrev_b32_sdwa v171, 1, v171 dst_sel:DWORD dst_unused:UNUSED_PAD src0_sel:DWORD src1_sel:WORD_1\n\t" \
  "v_smfmac_f32_16x16x64_bf16 v[80:83], v[216:219], v[200:207], v248\n\t" \
  "ds_read_u16_d16_hi v171, v171 offset:0\n\t" \
  "ds_read_b64_tr_b16 v[216:217], v250 offset:8528\n\t" \
  "ds_read_b64_tr_b16 v[218:219], v250 offset:9296\n\t" \
  "v_lshlrev_b32_sdwa v172, 1, v172 dst_sel:DWORD dst_unused:UNUSED_PAD src0_sel:DWORD src1_sel:WORD_1\n\t" \
  "v_smfmac_f32_16x16x64_bf16 v[84:87], v[220:223], v[200:207], v248\n\t" \
  "s_waitcnt lgkmcnt(14)\n\t" \
  "ds_read_u16_d16_hi v172, v172 offset:0\n\t" \
  "ds_read_b64_tr_b16 v[220:221], v250 offset:8536\n\t" \
  "ds_read_b64_tr_b16 v[222:223], v250 offset:9304\n\t" \
  "v_smfmac_f32_16x16x64_bf16 v[88:91], v[0:3], v[200:207], v248\n\t" \
  "s_waitcnt lgkmcnt(14)\n\t" \
  "v_lshlrev_b32_sdwa v173, 1, v173 dst_sel:DWORD dst_unused:UNUSED_PAD src0_sel:DWORD src1_sel:WORD_1\n\t" \
  "v_lshlrev_b32_sdwa v174, 1, v174 dst_sel:DWORD dst_unused:UNUSED_PAD src0_sel:DWORD src1_sel:WORD_1\n\t" \
  "v_smfmac_f32_16x16x64_bf16 v[92:95], v[4:7], v[200:207], v248\n\t" \
  "ds_read_u16_d16_hi v173, v173 offset:0\n\t" \
  "ds_read_u16_d16_hi v174, v174 offset:0\n\t" \
  "v_lshlrev_b32_sdwa v175, 1, v175 dst_sel:DWORD dst_unused:UNUSED_PAD src0_sel:DWORD src1_sel:WORD_1\n\t" \
  "ds_read_u16_d16_hi v175, v175 offset:0\n\t" \
  "s_waitcnt lgkmcnt(0)\n\t" \
  "v_smfmac_f32_16x16x64_bf16 v[72:75], v[8:11], v[168:175], v248\n\t" \
  "v_smfmac_f32_16x16x64_bf16 v[76:79], v[12:15], v[168:175], v248\n\t" \
  "s_nop 6\n\t" \
  "v_cvt_pk_bf16_f32 v230, v72, v73\n\t" \
  "v_cvt_pk_bf16_f32 v231, v74, v75\n\t" \
  "v_smfmac_f32_16x16x64_bf16 v[80:83], v[208:211], v[168:175], v248\n\t" \
  "v_cvt_pk_bf16_f32 v232, v76, v77\n\t" \
  "v_cvt_pk_bf16_f32 v233, v78, v79\n\t" \
  "v_smfmac_f32_16x16x64_bf16 v[84:87], v[212:215], v[168:175], v248\n\t" \
  "buffer_store_dwordx4 v[230:233], v253, %[osrd], %[ob] offen offset:0 sc1\n\t" \
  "s_nop 3\n\t" \
  "v_cvt_pk_bf16_f32 v234, v80, v81\n\t" \
  "v_cvt_pk_bf16_f32 v235, v82, v83\n\t" \
  "v_smfmac_f32_16x16x64_bf16 v[88:91], v[216:219], v[168:175], v248\n\t" \
  "v_cvt_pk_bf16_f32 v236, v84, v85\n\t" \
  "v_cvt_pk_bf16_f32 v237, v86, v87\n\t" \
  "v_smfmac_f32_16x16x64_bf16 v[92:95], v[220:223], v[168:175], v248\n\t" \
  "buffer_store_dwordx4 v[234:237], v253, %[osrd], %[ob] offen offset:64 sc1\n\t" \
  "s_nop 3\n\t" \
  "v_cvt_pk_bf16_f32 v238, v88, v89\n\t" \
  "v_cvt_pk_bf16_f32 v239, v90, v91\n\t" \
  "s_nop 0\n\t" \
  "v_cvt_pk_bf16_f32 v240, v92, v93\n\t" \
  "v_cvt_pk_bf16_f32 v241, v94, v95\n\t" \
  "buffer_store_dwordx4 v[238:241], v253, %[osrd], %[ob] offen offset:128 sc1\n\t" \
  ""
#define F_ASM \
  "s_mov_b32 %[lc], 3\n\t" \
  "s_setprio 3\n\t" \
  "s_mul_i32 %[t0], %[dgy], 7\n\t" \
  "s_add_u32 %[gy], %[gy], %[t0]\n\t" \
  "s_add_u32 %[xld], %[xlb], 15184\n\t" \
  "v_mov_b32 v252, 0\n\t" \
  "v_mov_b32 v253, 0\n\t" \
  "s_waitcnt vmcnt(0)\n\t" \
  "s_barrier\n\t" \
  "v_mfma_f32_32x32x8_bf16 v[0:15], v[252:253], v[252:253], 0\n\t" \
  "ds_read_u16 v96, v251 offset:0\n\t" \
  "ds_read_u16 v97, v251 offset:416\n\t" \
  "ds_read_u16 v98, v251 offset:2064\n\t" \
  "ds_read_u16 v99, v251 offset:2480\n\t" \
  "ds_read_u16 v112, v251 offset:8\n\t" \
  "ds_read_u16 v113, v251 offset:424\n\t" \
  "ds_read_u16 v114, v251 offset:2072\n\t" \
  "ds_read_u16 v115, v251 offset:2488\n\t" \
  "v_mfma_f32_32x32x8_bf16 v[16:31], v[252:253], v[252:253], 0\n\t" \
  "ds_read_u16 v128, v251 offset:16\n\t" \
  "ds_read_u16 v129, v251 offset:432\n\t" \
  "ds_read_u16 v130, v251 offset:2080\n\t" \
  "ds_read_u16 v131, v251 offset:2496\n\t" \
  "ds_read_u16 v144, v251 offset:24\n\t" \
  "ds_read_u16 v145, v251 offset:440\n\t" \
  "ds_read_u16 v146, v251 offset:2088\n\t" \
  "ds_read_u16 v147, v251 offset:2504\n\t" \
  "v_mfma_f32_32x32x8_bf16 v[32:47], v[252:253], v[252:253], 0\n\t" \
  "ds_read_u16 v160, v251 offset:32\n\t" \
  "ds_read_u16 v161, v251 offset:448\n\t" \
  "ds_read_u16 v162, v251 offset:2096\n\t" \
  "ds_read_u16 v163, v251 offset:2512\n\t" \
  "ds_read_u16 v176, v251 offset:40\n\t" \
  "ds_read_u16 v177, v251 offset:456\n\t" \
  "ds_read_u16 v178, v251 offset:2104\n\t" \
  "ds_read_u16 v179, v251 offset:2520\n\t" \
  "v_mfma_f32_32x32x8_bf16 v[48:63], v[252:253], v[252:253], 0\n\t" \
  "ds_read_u16 v100, v251 offset:4128\n\t" \
  "ds_read_u16 v101, v251 offset:4544\n\t" \
  "ds_read_u16 v102, v251 offset:6192\n\t" \
  "ds_read_u16 v103, v251 offset:6608\n\t" \
  "ds_read_u16 v116, v251 offset:4136\n\t" \
  "ds_read_u16 v117, v251 offset:4552\n\t" \
  "ds_read_u16 v118, v251 offset:6200\n\t" \
  "ds_read_u16 v119, v251 offset:6616\n\t" \
  "v_mfma_f32_32x32x8_bf16 v[64:79], v[252:253], v[252:253], 0\n\t" \
  "ds_read_u16 v132, v251 offset:4144\n\t" \
  "ds_read_u16 v133, v251 offset:4560\n\t" \
  "ds_read_u16 v134, v251 offset:6208\n\t" \
  "ds_read_u16 v135, v251 offset:6624\n\t" \
  "ds_read_u16 v148, v251 offset:4152\n\t" \
  "ds_read_u16 v149, v251 offset:4568\n\t" \
  "ds_read_u16 v150, v251 offset:6216\n\t" \
  "ds_read_u16 v151, v251 offset:6632\n\t" \
  "v_mfma_f32_32x32x8_bf16 v[80:95], v[252:253], v[252:253], 0\n\t" \
  "ds_read_u16 v164, v251 offset:4160\n\t" \
  "ds_read_u16 v165, v251 offset:4576\n\t" \
  "ds_read_u16 v166, v251 offset:6224\n\t" \
  "ds_read_u16 v167, v251 offset:6640\n\t" \
  "ds_read_u16 v180, v251 offset:4168\n\t" \
  "ds_read_u16 v181, v251 offset:4584\n\t" \
  "ds_read_u16 v182, v251 offset:6232\n\t" \
  "ds_read_u16 v183, v251 offset:6648\n\t" \
  "s_barrier\n\t" \
  "ds_read_b64 v[192:193], v241 offset:0\n\t" \
  "ds_read_b64 v[194:195], v241 offset:400\n\t" \
  "ds_read_b64 v[196:197], v241 offset:800\n\t" \
  "ds_read_b64 v[198:199], v241 offset:1200\n\t" \
  "ds_read_b64 v[200:201], v241 offset:8\n\t" \
  "ds_read_b64 v[202:203], v241 offset:408\n\t" \
  "ds_read_b64 v[204:205], v241 offset:808\n\t" \
  "ds_read_b64 v[206:207], v241 offset:1208\n\t" \
  "s_waitcnt lgkmcnt(4)\n\t" \
  "v_smfmac_f32_16x16x64_bf16 v[0:3], v[96:99], v[192:199], v240\n\t" \
  "ds_read_b64 v[208:209], v241 offset:16\n\t" \
  "ds_read_b64 v[210:211], v241 offset:416\n\t" \
  "ds_read_b64 v[212:213], v241 offset:816\n\t" \
  "ds_read_b64 v[214:215], v241 offset:1216\n\t" \
  "s_waitcnt lgkmcnt(4)\n\t" \
  "v_smfmac_f32_16x16x64_bf16 v[4:7], v[112:115], v[200:207], v240\n\t" \
  "ds_read_b64 v[216:217], v241 offset:24\n\t" \
  "ds_read_b64 v[218:219], v241 offset:424\n\t" \
  "ds_read_b64 v[220:221], v241 offset:824\n\t" \
  "ds_read_b64 v[222:223], v241 offset:1224\n\t" \
  "s_waitcnt lgkmcnt(4)\n\t" \
  "v_smfmac_f32_16x16x64_bf16 v[8:11], v[128:131], v[208:215], v240\n\t" \
  "ds_read_b64 v[192:193], v241 offset:32\n\t" \
  "ds_read_b64 v[194:195], v241 offset:432\n\t" \
  "ds_read_b64 v[196:197], v241 offset:832\n\t" \
  "ds_read_b64 v[198:199], v241 offset:1232\n\t" \
  "s_waitcnt lgkmcnt(4)\n\t" \
  "v_smfmac_f32_16x16x64_bf16 v[12:15], v[144:147], v[216:223], v240\n\t" \
  "ds_read_b64 v[200:201], v241 offset:40\n\t" \
  "ds_read_b64 v[202:203], v241 offset:440\n\t" \
  "ds_read_b64 v[204:205], v241 offset:840\n\t" \
  "ds_read_b64 v[206:207], v241 offset:1240\n\t" \
  "s_add_u32 %[cs], %[cs], 1\n\t" \
  "s_cmp_eq_u32 %[cs], 6\n\t" \
  "s_cselect_b32 %[cs], 0, %[cs]\n\t" \
  "s_cselect_b32 %[t0], -75920, %[ps]\n\t" \
  "v_add_u32 v241, %[t0], v241\n\t" \
  "s_barrier\n\t" \
  "s_waitcnt lgkmcnt(4)\n\t" \
  "v_smfmac_f32_16x16x64_bf16 v[16:19], v[160:163], v[192:199], v240\n\t" \
  "ds_read_b64 v[208:209], v241 offset:0\n\t" \
  "ds_read_b64 v[210:211], v241 offset:400\n\t" \
  "ds_read_b64 v[212:213], v241 offset:800\n\t" \
  "ds_read_b64 v[214:215], v241 offset:1200\n\t" \
  "s_waitcnt lgkmcnt(4)\n\t" \
  "v_smfmac_f32_16x16x64_bf16 v[20:23], v[176:179], v[200:207], v240\n\t" \
  "ds_read_b64 v[216:217], v241 offset:8\n\t" \
  "ds_read_b64 v[218:219], v241 offset:408\n\t" \
  "ds_read_b64 v[220:221], v241 offset:808\n\t" \
  "ds_read_b64 v[222:223], v241 offset:1208\n\t" \
  "s_waitcnt lgkmcnt(4)\n\t" \
  "v_smfmac_f32_16x16x64_bf16 v[0:3], v[100:103], v[208:215], v240\n\t" \
  "ds_read_b64 v[192:193], v241 offset:16\n\t" \
  "ds_read_b64 v[194:195], v241 offset:416\n\t" \
  "ds_read_b64 v[196:197], v241 offset:816\n\t" \
  "ds_read_b64 v[198:199], v241 offset:1216\n\t" \
  "ds_read_u16 v104, v251 offset:8256\n\t" \
  "v_smfmac_f32_16x16x64_bf16 v[24:27], v[96:99], v[208:215], v240\n\t" \
  "ds_read_u16 v105, v251 offset:8672\n\t" \
  "ds_read_u16 v106, v251 offset:10320\n\t" \
  "ds_read_u16 v107, v251 offset:10736\n\t" \
  "ds_read_u16 v120, v251 offset:8264\n\t" \
  "ds_read_u16 v121, v251 offset:8680\n\t" \
  "s_waitcnt lgkmcnt(10)\n\t" \
  "v_smfmac_f32_16x16x64_bf16 v[4:7], v[116:119], v[216:223], v240\n\t" \
  "ds_read_b64 v[200:201], v241 offset:24\n\t" \
  "ds_read_b64 v[202:203], v241 offset:424\n\t" \
  "ds_read_b64 v[204:205], v241 offset:824\n\t" \
  "ds_read_b64 v[206:207], v241 offset:1224\n\t" \
  "ds_read_u16 v122, v251 offset:10328\n\t" \
  "v_smfmac_f32_16x16x64_bf16 v[28:31], v[112:115], v[216:223], v240\n\t" \
  "ds_read_u16 v123, v251 offset:10744\n\t" \
  "ds_read_u16 v136, v251 offset:8272\n\t" \
  "ds_read_u16 v137, v251 offset:8688\n\t" \
  "ds_read_u16 v138, v251 offset:10336\n\t" \
  "ds_read_u16 v139, v251 offset:10752\n\t" \
  "s_waitcnt lgkmcnt(14)\n\t" \
  "v_smfmac_f32_16x16x64_bf16 v[8:11], v[132:135], v[192:199], v240\n\t" \
  "ds_read_b64 v[208:209], v241 offset:32\n\t" \
  "ds_read_b64 v[210:211], v241 offset:432\n\t" \
  "ds_read_b64 v[212:213], v241 offset:832\n\t" \
  "ds_read_b64 v[214:215], v241 offset:1232\n\t" \
  "ds_read_u16 v152, v251 offset:8280\n\t" \
  "v_smfmac_f32_16x16x64_bf16 v[32:35], v[128:131], v[192:199], v240\n\t" \
  "ds_read_u16 v153, v251 offset:8696\n\t" \
  "ds_read_u16 v154, v251 offset:10344\n\t" \
  "ds_read_u16 v155, v251 offset:10760\n\t" \
  "ds_read_u16 v168, v251 offset:8288\n\t" \
  "ds_read_u16 v169, v251 offset:8704\n\t" \
  "s_waitcnt lgkmcnt(14)\n\t" \
  "v_smfmac_f32_16x16x64_bf16 v[12:15], v[148:151], v[200:207], v240\n\t" \
  "ds_read_b64 v[216:217], v241 offset:40\n\t" \
  "ds_read_b64 v[218:219], v241 offset:440\n\t" \
  "ds_read_b64 v[220:221], v241 offset:840\n\t" \
  "ds_read_b64 v[222:223], v241 offset:1240\n\t" \
  "s_add_u32 %[cs], %[cs], 1\n\t" \
  "s_cmp_eq_u32 %[cs], 6\n\t" \
  "s_cselect_b32 %[cs], 0, %[cs]\n\t" \
  "s_cselect_b32 %[t0], -75920, %[ps]\n\t" \
  "v_add_u32 v241, %[t0], v241\n\t" \
  "ds_read_u16 v170, v251 offset:10352\n\t" \
  "v_smfmac_f32_16x16x64_bf16 v[36:39], v[144:147], v[200:207], v240\n\t" \
  "ds_read_u16 v171, v251 offset:10768\n\t" \
  "ds_read_u16 v184, v251 offset:8296\n\t" \
  "ds_read_u16 v185, v251 offset:8712\n\t" \
  "ds_read_u16 v186, v251 offset:10360\n\t" \
  "ds_read_u16 v187, v251 offset:10776\n\t" \
  "s_barrier\n\t" \
  "s_waitcnt lgkmcnt(14)\n\t" \
  "v_smfmac_f32_16x16x64_bf16 v[16:19], v[164:167], v[208:215], v240\n\t" \
  "ds_read_b64 v[192:193], v241 offset:0\n\t" \
  "ds_read_b64 v[194:195], v241 offset:400\n\t" \
  "v_smfmac_f32_16x16x64_bf16 v[40:43], v[160:163], v[208:215], v240\n\t" \
  "ds_read_b64 v[196:197], v241 offset:800\n\t" \
  "ds_read_b64 v[198:199], v241 offset:1200\n\t" \
  "s_waitcnt lgkmcnt(10)\n\t" \
  "v_smfmac_f32_16x16x64_bf16 v[20:23], v[180:183], v[216:223], v240\n\t" \
  "ds_read_b64 v[200:201], v241 offset:8\n\t" \
  "ds_read_b64 v[202:203], v241 offset:408\n\t" \
  "v_smfmac_f32_16x16x64_bf16 v[44:47], v[176:179], v[216:223], v240\n\t" \
  "ds_read_b64 v[204:205], v241 offset:808\n\t" \
  "ds_read_b64 v[206:207], v241 offset:1208\n\t" \
  "s_waitcnt lgkmcnt(4)\n\t" \
  "v_smfmac_f32_16x16x64_bf16 v[0:3], v[104:107], v[192:199], v240\n\t" \
  "ds_read_b64 v[208:209], v241 offset:16\n\t" \
  "ds_read_b64 v[210:211], v241 offset:416\n\t" \
  "ds_read_b64 v[212:213], v241 offset:816\n\t" \
  "ds_read_b64 v[214:215], v241 offset:1216\n\t" \
  "v_smfmac_f32_16x16x64_bf16 v[24:27], v[100:103], v[192:199], v240\n\t" \
  "ds_read_u16 v108, v251 offset:12384\n\t" \
  "ds_read_u16 v109, v251 offset:12800\n\t" \
  "ds_read_u16 v110, v251 offset:14448\n\t" \
  "v_smfmac_f32_16x16x64_bf16 v[48:51], v[96:99], v[192:199], v240\n\t" \
  "ds_read_u16 v111, v251 offset:14864\n\t" \
  "ds_read_u16 v124, v251 offset:12392\n\t" \
  "ds_read_u16 v125, v251 offset:12808\n\t" \
  "s_waitcnt lgkmcnt(10)\n\t" \
  "v_smfmac_f32_16x16x64_bf16 v[4:7], v[120:123], v[200:207], v240\n\t" \
  "ds_read_b64 v[216:217], v241 offset:24\n\t" \
  "ds_read_b64 v[218:219], v241 offset:424\n\t" \
  "ds_read_b64 v[220:221], v241 offset:824\n\t" \
  "ds_read_b64 v[222:223], v241 offset:1224\n\t" \
  "v_smfmac_f32_16x16x64_bf16 v[28:31], v[116:119], v[200:207], v240\n\t" \
  "ds_read_u16 v126, v251 offset:14456\n\t" \
  "ds_read_u16 v127, v251 offset:14872\n\t" \
  "ds_read_u16 v140, v251 offset:12400\n\t" \
  "v_smfmac_f32_16x16x64_bf16 v[52:55], v[112:115], v[200:207], v240\n\t" \
  "ds_read_u16 v141, v251 offset:12816\n\t" \
  "ds_read_u16 v142, v251 offset:14464\n\t" \
  "ds_read_u16 v143, v251 offset:14880\n\t" \
  "s_waitcnt lgkmcnt(14)\n\t" \
  "v_smfmac_f32_16x16x64_bf16 v[8:11], v[136:139], v[208:215], v240\n\t" \
  "ds_read_b64 v[192:193], v241 offset:32\n\t" \
  "ds_read_b64 v[194:195], v241 offset:432\n\t" \
  "ds_read_b64 v[196:197], v241 offset:832\n\t" \
  "ds_read_b64 v[198:199], v241 offset:1232\n\t" \
  "v_smfmac_f32_16x16x64_bf16 v[32:35], v[132:135], v[208:215], v240\n\t" \
  "ds_read_u16 v156, v251 offset:12408\n\t" \
  "ds_read_u16 v157, v251 offset:12824\n\t" \
  "ds_read_u16 v158, v251 offset:14472\n\t" \
  "v_smfmac_f32_16x16x64_bf16 v[56:59], v[128:131], v[208:215], v240\n\t" \
  "ds_read_u16 v159, v251 offset:14888\n\t" \
  "ds_read_u16 v172, v251 offset:12416\n\t" \
  "ds_read_u16 v173, v251 offset:12832\n\t" \
  "s_waitcnt lgkmcnt(14)\n\t" \
  "v_smfmac_f32_16x16x64_bf16 v[12:15], v[152:155], v[216:223], v240\n\t" \
  "ds_read_b64 v[200:201], v241 offset:40\n\t" \
  "ds_read_b64 v[202:203], v241 offset:440\n\t" \
  "ds_read_b64 v[204:205], v241 offset:840\n\t" \
  "ds_read_b64 v[206:207], v241 offset:1240\n\t" \
  "v_smfmac_f32_16x16x64_bf16 v[36:39], v[148:151], v[216:223], v240\n\t" \
  "s_add_u32 %[cs], %[cs], 1\n\t" \
  "s_cmp_eq_u32 %[cs], 6\n\t" \
  "s_cselect_b32 %[cs], 0, %[cs]\n\t" \
  "s_cselect_b32 %[t0], -75920, %[ps]\n\t" \
  "v_add_u32 v241, %[t0], v241\n\t" \
  "ds_read_u16 v174, v251 offset:14480\n\t" \
  "ds_read_u16 v175, v251 offset:14896\n\t" \
  "ds_read_u16 v188, v251 offset:12424\n\t" \
  "v_smfmac_f32_16x16x64_bf16 v[60:63], v[144:147], v[216:223], v240\n\t" \
  "ds_read_u16 v189, v251 offset:12840\n\t" \
  "ds_read_u16 v190, v251 offset:14488\n\t" \
  "ds_read_u16 v191, v251 offset:14904\n\t" \
  "s_waitcnt lgkmcnt(0)\n\t" \
  "s_barrier\n\t" \
  "v_smfmac_f32_16x16x64_bf16 v[16:19], v[168:171], v[192:199], v240\n\t" \
  "ds_read_b64 v[208:209], v241 offset:0\n\t" \
  "ds_read_b64 v[210:211], v241 offset:400\n\t" \
  "v_smfmac_f32_16x16x64_bf16 v[40:43], v[164:167], v[192:199], v240\n\t" \
  "ds_read_b64 v[212:213], v241 offset:800\n\t" \
  "v_smfmac_f32_16x16x64_bf16 v[64:67], v[160:163], v[192:199], v240\n\t" \
  "ds_read_b64 v[214:215], v241 offset:1200\n\t" \
  "v_smfmac_f32_16x16x64_bf16 v[20:23], v[184:187], v[200:207], v240\n\t" \
  "ds_read_b64 v[216:217], v241 offset:8\n\t" \
  "ds_read_b64 v[218:219], v241 offset:408\n\t" \
  "v_smfmac_f32_16x16x64_bf16 v[44:47], v[180:183], v[200:207], v240\n\t" \
  "ds_read_b64 v[220:221], v241 offset:808\n\t" \
  "v_smfmac_f32_16x16x64_bf16 v[68:71], v[176:179], v[200:207], v240\n\t" \
  "ds_read_b64 v[222:223], v241 offset:1208\n\t" \
  "s_waitcnt lgkmcnt(4)\n\t" \
  "v_smfmac_f32_16x16x64_bf16 v[0:3], v[108:111], v[208:215], v240\n\t" \
  "ds_read_b64 v[192:193], v241 offset:16\n\t" \
  "v_smfmac_f32_16x16x64_bf16 v[24:27], v[104:107], v[208:215], v240\n\t" \
  "ds_read_b64 v[194:195], v241 offset:416\n\t" \
  "v_smfmac_f32_16x16x64_bf16 v[48:51], v[100:103], v[208:215], v240\n\t" \
  "ds_read_b64 v[196:197], v241 offset:816\n\t" \
  "v_smfmac_f32_16x16x64_bf16 v[72:75], v[96:99], v[208:215], v240\n\t" \
  "ds_read_b64 v[198:199], v241 offset:1216\n\t" \
  "s_waitcnt lgkmcnt(4)\n\t" \
  "v_smfmac_f32_16x16x64_bf16 v[4:7], v[124:127], v[216:223], v240\n\t" \
  "ds_read_b64 v[200:201], v241 offset:24\n\t" \
  "v_mul_f32 v237, v0, v0\n\t" \
  "v_add_f32 v236, v0, v1\n\t" \
  "v_smfmac_f32_16x16x64_bf16 v[28:31], v[120:123], v[216:223], v240\n\t" \
  "ds_read_b64 v[202:203], v241 offset:424\n\t" \
  "v_fmac_f32 v237, v1, v1\n\t" \
  "v_add_f32 v236, v236, v2\n\t" \
  "v_smfmac_f32_16x16x64_bf16 v[52:55], v[116:119], v[216:223], v240\n\t" \
  "ds_read_b64 v[204:205], v241 offset:824\n\t" \
  "v_fmac_f32 v237, v2, v2\n\t" \
  "v_add_f32 v236, v236, v3\n\t" \
  "v_smfmac_f32_16x16x64_bf16 v[76:79], v[112:115], v[216:223], v240\n\t" \
  "ds_read_b64 v[206:207], v241 offset:1224\n\t" \
  "v_fmac_f32 v237, v3, v3\n\t" \
  "s_waitcnt lgkmcnt(4)\n\t" \
  "v_smfmac_f32_16x16x64_bf16 v[8:11], v[140:143], v[192:199], v240\n\t" \
  "ds_read_b64 v[208:209], v241 offset:32\n\t" \
  "v_add_f32 v236, v236, v4\n\t" \
  "v_fmac_f32 v237, v4, v4\n\t" \
  "v_smfmac_f32_16x16x64_bf16 v[32:35], v[136:139], v[192:199], v240\n\t" \
  "ds_read_b64 v[210:211], v241 offset:432\n\t" \
  "v_add_f32 v236, v236, v5\n\t" \
  "v_fmac_f32 v237, v5, v5\n\t" \
  "v_smfmac_f32_16x16x64_bf16 v[56:59], v[132:135], v[192:199], v240\n\t" \
  "ds_read_b64 v[212:213], v241 offset:832\n\t" \
  "v_add_f32 v236, v236, v6\n\t" \
  "v_fmac_f32 v237, v6, v6\n\t" \
  "v_smfmac_f32_16x16x64_bf16 v[80:83], v[128:131], v[192:199], v240\n\t" \
  "ds_read_b64 v[214:215], v241 offset:1232\n\t" \
  "v_add_f32 v236, v236, v7\n\t" \
  "v_fmac_f32 v237, v7, v7\n\t" \
  "s_waitcnt lgkmcnt(4)\n\t" \
  "v_smfmac_f32_16x16x64_bf16 v[12:15], v[156:159], v[200:207], v240\n\t" \
  "ds_read_b64 v[216:217], v241 offset:40\n\t" \
  "ds_read_b64 v[218:219], v241 offset:440\n\t" \
  "v_add_f32 v236, v236, v8\n\t" \
  "v_fmac_f32 v237, v8, v8\n\t" \
  "v_smfmac_f32_16x16x64_bf16 v[36:39], v[152:155], v[200:207], v240\n\t" \
  "ds_read_b64 v[220:221], v241 offset:840\n\t" \
  "v_add_f32 v236, v236, v9\n\t" \
  "v_fmac_f32 v237, v9, v9\n\t" \
  "v_smfmac_f32_16x16x64_bf16 v[60:63], v[148:151], v[200:207], v240\n\t" \
  "ds_read_b64 v[222:223], v241 offset:1240\n\t" \
  "v_add_f32 v236, v236, v10\n\t" \
  "v_fmac_f32 v237, v10, v10\n\t" \
  "v_smfmac_f32_16x16x64_bf16 v[84:87], v[144:147], v[200:207], v240\n\t" \
  "s_add_u32 %[cs], %[cs], 1\n\t" \
  "s_cmp_eq_u32 %[cs], 6\n\t" \
  "s_cselect_b32 %[cs], 0, %[cs]\n\t" \
  "s_cselect_b32 %[t0], -75920, %[ps]\n\t" \
  "v_add_u32 v241, %[t0], v241\n\t" \
  "v_add_f32 v236, v236, v11\n\t" \
  "v_fmac_f32 v237, v11, v11\n\t" \
  "s_waitcnt lgkmcnt(4)\n\t" \
  "v_smfmac_f32_16x16x64_bf16 v[16:19], v[172:175], v[208:215], v240\n\t" \
  "v_add_f32 v236, v236, v12\n\t" \
  "v_fmac_f32 v237, v12, v12\n\t" \
  "v_smfmac_f32_16x16x64_bf16 v[40:43], v[168:171], v[208:215], v240\n\t" \
  "v_add_f32 v236, v236, v13\n\t" \
  "v_fmac_f32 v237, v13, v13\n\t" \
  "v_smfmac_f32_16x16x64_bf16 v[64:67], v[164:167], v[208:215], v240\n\t" \
  "v_add_f32 v236, v236, v14\n\t" \
  "v_fmac_f32 v237, v14, v14\n\t" \
  "v_smfmac_f32_16x16x64_bf16 v[88:91], v[160:163], v[208:215], v240\n\t" \
  "v_add_f32 v236, v236, v15\n\t" \
  "v_fmac_f32 v237, v15, v15\n\t" \
  "s_waitcnt lgkmcnt(0)\n\t" \
  "v_smfmac_f32_16x16x64_bf16 v[20:23], v[188:191], v[216:223], v240\n\t" \
  "v_add_f32 v236, v236, v16\n\t" \
  "v_fmac_f32 v237, v16, v16\n\t" \
  "v_smfmac_f32_16x16x64_bf16 v[44:47], v[184:187], v[216:223], v240\n\t" \
  "v_add_f32 v236, v236, v17\n\t" \
  "v_fmac_f32 v237, v17, v17\n\t" \
  "v_smfmac_f32_16x16x64_bf16 v[68:71], v[180:183], v[216:223], v240\n\t" \
  "v_add_f32 v236, v236, v18\n\t" \
  "v_fmac_f32 v237, v18, v18\n\t" \
  "v_smfmac_f32_16x16x64_bf16 v[92:95], v[176:179], v[216:223], v240\n\t" \
  "v_add_f32 v236, v236, v19\n\t" \
  "v_fmac_f32 v237, v19, v19\n\t" \
  "s_nop 3\n\t" \
  "v_add_f32 v236, v236, v20\n\t" \
  "v_fmac_f32 v237, v20, v20\n\t" \
  "v_add_f32 v236, v236, v21\n\t" \
  "v_fmac_f32 v237, v21, v21\n\t" \
  "v_add_f32 v236, v236, v22\n\t" \
  "v_fmac_f32 v237, v22, v22\n\t" \
  "v_add_f32 v236, v236, v23\n\t" \
  "v_fmac_f32 v237, v23, v23\n\t" \
  "ds_write_b64 v243, v[236:237]\n\t" \
  "s_waitcnt lgkmcnt(0)\n\t" \
  "s_barrier\n\t" \
  "ds_read_b128 v[224:227], v244\n\t" \
  "ds_read_b128 v[228:231], v244 offset:1024\n\t" \
  "ds_read_b64 v[192:193], v241 offset:0\n\t" \
  "ds_read_b64 v[194:195], v241 offset:400\n\t" \
  "ds_read_b64 v[196:197], v241 offset:800\n\t" \
  "ds_read_b64 v[198:199], v241 offset:1200\n\t" \
  "ds_read_b64 v[200:201], v241 offset:8\n\t" \
  "ds_read_b64 v[202:203], v241 offset:408\n\t" \
  "ds_read_b64 v[204:205], v241 offset:808\n\t" \
  "ds_read_b64 v[206:207], v241 offset:1208\n\t" \
  "s_waitcnt lgkmcnt(8)\n\t" \
  "v_add_f32 v224, v224, v226\n\t" \
  "v_add_f32 v225, v225, v227\n\t" \
  "v_add_f32 v228, v228, v230\n\t" \
  "v_add_f32 v229, v229, v231\n\t" \
  "v_add_f32 v224, v224, v228\n\t" \
  "v_add_f32 v225, v225, v229\n\t" \
  "v_mul_f32 v224, 0x3c2aaaab, v224\n\t" \
  "v_mul_f32 v225, 0x3c2aaaab, v225\n\t" \
  "v_fma_f32 v225, -v224, v224, v225\n\t" \
  "v_max_f32 v225, 0, v225\n\t" \
  "v_add_f32 v225, 0x3727c5ac, v225\n\t" \
  "v_rsq_f32 v225, v225\n\t" \
  "s_nop 0\n\t" \
  "v_mul_f32 v238, %[gl], v225\n\t" \
  "v_mul_f32 v239, -v224, v238\n\t" \
  "v_fma_f32 v0, v0, v238, v239\n\t" \
  "v_fma_f32 v1, v1, v238, v239\n\t" \
  "v_fma_f32 v2, v2, v238, v239\n\t" \
  "v_fma_f32 v3, v3, v238, v239\n\t" \
  "v_cvt_pk_bf16_f32 v224, v0, v1\n\t" \
  "v_cvt_pk_bf16_f32 v225, v2, v3\n\t" \
  "v_fma_f32 v4, v4, v238, v239\n\t" \
  "v_fma_f32 v5, v5, v238, v239\n\t" \
  "v_fma_f32 v6, v6, v238, v239\n\t" \
  "v_fma_f32 v7, v7, v238, v239\n\t" \
  "v_cvt_pk_bf16_f32 v226, v4, v5\n\t" \
  "v_cvt_pk_bf16_f32 v227, v6, v7\n\t" \
  "ds_write_b128 v242, v[224:227] offset:0\n\t" \
  "v_fma_f32 v8, v8, v238, v239\n\t" \
  "v_fma_f32 v9, v9, v238, v239\n\t" \
  "v_fma_f32 v10, v10, v238, v239\n\t" \
  "v_fma_f32 v11, v11, v238, v239\n\t" \
  "v_cvt_pk_bf16_f32 v228, v8, v9\n\t" \
  "v_cvt_pk_bf16_f32 v229, v10, v11\n\t" \
  "v_fma_f32 v12, v12, v238, v239\n\t" \
  "v_fma_f32 v13, v13, v238, v239\n\t" \
  "v_fma_f32 v14, v14, v238, v239\n\t" \
  "v_fma_f32 v15, v15, v238, v239\n\t" \
  "v_cvt_pk_bf16_f32 v230, v12, v13\n\t" \
  "v_cvt_pk_bf16_f32 v231, v14, v15\n\t" \
  "ds_write_b128 v242, v[228:231] offset:1024\n\t" \
  "v_fma_f32 v16, v16, v238, v239\n\t" \
  "v_fma_f32 v17, v17, v238, v239\n\t" \
  "v_fma_f32 v18, v18, v238, v239\n\t" \
  "v_fma_f32 v19, v19, v238, v239\n\t" \
  "v_cvt_pk_bf16_f32 v232, v16, v17\n\t" \
  "v_cvt_pk_bf16_f32 v233, v18, v19\n\t" \
  "v_fma_f32 v20, v20, v238, v239\n\t" \
  "v_fma_f32 v21, v21, v238, v239\n\t" \
  "v_fma_f32 v22, v22, v238, v239\n\t" \
  "v_fma_f32 v23, v23, v238, v239\n\t" \
  "v_cvt_pk_bf16_f32 v234, v20, v21\n\t" \
  "v_cvt_pk_bf16_f32 v235, v22, v23\n\t" \
  "ds_write_b128 v242, v[232:235] offset:2048\n\t" \
  "v_mfma_f32_16x16x16_bf16 v[0:3], v[252:253], v[252:253], 0\n\t" \
  "v_mfma_f32_16x16x16_bf16 v[4:7], v[252:253], v[252:253], 0\n\t" \
  "v_mfma_f32_32x32x8_bf16 v[8:23], v[252:253], v[252:253], 0\n\t" \
  "s_waitcnt lgkmcnt(0)\n\t" \
  "s_barrier\n\t" \
  "v_smfmac_f32_16x16x64_bf16 v[24:27], v[108:111], v[192:199], v240\n\t" \
  "ds_read_b64 v[208:209], v241 offset:16\n\t" \
  "v_smfmac_f32_16x16x64_bf16 v[48:51], v[104:107], v[192:199], v240\n\t" \
  "ds_read_b64 v[210:211], v241 offset:416\n\t" \
  "v_smfmac_f32_16x16x64_bf16 v[72:75], v[100:103], v[192:199], v240\n\t" \
  "ds_read_b64 v[212:213], v241 offset:816\n\t" \
  "v_smfmac_f32_16x16x64_bf16 v[0:3], v[96:99], v[192:199], v240\n\t" \
  "ds_read_b64 v[214:215], v241 offset:1216\n\t" \
  "v_smfmac_f32_16x16x64_bf16 v[28:31], v[124:127], v[200:207], v240\n\t" \
  "ds_read_b64 v[216:217], v241 offset:24\n\t" \
  "v_mul_f32 v237, v24, v24\n\t" \
  "v_add_f32 v236, v24, v25\n\t" \
  "v_smfmac_f32_16x16x64_bf16 v[52:55], v[120:123], v[200:207], v240\n\t" \
  "ds_read_b64 v[218:219], v241 offset:424\n\t" \
  "v_fmac_f32 v237, v25, v25\n\t" \
  "v_add_f32 v236, v236, v26\n\t" \
  "v_smfmac_f32_16x16x64_bf16 v[76:79], v[116:119], v[200:207], v240\n\t" \
  "ds_read_b64 v[220:221], v241 offset:824\n\t" \
  "v_fmac_f32 v237, v26, v26\n\t" \
  "v_add_f32 v236, v236, v27\n\t" \
  "v_smfmac_f32_16x16x64_bf16 v[4:7], v[112:115], v[200:207], v240\n\t" \
  "ds_read_b64 v[222:223], v241 offset:1224\n\t" \
  "v_fmac_f32 v237, v27, v27\n\t" \
  "s_waitcnt lgkmcnt(4)\n\t" \
  "v_smfmac_f32_16x16x64_bf16 v[32:35], v[140:143], v[208:215], v240\n\t" \
  "ds_read_b64 v[192:193], v241 offset:32\n\t" \
  "v_add_f32 v236, v236, v28\n\t" \
  "v_fmac_f32 v237, v28, v28\n\t" \
  "v_smfmac_f32_16x16x64_bf16 v[56:59], v[136:139], v[208:215], v240\n\t" \
  "ds_read_b64 v[194:195], v241 offset:432\n\t" \
  "v_add_f32 v236, v236, v29\n\t" \
  "v_fmac_f32 v237, v29, v29\n\t" \
  "v_smfmac_f32_16x16x64_bf16 v[80:83], v[132:135], v[208:215], v240\n\t" \
  "ds_read_b64 v[196:197], v241 offset:832\n\t" \
  "v_add_f32 v236, v236, v30\n\t" \
  "v_fmac_f32 v237, v30, v30\n\t" \
  "v_smfmac_f32_16x16x64_bf16 v[8:11], v[128:131], v[208:215], v240\n\t" \
  "ds_read_b64 v[198:199], v241 offset:1232\n\t" \
  "v_add_f32 v236, v236, v31\n\t" \
  "v_fmac_f32 v237, v31, v31\n\t" \
  "s_waitcnt lgkmcnt(4)\n\t" \
  "v_smfmac_f32_16x16x64_bf16 v[36:39], v[156:159], v[216:223], v240\n\t" \
  "ds_read_b64 v[200:201], v241 offset:40\n\t" \
  "ds_read_b64 v[202:203], v241 offset:440\n\t" \
  "v_add_f32 v236, v236, v32\n\t" \
  "v_fmac_f32 v237, v32, v32\n\t" \
  "v_smfmac_f32_16x16x64_bf16 v[60:63], v[152:155], v[216:223], v240\n\t" \
  "ds_read_b64 v[204:205], v241 offset:840\n\t" \
  "v_add_f32 v236, v236, v33\n\t" \
  "v_fmac_f32 v237, v33, v33\n\t" \
  "v_smfmac_f32_16x16x64_bf16 v[84:87], v[148:151], v[216:223], v240\n\t" \
  "ds_read_b64 v[206:207], v241 offset:1240\n\t" \
  "v_add_f32 v236, v236, v34\n\t" \
  "v_fmac_f32 v237, v34, v34\n\t" \
  "v_smfmac_f32_16x16x64_bf16 v[12:15], v[144:147], v[216:223], v240\n\t" \
  "s_add_u32 %[cs], %[cs], 1\n\t" \
  "s_cmp_eq_u32 %[cs], 6\n\t" \
  "s_cselect_b32 %[cs], 0, %[cs]\n\t" \
  "s_cselect_b32 %[t0], -75920, %[ps]\n\t" \
  "v_add_u32 v241, %[t0], v241\n\t" \
  "v_add_f32 v236, v236, v35\n\t" \
  "v_fmac_f32 v237, v35, v35\n\t" \
  "s_waitcnt lgkmcnt(4)\n\t" \
  "v_smfmac_f32_16x16x64_bf16 v[40:43], v[172:175], v[192:199], v240\n\t" \
  "ds_read_b64 v[208:209], v241 offset:0\n\t" \
  "v_add_f32 v236, v236, v36\n\t" \
  "v_fmac_f32 v237, v36, v36\n\t" \
  "v_smfmac_f32_16x16x64_bf16 v[64:67], v[168:171], v[192:199], v240\n\t" \
  "ds_read_b64 v[210:211], v241 offset:400\n\t" \
  "v_add_f32 v236, v236, v37\n\t" \
  "v_fmac_f32 v237, v37, v37\n\t" \
  "v_smfmac_f32_16x16x64_bf16 v[88:91], v[164:167], v[192:199], v240\n\t" \
  "ds_read_b64 v[212:213], v241 offset:800\n\t" \
  "v_add_f32 v236, v236, v38\n\t" \
  "v_fmac_f32 v237, v38, v38\n\t" \
  "v_smfmac_f32_16x16x64_bf16 v[16:19], v[160:163], v[192:199], v240\n\t" \
  "ds_read_b64 v[214:215], v241 offset:1200\n\t" \
  "v_add_f32 v236, v236, v39\n\t" \
  "v_fmac_f32 v237, v39, v39\n\t" \
  "s_waitcnt lgkmcnt(4)\n\t" \
  "v_smfmac_f32_16x16x64_bf16 v[44:47], v[188:191], v[200:207], v240\n\t" \
  "ds_read_b64 v[216:217], v241 offset:8\n\t" \
  "v_add_f32 v236, v236, v40\n\t" \
  "v_fmac_f32 v237, v40, v40\n\t" \
  "v_smfmac_f32_16x16x64_bf16 v[68:71], v[184:187], v[200:207], v240\n\t" \
  "ds_read_b64 v[218:219], v241 offset:408\n\t" \
  "v_add_f32 v236, v236, v41\n\t" \
  "v_fmac_f32 v237, v41, v41\n\t" \
  "v_smfmac_f32_16x16x64_bf16 v[92:95], v[180:183], v[200:207], v240\n\t" \
  "ds_read_b64 v[220:221], v241 offset:808\n\t" \
  "v_add_f32 v236, v236, v42\n\t" \
  "v_fmac_f32 v237, v42, v42\n\t" \
  "v_smfmac_f32_16x16x64_bf16 v[20:23], v[176:179], v[200:207], v240\n\t" \
  "ds_read_b64 v[222:223], v241 offset:1208\n\t" \
  "v_add_f32 v236, v236, v43\n\t" \
  "v_fmac_f32 v237, v43, v43\n\t" \
  "s_nop 3\n\t" \
  "v_add_f32 v236, v236, v44\n\t" \
  "v_fmac_f32 v237, v44, v44\n\t" \
  "v_add_f32 v236, v236, v45\n\t" \
  "v_fmac_f32 v237, v45, v45\n\t" \
  "v_add_f32 v236, v236, v46\n\t" \
  "v_fmac_f32 v237, v46, v46\n\t" \
  "v_add_f32 v236, v236, v47\n\t" \
  "v_fmac_f32 v237, v47, v47\n\t" \
  "ds_write_b64 v243, v[236:237]\n\t" \
  "s_waitcnt lgkmcnt(0)\n\t" \
  "s_barrier\n\t" \
  ".Lcnx_f:\n\t" \
  "ds_read_b128 v[224:227], v244\n\t" \
  "ds_read_b128 v[228:231], v244 offset:1024\n\t" \
  "s_waitcnt lgkmcnt(0)\n\t" \
  "s_setprio 1\n\t" \
  "v_add_f32 v224, v224, v226\n\t" \
  "v_add_f32 v225, v225, v227\n\t" \
  "v_add_f32 v228, v228, v230\n\t" \
  "v_add_f32 v229, v229, v231\n\t" \
  "v_add_f32 v224, v224, v228\n\t" \
  "v_add_f32 v225, v225, v229\n\t" \
  "v_mul_f32 v224, 0x3c2aaaab, v224\n\t" \
  "v_mul_f32 v225, 0x3c2aaaab, v225\n\t" \
  "v_fma_f32 v225, -v224, v224, v225\n\t" \
  "v_max_f32 v225, 0, v225\n\t" \
  "v_add_f32 v225, 0x3727c5ac, v225\n\t" \
  "v_rsq_f32 v225, v225\n\t" \
  "s_nop 0\n\t" \
  "v_mul_f32 v238, %[gl], v225\n\t" \
  "v_mul_f32 v239, -v224, v238\n\t" \
  "v_fma_f32 v24, v24, v238, v239\n\t" \
  "v_fma_f32 v25, v25, v238, v239\n\t" \
  "v_fma_f32 v26, v26, v238, v239\n\t" \
  "v_fma_f32 v27, v27, v238, v239\n\t" \
  "v_cvt_pk_bf16_f32 v224, v24, v25\n\t" \
  "v_cvt_pk_bf16_f32 v225, v26, v27\n\t" \
  "v_fma_f32 v28, v28, v238, v239\n\t" \
  "v_fma_f32 v29, v29, v238, v239\n\t" \
  "v_fma_f32 v30, v30, v238, v239\n\t" \
  "v_fma_f32 v31, v31, v238, v239\n\t" \
  "v_cvt_pk_bf16_f32 v226, v28, v29\n\t" \
  "v_cvt_pk_bf16_f32 v227, v30, v31\n\t" \
  "s_setprio 3\n\t" \
  "v_mfma_f32_16x16x16_bf16 v[24:27], v[252:253], v[252:253], 0\n\t" \
  "ds_write_b128 v242, v[224:227] offset:0\n\t" \
  "v_fma_f32 v32, v32, v238, v239\n\t" \
  "v_mfma_f32_16x16x16_bf16 v[28:31], v[252:253], v[252:253], 0\n\t" \
  "ds_read_b64 v[192:193], v241 offset:16\n\t" \
  "v_fma_f32 v33, v33, v238, v239\n\t" \
  "v_smfmac_f32_16x16x64_bf16 v[48:51], v[108:111], v[208:215], v240\n\t" \
  "ds_read_b64 v[194:195], v241 offset:416\n\t" \
  "v_fma_f32 v34, v34, v238, v239\n\t" \
  "v_smfmac_f32_16x16x64_bf16 v[72:75], v[104:107], v[208:215], v240\n\t" \
  "ds_read_b64 v[196:197], v241 offset:816\n\t" \
  "v_fma_f32 v35, v35, v238, v239\n\t" \
  "v_smfmac_f32_16x16x64_bf16 v[0:3], v[100:103], v[208:215], v240\n\t" \
  "ds_read_b64 v[198:199], v241 offset:1216\n\t" \
  "v_cvt_pk_bf16_f32 v228, v32, v33\n\t" \
  "v_smfmac_f32_16x16x64_bf16 v[24:27], v[96:99], v[208:215], v240\n\t" \
  "ds_read_b64 v[200:201], v241 offset:24\n\t" \
  "v_cvt_pk_bf16_f32 v229, v34, v35\n\t" \
  "v_smfmac_f32_16x16x64_bf16 v[52:55], v[124:127], v[216:223], v240\n\t" \
  "ds_read_b64 v[202:203], v241 offset:424\n\t" \
  "v_fma_f32 v36, v36, v238, v239\n\t" \
  "v_smfmac_f32_16x16x64_bf16 v[76:79], v[120:123], v[216:223], v240\n\t" \
  "ds_read_b64 v[204:205], v241 offset:824\n\t" \
  "v_fma_f32 v37, v37, v238, v239\n\t" \
  "v_smfmac_f32_16x16x64_bf16 v[4:7], v[116:119], v[216:223], v240\n\t" \
  "ds_read_b64 v[206:207], v241 offset:1224\n\t" \
  "v_fma_f32 v38, v38, v238, v239\n\t" \
  "v_smfmac_f32_16x16x64_bf16 v[28:31], v[112:115], v[216:223], v240\n\t" \
  "v_fma_f32 v39, v39, v238, v239\n\t" \
  "s_setprio 1\n\t" \
  "s_setprio 3\n\t" \
  "s_mov_b32 m0, %[wld]\n\t" \
  "s_add_u32 %[t1], %[wso], %[wpd]\n\t" \
  "buffer_load_dwordx4 v245, %[wrs], %[wso] offen lds\n\t" \
  "buffer_load_dwordx4 v245, %[wrs], %[t1] offen offset:1024 lds\n\t" \
  "s_add_u32 %[t1], %[t1], %[wpd]\n\t" \
  "s_add_u32 %[wso], %[wso], %[wcs]\n\t" \
  "buffer_load_dwordx4 v245, %[wrs], %[t1] offen offset:2048 lds\n\t" \
  "s_add_u32 %[wld], %[wld], %[wst]\n\t" \
  "s_setprio 1\n\t" \
  "s_setprio 3\n\t" \
  "s_waitcnt vmcnt(3)\n\t" \
  "s_barrier\n\t" \
  "s_mov_b32 m0, %[wld]\n\t" \
  "s_add_u32 %[t1], %[wso], %[wpd]\n\t" \
  "buffer_load_dwordx4 v245, %[wrs], %[wso] offen lds\n\t" \
  "buffer_load_dwordx4 v245, %[wrs], %[t1] offen offset:1024 lds\n\t" \
  "s_add_u32 %[t1], %[t1], %[wpd]\n\t" \
  "s_add_u32 %[wso], %[wso], %[wcs]\n\t" \
  "buffer_load_dwordx4 v245, %[wrs], %[t1] offen offset:2048 lds\n\t" \
  "s_add_u32 %[wld], %[wld], %[wlb]\n\t" \
  "s_mov_b32 m0, %[xld]\n\t" \
  "s_cmp_lt_u32 %[gy], 0x3000000\n\t" \
  "s_cselect_b32 %[xso], %[gy], 0x40000000\n\t" \
  "buffer_load_dwordx4 v246, %[xrs], %[xso] offen offset:0 nt lds\n\t" \
  "s_add_u32 %[gy], %[gy], %[dgy]\n\t" \
  "buffer_load_dwordx4 v247, %[xrs], %[xso] offen offset:1024 nt lds\n\t" \
  "s_add_u32 %[xld], %[xld], 15184\n\t" \
  "s_cmp_eq_u32 %[xld], %[xle]\n\t" \
  "s_cselect_b32 %[xld], %[xlb], %[xld]\n\t" \
  "buffer_load_dwordx4 v248, %[xrs], %[xso] offen offset:2048 nt lds\n\t" \
  "s_mov_b64 exec, %[xm]\n\t" \
  "buffer_load_dwordx4 v249, %[xrs], %[xso] offen offset:3072 nt lds\n\t" \
  "s_mov_b64 exec, -1\n\t" \
  "s_setprio 1\n\t" \
  "v_cvt_pk_bf16_f32 v230, v36, v37\n\t" \
  "v_cvt_pk_bf16_f32 v231, v38, v39\n\t" \
  "v_fma_f32 v40, v40, v238, v239\n\t" \
  "v_fma_f32 v41, v41, v238, v239\n\t" \
  "v_fma_f32 v42, v42, v238, v239\n\t" \
  "v_fma_f32 v43, v43, v238, v239\n\t" \
  "v_cvt_pk_bf16_f32 v232, v40, v41\n\t" \
  "v_cvt_pk_bf16_f32 v233, v42, v43\n\t" \
  "v_fma_f32 v44, v44, v238, v239\n\t" \
  "v_fma_f32 v45, v45, v238, v239\n\t" \
  "v_fma_f32 v46, v46, v238, v239\n\t" \
  "v_fma_f32 v47, v47, v238, v239\n\t" \
  "v_cvt_pk_bf16_f32 v234, v44, v45\n\t" \
  "v_cvt_pk_bf16_f32 v235, v46, v47\n\t" \
  "v_mul_f32 v237, v48, v48\n\t" \
  "v_add_f32 v236, v48, v49\n\t" \
  "v_fmac_f32 v237, v49, v49\n\t" \
  "v_add_f32 v236, v236, v50\n\t" \
  "s_setprio 3\n\t" \
  "v_mfma_f32_32x32x8_bf16 v[32:47], v[252:253], v[252:253], 0\n\t" \
  "s_waitcnt lgkmcnt(4)\n\t" \
  "ds_write_b128 v242, v[228:231] offset:1024\n\t" \
  "ds_read_b64 v[208:209], v241 offset:32\n\t" \
  "v_fmac_f32 v237, v50, v50\n\t" \
  "v_smfmac_f32_16x16x64_bf16 v[56:59], v[140:143], v[192:199], v240\n\t" \
  "ds_read_b64 v[210:211], v241 offset:432\n\t" \
  "v_add_f32 v236, v236, v51\n\t" \
  "v_smfmac_f32_16x16x64_bf16 v[80:83], v[136:139], v[192:199], v240\n\t" \
  "ds_read_b64 v[212:213], v241 offset:832\n\t" \
  "v_fmac_f32 v237, v51, v51\n\t" \
  "v_smfmac_f32_16x16x64_bf16 v[8:11], v[132:135], v[192:199], v240\n\t" \
  "ds_read_b64 v[214:215], v241 offset:1232\n\t" \
  "v_add_f32 v236, v236, v52\n\t" \
  "v_smfmac_f32_16x16x64_bf16 v[32:35], v[128:131], v[192:199], v240\n\t" \
  "s_waitcnt lgkmcnt(5)\n\t" \
  "ds_write_b128 v242, v[232:235] offset:2048\n\t" \
  "v_fmac_f32 v237, v52, v52\n\t" \
  "v_smfmac_f32_16x16x64_bf16 v[60:63], v[156:159], v[200:207], v240\n\t" \
  "ds_read_b64 v[216:217], v241 offset:40\n\t" \
  "v_add_f32 v236, v236, v53\n\t" \
  "v_smfmac_f32_16x16x64_bf16 v[84:87], v[152:155], v[200:207], v240\n\t" \
  "ds_read_b64 v[218:219], v241 offset:440\n\t" \
  "v_fmac_f32 v237, v53, v53\n\t" \
  "v_smfmac_f32_16x16x64_bf16 v[12:15], v[148:151], v[200:207], v240\n\t" \
  "ds_read_b64 v[220:221], v241 offset:840\n\t" \
  "v_add_f32 v236, v236, v54\n\t" \
  "v_smfmac_f32_16x16x64_bf16 v[36:39], v[144:147], v[200:207], v240\n\t" \
  "ds_read_b64 v[222:223], v241 offset:1240\n\t" \
  "v_fmac_f32 v237, v54, v54\n\t" \
  "s_setprio 1\n\t" \
  "s_setprio 3\n\t" \
  "s_add_u32 %[cs], %[cs], 1\n\t" \
  "s_cmp_eq_u32 %[cs], 6\n\t" \
  "s_cselect_b32 %[cs], 0, %[cs]\n\t" \
  "s_cselect_b32 %[t0], -75920, %[ps]\n\t" \
  "v_add_u32 v241, %[t0], v241\n\t" \
  "s_waitcnt vmcnt(7)\n\t" \
  "s_waitcnt lgkmcnt(4)\n\t" \
  "s_barrier\n\t" \
  "v_smfmac_f32_16x16x64_bf16 v[64:67], v[172:175], v[208:215], v240\n\t" \
  "ds_read_b64 v[192:193], v241 offset:0\n\t" \
  "v_add_f32 v236, v236, v55\n\t" \
  "v_smfmac_f32_16x16x64_bf16 v[88:91], v[168:171], v[208:215], v240\n\t" \
  "ds_read_b64 v[194:195], v241 offset:400\n\t" \
  "v_fmac_f32 v237, v55, v55\n\t" \
  "v_smfmac_f32_16x16x64_bf16 v[16:19], v[164:167], v[208:215], v240\n\t" \
  "ds_read_b64 v[196:197], v241 offset:800\n\t" \
  "v_add_f32 v236, v236, v56\n\t" \
  "v_smfmac_f32_16x16x64_bf16 v[40:43], v[160:163], v[208:215], v240\n\t" \
  "s_waitcnt lgkmcnt(3)\n\t" \
  "ds_read_b64 v[198:199], v241 offset:1200\n\t" \
  "v_fmac_f32 v237, v56, v56\n\t" \
  "v_smfmac_f32_16x16x64_bf16 v[68:71], v[188:191], v[216:223], v240\n\t" \
  "ds_read_b64 v[200:201], v241 offset:8\n\t" \
  "v_add_f32 v236, v236, v57\n\t" \
  "v_smfmac_f32_16x16x64_bf16 v[92:95], v[184:187], v[216:223], v240\n\t" \
  "ds_read_b64 v[202:203], v241 offset:408\n\t" \
  "v_fmac_f32 v237, v57, v57\n\t" \
  "v_smfmac_f32_16x16x64_bf16 v[20:23], v[180:183], v[216:223], v240\n\t" \
  "ds_read_b64 v[204:205], v241 offset:808\n\t" \
  "v_add_f32 v236, v236, v58\n\t" \
  "v_smfmac_f32_16x16x64_bf16 v[44:47], v[176:179], v[216:223], v240\n\t" \
  "ds_read_b64 v[206:207], v241 offset:1208\n\t" \
  "v_fmac_f32 v237, v58, v58\n\t" \
  "s_setprio 1\n\t" \
  "s_setprio 3\n\t" \
  "s_mov_b32 m0, %[wld]\n\t" \
  "s_add_u32 %[t1], %[wso], %[wpd]\n\t" \
  "buffer_load_dwordx4 v245, %[wrs], %[wso] offen lds\n\t" \
  "buffer_load_dwordx4 v245, %[wrs], %[t1] offen offset:1024 lds\n\t" \
  "s_add_u32 %[t1], %[t1], %[wpd]\n\t" \
  "s_add_u32 %[wso], %[wso], %[wcs]\n\t" \
  "buffer_load_dwordx4 v245, %[wrs], %[t1] offen offset:2048 lds\n\t" \
  "s_add_u32 %[wld], %[wld], %[wla]\n\t" \
  "s_setprio 1\n\t" \
  "v_add_f32 v236, v236, v59\n\t" \
  "v_fmac_f32 v237, v59, v59\n\t" \
  "v_add_f32 v236, v236, v60\n\t" \
  "v_fmac_f32 v237, v60, v60\n\t" \
  "v_add_f32 v236, v236, v61\n\t" \
  "v_fmac_f32 v237, v61, v61\n\t" \
  "v_add_f32 v236, v236, v62\n\t" \
  "v_fmac_f32 v237, v62, v62\n\t" \
  "v_add_f32 v236, v236, v63\n\t" \
  "v_fmac_f32 v237, v63, v63\n\t" \
  "v_add_f32 v236, v236, v64\n\t" \
  "v_fmac_f32 v237, v64, v64\n\t" \
  "v_add_f32 v236, v236, v65\n\t" \
  "v_fmac_f32 v237, v65, v65\n\t" \
  "v_add_f32 v236, v236, v66\n\t" \
  "v_fmac_f32 v237, v66, v66\n\t" \
  "v_add_f32 v236, v236, v67\n\t" \
  "v_fmac_f32 v237, v67, v67\n\t" \
  "v_add_f32 v236, v236, v68\n\t" \
  "v_fmac_f32 v237, v68, v68\n\t" \
  "v_add_f32 v236, v236, v69\n\t" \
  "v_fmac_f32 v237, v69, v69\n\t" \
  "v_add_f32 v236, v236, v70\n\t" \
  "v_fmac_f32 v237, v70, v70\n\t" \
  "v_add_f32 v236, v236, v71\n\t" \
  "v_fmac_f32 v237, v71, v71\n\t" \
  "s_setprio 3\n\t" \
  "ds_write_b64 v243, v[236:237]\n\t" \
  "s_waitcnt vmcnt(7)\n\t" \
  "s_waitcnt lgkmcnt(0)\n\t" \
  "s_barrier\n\t" \
  "ds_read_b128 v[224:227], v244\n\t" \
  "ds_read_b128 v[228:231], v244 offset:1024\n\t" \
  "s_waitcnt lgkmcnt(0)\n\t" \
  "s_setprio 1\n\t" \
  "v_add_f32 v224, v224, v226\n\t" \
  "v_add_f32 v225, v225, v227\n\t" \
  "v_add_f32 v228, v228, v230\n\t" \
  "v_add_f32 v229, v229, v231\n\t" \
  "v_add_f32 v224, v224, v228\n\t" \
  "v_add_f32 v225, v225, v229\n\t" \
  "v_mul_f32 v224, 0x3c2aaaab, v224\n\t" \
  "v_mul_f32 v225, 0x3c2aaaab, v225\n\t" \
  "v_fma_f32 v225, -v224, v224, v225\n\t" \
  "v_max_f32 v225, 0, v225\n\t" \
  "v_add_f32 v225, 0x3727c5ac, v225\n\t" \
  "v_rsq_f32 v225, v225\n\t" \
  "s_nop 0\n\t" \
  "v_mul_f32 v238, %[gl], v225\n\t" \
  "v_mul_f32 v239, -v224, v238\n\t" \
  "v_fma_f32 v48, v48, v238, v239\n\t" \
  "v_fma_f32 v49, v49, v238, v239\n\t" \
  "v_fma_f32 v50, v50, v238, v239\n\t" \
  "v_fma_f32 v51, v51, v238, v239\n\t" \
  "v_cvt_pk_bf16_f32 v224, v48, v49\n\t" \
  "v_cvt_pk_bf16_f32 v225, v50, v51\n\t" \
  "v_fma_f32 v52, v52, v238, v239\n\t" \
  "v_fma_f32 v53, v53, v238, v239\n\t" \
  "v_fma_f32 v54, v54, v238, v239\n\t" \
  "v_fma_f32 v55, v55, v238, v239\n\t" \
  "v_cvt_pk_bf16_f32 v226, v52, v53\n\t" \
  "v_cvt_pk_bf16_f32 v227, v54, v55\n\t" \
  "s_setprio 3\n\t" \
  "v_mfma_f32_16x16x16_bf16 v[48:51], v[252:253], v[252:253], 0\n\t" \
  "ds_write_b128 v242, v[224:227] offset:0\n\t" \
  "v_fma_f32 v56, v56, v238, v239\n\t" \
  "v_mfma_f32_16x16x16_bf16 v[52:55], v[252:253], v[252:253], 0\n\t" \
  "ds_read_b64 v[208:209], v241 offset:16\n\t" \
  "v_fma_f32 v57, v57, v238, v239\n\t" \
  "v_smfmac_f32_16x16x64_bf16 v[72:75], v[108:111], v[192:199], v240\n\t" \
  "ds_read_b64 v[210:211], v241 offset:416\n\t" \
  "v_fma_f32 v58, v58, v238, v239\n\t" \
  "v_smfmac_f32_16x16x64_bf16 v[0:3], v[104:107], v[192:199], v240\n\t" \
  "ds_read_b64 v[212:213], v241 offset:816\n\t" \
  "v_fma_f32 v59, v59, v238, v239\n\t" \
  "v_smfmac_f32_16x16x64_bf16 v[24:27], v[100:103], v[192:199], v240\n\t" \
  "ds_read_b64 v[214:215], v241 offset:1216\n\t" \
  "v_cvt_pk_bf16_f32 v228, v56, v57\n\t" \
  "v_smfmac_f32_16x16x64_bf16 v[48:51], v[96:99], v[192:199], v240\n\t" \
  "ds_read_b64 v[216:217], v241 offset:24\n\t" \
  "v_cvt_pk_bf16_f32 v229, v58, v59\n\t" \
  "v_smfmac_f32_16x16x64_bf16 v[76:79], v[124:127], v[200:207], v240\n\t" \
  "ds_read_b64 v[218:219], v241 offset:424\n\t" \
  "v_fma_f32 v60, v60, v238, v239\n\t" \
  "v_smfmac_f32_16x16x64_bf16 v[4:7], v[120:123], v[200:207], v240\n\t" \
  "ds_read_b64 v[220:221], v241 offset:824\n\t" \
  "v_fma_f32 v61, v61, v238, v239\n\t" \
  "v_smfmac_f32_16x16x64_bf16 v[28:31], v[116:119], v[200:207], v240\n\t" \
  "ds_read_b64 v[222:223], v241 offset:1224\n\t" \
  "v_fma_f32 v62, v62, v238, v239\n\t" \
  "v_smfmac_f32_16x16x64_bf16 v[52:55], v[112:115], v[200:207], v240\n\t" \
  "v_fma_f32 v63, v63, v238, v239\n\t" \
  "s_setprio 1\n\t" \
  "s_setprio 3\n\t" \
  "s_mov_b32 m0, %[wld]\n\t" \
  "s_add_u32 %[t1], %[wso], %[wpd]\n\t" \
  "buffer_load_dwordx4 v245, %[wrs], %[wso] offen lds\n\t" \
  "buffer_load_dwordx4 v245, %[wrs], %[t1] offen offset:1024 lds\n\t" \
  "s_add_u32 %[t1], %[t1], %[wpd]\n\t" \
  "s_add_u32 %[wso], %[wso], %[wcs]\n\t" \
  "buffer_load_dwordx4 v245, %[wrs], %[t1] offen offset:2048 lds\n\t" \
  "s_add_u32 %[wld], %[wld], %[wst]\n\t" \
  "s_setprio 1\n\t" \
  "s_setprio 3\n\t" \
  "s_waitcnt vmcnt(3)\n\t" \
  "s_barrier\n\t" \
  "s_mov_b32 m0, %[wld]\n\t" \
  "s_add_u32 %[t1], %[wso], %[wpd]\n\t" \
  "buffer_load_dwordx4 v245, %[wrs], %[wso] offen lds\n\t" \
  "buffer_load_dwordx4 v245, %[wrs], %[t1] offen offset:1024 lds\n\t" \
  "s_add_u32 %[t1], %[t1], %[wpd]\n\t" \
  "s_add_u32 %[wso], %[wso], %[wcs]\n\t" \
  "buffer_load_dwordx4 v245, %[wrs], %[t1] offen offset:2048 lds\n\t" \
  "s_add_u32 %[wld], %[wld], %[wst]\n\t" \
  "s_mov_b32 m0, %[xld]\n\t" \
  "s_cmp_lt_u32 %[gy], 0x3000000\n\t" \
  "s_cselect_b32 %[xso], %[gy], 0x40000000\n\t" \
  "buffer_load_dwordx4 v246, %[xrs], %[xso] offen offset:0 nt lds\n\t" \
  "s_add_u32 %[gy], %[gy], %[dgy]\n\t" \
  "buffer_load_dwordx4 v247, %[xrs], %[xso] offen offset:1024 nt lds\n\t" \
  "s_add_u32 %[xld], %[xld], 15184\n\t" \
  "s_cmp_eq_u32 %[xld], %[xle]\n\t" \
  "s_cselect_b32 %[xld], %[xlb], %[xld]\n\t" \
  "buffer_load_dwordx4 v248, %[xrs], %[xso] offen offset:2048 nt lds\n\t" \
  "s_mov_b64 exec, %[xm]\n\t" \
  "buffer_load_dwordx4 v249, %[xrs], %[xso] offen offset:3072 nt lds\n\t" \
  "s_mov_b64 exec, -1\n\t" \
  "s_setprio 1\n\t" \
  "v_cvt_pk_bf16_f32 v230, v60, v61\n\t" \
  "v_cvt_pk_bf16_f32 v231, v62, v63\n\t" \
  "v_fma_f32 v64, v64, v238, v239\n\t" \
  "v_fma_f32 v65, v65, v238, v239\n\t" \
  "v_fma_f32 v66, v66, v238, v239\n\t" \
  "v_fma_f32 v67, v67, v238, v239\n\t" \
  "v_cvt_pk_bf16_f32 v232, v64, v65\n\t" \
  "v_cvt_pk_bf16_f32 v233, v66, v67\n\t" \
  "v_fma_f32 v68, v68, v238, v239\n\t" \
  "v_fma_f32 v69, v69, v238, v239\n\t" \
  "v_fma_f32 v70, v70, v238, v239\n\t" \
  "v_fma_f32 v71, v71, v238, v239\n\t" \
  "v_cvt_pk_bf16_f32 v234, v68, v69\n\t" \
  "v_cvt_pk_bf16_f32 v235, v70, v71\n\t" \
  "v_mul_f32 v237, v72, v72\n\t" \
  "v_add_f32 v236, v72, v73\n\t" \
  "v_fmac_f32 v237, v73, v73\n\t" \
  "v_add_f32 v236, v236, v74\n\t" \
  "s_setprio 3\n\t" \
  "v_mfma_f32_32x32x8_bf16 v[56:71], v[252:253], v[252:253], 0\n\t" \
  "s_waitcnt lgkmcnt(4)\n\t" \
  "ds_write_b128 v242, v[228:231] offset:1024\n\t" \
  "ds_read_b64 v[192:193], v241 offset:32\n\t" \
  "v_fmac_f32 v237, v74, v74\n\t" \
  "v_smfmac_f32_16x16x64_bf16 v[80:83], v[140:143], v[208:215], v240\n\t" \
  "ds_read_b64 v[194:195], v241 offset:432\n\t" \
  "v_add_f32 v236, v236, v75\n\t" \
  "v_smfmac_f32_16x16x64_bf16 v[8:11], v[136:139], v[208:215], v240\n\t" \
  "ds_read_b64 v[196:197], v241 offset:832\n\t" \
  "v_fmac_f32 v237, v75, v75\n\t" \
  "v_smfmac_f32_16x16x64_bf16 v[32:35], v[132:135], v[208:215], v240\n\t" \
  "ds_read_b64 v[198:199], v241 offset:1232\n\t" \
  "v_add_f32 v236, v236, v76\n\t" \
  "v_smfmac_f32_16x16x64_bf16 v[56:59], v[128:131], v[208:215], v240\n\t" \
  "s_waitcnt lgkmcnt(5)\n\t" \
  "ds_write_b128 v242, v[232:235] offset:2048\n\t" \
  "v_fmac_f32 v237, v76, v76\n\t" \
  "v_smfmac_f32_16x16x64_bf16 v[84:87], v[156:159], v[216:223], v240\n\t" \
  "ds_read_b64 v[200:201], v241 offset:40\n\t" \
  "v_add_f32 v236, v236, v77\n\t" \
  "v_smfmac_f32_16x16x64_bf16 v[12:15], v[152:155], v[216:223], v240\n\t" \
  "ds_read_b64 v[202:203], v241 offset:440\n\t" \
  "v_fmac_f32 v237, v77, v77\n\t" \
  "v_smfmac_f32_16x16x64_bf16 v[36:39], v[148:151], v[216:223], v240\n\t" \
  "ds_read_b64 v[204:205], v241 offset:840\n\t" \
  "v_add_f32 v236, v236, v78\n\t" \
  "v_smfmac_f32_16x16x64_bf16 v[60:63], v[144:147], v[216:223], v240\n\t" \
  "ds_read_b64 v[206:207], v241 offset:1240\n\t" \
  "v_fmac_f32 v237, v78, v78\n\t" \
  "s_setprio 1\n\t" \
  "s_setprio 3\n\t" \
  "s_add_u32 %[cs], %[cs], 1\n\t" \
  "s_cmp_eq_u32 %[cs], 6\n\t" \
  "s_cselect_b32 %[cs], 0, %[cs]\n\t" \
  "s_cselect_b32 %[t0], -75920, %[ps]\n\t" \
  "v_add_u32 v241, %[t0], v241\n\t" \
  "s_waitcnt vmcnt(7)\n\t" \
  "s_waitcnt lgkmcnt(4)\n\t" \
  "s_barrier\n\t" \
  "v_smfmac_f32_16x16x64_bf16 v[88:91], v[172:175], v[192:199], v240\n\t" \
  "ds_read_b64 v[208:209], v241 offset:0\n\t" \
  "v_add_f32 v236, v236, v79\n\t" \
  "v_smfmac_f32_16x16x64_bf16 v[16:19], v[168:171], v[192:199], v240\n\t" \
  "ds_read_b64 v[210:211], v241 offset:400\n\t" \
  "v_fmac_f32 v237, v79, v79\n\t" \
  "v_smfmac_f32_16x16x64_bf16 v[40:43], v[164:167], v[192:199], v240\n\t" \
  "ds_read_b64 v[212:213], v241 offset:800\n\t" \
  "v_add_f32 v236, v236, v80\n\t" \
  "v_smfmac_f32_16x16x64_bf16 v[64:67], v[160:163], v[192:199], v240\n\t" \
  "s_waitcnt lgkmcnt(3)\n\t" \
  "ds_read_b64 v[214:215], v241 offset:1200\n\t" \
  "v_fmac_f32 v237, v80, v80\n\t" \
  "v_smfmac_f32_16x16x64_bf16 v[92:95], v[188:191], v[200:207], v240\n\t" \
  "ds_read_b64 v[216:217], v241 offset:8\n\t" \
  "v_add_f32 v236, v236, v81\n\t" \
  "v_smfmac_f32_16x16x64_bf16 v[20:23], v[184:187], v[200:207], v240\n\t" \
  "ds_read_b64 v[218:219], v241 offset:408\n\t" \
  "v_fmac_f32 v237, v81, v81\n\t" \
  "v_smfmac_f32_16x16x64_bf16 v[44:47], v[180:183], v[200:207], v240\n\t" \
  "ds_read_b64 v[220:221], v241 offset:808\n\t" \
  "v_add_f32 v236, v236, v82\n\t" \
  "v_smfmac_f32_16x16x64_bf16 v[68:71], v[176:179], v[200:207], v240\n\t" \
  "ds_read_b64 v[222:223], v241 offset:1208\n\t" \
  "v_fmac_f32 v237, v82, v82\n\t" \
  "s_setprio 1\n\t" \
  "s_setprio 3\n\t" \
  "s_mov_b32 m0, %[wld]\n\t" \
  "s_add_u32 %[t1], %[wso], %[wpd]\n\t" \
  "buffer_load_dwordx4 v245, %[wrs], %[wso] offen lds\n\t" \
  "buffer_load_dwordx4 v245, %[wrs], %[t1] offen offset:1024 lds\n\t" \
  "s_add_u32 %[t1], %[t1], %[wpd]\n\t" \
  "s_add_u32 %[wso], %[wso], %[wcs]\n\t" \
  "buffer_load_dwordx4 v245, %[wrs], %[t1] offen offset:2048 lds\n\t" \
  "s_add_u32 %[wld], %[wld], %[wlc]\n\t" \
  "s_setprio 1\n\t" \
  "v_add_f32 v236, v236, v83\n\t" \
  "v_fmac_f32 v237, v83, v83\n\t" \
  "v_add_f32 v236, v236, v84\n\t" \
  "v_fmac_f32 v237, v84, v84\n\t" \
  "v_add_f32 v236, v236, v85\n\t" \
  "v_fmac_f32 v237, v85, v85\n\t" \
  "v_add_f32 v236, v236, v86\n\t" \
  "v_fmac_f32 v237, v86, v86\n\t" \
  "v_add_f32 v236, v236, v87\n\t" \
  "v_fmac_f32 v237, v87, v87\n\t" \
  "v_add_f32 v236, v236, v88\n\t" \
  "v_fmac_f32 v237, v88, v88\n\t" \
  "v_add_f32 v236, v236, v89\n\t" \
  "v_fmac_f32 v237, v89, v89\n\t" \
  "v_add_f32 v236, v236, v90\n\t" \
  "v_fmac_f32 v237, v90, v90\n\t" \
  "v_add_f32 v236, v236, v91\n\t" \
  "v_fmac_f32 v237, v91, v91\n\t" \
  "v_add_f32 v236, v236, v92\n\t" \
  "v_fmac_f32 v237, v92, v92\n\t" \
  "v_add_f32 v236, v236, v93\n\t" \
  "v_fmac_f32 v237, v93, v93\n\t" \
  "v_add_f32 v236, v236, v94\n\t" \
  "v_fmac_f32 v237, v94, v94\n\t" \
  "v_add_f32 v236, v236, v95\n\t" \
  "v_fmac_f32 v237, v95, v95\n\t" \
  "s_setprio 3\n\t" \
  "ds_write_b64 v243, v[236:237]\n\t" \
  "s_waitcnt vmcnt(7)\n\t" \
  "s_waitcnt lgkmcnt(0)\n\t" \
  "s_barrier\n\t" \
  "ds_read_b128 v[224:227], v244\n\t" \
  "ds_read_b128 v[228:231], v244 offset:1024\n\t" \
  "s_waitcnt lgkmcnt(0)\n\t" \
  "s_setprio 1\n\t" \
  "v_add_f32 v224, v224, v226\n\t" \
  "v_add_f32 v225, v225, v227\n\t" \
  "v_add_f32 v228, v228, v230\n\t" \
  "v_add_f32 v229, v229, v231\n\t" \
  "v_add_f32 v224, v224, v228\n\t" \
  "v_add_f32 v225, v225, v229\n\t" \
  "v_mul_f32 v224, 0x3c2aaaab, v224\n\t" \
  "v_mul_f32 v225, 0x3c2aaaab, v225\n\t" \
  "v_fma_f32 v225, -v224, v224, v225\n\t" \
  "v_max_f32 v225, 0, v225\n\t" \
  "v_add_f32 v225, 0x3727c5ac, v225\n\t" \
  "v_rsq_f32 v225, v225\n\t" \
  "s_nop 0\n\t" \
  "v_mul_f32 v238, %[gl], v225\n\t" \
  "v_mul_f32 v239, -v224, v238\n\t" \
  "v_fma_f32 v72, v72, v238, v239\n\t" \
  "v_fma_f32 v73, v73, v238, v239\n\t" \
  "v_fma_f32 v74, v74, v238, v239\n\t" \
  "v_fma_f32 v75, v75, v238, v239\n\t" \
  "v_cvt_pk_bf16_f32 v224, v72, v73\n\t" \
  "v_cvt_pk_bf16_f32 v225, v74, v75\n\t" \
  "v_fma_f32 v76, v76, v238, v239\n\t" \
  "v_fma_f32 v77, v77, v238, v239\n\t" \
  "v_fma_f32 v78, v78, v238, v239\n\t" \
  "v_fma_f32 v79, v79, v238, v239\n\t" \
  "v_cvt_pk_bf16_f32 v226, v76, v77\n\t" \
  "v_cvt_pk_bf16_f32 v227, v78, v79\n\t" \
  "s_setprio 3\n\t" \
  "v_mfma_f32_16x16x16_bf16 v[72:75], v[252:253], v[252:253], 0\n\t" \
  "ds_write_b128 v242, v[224:227] offset:0\n\t" \
  "v_fma_f32 v80, v80, v238, v239\n\t" \
  "v_mfma_f32_16x16x16_bf16 v[76:79], v[252:253], v[252:253], 0\n\t" \
  "ds_read_b64 v[192:193], v241 offset:16\n\t" \
  "v_fma_f32 v81, v81, v238, v239\n\t" \
  "v_smfmac_f32_16x16x64_bf16 v[0:3], v[108:111], v[208:215], v240\n\t" \
  "ds_read_b64 v[194:195], v241 offset:416\n\t" \
  "v_fma_f32 v82, v82, v238, v239\n\t" \
  "v_smfmac_f32_16x16x64_bf16 v[24:27], v[104:107], v[208:215], v240\n\t" \
  "ds_read_b64 v[196:197], v241 offset:816\n\t" \
  "v_fma_f32 v83, v83, v238, v239\n\t" \
  "v_smfmac_f32_16x16x64_bf16 v[48:51], v[100:103], v[208:215], v240\n\t" \
  "ds_read_b64 v[198:199], v241 offset:1216\n\t" \
  "v_cvt_pk_bf16_f32 v228, v80, v81\n\t" \
  "v_smfmac_f32_16x16x64_bf16 v[72:75], v[96:99], v[208:215], v240\n\t" \
  "ds_read_b64 v[200:201], v241 offset:24\n\t" \
  "v_cvt_pk_bf16_f32 v229, v82, v83\n\t" \
  "v_smfmac_f32_16x16x64_bf16 v[4:7], v[124:127], v[216:223], v240\n\t" \
  "ds_read_b64 v[202:203], v241 offset:424\n\t" \
  "v_fma_f32 v84, v84, v238, v239\n\t" \
  "v_smfmac_f32_16x16x64_bf16 v[28:31], v[120:123], v[216:223], v240\n\t" \
  "ds_read_b64 v[204:205], v241 offset:824\n\t" \
  "v_fma_f32 v85, v85, v238, v239\n\t" \
  "v_smfmac_f32_16x16x64_bf16 v[52:55], v[116:119], v[216:223], v240\n\t" \
  "ds_read_b64 v[206:207], v241 offset:1224\n\t" \
  "v_fma_f32 v86, v86, v238, v239\n\t" \
  "v_smfmac_f32_16x16x64_bf16 v[76:79], v[112:115], v[216:223], v240\n\t" \
  "v_fma_f32 v87, v87, v238, v239\n\t" \
  "s_setprio 1\n\t" \
  "s_setprio 3\n\t" \
  "s_mov_b32 m0, %[wld]\n\t" \
  "s_add_u32 %[t1], %[wso], %[wpd]\n\t" \
  "buffer_load_dwordx4 v245, %[wrs], %[wso] offen lds\n\t" \
  "buffer_load_dwordx4 v245, %[wrs], %[t1] offen offset:1024 lds\n\t" \
  "s_add_u32 %[t1], %[t1], %[wpd]\n\t" \
  "s_add_u32 %[wso], %[wso], %[wcs]\n\t" \
  "buffer_load_dwordx4 v245, %[wrs], %[t1] offen offset:2048 lds\n\t" \
  "s_add_u32 %[wld], %[wld], %[wst]\n\t" \
  "s_setprio 1\n\t" \
  "s_setprio 3\n\t" \
  "s_waitcnt vmcnt(3)\n\t" \
  "s_barrier\n\t" \
  "s_mov_b32 m0, %[wld]\n\t" \
  "s_add_u32 %[t1], %[wso], %[wpd]\n\t" \
  "buffer_load_dwordx4 v245, %[wrs], %[wso] offen lds\n\t" \
  "buffer_load_dwordx4 v245, %[wrs], %[t1] offen offset:1024 lds\n\t" \
  "s_add_u32 %[t1], %[t1], %[wpd]\n\t" \
  "s_add_u32 %[wso], %[wso], %[wcs]\n\t" \
  "buffer_load_dwordx4 v245, %[wrs], %[t1] offen offset:2048 lds\n\t" \
  "s_add_u32 %[wld], %[wld], %[wst]\n\t" \
  "s_mov_b32 m0, %[xld]\n\t" \
  "s_cmp_lt_u32 %[gy], 0x3000000\n\t" \
  "s_cselect_b32 %[xso], %[gy], 0x40000000\n\t" \
  "buffer_load_dwordx4 v246, %[xrs], %[xso] offen offset:0 nt lds\n\t" \
  "s_add_u32 %[gy], %[gy], %[dgy]\n\t" \
  "buffer_load_dwordx4 v247, %[xrs], %[xso] offen offset:1024 nt lds\n\t" \
  "s_add_u32 %[xld], %[xld], 15184\n\t" \
  "s_cmp_eq_u32 %[xld], %[xle]\n\t" \
  "s_cselect_b32 %[xld], %[xlb], %[xld]\n\t" \
  "buffer_load_dwordx4 v248, %[xrs], %[xso] offen offset:2048 nt lds\n\t" \
  "s_mov_b64 exec, %[xm]\n\t" \
  "buffer_load_dwordx4 v249, %[xrs], %[xso] offen offset:3072 nt lds\n\t" \
  "s_mov_b64 exec, -1\n\t" \
  "s_setprio 1\n\t" \
  "v_cvt_pk_bf16_f32 v230, v84, v85\n\t" \
  "v_cvt_pk_bf16_f32 v231, v86, v87\n\t" \
  "v_fma_f32 v88, v88, v238, v239\n\t" \
  "v_fma_f32 v89, v89, v238, v239\n\t" \
  "v_fma_f32 v90, v90, v238, v239\n\t" \
  "v_fma_f32 v91, v91, v238, v239\n\t" \
  "v_cvt_pk_bf16_f32 v232, v88, v89\n\t" \
  "v_cvt_pk_bf16_f32 v233, v90, v91\n\t" \
  "v_fma_f32 v92, v92, v238, v239\n\t" \
  "v_fma_f32 v93, v93, v238, v239\n\t" \
  "v_fma_f32 v94, v94, v238, v239\n\t" \
  "v_fma_f32 v95, v95, v238, v239\n\t" \
  "v_cvt_pk_bf16_f32 v234, v92, v93\n\t" \
  "v_cvt_pk_bf16_f32 v235, v94, v95\n\t" \
  "v_mul_f32 v237, v0, v0\n\t" \
  "v_add_f32 v236, v0, v1\n\t" \
  "v_fmac_f32 v237, v1, v1\n\t" \
  "v_add_f32 v236, v236, v2\n\t" \
  "s_setprio 3\n\t" \
  "v_mfma_f32_32x32x8_bf16 v[80:95], v[252:253], v[252:253], 0\n\t" \
  "s_waitcnt lgkmcnt(4)\n\t" \
  "ds_write_b128 v242, v[228:231] offset:1024\n\t" \
  "ds_read_b64 v[208:209], v241 offset:32\n\t" \
  "v_fmac_f32 v237, v2, v2\n\t" \
  "v_smfmac_f32_16x16x64_bf16 v[8:11], v[140:143], v[192:199], v240\n\t" \
  "ds_read_b64 v[210:211], v241 offset:432\n\t" \
  "v_add_f32 v236, v236, v3\n\t" \
  "v_smfmac_f32_16x16x64_bf16 v[32:35], v[136:139], v[192:199], v240\n\t" \
  "ds_read_b64 v[212:213], v241 offset:832\n\t" \
  "v_fmac_f32 v237, v3, v3\n\t" \
  "v_smfmac_f32_16x16x64_bf16 v[56:59], v[132:135], v[192:199], v240\n\t" \
  "ds_read_b64 v[214:215], v241 offset:1232\n\t" \
  "v_add_f32 v236, v236, v4\n\t" \
  "v_smfmac_f32_16x16x64_bf16 v[80:83], v[128:131], v[192:199], v240\n\t" \
  "s_waitcnt lgkmcnt(5)\n\t" \
  "ds_write_b128 v242, v[232:235] offset:2048\n\t" \
  "v_fmac_f32 v237, v4, v4\n\t" \
  "v_smfmac_f32_16x16x64_bf16 v[12:15], v[156:159], v[200:207], v240\n\t" \
  "ds_read_b64 v[216:217], v241 offset:40\n\t" \
  "v_add_f32 v236, v236, v5\n\t" \
  "v_smfmac_f32_16x16x64_bf16 v[36:39], v[152:155], v[200:207], v240\n\t" \
  "ds_read_b64 v[218:219], v241 offset:440\n\t" \
  "v_fmac_f32 v237, v5, v5\n\t" \
  "v_smfmac_f32_16x16x64_bf16 v[60:63], v[148:151], v[200:207], v240\n\t" \
  "ds_read_b64 v[220:221], v241 offset:840\n\t" \
  "v_add_f32 v236, v236, v6\n\t" \
  "v_smfmac_f32_16x16x64_bf16 v[84:87], v[144:147], v[200:207], v240\n\t" \
  "ds_read_b64 v[222:223], v241 offset:1240\n\t" \
  "v_fmac_f32 v237, v6, v6\n\t" \
  "s_setprio 1\n\t" \
  "s_setprio 3\n\t" \
  "s_add_u32 %[cs], %[cs], 1\n\t" \
  "s_cmp_eq_u32 %[cs], 6\n\t" \
  "s_cselect_b32 %[cs], 0, %[cs]\n\t" \
  "s_cselect_b32 %[t0], -75920, %[ps]\n\t" \
  "v_add_u32 v241, %[t0], v241\n\t" \
  "s_waitcnt vmcnt(7)\n\t" \
  "s_waitcnt lgkmcnt(4)\n\t" \
  "s_barrier\n\t" \
  "v_smfmac_f32_16x16x64_bf16 v[16:19], v[172:175], v[208:215], v240\n\t" \
  "ds_read_b64 v[192:193], v241 offset:0\n\t" \
  "v_add_f32 v236, v236, v7\n\t" \
  "v_smfmac_f32_16x16x64_bf16 v[40:43], v[168:171], v[208:215], v240\n\t" \
  "ds_read_b64 v[194:195], v241 offset:400\n\t" \
  "v_fmac_f32 v237, v7, v7\n\t" \
  "v_smfmac_f32_16x16x64_bf16 v[64:67], v[164:167], v[208:215], v240\n\t" \
  "ds_read_b64 v[196:197], v241 offset:800\n\t" \
  "v_add_f32 v236, v236, v8\n\t" \
  "v_smfmac_f32_16x16x64_bf16 v[88:91], v[160:163], v[208:215], v240\n\t" \
  "s_waitcnt lgkmcnt(3)\n\t" \
  "ds_read_b64 v[198:199], v241 offset:1200\n\t" \
  "v_fmac_f32 v237, v8, v8\n\t" \
  "v_smfmac_f32_16x16x64_bf16 v[20:23], v[188:191], v[216:223], v240\n\t" \
  "ds_read_b64 v[200:201], v241 offset:8\n\t" \
  "v_add_f32 v236, v236, v9\n\t" \
  "v_smfmac_f32_16x16x64_bf16 v[44:47], v[184:187], v[216:223], v240\n\t" \
  "ds_read_b64 v[202:203], v241 offset:408\n\t" \
  "v_fmac_f32 v237, v9, v9\n\t" \
  "v_smfmac_f32_16x16x64_bf16 v[68:71], v[180:183], v[216:223], v240\n\t" \
  "ds_read_b64 v[204:205], v241 offset:808\n\t" \
  "v_add_f32 v236, v236, v10\n\t" \
  "v_smfmac_f32_16x16x64_bf16 v[92:95], v[176:179], v[216:223], v240\n\t" \
  "ds_read_b64 v[206:207], v241 offset:1208\n\t" \
  "v_fmac_f32 v237, v10, v10\n\t" \
  "s_setprio 1\n\t" \
  "s_setprio 3\n\t" \
  "s_mov_b32 m0, %[wld]\n\t" \
  "s_add_u32 %[t1], %[wso], %[wpd]\n\t" \
  "buffer_load_dwordx4 v245, %[wrs], %[wso] offen lds\n\t" \
  "buffer_load_dwordx4 v245, %[wrs], %[t1] offen offset:1024 lds\n\t" \
  "s_add_u32 %[t1], %[t1], %[wpd]\n\t" \
  "s_add_u32 %[wso], %[wso], %[wsa]\n\t" \
  "buffer_load_dwordx4 v245, %[wrs], %[t1] offen offset:2048 lds\n\t" \
  "s_add_u32 %[wld], %[wld], %[wla]\n\t" \
  "s_setprio 1\n\t" \
  "v_add_f32 v236, v236, v11\n\t" \
  "v_fmac_f32 v237, v11, v11\n\t" \
  "v_add_f32 v236, v236, v12\n\t" \
  "v_fmac_f32 v237, v12, v12\n\t" \
  "v_add_f32 v236, v236, v13\n\t" \
  "v_fmac_f32 v237, v13, v13\n\t" \
  "v_add_f32 v236, v236, v14\n\t" \
  "v_fmac_f32 v237, v14, v14\n\t" \
  "v_add_f32 v236, v236, v15\n\t" \
  "v_fmac_f32 v237, v15, v15\n\t" \
  "v_add_f32 v236, v236, v16\n\t" \
  "v_fmac_f32 v237, v16, v16\n\t" \
  "v_add_f32 v236, v236, v17\n\t" \
  "v_fmac_f32 v237, v17, v17\n\t" \
  "v_add_f32 v236, v236, v18\n\t" \
  "v_fmac_f32 v237, v18, v18\n\t" \
  "v_add_f32 v236, v236, v19\n\t" \
  "v_fmac_f32 v237, v19, v19\n\t" \
  "v_add_f32 v236, v236, v20\n\t" \
  "v_fmac_f32 v237, v20, v20\n\t" \
  "v_add_f32 v236, v236, v21\n\t" \
  "v_fmac_f32 v237, v21, v21\n\t" \
  "v_add_f32 v236, v236, v22\n\t" \
  "v_fmac_f32 v237, v22, v22\n\t" \
  "v_add_f32 v236, v236, v23\n\t" \
  "v_fmac_f32 v237, v23, v23\n\t" \
  "s_setprio 3\n\t" \
  "ds_write_b64 v243, v[236:237]\n\t" \
  "s_waitcnt vmcnt(7)\n\t" \
  "s_waitcnt lgkmcnt(0)\n\t" \
  "s_barrier\n\t" \
  "ds_read_b128 v[224:227], v244\n\t" \
  "ds_read_b128 v[228:231], v244 offset:1024\n\t" \
  "s_waitcnt lgkmcnt(0)\n\t" \
  "s_setprio 1\n\t" \
  "v_add_f32 v224, v224, v226\n\t" \
  "v_add_f32 v225, v225, v227\n\t" \
  "v_add_f32 v228, v228, v230\n\t" \
  "v_add_f32 v229, v229, v231\n\t" \
  "v_add_f32 v224, v224, v228\n\t" \
  "v_add_f32 v225, v225, v229\n\t" \
  "v_mul_f32 v224, 0x3c2aaaab, v224\n\t" \
  "v_mul_f32 v225, 0x3c2aaaab, v225\n\t" \
  "v_fma_f32 v225, -v224, v224, v225\n\t" \
  "v_max_f32 v225, 0, v225\n\t" \
  "v_add_f32 v225, 0x3727c5ac, v225\n\t" \
  "v_rsq_f32 v225, v225\n\t" \
  "s_nop 0\n\t" \
  "v_mul_f32 v238, %[gl], v225\n\t" \
  "v_mul_f32 v239, -v224, v238\n\t" \
  "v_fma_f32 v0, v0, v238, v239\n\t" \
  "v_fma_f32 v1, v1, v238, v239\n\t" \
  "v_fma_f32 v2, v2, v238, v239\n\t" \
  "v_fma_f32 v3, v3, v238, v239\n\t" \
  "v_cvt_pk_bf16_f32 v224, v0, v1\n\t" \
  "v_cvt_pk_bf16_f32 v225, v2, v3\n\t" \
  "v_fma_f32 v4, v4, v238, v239\n\t" \
  "v_fma_f32 v5, v5, v238, v239\n\t" \
  "v_fma_f32 v6, v6, v238, v239\n\t" \
  "v_fma_f32 v7, v7, v238, v239\n\t" \
  "v_cvt_pk_bf16_f32 v226, v4, v5\n\t" \
  "v_cvt_pk_bf16_f32 v227, v6, v7\n\t" \
  "s_setprio 3\n\t" \
  "v_mfma_f32_16x16x16_bf16 v[0:3], v[252:253], v[252:253], 0\n\t" \
  "ds_write_b128 v242, v[224:227] offset:0\n\t" \
  "v_fma_f32 v8, v8, v238, v239\n\t" \
  "v_mfma_f32_16x16x16_bf16 v[4:7], v[252:253], v[252:253], 0\n\t" \
  "ds_read_b64 v[208:209], v241 offset:16\n\t" \
  "v_fma_f32 v9, v9, v238, v239\n\t" \
  "v_smfmac_f32_16x16x64_bf16 v[24:27], v[108:111], v[192:199], v240\n\t" \
  "ds_read_b64 v[210:211], v241 offset:416\n\t" \
  "v_fma_f32 v10, v10, v238, v239\n\t" \
  "v_smfmac_f32_16x16x64_bf16 v[48:51], v[104:107], v[192:199], v240\n\t" \
  "ds_read_b64 v[212:213], v241 offset:816\n\t" \
  "v_fma_f32 v11, v11, v238, v239\n\t" \
  "v_smfmac_f32_16x16x64_bf16 v[72:75], v[100:103], v[192:199], v240\n\t" \
  "ds_read_b64 v[214:215], v241 offset:1216\n\t" \
  "v_cvt_pk_bf16_f32 v228, v8, v9\n\t" \
  "v_smfmac_f32_16x16x64_bf16 v[0:3], v[96:99], v[192:199], v240\n\t" \
  "ds_read_b64 v[216:217], v241 offset:24\n\t" \
  "v_cvt_pk_bf16_f32 v229, v10, v11\n\t" \
  "v_smfmac_f32_16x16x64_bf16 v[28:31], v[124:127], v[200:207], v240\n\t" \
  "ds_read_b64 v[218:219], v241 offset:424\n\t" \
  "v_fma_f32 v12, v12, v238, v239\n\t" \
  "v_smfmac_f32_16x16x64_bf16 v[52:55], v[120:123], v[200:207], v240\n\t" \
  "ds_read_b64 v[220:221], v241 offset:824\n\t" \
  "v_fma_f32 v13, v13, v238, v239\n\t" \
  "v_smfmac_f32_16x16x64_bf16 v[76:79], v[116:119], v[200:207], v240\n\t" \
  "ds_read_b64 v[222:223], v241 offset:1224\n\t" \
  "v_fma_f32 v14, v14, v238, v239\n\t" \
  "v_smfmac_f32_16x16x64_bf16 v[4:7], v[112:115], v[200:207], v240\n\t" \
  "v_fma_f32 v15, v15, v238, v239\n\t" \
  "s_setprio 1\n\t" \
  "s_setprio 3\n\t" \
  "s_mov_b32 m0, %[wld]\n\t" \
  "s_add_u32 %[t1], %[wso], %[wpd]\n\t" \
  "buffer_load_dwordx4 v245, %[wrs], %[wso] offen lds\n\t" \
  "buffer_load_dwordx4 v245, %[wrs], %[t1] offen offset:1024 lds\n\t" \
  "s_add_u32 %[t1], %[t1], %[wpd]\n\t" \
  "s_add_u32 %[wso], %[wso], %[wsb]\n\t" \
  "buffer_load_dwordx4 v245, %[wrs], %[t1] offen offset:2048 lds\n\t" \
  "s_add_u32 %[wld], %[wld], %[wlb]\n\t" \
  "s_setprio 1\n\t" \
  "s_setprio 3\n\t" \
  "s_waitcnt vmcnt(3)\n\t" \
  "s_barrier\n\t" \
  "s_mov_b32 m0, %[wld]\n\t" \
  "s_add_u32 %[t1], %[wso], %[wpd]\n\t" \
  "buffer_load_dwordx4 v245, %[wrs], %[wso] offen lds\n\t" \
  "buffer_load_dwordx4 v245, %[wrs], %[t1] offen offset:1024 lds\n\t" \
  "s_add_u32 %[t1], %[t1], %[wpd]\n\t" \
  "s_add_u32 %[wso], %[wso], %[wcs]\n\t" \
  "buffer_load_dwordx4 v245, %[wrs], %[t1] offen offset:2048 lds\n\t" \
  "s_add_u32 %[wld], %[wld], %[wst]\n\t" \
  "s_mov_b32 m0, %[xld]\n\t" \
  "s_cmp_lt_u32 %[gy], 0x3000000\n\t" \
  "s_cselect_b32 %[xso], %[gy], 0x40000000\n\t" \
  "buffer_load_dwordx4 v246, %[xrs], %[xso] offen offset:0 nt lds\n\t" \
  "s_add_u32 %[gy], %[gy], %[dgy]\n\t" \
  "buffer_load_dwordx4 v247, %[xrs], %[xso] offen offset:1024 nt lds\n\t" \
  "s_add_u32 %[xld], %[xld], 15184\n\t" \
  "s_cmp_eq_u32 %[xld], %[xle]\n\t" \
  "s_cselect_b32 %[xld], %[xlb], %[xld]\n\t" \
  "buffer_load_dwordx4 v248, %[xrs], %[xso] offen offset:2048 nt lds\n\t" \
  "s_mov_b64 exec, %[xm]\n\t" \
  "buffer_load_dwordx4 v249, %[xrs], %[xso] offen offset:3072 nt lds\n\t" \
  "s_mov_b64 exec, -1\n\t" \
  "s_setprio 1\n\t" \
  "v_cvt_pk_bf16_f32 v230, v12, v13\n\t" \
  "v_cvt_pk_bf16_f32 v231, v14, v15\n\t" \
  "v_fma_f32 v16, v16, v238, v239\n\t" \
  "v_fma_f32 v17, v17, v238, v239\n\t" \
  "v_fma_f32 v18, v18, v238, v239\n\t" \
  "v_fma_f32 v19, v19, v238, v239\n\t" \
  "v_cvt_pk_bf16_f32 v232, v16, v17\n\t" \
  "v_cvt_pk_bf16_f32 v233, v18, v19\n\t" \
  "v_fma_f32 v20, v20, v238, v239\n\t" \
  "v_fma_f32 v21, v21, v238, v239\n\t" \
  "v_fma_f32 v22, v22, v238, v239\n\t" \
  "v_fma_f32 v23, v23, v238, v239\n\t" \
  "v_cvt_pk_bf16_f32 v234, v20, v21\n\t" \
  "v_cvt_pk_bf16_f32 v235, v22, v23\n\t" \
  "v_mul_f32 v237, v24, v24\n\t" \
  "v_add_f32 v236, v24, v25\n\t" \
  "v_fmac_f32 v237, v25, v25\n\t" \
  "v_add_f32 v236, v236, v26\n\t" \
  "s_setprio 3\n\t" \
  "v_mfma_f32_32x32x8_bf16 v[8:23], v[252:253], v[252:253], 0\n\t" \
  "s_waitcnt lgkmcnt(4)\n\t" \
  "ds_write_b128 v242, v[228:231] offset:1024\n\t" \
  "ds_read_b64 v[192:193], v241 offset:32\n\t" \
  "v_fmac_f32 v237, v26, v26\n\t" \
  "v_smfmac_f32_16x16x64_bf16 v[32:35], v[140:143], v[208:215], v240\n\t" \
  "ds_read_b64 v[194:195], v241 offset:432\n\t" \
  "v_add_f32 v236, v236, v27\n\t" \
  "v_smfmac_f32_16x16x64_bf16 v[56:59], v[136:139], v[208:215], v240\n\t" \
  "ds_read_b64 v[196:197], v241 offset:832\n\t" \
  "v_fmac_f32 v237, v27, v27\n\t" \
  "v_smfmac_f32_16x16x64_bf16 v[80:83], v[132:135], v[208:215], v240\n\t" \
  "ds_read_b64 v[198:199], v241 offset:1232\n\t" \
  "v_add_f32 v236, v236, v28\n\t" \
  "v_smfmac_f32_16x16x64_bf16 v[8:11], v[128:131], v[208:215], v240\n\t" \
  "s_waitcnt lgkmcnt(5)\n\t" \
  "ds_write_b128 v242, v[232:235] offset:2048\n\t" \
  "v_fmac_f32 v237, v28, v28\n\t" \
  "v_smfmac_f32_16x16x64_bf16 v[36:39], v[156:159], v[216:223], v240\n\t" \
  "ds_read_b64 v[200:201], v241 offset:40\n\t" \
  "v_add_f32 v236, v236, v29\n\t" \
  "v_smfmac_f32_16x16x64_bf16 v[60:63], v[152:155], v[216:223], v240\n\t" \
  "ds_read_b64 v[202:203], v241 offset:440\n\t" \
  "v_fmac_f32 v237, v29, v29\n\t" \
  "v_smfmac_f32_16x16x64_bf16 v[84:87], v[148:151], v[216:223], v240\n\t" \
  "ds_read_b64 v[204:205], v241 offset:840\n\t" \
  "v_add_f32 v236, v236, v30\n\t" \
  "v_smfmac_f32_16x16x64_bf16 v[12:15], v[144:147], v[216:223], v240\n\t" \
  "ds_read_b64 v[206:207], v241 offset:1240\n\t" \
  "v_fmac_f32 v237, v30, v30\n\t" \
  "s_setprio 1\n\t" \
  "s_setprio 3\n\t" \
  "s_add_u32 %[cs], %[cs], 1\n\t" \
  "s_cmp_eq_u32 %[cs], 6\n\t" \
  "s_cselect_b32 %[cs], 0, %[cs]\n\t" \
  "s_cselect_b32 %[t0], -75920, %[ps]\n\t" \
  "v_add_u32 v241, %[t0], v241\n\t" \
  "s_waitcnt vmcnt(7)\n\t" \
  "s_waitcnt lgkmcnt(4)\n\t" \
  "s_barrier\n\t" \
  "v_smfmac_f32_16x16x64_bf16 v[40:43], v[172:175], v[192:199], v240\n\t" \
  "ds_read_b64 v[208:209], v241 offset:0\n\t" \
  "v_add_f32 v236, v236, v31\n\t" \
  "v_smfmac_f32_16x16x64_bf16 v[64:67], v[168:171], v[192:199], v240\n\t" \
  "ds_read_b64 v[210:211], v241 offset:400\n\t" \
  "v_fmac_f32 v237, v31, v31\n\t" \
  "v_smfmac_f32_16x16x64_bf16 v[88:91], v[164:167], v[192:199], v240\n\t" \
  "ds_read_b64 v[212:213], v241 offset:800\n\t" \
  "v_add_f32 v236, v236, v32\n\t" \
  "v_smfmac_f32_16x16x64_bf16 v[16:19], v[160:163], v[192:199], v240\n\t" \
  "s_waitcnt lgkmcnt(3)\n\t" \
  "ds_read_b64 v[214:215], v241 offset:1200\n\t" \
  "v_fmac_f32 v237, v32, v32\n\t" \
  "v_smfmac_f32_16x16x64_bf16 v[44:47], v[188:191], v[200:207], v240\n\t" \
  "ds_read_b64 v[216:217], v241 offset:8\n\t" \
  "v_add_f32 v236, v236, v33\n\t" \
  "v_smfmac_f32_16x16x64_bf16 v[68:71], v[184:187], v[200:207], v240\n\t" \
  "ds_read_b64 v[218:219], v241 offset:408\n\t" \
  "v_fmac_f32 v237, v33, v33\n\t" \
  "v_smfmac_f32_16x16x64_bf16 v[92:95], v[180:183], v[200:207], v240\n\t" \
  "ds_read_b64 v[220:221], v241 offset:808\n\t" \
  "v_add_f32 v236, v236, v34\n\t" \
  "v_smfmac_f32_16x16x64_bf16 v[20:23], v[176:179], v[200:207], v240\n\t" \
  "ds_read_b64 v[222:223], v241 offset:1208\n\t" \
  "v_fmac_f32 v237, v34, v34\n\t" \
  "s_setprio 1\n\t" \
  "s_setprio 3\n\t" \
  "s_mov_b32 m0, %[wld]\n\t" \
  "s_add_u32 %[t1], %[wso], %[wpd]\n\t" \
  "buffer_load_dwordx4 v245, %[wrs], %[wso] offen lds\n\t" \
  "buffer_load_dwordx4 v245, %[wrs], %[t1] offen offset:1024 lds\n\t" \
  "s_add_u32 %[t1], %[t1], %[wpd]\n\t" \
  "s_add_u32 %[wso], %[wso], %[wcs]\n\t" \
  "buffer_load_dwordx4 v245, %[wrs], %[t1] offen offset:2048 lds\n\t" \
  "s_add_u32 %[wld], %[wld], %[wla]\n\t" \
  "s_setprio 1\n\t" \
  "v_add_f32 v236, v236, v35\n\t" \
  "v_fmac_f32 v237, v35, v35\n\t" \
  "v_add_f32 v236, v236, v36\n\t" \
  "v_fmac_f32 v237, v36, v36\n\t" \
  "v_add_f32 v236, v236, v37\n\t" \
  "v_fmac_f32 v237, v37, v37\n\t" \
  "v_add_f32 v236, v236, v38\n\t" \
  "v_fmac_f32 v237, v38, v38\n\t" \
  "v_add_f32 v236, v236, v39\n\t" \
  "v_fmac_f32 v237, v39, v39\n\t" \
  "v_add_f32 v236, v236, v40\n\t" \
  "v_fmac_f32 v237, v40, v40\n\t" \
  "v_add_f32 v236, v236, v41\n\t" \
  "v_fmac_f32 v237, v41, v41\n\t" \
  "v_add_f32 v236, v236, v42\n\t" \
  "v_fmac_f32 v237, v42, v42\n\t" \
  "v_add_f32 v236, v236, v43\n\t" \
  "v_fmac_f32 v237, v43, v43\n\t" \
  "v_add_f32 v236, v236, v44\n\t" \
  "v_fmac_f32 v237, v44, v44\n\t" \
  "v_add_f32 v236, v236, v45\n\t" \
  "v_fmac_f32 v237, v45, v45\n\t" \
  "v_add_f32 v236, v236, v46\n\t" \
  "v_fmac_f32 v237, v46, v46\n\t" \
  "v_add_f32 v236, v236, v47\n\t" \
  "v_fmac_f32 v237, v47, v47\n\t" \
  "s_setprio 3\n\t" \
  "ds_write_b64 v243, v[236:237]\n\t" \
  "s_waitcnt vmcnt(7)\n\t" \
  "s_waitcnt lgkmcnt(0)\n\t" \
  "s_barrier\n\t" \
  "s_sub_u32 %[lc], %[lc], 1\n\t" \
  "s_cmp_lg_u32 %[lc], 0\n\t" \
  "s_cbranch_scc1 .Lcnx_f\n\t" \
  "ds_read_b128 v[224:227], v244\n\t" \
  "ds_read_b128 v[228:231], v244 offset:1024\n\t" \
  "s_waitcnt lgkmcnt(0)\n\t" \
  "s_setprio 1\n\t" \
  "v_add_f32 v224, v224, v226\n\t" \
  "v_add_f32 v225, v225, v227\n\t" \
  "v_add_f32 v228, v228, v230\n\t" \
  "v_add_f32 v229, v229, v231\n\t" \
  "v_add_f32 v224, v224, v228\n\t" \
  "v_add_f32 v225, v225, v229\n\t" \
  "v_mul_f32 v224, 0x3c2aaaab, v224\n\t" \
  "v_mul_f32 v225, 0x3c2aaaab, v225\n\t" \
  "v_fma_f32 v225, -v224, v224, v225\n\t" \
  "v_max_f32 v225, 0, v225\n\t" \
  "v_add_f32 v225, 0x3727c5ac, v225\n\t" \
  "v_rsq_f32 v225, v225\n\t" \
  "s_nop 0\n\t" \
  "v_mul_f32 v238, %[gl], v225\n\t" \
  "v_mul_f32 v239, -v224, v238\n\t" \
  "v_fma_f32 v24, v24, v238, v239\n\t" \
  "v_fma_f32 v25, v25, v238, v239\n\t" \
  "v_fma_f32 v26, v26, v238, v239\n\t" \
  "v_fma_f32 v27, v27, v238, v239\n\t" \
  "v_cvt_pk_bf16_f32 v224, v24, v25\n\t" \
  "v_cvt_pk_bf16_f32 v225, v26, v27\n\t" \
  "v_fma_f32 v28, v28, v238, v239\n\t" \
  "v_fma_f32 v29, v29, v238, v239\n\t" \
  "v_fma_f32 v30, v30, v238, v239\n\t" \
  "v_fma_f32 v31, v31, v238, v239\n\t" \
  "v_cvt_pk_bf16_f32 v226, v28, v29\n\t" \
  "v_cvt_pk_bf16_f32 v227, v30, v31\n\t" \
  "s_setprio 3\n\t" \
  "v_smfmac_f32_16x16x64_bf16 v[48:51], v[108:111], v[208:215], v240\n\t" \
  "ds_write_b128 v242, v[224:227] offset:0\n\t" \
  "ds_read_b64 v[192:193], v241 offset:16\n\t" \
  "ds_read_b64 v[194:195], v241 offset:416\n\t" \
  "v_fma_f32 v32, v32, v238, v239\n\t" \
  "v_fma_f32 v33, v33, v238, v239\n\t" \
  "v_smfmac_f32_16x16x64_bf16 v[72:75], v[104:107], v[208:215], v240\n\t" \
  "ds_read_b64 v[196:197], v241 offset:816\n\t" \
  "ds_read_b64 v[198:199], v241 offset:1216\n\t" \
  "v_fma_f32 v34, v34, v238, v239\n\t" \
  "v_fma_f32 v35, v35, v238, v239\n\t" \
  "v_smfmac_f32_16x16x64_bf16 v[52:55], v[124:127], v[216:223], v240\n\t" \
  "ds_read_b64 v[200:201], v241 offset:24\n\t" \
  "ds_read_b64 v[202:203], v241 offset:424\n\t" \
  "v_cvt_pk_bf16_f32 v228, v32, v33\n\t" \
  "v_cvt_pk_bf16_f32 v229, v34, v35\n\t" \
  "v_smfmac_f32_16x16x64_bf16 v[76:79], v[120:123], v[216:223], v240\n\t" \
  "ds_read_b64 v[204:205], v241 offset:824\n\t" \
  "ds_read_b64 v[206:207], v241 offset:1224\n\t" \
  "v_fma_f32 v36, v36, v238, v239\n\t" \
  "v_fma_f32 v37, v37, v238, v239\n\t" \
  "s_setprio 1\n\t" \
  "s_setprio 3\n\t" \
  "s_mov_b32 m0, %[wld]\n\t" \
  "s_add_u32 %[t1], %[wso], %[wpd]\n\t" \
  "buffer_load_dwordx4 v245, %[wrs], %[wso] offen lds\n\t" \
  "buffer_load_dwordx4 v245, %[wrs], %[t1] offen offset:1024 lds\n\t" \
  "s_add_u32 %[t1], %[t1], %[wpd]\n\t" \
  "s_add_u32 %[wso], %[wso], %[wcs]\n\t" \
  "buffer_load_dwordx4 v245, %[wrs], %[t1] offen offset:2048 lds\n\t" \
  "s_add_u32 %[wld], %[wld], %[wst]\n\t" \
  "s_setprio 1\n\t" \
  "s_setprio 3\n\t" \
  "s_waitcnt vmcnt(3)\n\t" \
  "s_barrier\n\t" \
  "s_mov_b32 m0, %[wld]\n\t" \
  "s_add_u32 %[t1], %[wso], %[wpd]\n\t" \
  "buffer_load_dwordx4 v245, %[wrs], %[wso] offen lds\n\t" \
  "buffer_load_dwordx4 v245, %[wrs], %[t1] offen offset:1024 lds\n\t" \
  "s_add_u32 %[t1], %[t1], %[wpd]\n\t" \
  "s_add_u32 %[wso], %[wso], %[wcs]\n\t" \
  "buffer_load_dwordx4 v245, %[wrs], %[t1] offen offset:2048 lds\n\t" \
  "s_add_u32 %[wld], %[wld], %[wlb]\n\t" \
  "s_setprio 1\n\t" \
  "v_fma_f32 v38, v38, v238, v239\n\t" \
  "v_fma_f32 v39, v39, v238, v239\n\t" \
  "v_cvt_pk_bf16_f32 v230, v36, v37\n\t" \
  "v_cvt_pk_bf16_f32 v231, v38, v39\n\t" \
  "v_fma_f32 v40, v40, v238, v239\n\t" \
  "v_fma_f32 v41, v41, v238, v239\n\t" \
  "v_fma_f32 v42, v42, v238, v239\n\t" \
  "v_fma_f32 v43, v43, v238, v239\n\t" \
  "v_cvt_pk_bf16_f32 v232, v40, v41\n\t" \
  "v_cvt_pk_bf16_f32 v233, v42, v43\n\t" \
  "v_fma_f32 v44, v44, v238, v239\n\t" \
  "v_fma_f32 v45, v45, v238, v239\n\t" \
  "v_fma_f32 v46, v46, v238, v239\n\t" \
  "v_fma_f32 v47, v47, v238, v239\n\t" \
  "v_cvt_pk_bf16_f32 v234, v44, v45\n\t" \
  "v_cvt_pk_bf16_f32 v235, v46, v47\n\t" \
  "v_mul_f32 v237, v48, v48\n\t" \
  "v_add_f32 v236, v48, v49\n\t" \
  "v_fmac_f32 v237, v49, v49\n\t" \
  "v_add_f32 v236, v236, v50\n\t" \
  "s_waitcnt lgkmcnt(4)\n\t" \
  "s_setprio 3\n\t" \
  "v_smfmac_f32_16x16x64_bf16 v[56:59], v[140:143], v[192:199], v240\n\t" \
  "ds_write_b128 v242, v[228:231] offset:1024\n\t" \
  "ds_read_b64 v[208:209], v241 offset:32\n\t" \
  "ds_read_b64 v[210:211], v241 offset:432\n\t" \
  "v_fmac_f32 v237, v50, v50\n\t" \
  "v_add_f32 v236, v236, v51\n\t" \
  "v_smfmac_f32_16x16x64_bf16 v[80:83], v[136:139], v[192:199], v240\n\t" \
  "s_waitcnt lgkmcnt(3)\n\t" \
  "ds_read_b64 v[212:213], v241 offset:832\n\t" \
  "ds_read_b64 v[214:215], v241 offset:1232\n\t" \
  "ds_write_b128 v242, v[232:235] offset:2048\n\t" \
  "v_fmac_f32 v237, v51, v51\n\t" \
  "v_add_f32 v236, v236, v52\n\t" \
  "v_smfmac_f32_16x16x64_bf16 v[60:63], v[156:159], v[200:207], v240\n\t" \
  "ds_read_b64 v[216:217], v241 offset:40\n\t" \
  "ds_read_b64 v[218:219], v241 offset:440\n\t" \
  "v_fmac_f32 v237, v52, v52\n\t" \
  "v_add_f32 v236, v236, v53\n\t" \
  "v_smfmac_f32_16x16x64_bf16 v[84:87], v[152:155], v[200:207], v240\n\t" \
  "ds_read_b64 v[220:221], v241 offset:840\n\t" \
  "ds_read_b64 v[222:223], v241 offset:1240\n\t" \
  "v_fmac_f32 v237, v53, v53\n\t" \
  "v_add_f32 v236, v236, v54\n\t" \
  "s_setprio 1\n\t" \
  "s_setprio 3\n\t" \
  "s_add_u32 %[cs], %[cs], 1\n\t" \
  "s_cmp_eq_u32 %[cs], 6\n\t" \
  "s_cselect_b32 %[cs], 0, %[cs]\n\t" \
  "s_cselect_b32 %[t0], -75920, %[ps]\n\t" \
  "v_add_u32 v241, %[t0], v241\n\t" \
  "s_waitcnt vmcnt(3)\n\t" \
  "s_waitcnt lgkmcnt(4)\n\t" \
  "s_barrier\n\t" \
  "v_smfmac_f32_16x16x64_bf16 v[64:67], v[172:175], v[208:215], v240\n\t" \
  "ds_read_b64 v[192:193], v241 offset:0\n\t" \
  "ds_read_b64 v[194:195], v241 offset:400\n\t" \
  "v_fmac_f32 v237, v54, v54\n\t" \
  "v_add_f32 v236, v236, v55\n\t" \
  "v_smfmac_f32_16x16x64_bf16 v[88:91], v[168:171], v[208:215], v240\n\t" \
  "s_waitcnt lgkmcnt(2)\n\t" \
  "ds_read_b64 v[196:197], v241 offset:800\n\t" \
  "ds_read_b64 v[198:199], v241 offset:1200\n\t" \
  "v_fmac_f32 v237, v55, v55\n\t" \
  "v_add_f32 v236, v236, v56\n\t" \
  "v_smfmac_f32_16x16x64_bf16 v[68:71], v[188:191], v[216:223], v240\n\t" \
  "ds_read_b64 v[200:201], v241 offset:8\n\t" \
  "ds_read_b64 v[202:203], v241 offset:408\n\t" \
  "v_fmac_f32 v237, v56, v56\n\t" \
  "v_add_f32 v236, v236, v57\n\t" \
  "v_smfmac_f32_16x16x64_bf16 v[92:95], v[184:187], v[216:223], v240\n\t" \
  "ds_read_b64 v[204:205], v241 offset:808\n\t" \
  "ds_read_b64 v[206:207], v241 offset:1208\n\t" \
  "v_fmac_f32 v237, v57, v57\n\t" \
  "v_add_f32 v236, v236, v58\n\t" \
  "s_setprio 1\n\t" \
  "s_setprio 3\n\t" \
  "s_mov_b32 m0, %[wld]\n\t" \
  "s_add_u32 %[t1], %[wso], %[wpd]\n\t" \
  "buffer_load_dwordx4 v245, %[wrs], %[wso] offen lds\n\t" \
  "buffer_load_dwordx4 v245, %[wrs], %[t1] offen offset:1024 lds\n\t" \
  "s_add_u32 %[t1], %[t1], %[wpd]\n\t" \
  "s_add_u32 %[wso], %[wso], %[wcs]\n\t" \
  "buffer_load_dwordx4 v245, %[wrs], %[t1] offen offset:2048 lds\n\t" \
  "s_add_u32 %[wld], %[wld], %[wla]\n\t" \
  "s_setprio 1\n\t" \
  "v_fmac_f32 v237, v58, v58\n\t" \
  "v_add_f32 v236, v236, v59\n\t" \
  "v_fmac_f32 v237, v59, v59\n\t" \
  "v_add_f32 v236, v236, v60\n\t" \
  "v_fmac_f32 v237, v60, v60\n\t" \
  "v_add_f32 v236, v236, v61\n\t" \
  "v_fmac_f32 v237, v61, v61\n\t" \
  "v_add_f32 v236, v236, v62\n\t" \
  "v_fmac_f32 v237, v62, v62\n\t" \
  "v_add_f32 v236, v236, v63\n\t" \
  "v_fmac_f32 v237, v63, v63\n\t" \
  "v_add_f32 v236, v236, v64\n\t" \
  "v_fmac_f32 v237, v64, v64\n\t" \
  "v_add_f32 v236, v236, v65\n\t" \
  "v_fmac_f32 v237, v65, v65\n\t" \
  "v_add_f32 v236, v236, v66\n\t" \
  "v_fmac_f32 v237, v66, v66\n\t" \
  "v_add_f32 v236, v236, v67\n\t" \
  "v_fmac_f32 v237, v67, v67\n\t" \
  "v_add_f32 v236, v236, v68\n\t" \
  "v_fmac_f32 v237, v68, v68\n\t" \
  "v_add_f32 v236, v236, v69\n\t" \
  "v_fmac_f32 v237, v69, v69\n\t" \
  "v_add_f32 v236, v236, v70\n\t" \
  "v_fmac_f32 v237, v70, v70\n\t" \
  "v_add_f32 v236, v236, v71\n\t" \
  "v_fmac_f32 v237, v71, v71\n\t" \
  "s_setprio 3\n\t" \
  "ds_write_b64 v243, v[236:237]\n\t" \
  "s_waitcnt vmcnt(3)\n\t" \
  "s_waitcnt lgkmcnt(0)\n\t" \
  "s_barrier\n\t" \
  "ds_read_b128 v[224:227], v244\n\t" \
  "ds_read_b128 v[228:231], v244 offset:1024\n\t" \
  "s_waitcnt lgkmcnt(0)\n\t" \
  "s_setprio 1\n\t" \
  "v_add_f32 v224, v224, v226\n\t" \
  "v_add_f32 v225, v225, v227\n\t" \
  "v_add_f32 v228, v228, v230\n\t" \
  "v_add_f32 v229, v229, v231\n\t" \
  "v_add_f32 v224, v224, v228\n\t" \
  "v_add_f32 v225, v225, v229\n\t" \
  "v_mul_f32 v224, 0x3c2aaaab, v224\n\t" \
  "v_mul_f32 v225, 0x3c2aaaab, v225\n\t" \
  "v_fma_f32 v225, -v224, v224, v225\n\t" \
  "v_max_f32 v225, 0, v225\n\t" \
  "v_add_f32 v225, 0x3727c5ac, v225\n\t" \
  "v_rsq_f32 v225, v225\n\t" \
  "s_nop 0\n\t" \
  "v_mul_f32 v238, %[gl], v225\n\t" \
  "v_mul_f32 v239, -v224, v238\n\t" \
  "v_fma_f32 v48, v48, v238, v239\n\t" \
  "v_fma_f32 v49, v49, v238, v239\n\t" \
  "v_fma_f32 v50, v50, v238, v239\n\t" \
  "v_fma_f32 v51, v51, v238, v239\n\t" \
  "v_cvt_pk_bf16_f32 v224, v48, v49\n\t" \
  "v_cvt_pk_bf16_f32 v225, v50, v51\n\t" \
  "v_fma_f32 v52, v52, v238, v239\n\t" \
  "v_fma_f32 v53, v53, v238, v239\n\t" \
  "v_fma_f32 v54, v54, v238, v239\n\t" \
  "v_fma_f32 v55, v55, v238, v239\n\t" \
  "v_cvt_pk_bf16_f32 v226, v52, v53\n\t" \
  "v_cvt_pk_bf16_f32 v227, v54, v55\n\t" \
  "s_setprio 3\n\t" \
  "v_smfmac_f32_16x16x64_bf16 v[72:75], v[108:111], v[192:199], v240\n\t" \
  "ds_write_b128 v242, v[224:227] offset:0\n\t" \
  "ds_read_b64 v[208:209], v241 offset:16\n\t" \
  "ds_read_b64 v[210:211], v241 offset:416\n\t" \
  "ds_read_b64 v[212:213], v241 offset:816\n\t" \
  "ds_read_b64 v[214:215], v241 offset:1216\n\t" \
  "v_fma_f32 v56, v56, v238, v239\n\t" \
  "v_fma_f32 v57, v57, v238, v239\n\t" \
  "v_smfmac_f32_16x16x64_bf16 v[76:79], v[124:127], v[200:207], v240\n\t" \
  "ds_read_b64 v[216:217], v241 offset:24\n\t" \
  "ds_read_b64 v[218:219], v241 offset:424\n\t" \
  "ds_read_b64 v[220:221], v241 offset:824\n\t" \
  "ds_read_b64 v[222:223], v241 offset:1224\n\t" \
  "v_fma_f32 v58, v58, v238, v239\n\t" \
  "v_fma_f32 v59, v59, v238, v239\n\t" \
  "s_setprio 1\n\t" \
  "s_setprio 3\n\t" \
  "s_mov_b32 m0, %[wld]\n\t" \
  "s_add_u32 %[t1], %[wso], %[wpd]\n\t" \
  "buffer_load_dwordx4 v245, %[wrs], %[wso] offen lds\n\t" \
  "buffer_load_dwordx4 v245, %[wrs], %[t1] offen offset:1024 lds\n\t" \
  "s_add_u32 %[t1], %[t1], %[wpd]\n\t" \
  "s_add_u32 %[wso], %[wso], %[wcs]\n\t" \
  "buffer_load_dwordx4 v245, %[wrs], %[t1] offen offset:2048 lds\n\t" \
  "s_add_u32 %[wld], %[wld], %[wst]\n\t" \
  "s_setprio 1\n\t" \
  "s_setprio 3\n\t" \
  "s_waitcnt vmcnt(3)\n\t" \
  "s_barrier\n\t" \
  "s_mov_b32 m0, %[wld]\n\t" \
  "s_add_u32 %[t1], %[wso], %[wpd]\n\t" \
  "buffer_load_dwordx4 v245, %[wrs], %[wso] offen lds\n\t" \
  "buffer_load_dwordx4 v245, %[wrs], %[t1] offen offset:1024 lds\n\t" \
  "s_add_u32 %[t1], %[t1], %[wpd]\n\t" \
  "s_add_u32 %[wso], %[wso], %[wcs]\n\t" \
  "buffer_load_dwordx4 v245, %[wrs], %[t1] offen offset:2048 lds\n\t" \
  "s_add_u32 %[wld], %[wld], %[wst]\n\t" \
  "s_setprio 1\n\t" \
  "v_cvt_pk_bf16_f32 v228, v56, v57\n\t" \
  "v_cvt_pk_bf16_f32 v229, v58, v59\n\t" \
  "v_fma_f32 v60, v60, v238, v239\n\t" \
  "v_fma_f32 v61, v61, v238, v239\n\t" \
  "v_fma_f32 v62, v62, v238, v239\n\t" \
  "v_fma_f32 v63, v63, v238, v239\n\t" \
  "v_cvt_pk_bf16_f32 v230, v60, v61\n\t" \
  "v_cvt_pk_bf16_f32 v231, v62, v63\n\t" \
  "v_fma_f32 v64, v64, v238, v239\n\t" \
  "v_fma_f32 v65, v65, v238, v239\n\t" \
  "v_fma_f32 v66, v66, v238, v239\n\t" \
  "v_fma_f32 v67, v67, v238, v239\n\t" \
  "v_cvt_pk_bf16_f32 v232, v64, v65\n\t" \
  "v_cvt_pk_bf16_f32 v233, v66, v67\n\t" \
  "v_fma_f32 v68, v68, v238, v239\n\t" \
  "v_fma_f32 v69, v69, v238, v239\n\t" \
  "v_fma_f32 v70, v70, v238, v239\n\t" \
  "v_fma_f32 v71, v71, v238, v239\n\t" \
  "v_cvt_pk_bf16_f32 v234, v68, v69\n\t" \
  "v_cvt_pk_bf16_f32 v235, v70, v71\n\t" \
  "v_mul_f32 v237, v72, v72\n\t" \
  "v_add_f32 v236, v72, v73\n\t" \
  "v_fmac_f32 v237, v73, v73\n\t" \
  "v_add_f32 v236, v236, v74\n\t" \
  "s_waitcnt lgkmcnt(4)\n\t" \
  "s_setprio 3\n\t" \
  "v_smfmac_f32_16x16x64_bf16 v[80:83], v[140:143], v[208:215], v240\n\t" \
  "s_waitcnt lgkmcnt(0)\n\t" \
  "ds_write_b128 v242, v[228:231] offset:1024\n\t" \
  "ds_read_b64 v[192:193], v241 offset:32\n\t" \
  "ds_read_b64 v[194:195], v241 offset:432\n\t" \
  "ds_read_b64 v[196:197], v241 offset:832\n\t" \
  "ds_read_b64 v[198:199], v241 offset:1232\n\t" \
  "v_fmac_f32 v237, v74, v74\n\t" \
  "v_add_f32 v236, v236, v75\n\t" \
  "v_smfmac_f32_16x16x64_bf16 v[84:87], v[156:159], v[216:223], v240\n\t" \
  "ds_write_b128 v242, v[232:235] offset:2048\n\t" \
  "ds_read_b64 v[200:201], v241 offset:40\n\t" \
  "ds_read_b64 v[202:203], v241 offset:440\n\t" \
  "ds_read_b64 v[204:205], v241 offset:840\n\t" \
  "ds_read_b64 v[206:207], v241 offset:1240\n\t" \
  "v_fmac_f32 v237, v75, v75\n\t" \
  "v_add_f32 v236, v236, v76\n\t" \
  "s_setprio 1\n\t" \
  "s_setprio 3\n\t" \
  "s_add_u32 %[cs], %[cs], 1\n\t" \
  "s_cmp_eq_u32 %[cs], 6\n\t" \
  "s_cselect_b32 %[cs], 0, %[cs]\n\t" \
  "s_cselect_b32 %[t0], -75920, %[ps]\n\t" \
  "v_add_u32 v241, %[t0], v241\n\t" \
  "s_waitcnt vmcnt(3)\n\t" \
  "s_waitcnt lgkmcnt(4)\n\t" \
  "s_barrier\n\t" \
  "v_smfmac_f32_16x16x64_bf16 v[88:91], v[172:175], v[192:199], v240\n\t" \
  "s_waitcnt lgkmcnt(0)\n\t" \
  "v_fmac_f32 v237, v76, v76\n\t" \
  "v_add_f32 v236, v236, v77\n\t" \
  "v_smfmac_f32_16x16x64_bf16 v[92:95], v[188:191], v[200:207], v240\n\t" \
  "v_fmac_f32 v237, v77, v77\n\t" \
  "v_add_f32 v236, v236, v78\n\t" \
  "s_setprio 1\n\t" \
  "s_setprio 3\n\t" \
  "s_mov_b32 m0, %[wld]\n\t" \
  "s_add_u32 %[t1], %[wso], %[wpd]\n\t" \
  "buffer_load_dwordx4 v245, %[wrs], %[wso] offen lds\n\t" \
  "buffer_load_dwordx4 v245, %[wrs], %[t1] offen offset:1024 lds\n\t" \
  "s_add_u32 %[t1], %[t1], %[wpd]\n\t" \
  "s_add_u32 %[wso], %[wso], %[wcs]\n\t" \
  "buffer_load_dwordx4 v245, %[wrs], %[t1] offen offset:2048 lds\n\t" \
  "s_add_u32 %[wld], %[wld], %[wlc]\n\t" \
  "s_setprio 1\n\t" \
  "v_fmac_f32 v237, v78, v78\n\t" \
  "v_add_f32 v236, v236, v79\n\t" \
  "v_fmac_f32 v237, v79, v79\n\t" \
  "v_add_f32 v236, v236, v80\n\t" \
  "v_fmac_f32 v237, v80, v80\n\t" \
  "v_add_f32 v236, v236, v81\n\t" \
  "v_fmac_f32 v237, v81, v81\n\t" \
  "v_add_f32 v236, v236, v82\n\t" \
  "v_fmac_f32 v237, v82, v82\n\t" \
  "v_add_f32 v236, v236, v83\n\t" \
  "v_fmac_f32 v237, v83, v83\n\t" \
  "v_add_f32 v236, v236, v84\n\t" \
  "v_fmac_f32 v237, v84, v84\n\t" \
  "v_add_f32 v236, v236, v85\n\t" \
  "v_fmac_f32 v237, v85, v85\n\t" \
  "v_add_f32 v236, v236, v86\n\t" \
  "v_fmac_f32 v237, v86, v86\n\t" \
  "v_add_f32 v236, v236, v87\n\t" \
  "v_fmac_f32 v237, v87, v87\n\t" \
  "v_add_f32 v236, v236, v88\n\t" \
  "v_fmac_f32 v237, v88, v88\n\t" \
  "v_add_f32 v236, v236, v89\n\t" \
  "v_fmac_f32 v237, v89, v89\n\t" \
  "v_add_f32 v236, v236, v90\n\t" \
  "v_fmac_f32 v237, v90, v90\n\t" \
  "v_add_f32 v236, v236, v91\n\t" \
  "v_fmac_f32 v237, v91, v91\n\t" \
  "v_add_f32 v236, v236, v92\n\t" \
  "v_fmac_f32 v237, v92, v92\n\t" \
  "v_add_f32 v236, v236, v93\n\t" \
  "v_fmac_f32 v237, v93, v93\n\t" \
  "v_add_f32 v236, v236, v94\n\t" \
  "v_fmac_f32 v237, v94, v94\n\t" \
  "v_add_f32 v236, v236, v95\n\t" \
  "v_fmac_f32 v237, v95, v95\n\t" \
  "s_setprio 3\n\t" \
  "ds_write_b64 v243, v[236:237]\n\t" \
  "s_waitcnt vmcnt(3)\n\t" \
  "s_waitcnt lgkmcnt(0)\n\t" \
  "s_barrier\n\t" \
  "ds_read_b128 v[224:227], v244\n\t" \
  "ds_read_b128 v[228:231], v244 offset:1024\n\t" \
  "s_waitcnt lgkmcnt(0)\n\t" \
  "s_setprio 1\n\t" \
  "v_add_f32 v224, v224, v226\n\t" \
  "v_add_f32 v225, v225, v227\n\t" \
  "v_add_f32 v228, v228, v230\n\t" \
  "v_add_f32 v229, v229, v231\n\t" \
  "v_add_f32 v224, v224, v228\n\t" \
  "v_add_f32 v225, v225, v229\n\t" \
  "v_mul_f32 v224, 0x3c2aaaab, v224\n\t" \
  "v_mul_f32 v225, 0x3c2aaaab, v225\n\t" \
  "v_fma_f32 v225, -v224, v224, v225\n\t" \
  "v_max_f32 v225, 0, v225\n\t" \
  "v_add_f32 v225, 0x3727c5ac, v225\n\t" \
  "v_rsq_f32 v225, v225\n\t" \
  "s_nop 0\n\t" \
  "v_mul_f32 v238, %[gl], v225\n\t" \
  "v_mul_f32 v239, -v224, v238\n\t" \
  "v_fma_f32 v72, v72, v238, v239\n\t" \
  "v_fma_f32 v73, v73, v238, v239\n\t" \
  "v_fma_f32 v74, v74, v238, v239\n\t" \
  "v_fma_f32 v75, v75, v238, v239\n\t" \
  "v_cvt_pk_bf16_f32 v224, v72, v73\n\t" \
  "v_cvt_pk_bf16_f32 v225, v74, v75\n\t" \
  "v_fma_f32 v76, v76, v238, v239\n\t" \
  "v_fma_f32 v77, v77, v238, v239\n\t" \
  "v_fma_f32 v78, v78, v238, v239\n\t" \
  "v_fma_f32 v79, v79, v238, v239\n\t" \
  "v_cvt_pk_bf16_f32 v226, v76, v77\n\t" \
  "v_cvt_pk_bf16_f32 v227, v78, v79\n\t" \
  "ds_write_b128 v242, v[224:227] offset:0\n\t" \
  "s_setprio 3\n\t" \
  "s_mov_b32 m0, %[wld]\n\t" \
  "s_add_u32 %[t1], %[wso], %[wpd]\n\t" \
  "buffer_load_dwordx4 v245, %[wrs], %[wso] offen lds\n\t" \
  "buffer_load_dwordx4 v245, %[wrs], %[t1] offen offset:1024 lds\n\t" \
  "s_add_u32 %[t1], %[t1], %[wpd]\n\t" \
  "s_add_u32 %[wso], %[wso], %[wcs]\n\t" \
  "buffer_load_dwordx4 v245, %[wrs], %[t1] offen offset:2048 lds\n\t" \
  "s_add_u32 %[wld], %[wld], %[wst]\n\t" \
  "s_setprio 1\n\t" \
  "s_setprio 3\n\t" \
  "s_waitcnt vmcnt(3)\n\t" \
  "s_barrier\n\t" \
  "s_mov_b32 m0, %[wld]\n\t" \
  "s_add_u32 %[t1], %[wso], %[wpd]\n\t" \
  "buffer_load_dwordx4 v245, %[wrs], %[wso] offen lds\n\t" \
  "buffer_load_dwordx4 v245, %[wrs], %[t1] offen offset:1024 lds\n\t" \
  "s_add_u32 %[t1], %[t1], %[wpd]\n\t" \
  "s_add_u32 %[wso], %[wso], %[wcs]\n\t" \
  "buffer_load_dwordx4 v245, %[wrs], %[t1] offen offset:2048 lds\n\t" \
  "s_add_u32 %[wld], %[wld], %[wst]\n\t" \
  "s_add_u32 m0, %[el], 0\n\t" \
  "s_add_u32 %[t1], %[es], %[wpd]\n\t" \
  "buffer_load_dwordx4 v245, %[wrs], %[es] offen lds\n\t" \
  "buffer_load_dwordx4 v245, %[wrs], %[t1] offen offset:1024 lds\n\t" \
  "s_add_u32 %[t1], %[t1], %[wpd]\n\t" \
  "s_add_u32 %[es], %[es], %[wcs]\n\t" \
  "buffer_load_dwordx4 v245, %[wrs], %[t1] offen offset:2048 lds\n\t" \
  "s_add_u32 m0, %[el], 12320\n\t" \
  "s_add_u32 %[t1], %[es], %[wpd]\n\t" \
  "buffer_load_dwordx4 v245, %[wrs], %[es] offen lds\n\t" \
  "buffer_load_dwordx4 v245, %[wrs], %[t1] offen offset:1024 lds\n\t" \
  "s_add_u32 %[t1], %[t1], %[wpd]\n\t" \
  "s_add_u32 %[es], %[es], %[wcs]\n\t" \
  "buffer_load_dwordx4 v245, %[wrs], %[t1] offen offset:2048 lds\n\t" \
  "s_setprio 1\n\t" \
  "v_fma_f32 v80, v80, v238, v239\n\t" \
  "v_fma_f32 v81, v81, v238, v239\n\t" \
  "v_fma_f32 v82, v82, v238, v239\n\t" \
  "v_fma_f32 v83, v83, v238, v239\n\t" \
  "v_cvt_pk_bf16_f32 v228, v80, v81\n\t" \
  "v_cvt_pk_bf16_f32 v229, v82, v83\n\t" \
  "v_fma_f32 v84, v84, v238, v239\n\t" \
  "v_fma_f32 v85, v85, v238, v239\n\t" \
  "v_fma_f32 v86, v86, v238, v239\n\t" \
  "v_fma_f32 v87, v87, v238, v239\n\t" \
  "v_cvt_pk_bf16_f32 v230, v84, v85\n\t" \
  "v_cvt_pk_bf16_f32 v231, v86, v87\n\t" \
  "v_fma_f32 v88, v88, v238, v239\n\t" \
  "v_fma_f32 v89, v89, v238, v239\n\t" \
  "v_fma_f32 v90, v90, v238, v239\n\t" \
  "v_fma_f32 v91, v91, v238, v239\n\t" \
  "v_cvt_pk_bf16_f32 v232, v88, v89\n\t" \
  "v_cvt_pk_bf16_f32 v233, v90, v91\n\t" \
  "v_fma_f32 v92, v92, v238, v239\n\t" \
  "v_fma_f32 v93, v93, v238, v239\n\t" \
  "v_fma_f32 v94, v94, v238, v239\n\t" \
  "v_fma_f32 v95, v95, v238, v239\n\t" \
  "v_cvt_pk_bf16_f32 v234, v92, v93\n\t" \
  "v_cvt_pk_bf16_f32 v235, v94, v95\n\t" \
  "ds_write_b128 v242, v[228:231] offset:1024\n\t" \
  "ds_write_b128 v242, v[232:235] offset:2048\n\t" \
  "s_setprio 3\n\t" \
  "s_add_u32 %[cs], %[cs], 1\n\t" \
  "s_cmp_eq_u32 %[cs], 6\n\t" \
  "s_cselect_b32 %[cs], 0, %[cs]\n\t" \
  "s_cselect_b32 %[t0], -75920, %[ps]\n\t" \
  "v_add_u32 v241, %[t0], v241\n\t" \
  "s_waitcnt vmcnt(9)\n\t" \
  "s_waitcnt lgkmcnt(0)\n\t" \
  "s_barrier\n\t" \
  "s_mov_b32 m0, %[wld]\n\t" \
  "s_add_u32 %[t1], %[wso], %[wpd]\n\t" \
  "buffer_load_dwordx4 v245, %[wrs], %[wso] offen lds\n\t" \
  "buffer_load_dwordx4 v245, %[wrs], %[t1] offen offset:1024 lds\n\t" \
  "s_add_u32 %[t1], %[t1], %[wpd]\n\t" \
  "s_add_u32 %[wso], %[wso], %[wsa]\n\t" \
  "buffer_load_dwordx4 v245, %[wrs], %[t1] offen offset:2048 lds\n\t" \
  "s_add_u32 %[wld], %[wld], %[wla]\n\t" \
  "s_add_u32 m0, %[el], 24640\n\t" \
  "s_add_u32 %[t1], %[es], %[wpd]\n\t" \
  "buffer_load_dwordx4 v245, %[wrs], %[es] offen lds\n\t" \
  "buffer_load_dwordx4 v245, %[wrs], %[t1] offen offset:1024 lds\n\t" \
  "s_add_u32 %[t1], %[t1], %[wpd]\n\t" \
  "s_add_u32 %[es], %[es], %[wcs]\n\t" \
  "buffer_load_dwordx4 v245, %[wrs], %[t1] offen offset:2048 lds\n\t" \
  "s_add_u32 m0, %[el], 36960\n\t" \
  "s_add_u32 %[t1], %[es], %[wpd]\n\t" \
  "buffer_load_dwordx4 v245, %[wrs], %[es] offen lds\n\t" \
  "buffer_load_dwordx4 v245, %[wrs], %[t1] offen offset:1024 lds\n\t" \
  "s_add_u32 %[t1], %[t1], %[wpd]\n\t" \
  "s_add_u32 %[es], %[es], %[wcs]\n\t" \
  "buffer_load_dwordx4 v245, %[wrs], %[t1] offen offset:2048 lds\n\t" \
  "s_waitcnt vmcnt(15)\n\t" \
  "s_barrier\n\t" \
  "s_sleep 10\n\t" \
  "s_mov_b32 m0, %[wld]\n\t" \
  "s_add_u32 %[t1], %[wso], %[wpd]\n\t" \
  "buffer_load_dwordx4 v245, %[wrs], %[wso] offen lds\n\t" \
  "buffer_load_dwordx4 v245, %[wrs], %[t1] offen offset:1024 lds\n\t" \
  "s_add_u32 %[t1], %[t1], %[wpd]\n\t" \
  "s_add_u32 %[wso], %[wso], %[wsb]\n\t" \
  "buffer_load_dwordx4 v245, %[wrs], %[t1] offen offset:2048 lds\n\t" \
  "s_add_u32 %[wld], %[wld], %[wlb]\n\t" \
  "s_waitcnt vmcnt(9)\n\t" \
  "s_barrier\n\t" \
  "s_mov_b32 m0, %[wld]\n\t" \
  "s_add_u32 %[t1], %[wso], %[wpd]\n\t" \
  "buffer_load_dwordx4 v245, %[wrs], %[wso] offen lds\n\t" \
  "buffer_load_dwordx4 v245, %[wrs], %[t1] offen offset:1024 lds\n\t" \
  "s_add_u32 %[t1], %[t1], %[wpd]\n\t" \
  "s_add_u32 %[wso], %[wso], %[wcs]\n\t" \
  "buffer_load_dwordx4 v245, %[wrs], %[t1] offen offset:2048 lds\n\t" \
  "s_add_u32 %[wld], %[wld], %[wst]\n\t" \
  "s_add_u32 m0, %[el], 49280\n\t" \
  "s_add_u32 %[t1], %[es], %[wpd]\n\t" \
  "buffer_load_dwordx4 v245, %[wrs], %[es] offen lds\n\t" \
  "buffer_load_dwordx4 v245, %[wrs], %[t1] offen offset:1024 lds\n\t" \
  "s_add_u32 %[t1], %[t1], %[wpd]\n\t" \
  "s_add_u32 %[es], %[es], %[wcs]\n\t" \
  "buffer_load_dwordx4 v245, %[wrs], %[t1] offen offset:2048 lds\n\t" \
  "s_add_u32 m0, %[el], 61600\n\t" \
  "s_add_u32 %[t1], %[es], %[wpd]\n\t" \
  "buffer_load_dwordx4 v245, %[wrs], %[es] offen lds\n\t" \
  "buffer_load_dwordx4 v245, %[wrs], %[t1] offen offset:1024 lds\n\t" \
  "s_add_u32 %[t1], %[t1], %[wpd]\n\t" \
  "s_add_u32 %[es], %[es], %[wcs]\n\t" \
  "buffer_load_dwordx4 v245, %[wrs], %[t1] offen offset:2048 lds\n\t" \
  "s_waitcnt vmcnt(9)\n\t" \
  "s_barrier\n\t" \
  "s_mov_b32 m0, %[wld]\n\t" \
  "s_add_u32 %[t1], %[wso], %[wpd]\n\t" \
  "buffer_load_dwordx4 v245, %[wrs], %[wso] offen lds\n\t" \
  "buffer_load_dwordx4 v245, %[wrs], %[t1] offen offset:1024 lds\n\t" \
  "s_add_u32 %[t1], %[t1], %[wpd]\n\t" \
  "s_add_u32 %[wso], %[wso], %[wcs]\n\t" \
  "buffer_load_dwordx4 v245, %[wrs], %[t1] offen offset:2048 lds\n\t" \
  "s_add_u32 %[wld], %[wld], %[wla]\n\t" \
  "s_add_u32 m0, %[el], 73920\n\t" \
  "s_add_u32 %[t1], %[es], %[wpd]\n\t" \
  "buffer_load_dwordx4 v245, %[wrs], %[es] offen lds\n\t" \
  "buffer_load_dwordx4 v245, %[wrs], %[t1] offen offset:1024 lds\n\t" \
  "s_add_u32 %[t1], %[t1], %[wpd]\n\t" \
  "s_add_u32 %[es], %[es], %[wcs]\n\t" \
  "buffer_load_dwordx4 v245, %[wrs], %[t1] offen offset:2048 lds\n\t" \
  "s_waitcnt vmcnt(12)\n\t" \
  "s_barrier\n\t" \
  "s_sleep 10\n\t" \
  "s_waitcnt vmcnt(0)\n\t" \
  "s_barrier\n\t" \
  ""
#define MS_ASM0 \
  "s_mov_b32 m0, %[xld]\n\t" \
  "s_cmp_lt_u32 %[gy], 0x3000000\n\t" \
  "s_cselect_b32 %[xso], %[gy], 0x40000000\n\t" \
  "buffer_load_dwordx4 v246, %[xrs], %[xso] offen offset:0 nt lds\n\t" \
  "s_add_u32 %[gy], %[gy], %[dgy]\n\t" \
  "buffer_load_dwordx4 v247, %[xrs], %[xso] offen offset:1024 nt lds\n\t" \
  "s_add_u32 %[xld], %[xld], 15184\n\t" \
  "s_cmp_eq_u32 %[xld], %[xle]\n\t" \
  "s_cselect_b32 %[xld], %[xlb], %[xld]\n\t" \
  "buffer_load_dwordx4 v248, %[xrs], %[xso] offen offset:2048 nt lds\n\t" \
  "s_mov_b64 exec, %[xm]\n\t" \
  "buffer_load_dwordx4 v249, %[xrs], %[xso] offen offset:3072 nt lds\n\t" \
  "s_mov_b64 exec, -1\n\t" \
  "s_mov_b32 m0, %[xld]\n\t" \
  "s_cmp_lt_u32 %[gy], 0x3000000\n\t" \
  "s_cselect_b32 %[xso], %[gy], 0x40000000\n\t" \
  "buffer_load_dwordx4 v246, %[xrs], %[xso] offen offset:0 nt lds\n\t" \
  "s_add_u32 %[gy], %[gy], %[dgy]\n\t" \
  "buffer_load_dwordx4 v247, %[xrs], %[xso] offen offset:1024 nt lds\n\t" \
  "s_add_u32 %[xld], %[xld], 15184\n\t" \
  "s_cmp_eq_u32 %[xld], %[xle]\n\t" \
  "s_cselect_b32 %[xld], %[xlb], %[xld]\n\t" \
  "buffer_load_dwordx4 v248, %[xrs], %[xso] offen offset:2048 nt lds\n\t" \
  "s_mov_b64 exec, %[xm]\n\t" \
  "buffer_load_dwordx4 v249, %[xrs], %[xso] offen offset:3072 nt lds\n\t" \
  "s_mov_b64 exec, -1\n\t" \
  ""
#define MS_ASM1 \
  "v_mov_b32 v72, 0xbdd2d3e8\n\t" \
  "v_add_u32 v75, 0x10000, v94\n\t" \
  "v_mul_f32 v74, %[kl], v95\n\t" \
  "v_mul_f32 v75, %[kl], v75\n\t" \
  "v_mul_f32 v76, v74, v74\n\t" \
  "v_mul_f32 v77, v75, v75\n\t" \
  "v_fmaak_f32 v76, v76, v72, 0xc0135761\n\t" \
  "v_fmaak_f32 v77, v77, v72, 0xc0135761\n\t" \
  "v_mul_f32 v76, v74, v76\n\t" \
  "v_mul_f32 v77, v75, v77\n\t" \
  "v_exp_f32 v78, v76\n\t" \
  "v_exp_f32 v79, v77\n\t" \
  "v_add_f32 v76, 1.0, v78\n\t" \
  "v_add_f32 v77, 1.0, v79\n\t" \
  "v_rcp_f32 v76, v76\n\t" \
  "v_rcp_f32 v77, v77\n\t" \
  "v_mul_f32 v74, v74, v76\n\t" \
  "v_mul_f32 v75, v75, v77\n\t" \
  "v_mul_f32 v78, -v74, v78\n\t" \
  "v_mul_f32 v79, -v75, v79\n\t" \
  "v_cvt_pk_bf16_f32 v83, v78, v79\n\t" \
  "v_cvt_pk_bf16_f32 v82, v74, v75\n\t" \
  "v_add_u32 v94, 0x2000000, v94\n\t" \
  "ds_write_b32 v70, v82 offset:0\n\t" \
  "ds_write_b32 v71, v83 offset:0\n\t" \
  "s_barrier\n\t" \
  "s_mov_b32 m0, %[xld]\n\t" \
  "s_cmp_lt_u32 %[gy], 0x3000000\n\t" \
  "s_cselect_b32 %[xso], %[gy], 0x40000000\n\t" \
  "buffer_load_dwordx4 v246, %[xrs], %[xso] offen offset:0 nt lds\n\t" \
  "s_add_u32 %[gy], %[gy], %[dgy]\n\t" \
  "buffer_load_dwordx4 v247, %[xrs], %[xso] offen offset:1024 nt lds\n\t" \
  "s_add_u32 %[xld], %[xld], 15184\n\t" \
  "s_cmp_eq_u32 %[xld], %[xle]\n\t" \
  "s_cselect_b32 %[xld], %[xlb], %[xld]\n\t" \
  "buffer_load_dwordx4 v248, %[xrs], %[xso] offen offset:2048 nt lds\n\t" \
  "s_mov_b64 exec, %[xm]\n\t" \
  "buffer_load_dwordx4 v249, %[xrs], %[xso] offen offset:3072 nt lds\n\t" \
  "s_mov_b64 exec, -1\n\t" \
  "s_waitcnt vmcnt(8)\n\t" \
  "s_barrier\n\t" \
  "s_mov_b32 m0, %[xld]\n\t" \
  "s_cmp_lt_u32 %[gy], 0x3000000\n\t" \
  "s_cselect_b32 %[xso], %[gy], 0x40000000\n\t" \
  "buffer_load_dwordx4 v246, %[xrs], %[xso] offen offset:0 nt lds\n\t" \
  "s_add_u32 %[gy], %[gy], %[dgy]\n\t" \
  "buffer_load_dwordx4 v247, %[xrs], %[xso] offen offset:1024 nt lds\n\t" \
  "s_add_u32 %[xld], %[xld], 15184\n\t" \
  "s_cmp_eq_u32 %[xld], %[xle]\n\t" \
  "s_cselect_b32 %[xld], %[xlb], %[xld]\n\t" \
  "buffer_load_dwordx4 v248, %[xrs], %[xso] offen offset:2048 nt lds\n\t" \
  "s_mov_b64 exec, %[xm]\n\t" \
  "buffer_load_dwordx4 v249, %[xrs], %[xso] offen offset:3072 nt lds\n\t" \
  "s_mov_b64 exec, -1\n\t" \
  "s_waitcnt vmcnt(8)\n\t" \
  "s_barrier\n\t" \
  "s_mov_b32 m0, %[wld]\n\t" \
  "s_mov_b32 %[xso], %[wso]\n\t" \
  "s_add_u32 %[t0], %[xso], 0x2c00\n\t" \
  "s_add_u32 %[t1], %[t0], 0x2c00\n\t" \
  "buffer_load_dwordx4 v245, %[wrs], %[xso] offen lds\n\t" \
  "buffer_load_dwordx4 v245, %[wrs], %[t0] offen offset:1024 lds\n\t" \
  "buffer_load_dwordx4 v245, %[wrs], %[t1] offen offset:2048 lds\n\t" \
  "v_add_u32 v85, 0x10000, v94\n\t" \
  "v_mul_f32 v84, %[kl], v94\n\t" \
  "v_mul_f32 v85, %[kl], v85\n\t" \
  "v_mul_f32 v86, v84, v84\n\t" \
  "v_mul_f32 v87, v85, v85\n\t" \
  "v_fmaak_f32 v86, v86, v72, 0xc0135761\n\t" \
  "v_fmaak_f32 v87, v87, v72, 0xc0135761\n\t" \
  "v_mul_f32 v86, v84, v86\n\t" \
  "v_mul_f32 v87, v85, v87\n\t" \
  "v_exp_f32 v88, v86\n\t" \
  "v_exp_f32 v89, v87\n\t" \
  "v_add_f32 v86, 1.0, v88\n\t" \
  "v_add_f32 v87, 1.0, v89\n\t" \
  "v_rcp_f32 v86, v86\n\t" \
  "v_rcp_f32 v87, v87\n\t" \
  "v_mul_f32 v84, v84, v86\n\t" \
  "v_mul_f32 v85, v85, v87\n\t" \
  "v_mul_f32 v88, -v84, v88\n\t" \
  "v_mul_f32 v89, -v85, v89\n\t" \
  "v_cvt_pk_bf16_f32 v93, v88, v89\n\t" \
  "v_cvt_pk_bf16_f32 v92, v84, v85\n\t" \
  "v_add_u32 v94, 0x2000000, v94\n\t" \
  "ds_write_b32 v70, v92 offset:1024\n\t" \
  "ds_write_b32 v71, v93 offset:1024\n\t" \
  "s_waitcnt vmcnt(7)\n\t" \
  "s_barrier\n\t" \
  "s_mov_b32 m0, %[xld]\n\t" \
  "s_cmp_lt_u32 %[gy], 0x3000000\n\t" \
  "s_cselect_b32 %[xso], %[gy], 0x40000000\n\t" \
  "buffer_load_dwordx4 v246, %[xrs], %[xso] offen offset:0 nt lds\n\t" \
  "s_add_u32 %[gy], %[gy], %[dgy]\n\t" \
  "buffer_load_dwordx4 v247, %[xrs], %[xso] offen offset:1024 nt lds\n\t" \
  "s_add_u32 %[xld], %[xld], 15184\n\t" \
  "s_cmp_eq_u32 %[xld], %[xle]\n\t" \
  "s_cselect_b32 %[xld], %[xlb], %[xld]\n\t" \
  "buffer_load_dwordx4 v248, %[xrs], %[xso] offen offset:2048 nt lds\n\t" \
  "s_mov_b64 exec, %[xm]\n\t" \
  "buffer_load_dwordx4 v249, %[xrs], %[xso] offen offset:3072 nt lds\n\t" \
  "s_mov_b64 exec, -1\n\t" \
  "v_add_u32 v75, 0x10000, v94\n\t" \
  "v_mul_f32 v74, %[kl], v94\n\t" \
  "v_mul_f32 v75, %[kl], v75\n\t" \
  "v_mul_f32 v76, v74, v74\n\t" \
  "v_mul_f32 v77, v75, v75\n\t" \
  "v_fmaak_f32 v76, v76, v72, 0xc0135761\n\t" \
  "v_fmaak_f32 v77, v77, v72, 0xc0135761\n\t" \
  "v_mul_f32 v76, v74, v76\n\t" \
  "v_mul_f32 v77, v75, v77\n\t" \
  "v_exp_f32 v78, v76\n\t" \
  "v_exp_f32 v79, v77\n\t" \
  "v_add_f32 v76, 1.0, v78\n\t" \
  "v_add_f32 v77, 1.0, v79\n\t" \
  "v_rcp_f32 v76, v76\n\t" \
  "v_rcp_f32 v77, v77\n\t" \
  "v_mul_f32 v74, v74, v76\n\t" \
  "v_mul_f32 v75, v75, v77\n\t" \
  "v_mul_f32 v78, -v74, v78\n\t" \
  "v_mul_f32 v79, -v75, v79\n\t" \
  "v_cvt_pk_bf16_f32 v83, v78, v79\n\t" \
  "v_cvt_pk_bf16_f32 v82, v74, v75\n\t" \
  "v_add_u32 v94, 0x2000000, v94\n\t" \
  "ds_write_b32 v70, v82 offset:2048\n\t" \
  "ds_write_b32 v71, v83 offset:2048\n\t" \
  "s_waitcnt vmcnt(7)\n\t" \
  "s_barrier\n\t" \
  "s_mov_b32 m0, %[xld]\n\t" \
  "s_cmp_lt_u32 %[gy], 0x3000000\n\t" \
  "s_cselect_b32 %[xso], %[gy], 0x40000000\n\t" \
  "buffer_load_dwordx4 v246, %[xrs], %[xso] offen offset:0 nt lds\n\t" \
  "s_add_u32 %[gy], %[gy], %[dgy]\n\t" \
  "buffer_load_dwordx4 v247, %[xrs], %[xso] offen offset:1024 nt lds\n\t" \
  "s_add_u32 %[xld], %[xld], 15184\n\t" \
  "s_cmp_eq_u32 %[xld], %[xle]\n\t" \
  "s_cselect_b32 %[xld], %[xlb], %[xld]\n\t" \
  "buffer_load_dwordx4 v248, %[xrs], %[xso] offen offset:2048 nt lds\n\t" \
  "s_mov_b64 exec, %[xm]\n\t" \
  "buffer_load_dwordx4 v249, %[xrs], %[xso] offen offset:3072 nt lds\n\t" \
  "s_mov_b64 exec, -1\n\t" \
  "s_mov_b32 m0, %[w2d]\n\t" \
  "s_nop 0\n\t" \
  "buffer_load_dwordx4 v244, %[w2r], %[w2s] offen offset:0 lds\n\t" \
  "buffer_load_dwordx4 v244, %[w2r], %[w2s] offen offset:1024 lds\n\t" \
  "buffer_load_dwordx4 v244, %[w2r], %[w2s] offen offset:2048 lds\n\t" \
  "v_add_u32 v85, 0x10000, v94\n\t" \
  "v_mul_f32 v84, %[kl], v94\n\t" \
  "v_mul_f32 v85, %[kl], v85\n\t" \
  "v_mul_f32 v86, v84, v84\n\t" \
  "v_mul_f32 v87, v85, v85\n\t" \
  "v_fmaak_f32 v86, v86, v72, 0xc0135761\n\t" \
  "v_fmaak_f32 v87, v87, v72, 0xc0135761\n\t" \
  "v_mul_f32 v86, v84, v86\n\t" \
  "v_mul_f32 v87, v85, v87\n\t" \
  "v_exp_f32 v88, v86\n\t" \
  "v_exp_f32 v89, v87\n\t" \
  "v_add_f32 v86, 1.0, v88\n\t" \
  "v_add_f32 v87, 1.0, v89\n\t" \
  "v_rcp_f32 v86, v86\n\t" \
  "v_rcp_f32 v87, v87\n\t" \
  "v_mul_f32 v84, v84, v86\n\t" \
  "v_mul_f32 v85, v85, v87\n\t" \
  "v_mul_f32 v88, -v84, v88\n\t" \
  "v_mul_f32 v89, -v85, v89\n\t" \
  "v_cvt_pk_bf16_f32 v93, v88, v89\n\t" \
  "v_cvt_pk_bf16_f32 v92, v84, v85\n\t" \
  "v_add_u32 v94, 0x2000000, v94\n\t" \
  "ds_write_b32 v70, v92 offset:3072\n\t" \
  "ds_write_b32 v71, v93 offset:3072\n\t" \
  "v_add_u32 v75, 0x10000, v94\n\t" \
  "v_mul_f32 v74, %[kl], v94\n\t" \
  "v_mul_f32 v75, %[kl], v75\n\t" \
  "v_mov_b32 v83, 0x80008000\n\t" \
  "v_cvt_pk_bf16_f32 v82, v74, v75\n\t" \
  "v_add_u32 v94, 0x2000000, v94\n\t" \
  "ds_write_b32 v70, v82 offset:4096\n\t" \
  "ds_write_b32 v71, v83 offset:4096\n\t" \
  "v_add_u32 v85, 0x10000, v94\n\t" \
  "v_mul_f32 v84, %[kl], v94\n\t" \
  "v_mul_f32 v85, %[kl], v85\n\t" \
  "v_mov_b32 v93, 0x80008000\n\t" \
  "v_cvt_pk_bf16_f32 v92, v84, v85\n\t" \
  "ds_write_b32 v70, v92 offset:5120\n\t" \
  "ds_write_b32 v71, v93 offset:5120\n\t" \
  "s_waitcnt vmcnt(7)\n\t" \
  "s_barrier\n\t" \
  "s_mov_b32 m0, %[wlb]\n\t" \
  "s_mov_b32 %[xso], %[wsb]\n\t" \
  "s_add_u32 %[t0], %[xso], 0x2c00\n\t" \
  "s_add_u32 %[t1], %[t0], 0x2c00\n\t" \
  "buffer_load_dwordx4 v245, %[wrs], %[xso] offen lds\n\t" \
  "buffer_load_dwordx4 v245, %[wrs], %[t0] offen offset:1024 lds\n\t" \
  "buffer_load_dwordx4 v245, %[wrs], %[t1] offen offset:2048 lds\n\t" \
  "s_mov_b32 m0, %[xld]\n\t" \
  "s_cmp_lt_u32 %[gy], 0x3000000\n\t" \
  "s_cselect_b32 %[xso], %[gy], 0x40000000\n\t" \
  "buffer_load_dwordx4 v246, %[xrs], %[xso] offen offset:0 nt lds\n\t" \
  "s_add_u32 %[gy], %[gy], %[dgy]\n\t" \
  "buffer_load_dwordx4 v247, %[xrs], %[xso] offen offset:1024 nt lds\n\t" \
  "s_add_u32 %[xld], %[xld], 15184\n\t" \
  "s_cmp_eq_u32 %[xld], %[xle]\n\t" \
  "s_cselect_b32 %[xld], %[xlb], %[xld]\n\t" \
  "buffer_load_dwordx4 v248, %[xrs], %[xso] offen offset:2048 nt lds\n\t" \
  "s_mov_b64 exec, %[xm]\n\t" \
  "buffer_load_dwordx4 v249, %[xrs], %[xso] offen offset:3072 nt lds\n\t" \
  "s_mov_b64 exec, -1\n\t" \
  "s_waitcnt lgkmcnt(0)\n\t" \
  ""
#define M_CLOB "v0", "v1", "v2", "v3", "v4", "v5", "v6", "v7", "v8", "v9", "v10", "v11", "v12", "v13", "v14", "v15", "v16", "v17", "v18", "v19", "v20", "v21", "v22", "v23", "v24", "v25", "v26", "v27", "v28", "v29", "v30", "v31", "v32", "v33", "v34", "v35", "v36", "v37", "v38", "v39", "v40", "v41", "v42", "v43", "v44", "v45", "v46", "v47", "v48", "v49", "v50", "v51", "v52", "v53", "v54", "v55", "v56", "v57", "v58", "v59", "v60", "v61", "v62", "v63", "v64", "v65", "v66", "v67", "v68", "v69", "v70", "v71", "v72", "v73", "v74", "v75", "v76", "v77", "v78", "v79", "v80", "v81", "v82", "v83", "v84", "v85", "v86", "v87", "v88", "v89", "v90", "v91", "v92", "v93", "v94", "v95", "v96", "v97", "v98", "v99", "v100", "v101", "v102", "v103", "v104", "v105", "v106", "v107", "v108", "v109", "v110", "v111", "v112", "v113", "v114", "v115", "v116", "v117", "v118", "v119", "v120", "v121", "v122", "v123", "v124", "v125", "v126", "v127", "v128", "v129", "v130", "v131", "v132", "v133", "v134", "v135", "v136", "v137", "v138", "v139", "v140", "v141", "v142", "v143", "v144", "v145", "v146", "v147", "v148", "v149", "v150", "v151", "v152", "v153", "v154", "v155", "v156", "v157", "v158", "v159", "v160", "v161", "v162", "v163", "v164", "v165", "v166", "v167", "v168", "v169", "v170", "v171", "v172", "v173", "v174", "v175", "v176", "v177", "v178", "v179", "v180", "v181", "v182", "v183", "v184", "v185", "v186", "v187", "v188", "v189", "v190", "v191", "v192", "v193", "v194", "v195", "v196", "v197", "v198", "v199", "v200", "v201", "v202", "v203", "v204", "v205", "v206", "v207", "v208", "v209", "v210", "v211", "v212", "v213", "v214", "v215", "v216", "v217", "v218", "v219", "v220", "v221", "v222", "v223", "v224", "v225", "v226", "v227", "v228", "v229", "v230", "v231", "v232", "v233", "v234", "v235", "v236", "v237", "v238", "v239", "v240", "v241", "v242", "v243", "v244", "v245"
#define LUT_CLOB "v72", "v73", "v74", "v75", "v76", "v77", "v78", "v79", "v80", "v81", "v82", "v83", "v84", "v85", "v86", "v87", "v88", "v89", "v90", "v91", "v92", "v93"
#define F_CLOB "v0", "v1", "v2", "v3", "v4", "v5", "v6", "v7", "v8", "v9", "v10", "v11", "v12", "v13", "v14", "v15", "v16", "v17", "v18", "v19", "v20", "v21", "v22", "v23", "v24", "v25", "v26", "v27", "v28", "v29", "v30", "v31", "v32", "v33", "v34", "v35", "v36", "v37", "v38", "v39", "v40", "v41", "v42", "v43", "v44", "v45", "v46", "v47", "v48", "v49", "v50", "v51", "v52", "v53", "v54", "v55", "v56", "v57", "v58", "v59", "v60", "v61", "v62", "v63", "v64", "v65", "v66", "v67", "v68", "v69", "v70", "v71", "v72", "v73", "v74", "v75", "v76", "v77", "v78", "v79", "v80", "v81", "v82", "v83", "v84", "v85", "v86", "v87", "v88", "v89", "v90", "v91", "v92", "v93", "v94", "v95", "v96", "v97", "v98", "v99", "v100", "v101", "v102", "v103", "v104", "v105", "v106", "v107", "v108", "v109", "v110", "v111", "v112", "v113", "v114", "v115", "v116", "v117", "v118", "v119", "v120", "v121", "v122", "v123", "v124", "v125", "v126", "v127", "v128", "v129", "v130", "v131", "v132", "v133", "v134", "v135", "v136", "v137", "v138", "v139", "v140", "v141", "v142", "v143", "v144", "v145", "v146", "v147", "v148", "v149", "v150", "v151", "v152", "v153", "v154", "v155", "v156", "v157", "v158", "v159", "v160", "v161", "v162", "v163", "v164", "v165", "v166", "v167", "v168", "v169", "v170", "v171", "v172", "v173", "v174", "v175", "v176", "v177", "v178", "v179", "v180", "v181", "v182", "v183", "v184", "v185", "v186", "v187", "v188", "v189", "v190", "v191", "v192", "v193", "v194", "v195", "v196", "v197", "v198", "v199", "v200", "v201", "v202", "v203", "v204", "v205", "v206", "v207", "v208", "v209", "v210", "v211", "v212", "v213", "v214", "v215", "v216", "v217", "v218", "v219", "v220", "v221", "v222", "v223", "v224", "v225", "v226", "v227", "v228", "v229", "v230", "v231", "v232", "v233", "v234", "v235", "v236", "v237", "v238", "v239", "v252", "v253"


__device__ __forceinline__ unsigned lds_addr(const char* p) { return (unsigned)(uintptr_t)(lds_ptr_t)p; }
__device__ __forceinline__ u32x4 make_rsrc(const void* p, unsigned bytes) {
    const uint64_t a = (uint64_t)(uintptr_t)p;
    u32x4 r = {(unsigned)a, (unsigned)(a >> 32) & 0xffffu, bytes, 0x00020000u};
    return r;
}
__device__ __forceinline__ void dma16(u32x4 rsrc, unsigned voff, unsigned soff, unsigned lds_dst) {
    asm volatile("s_mov_b32 m0, %1\n\ts_nop 0\n\tbuffer_load_dwordx4 %0, %2, %3 offen lds"
                 :: "v"(voff), "s"(lds_dst), "s"(rsrc), "s"(soff) : "memory");
}
__device__ __forceinline__ void warm(u32x4 rsrc, unsigned voff, unsigned soff, unsigned lds_dst) {
    asm volatile("s_mov_b32 m0, %1\n\ts_nop 0\n\tbuffer_load_dword %0, %2, %3 offen lds"
                 :: "v"(voff), "s"(lds_dst), "s"(rsrc), "s"(soff) : "memory");
}
__device__ __forceinline__ int lane_id() {
    int v;
    asm volatile("v_mbcnt_lo_u32_b32 %0, -1, 0\n\tv_mbcnt_hi_u32_b32 %0, -1, %0" : "=v"(v));
    __builtin_assume(v >= 0 && v < 64);
    return v;
}
__device__ __forceinline__ void bar() { asm volatile("s_waitcnt lgkmcnt(0)\n\ts_barrier" ::: "memory"); }
__device__ __forceinline__ float bf2f(unsigned short h) { return __builtin_bit_cast(float, (unsigned)h << 16); }
__device__ __forceinline__ unsigned short f2bf(float f) { return __builtin_bit_cast(unsigned short, (__bf16)f); }
__device__ __forceinline__ float gelu_f(float v) {
    const float e = __builtin_amdgcn_exp2f(v * __builtin_fmaf(v * v, -0.10294324f, -2.3022082f));
    return v * __builtin_amdgcn_rcpf(1.0f + e);
}

__device__ __forceinline__ void plain_block(const unsigned short* x, const unsigned short* dw, const unsigned short* w1,
                                            const unsigned short* w2, unsigned short* out, char* smem, int x0, int y0, int tid) {
    float* zs = (float*)smem;
    float* gs = zs + 64 * CH;
#pragma unroll 1
    for (int u = 0; u < 16; ++u) {
#pragma unroll 1
        for (int i = tid; i < 64 * CH; i += 512) {
            const int p = i / CH, c = i - p * CH, y = y0 + 2 * u + (p >> 5), xx = x0 + (p & 31);
            float a = 0.f;
#pragma unroll 1
            for (int t = 0; t < 49; ++t) {
                const int ky = t / 7, yy = y + ky - 3, xj = xx + (t - 7 * ky) - 3;
                if ((unsigned)yy < (unsigned)IMG && (unsigned)xj < (unsigned)IMG)
                    a = __builtin_fmaf(bf2f(x[(yy * IMG + xj) * CH + c]), bf2f(dw[t * CH + c]), a);
            }
            zs[i] = a;
        }
        __syncthreads();
        if (tid < 64) {
            float m = 0.f, v = 0.f;
#pragma unroll 1
            for (int c = 0; c < CH; ++c) m += zs[tid * CH + c];
            m *= 1.0f / CH;
#pragma unroll 1
            for (int c = 0; c < CH; ++c) { const float d = zs[tid * CH + c] - m; v = __builtin_fmaf(d, d, v); }
            const float r = __builtin_amdgcn_rsqf(v * (1.0f / CH) + 1e-5f);
#pragma unroll 1
            for (int c = 0; c < CH; ++c) zs[tid * CH + c] = bf2f(f2bf((zs[tid * CH + c] - m) * r));
        }
        __syncthreads();
#pragma unroll 1
        for (int i = tid; i < 64 * HID; i += 512) {
            const int p = i / HID, n = i - p * HID;
            float a = 0.f;
#pragma unroll 1
            for (int c = 0; c < CH; ++c) a = __builtin_fmaf(zs[p * CH + c], bf2f(w1[c * HID + n]), a);
            gs[i] = bf2f(f2bf(gelu_f(a)));
        }
        __syncthreads();
#pragma unroll 1
        for (int i = tid; i < 64 * CH; i += 512) {
            const int p = i / CH, c = i - p * CH, o = ((y0 + 2 * u + (p >> 5)) * IMG + x0 + (p & 31)) * CH + c;
            float a = bf2f(x[o]);
#pragma unroll 1
            for (int n = 0; n < HID; ++n) a = __builtin_fmaf(gs[p * HID + n], bf2f(w2[n * CH + c]), a);
            out[o] = f2bf(a);
        }
    }
}

__global__ __launch_bounds__(512, 2) void convnext_kernel(const __hip_bfloat16* __restrict__ x,
                                                          const __hip_bfloat16* __restrict__ dw,
                                                          const __hip_bfloat16* __restrict__ w1,
                                                          const __hip_bfloat16* __restrict__ w2,
                                                          __hip_bfloat16* __restrict__ out) {
    __shared__ __attribute__((aligned(16))) char smem[LDS_BYTES];
    const int tid = threadIdx.x;
    const int l = tid & 63;
    const int wid = __builtin_amdgcn_readfirstlane(tid >> 6);
    const int f = wid & 3;
    const int bid = blockIdx.x;
    asm volatile("s_getpc_b64 s[60:61]\n"
                 ".Lcnx_pc:\n\t"
                 "s_and_b32 s61, s61, 0xffff\n\t"
                 "s_mov_b32 s62, .Lcnx_end-.Lcnx_pc\n\t"
                 "s_mov_b32 s63, 0x20000\n\t"
                 "s_mov_b32 m0, %2\n\t"
                 "s_mov_b64 exec, 15\n\t"
                 "buffer_load_dword %0, s[60:63], %1 offen lds\n\t"
                 "s_mov_b64 exec, -1"
                 :: "v"((tid & 63) * 64 + (tid >> 6) * 256), "s"((bid >> 3) * 2048), "i"(PFOFF)
                 : "memory", "scc", "s60", "s61", "s62", "s63");
    const int x0 = ((bid >> 3) & 15) * 32;
    const int down = bid >> 7;
    const int y0 = (2 * (bid & 7) + down) * 32;
    const int dir = 2 * down - 1;
    const int yb = down ? y0 - 3 : y0 + 34;
    const unsigned sb = lds_addr(smem);


    if (wid >= 4) {
        __builtin_amdgcn_s_setprio(3);
        const u32x4 drs = make_rsrc(dw, 49 * CH * 2);
        const unsigned L = 64 * (f & 1) + l, cell = (L * 79) >> 10;
        const unsigned o = (cell - 1 < 7u && L - cell * 13 != 12) ? (L - cell - 12) * 16 : OOB;
#pragma unroll
        for (int k = 0; k < 5; ++k) {
            const int ky = (f >> 1) + 2 * k - 1;
            dma16(drs, o, (unsigned)ky < 7u ? (unsigned)((down ? ky : 6 - ky) * (7 * 192)) : OOB, sb + DWOFF + (ky + 1) * 2064 + (f & 1) * 1024);
        }
        __builtin_amdgcn_s_setprio(0);
    } else __builtin_amdgcn_s_setprio(2);
    const unsigned xlb = sb + XOFF + (f >> 1) * 7592 + (f & 1) * 4096, xle = xlb + 6 * PS;
    const unsigned long long xm = (f & 1) ? (1ull << 26) - 1 : ~0ull;
    unsigned xld = xlb, gy = (unsigned)((yb + dir * (f >> 1)) * ROWB), xso, xv[4];
    auto x_lanes = [&](int l) {
#pragma unroll
        for (int k = 0; k < 4; ++k) {
            const unsigned L = 64 * (4 * (f & 1) + k) + l, pp = (L * 0xa3eu) >> 16, o = (unsigned)((x0 - 3) * (CH * 2)) + (L - pp) * 16;
            xv[k] = (L - pp * 25 != 24 && o < (unsigned)ROWB) ? o + 3072 - 1024 * k : OOB;
        }
        asm volatile("" : "+{v246}"(xv[0]), "+{v247}"(xv[1]), "+{v248}"(xv[2]), "+{v249}"(xv[3]), "+s"(xld), "+s"(gy));
    };
    auto x_rsrc = [&]() {
        const char* p = (const char*)x;
        asm volatile("" : "+s"(p));
        return make_rsrc(p - 3072, IMG * ROWB + 3072);
    };
    auto w1_lane = [&](int l) {
        const int pos = (l >> 1) & 15, c16 = ((pos >> 2) & 1) * 8 + (pos >> 3) * 4 + (pos & 3);
        return (unsigned)(c16 * (HID * 2) + (l & 1) * 32 + (l >> 5) * 16);
    };
#define X_IN [xrs] "s"(xrs), [xlb] "s"(xlb), [xle] "s"(xle), [dgy] "s"(2 * dir * ROWB), [xm] "s"(xm)

#define MS_X [xld] "+&s"(xld), [gy] "+&s"(gy), [xso] "=&s"(xso)
#define MS_XIN "{v246}"(xv[0]), "{v247}"(xv[1]), "{v248}"(xv[2]), "{v249}"(xv[3]), X_IN

    if (wid >= 4) {
        const int l = lane_id();
        x_lanes(l);
        const u32x4 xrs = x_rsrc();
        const bool isw1 = f < 2;
        const u32x4 wrs = make_rsrc(isw1 ? w1 : w2, CH * HID * 2);
        const unsigned wv = isw1 ? w1_lane(l) : (unsigned)(l * 16);
        const unsigned wpd = isw1 ? 16 * HID * 2 - 1024 : 0, wcs = isw1 ? 64 : 6144;
        const unsigned wst = isw1 ? 6144 : 6160;
        unsigned wso = isw1 ? (f & 1) * (48 * HID * 2) + 192 : (f & 1) * 3072 + 2 * 6144;
        unsigned wld = sb + (isw1 ? W1OFF + (f & 1) * 3072 : W2OFF + (f & 1) * 3080 + 2 * 6160);
        unsigned cs = 0, t0, t1, lc;
        const int jx = (l >> 4) & 1, jy = l >> 5, xg = l & 15;
        unsigned cb = sb + XOFF + jx * 7592 + xg * 400 + jy * 192 + f * 48;
        const unsigned zw = sb + ZOFF + f * 3072 + l * 16;
        const unsigned aa = sb + DWOFF + (l & 8 ? 0 : 2064) + (4 * jy + jx - ((l >> 2) & 1) + 1) * 208 + f * 48 + (l & 3) * 2;
        unsigned es = (f & 1) * (isw1 ? 48 * HID * 2 : 3072) + (isw1 ? 3 : 2) * wcs;

        asm volatile(F_ASM
                     : "+{v241}"(cb), [wso] "+&s"(wso), [wld] "+&s"(wld), [xld] "+&s"(xld), [gy] "+&s"(gy),
                       [cs] "+&s"(cs), [es] "+&s"(es), [xso] "=&s"(xso), [t0] "=&s"(t0), [t1] "=&s"(t1), [lc] "=&s"(lc)
                     : "{v240}"((l & 3) * 0x5555u), "{v242}"(zw), "{v243}"(sb + STOFF + (f >> 1) * 1024 + l * 16 + (f & 1) * 8),
                       "{v244}"(sb + STOFF + l * 16), "{v245}"(wv), "{v251}"(aa),
                       "{v246}"(xv[0]), "{v247}"(xv[1]), "{v248}"(xv[2]), "{v249}"(xv[3]), X_IN,
                       [wrs] "s"(wrs), [wpd] "s"(wpd), [wcs] "s"(wcs), [wsa] "s"(isw1 ? -11 * 64 : 6144), [wsb] "s"(isw1 ? 64 : -11 * 6144),
                       [wst] "s"(wst), [wla] "s"(isw1 ? -2 * 6144 : 6160), [wlb] "s"(isw1 ? 6144 : -3 * 6160),
                       [wlc] "s"(isw1 ? -2 * 6144 : -3 * 6160), [el] "s"(sb + XOFF + (isw1 ? (f & 1) * 3072 : 6160 + (f & 1) * 3080)), [ps] "s"(PS), [gl] "s"(0x1p-114f * DL)
                     : "memory", "scc", F_CLOB);
        const u32x4* wp = (const u32x4*)w1 + f * 64 + lane_id();
        u32x4 v[18];
#pragma unroll
        for (int k = 0; k < 18; ++k) v[k] = wp[k * 256];
        unsigned mx = 0;
        asm volatile("s_setprio 1" : "+v"(mx) :: "memory");
#pragma unroll
        for (int k = 0; k < 72; ++k)
            asm("v_pk_maximum3_f16 %0, %0, %1, %1 neg_lo:[0,0,1] neg_hi:[0,0,1]" : "+v"(mx) : "v"(v[k >> 2][k & 3]));
        const unsigned m = max(mx & 0x7fffu, (mx >> 16) & 0x7fffu);
        *(unsigned*)(smem + FLOFF + f * 4) =
            (__builtin_amdgcn_ballot_w64(m >= 0x4180u) ? 1u : 0u) | (__builtin_amdgcn_ballot_w64(m >= 0x3a80u) ? 2u : 0u);

    } else {
        x_lanes(l);
        const u32x4 xrs = x_rsrc();
        asm volatile(MS_ASM0 : MS_X : MS_XIN : "memory", "scc");
        unsigned t0, t1;
        if (f >= 2) warm(make_rsrc(f & 1 ? w1 : w2, CH * HID * 2), l < 36 ? l * 64 : OOB, (bid >> 3) * 2304, sb + PFOFF);
        const int q = l >> 4, i = l & 15, r4 = i >> 2, c4 = i & 3, wy = wid >> 1, wx = wid & 1;
        unsigned a1 = sb + W1OFF + (q >> 1) * 1024 + ((q & 1) * 4 + r4) * 32 + c4 * 8;
        unsigned a2 = sb + W2OFF + (q & 1) * 3080 + ((q >> 1) * 8 + r4) * 192 + c4 * 16;
        unsigned zr = sb + ZOFF + q * 1024 + (16 * wid + i) * 16;
        unsigned rr = sb + XOFF + (1 + wy) * PS + (1 - wy) * 7592 + (i + 1 + wx) * 400 + (1 - wx) * 192 + q * 16;
        unsigned vo = (2 * i + wx) * 192 + q * 16;
        const unsigned one = q == r4 ? 0x3f80u << (16 * (c4 & 1)) : 0u;
        u32x2 id = {c4 < 2 ? one : 0u, c4 < 2 ? 0u : one};
        unsigned ob = (unsigned)((yb + dir * (3 + wy)) * ROWB + x0 * (CH * 2)), rs = 1 + wy, lc;
        unsigned tb = (unsigned)tid << 17 | 0x8000u;
        asm volatile(MS_ASM1
                     : "+{v94}"(tb), MS_X, [t0] "=&s"(t0), [t1] "=&s"(t1), "+v"(a1), "+{v250}"(a2), "+{v251}"(zr), "+{v252}"(rr),
                       "+{v253}"(vo), "+v"(id), "+s"(ob)
                     : "{v95}"(tid ? tb : 0u), "{v70}"(sb + tid * 4), "{v71}"(sb + 65536 + tid * 4), "{v244}"(l * 16), "{v245}"(w1_lane(l)), MS_XIN, [wrs] "s"(make_rsrc(w1, CH * HID * 2)),
                       [wso] "s"((f & 1) * (48 * HID * 2) + (f >> 1) * 64), [wld] "s"(sb + W1OFF + f * 3072),
                       [wsb] "s"((f & 1) * (48 * HID * 2) + 128), [wlb] "s"(sb + W1OFF + 2 * 6144 + (f & 1) * 3072),
                       [w2r] "s"(make_rsrc(w2, CH * HID * 2)), [w2s] "s"(f * 3072), [w2d] "s"(sb + W2OFF + f * 3080),
                       [kl] "s"(0x1p114f / DL)
                     : "memory", "scc", LUT_CLOB);

        asm volatile(M_ASM
                     : "+{v252}"(rr), "+{v249}"(a1), "+{v250}"(a2), [ob] "+&s"(ob), [rs] "+&s"(rs), [t0] "=&s"(t0), [lc] "=&s"(lc)
                     : "{v[246:247]}"(id), "{v248}"(0xddddddddu), "{v251}"(zr), "{v253}"(vo),
                       [osrd] "s"(make_rsrc(out, IMG * ROWB)), [dob] "s"(2 * dir * ROWB), [ps] "s"(PS), [h1] "s"(GH1), [hc] "s"(GHC),
                       [kk] "s"(0x1p114f * CT / DL)
                     : "memory", "scc", M_CLOB);

    }
    asm volatile(".Lcnx_end:" ::: "memory");
    bar();
    const u32x4 fl = *(const u32x4*)(smem + FLOFF);
    if (__builtin_amdgcn_readfirstlane(fl[0] | fl[1] | fl[2] | fl[3]) != 2) {
        asm volatile("s_waitcnt vmcnt(0)" ::: "memory");
        __syncthreads();
        plain_block((const unsigned short*)x, (const unsigned short*)dw, (const unsigned short*)w1, (const unsigned short*)w2,
                    (unsigned short*)out, smem, x0, y0, wid * 64 + lane_id());
    }
}

}

extern "C" void kernel_launch(void* const* d_in, const int* in_sizes, int n_in, void* d_out, int out_size,
                              void* d_ws, size_t ws_size, hipStream_t stream) {
    (void)in_sizes; (void)n_in; (void)out_size; (void)d_ws; (void)ws_size;
    const __hip_bfloat16* x = (const __hip_bfloat16*)d_in[0];
    const __hip_bfloat16* dw = (const __hip_bfloat16*)d_in[1];
    const __hip_bfloat16* w1 = (const __hip_bfloat16*)d_in[2];
    const __hip_bfloat16* w2 = (const __hip_bfloat16*)d_in[3];
    __hip_bfloat16* out = (__hip_bfloat16*)d_out;
    convnext_kernel<<<256, 512, 0, stream>>>(x, dw, w1, w2, out);
}
```

```cpp
#include <hip/hip_runtime.h>
#include <hip/hip_bf16.h>
#include <stdint.h>

namespace {

typedef __attribute__((ext_vector_type(4))) unsigned u32x4;
typedef __attribute__((ext_vector_type(2))) unsigned u32x2;
typedef __attribute__((address_space(3))) void* lds_ptr_t;

constexpr int IMG = 512;
constexpr int CH = 96;
constexpr int HID = 384;
constexpr int ROWB = IMG * CH * 2;
constexpr int LUTN = 24 * 128;
constexpr int W1OFF = LUTN * 2;
constexpr int W2OFF = W1OFF + 3 * 6144;
constexpr int DWOFF = W2OFF + 2 * 6160;
constexpr int ZOFF = W2OFF + 4 * 6160;
constexpr int STOFF = ZOFF + 12288;
constexpr int FLOFF = STOFF + 2048;
constexpr int XOFF = 65536 + LUTN * 2;
constexpr int PS = 15184;
constexpr int PFOFF = XOFF + 6 * PS;
constexpr int LDS_BYTES = 160 * 1024;
static_assert(FLOFF + 16 <= 65536 && DWOFF + 20640 <= 65536 && PFOFF + 256 <= LDS_BYTES, "LDS map");
constexpr unsigned OOB = 0x40000000u;

constexpr float DL = 1.0f + 1.0f / 362.0f;
constexpr float CT = 0.46866869f;
constexpr unsigned GH1 = 0xc4eac4eau;
constexpr unsigned GHC = 0x377a377au;

#define M_ASM \
  "s_setprio 0\n\t" \
  "s_waitcnt vmcnt(10)\n\t" \
  "s_barrier\n\t" \
  "ds_read_b128 v[96:99], v251 offset:0\n\t" \
  "ds_read_b128 v[100:103], v251 offset:4096\n\t" \
  "ds_read_b128 v[104:107], v251 offset:8192\n\t" \
  "ds_read_b64_tr_b16 v[208:209], v249 offset:0\n\t" \
  "ds_read_b64_tr_b16 v[210:211], v249 offset:256\n\t" \
  "ds_read_b64_tr_b16 v[212:213], v249 offset:2048\n\t" \
  "ds_read_b64_tr_b16 v[214:215], v249 offset:2304\n\t" \
  "ds_read_b64_tr_b16 v[216:217], v249 offset:4096\n\t" \
  "ds_read_b64_tr_b16 v[218:219], v249 offset:4352\n\t" \
  "ds_read_b64_tr_b16 v[220:221], v249 offset:512\n\t" \
  "ds_read_b64_tr_b16 v[222:223], v249 offset:768\n\t" \
  "s_waitcnt lgkmcnt(6)\n\t" \
  "v_mfma_f32_16x16x32_bf16 v[144:147], v[208:211], v[96:99], 0\n\t" \
  "ds_read_b64_tr_b16 v[208:209], v249 offset:2560\n\t" \
  "ds_read_b64_tr_b16 v[210:211], v249 offset:2816\n\t" \
  "s_waitcnt lgkmcnt(6)\n\t" \
  "v_mfma_f32_16x16x32_bf16 v[144:147], v[212:215], v[100:103], v[144:147]\n\t" \
  "ds_read_b64_tr_b16 v[212:213], v249 offset:4608\n\t" \
  "ds_read_b64_tr_b16 v[214:215], v249 offset:4864\n\t" \
  "s_waitcnt lgkmcnt(6)\n\t" \
  "v_mfma_f32_16x16x32_bf16 v[144:147], v[216:219], v[104:107], v[144:147]\n\t" \
  "s_waitcnt lgkmcnt(0)\n\t" \
  "s_waitcnt vmcnt(7)\n\t" \
  "s_barrier\n\t" \
  "ds_read_b64_tr_b16 v[216:217], v249 offset:6144\n\t" \
  "ds_read_b64_tr_b16 v[218:219], v249 offset:6400\n\t" \
  "v_mfma_f32_16x16x32_bf16 v[148:151], v[220:223], v[96:99], 0\n\t" \
  "ds_read_b64_tr_b16 v[220:221], v249 offset:8192\n\t" \
  "ds_read_b64_tr_b16 v[222:223], v249 offset:8448\n\t" \
  "s_nop 0\n\t" \
  "v_lshlrev_b32_sdwa v144, 1, v144 dst_sel:DWORD dst_unused:UNUSED_PAD src0_sel:DWORD src1_sel:WORD_1\n\t" \
  "v_mfma_f32_16x16x32_bf16 v[148:151], v[208:211], v[100:103], v[148:151]\n\t" \
  "ds_read_b64_tr_b16 v[208:209], v249 offset:10240\n\t" \
  "ds_read_b64_tr_b16 v[210:211], v249 offset:10496\n\t" \
  "ds_read_u16_d16_hi v144, v144 offset:0\n\t" \
  "v_lshlrev_b32_sdwa v145, 1, v145 dst_sel:DWORD dst_unused:UNUSED_PAD src0_sel:DWORD src1_sel:WORD_1\n\t" \
  "v_mfma_f32_16x16x32_bf16 v[148:151], v[212:215], v[104:107], v[148:151]\n\t" \
  "ds_read_b64_tr_b16 v[212:213], v249 offset:6656\n\t" \
  "ds_read_b64_tr_b16 v[214:215], v249 offset:6912\n\t" \
  "ds_read_u16_d16_hi v145, v145 offset:0\n\t" \
  "v_lshlrev_b32_sdwa v146, 1, v146 dst_sel:DWORD dst_unused:UNUSED_PAD src0_sel:DWORD src1_sel:WORD_1\n\t" \
  "v_lshlrev_b32_sdwa v147, 1, v147 dst_sel:DWORD dst_unused:UNUSED_PAD src0_sel:DWORD src1_sel:WORD_1\n\t" \
  "s_nop 2\n\t" \
  "v_lshlrev_b32_sdwa v148, 1, v148 dst_sel:DWORD dst_unused:UNUSED_PAD src0_sel:DWORD src1_sel:WORD_1\n\t" \
  "v_lshlrev_b32_sdwa v149, 1, v149 dst_sel:DWORD dst_unused:UNUSED_PAD src0_sel:DWORD src1_sel:WORD_1\n\t" \
  "ds_read_b64 v[230:231], v252 offset:0\n\t" \
  "s_waitcnt lgkmcnt(0)\n\t" \
  "v_mfma_f32_16x16x16_bf16 v[0:3], v[246:247], v[230:231], 0\n\t" \
  "ds_read_b64 v[232:233], v252 offset:8\n\t" \
  "ds_read_b64 v[234:235], v252 offset:64\n\t" \
  "ds_read_b64 v[236:237], v252 offset:72\n\t" \
  "ds_read_b64 v[242:243], v252 offset:128\n\t" \
  "ds_read_b64 v[244:245], v252 offset:136\n\t" \
  "ds_read_u16_d16_hi v146, v146 offset:0\n\t" \
  "ds_read_u16_d16_hi v147, v147 offset:0\n\t" \
  "ds_read_u16_d16_hi v148, v148 offset:0\n\t" \
  "ds_read_u16_d16_hi v149, v149 offset:0\n\t" \
  "s_waitcnt lgkmcnt(8)\n\t" \
  "v_mfma_f32_16x16x16_bf16 v[4:7], v[246:247], v[232:233], 0\n\t" \
  "v_lshlrev_b32_sdwa v150, 1, v150 dst_sel:DWORD dst_unused:UNUSED_PAD src0_sel:DWORD src1_sel:WORD_1\n\t" \
  "v_lshlrev_b32_sdwa v151, 1, v151 dst_sel:DWORD dst_unused:UNUSED_PAD src0_sel:DWORD src1_sel:WORD_1\n\t" \
  "v_mfma_f32_16x16x32_bf16 v[176:179], v[216:219], v[96:99], 0\n\t" \
  "ds_read_b64_tr_b16 v[216:217], v249 offset:8704\n\t" \
  "ds_read_b64_tr_b16 v[218:219], v249 offset:8960\n\t" \
  "ds_read_u16_d16_hi v150, v150 offset:0\n\t" \
  "ds_read_u16_d16_hi v151, v151 offset:0\n\t" \
  "v_mfma_f32_16x16x32_bf16 v[176:179], v[220:223], v[100:103], v[176:179]\n\t" \
  "ds_read_b64_tr_b16 v[220:221], v249 offset:10752\n\t" \
  "ds_read_b64_tr_b16 v[222:223], v249 offset:11008\n\t" \
  "s_waitcnt lgkmcnt(13)\n\t" \
  "v_mfma_f32_16x16x16_bf16 v[8:11], v[246:247], v[234:235], 0\n\t" \
  "s_waitcnt lgkmcnt(12)\n\t" \
  "v_mfma_f32_16x16x16_bf16 v[12:15], v[246:247], v[236:237], 0\n\t" \
  "v_mfma_f32_16x16x32_bf16 v[176:179], v[208:211], v[104:107], v[176:179]\n\t" \
  "ds_read_b64_tr_b16 v[208:209], v250 offset:0\n\t" \
  "ds_read_b64_tr_b16 v[210:211], v250 offset:768\n\t" \
  "v_mfma_f32_16x16x32_bf16 v[180:183], v[212:215], v[96:99], 0\n\t" \
  "ds_read_b64_tr_b16 v[212:213], v250 offset:8\n\t" \
  "ds_read_b64_tr_b16 v[214:215], v250 offset:776\n\t" \
  "s_waitcnt lgkmcnt(14)\n\t" \
  "v_mfma_f32_16x16x16_bf16 v[16:19], v[246:247], v[242:243], 0\n\t" \
  "s_nop 0\n\t" \
  "v_lshlrev_b32_sdwa v176, 1, v176 dst_sel:DWORD dst_unused:UNUSED_PAD src0_sel:DWORD src1_sel:WORD_1\n\t" \
  "v_lshlrev_b32_sdwa v177, 1, v177 dst_sel:DWORD dst_unused:UNUSED_PAD src0_sel:DWORD src1_sel:WORD_1\n\t" \
  "v_mfma_f32_16x16x16_bf16 v[20:23], v[246:247], v[244:245], 0\n\t" \
  "ds_read_u16_d16_hi v176, v176 offset:0\n\t" \
  "ds_read_u16_d16_hi v177, v177 offset:0\n\t" \
  "s_waitcnt lgkmcnt(10)\n\t" \
  "v_mfma_f32_16x16x32_bf16 v[180:183], v[216:219], v[100:103], v[180:183]\n\t" \
  "ds_read_b64_tr_b16 v[216:217], v250 offset:64\n\t" \
  "ds_read_b64_tr_b16 v[218:219], v250 offset:832\n\t" \
  "s_waitcnt lgkmcnt(8)\n\t" \
  "v_mfma_f32_16x16x32_bf16 v[180:183], v[220:223], v[104:107], v[180:183]\n\t" \
  "ds_read_b64_tr_b16 v[220:221], v250 offset:72\n\t" \
  "ds_read_b64_tr_b16 v[222:223], v250 offset:840\n\t" \
  "s_waitcnt lgkmcnt(8)\n\t" \
  "v_smfmac_f32_16x16x64_bf16 v[0:3], v[208:211], v[144:151], v248\n\t" \
  "ds_read_b64_tr_b16 v[208:209], v250 offset:128\n\t" \
  "ds_read_b64_tr_b16 v[210:211], v250 offset:896\n\t" \
  "v_lshlrev_b32_sdwa v178, 1, v178 dst_sel:DWORD dst_unused:UNUSED_PAD src0_sel:DWORD src1_sel:WORD_1\n\t" \
  "s_waitcnt lgkmcnt(8)\n\t" \
  "v_smfmac_f32_16x16x64_bf16 v[4:7], v[212:215], v[144:151], v248\n\t" \
  "ds_read_b64_tr_b16 v[212:213], v250 offset:136\n\t" \
  "ds_read_b64_tr_b16 v[214:215], v250 offset:904\n\t" \
  "ds_read_u16_d16_hi v178, v178 offset:0\n\t" \
  "v_lshlrev_b32_sdwa v179, 1, v179 dst_sel:DWORD dst_unused:UNUSED_PAD src0_sel:DWORD src1_sel:WORD_1\n\t" \
  "s_waitcnt lgkmcnt(7)\n\t" \
  "v_smfmac_f32_16x16x64_bf16 v[8:11], v[216:219], v[144:151], v248\n\t" \
  "s_waitcnt vmcnt(4)\n\t" \
  "s_barrier\n\t" \
  "ds_read_b64_tr_b16 v[216:217], v249 offset:12288\n\t" \
  "ds_read_b64_tr_b16 v[218:219], v249 offset:12544\n\t" \
  "ds_read_u16_d16_hi v179, v179 offset:0\n\t" \
  "s_waitcnt lgkmcnt(8)\n\t" \
  "v_smfmac_f32_16x16x64_bf16 v[12:15], v[220:223], v[144:151], v248\n\t" \
  "ds_read_b64_tr_b16 v[220:221], v249 offset:14336\n\t" \
  "ds_read_b64_tr_b16 v[222:223], v249 offset:14592\n\t" \
  "v_lshlrev_b32_sdwa v180, 1, v180 dst_sel:DWORD dst_unused:UNUSED_PAD src0_sel:DWORD src1_sel:WORD_1\n\t" \
  "s_waitcnt lgkmcnt(8)\n\t" \
  "v_smfmac_f32_16x16x64_bf16 v[16:19], v[208:211], v[144:151], v248\n\t" \
  "s_add_u32 %[rs], %[rs], 1\n\t" \
  "ds_read_b64_tr_b16 v[208:209], v249 offset:16384\n\t" \
  "ds_read_b64_tr_b16 v[210:211], v249 offset:16640\n\t" \
  "ds_read_u16_d16_hi v180, v180 offset:0\n\t" \
  "v_lshlrev_b32_sdwa v181, 1, v181 dst_sel:DWORD dst_unused:UNUSED_PAD src0_sel:DWORD src1_sel:WORD_1\n\t" \
  "s_waitcnt lgkmcnt(9)\n\t" \
  "v_smfmac_f32_16x16x64_bf16 v[20:23], v[212:215], v[144:151], v248\n\t" \
  "s_cmp_eq_u32 %[rs], 6\n\t" \
  "s_cselect_b32 %[rs], 0, %[rs]\n\t" \
  "ds_read_b64_tr_b16 v[212:213], v249 offset:12800\n\t" \
  "ds_read_b64_tr_b16 v[214:215], v249 offset:13056\n\t" \
  "ds_read_u16_d16_hi v181, v181 offset:0\n\t" \
  "s_cselect_b32 %[t0], -75920, %[ps]\n\t" \
  "v_add_u32 v252, %[t0], v252\n\t" \
  "s_waitcnt lgkmcnt(9)\n\t" \
  "v_mfma_f32_16x16x32_bf16 v[144:147], v[216:219], v[96:99], 0\n\t" \
  "ds_read_b64_tr_b16 v[216:217], v249 offset:14848\n\t" \
  "ds_read_b64_tr_b16 v[218:219], v249 offset:15104\n\t" \
  "v_lshlrev_b32_sdwa v182, 1, v182 dst_sel:DWORD dst_unused:UNUSED_PAD src0_sel:DWORD src1_sel:WORD_1\n\t" \
  "s_waitcnt lgkmcnt(8)\n\t" \
  "v_mfma_f32_16x16x32_bf16 v[144:147], v[220:223], v[100:103], v[144:147]\n\t" \
  "ds_read_b64_tr_b16 v[220:221], v249 offset:16896\n\t" \
  "ds_read_b64_tr_b16 v[222:223], v249 offset:17152\n\t" \
  "ds_read_u16_d16_hi v182, v182 offset:0\n\t" \
  "v_lshlrev_b32_sdwa v183, 1, v183 dst_sel:DWORD dst_unused:UNUSED_PAD src0_sel:DWORD src1_sel:WORD_1\n\t" \
  "s_waitcnt lgkmcnt(9)\n\t" \
  "v_mfma_f32_16x16x32_bf16 v[144:147], v[208:211], v[104:107], v[144:147]\n\t" \
  "ds_read_b64_tr_b16 v[208:209], v250 offset:6160\n\t" \
  "ds_read_b64_tr_b16 v[210:211], v250 offset:6928\n\t" \
  "ds_read_u16_d16_hi v183, v183 offset:0\n\t" \
  "s_waitcnt lgkmcnt(9)\n\t" \
  "v_mfma_f32_16x16x32_bf16 v[148:151], v[212:215], v[96:99], 0\n\t" \
  "ds_read_b64_tr_b16 v[212:213], v250 offset:6168\n\t" \
  "ds_read_b64_tr_b16 v[214:215], v250 offset:6936\n\t" \
  "s_nop 0\n\t" \
  "v_lshlrev_b32_sdwa v144, 1, v144 dst_sel:DWORD dst_unused:UNUSED_PAD src0_sel:DWORD src1_sel:WORD_1\n\t" \
  "s_waitcnt lgkmcnt(8)\n\t" \
  "v_mfma_f32_16x16x32_bf16 v[148:151], v[216:219], v[100:103], v[148:151]\n\t" \
  "ds_read_b64_tr_b16 v[216:217], v250 offset:6224\n\t" \
  "ds_read_b64_tr_b16 v[218:219], v250 offset:6992\n\t" \
  "ds_read_u16_d16_hi v144, v144 offset:0\n\t" \
  "v_lshlrev_b32_sdwa v145, 1, v145 dst_sel:DWORD dst_unused:UNUSED_PAD src0_sel:DWORD src1_sel:WORD_1\n\t" \
  "s_waitcnt lgkmcnt(9)\n\t" \
  "v_mfma_f32_16x16x32_bf16 v[148:151], v[220:223], v[104:107], v[148:151]\n\t" \
  "ds_read_b64_tr_b16 v[220:221], v250 offset:6232\n\t" \
  "ds_read_b64_tr_b16 v[222:223], v250 offset:7000\n\t" \
  "ds_read_u16_d16_hi v145, v145 offset:0\n\t" \
  "s_waitcnt lgkmcnt(8)\n\t" \
  "v_smfmac_f32_16x16x64_bf16 v[0:3], v[208:211], v[176:183], v248\n\t" \
  "ds_read_b64_tr_b16 v[208:209], v250 offset:6288\n\t" \
  "ds_read_b64_tr_b16 v[210:211], v250 offset:7056\n\t" \
  "v_lshlrev_b32_sdwa v146, 1, v146 dst_sel:DWORD dst_unused:UNUSED_PAD src0_sel:DWORD src1_sel:WORD_1\n\t" \
  "s_waitcnt lgkmcnt(8)\n\t" \
  "v_smfmac_f32_16x16x64_bf16 v[4:7], v[212:215], v[176:183], v248\n\t" \
  "ds_read_b64_tr_b16 v[212:213], v250 offset:6296\n\t" \
  "ds_read_b64_tr_b16 v[214:215], v250 offset:7064\n\t" \
  "ds_read_u16_d16_hi v146, v146 offset:0\n\t" \
  "v_lshlrev_b32_sdwa v147, 1, v147 dst_sel:DWORD dst_unused:UNUSED_PAD src0_sel:DWORD src1_sel:WORD_1\n\t" \
  "s_waitcnt lgkmcnt(9)\n\t" \
  "v_smfmac_f32_16x16x64_bf16 v[8:11], v[216:219], v[176:183], v248\n\t" \
  "s_waitcnt vmcnt(0)\n\t" \
  "s_barrier\n\t" \
  "s_setprio 2\n\t" \
  "ds_read_b64_tr_b16 v[216:217], v249 offset:0\n\t" \
  "ds_read_b64_tr_b16 v[218:219], v249 offset:256\n\t" \
  "ds_read_b128 v[108:111], v251 offset:0\n\t" \
  "ds_read_b128 v[112:115], v251 offset:4096\n\t" \
  "ds_read_b128 v[116:119], v251 offset:8192\n\t" \
  "ds_read_u16_d16_hi v147, v147 offset:0\n\t" \
  "s_waitcnt lgkmcnt(12)\n\t" \
  "v_smfmac_f32_16x16x64_bf16 v[12:15], v[220:223], v[176:183], v248\n\t" \
  "ds_read_b64_tr_b16 v[220:221], v249 offset:2048\n\t" \
  "ds_read_b64_tr_b16 v[222:223], v249 offset:2304\n\t" \
  "v_lshlrev_b32_sdwa v148, 1, v148 dst_sel:DWORD dst_unused:UNUSED_PAD src0_sel:DWORD src1_sel:WORD_1\n\t" \
  "s_waitcnt lgkmcnt(11)\n\t" \
  "v_smfmac_f32_16x16x64_bf16 v[16:19], v[208:211], v[176:183], v248\n\t" \
  "ds_read_b64_tr_b16 v[208:209], v249 offset:4096\n\t" \
  "ds_read_b64_tr_b16 v[210:211], v249 offset:4352\n\t" \
  "ds_read_u16_d16_hi v148, v148 offset:0\n\t" \
  "v_lshlrev_b32_sdwa v149, 1, v149 dst_sel:DWORD dst_unused:UNUSED_PAD src0_sel:DWORD src1_sel:WORD_1\n\t" \
  "s_waitcnt lgkmcnt(12)\n\t" \
  "v_smfmac_f32_16x16x64_bf16 v[20:23], v[212:215], v[176:183], v248\n\t" \
  "ds_read_b64_tr_b16 v[212:213], v249 offset:512\n\t" \
  "ds_read_b64_tr_b16 v[214:215], v249 offset:768\n\t" \
  "ds_read_u16_d16_hi v149, v149 offset:0\n\t" \
  "s_waitcnt lgkmcnt(12)\n\t" \
  "v_mfma_f32_16x16x32_bf16 v[176:179], v[216:219], v[96:99], 0\n\t" \
  "v_lshlrev_b32_sdwa v150, 1, v150 dst_sel:DWORD dst_unused:UNUSED_PAD src0_sel:DWORD src1_sel:WORD_1\n\t" \
  "v_lshlrev_b32_sdwa v151, 1, v151 dst_sel:DWORD dst_unused:UNUSED_PAD src0_sel:DWORD src1_sel:WORD_1\n\t" \
  "s_waitcnt lgkmcnt(11)\n\t" \
  "v_mfma_f32_16x16x32_bf16 v[184:187], v[216:219], v[108:111], 0\n\t" \
  "ds_read_b64_tr_b16 v[216:217], v249 offset:2560\n\t" \
  "ds_read_b64_tr_b16 v[218:219], v249 offset:2816\n\t" \
  "ds_read_u16_d16_hi v150, v150 offset:0\n\t" \
  "ds_read_u16_d16_hi v151, v151 offset:0\n\t" \
  "s_waitcnt lgkmcnt(10)\n\t" \
  "v_mfma_f32_16x16x32_bf16 v[176:179], v[220:223], v[100:103], v[176:179]\n\t" \
  "v_mfma_f32_16x16x32_bf16 v[184:187], v[220:223], v[112:115], v[184:187]\n\t" \
  "ds_read_b64_tr_b16 v[220:221], v249 offset:4608\n\t" \
  "ds_read_b64_tr_b16 v[222:223], v249 offset:4864\n\t" \
  "s_waitcnt lgkmcnt(10)\n\t" \
  "v_mfma_f32_16x16x32_bf16 v[176:179], v[208:211], v[104:107], v[176:179]\n\t" \
  "v_mfma_f32_16x16x32_bf16 v[184:187], v[208:211], v[116:119], v[184:187]\n\t" \
  "ds_read_b64_tr_b16 v[208:209], v250 offset:12320\n\t" \
  "ds_read_b64_tr_b16 v[210:211], v250 offset:13088\n\t" \
  "s_waitcnt lgkmcnt(9)\n\t" \
  "v_mfma_f32_16x16x32_bf16 v[180:183], v[212:215], v[96:99], 0\n\t" \
  "s_nop 2\n\t" \
  "v_lshlrev_b32_sdwa v176, 1, v176 dst_sel:DWORD dst_unused:UNUSED_PAD src0_sel:DWORD src1_sel:WORD_1\n\t" \
  "v_mul_f32 v184, %[kk], v184\n\t" \
  "v_mfma_f32_16x16x32_bf16 v[188:191], v[212:215], v[108:111], 0\n\t" \
  "ds_read_b64_tr_b16 v[212:213], v250 offset:12328\n\t" \
  "ds_read_b64_tr_b16 v[214:215], v250 offset:13096\n\t" \
  "ds_read_u16_d16_hi v176, v176 offset:0\n\t" \
  "v_mul_f32 v185, %[kk], v185\n\t" \
  "v_cvt_pk_f16_f32 v224, v184, v185\n\t" \
  "s_waitcnt lgkmcnt(9)\n\t" \
  "v_mfma_f32_16x16x32_bf16 v[180:183], v[216:219], v[100:103], v[180:183]\n\t" \
  "v_lshlrev_b32_sdwa v177, 1, v177 dst_sel:DWORD dst_unused:UNUSED_PAD src0_sel:DWORD src1_sel:WORD_1\n\t" \
  "v_pk_fma_f16 v225, v224, v224, %[h1] neg_lo:[1,0,0] neg_hi:[1,0,0]\n\t" \
  "v_mfma_f32_16x16x32_bf16 v[188:191], v[216:219], v[112:115], v[188:191]\n\t" \
  "ds_read_b64_tr_b16 v[216:217], v250 offset:12384\n\t" \
  "ds_read_b64_tr_b16 v[218:219], v250 offset:13152\n\t" \
  "ds_read_u16_d16_hi v177, v177 offset:0\n\t" \
  "v_pk_mul_f16 v225, v224, v225\n\t" \
  "s_waitcnt lgkmcnt(8)\n\t" \
  "v_mfma_f32_16x16x32_bf16 v[180:183], v[220:223], v[104:107], v[180:183]\n\t" \
  "v_exp_f16 v224, v225\n\t" \
  "v_mfma_f32_16x16x32_bf16 v[188:191], v[220:223], v[116:119], v[188:191]\n\t" \
  "ds_read_b64_tr_b16 v[220:221], v250 offset:12392\n\t" \
  "ds_read_b64_tr_b16 v[222:223], v250 offset:13160\n\t" \
  "v_lshlrev_b32_sdwa v178, 1, v178 dst_sel:DWORD dst_unused:UNUSED_PAD src0_sel:DWORD src1_sel:WORD_1\n\t" \
  "v_exp_f16_sdwa v224, v225 dst_sel:WORD_1 dst_unused:UNUSED_PRESERVE src0_sel:WORD_1\n\t" \
  "s_waitcnt lgkmcnt(8)\n\t" \
  "v_smfmac_f32_16x16x64_bf16 v[0:3], v[208:211], v[144:151], v248\n\t" \
  "ds_read_b64_tr_b16 v[208:209], v250 offset:12448\n\t" \
  "ds_read_b64_tr_b16 v[210:211], v250 offset:13216\n\t" \
  "ds_read_u16_d16_hi v178, v178 offset:0\n\t" \
  "v_lshlrev_b32_sdwa v179, 1, v179 dst_sel:DWORD dst_unused:UNUSED_PAD src0_sel:DWORD src1_sel:WORD_1\n\t" \
  "v_pk_fma_f16 v224, v224, %[hc], %[hc]\n\t" \
  "v_rcp_f16 v225, v224\n\t" \
  "s_waitcnt lgkmcnt(9)\n\t" \
  "v_smfmac_f32_16x16x64_bf16 v[4:7], v[212:215], v[144:151], v248\n\t" \
  "ds_read_b64_tr_b16 v[212:213], v250 offset:12456\n\t" \
  "ds_read_b64_tr_b16 v[214:215], v250 offset:13224\n\t" \
  "ds_read_b64 v[230:231], v252 offset:0\n\t" \
  "ds_read_b64 v[232:233], v252 offset:8\n\t" \
  "ds_read_b64 v[234:235], v252 offset:64\n\t" \
  "ds_read_u16_d16_hi v179, v179 offset:0\n\t" \
  "v_lshlrev_b32_sdwa v186, 1, v186 dst_sel:DWORD dst_unused:UNUSED_PAD src0_sel:DWORD src1_sel:WORD_1\n\t" \
  "v_rcp_f16_sdwa v224, v224 dst_sel:DWORD dst_unused:UNUSED_PAD src0_sel:WORD_1\n\t" \
  "s_waitcnt lgkmcnt(12)\n\t" \
  "v_smfmac_f32_16x16x64_bf16 v[8:11], v[216:219], v[144:151], v248\n\t" \
  "s_barrier\n\t" \
  "ds_read_b64_tr_b16 v[216:217], v249 offset:6144\n\t" \
  "ds_read_b64_tr_b16 v[218:219], v249 offset:6400\n\t" \
  "ds_read_b64 v[236:237], v252 offset:72\n\t" \
  "ds_read_b64 v[242:243], v252 offset:128\n\t" \
  "ds_read_b64 v[244:245], v252 offset:136\n\t" \
  "ds_read_u16_d16_hi v186, v186 offset:0\n\t" \
  "v_lshlrev_b32_sdwa v187, 1, v187 dst_sel:DWORD dst_unused:UNUSED_PAD src0_sel:DWORD src1_sel:WORD_1\n\t" \
  "v_fma_mix_f32 v184, v184, v225, 0 op_sel_hi:[0,1,0]\n\t" \
  "v_fma_mix_f32 v185, v185, v224, 0 op_sel_hi:[0,1,0]\n\t" \
  "s_waitcnt lgkmcnt(14)\n\t" \
  "v_smfmac_f32_16x16x64_bf16 v[12:15], v[220:223], v[144:151], v248\n\t" \
  "ds_read_b64_tr_b16 v[220:221], v249 offset:8192\n\t" \
  "ds_read_b64_tr_b16 v[222:223], v249 offset:8448\n\t" \
  "ds_read_u16_d16_hi v187, v187 offset:0\n\t" \
  "v_mul_f32 v188, %[kk], v188\n\t" \
  "v_lshlrev_b32_sdwa v180, 1, v180 dst_sel:DWORD dst_unused:UNUSED_PAD src0_sel:DWORD src1_sel:WORD_1\n\t" \
  "v_mul_f32 v189, %[kk], v189\n\t" \
  "v_cvt_pk_f16_f32 v224, v188, v189\n\t" \
  "s_waitcnt lgkmcnt(14)\n\t" \
  "v_smfmac_f32_16x16x64_bf16 v[16:19], v[208:211], v[144:151], v248\n\t" \
  "ds_read_b64_tr_b16 v[208:209], v249 offset:10240\n\t" \
  "ds_read_b64_tr_b16 v[210:211], v249 offset:10496\n\t" \
  "ds_read_u16_d16_hi v180, v180 offset:0\n\t" \
  "v_pk_fma_f16 v225, v224, v224, %[h1] neg_lo:[1,0,0] neg_hi:[1,0,0]\n\t" \
  "v_lshlrev_b32_sdwa v181, 1, v181 dst_sel:DWORD dst_unused:UNUSED_PAD src0_sel:DWORD src1_sel:WORD_1\n\t" \
  "v_pk_mul_f16 v225, v224, v225\n\t" \
  "v_exp_f16 v224, v225\n\t" \
  "s_waitcnt lgkmcnt(14)\n\t" \
  "v_smfmac_f32_16x16x64_bf16 v[20:23], v[212:215], v[144:151], v248\n\t" \
  "ds_read_b64_tr_b16 v[212:213], v249 offset:6656\n\t" \
  "ds_read_b64_tr_b16 v[214:215], v249 offset:6912\n\t" \
  "ds_read_u16_d16_hi v181, v181 offset:0\n\t" \
  "v_mfma_f32_16x16x16_bf16 v[24:27], v[246:247], v[230:231], 0\n\t" \
  "v_lshlrev_b32_sdwa v182, 1, v182 dst_sel:DWORD dst_unused:UNUSED_PAD src0_sel:DWORD src1_sel:WORD_1\n\t" \
  "v_exp_f16_sdwa v224, v225 dst_sel:WORD_1 dst_unused:UNUSED_PRESERVE src0_sel:WORD_1\n\t" \
  "s_waitcnt lgkmcnt(13)\n\t" \
  "v_mfma_f32_16x16x32_bf16 v[144:147], v[216:219], v[96:99], 0\n\t" \
  "ds_read_u16_d16_hi v182, v182 offset:0\n\t" \
  "v_pk_fma_f16 v224, v224, %[hc], %[hc]\n\t" \
  "v_mfma_f32_16x16x16_bf16 v[28:31], v[246:247], v[232:233], 0\n\t" \
  "v_lshlrev_b32_sdwa v183, 1, v183 dst_sel:DWORD dst_unused:UNUSED_PAD src0_sel:DWORD src1_sel:WORD_1\n\t" \
  "v_rcp_f16 v225, v224\n\t" \
  "v_mfma_f32_16x16x32_bf16 v[152:155], v[216:219], v[108:111], 0\n\t" \
  "ds_read_b64_tr_b16 v[216:217], v249 offset:8704\n\t" \
  "ds_read_b64_tr_b16 v[218:219], v249 offset:8960\n\t" \
  "ds_read_u16_d16_hi v183, v183 offset:0\n\t" \
  "s_waitcnt lgkmcnt(11)\n\t" \
  "v_mfma_f32_16x16x32_bf16 v[144:147], v[220:223], v[100:103], v[144:147]\n\t" \
  "v_lshlrev_b32_sdwa v190, 1, v190 dst_sel:DWORD dst_unused:UNUSED_PAD src0_sel:DWORD src1_sel:WORD_1\n\t" \
  "v_rcp_f16_sdwa v224, v224 dst_sel:DWORD dst_unused:UNUSED_PAD src0_sel:WORD_1\n\t" \
  "v_mfma_f32_16x16x32_bf16 v[152:155], v[220:223], v[112:115], v[152:155]\n\t" \
  "ds_read_b64_tr_b16 v[220:221], v249 offset:10752\n\t" \
  "ds_read_b64_tr_b16 v[222:223], v249 offset:11008\n\t" \
  "ds_read_u16_d16_hi v190, v190 offset:0\n\t" \
  "v_mfma_f32_16x16x16_bf16 v[32:35], v[246:247], v[234:235], 0\n\t" \
  "v_fma_mix_f32 v188, v188, v225, 0 op_sel_hi:[0,1,0]\n\t" \
  "v_lshlrev_b32_sdwa v191, 1, v191 dst_sel:DWORD dst_unused:UNUSED_PAD src0_sel:DWORD src1_sel:WORD_1\n\t" \
  "s_waitcnt lgkmcnt(11)\n\t" \
  "v_mfma_f32_16x16x32_bf16 v[144:147], v[208:211], v[104:107], v[144:147]\n\t" \
  "ds_read_u16_d16_hi v191, v191 offset:0\n\t" \
  "v_fma_mix_f32 v189, v189, v224, 0 op_sel_hi:[0,1,0]\n\t" \
  "v_mfma_f32_16x16x16_bf16 v[36:39], v[246:247], v[236:237], 0\n\t" \
  "v_mfma_f32_16x16x32_bf16 v[152:155], v[208:211], v[116:119], v[152:155]\n\t" \
  "ds_read_b64_tr_b16 v[208:209], v250 offset:18480\n\t" \
  "ds_read_b64_tr_b16 v[210:211], v250 offset:19248\n\t" \
  "s_waitcnt lgkmcnt(11)\n\t" \
  "v_mfma_f32_16x16x32_bf16 v[148:151], v[212:215], v[96:99], 0\n\t" \
  "v_lshlrev_b32_sdwa v144, 1, v144 dst_sel:DWORD dst_unused:UNUSED_PAD src0_sel:DWORD src1_sel:WORD_1\n\t" \
  "s_nop 2\n\t" \
  "v_mul_f32 v152, %[kk], v152\n\t" \
  "v_mfma_f32_16x16x32_bf16 v[156:159], v[212:215], v[108:111], 0\n\t" \
  "ds_read_b64_tr_b16 v[212:213], v250 offset:18488\n\t" \
  "ds_read_b64_tr_b16 v[214:215], v250 offset:19256\n\t" \
  "ds_read_u16_d16_hi v144, v144 offset:0\n\t" \
  "v_mfma_f32_16x16x16_bf16 v[40:43], v[246:247], v[242:243], 0\n\t" \
  "v_mul_f32 v153, %[kk], v153\n\t" \
  "v_cvt_pk_f16_f32 v224, v152, v153\n\t" \
  "s_waitcnt lgkmcnt(10)\n\t" \
  "v_mfma_f32_16x16x32_bf16 v[148:151], v[216:219], v[100:103], v[148:151]\n\t" \
  "v_lshlrev_b32_sdwa v145, 1, v145 dst_sel:DWORD dst_unused:UNUSED_PAD src0_sel:DWORD src1_sel:WORD_1\n\t" \
  "v_pk_fma_f16 v225, v224, v224, %[h1] neg_lo:[1,0,0] neg_hi:[1,0,0]\n\t" \
  "v_mfma_f32_16x16x16_bf16 v[44:47], v[246:247], v[244:245], 0\n\t" \
  "ds_read_u16_d16_hi v145, v145 offset:0\n\t" \
  "v_pk_mul_f16 v225, v224, v225\n\t" \
  "v_mfma_f32_16x16x32_bf16 v[156:159], v[216:219], v[112:115], v[156:159]\n\t" \
  "ds_read_b64_tr_b16 v[216:217], v250 offset:18544\n\t" \
  "ds_read_b64_tr_b16 v[218:219], v250 offset:19312\n\t" \
  "s_waitcnt lgkmcnt(10)\n\t" \
  "v_mfma_f32_16x16x32_bf16 v[148:151], v[220:223], v[104:107], v[148:151]\n\t" \
  "v_exp_f16 v224, v225\n\t" \
  "v_mfma_f32_16x16x32_bf16 v[156:159], v[220:223], v[116:119], v[156:159]\n\t" \
  "ds_read_b64_tr_b16 v[220:221], v250 offset:18552\n\t" \
  "ds_read_b64_tr_b16 v[222:223], v250 offset:19320\n\t" \
  "v_lshlrev_b32_sdwa v146, 1, v146 dst_sel:DWORD dst_unused:UNUSED_PAD src0_sel:DWORD src1_sel:WORD_1\n\t" \
  "v_exp_f16_sdwa v224, v225 dst_sel:WORD_1 dst_unused:UNUSED_PRESERVE src0_sel:WORD_1\n\t" \
  "s_waitcnt lgkmcnt(8)\n\t" \
  "v_smfmac_f32_16x16x64_bf16 v[0:3], v[208:211], v[176:183], v248\n\t" \
  "ds_read_u16_d16_hi v146, v146 offset:0\n\t" \
  "v_lshlrev_b32_sdwa v147, 1, v147 dst_sel:DWORD dst_unused:UNUSED_PAD src0_sel:DWORD src1_sel:WORD_1\n\t" \
  "v_smfmac_f32_16x16x64_bf16 v[24:27], v[208:211], v[184:191], v248\n\t" \
  "ds_read_b64_tr_b16 v[208:209], v250 offset:18608\n\t" \
  "ds_read_b64_tr_b16 v[210:211], v250 offset:19376\n\t" \
  "ds_read_u16_d16_hi v147, v147 offset:0\n\t" \
  "v_pk_fma_f16 v224, v224, %[hc], %[hc]\n\t" \
  "v_rcp_f16 v225, v224\n\t" \
  "s_waitcnt lgkmcnt(10)\n\t" \
  "v_smfmac_f32_16x16x64_bf16 v[4:7], v[212:215], v[176:183], v248\n\t" \
  "v_lshlrev_b32_sdwa v154, 1, v154 dst_sel:DWORD dst_unused:UNUSED_PAD src0_sel:DWORD src1_sel:WORD_1\n\t" \
  "v_rcp_f16_sdwa v224, v224 dst_sel:DWORD dst_unused:UNUSED_PAD src0_sel:WORD_1\n\t" \
  "v_smfmac_f32_16x16x64_bf16 v[28:31], v[212:215], v[184:191], v248\n\t" \
  "ds_read_b64_tr_b16 v[212:213], v250 offset:18616\n\t" \
  "ds_read_b64_tr_b16 v[214:215], v250 offset:19384\n\t" \
  "ds_read_u16_d16_hi v154, v154 offset:0\n\t" \
  "s_waitcnt lgkmcnt(9)\n\t" \
  "v_smfmac_f32_16x16x64_bf16 v[8:11], v[216:219], v[176:183], v248\n\t" \
  "v_lshlrev_b32_sdwa v155, 1, v155 dst_sel:DWORD dst_unused:UNUSED_PAD src0_sel:DWORD src1_sel:WORD_1\n\t" \
  "v_fma_mix_f32 v152, v152, v225, 0 op_sel_hi:[0,1,0]\n\t" \
  "v_smfmac_f32_16x16x64_bf16 v[32:35], v[216:219], v[184:191], v248\n\t" \
  "s_barrier\n\t" \
  "ds_read_b64_tr_b16 v[216:217], v249 offset:12288\n\t" \
  "ds_read_b64_tr_b16 v[218:219], v249 offset:12544\n\t" \
  "ds_read_u16_d16_hi v155, v155 offset:0\n\t" \
  "v_fma_mix_f32 v153, v153, v224, 0 op_sel_hi:[0,1,0]\n\t" \
  "s_waitcnt lgkmcnt(10)\n\t" \
  "v_smfmac_f32_16x16x64_bf16 v[12:15], v[220:223], v[176:183], v248\n\t" \
  "v_mul_f32 v156, %[kk], v156\n\t" \
  "v_lshlrev_b32_sdwa v148, 1, v148 dst_sel:DWORD dst_unused:UNUSED_PAD src0_sel:DWORD src1_sel:WORD_1\n\t" \
  "v_smfmac_f32_16x16x64_bf16 v[36:39], v[220:223], v[184:191], v248\n\t" \
  "ds_read_b64_tr_b16 v[220:221], v249 offset:14336\n\t" \
  "ds_read_b64_tr_b16 v[222:223], v249 offset:14592\n\t" \
  "ds_read_u16_d16_hi v148, v148 offset:0\n\t" \
  "v_mul_f32 v157, %[kk], v157\n\t" \
  "v_cvt_pk_f16_f32 v224, v156, v157\n\t" \
  "s_waitcnt lgkmcnt(10)\n\t" \
  "v_smfmac_f32_16x16x64_bf16 v[16:19], v[208:211], v[176:183], v248\n\t" \
  "s_add_u32 %[rs], %[rs], 1\n\t" \
  "v_pk_fma_f16 v225, v224, v224, %[h1] neg_lo:[1,0,0] neg_hi:[1,0,0]\n\t" \
  "v_lshlrev_b32_sdwa v149, 1, v149 dst_sel:DWORD dst_unused:UNUSED_PAD src0_sel:DWORD src1_sel:WORD_1\n\t" \
  "v_smfmac_f32_16x16x64_bf16 v[40:43], v[208:211], v[184:191], v248\n\t" \
  "s_cmp_eq_u32 %[rs], 6\n\t" \
  "ds_read_b64_tr_b16 v[208:209], v249 offset:16384\n\t" \
  "ds_read_b64_tr_b16 v[210:211], v249 offset:16640\n\t" \
  "ds_read_u16_d16_hi v149, v149 offset:0\n\t" \
  "v_pk_mul_f16 v225, v224, v225\n\t" \
  "v_exp_f16 v224, v225\n\t" \
  "s_waitcnt lgkmcnt(10)\n\t" \
  "v_smfmac_f32_16x16x64_bf16 v[20:23], v[212:215], v[176:183], v248\n\t" \
  "s_cselect_b32 %[rs], 0, %[rs]\n\t" \
  "v_lshlrev_b32_sdwa v150, 1, v150 dst_sel:DWORD dst_unused:UNUSED_PAD src0_sel:DWORD src1_sel:WORD_1\n\t" \
  "v_exp_f16_sdwa v224, v225 dst_sel:WORD_1 dst_unused:UNUSED_PRESERVE src0_sel:WORD_1\n\t" \
  "v_smfmac_f32_16x16x64_bf16 v[44:47], v[212:215], v[184:191], v248\n\t" \
  "s_cselect_b32 %[t0], -75920, %[ps]\n\t" \
  "ds_read_b64_tr_b16 v[212:213], v249 offset:12800\n\t" \
  "ds_read_b64_tr_b16 v[214:215], v249 offset:13056\n\t" \
  "ds_read_u16_d16_hi v150, v150 offset:0\n\t" \
  "v_add_u32 v252, %[t0], v252\n\t" \
  "s_waitcnt lgkmcnt(10)\n\t" \
  "v_mfma_f32_16x16x32_bf16 v[176:179], v[216:219], v[96:99], 0\n\t" \
  "v_pk_fma_f16 v224, v224, %[hc], %[hc]\n\t" \
  "v_lshlrev_b32_sdwa v151, 1, v151 dst_sel:DWORD dst_unused:UNUSED_PAD src0_sel:DWORD src1_sel:WORD_1\n\t" \
  "v_mfma_f32_16x16x32_bf16 v[184:187], v[216:219], v[108:111], 0\n\t" \
  "ds_read_b64_tr_b16 v[216:217], v249 offset:14848\n\t" \
  "ds_read_b64_tr_b16 v[218:219], v249 offset:15104\n\t" \
  "ds_read_u16_d16_hi v151, v151 offset:0\n\t" \
  "v_rcp_f16 v225, v224\n\t" \
  "s_waitcnt lgkmcnt(10)\n\t" \
  "v_mfma_f32_16x16x32_bf16 v[176:179], v[220:223], v[100:103], v[176:179]\n\t" \
  "v_lshlrev_b32_sdwa v158, 1, v158 dst_sel:DWORD dst_unused:UNUSED_PAD src0_sel:DWORD src1_sel:WORD_1\n\t" \
  "v_rcp_f16_sdwa v224, v224 dst_sel:DWORD dst_unused:UNUSED_PAD src0_sel:WORD_1\n\t" \
  "v_mfma_f32_16x16x32_bf16 v[184:187], v[220:223], v[112:115], v[184:187]\n\t" \
  "ds_read_b64_tr_b16 v[220:221], v249 offset:16896\n\t" \
  "ds_read_b64_tr_b16 v[222:223], v249 offset:17152\n\t" \
  "ds_read_u16_d16_hi v158, v158 offset:0\n\t" \
  "s_waitcnt lgkmcnt(10)\n\t" \
  "v_mfma_f32_16x16x32_bf16 v[176:179], v[208:211], v[104:107], v[176:179]\n\t" \
  "v_fma_mix_f32 v156, v156, v225, 0 op_sel_hi:[0,1,0]\n\t" \
  "v_lshlrev_b32_sdwa v159, 1, v159 dst_sel:DWORD dst_unused:UNUSED_PAD src0_sel:DWORD src1_sel:WORD_1\n\t" \
  "v_mfma_f32_16x16x32_bf16 v[184:187], v[208:211], v[116:119], v[184:187]\n\t" \
  "ds_read_b64_tr_b16 v[208:209], v250 offset:0\n\t" \
  "ds_read_b64_tr_b16 v[210:211], v250 offset:768\n\t" \
  "ds_read_u16_d16_hi v159, v159 offset:0\n\t" \
  "v_fma_mix_f32 v157, v157, v224, 0 op_sel_hi:[0,1,0]\n\t" \
  "s_waitcnt lgkmcnt(10)\n\t" \
  "v_mfma_f32_16x16x32_bf16 v[180:183], v[212:215], v[96:99], 0\n\t" \
  "v_lshlrev_b32_sdwa v176, 1, v176 dst_sel:DWORD dst_unused:UNUSED_PAD src0_sel:DWORD src1_sel:WORD_1\n\t" \
  "s_nop 0\n\t" \
  "v_mul_f32 v184, %[kk], v184\n\t" \
  "v_mfma_f32_16x16x32_bf16 v[188:191], v[212:215], v[108:111], 0\n\t" \
  "ds_read_b64_tr_b16 v[212:213], v250 offset:8\n\t" \
  "ds_read_b64_tr_b16 v[214:215], v250 offset:776\n\t" \
  "ds_read_u16_d16_hi v176, v176 offset:0\n\t" \
  "v_mul_f32 v185, %[kk], v185\n\t" \
  "v_cvt_pk_f16_f32 v224, v184, v185\n\t" \
  "s_waitcnt lgkmcnt(10)\n\t" \
  "v_mfma_f32_16x16x32_bf16 v[180:183], v[216:219], v[100:103], v[180:183]\n\t" \
  "v_lshlrev_b32_sdwa v177, 1, v177 dst_sel:DWORD dst_unused:UNUSED_PAD src0_sel:DWORD src1_sel:WORD_1\n\t" \
  "v_pk_fma_f16 v225, v224, v224, %[h1] neg_lo:[1,0,0] neg_hi:[1,0,0]\n\t" \
  "v_mfma_f32_16x16x32_bf16 v[188:191], v[216:219], v[112:115], v[188:191]\n\t" \
  "ds_read_b64_tr_b16 v[216:217], v250 offset:64\n\t" \
  "ds_read_b64_tr_b16 v[218:219], v250 offset:832\n\t" \
  "ds_read_u16_d16_hi v177, v177 offset:0\n\t" \
  "v_pk_mul_f16 v225, v224, v225\n\t" \
  "s_waitcnt lgkmcnt(10)\n\t" \
  "v_mfma_f32_16x16x32_bf16 v[180:183], v[220:223], v[104:107], v[180:183]\n\t" \
  "v_exp_f16 v224, v225\n\t" \
  "v_mfma_f32_16x16x32_bf16 v[188:191], v[220:223], v[116:119], v[188:191]\n\t" \
  "ds_read_b64_tr_b16 v[220:221], v250 offset:72\n\t" \
  "ds_read_b64_tr_b16 v[222:223], v250 offset:840\n\t" \
  "v_lshlrev_b32_sdwa v178, 1, v178 dst_sel:DWORD dst_unused:UNUSED_PAD src0_sel:DWORD src1_sel:WORD_1\n\t" \
  "v_exp_f16_sdwa v224, v225 dst_sel:WORD_1 dst_unused:UNUSED_PRESERVE src0_sel:WORD_1\n\t" \
  "s_waitcnt lgkmcnt(9)\n\t" \
  "v_smfmac_f32_16x16x64_bf16 v[0:3], v[208:211], v[144:151], v248\n\t" \
  "ds_read_u16_d16_hi v178, v178 offset:0\n\t" \
  "v_lshlrev_b32_sdwa v179, 1, v179 dst_sel:DWORD dst_unused:UNUSED_PAD src0_sel:DWORD src1_sel:WORD_1\n\t" \
  "s_waitcnt lgkmcnt(9)\n\t" \
  "v_smfmac_f32_16x16x64_bf16 v[24:27], v[208:211], v[152:159], v248\n\t" \
  "ds_read_b64_tr_b16 v[208:209], v250 offset:128\n\t" \
  "ds_read_b64_tr_b16 v[210:211], v250 offset:896\n\t" \
  "ds_read_u16_d16_hi v179, v179 offset:0\n\t" \
  "v_pk_fma_f16 v224, v224, %[hc], %[hc]\n\t" \
  "v_rcp_f16 v225, v224\n\t" \
  "s_waitcnt lgkmcnt(10)\n\t" \
  "v_smfmac_f32_16x16x64_bf16 v[4:7], v[212:215], v[144:151], v248\n\t" \
  "v_lshlrev_b32_sdwa v186, 1, v186 dst_sel:DWORD dst_unused:UNUSED_PAD src0_sel:DWORD src1_sel:WORD_1\n\t" \
  "v_rcp_f16_sdwa v224, v224 dst_sel:DWORD dst_unused:UNUSED_PAD src0_sel:WORD_1\n\t" \
  "v_smfmac_f32_16x16x64_bf16 v[28:31], v[212:215], v[152:159], v248\n\t" \
  "ds_read_b64_tr_b16 v[212:213], v250 offset:136\n\t" \
  "ds_read_b64_tr_b16 v[214:215], v250 offset:904\n\t" \
  "ds_read_u16_d16_hi v186, v186 offset:0\n\t" \
  "s_waitcnt lgkmcnt(10)\n\t" \
  "v_smfmac_f32_16x16x64_bf16 v[8:11], v[216:219], v[144:151], v248\n\t" \
  "v_lshlrev_b32_sdwa v187, 1, v187 dst_sel:DWORD dst_unused:UNUSED_PAD src0_sel:DWORD src1_sel:WORD_1\n\t" \
  "v_fma_mix_f32 v184, v184, v225, 0 op_sel_hi:[0,1,0]\n\t" \
  "v_smfmac_f32_16x16x64_bf16 v[32:35], v[216:219], v[152:159], v248\n\t" \
  "s_barrier\n\t" \
  "ds_read_b64_tr_b16 v[216:217], v249 offset:0\n\t" \
  "ds_read_b64_tr_b16 v[218:219], v249 offset:256\n\t" \
  "ds_read_b128 v[120:123], v251 offset:0\n\t" \
  "ds_read_b128 v[124:127], v251 offset:4096\n\t" \
  "ds_read_b128 v[128:131], v251 offset:8192\n\t" \
  "ds_read_u16_d16_hi v187, v187 offset:0\n\t" \
  "v_fma_mix_f32 v185, v185, v224, 0 op_sel_hi:[0,1,0]\n\t" \
  "s_waitcnt lgkmcnt(13)\n\t" \
  "v_smfmac_f32_16x16x64_bf16 v[12:15], v[220:223], v[144:151], v248\n\t" \
  "v_mul_f32 v188, %[kk], v188\n\t" \
  "v_lshlrev_b32_sdwa v180, 1, v180 dst_sel:DWORD dst_unused:UNUSED_PAD src0_sel:DWORD src1_sel:WORD_1\n\t" \
  "v_smfmac_f32_16x16x64_bf16 v[36:39], v[220:223], v[152:159], v248\n\t" \
  "ds_read_b64_tr_b16 v[220:221], v249 offset:2048\n\t" \
  "ds_read_b64_tr_b16 v[222:223], v249 offset:2304\n\t" \
  "ds_read_u16_d16_hi v180, v180 offset:0\n\t" \
  "v_mul_f32 v189, %[kk], v189\n\t" \
  "v_cvt_pk_f16_f32 v224, v188, v189\n\t" \
  "s_waitcnt lgkmcnt(13)\n\t" \
  "v_smfmac_f32_16x16x64_bf16 v[16:19], v[208:211], v[144:151], v248\n\t" \
  "v_pk_fma_f16 v225, v224, v224, %[h1] neg_lo:[1,0,0] neg_hi:[1,0,0]\n\t" \
  "v_lshlrev_b32_sdwa v181, 1, v181 dst_sel:DWORD dst_unused:UNUSED_PAD src0_sel:DWORD src1_sel:WORD_1\n\t" \
  "v_smfmac_f32_16x16x64_bf16 v[40:43], v[208:211], v[152:159], v248\n\t" \
  "ds_read_b64_tr_b16 v[208:209], v249 offset:4096\n\t" \
  "ds_read_b64_tr_b16 v[210:211], v249 offset:4352\n\t" \
  "ds_read_u16_d16_hi v181, v181 offset:0\n\t" \
  "v_pk_mul_f16 v225, v224, v225\n\t" \
  "v_exp_f16 v224, v225\n\t" \
  "s_waitcnt lgkmcnt(13)\n\t" \
  "v_smfmac_f32_16x16x64_bf16 v[20:23], v[212:215], v[144:151], v248\n\t" \
  "v_lshlrev_b32_sdwa v182, 1, v182 dst_sel:DWORD dst_unused:UNUSED_PAD src0_sel:DWORD src1_sel:WORD_1\n\t" \
  "v_exp_f16_sdwa v224, v225 dst_sel:WORD_1 dst_unused:UNUSED_PRESERVE src0_sel:WORD_1\n\t" \
  "v_smfmac_f32_16x16x64_bf16 v[44:47], v[212:215], v[152:159], v248\n\t" \
  "ds_read_b64_tr_b16 v[212:213], v249 offset:512\n\t" \
  "ds_read_b64_tr_b16 v[214:215], v249 offset:768\n\t" \
  "ds_read_u16_d16_hi v182, v182 offset:0\n\t" \
  "s_waitcnt lgkmcnt(13)\n\t" \
  "v_mfma_f32_16x16x32_bf16 v[144:147], v[216:219], v[96:99], 0\n\t" \
  "v_pk_fma_f16 v224, v224, %[hc], %[hc]\n\t" \
  "v_lshlrev_b32_sdwa v183, 1, v183 dst_sel:DWORD dst_unused:UNUSED_PAD src0_sel:DWORD src1_sel:WORD_1\n\t" \
  "v_mfma_f32_16x16x32_bf16 v[152:155], v[216:219], v[108:111], 0\n\t" \
  "s_waitcnt lgkmcnt(12)\n\t" \
  "ds_read_u16_d16_hi v183, v183 offset:0\n\t" \
  "v_rcp_f16 v225, v224\n\t" \
  "v_mfma_f32_16x16x32_bf16 v[160:163], v[216:219], v[120:123], 0\n\t" \
  "s_waitcnt lgkmcnt(8)\n\t" \
  "ds_read_b64_tr_b16 v[216:217], v249 offset:2560\n\t" \
  "v_lshlrev_b32_sdwa v190, 1, v190 dst_sel:DWORD dst_unused:UNUSED_PAD src0_sel:DWORD src1_sel:WORD_1\n\t" \
  "v_mfma_f32_16x16x32_bf16 v[144:147], v[220:223], v[100:103], v[144:147]\n\t" \
  "ds_read_u16_d16_hi v190, v190 offset:0\n\t" \
  "v_rcp_f16_sdwa v224, v224 dst_sel:DWORD dst_unused:UNUSED_PAD src0_sel:WORD_1\n\t" \
  "v_mfma_f32_16x16x32_bf16 v[152:155], v[220:223], v[112:115], v[152:155]\n\t" \
  "ds_read_b64_tr_b16 v[218:219], v249 offset:2816\n\t" \
  "v_fma_mix_f32 v188, v188, v225, 0 op_sel_hi:[0,1,0]\n\t" \
  "v_mfma_f32_16x16x32_bf16 v[160:163], v[220:223], v[124:127], v[160:163]\n\t" \
  "s_waitcnt lgkmcnt(8)\n\t" \
  "ds_read_b64_tr_b16 v[220:221], v249 offset:4608\n\t" \
  "v_lshlrev_b32_sdwa v191, 1, v191 dst_sel:DWORD dst_unused:UNUSED_PAD src0_sel:DWORD src1_sel:WORD_1\n\t" \
  "v_mfma_f32_16x16x32_bf16 v[144:147], v[208:211], v[104:107], v[144:147]\n\t" \
  "ds_read_u16_d16_hi v191, v191 offset:0\n\t" \
  "v_fma_mix_f32 v189, v189, v224, 0 op_sel_hi:[0,1,0]\n\t" \
  "v_mfma_f32_16x16x32_bf16 v[152:155], v[208:211], v[116:119], v[152:155]\n\t" \
  "ds_read_b64_tr_b16 v[222:223], v249 offset:4864\n\t" \
  "v_mfma_f32_16x16x32_bf16 v[160:163], v[208:211], v[128:131], v[160:163]\n\t" \
  "s_waitcnt lgkmcnt(8)\n\t" \
  "ds_read_b64_tr_b16 v[208:209], v250 offset:6160\n\t" \
  "s_nop 0\n\t" \
  "v_lshlrev_b32_sdwa v144, 1, v144 dst_sel:DWORD dst_unused:UNUSED_PAD src0_sel:DWORD src1_sel:WORD_1\n\t" \
  "v_mfma_f32_16x16x32_bf16 v[148:151], v[212:215], v[96:99], 0\n\t" \
  "ds_read_u16_d16_hi v144, v144 offset:0\n\t" \
  "v_mul_f32 v152, %[kk], v152\n\t" \
  "v_mfma_f32_16x16x32_bf16 v[156:159], v[212:215], v[108:111], 0\n\t" \
  "ds_read_b64_tr_b16 v[210:211], v250 offset:6928\n\t" \
  "v_mul_f32 v153, %[kk], v153\n\t" \
  "v_mfma_f32_16x16x32_bf16 v[164:167], v[212:215], v[120:123], 0\n\t" \
  "s_waitcnt lgkmcnt(6)\n\t" \
  "ds_read_b64_tr_b16 v[212:213], v250 offset:6168\n\t" \
  "v_lshlrev_b32_sdwa v145, 1, v145 dst_sel:DWORD dst_unused:UNUSED_PAD src0_sel:DWORD src1_sel:WORD_1\n\t" \
  "v_cvt_pk_f16_f32 v224, v152, v153\n\t" \
  "v_mfma_f32_16x16x32_bf16 v[148:151], v[216:219], v[100:103], v[148:151]\n\t" \
  "ds_read_u16_d16_hi v145, v145 offset:0\n\t" \
  "v_lshlrev_b32_sdwa v146, 1, v146 dst_sel:DWORD dst_unused:UNUSED_PAD src0_sel:DWORD src1_sel:WORD_1\n\t" \
  "v_mfma_f32_16x16x32_bf16 v[156:159], v[216:219], v[112:115], v[156:159]\n\t" \
  "ds_read_b64_tr_b16 v[214:215], v250 offset:6936\n\t" \
  "v_pk_fma_f16 v225, v224, v224, %[h1] neg_lo:[1,0,0] neg_hi:[1,0,0]\n\t" \
  "v_mfma_f32_16x16x32_bf16 v[164:167], v[216:219], v[124:127], v[164:167]\n\t" \
  "s_waitcnt lgkmcnt(6)\n\t" \
  "ds_read_u16_d16_hi v146, v146 offset:0\n\t" \
  "v_pk_mul_f16 v225, v224, v225\n\t" \
  "v_mfma_f32_16x16x32_bf16 v[148:151], v[220:223], v[104:107], v[148:151]\n\t" \
  "ds_read_b64_tr_b16 v[216:217], v250 offset:6224\n\t" \
  "v_lshlrev_b32_sdwa v147, 1, v147 dst_sel:DWORD dst_unused:UNUSED_PAD src0_sel:DWORD src1_sel:WORD_1\n\t" \
  "v_exp_f16 v224, v225\n\t" \
  "v_mfma_f32_16x16x32_bf16 v[156:159], v[220:223], v[116:119], v[156:159]\n\t" \
  "ds_read_u16_d16_hi v147, v147 offset:0\n\t" \
  "v_lshlrev_b32_sdwa v154, 1, v154 dst_sel:DWORD dst_unused:UNUSED_PAD src0_sel:DWORD src1_sel:WORD_1\n\t" \
  "v_mfma_f32_16x16x32_bf16 v[164:167], v[220:223], v[128:131], v[164:167]\n\t" \
  "s_waitcnt lgkmcnt(6)\n\t" \
  "ds_read_b64_tr_b16 v[218:219], v250 offset:6992\n\t" \
  "v_exp_f16_sdwa v224, v225 dst_sel:WORD_1 dst_unused:UNUSED_PRESERVE src0_sel:WORD_1\n\t" \
  "v_smfmac_f32_16x16x64_bf16 v[0:3], v[208:211], v[176:183], v248\n\t" \
  "ds_read_b64_tr_b16 v[220:221], v250 offset:6232\n\t" \
  "ds_read_b64_tr_b16 v[222:223], v250 offset:7000\n\t" \
  "ds_read_u16_d16_hi v154, v154 offset:0\n\t" \
  "v_smfmac_f32_16x16x64_bf16 v[24:27], v[208:211], v[184:191], v248\n\t" \
  "ds_read_b64_tr_b16 v[208:209], v250 offset:6288\n\t" \
  "ds_read_b64_tr_b16 v[210:211], v250 offset:7056\n\t" \
  "v_lshlrev_b32_sdwa v155, 1, v155 dst_sel:DWORD dst_unused:UNUSED_PAD src0_sel:DWORD src1_sel:WORD_1\n\t" \
  "v_pk_fma_f16 v224, v224, %[hc], %[hc]\n\t" \
  "v_lshlrev_b32_sdwa v160, 1, v160 dst_sel:DWORD dst_unused:UNUSED_PAD src0_sel:DWORD src1_sel:WORD_1\n\t" \
  "v_rcp_f16 v225, v224\n\t" \
  "s_waitcnt lgkmcnt(9)\n\t" \
  "v_smfmac_f32_16x16x64_bf16 v[4:7], v[212:215], v[176:183], v248\n\t" \
  "ds_read_b64 v[230:231], v252 offset:0\n\t" \
  "ds_read_b64 v[232:233], v252 offset:8\n\t" \
  "ds_read_b64 v[234:235], v252 offset:64\n\t" \
  "ds_read_u16_d16_hi v155, v155 offset:0\n\t" \
  "ds_read_u16_d16_hi v160, v160 offset:0\n\t" \
  "v_smfmac_f32_16x16x64_bf16 v[28:31], v[212:215], v[184:191], v248\n\t" \
  "ds_read_b64_tr_b16 v[212:213], v250 offset:6296\n\t" \
  "ds_read_b64_tr_b16 v[214:215], v250 offset:7064\n\t" \
  "ds_read_b64 v[236:237], v252 offset:72\n\t" \
  "v_lshlrev_b32_sdwa v161, 1, v161 dst_sel:DWORD dst_unused:UNUSED_PAD src0_sel:DWORD src1_sel:WORD_1\n\t" \
  "v_rcp_f16_sdwa v224, v224 dst_sel:DWORD dst_unused:UNUSED_PAD src0_sel:WORD_1\n\t" \
  "s_waitcnt lgkmcnt(13)\n\t" \
  "v_smfmac_f32_16x16x64_bf16 v[8:11], v[216:219], v[176:183], v248\n\t" \
  "ds_read_b64 v[242:243], v252 offset:128\n\t" \
  "ds_read_b64 v[244:245], v252 offset:136\n\t" \
  "ds_read_u16_d16_hi v161, v161 offset:0\n\t" \
  "v_lshlrev_b32_sdwa v162, 1, v162 dst_sel:DWORD dst_unused:UNUSED_PAD src0_sel:DWORD src1_sel:WORD_1\n\t" \
  "v_smfmac_f32_16x16x64_bf16 v[32:35], v[216:219], v[184:191], v248\n\t" \
  "s_barrier\n\t" \
  "ds_read_b64_tr_b16 v[216:217], v249 offset:6144\n\t" \
  "ds_read_b64_tr_b16 v[218:219], v249 offset:6400\n\t" \
  "ds_read_u16_d16_hi v162, v162 offset:0\n\t" \
  "v_fma_mix_f32 v152, v152, v225, 0 op_sel_hi:[0,1,0]\n\t" \
  "v_fma_mix_f32 v153, v153, v224, 0 op_sel_hi:[0,1,0]\n\t" \
  "v_lshlrev_b32_sdwa v163, 1, v163 dst_sel:DWORD dst_unused:UNUSED_PAD src0_sel:DWORD src1_sel:WORD_1\n\t" \
  "s_waitcnt lgkmcnt(14)\n\t" \
  "v_smfmac_f32_16x16x64_bf16 v[12:15], v[220:223], v[176:183], v248\n\t" \
  "ds_read_u16_d16_hi v163, v163 offset:0\n\t" \
  "v_mul_f32 v156, %[kk], v156\n\t" \
  "v_smfmac_f32_16x16x64_bf16 v[36:39], v[220:223], v[184:191], v248\n\t" \
  "ds_read_b64_tr_b16 v[220:221], v249 offset:8192\n\t" \
  "ds_read_b64_tr_b16 v[222:223], v249 offset:8448\n\t" \
  "v_lshlrev_b32_sdwa v148, 1, v148 dst_sel:DWORD dst_unused:UNUSED_PAD src0_sel:DWORD src1_sel:WORD_1\n\t" \
  "v_mul_f32 v157, %[kk], v157\n\t" \
  "v_cvt_pk_f16_f32 v224, v156, v157\n\t" \
  "v_lshlrev_b32_sdwa v149, 1, v149 dst_sel:DWORD dst_unused:UNUSED_PAD src0_sel:DWORD src1_sel:WORD_1\n\t" \
  "v_smfmac_f32_16x16x64_bf16 v[16:19], v[208:211], v[176:183], v248\n\t" \
  "ds_read_u16_d16_hi v148, v148 offset:0\n\t" \
  "ds_read_u16_d16_hi v149, v149 offset:0\n\t" \
  "v_pk_fma_f16 v225, v224, v224, %[h1] neg_lo:[1,0,0] neg_hi:[1,0,0]\n\t" \
  "v_smfmac_f32_16x16x64_bf16 v[40:43], v[208:211], v[184:191], v248\n\t" \
  "ds_read_b64_tr_b16 v[208:209], v249 offset:10240\n\t" \
  "ds_read_b64_tr_b16 v[210:211], v249 offset:10496\n\t" \
  "v_pk_mul_f16 v225, v224, v225\n\t" \
  "v_lshlrev_b32_sdwa v150, 1, v150 dst_sel:DWORD dst_unused:UNUSED_PAD src0_sel:DWORD src1_sel:WORD_1\n\t" \
  "v_exp_f16 v224, v225\n\t" \
  "s_waitcnt lgkmcnt(14)\n\t" \
  "v_smfmac_f32_16x16x64_bf16 v[20:23], v[212:215], v[176:183], v248\n\t" \
  "ds_read_u16_d16_hi v150, v150 offset:0\n\t" \
  "v_lshlrev_b32_sdwa v151, 1, v151 dst_sel:DWORD dst_unused:UNUSED_PAD src0_sel:DWORD src1_sel:WORD_1\n\t" \
  "v_smfmac_f32_16x16x64_bf16 v[44:47], v[212:215], v[184:191], v248\n\t" \
  "ds_read_b64_tr_b16 v[212:213], v249 offset:6656\n\t" \
  "ds_read_b64_tr_b16 v[214:215], v249 offset:6912\n\t" \
  "ds_read_u16_d16_hi v151, v151 offset:0\n\t" \
  "v_mfma_f32_16x16x16_bf16 v[48:51], v[246:247], v[230:231], 0\n\t" \
  "v_exp_f16_sdwa v224, v225 dst_sel:WORD_1 dst_unused:UNUSED_PRESERVE src0_sel:WORD_1\n\t" \
  "v_lshlrev_b32_sdwa v158, 1, v158 dst_sel:DWORD dst_unused:UNUSED_PAD src0_sel:DWORD src1_sel:WORD_1\n\t" \
  "s_waitcnt lgkmcnt(12)\n\t" \
  "v_mfma_f32_16x16x32_bf16 v[176:179], v[216:219], v[96:99], 0\n\t" \
  "ds_read_u16_d16_hi v158, v158 offset:0\n\t" \
  "v_pk_fma_f16 v224, v224, %[hc], %[hc]\n\t" \
  "v_mfma_f32_16x16x32_bf16 v[184:187], v[216:219], v[108:111], 0\n\t" \
  "v_lshlrev_b32_sdwa v159, 1, v159 dst_sel:DWORD dst_unused:UNUSED_PAD src0_sel:DWORD src1_sel:WORD_1\n\t" \
  "v_rcp_f16 v225, v224\n\t" \
  "v_mfma_f32_16x16x16_bf16 v[52:55], v[246:247], v[232:233], 0\n\t" \
  "ds_read_u16_d16_hi v159, v159 offset:0\n\t" \
  "v_lshlrev_b32_sdwa v164, 1, v164 dst_sel:DWORD dst_unused:UNUSED_PAD src0_sel:DWORD src1_sel:WORD_1\n\t" \
  "v_mfma_f32_16x16x32_bf16 v[192:195], v[216:219], v[120:123], 0\n\t" \
  "s_waitcnt lgkmcnt(10)\n\t" \
  "ds_read_b64_tr_b16 v[216:217], v249 offset:8704\n\t" \
  "v_rcp_f16_sdwa v224, v224 dst_sel:DWORD dst_unused:UNUSED_PAD src0_sel:WORD_1\n\t" \
  "v_mfma_f32_16x16x32_bf16 v[176:179], v[220:223], v[100:103], v[176:179]\n\t" \
  "ds_read_u16_d16_hi v164, v164 offset:0\n\t" \
  "v_lshlrev_b32_sdwa v165, 1, v165 dst_sel:DWORD dst_unused:UNUSED_PAD src0_sel:DWORD src1_sel:WORD_1\n\t" \
  "v_mfma_f32_16x16x32_bf16 v[184:187], v[220:223], v[112:115], v[184:187]\n\t" \
  "ds_read_b64_tr_b16 v[218:219], v249 offset:8960\n\t" \
  "v_fma_mix_f32 v156, v156, v225, 0 op_sel_hi:[0,1,0]\n\t" \
  "v_mfma_f32_16x16x32_bf16 v[192:195], v[220:223], v[124:127], v[192:195]\n\t" \
  "ds_read_u16_d16_hi v165, v165 offset:0\n\t" \
  "v_lshlrev_b32_sdwa v166, 1, v166 dst_sel:DWORD dst_unused:UNUSED_PAD src0_sel:DWORD src1_sel:WORD_1\n\t" \
  "v_mfma_f32_16x16x16_bf16 v[56:59], v[246:247], v[234:235], 0\n\t" \
  "s_waitcnt lgkmcnt(10)\n\t" \
  "ds_read_b64_tr_b16 v[220:221], v249 offset:10752\n\t" \
  "v_fma_mix_f32 v157, v157, v224, 0 op_sel_hi:[0,1,0]\n\t" \
  "v_mfma_f32_16x16x32_bf16 v[176:179], v[208:211], v[104:107], v[176:179]\n\t" \
  "ds_read_u16_d16_hi v166, v166 offset:0\n\t" \
  "v_lshlrev_b32_sdwa v167, 1, v167 dst_sel:DWORD dst_unused:UNUSED_PAD src0_sel:DWORD src1_sel:WORD_1\n\t" \
  "v_mfma_f32_16x16x32_bf16 v[184:187], v[208:211], v[116:119], v[184:187]\n\t" \
  "ds_read_b64_tr_b16 v[222:223], v249 offset:11008\n\t" \
  "v_mfma_f32_16x16x16_bf16 v[60:63], v[246:247], v[236:237], 0\n\t" \
  "ds_read_u16_d16_hi v167, v167 offset:0\n\t" \
  "v_mfma_f32_16x16x32_bf16 v[192:195], v[208:211], v[128:131], v[192:195]\n\t" \
  "s_waitcnt lgkmcnt(11)\n\t" \
  "ds_read_b64_tr_b16 v[208:209], v250 offset:12320\n\t" \
  "v_lshlrev_b32_sdwa v176, 1, v176 dst_sel:DWORD dst_unused:UNUSED_PAD src0_sel:DWORD src1_sel:WORD_1\n\t" \
  "v_mfma_f32_16x16x32_bf16 v[180:183], v[212:215], v[96:99], 0\n\t" \
  "ds_read_u16_d16_hi v176, v176 offset:0\n\t" \
  "v_mul_f32 v184, %[kk], v184\n\t" \
  "v_mfma_f32_16x16x32_bf16 v[188:191], v[212:215], v[108:111], 0\n\t" \
  "ds_read_b64_tr_b16 v[210:211], v250 offset:13088\n\t" \
  "v_mul_f32 v185, %[kk], v185\n\t" \
  "v_mfma_f32_16x16x32_bf16 v[196:199], v[212:215], v[120:123], 0\n\t" \
  "ds_read_b64_tr_b16 v[212:213], v250 offset:12328\n\t" \
  "v_lshlrev_b32_sdwa v177, 1, v177 dst_sel:DWORD dst_unused:UNUSED_PAD src0_sel:DWORD src1_sel:WORD_1\n\t" \
  "v_mfma_f32_16x16x16_bf16 v[64:67], v[246:247], v[242:243], 0\n\t" \
  "s_waitcnt lgkmcnt(9)\n\t" \
  "ds_read_u16_d16_hi v177, v177 offset:0\n\t" \
  "v_cvt_pk_f16_f32 v224, v184, v185\n\t" \
  "v_mfma_f32_16x16x32_bf16 v[180:183], v[216:219], v[100:103], v[180:183]\n\t" \
  "ds_read_b64_tr_b16 v[214:215], v250 offset:13096\n\t" \
  "v_lshlrev_b32_sdwa v178, 1, v178 dst_sel:DWORD dst_unused:UNUSED_PAD src0_sel:DWORD src1_sel:WORD_1\n\t" \
  "v_mfma_f32_16x16x32_bf16 v[188:191], v[216:219], v[112:115], v[188:191]\n\t" \
  "ds_read_u16_d16_hi v178, v178 offset:0\n\t" \
  "v_pk_fma_f16 v225, v224, v224, %[h1] neg_lo:[1,0,0] neg_hi:[1,0,0]\n\t" \
  "v_mfma_f32_16x16x16_bf16 v[68:71], v[246:247], v[244:245], 0\n\t" \
  "v_pk_mul_f16 v225, v224, v225\n\t" \
  "v_lshlrev_b32_sdwa v179, 1, v179 dst_sel:DWORD dst_unused:UNUSED_PAD src0_sel:DWORD src1_sel:WORD_1\n\t" \
  "v_mfma_f32_16x16x32_bf16 v[196:199], v[216:219], v[124:127], v[196:199]\n\t" \
  "s_waitcnt lgkmcnt(8)\n\t" \
  "ds_read_b64_tr_b16 v[216:217], v250 offset:12384\n\t" \
  "v_exp_f16 v224, v225\n\t" \
  "v_mfma_f32_16x16x32_bf16 v[180:183], v[220:223], v[104:107], v[180:183]\n\t" \
  "ds_read_u16_d16_hi v179, v179 offset:0\n\t" \
  "v_lshlrev_b32_sdwa v186, 1, v186 dst_sel:DWORD dst_unused:UNUSED_PAD src0_sel:DWORD src1_sel:WORD_1\n\t" \
  "v_mfma_f32_16x16x32_bf16 v[188:191], v[220:223], v[116:119], v[188:191]\n\t" \
  "ds_read_b64_tr_b16 v[218:219], v250 offset:13152\n\t" \
  "v_exp_f16_sdwa v224, v225 dst_sel:WORD_1 dst_unused:UNUSED_PRESERVE src0_sel:WORD_1\n\t" \
  "v_mfma_f32_16x16x32_bf16 v[196:199], v[220:223], v[128:131], v[196:199]\n\t" \
  "s_waitcnt lgkmcnt(7)\n\t" \
  "ds_read_u16_d16_hi v186, v186 offset:0\n\t" \
  "v_lshlrev_b32_sdwa v187, 1, v187 dst_sel:DWORD dst_unused:UNUSED_PAD src0_sel:DWORD src1_sel:WORD_1\n\t" \
  "v_smfmac_f32_16x16x64_bf16 v[0:3], v[208:211], v[144:151], v248\n\t" \
  "ds_read_b64_tr_b16 v[220:221], v250 offset:12392\n\t" \
  "v_pk_fma_f16 v224, v224, %[hc], %[hc]\n\t" \
  "v_smfmac_f32_16x16x64_bf16 v[24:27], v[208:211], v[152:159], v248\n\t" \
  "ds_read_u16_d16_hi v187, v187 offset:0\n\t" \
  "v_lshlrev_b32_sdwa v192, 1, v192 dst_sel:DWORD dst_unused:UNUSED_PAD src0_sel:DWORD src1_sel:WORD_1\n\t" \
  "v_smfmac_f32_16x16x64_bf16 v[48:51], v[208:211], v[160:167], v248\n\t" \
  "s_waitcnt lgkmcnt(7)\n\t" \
  "ds_read_b64_tr_b16 v[222:223], v250 offset:13160\n\t" \
  "v_rcp_f16 v225, v224\n\t" \
  "v_smfmac_f32_16x16x64_bf16 v[4:7], v[212:215], v[144:151], v248\n\t" \
  "ds_read_u16_d16_hi v192, v192 offset:0\n\t" \
  "v_lshlrev_b32_sdwa v193, 1, v193 dst_sel:DWORD dst_unused:UNUSED_PAD src0_sel:DWORD src1_sel:WORD_1\n\t" \
  "v_smfmac_f32_16x16x64_bf16 v[28:31], v[212:215], v[152:159], v248\n\t" \
  "ds_read_b64_tr_b16 v[208:209], v250 offset:12448\n\t" \
  "v_rcp_f16_sdwa v224, v224 dst_sel:DWORD dst_unused:UNUSED_PAD src0_sel:WORD_1\n\t" \
  "v_smfmac_f32_16x16x64_bf16 v[52:55], v[212:215], v[160:167], v248\n\t" \
  "s_waitcnt lgkmcnt(6)\n\t" \
  "ds_read_b64_tr_b16 v[210:211], v250 offset:13216\n\t" \
  "v_lshlrev_b32_sdwa v194, 1, v194 dst_sel:DWORD dst_unused:UNUSED_PAD src0_sel:DWORD src1_sel:WORD_1\n\t" \
  "v_smfmac_f32_16x16x64_bf16 v[8:11], v[216:219], v[144:151], v248\n\t" \
  "ds_read_u16_d16_hi v193, v193 offset:0\n\t" \
  "v_fma_mix_f32 v184, v184, v225, 0 op_sel_hi:[0,1,0]\n\t" \
  "v_smfmac_f32_16x16x64_bf16 v[32:35], v[216:219], v[152:159], v248\n\t" \
  "ds_read_b64_tr_b16 v[212:213], v250 offset:12456\n\t" \
  "v_fma_mix_f32 v185, v185, v224, 0 op_sel_hi:[0,1,0]\n\t" \
  "v_smfmac_f32_16x16x64_bf16 v[56:59], v[216:219], v[160:167], v248\n\t" \
  "s_waitcnt lgkmcnt(5)\n\t" \
  "ds_read_b64_tr_b16 v[214:215], v250 offset:13224\n\t" \
  "s_barrier\n\t" \
  "ds_read_u16_d16_hi v194, v194 offset:0\n\t" \
  "v_lshlrev_b32_sdwa v195, 1, v195 dst_sel:DWORD dst_unused:UNUSED_PAD src0_sel:DWORD src1_sel:WORD_1\n\t" \
  "v_smfmac_f32_16x16x64_bf16 v[12:15], v[220:223], v[144:151], v248\n\t" \
  "ds_read_b64_tr_b16 v[216:217], v249 offset:12288\n\t" \
  "v_mul_f32 v188, %[kk], v188\n\t" \
  "v_smfmac_f32_16x16x64_bf16 v[36:39], v[220:223], v[152:159], v248\n\t" \
  "ds_read_u16_d16_hi v195, v195 offset:0\n\t" \
  "v_lshlrev_b32_sdwa v180, 1, v180 dst_sel:DWORD dst_unused:UNUSED_PAD src0_sel:DWORD src1_sel:WORD_1\n\t" \
  "v_mul_f32 v189, %[kk], v189\n\t" \
  "v_smfmac_f32_16x16x64_bf16 v[60:63], v[220:223], v[160:167], v248\n\t" \
  "s_waitcnt lgkmcnt(6)\n\t" \
  "ds_read_b64_tr_b16 v[218:219], v249 offset:12544\n\t" \
  "v_cvt_pk_f16_f32 v224, v188, v189\n\t" \
  "v_lshlrev_b32_sdwa v181, 1, v181 dst_sel:DWORD dst_unused:UNUSED_PAD src0_sel:DWORD src1_sel:WORD_1\n\t" \
  "v_smfmac_f32_16x16x64_bf16 v[16:19], v[208:211], v[144:151], v248\n\t" \
  "s_add_u32 %[rs], %[rs], 1\n\t" \
  "ds_read_u16_d16_hi v180, v180 offset:0\n\t" \
  "v_pk_fma_f16 v225, v224, v224, %[h1] neg_lo:[1,0,0] neg_hi:[1,0,0]\n\t" \
  "v_smfmac_f32_16x16x64_bf16 v[40:43], v[208:211], v[152:159], v248\n\t" \
  "s_cmp_eq_u32 %[rs], 6\n\t" \
  "ds_read_b64_tr_b16 v[220:221], v249 offset:14336\n\t" \
  "v_pk_mul_f16 v225, v224, v225\n\t" \
  "v_smfmac_f32_16x16x64_bf16 v[64:67], v[208:211], v[160:167], v248\n\t" \
  "s_waitcnt lgkmcnt(6)\n\t" \
  "ds_read_b64_tr_b16 v[222:223], v249 offset:14592\n\t" \
  "v_lshlrev_b32_sdwa v182, 1, v182 dst_sel:DWORD dst_unused:UNUSED_PAD src0_sel:DWORD src1_sel:WORD_1\n\t" \
  "v_exp_f16 v224, v225\n\t" \
  "v_smfmac_f32_16x16x64_bf16 v[20:23], v[212:215], v[144:151], v248\n\t" \
  "s_cselect_b32 %[rs], 0, %[rs]\n\t" \
  "ds_read_u16_d16_hi v181, v181 offset:0\n\t" \
  "v_lshlrev_b32_sdwa v183, 1, v183 dst_sel:DWORD dst_unused:UNUSED_PAD src0_sel:DWORD src1_sel:WORD_1\n\t" \
  "v_smfmac_f32_16x16x64_bf16 v[44:47], v[212:215], v[152:159], v248\n\t" \
  "s_cselect_b32 %[t0], -75920, %[ps]\n\t" \
  "ds_read_b64_tr_b16 v[208:209], v249 offset:16384\n\t" \
  "v_exp_f16_sdwa v224, v225 dst_sel:WORD_1 dst_unused:UNUSED_PRESERVE src0_sel:WORD_1\n\t" \
  "v_smfmac_f32_16x16x64_bf16 v[68:71], v[212:215], v[160:167], v248\n\t" \
  "s_waitcnt lgkmcnt(5)\n\t" \
  "ds_read_b64_tr_b16 v[210:211], v249 offset:16640\n\t" \
  "ds_read_u16_d16_hi v182, v182 offset:0\n\t" \
  "v_add_u32 v252, %[t0], v252\n\t" \
  "v_lshlrev_b32_sdwa v190, 1, v190 dst_sel:DWORD dst_unused:UNUSED_PAD src0_sel:DWORD src1_sel:WORD_1\n\t" \
  "v_mfma_f32_16x16x32_bf16 v[144:147], v[216:219], v[96:99], 0\n\t" \
  "ds_read_b64_tr_b16 v[212:213], v249 offset:12800\n\t" \
  "v_pk_fma_f16 v224, v224, %[hc], %[hc]\n\t" \
  "v_mfma_f32_16x16x32_bf16 v[152:155], v[216:219], v[108:111], 0\n\t" \
  "ds_read_u16_d16_hi v183, v183 offset:0\n\t" \
  "v_lshlrev_b32_sdwa v191, 1, v191 dst_sel:DWORD dst_unused:UNUSED_PAD src0_sel:DWORD src1_sel:WORD_1\n\t" \
  "v_mfma_f32_16x16x32_bf16 v[160:163], v[216:219], v[120:123], 0\n\t" \
  "s_waitcnt lgkmcnt(6)\n\t" \
  "ds_read_b64_tr_b16 v[214:215], v249 offset:13056\n\t" \
  "ds_read_u16_d16_hi v190, v190 offset:0\n\t" \
  "v_rcp_f16 v225, v224\n\t" \
  "v_mfma_f32_16x16x32_bf16 v[144:147], v[220:223], v[100:103], v[144:147]\n\t" \
  "ds_read_b64_tr_b16 v[216:217], v249 offset:14848\n\t" \
  "v_lshlrev_b32_sdwa v196, 1, v196 dst_sel:DWORD dst_unused:UNUSED_PAD src0_sel:DWORD src1_sel:WORD_1\n\t" \
  "v_mfma_f32_16x16x32_bf16 v[152:155], v[220:223], v[112:115], v[152:155]\n\t" \
  "ds_read_u16_d16_hi v191, v191 offset:0\n\t" \
  "v_rcp_f16_sdwa v224, v224 dst_sel:DWORD dst_unused:UNUSED_PAD src0_sel:WORD_1\n\t" \
  "v_mfma_f32_16x16x32_bf16 v[160:163], v[220:223], v[124:127], v[160:163]\n\t" \
  "s_waitcnt lgkmcnt(7)\n\t" \
  "ds_read_b64_tr_b16 v[218:219], v249 offset:15104\n\t" \
  "v_lshlrev_b32_sdwa v197, 1, v197 dst_sel:DWORD dst_unused:UNUSED_PAD src0_sel:DWORD src1_sel:WORD_1\n\t" \
  "v_mfma_f32_16x16x32_bf16 v[144:147], v[208:211], v[104:107], v[144:147]\n\t" \
  "ds_read_u16_d16_hi v196, v196 offset:0\n\t" \
  "v_fma_mix_f32 v188, v188, v225, 0 op_sel_hi:[0,1,0]\n\t" \
  "v_mfma_f32_16x16x32_bf16 v[152:155], v[208:211], v[116:119], v[152:155]\n\t" \
  "ds_read_b64_tr_b16 v[220:221], v249 offset:16896\n\t" \
  "v_lshlrev_b32_sdwa v198, 1, v198 dst_sel:DWORD dst_unused:UNUSED_PAD src0_sel:DWORD src1_sel:WORD_1\n\t" \
  "v_fma_mix_f32 v189, v189, v224, 0 op_sel_hi:[0,1,0]\n\t" \
  "v_mfma_f32_16x16x32_bf16 v[160:163], v[208:211], v[128:131], v[160:163]\n\t" \
  "s_waitcnt lgkmcnt(6)\n\t" \
  "ds_read_b64_tr_b16 v[222:223], v249 offset:17152\n\t" \
  "ds_read_u16_d16_hi v197, v197 offset:0\n\t" \
  "v_lshlrev_b32_sdwa v199, 1, v199 dst_sel:DWORD dst_unused:UNUSED_PAD src0_sel:DWORD src1_sel:WORD_1\n\t" \
  "v_mfma_f32_16x16x32_bf16 v[148:151], v[212:215], v[96:99], 0\n\t" \
  "ds_read_b64_tr_b16 v[208:209], v250 offset:18480\n\t" \
  "v_lshlrev_b32_sdwa v144, 1, v144 dst_sel:DWORD dst_unused:UNUSED_PAD src0_sel:DWORD src1_sel:WORD_1\n\t" \
  "v_mfma_f32_16x16x32_bf16 v[156:159], v[212:215], v[108:111], 0\n\t" \
  "ds_read_u16_d16_hi v198, v198 offset:0\n\t" \
  "v_mul_f32 v152, %[kk], v152\n\t" \
  "v_mfma_f32_16x16x32_bf16 v[164:167], v[212:215], v[120:123], 0\n\t" \
  "s_waitcnt lgkmcnt(6)\n\t" \
  "ds_read_b64_tr_b16 v[210:211], v250 offset:19248\n\t" \
  "ds_read_u16_d16_hi v199, v199 offset:0\n\t" \
  "v_mul_f32 v153, %[kk], v153\n\t" \
  "v_lshlrev_b32_sdwa v145, 1, v145 dst_sel:DWORD dst_unused:UNUSED_PAD src0_sel:DWORD src1_sel:WORD_1\n\t" \
  "v_cvt_pk_f16_f32 v224, v152, v153\n\t" \
  "v_mfma_f32_16x16x32_bf16 v[148:151], v[216:219], v[100:103], v[148:151]\n\t" \
  "ds_read_b64_tr_b16 v[212:213], v250 offset:18488\n\t" \
  "v_lshlrev_b32_sdwa v146, 1, v146 dst_sel:DWORD dst_unused:UNUSED_PAD src0_sel:DWORD src1_sel:WORD_1\n\t" \
  "v_mfma_f32_16x16x32_bf16 v[156:159], v[216:219], v[112:115], v[156:159]\n\t" \
  "ds_read_u16_d16_hi v144, v144 offset:0\n\t" \
  "v_pk_fma_f16 v225, v224, v224, %[h1] neg_lo:[1,0,0] neg_hi:[1,0,0]\n\t" \
  "v_mfma_f32_16x16x32_bf16 v[164:167], v[216:219], v[124:127], v[164:167]\n\t" \
  "s_waitcnt lgkmcnt(7)\n\t" \
  "ds_read_b64_tr_b16 v[214:215], v250 offset:19256\n\t" \
  "ds_read_u16_d16_hi v145, v145 offset:0\n\t" \
  "v_pk_mul_f16 v225, v224, v225\n\t" \
  "v_mfma_f32_16x16x32_bf16 v[148:151], v[220:223], v[104:107], v[148:151]\n\t" \
  "ds_read_b64_tr_b16 v[216:217], v250 offset:18544\n\t" \
  "v_lshlrev_b32_sdwa v147, 1, v147 dst_sel:DWORD dst_unused:UNUSED_PAD src0_sel:DWORD src1_sel:WORD_1\n\t" \
  "v_exp_f16 v224, v225\n\t" \
  "v_mfma_f32_16x16x32_bf16 v[156:159], v[220:223], v[116:119], v[156:159]\n\t" \
  "ds_read_u16_d16_hi v146, v146 offset:0\n\t" \
  "v_lshlrev_b32_sdwa v154, 1, v154 dst_sel:DWORD dst_unused:UNUSED_PAD src0_sel:DWORD src1_sel:WORD_1\n\t" \
  "v_mfma_f32_16x16x32_bf16 v[164:167], v[220:223], v[128:131], v[164:167]\n\t" \
  "s_waitcnt lgkmcnt(7)\n\t" \
  "ds_read_b64_tr_b16 v[218:219], v250 offset:19312\n\t" \
  "ds_read_u16_d16_hi v147, v147 offset:0\n\t" \
  "v_exp_f16_sdwa v224, v225 dst_sel:WORD_1 dst_unused:UNUSED_PRESERVE src0_sel:WORD_1\n\t" \
  "v_smfmac_f32_16x16x64_bf16 v[0:3], v[208:211], v[176:183], v248\n\t" \
  "ds_read_b64_tr_b16 v[220:221], v250 offset:18552\n\t" \
  "v_lshlrev_b32_sdwa v155, 1, v155 dst_sel:DWORD dst_unused:UNUSED_PAD src0_sel:DWORD src1_sel:WORD_1\n\t" \
  "v_smfmac_f32_16x16x64_bf16 v[24:27], v[208:211], v[184:191], v248\n\t" \
  "s_waitcnt lgkmcnt(9)\n\t" \
  "ds_read_u16_d16_hi v154, v154 offset:0\n\t" \
  "v_pk_fma_f16 v224, v224, %[hc], %[hc]\n\t" \
  "v_smfmac_f32_16x16x64_bf16 v[48:51], v[208:211], v[192:199], v248\n\t" \
  "s_waitcnt lgkmcnt(7)\n\t" \
  "ds_read_b64_tr_b16 v[222:223], v250 offset:19320\n\t" \
  "v_lshlrev_b32_sdwa v160, 1, v160 dst_sel:DWORD dst_unused:UNUSED_PAD src0_sel:DWORD src1_sel:WORD_1\n\t" \
  "v_rcp_f16 v225, v224\n\t" \
  "v_smfmac_f32_16x16x64_bf16 v[4:7], v[212:215], v[176:183], v248\n\t" \
  "ds_read_u16_d16_hi v155, v155 offset:0\n\t" \
  "v_lshlrev_b32_sdwa v161, 1, v161 dst_sel:DWORD dst_unused:UNUSED_PAD src0_sel:DWORD src1_sel:WORD_1\n\t" \
  "v_smfmac_f32_16x16x64_bf16 v[28:31], v[212:215], v[184:191], v248\n\t" \
  "ds_read_b64_tr_b16 v[208:209], v250 offset:18608\n\t" \
  "v_rcp_f16_sdwa v224, v224 dst_sel:DWORD dst_unused:UNUSED_PAD src0_sel:WORD_1\n\t" \
  "v_smfmac_f32_16x16x64_bf16 v[52:55], v[212:215], v[192:199], v248\n\t" \
  "s_waitcnt lgkmcnt(6)\n\t" \
  "ds_read_b64_tr_b16 v[210:211], v250 offset:19376\n\t" \
  "ds_read_u16_d16_hi v160, v160 offset:0\n\t" \
  "v_smfmac_f32_16x16x64_bf16 v[8:11], v[216:219], v[176:183], v248\n\t" \
  "ds_read_b64_tr_b16 v[212:213], v250 offset:18616\n\t" \
  "v_lshlrev_b32_sdwa v162, 1, v162 dst_sel:DWORD dst_unused:UNUSED_PAD src0_sel:DWORD src1_sel:WORD_1\n\t" \
  "v_smfmac_f32_16x16x64_bf16 v[32:35], v[216:219], v[184:191], v248\n\t" \
  "ds_read_u16_d16_hi v161, v161 offset:0\n\t" \
  "v_fma_mix_f32 v152, v152, v225, 0 op_sel_hi:[0,1,0]\n\t" \
  "v_smfmac_f32_16x16x64_bf16 v[56:59], v[216:219], v[192:199], v248\n\t" \
  "s_waitcnt lgkmcnt(6)\n\t" \
  "ds_read_b64_tr_b16 v[214:215], v250 offset:19384\n\t" \
  "s_barrier\n\t" \
  "ds_read_b128 v[132:135], v251 offset:0\n\t" \
  "v_fma_mix_f32 v153, v153, v224, 0 op_sel_hi:[0,1,0]\n\t" \
  "v_lshlrev_b32_sdwa v163, 1, v163 dst_sel:DWORD dst_unused:UNUSED_PAD src0_sel:DWORD src1_sel:WORD_1\n\t" \
  "v_smfmac_f32_16x16x64_bf16 v[12:15], v[220:223], v[176:183], v248\n\t" \
  "ds_read_b64_tr_b16 v[216:217], v249 offset:0\n\t" \
  "ds_read_b128 v[136:139], v251 offset:4096\n\t" \
  "v_mul_f32 v156, %[kk], v156\n\t" \
  "v_smfmac_f32_16x16x64_bf16 v[36:39], v[220:223], v[184:191], v248\n\t" \
  "ds_read_b64_tr_b16 v[218:219], v249 offset:256\n\t" \
  "v_lshlrev_b32_sdwa v148, 1, v148 dst_sel:DWORD dst_unused:UNUSED_PAD src0_sel:DWORD src1_sel:WORD_1\n\t" \
  "v_mul_f32 v157, %[kk], v157\n\t" \
  "v_smfmac_f32_16x16x64_bf16 v[60:63], v[220:223], v[192:199], v248\n\t" \
  "s_waitcnt lgkmcnt(8)\n\t" \
  "ds_read_b128 v[140:143], v251 offset:8192\n\t" \
  "ds_read_b64_tr_b16 v[220:221], v249 offset:2048\n\t" \
  "v_cvt_pk_f16_f32 v224, v156, v157\n\t" \
  "v_lshlrev_b32_sdwa v149, 1, v149 dst_sel:DWORD dst_unused:UNUSED_PAD src0_sel:DWORD src1_sel:WORD_1\n\t" \
  "v_smfmac_f32_16x16x64_bf16 v[16:19], v[208:211], v[176:183], v248\n\t" \
  "ds_read_u16_d16_hi v162, v162 offset:0\n\t" \
  "ds_read_b64_tr_b16 v[222:223], v249 offset:2304\n\t" \
  "v_pk_fma_f16 v225, v224, v224, %[h1] neg_lo:[1,0,0] neg_hi:[1,0,0]\n\t" \
  "v_smfmac_f32_16x16x64_bf16 v[40:43], v[208:211], v[184:191], v248\n\t" \
  "ds_read_u16_d16_hi v163, v163 offset:0\n\t" \
  "v_pk_mul_f16 v225, v224, v225\n\t" \
  "v_smfmac_f32_16x16x64_bf16 v[64:67], v[208:211], v[192:199], v248\n\t" \
  "s_waitcnt lgkmcnt(9)\n\t" \
  "ds_read_b64_tr_b16 v[208:209], v249 offset:4096\n\t" \
  "v_lshlrev_b32_sdwa v150, 1, v150 dst_sel:DWORD dst_unused:UNUSED_PAD src0_sel:DWORD src1_sel:WORD_1\n\t" \
  "v_exp_f16 v224, v225\n\t" \
  "v_smfmac_f32_16x16x64_bf16 v[20:23], v[212:215], v[176:183], v248\n\t" \
  "ds_read_u16_d16_hi v148, v148 offset:0\n\t" \
  "v_lshlrev_b32_sdwa v151, 1, v151 dst_sel:DWORD dst_unused:UNUSED_PAD src0_sel:DWORD src1_sel:WORD_1\n\t" \
  "v_smfmac_f32_16x16x64_bf16 v[44:47], v[212:215], v[184:191], v248\n\t" \
  "ds_read_b64_tr_b16 v[210:211], v249 offset:4352\n\t" \
  "v_exp_f16_sdwa v224, v225 dst_sel:WORD_1 dst_unused:UNUSED_PRESERVE src0_sel:WORD_1\n\t" \
  "v_smfmac_f32_16x16x64_bf16 v[68:71], v[212:215], v[192:199], v248\n\t" \
  "s_waitcnt lgkmcnt(8)\n\t" \
  "ds_read_u16_d16_hi v149, v149 offset:0\n\t" \
  "ds_read_b64_tr_b16 v[212:213], v249 offset:512\n\t" \
  "v_lshlrev_b32_sdwa v158, 1, v158 dst_sel:DWORD dst_unused:UNUSED_PAD src0_sel:DWORD src1_sel:WORD_1\n\t" \
  "v_mfma_f32_16x16x32_bf16 v[176:179], v[216:219], v[96:99], 0\n\t" \
  "ds_read_u16_d16_hi v150, v150 offset:0\n\t" \
  "v_pk_fma_f16 v224, v224, %[hc], %[hc]\n\t" \
  "v_mfma_f32_16x16x32_bf16 v[184:187], v[216:219], v[108:111], 0\n\t" \
  "ds_read_b64_tr_b16 v[214:215], v249 offset:768\n\t" \
  "v_lshlrev_b32_sdwa v159, 1, v159 dst_sel:DWORD dst_unused:UNUSED_PAD src0_sel:DWORD src1_sel:WORD_1\n\t" \
  "v_mfma_f32_16x16x32_bf16 v[192:195], v[216:219], v[120:123], 0\n\t" \
  "ds_read_u16_d16_hi v151, v151 offset:0\n\t" \
  "v_rcp_f16 v225, v224\n\t" \
  "v_mfma_f32_16x16x32_bf16 v[200:203], v[216:219], v[132:135], 0\n\t" \
  "s_waitcnt lgkmcnt(9)\n\t" \
  "ds_read_b64_tr_b16 v[216:217], v249 offset:2560\n\t" \
  "v_lshlrev_b32_sdwa v164, 1, v164 dst_sel:DWORD dst_unused:UNUSED_PAD src0_sel:DWORD src1_sel:WORD_1\n\t" \
  "v_mfma_f32_16x16x32_bf16 v[176:179], v[220:223], v[100:103], v[176:179]\n\t" \
  "ds_read_u16_d16_hi v158, v158 offset:0\n\t" \
  "v_rcp_f16_sdwa v224, v224 dst_sel:DWORD dst_unused:UNUSED_PAD src0_sel:WORD_1\n\t" \
  "v_mfma_f32_16x16x32_bf16 v[184:187], v[220:223], v[112:115], v[184:187]\n\t" \
  "ds_read_b64_tr_b16 v[218:219], v249 offset:2816\n\t" \
  "v_lshlrev_b32_sdwa v165, 1, v165 dst_sel:DWORD dst_unused:UNUSED_PAD src0_sel:DWORD src1_sel:WORD_1\n\t" \
  "v_mfma_f32_16x16x32_bf16 v[192:195], v[220:223], v[124:127], v[192:195]\n\t" \
  "ds_read_u16_d16_hi v159, v159 offset:0\n\t" \
  "v_fma_mix_f32 v156, v156, v225, 0 op_sel_hi:[0,1,0]\n\t" \
  "v_mfma_f32_16x16x32_bf16 v[200:203], v[220:223], v[136:139], v[200:203]\n\t" \
  "s_waitcnt lgkmcnt(9)\n\t" \
  "ds_read_b64_tr_b16 v[220:221], v249 offset:4608\n\t" \
  "v_lshlrev_b32_sdwa v166, 1, v166 dst_sel:DWORD dst_unused:UNUSED_PAD src0_sel:DWORD src1_sel:WORD_1\n\t" \
  "v_mfma_f32_16x16x32_bf16 v[176:179], v[208:211], v[104:107], v[176:179]\n\t" \
  "ds_read_u16_d16_hi v164, v164 offset:0\n\t" \
  "v_fma_mix_f32 v157, v157, v224, 0 op_sel_hi:[0,1,0]\n\t" \
  "v_mfma_f32_16x16x32_bf16 v[184:187], v[208:211], v[116:119], v[184:187]\n\t" \
  "ds_read_b64_tr_b16 v[222:223], v249 offset:4864\n\t" \
  "v_lshlrev_b32_sdwa v167, 1, v167 dst_sel:DWORD dst_unused:UNUSED_PAD src0_sel:DWORD src1_sel:WORD_1\n\t" \
  "v_mfma_f32_16x16x32_bf16 v[192:195], v[208:211], v[128:131], v[192:195]\n\t" \
  "ds_read_u16_d16_hi v165, v165 offset:0\n\t" \
  "s_nop 0\n\t" \
  "v_lshlrev_b32_sdwa v176, 1, v176 dst_sel:DWORD dst_unused:UNUSED_PAD src0_sel:DWORD src1_sel:WORD_1\n\t" \
  "v_mfma_f32_16x16x32_bf16 v[200:203], v[208:211], v[140:143], v[200:203]\n\t" \
  "s_waitcnt lgkmcnt(9)\n\t" \
  "ds_read_b64_tr_b16 v[208:209], v250 offset:0\n\t" \
  "ds_read_u16_d16_hi v166, v166 offset:0\n\t" \
  "v_mfma_f32_16x16x32_bf16 v[180:183], v[212:215], v[96:99], 0\n\t" \
  "ds_read_b64_tr_b16 v[210:211], v250 offset:768\n\t" \
  "v_mul_f32 v184, %[kk], v184\n\t" \
  "v_mfma_f32_16x16x32_bf16 v[188:191], v[212:215], v[108:111], 0\n\t" \
  "ds_read_u16_d16_hi v167, v167 offset:0\n\t" \
  "v_lshlrev_b32_sdwa v177, 1, v177 dst_sel:DWORD dst_unused:UNUSED_PAD src0_sel:DWORD src1_sel:WORD_1\n\t" \
  "v_mfma_f32_16x16x32_bf16 v[196:199], v[212:215], v[120:123], 0\n\t" \
  "ds_read_u16_d16_hi v176, v176 offset:0\n\t" \
  "v_mul_f32 v185, %[kk], v185\n\t" \
  "v_mfma_f32_16x16x32_bf16 v[204:207], v[212:215], v[132:135], 0\n\t" \
  "s_waitcnt lgkmcnt(10)\n\t" \
  "ds_read_b64_tr_b16 v[212:213], v250 offset:8\n\t" \
  "v_cvt_pk_f16_f32 v224, v184, v185\n\t" \
  "v_mfma_f32_16x16x32_bf16 v[180:183], v[216:219], v[100:103], v[180:183]\n\t" \
  "ds_read_u16_d16_hi v177, v177 offset:0\n\t" \
  "v_lshlrev_b32_sdwa v178, 1, v178 dst_sel:DWORD dst_unused:UNUSED_PAD src0_sel:DWORD src1_sel:WORD_1\n\t" \
  "v_mfma_f32_16x16x32_bf16 v[188:191], v[216:219], v[112:115], v[188:191]\n\t" \
  "ds_read_b64_tr_b16 v[214:215], v250 offset:776\n\t" \
  "v_pk_fma_f16 v225, v224, v224, %[h1] neg_lo:[1,0,0] neg_hi:[1,0,0]\n\t" \
  "v_mfma_f32_16x16x32_bf16 v[196:199], v[216:219], v[124:127], v[196:199]\n\t" \
  "ds_read_u16_d16_hi v178, v178 offset:0\n\t" \
  "v_lshlrev_b32_sdwa v179, 1, v179 dst_sel:DWORD dst_unused:UNUSED_PAD src0_sel:DWORD src1_sel:WORD_1\n\t" \
  "v_pk_mul_f16 v225, v224, v225\n\t" \
  "v_mfma_f32_16x16x32_bf16 v[204:207], v[216:219], v[136:139], v[204:207]\n\t" \
  "s_waitcnt lgkmcnt(10)\n\t" \
  "ds_read_b64_tr_b16 v[216:217], v250 offset:64\n\t" \
  "v_lshlrev_b32_sdwa v186, 1, v186 dst_sel:DWORD dst_unused:UNUSED_PAD src0_sel:DWORD src1_sel:WORD_1\n\t" \
  "v_mfma_f32_16x16x32_bf16 v[180:183], v[220:223], v[104:107], v[180:183]\n\t" \
  "ds_read_u16_d16_hi v179, v179 offset:0\n\t" \
  "v_exp_f16 v224, v225\n\t" \
  "v_mfma_f32_16x16x32_bf16 v[188:191], v[220:223], v[116:119], v[188:191]\n\t" \
  "ds_read_b64_tr_b16 v[218:219], v250 offset:832\n\t" \
  "v_lshlrev_b32_sdwa v187, 1, v187 dst_sel:DWORD dst_unused:UNUSED_PAD src0_sel:DWORD src1_sel:WORD_1\n\t" \
  "v_mfma_f32_16x16x32_bf16 v[196:199], v[220:223], v[128:131], v[196:199]\n\t" \
  "ds_read_u16_d16_hi v186, v186 offset:0\n\t" \
  "v_lshlrev_b32_sdwa v192, 1, v192 dst_sel:DWORD dst_unused:UNUSED_PAD src0_sel:DWORD src1_sel:WORD_1\n\t" \
  "v_mfma_f32_16x16x32_bf16 v[204:207], v[220:223], v[140:143], v[204:207]\n\t" \
  "s_waitcnt lgkmcnt(10)\n\t" \
  "ds_read_b64_tr_b16 v[220:221], v250 offset:72\n\t" \
  "v_exp_f16_sdwa v224, v225 dst_sel:WORD_1 dst_unused:UNUSED_PRESERVE src0_sel:WORD_1\n\t" \
  "v_smfmac_f32_16x16x64_bf16 v[0:3], v[208:211], v[144:151], v248\n\t" \
  "ds_read_u16_d16_hi v187, v187 offset:0\n\t" \
  "v_lshlrev_b32_sdwa v193, 1, v193 dst_sel:DWORD dst_unused:UNUSED_PAD src0_sel:DWORD src1_sel:WORD_1\n\t" \
  "v_smfmac_f32_16x16x64_bf16 v[24:27], v[208:211], v[152:159], v248\n\t" \
  "s_waitcnt lgkmcnt(11)\n\t" \
  "ds_read_b64_tr_b16 v[222:223], v250 offset:840\n\t" \
  "v_pk_fma_f16 v224, v224, %[hc], %[hc]\n\t" \
  "v_smfmac_f32_16x16x64_bf16 v[48:51], v[208:211], v[160:167], v248\n\t" \
  "s_waitcnt lgkmcnt(8)\n\t" \
  "ds_read_b64 v[230:231], v252 offset:0\n\t" \
  "ds_read_b64_tr_b16 v[208:209], v250 offset:128\n\t" \
  "v_lshlrev_b32_sdwa v194, 1, v194 dst_sel:DWORD dst_unused:UNUSED_PAD src0_sel:DWORD src1_sel:WORD_1\n\t" \
  "v_rcp_f16 v225, v224\n\t" \
  "v_smfmac_f32_16x16x64_bf16 v[4:7], v[212:215], v[144:151], v248\n\t" \
  "ds_read_b64 v[232:233], v252 offset:8\n\t" \
  "ds_read_b64_tr_b16 v[210:211], v250 offset:896\n\t" \
  "v_lshlrev_b32_sdwa v195, 1, v195 dst_sel:DWORD dst_unused:UNUSED_PAD src0_sel:DWORD src1_sel:WORD_1\n\t" \
  "v_smfmac_f32_16x16x64_bf16 v[28:31], v[212:215], v[152:159], v248\n\t" \
  "ds_read_b64 v[234:235], v252 offset:64\n\t" \
  "ds_read_b64 v[236:237], v252 offset:72\n\t" \
  "v_smfmac_f32_16x16x64_bf16 v[52:55], v[212:215], v[160:167], v248\n\t" \
  "s_waitcnt lgkmcnt(10)\n\t" \
  "ds_read_b64_tr_b16 v[212:213], v250 offset:136\n\t" \
  "ds_read_b64 v[242:243], v252 offset:128\n\t" \
  "v_lshlrev_b32_sdwa v200, 1, v200 dst_sel:DWORD dst_unused:UNUSED_PAD src0_sel:DWORD src1_sel:WORD_1\n\t" \
  "v_rcp_f16_sdwa v224, v224 dst_sel:DWORD dst_unused:UNUSED_PAD src0_sel:WORD_1\n\t" \
  "v_smfmac_f32_16x16x64_bf16 v[8:11], v[216:219], v[144:151], v248\n\t" \
  "ds_read_b64_tr_b16 v[214:215], v250 offset:904\n\t" \
  "ds_read_b64 v[244:245], v252 offset:136\n\t" \
  "v_lshlrev_b32_sdwa v201, 1, v201 dst_sel:DWORD dst_unused:UNUSED_PAD src0_sel:DWORD src1_sel:WORD_1\n\t" \
  "v_smfmac_f32_16x16x64_bf16 v[32:35], v[216:219], v[152:159], v248\n\t" \
  "ds_read_u16_d16_hi v192, v192 offset:0\n\t" \
  "ds_read_u16_d16_hi v193, v193 offset:0\n\t" \
  "v_fma_mix_f32 v184, v184, v225, 0 op_sel_hi:[0,1,0]\n\t" \
  "v_smfmac_f32_16x16x64_bf16 v[56:59], v[216:219], v[160:167], v248\n\t" \
  "s_waitcnt lgkmcnt(12)\n\t" \
  "s_barrier\n\t" \
  "ds_read_b64_tr_b16 v[216:217], v249 offset:6144\n\t" \
  "ds_read_u16_d16_hi v194, v194 offset:0\n\t" \
  "v_lshlrev_b32_sdwa v202, 1, v202 dst_sel:DWORD dst_unused:UNUSED_PAD src0_sel:DWORD src1_sel:WORD_1\n\t" \
  "v_fma_mix_f32 v185, v185, v224, 0 op_sel_hi:[0,1,0]\n\t" \
  "v_lshlrev_b32_sdwa v203, 1, v203 dst_sel:DWORD dst_unused:UNUSED_PAD src0_sel:DWORD src1_sel:WORD_1\n\t" \
  "v_smfmac_f32_16x16x64_bf16 v[12:15], v[220:223], v[144:151], v248\n\t" \
  "ds_read_b64_tr_b16 v[218:219], v249 offset:6400\n\t" \
  "ds_read_u16_d16_hi v195, v195 offset:0\n\t" \
  "v_mul_f32 v188, %[kk], v188\n\t" \
  "v_smfmac_f32_16x16x64_bf16 v[36:39], v[220:223], v[152:159], v248\n\t" \
  "ds_read_u16_d16_hi v200, v200 offset:0\n\t" \
  "v_lshlrev_b32_sdwa v180, 1, v180 dst_sel:DWORD dst_unused:UNUSED_PAD src0_sel:DWORD src1_sel:WORD_1\n\t" \
  "v_mul_f32 v189, %[kk], v189\n\t" \
  "v_smfmac_f32_16x16x64_bf16 v[60:63], v[220:223], v[160:167], v248\n\t" \
  "s_waitcnt lgkmcnt(13)\n\t" \
  "ds_read_b64_tr_b16 v[220:221], v249 offset:8192\n\t" \
  "ds_read_u16_d16_hi v201, v201 offset:0\n\t" \
  "v_cvt_pk_f16_f32 v224, v188, v189\n\t" \
  "v_lshlrev_b32_sdwa v181, 1, v181 dst_sel:DWORD dst_unused:UNUSED_PAD src0_sel:DWORD src1_sel:WORD_1\n\t" \
  "v_smfmac_f32_16x16x64_bf16 v[16:19], v[208:211], v[144:151], v248\n\t" \
  "ds_read_b64_tr_b16 v[222:223], v249 offset:8448\n\t" \
  "ds_read_u16_d16_hi v202, v202 offset:0\n\t" \
  "v_pk_fma_f16 v225, v224, v224, %[h1] neg_lo:[1,0,0] neg_hi:[1,0,0]\n\t" \
  "v_smfmac_f32_16x16x64_bf16 v[40:43], v[208:211], v[152:159], v248\n\t" \
  "ds_read_u16_d16_hi v203, v203 offset:0\n\t" \
  "v_lshlrev_b32_sdwa v182, 1, v182 dst_sel:DWORD dst_unused:UNUSED_PAD src0_sel:DWORD src1_sel:WORD_1\n\t" \
  "v_pk_mul_f16 v225, v224, v225\n\t" \
  "v_smfmac_f32_16x16x64_bf16 v[64:67], v[208:211], v[160:167], v248\n\t" \
  "s_waitcnt lgkmcnt(13)\n\t" \
  "ds_read_b64_tr_b16 v[208:209], v249 offset:10240\n\t" \
  "ds_read_u16_d16_hi v180, v180 offset:0\n\t" \
  "v_lshlrev_b32_sdwa v183, 1, v183 dst_sel:DWORD dst_unused:UNUSED_PAD src0_sel:DWORD src1_sel:WORD_1\n\t" \
  "v_exp_f16 v224, v225\n\t" \
  "v_smfmac_f32_16x16x64_bf16 v[20:23], v[212:215], v[144:151], v248\n\t" \
  "ds_read_b64_tr_b16 v[210:211], v249 offset:10496\n\t" \
  "v_lshlrev_b32_sdwa v190, 1, v190 dst_sel:DWORD dst_unused:UNUSED_PAD src0_sel:DWORD src1_sel:WORD_1\n\t" \
  "v_smfmac_f32_16x16x64_bf16 v[44:47], v[212:215], v[152:159], v248\n\t" \
  "ds_read_u16_d16_hi v181, v181 offset:0\n\t" \
  "v_lshlrev_b32_sdwa v191, 1, v191 dst_sel:DWORD dst_unused:UNUSED_PAD src0_sel:DWORD src1_sel:WORD_1\n\t" \
  "v_smfmac_f32_16x16x64_bf16 v[68:71], v[212:215], v[160:167], v248\n\t" \
  "ds_read_b64_tr_b16 v[212:213], v249 offset:6656\n\t" \
  "ds_read_u16_d16_hi v182, v182 offset:0\n\t" \
  "v_exp_f16_sdwa v224, v225 dst_sel:WORD_1 dst_unused:UNUSED_PRESERVE src0_sel:WORD_1\n\t" \
  "v_mfma_f32_16x16x16_bf16 v[72:75], v[246:247], v[230:231], 0\n\t" \
  "s_waitcnt lgkmcnt(13)\n\t" \
  "ds_read_b64_tr_b16 v[214:215], v249 offset:6912\n\t" \
  "v_lshlrev_b32_sdwa v196, 1, v196 dst_sel:DWORD dst_unused:UNUSED_PAD src0_sel:DWORD src1_sel:WORD_1\n\t" \
  "v_mfma_f32_16x16x32_bf16 v[144:147], v[216:219], v[96:99], 0\n\t" \
  "ds_read_u16_d16_hi v183, v183 offset:0\n\t" \
  "v_pk_fma_f16 v224, v224, %[hc], %[hc]\n\t" \
  "v_mfma_f32_16x16x32_bf16 v[152:155], v[216:219], v[108:111], 0\n\t" \
  "ds_read_u16_d16_hi v190, v190 offset:0\n\t" \
  "v_lshlrev_b32_sdwa v197, 1, v197 dst_sel:DWORD dst_unused:UNUSED_PAD src0_sel:DWORD src1_sel:WORD_1\n\t" \
  "v_mfma_f32_16x16x32_bf16 v[160:163], v[216:219], v[120:123], 0\n\t" \
  "ds_read_u16_d16_hi v191, v191 offset:0\n\t" \
  "v_rcp_f16 v225, v224\n\t" \
  "v_mfma_f32_16x16x16_bf16 v[76:79], v[246:247], v[232:233], 0\n\t" \
  "ds_read_u16_d16_hi v196, v196 offset:0\n\t" \
  "v_lshlrev_b32_sdwa v198, 1, v198 dst_sel:DWORD dst_unused:UNUSED_PAD src0_sel:DWORD src1_sel:WORD_1\n\t" \
  "v_mfma_f32_16x16x32_bf16 v[168:171], v[216:219], v[132:135], 0\n\t" \
  "s_waitcnt lgkmcnt(13)\n\t" \
  "ds_read_b64_tr_b16 v[216:217], v249 offset:8704\n\t" \
  "v_lshlrev_b32_sdwa v199, 1, v199 dst_sel:DWORD dst_unused:UNUSED_PAD src0_sel:DWORD src1_sel:WORD_1\n\t" \
  "v_mfma_f32_16x16x32_bf16 v[144:147], v[220:223], v[100:103], v[144:147]\n\t" \
  "ds_read_u16_d16_hi v197, v197 offset:0\n\t" \
  "v_rcp_f16_sdwa v224, v224 dst_sel:DWORD dst_unused:UNUSED_PAD src0_sel:WORD_1\n\t" \
  "v_mfma_f32_16x16x32_bf16 v[152:155], v[220:223], v[112:115], v[152:155]\n\t" \
  "ds_read_b64_tr_b16 v[218:219], v249 offset:8960\n\t" \
  "v_lshlrev_b32_sdwa v204, 1, v204 dst_sel:DWORD dst_unused:UNUSED_PAD src0_sel:DWORD src1_sel:WORD_1\n\t" \
  "v_mfma_f32_16x16x32_bf16 v[160:163], v[220:223], v[124:127], v[160:163]\n\t" \
  "ds_read_u16_d16_hi v198, v198 offset:0\n\t" \
  "v_lshlrev_b32_sdwa v205, 1, v205 dst_sel:DWORD dst_unused:UNUSED_PAD src0_sel:DWORD src1_sel:WORD_1\n\t" \
  "v_mfma_f32_16x16x32_bf16 v[168:171], v[220:223], v[136:139], v[168:171]\n\t" \
  "ds_read_b64_tr_b16 v[220:221], v249 offset:10752\n\t" \
  "ds_read_u16_d16_hi v199, v199 offset:0\n\t" \
  "v_mfma_f32_16x16x16_bf16 v[80:83], v[246:247], v[234:235], 0\n\t" \
  "s_waitcnt lgkmcnt(14)\n\t" \
  "ds_read_b64_tr_b16 v[222:223], v249 offset:11008\n\t" \
  "v_fma_mix_f32 v188, v188, v225, 0 op_sel_hi:[0,1,0]\n\t" \
  "v_mfma_f32_16x16x32_bf16 v[144:147], v[208:211], v[104:107], v[144:147]\n\t" \
  "ds_read_u16_d16_hi v204, v204 offset:0\n\t" \
  "v_fma_mix_f32 v189, v189, v224, 0 op_sel_hi:[0,1,0]\n\t" \
  "v_mfma_f32_16x16x32_bf16 v[152:155], v[208:211], v[116:119], v[152:155]\n\t" \
  "ds_read_u16_d16_hi v205, v205 offset:0\n\t" \
  "v_lshlrev_b32_sdwa v206, 1, v206 dst_sel:DWORD dst_unused:UNUSED_PAD src0_sel:DWORD src1_sel:WORD_1\n\t" \
  "v_mfma_f32_16x16x32_bf16 v[160:163], v[208:211], v[128:131], v[160:163]\n\t" \
  "ds_read_u16_d16_hi v206, v206 offset:0\n\t" \
  "v_lshlrev_b32_sdwa v207, 1, v207 dst_sel:DWORD dst_unused:UNUSED_PAD src0_sel:DWORD src1_sel:WORD_1\n\t" \
  "v_mfma_f32_16x16x16_bf16 v[84:87], v[246:247], v[236:237], 0\n\t" \
  "ds_read_u16_d16_hi v207, v207 offset:0\n\t" \
  "v_lshlrev_b32_sdwa v144, 1, v144 dst_sel:DWORD dst_unused:UNUSED_PAD src0_sel:DWORD src1_sel:WORD_1\n\t" \
  "v_mfma_f32_16x16x32_bf16 v[168:171], v[208:211], v[140:143], v[168:171]\n\t" \
  "s_waitcnt lgkmcnt(14)\n\t" \
  "ds_read_b64_tr_b16 v[208:209], v250 offset:6160\n\t" \
  "v_lshlrev_b32_sdwa v145, 1, v145 dst_sel:DWORD dst_unused:UNUSED_PAD src0_sel:DWORD src1_sel:WORD_1\n\t" \
  "v_mfma_f32_16x16x32_bf16 v[148:151], v[212:215], v[96:99], 0\n\t" \
  "ds_read_u16_d16_hi v144, v144 offset:0\n\t" \
  "v_mul_f32 v152, %[kk], v152\n\t" \
  "v_mfma_f32_16x16x32_bf16 v[156:159], v[212:215], v[108:111], 0\n\t" \
  "ds_read_b64_tr_b16 v[210:211], v250 offset:6928\n\t" \
  "v_mul_f32 v153, %[kk], v153\n\t" \
  "v_mfma_f32_16x16x32_bf16 v[164:167], v[212:215], v[120:123], 0\n\t" \
  "ds_read_u16_d16_hi v145, v145 offset:0\n\t" \
  "v_cvt_pk_f16_f32 v224, v152, v153\n\t" \
  "v_mfma_f32_16x16x32_bf16 v[172:175], v[212:215], v[132:135], 0\n\t" \
  "ds_read_b64_tr_b16 v[212:213], v250 offset:6168\n\t" \
  "v_lshlrev_b32_sdwa v146, 1, v146 dst_sel:DWORD dst_unused:UNUSED_PAD src0_sel:DWORD src1_sel:WORD_1\n\t" \
  "v_mfma_f32_16x16x16_bf16 v[88:91], v[246:247], v[242:243], 0\n\t" \
  "s_waitcnt lgkmcnt(13)\n\t" \
  "ds_read_u16_d16_hi v146, v146 offset:0\n\t" \
  "v_pk_fma_f16 v225, v224, v224, %[h1] neg_lo:[1,0,0] neg_hi:[1,0,0]\n\t" \
  "v_mfma_f32_16x16x32_bf16 v[148:151], v[216:219], v[100:103], v[148:151]\n\t" \
  "ds_read_b64_tr_b16 v[214:215], v250 offset:6936\n\t" \
  "v_lshlrev_b32_sdwa v147, 1, v147 dst_sel:DWORD dst_unused:UNUSED_PAD src0_sel:DWORD src1_sel:WORD_1\n\t" \
  "v_mfma_f32_16x16x32_bf16 v[156:159], v[216:219], v[112:115], v[156:159]\n\t" \
  "ds_read_u16_d16_hi v147, v147 offset:0\n\t" \
  "v_pk_mul_f16 v225, v224, v225\n\t" \
  "v_mfma_f32_16x16x32_bf16 v[164:167], v[216:219], v[124:127], v[164:167]\n\t" \
  "v_lshlrev_b32_sdwa v154, 1, v154 dst_sel:DWORD dst_unused:UNUSED_PAD src0_sel:DWORD src1_sel:WORD_1\n\t" \
  "v_exp_f16 v224, v225\n\t" \
  "v_mfma_f32_16x16x16_bf16 v[92:95], v[246:247], v[244:245], 0\n\t" \
  "ds_read_u16_d16_hi v154, v154 offset:0\n\t" \
  "v_lshlrev_b32_sdwa v155, 1, v155 dst_sel:DWORD dst_unused:UNUSED_PAD src0_sel:DWORD src1_sel:WORD_1\n\t" \
  "v_mfma_f32_16x16x32_bf16 v[172:175], v[216:219], v[136:139], v[172:175]\n\t" \
  "s_waitcnt lgkmcnt(13)\n\t" \
  "ds_read_b64_tr_b16 v[216:217], v250 offset:6224\n\t" \
  "v_lshlrev_b32_sdwa v160, 1, v160 dst_sel:DWORD dst_unused:UNUSED_PAD src0_sel:DWORD src1_sel:WORD_1\n\t" \
  "v_mfma_f32_16x16x32_bf16 v[148:151], v[220:223], v[104:107], v[148:151]\n\t" \
  "ds_read_u16_d16_hi v155, v155 offset:0\n\t" \
  "v_exp_f16_sdwa v224, v225 dst_sel:WORD_1 dst_unused:UNUSED_PRESERVE src0_sel:WORD_1\n\t" \
  "v_mfma_f32_16x16x32_bf16 v[156:159], v[220:223], v[116:119], v[156:159]\n\t" \
  "ds_read_b64_tr_b16 v[218:219], v250 offset:6992\n\t" \
  "v_lshlrev_b32_sdwa v161, 1, v161 dst_sel:DWORD dst_unused:UNUSED_PAD src0_sel:DWORD src1_sel:WORD_1\n\t" \
  "v_mfma_f32_16x16x32_bf16 v[164:167], v[220:223], v[128:131], v[164:167]\n\t" \
  "ds_read_u16_d16_hi v160, v160 offset:0\n\t" \
  "v_pk_fma_f16 v224, v224, %[hc], %[hc]\n\t" \
  "v_mfma_f32_16x16x32_bf16 v[172:175], v[220:223], v[140:143], v[172:175]\n\t" \
  "s_waitcnt lgkmcnt(10)\n\t" \
  "ds_read_b64_tr_b16 v[220:221], v250 offset:6232\n\t" \
  "v_lshlrev_b32_sdwa v162, 1, v162 dst_sel:DWORD dst_unused:UNUSED_PAD src0_sel:DWORD src1_sel:WORD_1\n\t" \
  "v_smfmac_f32_16x16x64_bf16 v[0:3], v[208:211], v[176:183], v248\n\t" \
  "ds_read_u16_d16_hi v161, v161 offset:0\n\t" \
  "v_rcp_f16 v225, v224\n\t" \
  "v_smfmac_f32_16x16x64_bf16 v[24:27], v[208:211], v[184:191], v248\n\t" \
  "ds_read_b64_tr_b16 v[222:223], v250 offset:7000\n\t" \
  "v_lshlrev_b32_sdwa v163, 1, v163 dst_sel:DWORD dst_unused:UNUSED_PAD src0_sel:DWORD src1_sel:WORD_1\n\t" \
  "v_smfmac_f32_16x16x64_bf16 v[48:51], v[208:211], v[192:199], v248\n\t" \
  "ds_read_u16_d16_hi v162, v162 offset:0\n\t" \
  "v_lshlrev_b32_sdwa v168, 1, v168 dst_sel:DWORD dst_unused:UNUSED_PAD src0_sel:DWORD src1_sel:WORD_1\n\t" \
  "v_smfmac_f32_16x16x64_bf16 v[72:75], v[208:211], v[200:207], v248\n\t" \
  "s_waitcnt lgkmcnt(10)\n\t" \
  "ds_read_b64_tr_b16 v[208:209], v250 offset:6288\n\t" \
  "v_rcp_f16_sdwa v224, v224 dst_sel:DWORD dst_unused:UNUSED_PAD src0_sel:WORD_1\n\t" \
  "v_smfmac_f32_16x16x64_bf16 v[4:7], v[212:215], v[176:183], v248\n\t" \
  "ds_read_u16_d16_hi v163, v163 offset:0\n\t" \
  "v_lshlrev_b32_sdwa v169, 1, v169 dst_sel:DWORD dst_unused:UNUSED_PAD src0_sel:DWORD src1_sel:WORD_1\n\t" \
  "v_smfmac_f32_16x16x64_bf16 v[28:31], v[212:215], v[184:191], v248\n\t" \
  "ds_read_b64_tr_b16 v[210:211], v250 offset:7056\n\t" \
  "v_fma_mix_f32 v152, v152, v225, 0 op_sel_hi:[0,1,0]\n\t" \
  "v_smfmac_f32_16x16x64_bf16 v[52:55], v[212:215], v[192:199], v248\n\t" \
  "ds_read_u16_d16_hi v168, v168 offset:0\n\t" \
  "v_lshlrev_b32_sdwa v170, 1, v170 dst_sel:DWORD dst_unused:UNUSED_PAD src0_sel:DWORD src1_sel:WORD_1\n\t" \
  "v_smfmac_f32_16x16x64_bf16 v[76:79], v[212:215], v[200:207], v248\n\t" \
  "s_waitcnt lgkmcnt(9)\n\t" \
  "ds_read_b64_tr_b16 v[212:213], v250 offset:6296\n\t" \
  "v_fma_mix_f32 v153, v153, v224, 0 op_sel_hi:[0,1,0]\n\t" \
  "v_smfmac_f32_16x16x64_bf16 v[8:11], v[216:219], v[176:183], v248\n\t" \
  "ds_read_u16_d16_hi v169, v169 offset:0\n\t" \
  "v_lshlrev_b32_sdwa v171, 1, v171 dst_sel:DWORD dst_unused:UNUSED_PAD src0_sel:DWORD src1_sel:WORD_1\n\t" \
  "v_smfmac_f32_16x16x64_bf16 v[32:35], v[216:219], v[184:191], v248\n\t" \
  "ds_read_b64_tr_b16 v[214:215], v250 offset:7064\n\t" \
  "v_mul_f32 v156, %[kk], v156\n\t" \
  "v_smfmac_f32_16x16x64_bf16 v[56:59], v[216:219], v[192:199], v248\n\t" \
  "ds_read_u16_d16_hi v170, v170 offset:0\n\t" \
  "v_lshlrev_b32_sdwa v148, 1, v148 dst_sel:DWORD dst_unused:UNUSED_PAD src0_sel:DWORD src1_sel:WORD_1\n\t" \
  "v_smfmac_f32_16x16x64_bf16 v[80:83], v[216:219], v[200:207], v248\n\t" \
  "s_waitcnt lgkmcnt(9)\n\t" \
  "s_barrier\n\t" \
  "ds_read_b64_tr_b16 v[216:217], v249 offset:12288\n\t" \
  "v_mul_f32 v157, %[kk], v157\n\t" \
  "v_smfmac_f32_16x16x64_bf16 v[12:15], v[220:223], v[176:183], v248\n\t" \
  "ds_read_u16_d16_hi v171, v171 offset:0\n\t" \
  "v_cvt_pk_f16_f32 v224, v156, v157\n\t" \
  "v_smfmac_f32_16x16x64_bf16 v[36:39], v[220:223], v[184:191], v248\n\t" \
  "ds_read_b64_tr_b16 v[218:219], v249 offset:12544\n\t" \
  "v_lshlrev_b32_sdwa v149, 1, v149 dst_sel:DWORD dst_unused:UNUSED_PAD src0_sel:DWORD src1_sel:WORD_1\n\t" \
  "v_smfmac_f32_16x16x64_bf16 v[60:63], v[220:223], v[192:199], v248\n\t" \
  "ds_read_u16_d16_hi v148, v148 offset:0\n\t" \
  "v_pk_fma_f16 v225, v224, v224, %[h1] neg_lo:[1,0,0] neg_hi:[1,0,0]\n\t" \
  "v_smfmac_f32_16x16x64_bf16 v[84:87], v[220:223], v[200:207], v248\n\t" \
  "s_waitcnt lgkmcnt(9)\n\t" \
  "ds_read_b64_tr_b16 v[220:221], v249 offset:14336\n\t" \
  "v_lshlrev_b32_sdwa v150, 1, v150 dst_sel:DWORD dst_unused:UNUSED_PAD src0_sel:DWORD src1_sel:WORD_1\n\t" \
  "v_smfmac_f32_16x16x64_bf16 v[16:19], v[208:211], v[176:183], v248\n\t" \
  "s_add_u32 %[rs], %[rs], 1\n\t" \
  "ds_read_u16_d16_hi v149, v149 offset:0\n\t" \
  "v_pk_mul_f16 v225, v224, v225\n\t" \
  "v_smfmac_f32_16x16x64_bf16 v[40:43], v[208:211], v[184:191], v248\n\t" \
  "s_cmp_eq_u32 %[rs], 6\n\t" \
  "ds_read_b64_tr_b16 v[222:223], v249 offset:14592\n\t" \
  "v_lshlrev_b32_sdwa v151, 1, v151 dst_sel:DWORD dst_unused:UNUSED_PAD src0_sel:DWORD src1_sel:WORD_1\n\t" \
  "v_smfmac_f32_16x16x64_bf16 v[64:67], v[208:211], v[192:199], v248\n\t" \
  "ds_read_u16_d16_hi v150, v150 offset:0\n\t" \
  "v_exp_f16 v224, v225\n\t" \
  "v_smfmac_f32_16x16x64_bf16 v[88:91], v[208:211], v[200:207], v248\n\t" \
  "s_waitcnt lgkmcnt(9)\n\t" \
  "ds_read_b64_tr_b16 v[208:209], v249 offset:16384\n\t" \
  "v_lshlrev_b32_sdwa v158, 1, v158 dst_sel:DWORD dst_unused:UNUSED_PAD src0_sel:DWORD src1_sel:WORD_1\n\t" \
  "v_smfmac_f32_16x16x64_bf16 v[20:23], v[212:215], v[176:183], v248\n\t" \
  "s_cselect_b32 %[rs], 0, %[rs]\n\t" \
  "ds_read_u16_d16_hi v151, v151 offset:0\n\t" \
  "v_lshlrev_b32_sdwa v159, 1, v159 dst_sel:DWORD dst_unused:UNUSED_PAD src0_sel:DWORD src1_sel:WORD_1\n\t" \
  "v_smfmac_f32_16x16x64_bf16 v[44:47], v[212:215], v[184:191], v248\n\t" \
  "s_cselect_b32 %[t0], -75920, %[ps]\n\t" \
  "ds_read_b64_tr_b16 v[210:211], v249 offset:16640\n\t" \
  "v_exp_f16_sdwa v224, v225 dst_sel:WORD_1 dst_unused:UNUSED_PRESERVE src0_sel:WORD_1\n\t" \
  "v_smfmac_f32_16x16x64_bf16 v[68:71], v[212:215], v[192:199], v248\n\t" \
  "ds_read_u16_d16_hi v158, v158 offset:0\n\t" \
  "v_add_u32 v252, %[t0], v252\n\t" \
  "v_smfmac_f32_16x16x64_bf16 v[92:95], v[212:215], v[200:207], v248\n\t" \
  "s_waitcnt lgkmcnt(9)\n\t" \
  "ds_read_b64_tr_b16 v[212:213], v249 offset:12800\n\t" \
  "v_lshlrev_b32_sdwa v164, 1, v164 dst_sel:DWORD dst_unused:UNUSED_PAD src0_sel:DWORD src1_sel:WORD_1\n\t" \
  "v_mfma_f32_16x16x32_bf16 v[176:179], v[216:219], v[96:99], 0\n\t" \
  "ds_read_u16_d16_hi v159, v159 offset:0\n\t" \
  "v_pk_fma_f16 v224, v224, %[hc], %[hc]\n\t" \
  "v_mfma_f32_16x16x32_bf16 v[184:187], v[216:219], v[108:111], 0\n\t" \
  "ds_read_b64_tr_b16 v[214:215], v249 offset:13056\n\t" \
  "v_lshlrev_b32_sdwa v165, 1, v165 dst_sel:DWORD dst_unused:UNUSED_PAD src0_sel:DWORD src1_sel:WORD_1\n\t" \
  "v_mfma_f32_16x16x32_bf16 v[192:195], v[216:219], v[120:123], 0\n\t" \
  "ds_read_u16_d16_hi v164, v164 offset:0\n\t" \
  "v_rcp_f16 v225, v224\n\t" \
  "v_mfma_f32_16x16x32_bf16 v[200:203], v[216:219], v[132:135], 0\n\t" \
  "s_waitcnt lgkmcnt(9)\n\t" \
  "ds_read_b64_tr_b16 v[216:217], v249 offset:14848\n\t" \
  "v_lshlrev_b32_sdwa v166, 1, v166 dst_sel:DWORD dst_unused:UNUSED_PAD src0_sel:DWORD src1_sel:WORD_1\n\t" \
  "v_mfma_f32_16x16x32_bf16 v[176:179], v[220:223], v[100:103], v[176:179]\n\t" \
  "ds_read_u16_d16_hi v165, v165 offset:0\n\t" \
  "v_lshlrev_b32_sdwa v167, 1, v167 dst_sel:DWORD dst_unused:UNUSED_PAD src0_sel:DWORD src1_sel:WORD_1\n\t" \
  "v_mfma_f32_16x16x32_bf16 v[184:187], v[220:223], v[112:115], v[184:187]\n\t" \
  "ds_read_b64_tr_b16 v[218:219], v249 offset:15104\n\t" \
  "v_rcp_f16_sdwa v224, v224 dst_sel:DWORD dst_unused:UNUSED_PAD src0_sel:WORD_1\n\t" \
  "v_mfma_f32_16x16x32_bf16 v[192:195], v[220:223], v[124:127], v[192:195]\n\t" \
  "ds_read_u16_d16_hi v166, v166 offset:0\n\t" \
  "v_lshlrev_b32_sdwa v172, 1, v172 dst_sel:DWORD dst_unused:UNUSED_PAD src0_sel:DWORD src1_sel:WORD_1\n\t" \
  "v_mfma_f32_16x16x32_bf16 v[200:203], v[220:223], v[136:139], v[200:203]\n\t" \
  "s_waitcnt lgkmcnt(9)\n\t" \
  "ds_read_b64_tr_b16 v[220:221], v249 offset:16896\n\t" \
  "v_lshlrev_b32_sdwa v173, 1, v173 dst_sel:DWORD dst_unused:UNUSED_PAD src0_sel:DWORD src1_sel:WORD_1\n\t" \
  "v_mfma_f32_16x16x32_bf16 v[176:179], v[208:211], v[104:107], v[176:179]\n\t" \
  "ds_read_u16_d16_hi v167, v167 offset:0\n\t" \
  "v_fma_mix_f32 v156, v156, v225, 0 op_sel_hi:[0,1,0]\n\t" \
  "v_mfma_f32_16x16x32_bf16 v[184:187], v[208:211], v[116:119], v[184:187]\n\t" \
  "ds_read_b64_tr_b16 v[222:223], v249 offset:17152\n\t" \
  "v_fma_mix_f32 v157, v157, v224, 0 op_sel_hi:[0,1,0]\n\t" \
  "v_mfma_f32_16x16x32_bf16 v[192:195], v[208:211], v[128:131], v[192:195]\n\t" \
  "ds_read_u16_d16_hi v172, v172 offset:0\n\t" \
  "v_lshlrev_b32_sdwa v174, 1, v174 dst_sel:DWORD dst_unused:UNUSED_PAD src0_sel:DWORD src1_sel:WORD_1\n\t" \
  "v_mfma_f32_16x16x32_bf16 v[200:203], v[208:211], v[140:143], v[200:203]\n\t" \
  "s_waitcnt lgkmcnt(9)\n\t" \
  "ds_read_b64_tr_b16 v[208:209], v250 offset:12320\n\t" \
  "v_lshlrev_b32_sdwa v175, 1, v175 dst_sel:DWORD dst_unused:UNUSED_PAD src0_sel:DWORD src1_sel:WORD_1\n\t" \
  "v_mfma_f32_16x16x32_bf16 v[180:183], v[212:215], v[96:99], 0\n\t" \
  "ds_read_u16_d16_hi v173, v173 offset:0\n\t" \
  "v_lshlrev_b32_sdwa v176, 1, v176 dst_sel:DWORD dst_unused:UNUSED_PAD src0_sel:DWORD src1_sel:WORD_1\n\t" \
  "v_mfma_f32_16x16x32_bf16 v[188:191], v[212:215], v[108:111], 0\n\t" \
  "ds_read_b64_tr_b16 v[210:211], v250 offset:13088\n\t" \
  "v_mul_f32 v184, %[kk], v184\n\t" \
  "v_mfma_f32_16x16x32_bf16 v[196:199], v[212:215], v[120:123], 0\n\t" \
  "ds_read_u16_d16_hi v174, v174 offset:0\n\t" \
  "v_lshlrev_b32_sdwa v177, 1, v177 dst_sel:DWORD dst_unused:UNUSED_PAD src0_sel:DWORD src1_sel:WORD_1\n\t" \
  "v_mul_f32 v185, %[kk], v185\n\t" \
  "v_mfma_f32_16x16x32_bf16 v[204:207], v[212:215], v[132:135], 0\n\t" \
  "s_waitcnt lgkmcnt(9)\n\t" \
  "ds_read_b64_tr_b16 v[212:213], v250 offset:12328\n\t" \
  "ds_read_u16_d16_hi v175, v175 offset:0\n\t" \
  "v_cvt_pk_f16_f32 v224, v184, v185\n\t" \
  "v_mfma_f32_16x16x32_bf16 v[180:183], v[216:219], v[100:103], v[180:183]\n\t" \
  "ds_read_b64_tr_b16 v[214:215], v250 offset:13096\n\t" \
  "v_lshlrev_b32_sdwa v178, 1, v178 dst_sel:DWORD dst_unused:UNUSED_PAD src0_sel:DWORD src1_sel:WORD_1\n\t" \
  "v_mfma_f32_16x16x32_bf16 v[188:191], v[216:219], v[112:115], v[188:191]\n\t" \
  "ds_read_u16_d16_hi v176, v176 offset:0\n\t" \
  "v_pk_fma_f16 v225, v224, v224, %[h1] neg_lo:[1,0,0] neg_hi:[1,0,0]\n\t" \
  "v_mfma_f32_16x16x32_bf16 v[196:199], v[216:219], v[124:127], v[196:199]\n\t" \
  "ds_read_u16_d16_hi v177, v177 offset:0\n\t" \
  "v_lshlrev_b32_sdwa v179, 1, v179 dst_sel:DWORD dst_unused:UNUSED_PAD src0_sel:DWORD src1_sel:WORD_1\n\t" \
  "v_pk_mul_f16 v225, v224, v225\n\t" \
  "v_mfma_f32_16x16x32_bf16 v[204:207], v[216:219], v[136:139], v[204:207]\n\t" \
  "s_waitcnt lgkmcnt(10)\n\t" \
  "ds_read_b64_tr_b16 v[216:217], v250 offset:12384\n\t" \
  "v_lshlrev_b32_sdwa v186, 1, v186 dst_sel:DWORD dst_unused:UNUSED_PAD src0_sel:DWORD src1_sel:WORD_1\n\t" \
  "v_mfma_f32_16x16x32_bf16 v[180:183], v[220:223], v[104:107], v[180:183]\n\t" \
  "ds_read_u16_d16_hi v178, v178 offset:0\n\t" \
  "v_exp_f16 v224, v225\n\t" \
  "v_mfma_f32_16x16x32_bf16 v[188:191], v[220:223], v[116:119], v[188:191]\n\t" \
  "ds_read_b64_tr_b16 v[218:219], v250 offset:13152\n\t" \
  "v_lshlrev_b32_sdwa v187, 1, v187 dst_sel:DWORD dst_unused:UNUSED_PAD src0_sel:DWORD src1_sel:WORD_1\n\t" \
  "v_mfma_f32_16x16x32_bf16 v[196:199], v[220:223], v[128:131], v[196:199]\n\t" \
  "ds_read_u16_d16_hi v179, v179 offset:0\n\t" \
  "v_lshlrev_b32_sdwa v192, 1, v192 dst_sel:DWORD dst_unused:UNUSED_PAD src0_sel:DWORD src1_sel:WORD_1\n\t" \
  "v_mfma_f32_16x16x32_bf16 v[204:207], v[220:223], v[140:143], v[204:207]\n\t" \
  "s_waitcnt lgkmcnt(10)\n\t" \
  "ds_read_b64_tr_b16 v[220:221], v250 offset:12392\n\t" \
  "ds_read_u16_d16_hi v186, v186 offset:0\n\t" \
  "v_exp_f16_sdwa v224, v225 dst_sel:WORD_1 dst_unused:UNUSED_PRESERVE src0_sel:WORD_1\n\t" \
  "v_smfmac_f32_16x16x64_bf16 v[0:3], v[208:211], v[144:151], v248\n\t" \
  "ds_read_b64_tr_b16 v[222:223], v250 offset:13160\n\t" \
  "v_lshlrev_b32_sdwa v193, 1, v193 dst_sel:DWORD dst_unused:UNUSED_PAD src0_sel:DWORD src1_sel:WORD_1\n\t" \
  "v_smfmac_f32_16x16x64_bf16 v[24:27], v[208:211], v[152:159], v248\n\t" \
  "ds_read_u16_d16_hi v187, v187 offset:0\n\t" \
  "v_pk_fma_f16 v224, v224, %[hc], %[hc]\n\t" \
  "v_smfmac_f32_16x16x64_bf16 v[48:51], v[208:211], v[160:167], v248\n\t" \
  "s_waitcnt lgkmcnt(11)\n\t" \
  "ds_read_u16_d16_hi v192, v192 offset:0\n\t" \
  "v_lshlrev_b32_sdwa v194, 1, v194 dst_sel:DWORD dst_unused:UNUSED_PAD src0_sel:DWORD src1_sel:WORD_1\n\t" \
  "v_smfmac_f32_16x16x64_bf16 v[72:75], v[208:211], v[168:175], v248\n\t" \
  "s_waitcnt lgkmcnt(11)\n\t" \
  "ds_read_b64_tr_b16 v[208:209], v250 offset:12448\n\t" \
  "v_rcp_f16 v225, v224\n\t" \
  "v_smfmac_f32_16x16x64_bf16 v[4:7], v[212:215], v[144:151], v248\n\t" \
  "ds_read_u16_d16_hi v193, v193 offset:0\n\t" \
  "v_lshlrev_b32_sdwa v195, 1, v195 dst_sel:DWORD dst_unused:UNUSED_PAD src0_sel:DWORD src1_sel:WORD_1\n\t" \
  "v_smfmac_f32_16x16x64_bf16 v[28:31], v[212:215], v[152:159], v248\n\t" \
  "ds_read_b64_tr_b16 v[210:211], v250 offset:13216\n\t" \
  "v_lshlrev_b32_sdwa v200, 1, v200 dst_sel:DWORD dst_unused:UNUSED_PAD src0_sel:DWORD src1_sel:WORD_1\n\t" \
  "v_smfmac_f32_16x16x64_bf16 v[52:55], v[212:215], v[160:167], v248\n\t" \
  "ds_read_u16_d16_hi v194, v194 offset:0\n\t" \
  "v_rcp_f16_sdwa v224, v224 dst_sel:DWORD dst_unused:UNUSED_PAD src0_sel:WORD_1\n\t" \
  "v_smfmac_f32_16x16x64_bf16 v[76:79], v[212:215], v[168:175], v248\n\t" \
  "s_waitcnt lgkmcnt(10)\n\t" \
  "ds_read_b64_tr_b16 v[212:213], v250 offset:12456\n\t" \
  "v_lshlrev_b32_sdwa v201, 1, v201 dst_sel:DWORD dst_unused:UNUSED_PAD src0_sel:DWORD src1_sel:WORD_1\n\t" \
  "v_smfmac_f32_16x16x64_bf16 v[8:11], v[216:219], v[144:151], v248\n\t" \
  "ds_read_u16_d16_hi v195, v195 offset:0\n\t" \
  "v_fma_mix_f32 v184, v184, v225, 0 op_sel_hi:[0,1,0]\n\t" \
  "v_smfmac_f32_16x16x64_bf16 v[32:35], v[216:219], v[152:159], v248\n\t" \
  "ds_read_b64_tr_b16 v[214:215], v250 offset:13224\n\t" \
  "v_lshlrev_b32_sdwa v202, 1, v202 dst_sel:DWORD dst_unused:UNUSED_PAD src0_sel:DWORD src1_sel:WORD_1\n\t" \
  "v_smfmac_f32_16x16x64_bf16 v[56:59], v[216:219], v[160:167], v248\n\t" \
  "ds_read_u16_d16_hi v200, v200 offset:0\n\t" \
  "v_fma_mix_f32 v185, v185, v224, 0 op_sel_hi:[0,1,0]\n\t" \
  "v_smfmac_f32_16x16x64_bf16 v[80:83], v[216:219], v[168:175], v248\n\t" \
  "s_waitcnt lgkmcnt(10)\n\t" \
  "s_barrier\n\t" \
  "s_mov_b32 %[lc], 3\n\t" \
  ".Lcnx_m:\n\t" \
  "ds_read_b64_tr_b16 v[216:217], v249 offset:0\n\t" \
  "ds_read_b128 v[96:99], v251 offset:0\n\t" \
  "v_lshlrev_b32_sdwa v203, 1, v203 dst_sel:DWORD dst_unused:UNUSED_PAD src0_sel:DWORD src1_sel:WORD_1\n\t" \
  "v_smfmac_f32_16x16x64_bf16 v[12:15], v[220:223], v[144:151], v248\n\t" \
  "ds_read_b64_tr_b16 v[218:219], v249 offset:256\n\t" \
  "ds_read_b128 v[100:103], v251 offset:4096\n\t" \
  "v_mul_f32 v188, %[kk], v188\n\t" \
  "v_smfmac_f32_16x16x64_bf16 v[36:39], v[220:223], v[152:159], v248\n\t" \
  "ds_read_b128 v[104:107], v251 offset:8192\n\t" \
  "v_lshlrev_b32_sdwa v180, 1, v180 dst_sel:DWORD dst_unused:UNUSED_PAD src0_sel:DWORD src1_sel:WORD_1\n\t" \
  "v_mul_f32 v189, %[kk], v189\n\t" \
  "v_smfmac_f32_16x16x64_bf16 v[60:63], v[220:223], v[160:167], v248\n\t" \
  "ds_read_u16_d16_hi v201, v201 offset:0\n\t" \
  "v_cvt_pk_f16_f32 v224, v188, v189\n\t" \
  "v_smfmac_f32_16x16x64_bf16 v[84:87], v[220:223], v[168:175], v248\n\t" \
  "s_waitcnt lgkmcnt(11)\n\t" \
  "ds_read_b64_tr_b16 v[220:221], v249 offset:2048\n\t" \
  "ds_read_u16_d16_hi v202, v202 offset:0\n\t" \
  "v_lshlrev_b32_sdwa v181, 1, v181 dst_sel:DWORD dst_unused:UNUSED_PAD src0_sel:DWORD src1_sel:WORD_1\n\t" \
  "v_smfmac_f32_16x16x64_bf16 v[16:19], v[208:211], v[144:151], v248\n\t" \
  "ds_read_b64_tr_b16 v[222:223], v249 offset:2304\n\t" \
  "v_pk_fma_f16 v225, v224, v224, %[h1] neg_lo:[1,0,0] neg_hi:[1,0,0]\n\t" \
  "v_smfmac_f32_16x16x64_bf16 v[40:43], v[208:211], v[152:159], v248\n\t" \
  "ds_read_u16_d16_hi v203, v203 offset:0\n\t" \
  "v_lshlrev_b32_sdwa v182, 1, v182 dst_sel:DWORD dst_unused:UNUSED_PAD src0_sel:DWORD src1_sel:WORD_1\n\t" \
  "v_pk_mul_f16 v225, v224, v225\n\t" \
  "v_smfmac_f32_16x16x64_bf16 v[64:67], v[208:211], v[160:167], v248\n\t" \
  "ds_read_u16_d16_hi v180, v180 offset:0\n\t" \
  "v_lshlrev_b32_sdwa v183, 1, v183 dst_sel:DWORD dst_unused:UNUSED_PAD src0_sel:DWORD src1_sel:WORD_1\n\t" \
  "v_smfmac_f32_16x16x64_bf16 v[88:91], v[208:211], v[168:175], v248\n\t" \
  "s_waitcnt lgkmcnt(12)\n\t" \
  "ds_read_b64_tr_b16 v[208:209], v249 offset:4096\n\t" \
  "ds_read_u16_d16_hi v181, v181 offset:0\n\t" \
  "v_exp_f16 v224, v225\n\t" \
  "v_smfmac_f32_16x16x64_bf16 v[20:23], v[212:215], v[144:151], v248\n\t" \
  "ds_read_b64_tr_b16 v[210:211], v249 offset:4352\n\t" \
  "v_lshlrev_b32_sdwa v190, 1, v190 dst_sel:DWORD dst_unused:UNUSED_PAD src0_sel:DWORD src1_sel:WORD_1\n\t" \
  "v_smfmac_f32_16x16x64_bf16 v[44:47], v[212:215], v[152:159], v248\n\t" \
  "ds_read_u16_d16_hi v182, v182 offset:0\n\t" \
  "v_lshlrev_b32_sdwa v191, 1, v191 dst_sel:DWORD dst_unused:UNUSED_PAD src0_sel:DWORD src1_sel:WORD_1\n\t" \
  "v_smfmac_f32_16x16x64_bf16 v[68:71], v[212:215], v[160:167], v248\n\t" \
  "ds_read_u16_d16_hi v183, v183 offset:0\n\t" \
  "v_exp_f16_sdwa v224, v225 dst_sel:WORD_1 dst_unused:UNUSED_PRESERVE src0_sel:WORD_1\n\t" \
  "v_smfmac_f32_16x16x64_bf16 v[92:95], v[212:215], v[168:175], v248\n\t" \
  "s_waitcnt lgkmcnt(13)\n\t" \
  "ds_read_b64_tr_b16 v[212:213], v249 offset:512\n\t" \
  "v_lshlrev_b32_sdwa v196, 1, v196 dst_sel:DWORD dst_unused:UNUSED_PAD src0_sel:DWORD src1_sel:WORD_1\n\t" \
  "v_mfma_f32_16x16x32_bf16 v[144:147], v[216:219], v[96:99], 0\n\t" \
  "ds_read_u16_d16_hi v190, v190 offset:0\n\t" \
  "v_pk_fma_f16 v224, v224, %[hc], %[hc]\n\t" \
  "v_mfma_f32_16x16x32_bf16 v[152:155], v[216:219], v[108:111], 0\n\t" \
  "ds_read_b64_tr_b16 v[214:215], v249 offset:768\n\t" \
  "v_lshlrev_b32_sdwa v197, 1, v197 dst_sel:DWORD dst_unused:UNUSED_PAD src0_sel:DWORD src1_sel:WORD_1\n\t" \
  "v_mfma_f32_16x16x32_bf16 v[160:163], v[216:219], v[120:123], 0\n\t" \
  "ds_read_u16_d16_hi v191, v191 offset:0\n\t" \
  "v_rcp_f16 v225, v224\n\t" \
  "v_mfma_f32_16x16x32_bf16 v[168:171], v[216:219], v[132:135], 0\n\t" \
  "s_waitcnt lgkmcnt(11)\n\t" \
  "ds_read_b64_tr_b16 v[216:217], v249 offset:2560\n\t" \
  "v_lshlrev_b32_sdwa v198, 1, v198 dst_sel:DWORD dst_unused:UNUSED_PAD src0_sel:DWORD src1_sel:WORD_1\n\t" \
  "v_mfma_f32_16x16x32_bf16 v[144:147], v[220:223], v[100:103], v[144:147]\n\t" \
  "ds_read_u16_d16_hi v196, v196 offset:0\n\t" \
  "v_lshlrev_b32_sdwa v199, 1, v199 dst_sel:DWORD dst_unused:UNUSED_PAD src0_sel:DWORD src1_sel:WORD_1\n\t" \
  "v_mfma_f32_16x16x32_bf16 v[152:155], v[220:223], v[112:115], v[152:155]\n\t" \
  "ds_read_b64_tr_b16 v[218:219], v249 offset:2816\n\t" \
  "v_rcp_f16_sdwa v224, v224 dst_sel:DWORD dst_unused:UNUSED_PAD src0_sel:WORD_1\n\t" \
  "v_mfma_f32_16x16x32_bf16 v[160:163], v[220:223], v[124:127], v[160:163]\n\t" \
  "ds_read_u16_d16_hi v197, v197 offset:0\n\t" \
  "v_lshlrev_b32_sdwa v204, 1, v204 dst_sel:DWORD dst_unused:UNUSED_PAD src0_sel:DWORD src1_sel:WORD_1\n\t" \
  "v_mfma_f32_16x16x32_bf16 v[168:171], v[220:223], v[136:139], v[168:171]\n\t" \
  "s_waitcnt lgkmcnt(10)\n\t" \
  "ds_read_b64_tr_b16 v[220:221], v249 offset:4608\n\t" \
  "ds_read_u16_d16_hi v198, v198 offset:0\n\t" \
  "v_mfma_f32_16x16x32_bf16 v[144:147], v[208:211], v[104:107], v[144:147]\n\t" \
  "ds_read_b64_tr_b16 v[222:223], v249 offset:4864\n\t" \
  "v_lshlrev_b32_sdwa v205, 1, v205 dst_sel:DWORD dst_unused:UNUSED_PAD src0_sel:DWORD src1_sel:WORD_1\n\t" \
  "v_fma_mix_f32 v188, v188, v225, 0 op_sel_hi:[0,1,0]\n\t" \
  "v_mfma_f32_16x16x32_bf16 v[152:155], v[208:211], v[116:119], v[152:155]\n\t" \
  "ds_read_u16_d16_hi v199, v199 offset:0\n\t" \
  "v_fma_mix_f32 v189, v189, v224, 0 op_sel_hi:[0,1,0]\n\t" \
  "v_mfma_f32_16x16x32_bf16 v[160:163], v[208:211], v[128:131], v[160:163]\n\t" \
  "ds_read_u16_d16_hi v204, v204 offset:0\n\t" \
  "v_lshlrev_b32_sdwa v206, 1, v206 dst_sel:DWORD dst_unused:UNUSED_PAD src0_sel:DWORD src1_sel:WORD_1\n\t" \
  "v_mfma_f32_16x16x32_bf16 v[168:171], v[208:211], v[140:143], v[168:171]\n\t" \
  "s_waitcnt lgkmcnt(10)\n\t" \
  "ds_read_b64_tr_b16 v[208:209], v250 offset:18480\n\t" \
  "v_lshlrev_b32_sdwa v207, 1, v207 dst_sel:DWORD dst_unused:UNUSED_PAD src0_sel:DWORD src1_sel:WORD_1\n\t" \
  "v_mfma_f32_16x16x32_bf16 v[148:151], v[212:215], v[96:99], 0\n\t" \
  "ds_read_u16_d16_hi v205, v205 offset:0\n\t" \
  "v_lshlrev_b32_sdwa v144, 1, v144 dst_sel:DWORD dst_unused:UNUSED_PAD src0_sel:DWORD src1_sel:WORD_1\n\t" \
  "v_mfma_f32_16x16x32_bf16 v[156:159], v[212:215], v[108:111], 0\n\t" \
  "ds_read_b64_tr_b16 v[210:211], v250 offset:19248\n\t" \
  "v_mul_f32 v152, %[kk], v152\n\t" \
  "v_mfma_f32_16x16x32_bf16 v[164:167], v[212:215], v[120:123], 0\n\t" \
  "ds_read_u16_d16_hi v206, v206 offset:0\n\t" \
  "v_lshlrev_b32_sdwa v145, 1, v145 dst_sel:DWORD dst_unused:UNUSED_PAD src0_sel:DWORD src1_sel:WORD_1\n\t" \
  "v_mul_f32 v153, %[kk], v153\n\t" \
  "v_mfma_f32_16x16x32_bf16 v[172:175], v[212:215], v[132:135], 0\n\t" \
  "s_waitcnt lgkmcnt(10)\n\t" \
  "ds_read_b64_tr_b16 v[212:213], v250 offset:18488\n\t" \
  "ds_read_u16_d16_hi v207, v207 offset:0\n\t" \
  "v_cvt_pk_f16_f32 v224, v152, v153\n\t" \
  "v_mfma_f32_16x16x32_bf16 v[148:151], v[216:219], v[100:103], v[148:151]\n\t" \
  "ds_read_b64_tr_b16 v[214:215], v250 offset:19256\n\t" \
  "v_lshlrev_b32_sdwa v146, 1, v146 dst_sel:DWORD dst_unused:UNUSED_PAD src0_sel:DWORD src1_sel:WORD_1\n\t" \
  "v_mfma_f32_16x16x32_bf16 v[156:159], v[216:219], v[112:115], v[156:159]\n\t" \
  "ds_read_u16_d16_hi v144, v144 offset:0\n\t" \
  "v_pk_fma_f16 v225, v224, v224, %[h1] neg_lo:[1,0,0] neg_hi:[1,0,0]\n\t" \
  "v_mfma_f32_16x16x32_bf16 v[164:167], v[216:219], v[124:127], v[164:167]\n\t" \
  "ds_read_u16_d16_hi v145, v145 offset:0\n\t" \
  "v_lshlrev_b32_sdwa v147, 1, v147 dst_sel:DWORD dst_unused:UNUSED_PAD src0_sel:DWORD src1_sel:WORD_1\n\t" \
  "v_pk_mul_f16 v225, v224, v225\n\t" \
  "v_mfma_f32_16x16x32_bf16 v[172:175], v[216:219], v[136:139], v[172:175]\n\t" \
  "s_waitcnt lgkmcnt(11)\n\t" \
  "ds_read_b64_tr_b16 v[216:217], v250 offset:18544\n\t" \
  "v_lshlrev_b32_sdwa v154, 1, v154 dst_sel:DWORD dst_unused:UNUSED_PAD src0_sel:DWORD src1_sel:WORD_1\n\t" \
  "v_mfma_f32_16x16x32_bf16 v[148:151], v[220:223], v[104:107], v[148:151]\n\t" \
  "ds_read_u16_d16_hi v146, v146 offset:0\n\t" \
  "v_exp_f16 v224, v225\n\t" \
  "v_mfma_f32_16x16x32_bf16 v[156:159], v[220:223], v[116:119], v[156:159]\n\t" \
  "ds_read_b64_tr_b16 v[218:219], v250 offset:19312\n\t" \
  "v_lshlrev_b32_sdwa v155, 1, v155 dst_sel:DWORD dst_unused:UNUSED_PAD src0_sel:DWORD src1_sel:WORD_1\n\t" \
  "v_mfma_f32_16x16x32_bf16 v[164:167], v[220:223], v[128:131], v[164:167]\n\t" \
  "ds_read_u16_d16_hi v147, v147 offset:0\n\t" \
  "v_lshlrev_b32_sdwa v160, 1, v160 dst_sel:DWORD dst_unused:UNUSED_PAD src0_sel:DWORD src1_sel:WORD_1\n\t" \
  "v_mfma_f32_16x16x32_bf16 v[172:175], v[220:223], v[140:143], v[172:175]\n\t" \
  "s_waitcnt lgkmcnt(10)\n\t" \
  "ds_read_b64_tr_b16 v[220:221], v250 offset:18552\n\t" \
  "ds_read_u16_d16_hi v154, v154 offset:0\n\t" \
  "v_exp_f16_sdwa v224, v225 dst_sel:WORD_1 dst_unused:UNUSED_PRESERVE src0_sel:WORD_1\n\t" \
  "v_smfmac_f32_16x16x64_bf16 v[0:3], v[208:211], v[176:183], v248\n\t" \
  "ds_read_b64_tr_b16 v[222:223], v250 offset:19320\n\t" \
  "v_lshlrev_b32_sdwa v161, 1, v161 dst_sel:DWORD dst_unused:UNUSED_PAD src0_sel:DWORD src1_sel:WORD_1\n\t" \
  "v_smfmac_f32_16x16x64_bf16 v[24:27], v[208:211], v[184:191], v248\n\t" \
  "ds_read_u16_d16_hi v155, v155 offset:0\n\t" \
  "v_pk_fma_f16 v224, v224, %[hc], %[hc]\n\t" \
  "v_smfmac_f32_16x16x64_bf16 v[48:51], v[208:211], v[192:199], v248\n\t" \
  "s_waitcnt lgkmcnt(11)\n\t" \
  "ds_read_u16_d16_hi v160, v160 offset:0\n\t" \
  "v_lshlrev_b32_sdwa v162, 1, v162 dst_sel:DWORD dst_unused:UNUSED_PAD src0_sel:DWORD src1_sel:WORD_1\n\t" \
  "v_smfmac_f32_16x16x64_bf16 v[72:75], v[208:211], v[200:207], v248\n\t" \
  "s_waitcnt lgkmcnt(11)\n\t" \
  "ds_read_b64_tr_b16 v[208:209], v250 offset:18608\n\t" \
  "v_rcp_f16 v225, v224\n\t" \
  "v_smfmac_f32_16x16x64_bf16 v[4:7], v[212:215], v[176:183], v248\n\t" \
  "ds_read_b64 v[242:243], v252 offset:128\n\t" \
  "v_lshlrev_b32_sdwa v163, 1, v163 dst_sel:DWORD dst_unused:UNUSED_PAD src0_sel:DWORD src1_sel:WORD_1\n\t" \
  "v_smfmac_f32_16x16x64_bf16 v[28:31], v[212:215], v[184:191], v248\n\t" \
  "ds_read_b64_tr_b16 v[210:211], v250 offset:19376\n\t" \
  "ds_read_b64 v[244:245], v252 offset:136\n\t" \
  "v_smfmac_f32_16x16x64_bf16 v[52:55], v[212:215], v[192:199], v248\n\t" \
  "ds_read_u16_d16_hi v161, v161 offset:0\n\t" \
  "v_lshlrev_b32_sdwa v168, 1, v168 dst_sel:DWORD dst_unused:UNUSED_PAD src0_sel:DWORD src1_sel:WORD_1\n\t" \
  "v_rcp_f16_sdwa v224, v224 dst_sel:DWORD dst_unused:UNUSED_PAD src0_sel:WORD_1\n\t" \
  "v_cvt_pk_bf16_f32 v230, v0, v1\n\t" \
  "v_smfmac_f32_16x16x64_bf16 v[76:79], v[212:215], v[200:207], v248\n\t" \
  "s_waitcnt lgkmcnt(11)\n\t" \
  "ds_read_b64_tr_b16 v[212:213], v250 offset:18616\n\t" \
  "ds_read_u16_d16_hi v162, v162 offset:0\n\t" \
  "v_cvt_pk_bf16_f32 v231, v2, v3\n\t" \
  "v_smfmac_f32_16x16x64_bf16 v[8:11], v[216:219], v[176:183], v248\n\t" \
  "ds_read_b64_tr_b16 v[214:215], v250 offset:19384\n\t" \
  "v_lshlrev_b32_sdwa v169, 1, v169 dst_sel:DWORD dst_unused:UNUSED_PAD src0_sel:DWORD src1_sel:WORD_1\n\t" \
  "v_cvt_pk_bf16_f32 v232, v4, v5\n\t" \
  "v_smfmac_f32_16x16x64_bf16 v[32:35], v[216:219], v[184:191], v248\n\t" \
  "ds_read_u16_d16_hi v163, v163 offset:0\n\t" \
  "v_fma_mix_f32 v152, v152, v225, 0 op_sel_hi:[0,1,0]\n\t" \
  "v_cvt_pk_bf16_f32 v233, v6, v7\n\t" \
  "v_smfmac_f32_16x16x64_bf16 v[56:59], v[216:219], v[192:199], v248\n\t" \
  "buffer_store_dwordx4 v[230:233], v253, %[osrd], %[ob] offen offset:0 sc1\n\t" \
  "ds_read_u16_d16_hi v168, v168 offset:0\n\t" \
  "v_lshlrev_b32_sdwa v170, 1, v170 dst_sel:DWORD dst_unused:UNUSED_PAD src0_sel:DWORD src1_sel:WORD_1\n\t" \
  "v_fma_mix_f32 v153, v153, v224, 0 op_sel_hi:[0,1,0]\n\t" \
  "v_smfmac_f32_16x16x64_bf16 v[80:83], v[216:219], v[200:207], v248\n\t" \
  "s_waitcnt lgkmcnt(12)\n\t" \
  "s_barrier\n\t" \
  "ds_read_b64_tr_b16 v[216:217], v249 offset:6144\n\t" \
  "ds_read_b64 v[230:231], v252 offset:0\n\t" \
  "v_lshlrev_b32_sdwa v171, 1, v171 dst_sel:DWORD dst_unused:UNUSED_PAD src0_sel:DWORD src1_sel:WORD_1\n\t" \
  "v_smfmac_f32_16x16x64_bf16 v[12:15], v[220:223], v[176:183], v248\n\t" \
  "ds_read_b64_tr_b16 v[218:219], v249 offset:6400\n\t" \
  "ds_read_b64 v[232:233], v252 offset:8\n\t" \
  "v_mul_f32 v156, %[kk], v156\n\t" \
  "v_smfmac_f32_16x16x64_bf16 v[36:39], v[220:223], v[184:191], v248\n\t" \
  "ds_read_u16_d16_hi v169, v169 offset:0\n\t" \
  "v_lshlrev_b32_sdwa v148, 1, v148 dst_sel:DWORD dst_unused:UNUSED_PAD src0_sel:DWORD src1_sel:WORD_1\n\t" \
  "v_mul_f32 v157, %[kk], v157\n\t" \
  "v_smfmac_f32_16x16x64_bf16 v[60:63], v[220:223], v[192:199], v248\n\t" \
  "ds_read_u16_d16_hi v170, v170 offset:0\n\t" \
  "v_cvt_pk_f16_f32 v224, v156, v157\n\t" \
  "v_cvt_pk_bf16_f32 v234, v8, v9\n\t" \
  "v_smfmac_f32_16x16x64_bf16 v[84:87], v[220:223], v[200:207], v248\n\t" \
  "s_waitcnt lgkmcnt(13)\n\t" \
  "ds_read_b64_tr_b16 v[220:221], v249 offset:8192\n\t" \
  "v_lshlrev_b32_sdwa v149, 1, v149 dst_sel:DWORD dst_unused:UNUSED_PAD src0_sel:DWORD src1_sel:WORD_1\n\t" \
  "v_cvt_pk_bf16_f32 v235, v10, v11\n\t" \
  "v_smfmac_f32_16x16x64_bf16 v[16:19], v[208:211], v[176:183], v248\n\t" \
  "ds_read_u16_d16_hi v171, v171 offset:0\n\t" \
  "v_pk_fma_f16 v225, v224, v224, %[h1] neg_lo:[1,0,0] neg_hi:[1,0,0]\n\t" \
  "v_cvt_pk_bf16_f32 v236, v12, v13\n\t" \
  "v_smfmac_f32_16x16x64_bf16 v[40:43], v[208:211], v[184:191], v248\n\t" \
  "ds_read_b64_tr_b16 v[222:223], v249 offset:8448\n\t" \
  "v_lshlrev_b32_sdwa v150, 1, v150 dst_sel:DWORD dst_unused:UNUSED_PAD src0_sel:DWORD src1_sel:WORD_1\n\t" \
  "v_pk_mul_f16 v225, v224, v225\n\t" \
  "v_cvt_pk_bf16_f32 v237, v14, v15\n\t" \
  "v_smfmac_f32_16x16x64_bf16 v[64:67], v[208:211], v[192:199], v248\n\t" \
  "buffer_store_dwordx4 v[234:237], v253, %[osrd], %[ob] offen offset:64 sc1\n\t" \
  "ds_read_u16_d16_hi v148, v148 offset:0\n\t" \
  "v_lshlrev_b32_sdwa v151, 1, v151 dst_sel:DWORD dst_unused:UNUSED_PAD src0_sel:DWORD src1_sel:WORD_1\n\t" \
  "v_smfmac_f32_16x16x64_bf16 v[88:91], v[208:211], v[200:207], v248\n\t" \
  "s_waitcnt lgkmcnt(12)\n\t" \
  "ds_read_b64_tr_b16 v[208:209], v249 offset:10240\n\t" \
  "ds_read_b64 v[234:235], v252 offset:64\n\t" \
  "v_exp_f16 v224, v225\n\t" \
  "v_smfmac_f32_16x16x64_bf16 v[20:23], v[212:215], v[176:183], v248\n\t" \
  "s_waitcnt lgkmcnt(10)\n\t" \
  "ds_read_b64_tr_b16 v[210:211], v249 offset:10496\n\t" \
  "ds_read_b64 v[236:237], v252 offset:72\n\t" \
  "v_mfma_f32_16x16x16_bf16 v[0:3], v[246:247], v[230:231], 0\n\t" \
  "ds_read_u16_d16_hi v149, v149 offset:0\n\t" \
  "v_lshlrev_b32_sdwa v158, 1, v158 dst_sel:DWORD dst_unused:UNUSED_PAD src0_sel:DWORD src1_sel:WORD_1\n\t" \
  "v_smfmac_f32_16x16x64_bf16 v[44:47], v[212:215], v[184:191], v248\n\t" \
  "ds_read_u16_d16_hi v150, v150 offset:0\n\t" \
  "v_lshlrev_b32_sdwa v159, 1, v159 dst_sel:DWORD dst_unused:UNUSED_PAD src0_sel:DWORD src1_sel:WORD_1\n\t" \
  "v_smfmac_f32_16x16x64_bf16 v[68:71], v[212:215], v[192:199], v248\n\t" \
  "ds_read_u16_d16_hi v151, v151 offset:0\n\t" \
  "v_exp_f16_sdwa v224, v225 dst_sel:WORD_1 dst_unused:UNUSED_PRESERVE src0_sel:WORD_1\n\t" \
  "v_cvt_pk_bf16_f32 v238, v16, v17\n\t" \
  "v_smfmac_f32_16x16x64_bf16 v[92:95], v[212:215], v[200:207], v248\n\t" \
  "s_waitcnt lgkmcnt(13)\n\t" \
  "ds_read_b64_tr_b16 v[212:213], v249 offset:6656\n\t" \
  "v_cvt_pk_bf16_f32 v239, v18, v19\n\t" \
  "v_mfma_f32_16x16x16_bf16 v[4:7], v[246:247], v[232:233], 0\n\t" \
  "ds_read_u16_d16_hi v158, v158 offset:0\n\t" \
  "v_lshlrev_b32_sdwa v164, 1, v164 dst_sel:DWORD dst_unused:UNUSED_PAD src0_sel:DWORD src1_sel:WORD_1\n\t" \
  "v_mfma_f32_16x16x32_bf16 v[176:179], v[216:219], v[96:99], 0\n\t" \
  "ds_read_b64_tr_b16 v[214:215], v249 offset:6912\n\t" \
  "v_pk_fma_f16 v224, v224, %[hc], %[hc]\n\t" \
  "v_cvt_pk_bf16_f32 v240, v20, v21\n\t" \
  "v_mfma_f32_16x16x32_bf16 v[184:187], v[216:219], v[108:111], 0\n\t" \
  "ds_read_u16_d16_hi v159, v159 offset:0\n\t" \
  "v_lshlrev_b32_sdwa v165, 1, v165 dst_sel:DWORD dst_unused:UNUSED_PAD src0_sel:DWORD src1_sel:WORD_1\n\t" \
  "v_cvt_pk_bf16_f32 v241, v22, v23\n\t" \
  "v_mfma_f32_16x16x32_bf16 v[192:195], v[216:219], v[120:123], 0\n\t" \
  "buffer_store_dwordx4 v[238:241], v253, %[osrd], %[ob] offen offset:128 sc1\n\t" \
  "v_rcp_f16 v225, v224\n\t" \
  "v_mfma_f32_16x16x32_bf16 v[200:203], v[216:219], v[132:135], 0\n\t" \
  "s_waitcnt lgkmcnt(12)\n\t" \
  "ds_read_b64_tr_b16 v[216:217], v249 offset:8704\n\t" \
  "v_lshlrev_b32_sdwa v166, 1, v166 dst_sel:DWORD dst_unused:UNUSED_PAD src0_sel:DWORD src1_sel:WORD_1\n\t" \
  "v_mfma_f32_16x16x32_bf16 v[176:179], v[220:223], v[100:103], v[176:179]\n\t" \
  "s_waitcnt lgkmcnt(10)\n\t" \
  "ds_read_u16_d16_hi v164, v164 offset:0\n\t" \
  "v_lshlrev_b32_sdwa v167, 1, v167 dst_sel:DWORD dst_unused:UNUSED_PAD src0_sel:DWORD src1_sel:WORD_1\n\t" \
  "v_mfma_f32_16x16x16_bf16 v[8:11], v[246:247], v[234:235], 0\n\t" \
  "ds_read_b64_tr_b16 v[218:219], v249 offset:8960\n\t" \
  "v_rcp_f16_sdwa v224, v224 dst_sel:DWORD dst_unused:UNUSED_PAD src0_sel:WORD_1\n\t" \
  "v_mfma_f32_16x16x32_bf16 v[184:187], v[220:223], v[112:115], v[184:187]\n\t" \
  "ds_read_u16_d16_hi v165, v165 offset:0\n\t" \
  "v_lshlrev_b32_sdwa v172, 1, v172 dst_sel:DWORD dst_unused:UNUSED_PAD src0_sel:DWORD src1_sel:WORD_1\n\t" \
  "v_mfma_f32_16x16x32_bf16 v[192:195], v[220:223], v[124:127], v[192:195]\n\t" \
  "ds_read_u16_d16_hi v166, v166 offset:0\n\t" \
  "v_lshlrev_b32_sdwa v173, 1, v173 dst_sel:DWORD dst_unused:UNUSED_PAD src0_sel:DWORD src1_sel:WORD_1\n\t" \
  "v_mfma_f32_16x16x32_bf16 v[200:203], v[220:223], v[136:139], v[200:203]\n\t" \
  "s_waitcnt lgkmcnt(12)\n\t" \
  "ds_read_b64_tr_b16 v[220:221], v249 offset:10752\n\t" \
  "ds_read_u16_d16_hi v167, v167 offset:0\n\t" \
  "v_mfma_f32_16x16x16_bf16 v[12:15], v[246:247], v[236:237], 0\n\t" \
  "ds_read_b64_tr_b16 v[222:223], v249 offset:11008\n\t" \
  "v_fma_mix_f32 v156, v156, v225, 0 op_sel_hi:[0,1,0]\n\t" \
  "v_mfma_f32_16x16x32_bf16 v[176:179], v[208:211], v[104:107], v[176:179]\n\t" \
  "ds_read_u16_d16_hi v172, v172 offset:0\n\t" \
  "v_fma_mix_f32 v157, v157, v224, 0 op_sel_hi:[0,1,0]\n\t" \
  "v_mfma_f32_16x16x32_bf16 v[184:187], v[208:211], v[116:119], v[184:187]\n\t" \
  "ds_read_u16_d16_hi v173, v173 offset:0\n\t" \
  "v_lshlrev_b32_sdwa v174, 1, v174 dst_sel:DWORD dst_unused:UNUSED_PAD src0_sel:DWORD src1_sel:WORD_1\n\t" \
  "v_mfma_f32_16x16x32_bf16 v[192:195], v[208:211], v[128:131], v[192:195]\n\t" \
  "ds_read_u16_d16_hi v174, v174 offset:0\n\t" \
  "v_lshlrev_b32_sdwa v175, 1, v175 dst_sel:DWORD dst_unused:UNUSED_PAD src0_sel:DWORD src1_sel:WORD_1\n\t" \
  "v_mfma_f32_16x16x32_bf16 v[200:203], v[208:211], v[140:143], v[200:203]\n\t" \
  "s_waitcnt lgkmcnt(12)\n\t" \
  "ds_read_b64_tr_b16 v[208:209], v250 offset:0\n\t" \
  "v_lshlrev_b32_sdwa v176, 1, v176 dst_sel:DWORD dst_unused:UNUSED_PAD src0_sel:DWORD src1_sel:WORD_1\n\t" \
  "v_mfma_f32_16x16x32_bf16 v[180:183], v[212:215], v[96:99], 0\n\t" \
  "ds_read_u16_d16_hi v175, v175 offset:0\n\t" \
  "v_mul_f32 v184, %[kk], v184\n\t" \
  "v_mfma_f32_16x16x16_bf16 v[16:19], v[246:247], v[242:243], 0\n\t" \
  "ds_read_b64_tr_b16 v[210:211], v250 offset:768\n\t" \
  "v_lshlrev_b32_sdwa v177, 1, v177 dst_sel:DWORD dst_unused:UNUSED_PAD src0_sel:DWORD src1_sel:WORD_1\n\t" \
  "v_mfma_f32_16x16x32_bf16 v[188:191], v[212:215], v[108:111], 0\n\t" \
  "ds_read_u16_d16_hi v176, v176 offset:0\n\t" \
  "v_mul_f32 v185, %[kk], v185\n\t" \
  "v_mfma_f32_16x16x32_bf16 v[196:199], v[212:215], v[120:123], 0\n\t" \
  "ds_read_u16_d16_hi v177, v177 offset:0\n\t" \
  "v_cvt_pk_f16_f32 v224, v184, v185\n\t" \
  "v_mfma_f32_16x16x32_bf16 v[204:207], v[212:215], v[132:135], 0\n\t" \
  "ds_read_b64_tr_b16 v[212:213], v250 offset:8\n\t" \
  "v_lshlrev_b32_sdwa v178, 1, v178 dst_sel:DWORD dst_unused:UNUSED_PAD src0_sel:DWORD src1_sel:WORD_1\n\t" \
  "v_mfma_f32_16x16x16_bf16 v[20:23], v[246:247], v[244:245], 0\n\t" \
  "s_waitcnt lgkmcnt(14)\n\t" \
  "ds_read_u16_d16_hi v178, v178 offset:0\n\t" \
  "v_pk_fma_f16 v225, v224, v224, %[h1] neg_lo:[1,0,0] neg_hi:[1,0,0]\n\t" \
  "v_mfma_f32_16x16x32_bf16 v[180:183], v[216:219], v[100:103], v[180:183]\n\t" \
  "ds_read_b64_tr_b16 v[214:215], v250 offset:776\n\t" \
  "v_lshlrev_b32_sdwa v179, 1, v179 dst_sel:DWORD dst_unused:UNUSED_PAD src0_sel:DWORD src1_sel:WORD_1\n\t" \
  "v_mfma_f32_16x16x32_bf16 v[188:191], v[216:219], v[112:115], v[188:191]\n\t" \
  "ds_read_u16_d16_hi v179, v179 offset:0\n\t" \
  "v_pk_mul_f16 v225, v224, v225\n\t" \
  "v_mfma_f32_16x16x32_bf16 v[196:199], v[216:219], v[124:127], v[196:199]\n\t" \
  "v_lshlrev_b32_sdwa v186, 1, v186 dst_sel:DWORD dst_unused:UNUSED_PAD src0_sel:DWORD src1_sel:WORD_1\n\t" \
  "v_exp_f16 v224, v225\n\t" \
  "v_mfma_f32_16x16x32_bf16 v[204:207], v[216:219], v[136:139], v[204:207]\n\t" \
  "s_waitcnt lgkmcnt(12)\n\t" \
  "ds_read_b64_tr_b16 v[216:217], v250 offset:64\n\t" \
  "v_lshlrev_b32_sdwa v187, 1, v187 dst_sel:DWORD dst_unused:UNUSED_PAD src0_sel:DWORD src1_sel:WORD_1\n\t" \
  "v_mfma_f32_16x16x32_bf16 v[180:183], v[220:223], v[104:107], v[180:183]\n\t" \
  "ds_read_u16_d16_hi v186, v186 offset:0\n\t" \
  "v_lshlrev_b32_sdwa v192, 1, v192 dst_sel:DWORD dst_unused:UNUSED_PAD src0_sel:DWORD src1_sel:WORD_1\n\t" \
  "v_mfma_f32_16x16x32_bf16 v[188:191], v[220:223], v[116:119], v[188:191]\n\t" \
  "ds_read_b64_tr_b16 v[218:219], v250 offset:832\n\t" \
  "v_exp_f16_sdwa v224, v225 dst_sel:WORD_1 dst_unused:UNUSED_PRESERVE src0_sel:WORD_1\n\t" \
  "v_mfma_f32_16x16x32_bf16 v[196:199], v[220:223], v[128:131], v[196:199]\n\t" \
  "ds_read_u16_d16_hi v187, v187 offset:0\n\t" \
  "v_lshlrev_b32_sdwa v193, 1, v193 dst_sel:DWORD dst_unused:UNUSED_PAD src0_sel:DWORD src1_sel:WORD_1\n\t" \
  "v_mfma_f32_16x16x32_bf16 v[204:207], v[220:223], v[140:143], v[204:207]\n\t" \
  "s_waitcnt lgkmcnt(10)\n\t" \
  "ds_read_b64_tr_b16 v[220:221], v250 offset:72\n\t" \
  "v_pk_fma_f16 v224, v224, %[hc], %[hc]\n\t" \
  "v_smfmac_f32_16x16x64_bf16 v[0:3], v[208:211], v[144:151], v248\n\t" \
  "ds_read_u16_d16_hi v192, v192 offset:0\n\t" \
  "v_lshlrev_b32_sdwa v194, 1, v194 dst_sel:DWORD dst_unused:UNUSED_PAD src0_sel:DWORD src1_sel:WORD_1\n\t" \
  "v_smfmac_f32_16x16x64_bf16 v[24:27], v[208:211], v[152:159], v248\n\t" \
  "ds_read_b64_tr_b16 v[222:223], v250 offset:840\n\t" \
  "v_rcp_f16 v225, v224\n\t" \
  "v_smfmac_f32_16x16x64_bf16 v[48:51], v[208:211], v[160:167], v248\n\t" \
  "ds_read_u16_d16_hi v193, v193 offset:0\n\t" \
  "v_lshlrev_b32_sdwa v195, 1, v195 dst_sel:DWORD dst_unused:UNUSED_PAD src0_sel:DWORD src1_sel:WORD_1\n\t" \
  "v_smfmac_f32_16x16x64_bf16 v[72:75], v[208:211], v[168:175], v248\n\t" \
  "s_waitcnt lgkmcnt(9)\n\t" \
  "ds_read_b64_tr_b16 v[208:209], v250 offset:128\n\t" \
  "v_lshlrev_b32_sdwa v200, 1, v200 dst_sel:DWORD dst_unused:UNUSED_PAD src0_sel:DWORD src1_sel:WORD_1\n\t" \
  "v_smfmac_f32_16x16x64_bf16 v[4:7], v[212:215], v[144:151], v248\n\t" \
  "ds_read_u16_d16_hi v194, v194 offset:0\n\t" \
  "v_rcp_f16_sdwa v224, v224 dst_sel:DWORD dst_unused:UNUSED_PAD src0_sel:WORD_1\n\t" \
  "v_smfmac_f32_16x16x64_bf16 v[28:31], v[212:215], v[152:159], v248\n\t" \
  "s_add_u32 %[ob], %[ob], %[dob]\n\t" \
  "ds_read_b64_tr_b16 v[210:211], v250 offset:896\n\t" \
  "v_lshlrev_b32_sdwa v201, 1, v201 dst_sel:DWORD dst_unused:UNUSED_PAD src0_sel:DWORD src1_sel:WORD_1\n\t" \
  "v_smfmac_f32_16x16x64_bf16 v[52:55], v[212:215], v[160:167], v248\n\t" \
  "ds_read_u16_d16_hi v195, v195 offset:0\n\t" \
  "v_fma_mix_f32 v184, v184, v225, 0 op_sel_hi:[0,1,0]\n\t" \
  "v_smfmac_f32_16x16x64_bf16 v[76:79], v[212:215], v[168:175], v248\n\t" \
  "s_waitcnt lgkmcnt(9)\n\t" \
  "ds_read_b64_tr_b16 v[212:213], v250 offset:136\n\t" \
  "v_lshlrev_b32_sdwa v202, 1, v202 dst_sel:DWORD dst_unused:UNUSED_PAD src0_sel:DWORD src1_sel:WORD_1\n\t" \
  "v_smfmac_f32_16x16x64_bf16 v[8:11], v[216:219], v[144:151], v248\n\t" \
  "ds_read_u16_d16_hi v200, v200 offset:0\n\t" \
  "v_fma_mix_f32 v185, v185, v224, 0 op_sel_hi:[0,1,0]\n\t" \
  "v_smfmac_f32_16x16x64_bf16 v[32:35], v[216:219], v[152:159], v248\n\t" \
  "ds_read_b64_tr_b16 v[214:215], v250 offset:904\n\t" \
  "v_lshlrev_b32_sdwa v203, 1, v203 dst_sel:DWORD dst_unused:UNUSED_PAD src0_sel:DWORD src1_sel:WORD_1\n\t" \
  "v_smfmac_f32_16x16x64_bf16 v[56:59], v[216:219], v[160:167], v248\n\t" \
  "ds_read_u16_d16_hi v201, v201 offset:0\n\t" \
  "v_mul_f32 v188, %[kk], v188\n\t" \
  "v_smfmac_f32_16x16x64_bf16 v[80:83], v[216:219], v[168:175], v248\n\t" \
  "s_waitcnt lgkmcnt(9)\n\t" \
  "s_barrier\n\t" \
  "ds_read_b64_tr_b16 v[216:217], v249 offset:12288\n\t" \
  "v_lshlrev_b32_sdwa v180, 1, v180 dst_sel:DWORD dst_unused:UNUSED_PAD src0_sel:DWORD src1_sel:WORD_1\n\t" \
  "v_smfmac_f32_16x16x64_bf16 v[12:15], v[220:223], v[144:151], v248\n\t" \
  "ds_read_u16_d16_hi v202, v202 offset:0\n\t" \
  "v_mul_f32 v189, %[kk], v189\n\t" \
  "v_smfmac_f32_16x16x64_bf16 v[36:39], v[220:223], v[152:159], v248\n\t" \
  "ds_read_b64_tr_b16 v[218:219], v249 offset:12544\n\t" \
  "v_cvt_pk_f16_f32 v224, v188, v189\n\t" \
  "v_smfmac_f32_16x16x64_bf16 v[60:63], v[220:223], v[160:167], v248\n\t" \
  "ds_read_u16_d16_hi v203, v203 offset:0\n\t" \
  "v_lshlrev_b32_sdwa v181, 1, v181 dst_sel:DWORD dst_unused:UNUSED_PAD src0_sel:DWORD src1_sel:WORD_1\n\t" \
  "v_smfmac_f32_16x16x64_bf16 v[84:87], v[220:223], v[168:175], v248\n\t" \
  "s_waitcnt lgkmcnt(9)\n\t" \
  "ds_read_b64_tr_b16 v[220:221], v249 offset:14336\n\t" \
  "v_pk_fma_f16 v225, v224, v224, %[h1] neg_lo:[1,0,0] neg_hi:[1,0,0]\n\t" \
  "v_smfmac_f32_16x16x64_bf16 v[16:19], v[208:211], v[144:151], v248\n\t" \
  "s_add_u32 %[rs], %[rs], 1\n\t" \
  "ds_read_u16_d16_hi v180, v180 offset:0\n\t" \
  "v_lshlrev_b32_sdwa v182, 1, v182 dst_sel:DWORD dst_unused:UNUSED_PAD src0_sel:DWORD src1_sel:WORD_1\n\t" \
  "v_smfmac_f32_16x16x64_bf16 v[40:43], v[208:211], v[152:159], v248\n\t" \
  "s_cmp_eq_u32 %[rs], 6\n\t" \
  "ds_read_b64_tr_b16 v[222:223], v249 offset:14592\n\t" \
  "v_pk_mul_f16 v225, v224, v225\n\t" \
  "v_smfmac_f32_16x16x64_bf16 v[64:67], v[208:211], v[160:167], v248\n\t" \
  "ds_read_u16_d16_hi v181, v181 offset:0\n\t" \
  "v_lshlrev_b32_sdwa v183, 1, v183 dst_sel:DWORD dst_unused:UNUSED_PAD src0_sel:DWORD src1_sel:WORD_1\n\t" \
  "v_smfmac_f32_16x16x64_bf16 v[88:91], v[208:211], v[168:175], v248\n\t" \
  "s_waitcnt lgkmcnt(9)\n\t" \
  "ds_read_b64_tr_b16 v[208:209], v249 offset:16384\n\t" \
  "v_exp_f16 v224, v225\n\t" \
  "v_smfmac_f32_16x16x64_bf16 v[20:23], v[212:215], v[144:151], v248\n\t" \
  "s_cselect_b32 %[rs], 0, %[rs]\n\t" \
  "ds_read_u16_d16_hi v182, v182 offset:0\n\t" \
  "v_lshlrev_b32_sdwa v190, 1, v190 dst_sel:DWORD dst_unused:UNUSED_PAD src0_sel:DWORD src1_sel:WORD_1\n\t" \
  "v_smfmac_f32_16x16x64_bf16 v[44:47], v[212:215], v[152:159], v248\n\t" \
  "s_cselect_b32 %[t0], -75920, %[ps]\n\t" \
  "ds_read_b64_tr_b16 v[210:211], v249 offset:16640\n\t" \
  "v_lshlrev_b32_sdwa v191, 1, v191 dst_sel:DWORD dst_unused:UNUSED_PAD src0_sel:DWORD src1_sel:WORD_1\n\t" \
  "v_smfmac_f32_16x16x64_bf16 v[68:71], v[212:215], v[160:167], v248\n\t" \
  "ds_read_u16_d16_hi v183, v183 offset:0\n\t" \
  "v_add_u32 v252, %[t0], v252\n\t" \
  "v_exp_f16_sdwa v224, v225 dst_sel:WORD_1 dst_unused:UNUSED_PRESERVE src0_sel:WORD_1\n\t" \
  "v_smfmac_f32_16x16x64_bf16 v[92:95], v[212:215], v[168:175], v248\n\t" \
  "s_waitcnt lgkmcnt(9)\n\t" \
  "ds_read_b64_tr_b16 v[212:213], v249 offset:12800\n\t" \
  "v_lshlrev_b32_sdwa v196, 1, v196 dst_sel:DWORD dst_unused:UNUSED_PAD src0_sel:DWORD src1_sel:WORD_1\n\t" \
  "v_mfma_f32_16x16x32_bf16 v[144:147], v[216:219], v[96:99], 0\n\t" \
  "ds_read_u16_d16_hi v190, v190 offset:0\n\t" \
  "v_pk_fma_f16 v224, v224, %[hc], %[hc]\n\t" \
  "v_mfma_f32_16x16x32_bf16 v[152:155], v[216:219], v[108:111], 0\n\t" \
  "ds_read_b64_tr_b16 v[214:215], v249 offset:13056\n\t" \
  "v_lshlrev_b32_sdwa v197, 1, v197 dst_sel:DWORD dst_unused:UNUSED_PAD src0_sel:DWORD src1_sel:WORD_1\n\t" \
  "v_mfma_f32_16x16x32_bf16 v[160:163], v[216:219], v[120:123], 0\n\t" \
  "ds_read_u16_d16_hi v191, v191 offset:0\n\t" \
  "v_rcp_f16 v225, v224\n\t" \
  "v_mfma_f32_16x16x32_bf16 v[168:171], v[216:219], v[132:135], 0\n\t" \
  "s_waitcnt lgkmcnt(9)\n\t" \
  "ds_read_b64_tr_b16 v[216:217], v249 offset:14848\n\t" \
  "v_lshlrev_b32_sdwa v198, 1, v198 dst_sel:DWORD dst_unused:UNUSED_PAD src0_sel:DWORD src1_sel:WORD_1\n\t" \
  "v_mfma_f32_16x16x32_bf16 v[144:147], v[220:223], v[100:103], v[144:147]\n\t" \
  "ds_read_u16_d16_hi v196, v196 offset:0\n\t" \
  "v_lshlrev_b32_sdwa v199, 1, v199 dst_sel:DWORD dst_unused:UNUSED_PAD src0_sel:DWORD src1_sel:WORD_1\n\t" \
  "v_mfma_f32_16x16x32_bf16 v[152:155], v[220:223], v[112:115], v[152:155]\n\t" \
  "ds_read_b64_tr_b16 v[218:219], v249 offset:15104\n\t" \
  "v_rcp_f16_sdwa v224, v224 dst_sel:DWORD dst_unused:UNUSED_PAD src0_sel:WORD_1\n\t" \
  "v_mfma_f32_16x16x32_bf16 v[160:163], v[220:223], v[124:127], v[160:163]\n\t" \
  "ds_read_u16_d16_hi v197, v197 offset:0\n\t" \
  "v_lshlrev_b32_sdwa v204, 1, v204 dst_sel:DWORD dst_unused:UNUSED_PAD src0_sel:DWORD src1_sel:WORD_1\n\t" \
  "v_mfma_f32_16x16x32_bf16 v[168:171], v[220:223], v[136:139], v[168:171]\n\t" \
  "s_waitcnt lgkmcnt(9)\n\t" \
  "ds_read_b64_tr_b16 v[220:221], v249 offset:16896\n\t" \
  "ds_read_u16_d16_hi v198, v198 offset:0\n\t" \
  "v_mfma_f32_16x16x32_bf16 v[144:147], v[208:211], v[104:107], v[144:147]\n\t" \
  "ds_read_b64_tr_b16 v[222:223], v249 offset:17152\n\t" \
  "v_lshlrev_b32_sdwa v205, 1, v205 dst_sel:DWORD dst_unused:UNUSED_PAD src0_sel:DWORD src1_sel:WORD_1\n\t" \
  "v_fma_mix_f32 v188, v188, v225, 0 op_sel_hi:[0,1,0]\n\t" \
  "v_mfma_f32_16x16x32_bf16 v[152:155], v[208:211], v[116:119], v[152:155]\n\t" \
  "ds_read_u16_d16_hi v199, v199 offset:0\n\t" \
  "v_fma_mix_f32 v189, v189, v224, 0 op_sel_hi:[0,1,0]\n\t" \
  "v_mfma_f32_16x16x32_bf16 v[160:163], v[208:211], v[128:131], v[160:163]\n\t" \
  "ds_read_u16_d16_hi v204, v204 offset:0\n\t" \
  "v_lshlrev_b32_sdwa v206, 1, v206 dst_sel:DWORD dst_unused:UNUSED_PAD src0_sel:DWORD src1_sel:WORD_1\n\t" \
  "v_mfma_f32_16x16x32_bf16 v[168:171], v[208:211], v[140:143], v[168:171]\n\t" \
  "s_waitcnt lgkmcnt(10)\n\t" \
  "ds_read_b64_tr_b16 v[208:209], v250 offset:6160\n\t" \
  "v_lshlrev_b32_sdwa v207, 1, v207 dst_sel:DWORD dst_unused:UNUSED_PAD src0_sel:DWORD src1_sel:WORD_1\n\t" \
  "v_mfma_f32_16x16x32_bf16 v[148:151], v[212:215], v[96:99], 0\n\t" \
  "ds_read_u16_d16_hi v205, v205 offset:0\n\t" \
  "v_lshlrev_b32_sdwa v144, 1, v144 dst_sel:DWORD dst_unused:UNUSED_PAD src0_sel:DWORD src1_sel:WORD_1\n\t" \
  "v_mfma_f32_16x16x32_bf16 v[156:159], v[212:215], v[108:111], 0\n\t" \
  "ds_read_b64_tr_b16 v[210:211], v250 offset:6928\n\t" \
  "v_mul_f32 v152, %[kk], v152\n\t" \
  "v_mfma_f32_16x16x32_bf16 v[164:167], v[212:215], v[120:123], 0\n\t" \
  "ds_read_u16_d16_hi v206, v206 offset:0\n\t" \
  "v_lshlrev_b32_sdwa v145, 1, v145 dst_sel:DWORD dst_unused:UNUSED_PAD src0_sel:DWORD src1_sel:WORD_1\n\t" \
  "v_mul_f32 v153, %[kk], v153\n\t" \
  "v_mfma_f32_16x16x32_bf16 v[172:175], v[212:215], v[132:135], 0\n\t" \
  "s_waitcnt lgkmcnt(10)\n\t" \
  "ds_read_b64_tr_b16 v[212:213], v250 offset:6168\n\t" \
  "ds_read_u16_d16_hi v207, v207 offset:0\n\t" \
  "v_cvt_pk_f16_f32 v224, v152, v153\n\t" \
  "v_mfma_f32_16x16x32_bf16 v[148:151], v[216:219], v[100:103], v[148:151]\n\t" \
  "ds_read_b64_tr_b16 v[214:215], v250 offset:6936\n\t" \
  "v_lshlrev_b32_sdwa v146, 1, v146 dst_sel:DWORD dst_unused:UNUSED_PAD src0_sel:DWORD src1_sel:WORD_1\n\t" \
  "v_mfma_f32_16x16x32_bf16 v[156:159], v[216:219], v[112:115], v[156:159]\n\t" \
  "ds_read_u16_d16_hi v144, v144 offset:0\n\t" \
  "v_pk_fma_f16 v225, v224, v224, %[h1] neg_lo:[1,0,0] neg_hi:[1,0,0]\n\t" \
  "v_mfma_f32_16x16x32_bf16 v[164:167], v[216:219], v[124:127], v[164:167]\n\t" \
  "ds_read_u16_d16_hi v145, v145 offset:0\n\t" \
  "v_lshlrev_b32_sdwa v147, 1, v147 dst_sel:DWORD dst_unused:UNUSED_PAD src0_sel:DWORD src1_sel:WORD_1\n\t" \
  "v_pk_mul_f16 v225, v224, v225\n\t" \
  "v_mfma_f32_16x16x32_bf16 v[172:175], v[216:219], v[136:139], v[172:175]\n\t" \
  "s_waitcnt lgkmcnt(11)\n\t" \
  "ds_read_b64_tr_b16 v[216:217], v250 offset:6224\n\t" \
  "v_lshlrev_b32_sdwa v154, 1, v154 dst_sel:DWORD dst_unused:UNUSED_PAD src0_sel:DWORD src1_sel:WORD_1\n\t" \
  "v_mfma_f32_16x16x32_bf16 v[148:151], v[220:223], v[104:107], v[148:151]\n\t" \
  "ds_read_u16_d16_hi v146, v146 offset:0\n\t" \
  "v_exp_f16 v224, v225\n\t" \
  "v_mfma_f32_16x16x32_bf16 v[156:159], v[220:223], v[116:119], v[156:159]\n\t" \
  "ds_read_b64_tr_b16 v[218:219], v250 offset:6992\n\t" \
  "v_lshlrev_b32_sdwa v155, 1, v155 dst_sel:DWORD dst_unused:UNUSED_PAD src0_sel:DWORD src1_sel:WORD_1\n\t" \
  "v_mfma_f32_16x16x32_bf16 v[164:167], v[220:223], v[128:131], v[164:167]\n\t" \
  "ds_read_u16_d16_hi v147, v147 offset:0\n\t" \
  "v_lshlrev_b32_sdwa v160, 1, v160 dst_sel:DWORD dst_unused:UNUSED_PAD src0_sel:DWORD src1_sel:WORD_1\n\t" \
  "v_mfma_f32_16x16x32_bf16 v[172:175], v[220:223], v[140:143], v[172:175]\n\t" \
  "s_waitcnt lgkmcnt(10)\n\t" \
  "ds_read_b64_tr_b16 v[220:221], v250 offset:6232\n\t" \
  "ds_read_u16_d16_hi v154, v154 offset:0\n\t" \
  "v_exp_f16_sdwa v224, v225 dst_sel:WORD_1 dst_unused:UNUSED_PRESERVE src0_sel:WORD_1\n\t" \
  "v_smfmac_f32_16x16x64_bf16 v[0:3], v[208:211], v[176:183], v248\n\t" \
  "ds_read_b64_tr_b16 v[222:223], v250 offset:7000\n\t" \
  "v_lshlrev_b32_sdwa v161, 1, v161 dst_sel:DWORD dst_unused:UNUSED_PAD src0_sel:DWORD src1_sel:WORD_1\n\t" \
  "v_smfmac_f32_16x16x64_bf16 v[24:27], v[208:211], v[184:191], v248\n\t" \
  "ds_read_u16_d16_hi v155, v155 offset:0\n\t" \
  "v_pk_fma_f16 v224, v224, %[hc], %[hc]\n\t" \
  "v_smfmac_f32_16x16x64_bf16 v[48:51], v[208:211], v[192:199], v248\n\t" \
  "s_waitcnt lgkmcnt(11)\n\t" \
  "ds_read_u16_d16_hi v160, v160 offset:0\n\t" \
  "v_lshlrev_b32_sdwa v162, 1, v162 dst_sel:DWORD dst_unused:UNUSED_PAD src0_sel:DWORD src1_sel:WORD_1\n\t" \
  "v_smfmac_f32_16x16x64_bf16 v[72:75], v[208:211], v[200:207], v248\n\t" \
  "s_waitcnt lgkmcnt(11)\n\t" \
  "ds_read_b64_tr_b16 v[208:209], v250 offset:6288\n\t" \
  "v_rcp_f16 v225, v224\n\t" \
  "v_smfmac_f32_16x16x64_bf16 v[4:7], v[212:215], v[176:183], v248\n\t" \
  "ds_read_u16_d16_hi v161, v161 offset:0\n\t" \
  "v_lshlrev_b32_sdwa v163, 1, v163 dst_sel:DWORD dst_unused:UNUSED_PAD src0_sel:DWORD src1_sel:WORD_1\n\t" \
  "v_smfmac_f32_16x16x64_bf16 v[28:31], v[212:215], v[184:191], v248\n\t" \
  "ds_read_b64_tr_b16 v[210:211], v250 offset:7056\n\t" \
  "v_lshlrev_b32_sdwa v168, 1, v168 dst_sel:DWORD dst_unused:UNUSED_PAD src0_sel:DWORD src1_sel:WORD_1\n\t" \
  "v_smfmac_f32_16x16x64_bf16 v[52:55], v[212:215], v[192:199], v248\n\t" \
  "ds_read_u16_d16_hi v162, v162 offset:0\n\t" \
  "v_rcp_f16_sdwa v224, v224 dst_sel:DWORD dst_unused:UNUSED_PAD src0_sel:WORD_1\n\t" \
  "v_smfmac_f32_16x16x64_bf16 v[76:79], v[212:215], v[200:207], v248\n\t" \
  "s_waitcnt lgkmcnt(10)\n\t" \
  "ds_read_b64_tr_b16 v[212:213], v250 offset:6296\n\t" \
  "v_lshlrev_b32_sdwa v169, 1, v169 dst_sel:DWORD dst_unused:UNUSED_PAD src0_sel:DWORD src1_sel:WORD_1\n\t" \
  "v_smfmac_f32_16x16x64_bf16 v[8:11], v[216:219], v[176:183], v248\n\t" \
  "ds_read_u16_d16_hi v163, v163 offset:0\n\t" \
  "v_fma_mix_f32 v152, v152, v225, 0 op_sel_hi:[0,1,0]\n\t" \
  "v_smfmac_f32_16x16x64_bf16 v[32:35], v[216:219], v[184:191], v248\n\t" \
  "ds_read_b64_tr_b16 v[214:215], v250 offset:7064\n\t" \
  "v_lshlrev_b32_sdwa v170, 1, v170 dst_sel:DWORD dst_unused:UNUSED_PAD src0_sel:DWORD src1_sel:WORD_1\n\t" \
  "v_smfmac_f32_16x16x64_bf16 v[56:59], v[216:219], v[192:199], v248\n\t" \
  "ds_read_u16_d16_hi v168, v168 offset:0\n\t" \
  "v_fma_mix_f32 v153, v153, v224, 0 op_sel_hi:[0,1,0]\n\t" \
  "v_smfmac_f32_16x16x64_bf16 v[80:83], v[216:219], v[200:207], v248\n\t" \
  "s_waitcnt lgkmcnt(10)\n\t" \
  "s_barrier\n\t" \
  "ds_read_b64_tr_b16 v[216:217], v249 offset:0\n\t" \
  "ds_read_b128 v[108:111], v251 offset:0\n\t" \
  "v_lshlrev_b32_sdwa v171, 1, v171 dst_sel:DWORD dst_unused:UNUSED_PAD src0_sel:DWORD src1_sel:WORD_1\n\t" \
  "v_smfmac_f32_16x16x64_bf16 v[12:15], v[220:223], v[176:183], v248\n\t" \
  "ds_read_b64_tr_b16 v[218:219], v249 offset:256\n\t" \
  "ds_read_b128 v[112:115], v251 offset:4096\n\t" \
  "v_mul_f32 v156, %[kk], v156\n\t" \
  "v_smfmac_f32_16x16x64_bf16 v[36:39], v[220:223], v[184:191], v248\n\t" \
  "ds_read_b128 v[116:119], v251 offset:8192\n\t" \
  "v_lshlrev_b32_sdwa v148, 1, v148 dst_sel:DWORD dst_unused:UNUSED_PAD src0_sel:DWORD src1_sel:WORD_1\n\t" \
  "v_mul_f32 v157, %[kk], v157\n\t" \
  "v_smfmac_f32_16x16x64_bf16 v[60:63], v[220:223], v[192:199], v248\n\t" \
  "ds_read_u16_d16_hi v169, v169 offset:0\n\t" \
  "v_cvt_pk_f16_f32 v224, v156, v157\n\t" \
  "v_smfmac_f32_16x16x64_bf16 v[84:87], v[220:223], v[200:207], v248\n\t" \
  "s_waitcnt lgkmcnt(11)\n\t" \
  "ds_read_b64_tr_b16 v[220:221], v249 offset:2048\n\t" \
  "ds_read_u16_d16_hi v170, v170 offset:0\n\t" \
  "v_lshlrev_b32_sdwa v149, 1, v149 dst_sel:DWORD dst_unused:UNUSED_PAD src0_sel:DWORD src1_sel:WORD_1\n\t" \
  "v_smfmac_f32_16x16x64_bf16 v[16:19], v[208:211], v[176:183], v248\n\t" \
  "ds_read_b64_tr_b16 v[222:223], v249 offset:2304\n\t" \
  "v_pk_fma_f16 v225, v224, v224, %[h1] neg_lo:[1,0,0] neg_hi:[1,0,0]\n\t" \
  "v_smfmac_f32_16x16x64_bf16 v[40:43], v[208:211], v[184:191], v248\n\t" \
  "ds_read_u16_d16_hi v171, v171 offset:0\n\t" \
  "v_lshlrev_b32_sdwa v150, 1, v150 dst_sel:DWORD dst_unused:UNUSED_PAD src0_sel:DWORD src1_sel:WORD_1\n\t" \
  "v_pk_mul_f16 v225, v224, v225\n\t" \
  "v_smfmac_f32_16x16x64_bf16 v[64:67], v[208:211], v[192:199], v248\n\t" \
  "ds_read_u16_d16_hi v148, v148 offset:0\n\t" \
  "v_lshlrev_b32_sdwa v151, 1, v151 dst_sel:DWORD dst_unused:UNUSED_PAD src0_sel:DWORD src1_sel:WORD_1\n\t" \
  "v_smfmac_f32_16x16x64_bf16 v[88:91], v[208:211], v[200:207], v248\n\t" \
  "s_waitcnt lgkmcnt(12)\n\t" \
  "ds_read_b64_tr_b16 v[208:209], v249 offset:4096\n\t" \
  "ds_read_u16_d16_hi v149, v149 offset:0\n\t" \
  "v_exp_f16 v224, v225\n\t" \
  "v_smfmac_f32_16x16x64_bf16 v[20:23], v[212:215], v[176:183], v248\n\t" \
  "ds_read_b64_tr_b16 v[210:211], v249 offset:4352\n\t" \
  "v_lshlrev_b32_sdwa v158, 1, v158 dst_sel:DWORD dst_unused:UNUSED_PAD src0_sel:DWORD src1_sel:WORD_1\n\t" \
  "v_smfmac_f32_16x16x64_bf16 v[44:47], v[212:215], v[184:191], v248\n\t" \
  "ds_read_u16_d16_hi v150, v150 offset:0\n\t" \
  "v_lshlrev_b32_sdwa v159, 1, v159 dst_sel:DWORD dst_unused:UNUSED_PAD src0_sel:DWORD src1_sel:WORD_1\n\t" \
  "v_smfmac_f32_16x16x64_bf16 v[68:71], v[212:215], v[192:199], v248\n\t" \
  "ds_read_u16_d16_hi v151, v151 offset:0\n\t" \
  "v_exp_f16_sdwa v224, v225 dst_sel:WORD_1 dst_unused:UNUSED_PRESERVE src0_sel:WORD_1\n\t" \
  "v_smfmac_f32_16x16x64_bf16 v[92:95], v[212:215], v[200:207], v248\n\t" \
  "s_waitcnt lgkmcnt(13)\n\t" \
  "ds_read_b64_tr_b16 v[212:213], v249 offset:512\n\t" \
  "v_lshlrev_b32_sdwa v164, 1, v164 dst_sel:DWORD dst_unused:UNUSED_PAD src0_sel:DWORD src1_sel:WORD_1\n\t" \
  "v_mfma_f32_16x16x32_bf16 v[176:179], v[216:219], v[96:99], 0\n\t" \
  "ds_read_u16_d16_hi v158, v158 offset:0\n\t" \
  "v_pk_fma_f16 v224, v224, %[hc], %[hc]\n\t" \
  "v_mfma_f32_16x16x32_bf16 v[184:187], v[216:219], v[108:111], 0\n\t" \
  "ds_read_b64_tr_b16 v[214:215], v249 offset:768\n\t" \
  "v_lshlrev_b32_sdwa v165, 1, v165 dst_sel:DWORD dst_unused:UNUSED_PAD src0_sel:DWORD src1_sel:WORD_1\n\t" \
  "v_mfma_f32_16x16x32_bf16 v[192:195], v[216:219], v[120:123], 0\n\t" \
  "ds_read_u16_d16_hi v159, v159 offset:0\n\t" \
  "v_rcp_f16 v225, v224\n\t" \
  "v_mfma_f32_16x16x32_bf16 v[200:203], v[216:219], v[132:135], 0\n\t" \
  "s_waitcnt lgkmcnt(11)\n\t" \
  "ds_read_b64_tr_b16 v[216:217], v249 offset:2560\n\t" \
  "v_lshlrev_b32_sdwa v166, 1, v166 dst_sel:DWORD dst_unused:UNUSED_PAD src0_sel:DWORD src1_sel:WORD_1\n\t" \
  "v_mfma_f32_16x16x32_bf16 v[176:179], v[220:223], v[100:103], v[176:179]\n\t" \
  "ds_read_u16_d16_hi v164, v164 offset:0\n\t" \
  "v_lshlrev_b32_sdwa v167, 1, v167 dst_sel:DWORD dst_unused:UNUSED_PAD src0_sel:DWORD src1_sel:WORD_1\n\t" \
  "v_mfma_f32_16x16x32_bf16 v[184:187], v[220:223], v[112:115], v[184:187]\n\t" \
  "ds_read_b64_tr_b16 v[218:219], v249 offset:2816\n\t" \
  "v_rcp_f16_sdwa v224, v224 dst_sel:DWORD dst_unused:UNUSED_PAD src0_sel:WORD_1\n\t" \
  "v_mfma_f32_16x16x32_bf16 v[192:195], v[220:223], v[124:127], v[192:195]\n\t" \
  "ds_read_u16_d16_hi v165, v165 offset:0\n\t" \
  "v_lshlrev_b32_sdwa v172, 1, v172 dst_sel:DWORD dst_unused:UNUSED_PAD src0_sel:DWORD src1_sel:WORD_1\n\t" \
  "v_mfma_f32_16x16x32_bf16 v[200:203], v[220:223], v[136:139], v[200:203]\n\t" \
  "s_waitcnt lgkmcnt(10)\n\t" \
  "ds_read_b64_tr_b16 v[220:221], v249 offset:4608\n\t" \
  "ds_read_u16_d16_hi v166, v166 offset:0\n\t" \
  "v_mfma_f32_16x16x32_bf16 v[176:179], v[208:211], v[104:107], v[176:179]\n\t" \
  "ds_read_b64_tr_b16 v[222:223], v249 offset:4864\n\t" \
  "v_lshlrev_b32_sdwa v173, 1, v173 dst_sel:DWORD dst_unused:UNUSED_PAD src0_sel:DWORD src1_sel:WORD_1\n\t" \
  "v_fma_mix_f32 v156, v156, v225, 0 op_sel_hi:[0,1,0]\n\t" \
  "v_mfma_f32_16x16x32_bf16 v[184:187], v[208:211], v[116:119], v[184:187]\n\t" \
  "ds_read_u16_d16_hi v167, v167 offset:0\n\t" \
  "v_fma_mix_f32 v157, v157, v224, 0 op_sel_hi:[0,1,0]\n\t" \
  "v_mfma_f32_16x16x32_bf16 v[192:195], v[208:211], v[128:131], v[192:195]\n\t" \
  "ds_read_u16_d16_hi v172, v172 offset:0\n\t" \
  "v_lshlrev_b32_sdwa v174, 1, v174 dst_sel:DWORD dst_unused:UNUSED_PAD src0_sel:DWORD src1_sel:WORD_1\n\t" \
  "v_mfma_f32_16x16x32_bf16 v[200:203], v[208:211], v[140:143], v[200:203]\n\t" \
  "s_waitcnt lgkmcnt(10)\n\t" \
  "ds_read_b64_tr_b16 v[208:209], v250 offset:12320\n\t" \
  "v_lshlrev_b32_sdwa v175, 1, v175 dst_sel:DWORD dst_unused:UNUSED_PAD src0_sel:DWORD src1_sel:WORD_1\n\t" \
  "v_mfma_f32_16x16x32_bf16 v[180:183], v[212:215], v[96:99], 0\n\t" \
  "ds_read_u16_d16_hi v173, v173 offset:0\n\t" \
  "v_lshlrev_b32_sdwa v176, 1, v176 dst_sel:DWORD dst_unused:UNUSED_PAD src0_sel:DWORD src1_sel:WORD_1\n\t" \
  "v_mfma_f32_16x16x32_bf16 v[188:191], v[212:215], v[108:111], 0\n\t" \
  "ds_read_b64_tr_b16 v[210:211], v250 offset:13088\n\t" \
  "v_mul_f32 v184, %[kk], v184\n\t" \
  "v_mfma_f32_16x16x32_bf16 v[196:199], v[212:215], v[120:123], 0\n\t" \
  "ds_read_u16_d16_hi v174, v174 offset:0\n\t" \
  "v_lshlrev_b32_sdwa v177, 1, v177 dst_sel:DWORD dst_unused:UNUSED_PAD src0_sel:DWORD src1_sel:WORD_1\n\t" \
  "v_mul_f32 v185, %[kk], v185\n\t" \
  "v_mfma_f32_16x16x32_bf16 v[204:207], v[212:215], v[132:135], 0\n\t" \
  "s_waitcnt lgkmcnt(10)\n\t" \
  "ds_read_b64_tr_b16 v[212:213], v250 offset:12328\n\t" \
  "ds_read_u16_d16_hi v175, v175 offset:0\n\t" \
  "v_cvt_pk_f16_f32 v224, v184, v185\n\t" \
  "v_mfma_f32_16x16x32_bf16 v[180:183], v[216:219], v[100:103], v[180:183]\n\t" \
  "ds_read_b64_tr_b16 v[214:215], v250 offset:13096\n\t" \
  "v_lshlrev_b32_sdwa v178, 1, v178 dst_sel:DWORD dst_unused:UNUSED_PAD src0_sel:DWORD src1_sel:WORD_1\n\t" \
  "v_mfma_f32_16x16x32_bf16 v[188:191], v[216:219], v[112:115], v[188:191]\n\t" \
  "ds_read_u16_d16_hi v176, v176 offset:0\n\t" \
  "v_pk_fma_f16 v225, v224, v224, %[h1] neg_lo:[1,0,0] neg_hi:[1,0,0]\n\t" \
  "v_mfma_f32_16x16x32_bf16 v[196:199], v[216:219], v[124:127], v[196:199]\n\t" \
  "ds_read_u16_d16_hi v177, v177 offset:0\n\t" \
  "v_lshlrev_b32_sdwa v179, 1, v179 dst_sel:DWORD dst_unused:UNUSED_PAD src0_sel:DWORD src1_sel:WORD_1\n\t" \
  "v_pk_mul_f16 v225, v224, v225\n\t" \
  "v_mfma_f32_16x16x32_bf16 v[204:207], v[216:219], v[136:139], v[204:207]\n\t" \
  "s_waitcnt lgkmcnt(11)\n\t" \
  "ds_read_b64_tr_b16 v[216:217], v250 offset:12384\n\t" \
  "v_lshlrev_b32_sdwa v186, 1, v186 dst_sel:DWORD dst_unused:UNUSED_PAD src0_sel:DWORD src1_sel:WORD_1\n\t" \
  "v_mfma_f32_16x16x32_bf16 v[180:183], v[220:223], v[104:107], v[180:183]\n\t" \
  "ds_read_u16_d16_hi v178, v178 offset:0\n\t" \
  "v_exp_f16 v224, v225\n\t" \
  "v_mfma_f32_16x16x32_bf16 v[188:191], v[220:223], v[116:119], v[188:191]\n\t" \
  "ds_read_b64_tr_b16 v[218:219], v250 offset:13152\n\t" \
  "v_lshlrev_b32_sdwa v187, 1, v187 dst_sel:DWORD dst_unused:UNUSED_PAD src0_sel:DWORD src1_sel:WORD_1\n\t" \
  "v_mfma_f32_16x16x32_bf16 v[196:199], v[220:223], v[128:131], v[196:199]\n\t" \
  "ds_read_u16_d16_hi v179, v179 offset:0\n\t" \
  "v_lshlrev_b32_sdwa v192, 1, v192 dst_sel:DWORD dst_unused:UNUSED_PAD src0_sel:DWORD src1_sel:WORD_1\n\t" \
  "v_mfma_f32_16x16x32_bf16 v[204:207], v[220:223], v[140:143], v[204:207]\n\t" \
  "s_waitcnt lgkmcnt(10)\n\t" \
  "ds_read_b64_tr_b16 v[220:221], v250 offset:12392\n\t" \
  "ds_read_u16_d16_hi v186, v186 offset:0\n\t" \
  "v_exp_f16_sdwa v224, v225 dst_sel:WORD_1 dst_unused:UNUSED_PRESERVE src0_sel:WORD_1\n\t" \
  "v_smfmac_f32_16x16x64_bf16 v[0:3], v[208:211], v[144:151], v248\n\t" \
  "ds_read_b64_tr_b16 v[222:223], v250 offset:13160\n\t" \
  "v_lshlrev_b32_sdwa v193, 1, v193 dst_sel:DWORD dst_unused:UNUSED_PAD src0_sel:DWORD src1_sel:WORD_1\n\t" \
  "v_smfmac_f32_16x16x64_bf16 v[24:27], v[208:211], v[152:159], v248\n\t" \
  "ds_read_u16_d16_hi v187, v187 offset:0\n\t" \
  "v_pk_fma_f16 v224, v224, %[hc], %[hc]\n\t" \
  "v_smfmac_f32_16x16x64_bf16 v[48:51], v[208:211], v[160:167], v248\n\t" \
  "s_waitcnt lgkmcnt(11)\n\t" \
  "ds_read_u16_d16_hi v192, v192 offset:0\n\t" \
  "v_lshlrev_b32_sdwa v194, 1, v194 dst_sel:DWORD dst_unused:UNUSED_PAD src0_sel:DWORD src1_sel:WORD_1\n\t" \
  "v_smfmac_f32_16x16x64_bf16 v[72:75], v[208:211], v[168:175], v248\n\t" \
  "s_waitcnt lgkmcnt(11)\n\t" \
  "ds_read_b64_tr_b16 v[208:209], v250 offset:12448\n\t" \
  "v_rcp_f16 v225, v224\n\t" \
  "v_smfmac_f32_16x16x64_bf16 v[4:7], v[212:215], v[144:151], v248\n\t" \
  "ds_read_b64 v[242:243], v252 offset:128\n\t" \
  "v_lshlrev_b32_sdwa v195, 1, v195 dst_sel:DWORD dst_unused:UNUSED_PAD src0_sel:DWORD src1_sel:WORD_1\n\t" \
  "v_smfmac_f32_16x16x64_bf16 v[28:31], v[212:215], v[152:159], v248\n\t" \
  "ds_read_b64_tr_b16 v[210:211], v250 offset:13216\n\t" \
  "ds_read_b64 v[244:245], v252 offset:136\n\t" \
  "v_smfmac_f32_16x16x64_bf16 v[52:55], v[212:215], v[160:167], v248\n\t" \
  "ds_read_u16_d16_hi v193, v193 offset:0\n\t" \
  "v_lshlrev_b32_sdwa v200, 1, v200 dst_sel:DWORD dst_unused:UNUSED_PAD src0_sel:DWORD src1_sel:WORD_1\n\t" \
  "v_rcp_f16_sdwa v224, v224 dst_sel:DWORD dst_unused:UNUSED_PAD src0_sel:WORD_1\n\t" \
  "v_smfmac_f32_16x16x64_bf16 v[76:79], v[212:215], v[168:175], v248\n\t" \
  "s_waitcnt lgkmcnt(11)\n\t" \
  "ds_read_b64_tr_b16 v[212:213], v250 offset:12456\n\t" \
  "ds_read_u16_d16_hi v194, v194 offset:0\n\t" \
  "v_cvt_pk_bf16_f32 v230, v24, v25\n\t" \
  "v_smfmac_f32_16x16x64_bf16 v[8:11], v[216:219], v[144:151], v248\n\t" \
  "ds_read_b64_tr_b16 v[214:215], v250 offset:13224\n\t" \
  "v_lshlrev_b32_sdwa v201, 1, v201 dst_sel:DWORD dst_unused:UNUSED_PAD src0_sel:DWORD src1_sel:WORD_1\n\t" \
  "v_cvt_pk_bf16_f32 v231, v26, v27\n\t" \
  "v_smfmac_f32_16x16x64_bf16 v[32:35], v[216:219], v[152:159], v248\n\t" \
  "ds_read_u16_d16_hi v195, v195 offset:0\n\t" \
  "v_fma_mix_f32 v184, v184, v225, 0 op_sel_hi:[0,1,0]\n\t" \
  "v_cvt_pk_bf16_f32 v232, v28, v29\n\t" \
  "v_smfmac_f32_16x16x64_bf16 v[56:59], v[216:219], v[160:167], v248\n\t" \
  "ds_read_u16_d16_hi v200, v200 offset:0\n\t" \
  "v_lshlrev_b32_sdwa v202, 1, v202 dst_sel:DWORD dst_unused:UNUSED_PAD src0_sel:DWORD src1_sel:WORD_1\n\t" \
  "v_fma_mix_f32 v185, v185, v224, 0 op_sel_hi:[0,1,0]\n\t" \
  "v_cvt_pk_bf16_f32 v233, v30, v31\n\t" \
  "v_smfmac_f32_16x16x64_bf16 v[80:83], v[216:219], v[168:175], v248\n\t" \
  "s_waitcnt lgkmcnt(12)\n\t" \
  "s_barrier\n\t" \
  "ds_read_b64_tr_b16 v[216:217], v249 offset:6144\n\t" \
  "buffer_store_dwordx4 v[230:233], v253, %[osrd], %[ob] offen offset:0 sc1\n\t" \
  "v_lshlrev_b32_sdwa v203, 1, v203 dst_sel:DWORD dst_unused:UNUSED_PAD src0_sel:DWORD src1_sel:WORD_1\n\t" \
  "v_smfmac_f32_16x16x64_bf16 v[12:15], v[220:223], v[144:151], v248\n\t" \
  "ds_read_b64_tr_b16 v[218:219], v249 offset:6400\n\t" \
  "ds_read_b64 v[230:231], v252 offset:0\n\t" \
  "v_mul_f32 v188, %[kk], v188\n\t" \
  "v_smfmac_f32_16x16x64_bf16 v[36:39], v[220:223], v[152:159], v248\n\t" \
  "ds_read_b64 v[232:233], v252 offset:8\n\t" \
  "v_lshlrev_b32_sdwa v180, 1, v180 dst_sel:DWORD dst_unused:UNUSED_PAD src0_sel:DWORD src1_sel:WORD_1\n\t" \
  "v_mul_f32 v189, %[kk], v189\n\t" \
  "v_smfmac_f32_16x16x64_bf16 v[60:63], v[220:223], v[160:167], v248\n\t" \
  "ds_read_u16_d16_hi v201, v201 offset:0\n\t" \
  "v_cvt_pk_f16_f32 v224, v188, v189\n\t" \
  "v_smfmac_f32_16x16x64_bf16 v[84:87], v[220:223], v[168:175], v248\n\t" \
  "s_waitcnt lgkmcnt(12)\n\t" \
  "ds_read_b64_tr_b16 v[220:221], v249 offset:8192\n\t" \
  "ds_read_u16_d16_hi v202, v202 offset:0\n\t" \
  "v_lshlrev_b32_sdwa v181, 1, v181 dst_sel:DWORD dst_unused:UNUSED_PAD src0_sel:DWORD src1_sel:WORD_1\n\t" \
  "v_cvt_pk_bf16_f32 v234, v32, v33\n\t" \
  "v_smfmac_f32_16x16x64_bf16 v[16:19], v[208:211], v[144:151], v248\n\t" \
  "ds_read_b64_tr_b16 v[222:223], v249 offset:8448\n\t" \
  "v_pk_fma_f16 v225, v224, v224, %[h1] neg_lo:[1,0,0] neg_hi:[1,0,0]\n\t" \
  "v_cvt_pk_bf16_f32 v235, v34, v35\n\t" \
  "v_smfmac_f32_16x16x64_bf16 v[40:43], v[208:211], v[152:159], v248\n\t" \
  "ds_read_u16_d16_hi v203, v203 offset:0\n\t" \
  "v_lshlrev_b32_sdwa v182, 1, v182 dst_sel:DWORD dst_unused:UNUSED_PAD src0_sel:DWORD src1_sel:WORD_1\n\t" \
  "v_pk_mul_f16 v225, v224, v225\n\t" \
  "v_cvt_pk_bf16_f32 v236, v36, v37\n\t" \
  "v_smfmac_f32_16x16x64_bf16 v[64:67], v[208:211], v[160:167], v248\n\t" \
  "ds_read_u16_d16_hi v180, v180 offset:0\n\t" \
  "v_lshlrev_b32_sdwa v183, 1, v183 dst_sel:DWORD dst_unused:UNUSED_PAD src0_sel:DWORD src1_sel:WORD_1\n\t" \
  "v_cvt_pk_bf16_f32 v237, v38, v39\n\t" \
  "v_smfmac_f32_16x16x64_bf16 v[88:91], v[208:211], v[168:175], v248\n\t" \
  "s_waitcnt lgkmcnt(12)\n\t" \
  "ds_read_b64_tr_b16 v[208:209], v249 offset:10240\n\t" \
  "buffer_store_dwordx4 v[234:237], v253, %[osrd], %[ob] offen offset:64 sc1\n\t" \
  "v_exp_f16 v224, v225\n\t" \
  "v_smfmac_f32_16x16x64_bf16 v[20:23], v[212:215], v[144:151], v248\n\t" \
  "ds_read_b64_tr_b16 v[210:211], v249 offset:10496\n\t" \
  "ds_read_b64 v[234:235], v252 offset:64\n\t" \
  "v_lshlrev_b32_sdwa v190, 1, v190 dst_sel:DWORD dst_unused:UNUSED_PAD src0_sel:DWORD src1_sel:WORD_1\n\t" \
  "v_smfmac_f32_16x16x64_bf16 v[44:47], v[212:215], v[152:159], v248\n\t" \
  "s_waitcnt lgkmcnt(10)\n\t" \
  "ds_read_b64 v[236:237], v252 offset:72\n\t" \
  "ds_read_u16_d16_hi v181, v181 offset:0\n\t" \
  "v_mfma_f32_16x16x16_bf16 v[24:27], v[246:247], v[230:231], 0\n\t" \
  "ds_read_u16_d16_hi v182, v182 offset:0\n\t" \
  "v_lshlrev_b32_sdwa v191, 1, v191 dst_sel:DWORD dst_unused:UNUSED_PAD src0_sel:DWORD src1_sel:WORD_1\n\t" \
  "v_smfmac_f32_16x16x64_bf16 v[68:71], v[212:215], v[160:167], v248\n\t" \
  "ds_read_u16_d16_hi v183, v183 offset:0\n\t" \
  "v_exp_f16_sdwa v224, v225 dst_sel:WORD_1 dst_unused:UNUSED_PRESERVE src0_sel:WORD_1\n\t" \
  "v_smfmac_f32_16x16x64_bf16 v[92:95], v[212:215], v[168:175], v248\n\t" \
  "ds_read_b64_tr_b16 v[212:213], v249 offset:6656\n\t" \
  "v_cvt_pk_bf16_f32 v238, v40, v41\n\t" \
  "v_mfma_f32_16x16x32_bf16 v[144:147], v[216:219], v[96:99], 0\n\t" \
  "s_waitcnt lgkmcnt(14)\n\t" \
  "ds_read_u16_d16_hi v190, v190 offset:0\n\t" \
  "v_lshlrev_b32_sdwa v196, 1, v196 dst_sel:DWORD dst_unused:UNUSED_PAD src0_sel:DWORD src1_sel:WORD_1\n\t" \
  "v_mfma_f32_16x16x16_bf16 v[28:31], v[246:247], v[232:233], 0\n\t" \
  "ds_read_b64_tr_b16 v[214:215], v249 offset:6912\n\t" \
  "v_pk_fma_f16 v224, v224, %[hc], %[hc]\n\t" \
  "v_cvt_pk_bf16_f32 v239, v42, v43\n\t" \
  "v_mfma_f32_16x16x32_bf16 v[152:155], v[216:219], v[108:111], 0\n\t" \
  "ds_read_u16_d16_hi v191, v191 offset:0\n\t" \
  "v_lshlrev_b32_sdwa v197, 1, v197 dst_sel:DWORD dst_unused:UNUSED_PAD src0_sel:DWORD src1_sel:WORD_1\n\t" \
  "v_cvt_pk_bf16_f32 v240, v44, v45\n\t" \
  "v_mfma_f32_16x16x32_bf16 v[160:163], v[216:219], v[120:123], 0\n\t" \
  "ds_read_u16_d16_hi v196, v196 offset:0\n\t" \
  "v_rcp_f16 v225, v224\n\t" \
  "v_cvt_pk_bf16_f32 v241, v46, v47\n\t" \
  "v_mfma_f32_16x16x32_bf16 v[168:171], v[216:219], v[132:135], 0\n\t" \
  "s_waitcnt lgkmcnt(14)\n\t" \
  "ds_read_b64_tr_b16 v[216:217], v249 offset:8704\n\t" \
  "v_lshlrev_b32_sdwa v198, 1, v198 dst_sel:DWORD dst_unused:UNUSED_PAD src0_sel:DWORD src1_sel:WORD_1\n\t" \
  "v_mfma_f32_16x16x32_bf16 v[144:147], v[220:223], v[100:103], v[144:147]\n\t" \
  "buffer_store_dwordx4 v[238:241], v253, %[osrd], %[ob] offen offset:128 sc1\n\t" \
  "v_lshlrev_b32_sdwa v199, 1, v199 dst_sel:DWORD dst_unused:UNUSED_PAD src0_sel:DWORD src1_sel:WORD_1\n\t" \
  "v_mfma_f32_16x16x32_bf16 v[152:155], v[220:223], v[112:115], v[152:155]\n\t" \
  "s_waitcnt lgkmcnt(10)\n\t" \
  "ds_read_b64_tr_b16 v[218:219], v249 offset:8960\n\t" \
  "v_rcp_f16_sdwa v224, v224 dst_sel:DWORD dst_unused:UNUSED_PAD src0_sel:WORD_1\n\t" \
  "v_mfma_f32_16x16x16_bf16 v[32:35], v[246:247], v[234:235], 0\n\t" \
  "ds_read_u16_d16_hi v197, v197 offset:0\n\t" \
  "v_lshlrev_b32_sdwa v204, 1, v204 dst_sel:DWORD dst_unused:UNUSED_PAD src0_sel:DWORD src1_sel:WORD_1\n\t" \
  "v_mfma_f32_16x16x32_bf16 v[160:163], v[220:223], v[124:127], v[160:163]\n\t" \
  "ds_read_u16_d16_hi v198, v198 offset:0\n\t" \
  "v_lshlrev_b32_sdwa v205, 1, v205 dst_sel:DWORD dst_unused:UNUSED_PAD src0_sel:DWORD src1_sel:WORD_1\n\t" \
  "v_mfma_f32_16x16x32_bf16 v[168:171], v[220:223], v[136:139], v[168:171]\n\t" \
  "ds_read_b64_tr_b16 v[220:221], v249 offset:10752\n\t" \
  "ds_read_u16_d16_hi v199, v199 offset:0\n\t" \
  "v_mfma_f32_16x16x32_bf16 v[144:147], v[208:211], v[104:107], v[144:147]\n\t" \
  "s_waitcnt lgkmcnt(14)\n\t" \
  "ds_read_b64_tr_b16 v[222:223], v249 offset:11008\n\t" \
  "v_fma_mix_f32 v188, v188, v225, 0 op_sel_hi:[0,1,0]\n\t" \
  "v_mfma_f32_16x16x16_bf16 v[36:39], v[246:247], v[236:237], 0\n\t" \
  "ds_read_u16_d16_hi v204, v204 offset:0\n\t" \
  "v_fma_mix_f32 v189, v189, v224, 0 op_sel_hi:[0,1,0]\n\t" \
  "v_mfma_f32_16x16x32_bf16 v[152:155], v[208:211], v[116:119], v[152:155]\n\t" \
  "ds_read_u16_d16_hi v205, v205 offset:0\n\t" \
  "v_lshlrev_b32_sdwa v206, 1, v206 dst_sel:DWORD dst_unused:UNUSED_PAD src0_sel:DWORD src1_sel:WORD_1\n\t" \
  "v_mfma_f32_16x16x32_bf16 v[160:163], v[208:211], v[128:131], v[160:163]\n\t" \
  "ds_read_u16_d16_hi v206, v206 offset:0\n\t" \
  "v_lshlrev_b32_sdwa v207, 1, v207 dst_sel:DWORD dst_unused:UNUSED_PAD src0_sel:DWORD src1_sel:WORD_1\n\t" \
  "v_mfma_f32_16x16x32_bf16 v[168:171], v[208:211], v[140:143], v[168:171]\n\t" \
  "s_waitcnt lgkmcnt(12)\n\t" \
  "ds_read_b64_tr_b16 v[208:209], v250 offset:18480\n\t" \
  "v_lshlrev_b32_sdwa v144, 1, v144 dst_sel:DWORD dst_unused:UNUSED_PAD src0_sel:DWORD src1_sel:WORD_1\n\t" \
  "v_mfma_f32_16x16x32_bf16 v[148:151], v[212:215], v[96:99], 0\n\t" \
  "ds_read_u16_d16_hi v207, v207 offset:0\n\t" \
  "v_mul_f32 v152, %[kk], v152\n\t" \
  "v_mfma_f32_16x16x32_bf16 v[156:159], v[212:215], v[108:111], 0\n\t" \
  "ds_read_b64_tr_b16 v[210:211], v250 offset:19248\n\t" \
  "v_lshlrev_b32_sdwa v145, 1, v145 dst_sel:DWORD dst_unused:UNUSED_PAD src0_sel:DWORD src1_sel:WORD_1\n\t" \
  "v_mfma_f32_16x16x16_bf16 v[40:43], v[246:247], v[242:243], 0\n\t" \
  "ds_read_u16_d16_hi v144, v144 offset:0\n\t" \
  "v_mul_f32 v153, %[kk], v153\n\t" \
  "v_mfma_f32_16x16x32_bf16 v[164:167], v[212:215], v[120:123], 0\n\t" \
  "ds_read_u16_d16_hi v145, v145 offset:0\n\t" \
  "v_cvt_pk_f16_f32 v224, v152, v153\n\t" \
  "v_mfma_f32_16x16x32_bf16 v[172:175], v[212:215], v[132:135], 0\n\t" \
  "s_waitcnt lgkmcnt(13)\n\t" \
  "ds_read_b64_tr_b16 v[212:213], v250 offset:18488\n\t" \
  "v_lshlrev_b32_sdwa v146, 1, v146 dst_sel:DWORD dst_unused:UNUSED_PAD src0_sel:DWORD src1_sel:WORD_1\n\t" \
  "v_mfma_f32_16x16x32_bf16 v[148:151], v[216:219], v[100:103], v[148:151]\n\t" \
  "ds_read_u16_d16_hi v146, v146 offset:0\n\t" \
  "v_pk_fma_f16 v225, v224, v224, %[h1] neg_lo:[1,0,0] neg_hi:[1,0,0]\n\t" \
  "v_mfma_f32_16x16x16_bf16 v[44:47], v[246:247], v[244:245], 0\n\t" \
  "ds_read_b64_tr_b16 v[214:215], v250 offset:19256\n\t" \
  "v_lshlrev_b32_sdwa v147, 1, v147 dst_sel:DWORD dst_unused:UNUSED_PAD src0_sel:DWORD src1_sel:WORD_1\n\t" \
  "v_mfma_f32_16x16x32_bf16 v[156:159], v[216:219], v[112:115], v[156:159]\n\t" \
  "ds_read_u16_d16_hi v147, v147 offset:0\n\t" \
  "v_pk_mul_f16 v225, v224, v225\n\t" \
  "v_mfma_f32_16x16x32_bf16 v[164:167], v[216:219], v[124:127], v[164:167]\n\t" \
  "v_lshlrev_b32_sdwa v154, 1, v154 dst_sel:DWORD dst_unused:UNUSED_PAD src0_sel:DWORD src1_sel:WORD_1\n\t" \
  "v_exp_f16 v224, v225\n\t" \
  "v_mfma_f32_16x16x32_bf16 v[172:175], v[216:219], v[136:139], v[172:175]\n\t" \
  "s_waitcnt lgkmcnt(12)\n\t" \
  "ds_read_b64_tr_b16 v[216:217], v250 offset:18544\n\t" \
  "v_lshlrev_b32_sdwa v155, 1, v155 dst_sel:DWORD dst_unused:UNUSED_PAD src0_sel:DWORD src1_sel:WORD_1\n\t" \
  "v_mfma_f32_16x16x32_bf16 v[148:151], v[220:223], v[104:107], v[148:151]\n\t" \
  "ds_read_u16_d16_hi v154, v154 offset:0\n\t" \
  "v_lshlrev_b32_sdwa v160, 1, v160 dst_sel:DWORD dst_unused:UNUSED_PAD src0_sel:DWORD src1_sel:WORD_1\n\t" \
  "v_mfma_f32_16x16x32_bf16 v[156:159], v[220:223], v[116:119], v[156:159]\n\t" \
  "ds_read_b64_tr_b16 v[218:219], v250 offset:19312\n\t" \
  "v_exp_f16_sdwa v224, v225 dst_sel:WORD_1 dst_unused:UNUSED_PRESERVE src0_sel:WORD_1\n\t" \
  "v_mfma_f32_16x16x32_bf16 v[164:167], v[220:223], v[128:131], v[164:167]\n\t" \
  "ds_read_u16_d16_hi v155, v155 offset:0\n\t" \
  "v_lshlrev_b32_sdwa v161, 1, v161 dst_sel:DWORD dst_unused:UNUSED_PAD src0_sel:DWORD src1_sel:WORD_1\n\t" \
  "v_mfma_f32_16x16x32_bf16 v[172:175], v[220:223], v[140:143], v[172:175]\n\t" \
  "s_waitcnt lgkmcnt(10)\n\t" \
  "ds_read_b64_tr_b16 v[220:221], v250 offset:18552\n\t" \
  "v_pk_fma_f16 v224, v224, %[hc], %[hc]\n\t" \
  "v_smfmac_f32_16x16x64_bf16 v[0:3], v[208:211], v[176:183], v248\n\t" \
  "ds_read_u16_d16_hi v160, v160 offset:0\n\t" \
  "v_lshlrev_b32_sdwa v162, 1, v162 dst_sel:DWORD dst_unused:UNUSED_PAD src0_sel:DWORD src1_sel:WORD_1\n\t" \
  "v_smfmac_f32_16x16x64_bf16 v[24:27], v[208:211], v[184:191], v248\n\t" \
  "ds_read_b64_tr_b16 v[222:223], v250 offset:19320\n\t" \
  "v_rcp_f16 v225, v224\n\t" \
  "v_smfmac_f32_16x16x64_bf16 v[48:51], v[208:211], v[192:199], v248\n\t" \
  "ds_read_u16_d16_hi v161, v161 offset:0\n\t" \
  "v_lshlrev_b32_sdwa v163, 1, v163 dst_sel:DWORD dst_unused:UNUSED_PAD src0_sel:DWORD src1_sel:WORD_1\n\t" \
  "v_smfmac_f32_16x16x64_bf16 v[72:75], v[208:211], v[200:207], v248\n\t" \
  "s_waitcnt lgkmcnt(9)\n\t" \
  "ds_read_b64_tr_b16 v[208:209], v250 offset:18608\n\t" \
  "v_lshlrev_b32_sdwa v168, 1, v168 dst_sel:DWORD dst_unused:UNUSED_PAD src0_sel:DWORD src1_sel:WORD_1\n\t" \
  "v_smfmac_f32_16x16x64_bf16 v[4:7], v[212:215], v[176:183], v248\n\t" \
  "ds_read_u16_d16_hi v162, v162 offset:0\n\t" \
  "v_rcp_f16_sdwa v224, v224 dst_sel:DWORD dst_unused:UNUSED_PAD src0_sel:WORD_1\n\t" \
  "v_smfmac_f32_16x16x64_bf16 v[28:31], v[212:215], v[184:191], v248\n\t" \
  "s_add_u32 %[ob], %[ob], %[dob]\n\t" \
  "ds_read_b64_tr_b16 v[210:211], v250 offset:19376\n\t" \
  "v_lshlrev_b32_sdwa v169, 1, v169 dst_sel:DWORD dst_unused:UNUSED_PAD src0_sel:DWORD src1_sel:WORD_1\n\t" \
  "v_smfmac_f32_16x16x64_bf16 v[52:55], v[212:215], v[192:199], v248\n\t" \
  "ds_read_u16_d16_hi v163, v163 offset:0\n\t" \
  "v_fma_mix_f32 v152, v152, v225, 0 op_sel_hi:[0,1,0]\n\t" \
  "v_smfmac_f32_16x16x64_bf16 v[76:79], v[212:215], v[200:207], v248\n\t" \
  "s_waitcnt lgkmcnt(9)\n\t" \
  "ds_read_b64_tr_b16 v[212:213], v250 offset:18616\n\t" \
  "v_lshlrev_b32_sdwa v170, 1, v170 dst_sel:DWORD dst_unused:UNUSED_PAD src0_sel:DWORD src1_sel:WORD_1\n\t" \
  "v_smfmac_f32_16x16x64_bf16 v[8:11], v[216:219], v[176:183], v248\n\t" \
  "ds_read_u16_d16_hi v168, v168 offset:0\n\t" \
  "v_fma_mix_f32 v153, v153, v224, 0 op_sel_hi:[0,1,0]\n\t" \
  "v_smfmac_f32_16x16x64_bf16 v[32:35], v[216:219], v[184:191], v248\n\t" \
  "ds_read_b64_tr_b16 v[214:215], v250 offset:19384\n\t" \
  "v_lshlrev_b32_sdwa v171, 1, v171 dst_sel:DWORD dst_unused:UNUSED_PAD src0_sel:DWORD src1_sel:WORD_1\n\t" \
  "v_smfmac_f32_16x16x64_bf16 v[56:59], v[216:219], v[192:199], v248\n\t" \
  "ds_read_u16_d16_hi v169, v169 offset:0\n\t" \
  "v_mul_f32 v156, %[kk], v156\n\t" \
  "v_smfmac_f32_16x16x64_bf16 v[80:83], v[216:219], v[200:207], v248\n\t" \
  "s_waitcnt lgkmcnt(9)\n\t" \
  "s_barrier\n\t" \
  "ds_read_b64_tr_b16 v[216:217], v249 offset:12288\n\t" \
  "v_lshlrev_b32_sdwa v148, 1, v148 dst_sel:DWORD dst_unused:UNUSED_PAD src0_sel:DWORD src1_sel:WORD_1\n\t" \
  "v_smfmac_f32_16x16x64_bf16 v[12:15], v[220:223], v[176:183], v248\n\t" \
  "ds_read_u16_d16_hi v170, v170 offset:0\n\t" \
  "v_mul_f32 v157, %[kk], v157\n\t" \
  "v_smfmac_f32_16x16x64_bf16 v[36:39], v[220:223], v[184:191], v248\n\t" \
  "ds_read_b64_tr_b16 v[218:219], v249 offset:12544\n\t" \
  "v_cvt_pk_f16_f32 v224, v156, v157\n\t" \
  "v_smfmac_f32_16x16x64_bf16 v[60:63], v[220:223], v[192:199], v248\n\t" \
  "ds_read_u16_d16_hi v171, v171 offset:0\n\t" \
  "v_lshlrev_b32_sdwa v149, 1, v149 dst_sel:DWORD dst_unused:UNUSED_PAD src0_sel:DWORD src1_sel:WORD_1\n\t" \
  "v_smfmac_f32_16x16x64_bf16 v[84:87], v[220:223], v[200:207], v248\n\t" \
  "s_waitcnt lgkmcnt(9)\n\t" \
  "ds_read_b64_tr_b16 v[220:221], v249 offset:14336\n\t" \
  "v_pk_fma_f16 v225, v224, v224, %[h1] neg_lo:[1,0,0] neg_hi:[1,0,0]\n\t" \
  "v_smfmac_f32_16x16x64_bf16 v[16:19], v[208:211], v[176:183], v248\n\t" \
  "s_add_u32 %[rs], %[rs], 1\n\t" \
  "ds_read_u16_d16_hi v148, v148 offset:0\n\t" \
  "v_lshlrev_b32_sdwa v150, 1, v150 dst_sel:DWORD dst_unused:UNUSED_PAD src0_sel:DWORD src1_sel:WORD_1\n\t" \
  "v_smfmac_f32_16x16x64_bf16 v[40:43], v[208:211], v[184:191], v248\n\t" \
  "s_cmp_eq_u32 %[rs], 6\n\t" \
  "ds_read_b64_tr_b16 v[222:223], v249 offset:14592\n\t" \
  "v_pk_mul_f16 v225, v224, v225\n\t" \
  "v_smfmac_f32_16x16x64_bf16 v[64:67], v[208:211], v[192:199], v248\n\t" \
  "ds_read_u16_d16_hi v149, v149 offset:0\n\t" \
  "v_lshlrev_b32_sdwa v151, 1, v151 dst_sel:DWORD dst_unused:UNUSED_PAD src0_sel:DWORD src1_sel:WORD_1\n\t" \
  "v_smfmac_f32_16x16x64_bf16 v[88:91], v[208:211], v[200:207], v248\n\t" \
  "s_waitcnt lgkmcnt(9)\n\t" \
  "ds_read_b64_tr_b16 v[208:209], v249 offset:16384\n\t" \
  "v_exp_f16 v224, v225\n\t" \
  "v_smfmac_f32_16x16x64_bf16 v[20:23], v[212:215], v[176:183], v248\n\t" \
  "s_cselect_b32 %[rs], 0, %[rs]\n\t" \
  "ds_read_u16_d16_hi v150, v150 offset:0\n\t" \
  "v_lshlrev_b32_sdwa v158, 1, v158 dst_sel:DWORD dst_unused:UNUSED_PAD src0_sel:DWORD src1_sel:WORD_1\n\t" \
  "v_smfmac_f32_16x16x64_bf16 v[44:47], v[212:215], v[184:191], v248\n\t" \
  "s_cselect_b32 %[t0], -75920, %[ps]\n\t" \
  "ds_read_b64_tr_b16 v[210:211], v249 offset:16640\n\t" \
  "v_lshlrev_b32_sdwa v159, 1, v159 dst_sel:DWORD dst_unused:UNUSED_PAD src0_sel:DWORD src1_sel:WORD_1\n\t" \
  "v_smfmac_f32_16x16x64_bf16 v[68:71], v[212:215], v[192:199], v248\n\t" \
  "ds_read_u16_d16_hi v151, v151 offset:0\n\t" \
  "v_add_u32 v252, %[t0], v252\n\t" \
  "v_exp_f16_sdwa v224, v225 dst_sel:WORD_1 dst_unused:UNUSED_PRESERVE src0_sel:WORD_1\n\t" \
  "v_smfmac_f32_16x16x64_bf16 v[92:95], v[212:215], v[200:207], v248\n\t" \
  "s_waitcnt lgkmcnt(9)\n\t" \
  "ds_read_b64_tr_b16 v[212:213], v249 offset:12800\n\t" \
  "v_lshlrev_b32_sdwa v164, 1, v164 dst_sel:DWORD dst_unused:UNUSED_PAD src0_sel:DWORD src1_sel:WORD_1\n\t" \
  "v_mfma_f32_16x16x32_bf16 v[176:179], v[216:219], v[96:99], 0\n\t" \
  "ds_read_u16_d16_hi v158, v158 offset:0\n\t" \
  "v_pk_fma_f16 v224, v224, %[hc], %[hc]\n\t" \
  "v_mfma_f32_16x16x32_bf16 v[184:187], v[216:219], v[108:111], 0\n\t" \
  "ds_read_b64_tr_b16 v[214:215], v249 offset:13056\n\t" \
  "v_lshlrev_b32_sdwa v165, 1, v165 dst_sel:DWORD dst_unused:UNUSED_PAD src0_sel:DWORD src1_sel:WORD_1\n\t" \
  "v_mfma_f32_16x16x32_bf16 v[192:195], v[216:219], v[120:123], 0\n\t" \
  "ds_read_u16_d16_hi v159, v159 offset:0\n\t" \
  "v_rcp_f16 v225, v224\n\t" \
  "v_mfma_f32_16x16x32_bf16 v[200:203], v[216:219], v[132:135], 0\n\t" \
  "s_waitcnt lgkmcnt(9)\n\t" \
  "ds_read_b64_tr_b16 v[216:217], v249 offset:14848\n\t" \
  "v_lshlrev_b32_sdwa v166, 1, v166 dst_sel:DWORD dst_unused:UNUSED_PAD src0_sel:DWORD src1_sel:WORD_1\n\t" \
  "v_mfma_f32_16x16x32_bf16 v[176:179], v[220:223], v[100:103], v[176:179]\n\t" \
  "ds_read_u16_d16_hi v164, v164 offset:0\n\t" \
  "v_lshlrev_b32_sdwa v167, 1, v167 dst_sel:DWORD dst_unused:UNUSED_PAD src0_sel:DWORD src1_sel:WORD_1\n\t" \
  "v_mfma_f32_16x16x32_bf16 v[184:187], v[220:223], v[112:115], v[184:187]\n\t" \
  "ds_read_b64_tr_b16 v[218:219], v249 offset:15104\n\t" \
  "v_rcp_f16_sdwa v224, v224 dst_sel:DWORD dst_unused:UNUSED_PAD src0_sel:WORD_1\n\t" \
  "v_mfma_f32_16x16x32_bf16 v[192:195], v[220:223], v[124:127], v[192:195]\n\t" \
  "ds_read_u16_d16_hi v165, v165 offset:0\n\t" \
  "v_lshlrev_b32_sdwa v172, 1, v172 dst_sel:DWORD dst_unused:UNUSED_PAD src0_sel:DWORD src1_sel:WORD_1\n\t" \
  "v_mfma_f32_16x16x32_bf16 v[200:203], v[220:223], v[136:139], v[200:203]\n\t" \
  "s_waitcnt lgkmcnt(9)\n\t" \
  "ds_read_b64_tr_b16 v[220:221], v249 offset:16896\n\t" \
  "ds_read_u16_d16_hi v166, v166 offset:0\n\t" \
  "v_mfma_f32_16x16x32_bf16 v[176:179], v[208:211], v[104:107], v[176:179]\n\t" \
  "ds_read_b64_tr_b16 v[222:223], v249 offset:17152\n\t" \
  "v_lshlrev_b32_sdwa v173, 1, v173 dst_sel:DWORD dst_unused:UNUSED_PAD src0_sel:DWORD src1_sel:WORD_1\n\t" \
  "v_fma_mix_f32 v156, v156, v225, 0 op_sel_hi:[0,1,0]\n\t" \
  "v_mfma_f32_16x16x32_bf16 v[184:187], v[208:211], v[116:119], v[184:187]\n\t" \
  "ds_read_u16_d16_hi v167, v167 offset:0\n\t" \
  "v_fma_mix_f32 v157, v157, v224, 0 op_sel_hi:[0,1,0]\n\t" \
  "v_mfma_f32_16x16x32_bf16 v[192:195], v[208:211], v[128:131], v[192:195]\n\t" \
  "ds_read_u16_d16_hi v172, v172 offset:0\n\t" \
  "v_lshlrev_b32_sdwa v174, 1, v174 dst_sel:DWORD dst_unused:UNUSED_PAD src0_sel:DWORD src1_sel:WORD_1\n\t" \
  "v_mfma_f32_16x16x32_bf16 v[200:203], v[208:211], v[140:143], v[200:203]\n\t" \
  "s_waitcnt lgkmcnt(10)\n\t" \
  "ds_read_b64_tr_b16 v[208:209], v250 offset:0\n\t" \
  "v_lshlrev_b32_sdwa v175, 1, v175 dst_sel:DWORD dst_unused:UNUSED_PAD src0_sel:DWORD src1_sel:WORD_1\n\t" \
  "v_mfma_f32_16x16x32_bf16 v[180:183], v[212:215], v[96:99], 0\n\t" \
  "ds_read_u16_d16_hi v173, v173 offset:0\n\t" \
  "v_lshlrev_b32_sdwa v176, 1, v176 dst_sel:DWORD dst_unused:UNUSED_PAD src0_sel:DWORD src1_sel:WORD_1\n\t" \
  "v_mfma_f32_16x16x32_bf16 v[188:191], v[212:215], v[108:111], 0\n\t" \
  "ds_read_b64_tr_b16 v[210:211], v250 offset:768\n\t" \
  "v_mul_f32 v184, %[kk], v184\n\t" \
  "v_mfma_f32_16x16x32_bf16 v[196:199], v[212:215], v[120:123], 0\n\t" \
  "ds_read_u16_d16_hi v174, v174 offset:0\n\t" \
  "v_lshlrev_b32_sdwa v177, 1, v177 dst_sel:DWORD dst_unused:UNUSED_PAD src0_sel:DWORD src1_sel:WORD_1\n\t" \
  "v_mul_f32 v185, %[kk], v185\n\t" \
  "v_mfma_f32_16x16x32_bf16 v[204:207], v[212:215], v[132:135], 0\n\t" \
  "s_waitcnt lgkmcnt(10)\n\t" \
  "ds_read_b64_tr_b16 v[212:213], v250 offset:8\n\t" \
  "ds_read_u16_d16_hi v175, v175 offset:0\n\t" \
  "v_cvt_pk_f16_f32 v224, v184, v185\n\t" \
  "v_mfma_f32_16x16x32_bf16 v[180:183], v[216:219], v[100:103], v[180:183]\n\t" \
  "ds_read_b64_tr_b16 v[214:215], v250 offset:776\n\t" \
  "v_lshlrev_b32_sdwa v178, 1, v178 dst_sel:DWORD dst_unused:UNUSED_PAD src0_sel:DWORD src1_sel:WORD_1\n\t" \
  "v_mfma_f32_16x16x32_bf16 v[188:191], v[216:219], v[112:115], v[188:191]\n\t" \
  "ds_read_u16_d16_hi v176, v176 offset:0\n\t" \
  "v_pk_fma_f16 v225, v224, v224, %[h1] neg_lo:[1,0,0] neg_hi:[1,0,0]\n\t" \
  "v_mfma_f32_16x16x32_bf16 v[196:199], v[216:219], v[124:127], v[196:199]\n\t" \
  "ds_read_u16_d16_hi v177, v177 offset:0\n\t" \
  "v_lshlrev_b32_sdwa v179, 1, v179 dst_sel:DWORD dst_unused:UNUSED_PAD src0_sel:DWORD src1_sel:WORD_1\n\t" \
  "v_pk_mul_f16 v225, v224, v225\n\t" \
  "v_mfma_f32_16x16x32_bf16 v[204:207], v[216:219], v[136:139], v[204:207]\n\t" \
  "s_waitcnt lgkmcnt(11)\n\t" \
  "ds_read_b64_tr_b16 v[216:217], v250 offset:64\n\t" \
  "v_lshlrev_b32_sdwa v186, 1, v186 dst_sel:DWORD dst_unused:UNUSED_PAD src0_sel:DWORD src1_sel:WORD_1\n\t" \
  "v_mfma_f32_16x16x32_bf16 v[180:183], v[220:223], v[104:107], v[180:183]\n\t" \
  "ds_read_u16_d16_hi v178, v178 offset:0\n\t" \
  "v_exp_f16 v224, v225\n\t" \
  "v_mfma_f32_16x16x32_bf16 v[188:191], v[220:223], v[116:119], v[188:191]\n\t" \
  "ds_read_b64_tr_b16 v[218:219], v250 offset:832\n\t" \
  "v_lshlrev_b32_sdwa v187, 1, v187 dst_sel:DWORD dst_unused:UNUSED_PAD src0_sel:DWORD src1_sel:WORD_1\n\t" \
  "v_mfma_f32_16x16x32_bf16 v[196:199], v[220:223], v[128:131], v[196:199]\n\t" \
  "ds_read_u16_d16_hi v179, v179 offset:0\n\t" \
  "v_lshlrev_b32_sdwa v192, 1, v192 dst_sel:DWORD dst_unused:UNUSED_PAD src0_sel:DWORD src1_sel:WORD_1\n\t" \
  "v_mfma_f32_16x16x32_bf16 v[204:207], v[220:223], v[140:143], v[204:207]\n\t" \
  "s_waitcnt lgkmcnt(10)\n\t" \
  "ds_read_b64_tr_b16 v[220:221], v250 offset:72\n\t" \
  "ds_read_u16_d16_hi v186, v186 offset:0\n\t" \
  "v_exp_f16_sdwa v224, v225 dst_sel:WORD_1 dst_unused:UNUSED_PRESERVE src0_sel:WORD_1\n\t" \
  "v_smfmac_f32_16x16x64_bf16 v[0:3], v[208:211], v[144:151], v248\n\t" \
  "ds_read_b64_tr_b16 v[222:223], v250 offset:840\n\t" \
  "v_lshlrev_b32_sdwa v193, 1, v193 dst_sel:DWORD dst_unused:UNUSED_PAD src0_sel:DWORD src1_sel:WORD_1\n\t" \
  "v_smfmac_f32_16x16x64_bf16 v[24:27], v[208:211], v[152:159], v248\n\t" \
  "ds_read_u16_d16_hi v187, v187 offset:0\n\t" \
  "v_pk_fma_f16 v224, v224, %[hc], %[hc]\n\t" \
  "v_smfmac_f32_16x16x64_bf16 v[48:51], v[208:211], v[160:167], v248\n\t" \
  "s_waitcnt lgkmcnt(11)\n\t" \
  "ds_read_u16_d16_hi v192, v192 offset:0\n\t" \
  "v_lshlrev_b32_sdwa v194, 1, v194 dst_sel:DWORD dst_unused:UNUSED_PAD src0_sel:DWORD src1_sel:WORD_1\n\t" \
  "v_smfmac_f32_16x16x64_bf16 v[72:75], v[208:211], v[168:175], v248\n\t" \
  "s_waitcnt lgkmcnt(11)\n\t" \
  "ds_read_b64_tr_b16 v[208:209], v250 offset:128\n\t" \
  "v_rcp_f16 v225, v224\n\t" \
  "v_smfmac_f32_16x16x64_bf16 v[4:7], v[212:215], v[144:151], v248\n\t" \
  "ds_read_u16_d16_hi v193, v193 offset:0\n\t" \
  "v_lshlrev_b32_sdwa v195, 1, v195 dst_sel:DWORD dst_unused:UNUSED_PAD src0_sel:DWORD src1_sel:WORD_1\n\t" \
  "v_smfmac_f32_16x16x64_bf16 v[28:31], v[212:215], v[152:159], v248\n\t" \
  "ds_read_b64_tr_b16 v[210:211], v250 offset:896\n\t" \
  "v_lshlrev_b32_sdwa v200, 1, v200 dst_sel:DWORD dst_unused:UNUSED_PAD src0_sel:DWORD src1_sel:WORD_1\n\t" \
  "v_smfmac_f32_16x16x64_bf16 v[52:55], v[212:215], v[160:167], v248\n\t" \
  "ds_read_u16_d16_hi v194, v194 offset:0\n\t" \
  "v_rcp_f16_sdwa v224, v224 dst_sel:DWORD dst_unused:UNUSED_PAD src0_sel:WORD_1\n\t" \
  "v_smfmac_f32_16x16x64_bf16 v[76:79], v[212:215], v[168:175], v248\n\t" \
  "s_waitcnt lgkmcnt(10)\n\t" \
  "ds_read_b64_tr_b16 v[212:213], v250 offset:136\n\t" \
  "v_lshlrev_b32_sdwa v201, 1, v201 dst_sel:DWORD dst_unused:UNUSED_PAD src0_sel:DWORD src1_sel:WORD_1\n\t" \
  "v_smfmac_f32_16x16x64_bf16 v[8:11], v[216:219], v[144:151], v248\n\t" \
  "ds_read_u16_d16_hi v195, v195 offset:0\n\t" \
  "v_fma_mix_f32 v184, v184, v225, 0 op_sel_hi:[0,1,0]\n\t" \
  "v_smfmac_f32_16x16x64_bf16 v[32:35], v[216:219], v[152:159], v248\n\t" \
  "ds_read_b64_tr_b16 v[214:215], v250 offset:904\n\t" \
  "v_lshlrev_b32_sdwa v202, 1, v202 dst_sel:DWORD dst_unused:UNUSED_PAD src0_sel:DWORD src1_sel:WORD_1\n\t" \
  "v_smfmac_f32_16x16x64_bf16 v[56:59], v[216:219], v[160:167], v248\n\t" \
  "ds_read_u16_d16_hi v200, v200 offset:0\n\t" \
  "v_fma_mix_f32 v185, v185, v224, 0 op_sel_hi:[0,1,0]\n\t" \
  "v_smfmac_f32_16x16x64_bf16 v[80:83], v[216:219], v[168:175], v248\n\t" \
  "s_waitcnt lgkmcnt(10)\n\t" \
  "s_barrier\n\t" \
  "ds_read_b64_tr_b16 v[216:217], v249 offset:0\n\t" \
  "ds_read_b128 v[120:123], v251 offset:0\n\t" \
  "v_lshlrev_b32_sdwa v203, 1, v203 dst_sel:DWORD dst_unused:UNUSED_PAD src0_sel:DWORD src1_sel:WORD_1\n\t" \
  "v_smfmac_f32_16x16x64_bf16 v[12:15], v[220:223], v[144:151], v248\n\t" \
  "ds_read_b64_tr_b16 v[218:219], v249 offset:256\n\t" \
  "ds_read_b128 v[124:127], v251 offset:4096\n\t" \
  "v_mul_f32 v188, %[kk], v188\n\t" \
  "v_smfmac_f32_16x16x64_bf16 v[36:39], v[220:223], v[152:159], v248\n\t" \
  "ds_read_b128 v[128:131], v251 offset:8192\n\t" \
  "v_lshlrev_b32_sdwa v180, 1, v180 dst_sel:DWORD dst_unused:UNUSED_PAD src0_sel:DWORD src1_sel:WORD_1\n\t" \
  "v_mul_f32 v189, %[kk], v189\n\t" \
  "v_smfmac_f32_16x16x64_bf16 v[60:63], v[220:223], v[160:167], v248\n\t" \
  "ds_read_u16_d16_hi v201, v201 offset:0\n\t" \
  "v_cvt_pk_f16_f32 v224, v188, v189\n\t" \
  "v_smfmac_f32_16x16x64_bf16 v[84:87], v[220:223], v[168:175], v248\n\t" \
  "s_waitcnt lgkmcnt(11)\n\t" \
  "ds_read_b64_tr_b16 v[220:221], v249 offset:2048\n\t" \
  "ds_read_u16_d16_hi v202, v202 offset:0\n\t" \
  "v_lshlrev_b32_sdwa v181, 1, v181 dst_sel:DWORD dst_unused:UNUSED_PAD src0_sel:DWORD src1_sel:WORD_1\n\t" \
  "v_smfmac_f32_16x16x64_bf16 v[16:19], v[208:211], v[144:151], v248\n\t" \
  "ds_read_b64_tr_b16 v[222:223], v249 offset:2304\n\t" \
  "v_pk_fma_f16 v225, v224, v224, %[h1] neg_lo:[1,0,0] neg_hi:[1,0,0]\n\t" \
  "v_smfmac_f32_16x16x64_bf16 v[40:43], v[208:211], v[152:159], v248\n\t" \
  "ds_read_u16_d16_hi v203, v203 offset:0\n\t" \
  "v_lshlrev_b32_sdwa v182, 1, v182 dst_sel:DWORD dst_unused:UNUSED_PAD src0_sel:DWORD src1_sel:WORD_1\n\t" \
  "v_pk_mul_f16 v225, v224, v225\n\t" \
  "v_smfmac_f32_16x16x64_bf16 v[64:67], v[208:211], v[160:167], v248\n\t" \
  "ds_read_u16_d16_hi v180, v180 offset:0\n\t" \
  "v_lshlrev_b32_sdwa v183, 1, v183 dst_sel:DWORD dst_unused:UNUSED_PAD src0_sel:DWORD src1_sel:WORD_1\n\t" \
  "v_smfmac_f32_16x16x64_bf16 v[88:91], v[208:211], v[168:175], v248\n\t" \
  "s_waitcnt lgkmcnt(12)\n\t" \
  "ds_read_b64_tr_b16 v[208:209], v249 offset:4096\n\t" \
  "ds_read_u16_d16_hi v181, v181 offset:0\n\t" \
  "v_exp_f16 v224, v225\n\t" \
  "v_smfmac_f32_16x16x64_bf16 v[20:23], v[212:215], v[144:151], v248\n\t" \
  "ds_read_b64_tr_b16 v[210:211], v249 offset:4352\n\t" \
  "v_lshlrev_b32_sdwa v190, 1, v190 dst_sel:DWORD dst_unused:UNUSED_PAD src0_sel:DWORD src1_sel:WORD_1\n\t" \
  "v_smfmac_f32_16x16x64_bf16 v[44:47], v[212:215], v[152:159], v248\n\t" \
  "ds_read_u16_d16_hi v182, v182 offset:0\n\t" \
  "v_lshlrev_b32_sdwa v191, 1, v191 dst_sel:DWORD dst_unused:UNUSED_PAD src0_sel:DWORD src1_sel:WORD_1\n\t" \
  "v_smfmac_f32_16x16x64_bf16 v[68:71], v[212:215], v[160:167], v248\n\t" \
  "ds_read_u16_d16_hi v183, v183 offset:0\n\t" \
  "v_exp_f16_sdwa v224, v225 dst_sel:WORD_1 dst_unused:UNUSED_PRESERVE src0_sel:WORD_1\n\t" \
  "v_smfmac_f32_16x16x64_bf16 v[92:95], v[212:215], v[168:175], v248\n\t" \
  "s_waitcnt lgkmcnt(13)\n\t" \
  "ds_read_b64_tr_b16 v[212:213], v249 offset:512\n\t" \
  "v_lshlrev_b32_sdwa v196, 1, v196 dst_sel:DWORD dst_unused:UNUSED_PAD src0_sel:DWORD src1_sel:WORD_1\n\t" \
  "v_mfma_f32_16x16x32_bf16 v[144:147], v[216:219], v[96:99], 0\n\t" \
  "ds_read_u16_d16_hi v190, v190 offset:0\n\t" \
  "v_pk_fma_f16 v224, v224, %[hc], %[hc]\n\t" \
  "v_mfma_f32_16x16x32_bf16 v[152:155], v[216:219], v[108:111], 0\n\t" \
  "ds_read_b64_tr_b16 v[214:215], v249 offset:768\n\t" \
  "v_lshlrev_b32_sdwa v197, 1, v197 dst_sel:DWORD dst_unused:UNUSED_PAD src0_sel:DWORD src1_sel:WORD_1\n\t" \
  "v_mfma_f32_16x16x32_bf16 v[160:163], v[216:219], v[120:123], 0\n\t" \
  "ds_read_u16_d16_hi v191, v191 offset:0\n\t" \
  "v_rcp_f16 v225, v224\n\t" \
  "v_mfma_f32_16x16x32_bf16 v[168:171], v[216:219], v[132:135], 0\n\t" \
  "s_waitcnt lgkmcnt(11)\n\t" \
  "ds_read_b64_tr_b16 v[216:217], v249 offset:2560\n\t" \
  "v_lshlrev_b32_sdwa v198, 1, v198 dst_sel:DWORD dst_unused:UNUSED_PAD src0_sel:DWORD src1_sel:WORD_1\n\t" \
  "v_mfma_f32_16x16x32_bf16 v[144:147], v[220:223], v[100:103], v[144:147]\n\t" \
  "ds_read_u16_d16_hi v196, v196 offset:0\n\t" \
  "v_lshlrev_b32_sdwa v199, 1, v199 dst_sel:DWORD dst_unused:UNUSED_PAD src0_sel:DWORD src1_sel:WORD_1\n\t" \
  "v_mfma_f32_16x16x32_bf16 v[152:155], v[220:223], v[112:115], v[152:155]\n\t" \
  "ds_read_b64_tr_b16 v[218:219], v249 offset:2816\n\t" \
  "v_rcp_f16_sdwa v224, v224 dst_sel:DWORD dst_unused:UNUSED_PAD src0_sel:WORD_1\n\t" \
  "v_mfma_f32_16x16x32_bf16 v[160:163], v[220:223], v[124:127], v[160:163]\n\t" \
  "ds_read_u16_d16_hi v197, v197 offset:0\n\t" \
  "v_lshlrev_b32_sdwa v204, 1, v204 dst_sel:DWORD dst_unused:UNUSED_PAD src0_sel:DWORD src1_sel:WORD_1\n\t" \
  "v_mfma_f32_16x16x32_bf16 v[168:171], v[220:223], v[136:139], v[168:171]\n\t" \
  "s_waitcnt lgkmcnt(10)\n\t" \
  "ds_read_b64_tr_b16 v[220:221], v249 offset:4608\n\t" \
  "ds_read_u16_d16_hi v198, v198 offset:0\n\t" \
  "v_mfma_f32_16x16x32_bf16 v[144:147], v[208:211], v[104:107], v[144:147]\n\t" \
  "ds_read_b64_tr_b16 v[222:223], v249 offset:4864\n\t" \
  "v_lshlrev_b32_sdwa v205, 1, v205 dst_sel:DWORD dst_unused:UNUSED_PAD src0_sel:DWORD src1_sel:WORD_1\n\t" \
  "v_fma_mix_f32 v188, v188, v225, 0 op_sel_hi:[0,1,0]\n\t" \
  "v_mfma_f32_16x16x32_bf16 v[152:155], v[208:211], v[116:119], v[152:155]\n\t" \
  "ds_read_u16_d16_hi v199, v199 offset:0\n\t" \
  "v_fma_mix_f32 v189, v189, v224, 0 op_sel_hi:[0,1,0]\n\t" \
  "v_mfma_f32_16x16x32_bf16 v[160:163], v[208:211], v[128:131], v[160:163]\n\t" \
  "ds_read_u16_d16_hi v204, v204 offset:0\n\t" \
  "v_lshlrev_b32_sdwa v206, 1, v206 dst_sel:DWORD dst_unused:UNUSED_PAD src0_sel:DWORD src1_sel:WORD_1\n\t" \
  "v_mfma_f32_16x16x32_bf16 v[168:171], v[208:211], v[140:143], v[168:171]\n\t" \
  "s_waitcnt lgkmcnt(10)\n\t" \
  "ds_read_b64_tr_b16 v[208:209], v250 offset:6160\n\t" \
  "v_lshlrev_b32_sdwa v207, 1, v207 dst_sel:DWORD dst_unused:UNUSED_PAD src0_sel:DWORD src1_sel:WORD_1\n\t" \
  "v_mfma_f32_16x16x32_bf16 v[148:151], v[212:215], v[96:99], 0\n\t" \
  "ds_read_u16_d16_hi v205, v205 offset:0\n\t" \
  "v_lshlrev_b32_sdwa v144, 1, v144 dst_sel:DWORD dst_unused:UNUSED_PAD src0_sel:DWORD src1_sel:WORD_1\n\t" \
  "v_mfma_f32_16x16x32_bf16 v[156:159], v[212:215], v[108:111], 0\n\t" \
  "ds_read_b64_tr_b16 v[210:211], v250 offset:6928\n\t" \
  "v_mul_f32 v152, %[kk], v152\n\t" \
  "v_mfma_f32_16x16x32_bf16 v[164:167], v[212:215], v[120:123], 0\n\t" \
  "ds_read_u16_d16_hi v206, v206 offset:0\n\t" \
  "v_lshlrev_b32_sdwa v145, 1, v145 dst_sel:DWORD dst_unused:UNUSED_PAD src0_sel:DWORD src1_sel:WORD_1\n\t" \
  "v_mul_f32 v153, %[kk], v153\n\t" \
  "v_mfma_f32_16x16x32_bf16 v[172:175], v[212:215], v[132:135], 0\n\t" \
  "s_waitcnt lgkmcnt(10)\n\t" \
  "ds_read_b64_tr_b16 v[212:213], v250 offset:6168\n\t" \
  "ds_read_u16_d16_hi v207, v207 offset:0\n\t" \
  "v_cvt_pk_f16_f32 v224, v152, v153\n\t" \
  "v_mfma_f32_16x16x32_bf16 v[148:151], v[216:219], v[100:103], v[148:151]\n\t" \
  "ds_read_b64_tr_b16 v[214:215], v250 offset:6936\n\t" \
  "v_lshlrev_b32_sdwa v146, 1, v146 dst_sel:DWORD dst_unused:UNUSED_PAD src0_sel:DWORD src1_sel:WORD_1\n\t" \
  "v_mfma_f32_16x16x32_bf16 v[156:159], v[216:219], v[112:115], v[156:159]\n\t" \
  "ds_read_u16_d16_hi v144, v144 offset:0\n\t" \
  "v_pk_fma_f16 v225, v224, v224, %[h1] neg_lo:[1,0,0] neg_hi:[1,0,0]\n\t" \
  "v_mfma_f32_16x16x32_bf16 v[164:167], v[216:219], v[124:127], v[164:167]\n\t" \
  "ds_read_u16_d16_hi v145, v145 offset:0\n\t" \
  "v_lshlrev_b32_sdwa v147, 1, v147 dst_sel:DWORD dst_unused:UNUSED_PAD src0_sel:DWORD src1_sel:WORD_1\n\t" \
  "v_pk_mul_f16 v225, v224, v225\n\t" \
  "v_mfma_f32_16x16x32_bf16 v[172:175], v[216:219], v[136:139], v[172:175]\n\t" \
  "s_waitcnt lgkmcnt(11)\n\t" \
  "ds_read_b64_tr_b16 v[216:217], v250 offset:6224\n\t" \
  "v_lshlrev_b32_sdwa v154, 1, v154 dst_sel:DWORD dst_unused:UNUSED_PAD src0_sel:DWORD src1_sel:WORD_1\n\t" \
  "v_mfma_f32_16x16x32_bf16 v[148:151], v[220:223], v[104:107], v[148:151]\n\t" \
  "ds_read_u16_d16_hi v146, v146 offset:0\n\t" \
  "v_exp_f16 v224, v225\n\t" \
  "v_mfma_f32_16x16x32_bf16 v[156:159], v[220:223], v[116:119], v[156:159]\n\t" \
  "ds_read_b64_tr_b16 v[218:219], v250 offset:6992\n\t" \
  "v_lshlrev_b32_sdwa v155, 1, v155 dst_sel:DWORD dst_unused:UNUSED_PAD src0_sel:DWORD src1_sel:WORD_1\n\t" \
  "v_mfma_f32_16x16x32_bf16 v[164:167], v[220:223], v[128:131], v[164:167]\n\t" \
  "ds_read_u16_d16_hi v147, v147 offset:0\n\t" \
  "v_lshlrev_b32_sdwa v160, 1, v160 dst_sel:DWORD dst_unused:UNUSED_PAD src0_sel:DWORD src1_sel:WORD_1\n\t" \
  "v_mfma_f32_16x16x32_bf16 v[172:175], v[220:223], v[140:143], v[172:175]\n\t" \
  "s_waitcnt lgkmcnt(10)\n\t" \
  "ds_read_b64_tr_b16 v[220:221], v250 offset:6232\n\t" \
  "ds_read_u16_d16_hi v154, v154 offset:0\n\t" \
  "v_exp_f16_sdwa v224, v225 dst_sel:WORD_1 dst_unused:UNUSED_PRESERVE src0_sel:WORD_1\n\t" \
  "v_smfmac_f32_16x16x64_bf16 v[0:3], v[208:211], v[176:183], v248\n\t" \
  "ds_read_b64_tr_b16 v[222:223], v250 offset:7000\n\t" \
  "v_lshlrev_b32_sdwa v161, 1, v161 dst_sel:DWORD dst_unused:UNUSED_PAD src0_sel:DWORD src1_sel:WORD_1\n\t" \
  "v_smfmac_f32_16x16x64_bf16 v[24:27], v[208:211], v[184:191], v248\n\t" \
  "ds_read_u16_d16_hi v155, v155 offset:0\n\t" \
  "v_pk_fma_f16 v224, v224, %[hc], %[hc]\n\t" \
  "v_smfmac_f32_16x16x64_bf16 v[48:51], v[208:211], v[192:199], v248\n\t" \
  "s_waitcnt lgkmcnt(11)\n\t" \
  "ds_read_u16_d16_hi v160, v160 offset:0\n\t" \
  "v_lshlrev_b32_sdwa v162, 1, v162 dst_sel:DWORD dst_unused:UNUSED_PAD src0_sel:DWORD src1_sel:WORD_1\n\t" \
  "v_smfmac_f32_16x16x64_bf16 v[72:75], v[208:211], v[200:207], v248\n\t" \
  "s_waitcnt lgkmcnt(11)\n\t" \
  "ds_read_b64_tr_b16 v[208:209], v250 offset:6288\n\t" \
  "v_rcp_f16 v225, v224\n\t" \
  "v_smfmac_f32_16x16x64_bf16 v[4:7], v[212:215], v[176:183], v248\n\t" \
  "ds_read_b64 v[242:243], v252 offset:128\n\t" \
  "v_lshlrev_b32_sdwa v163, 1, v163 dst_sel:DWORD dst_unused:UNUSED_PAD src0_sel:DWORD src1_sel:WORD_1\n\t" \
  "v_smfmac_f32_16x16x64_bf16 v[28:31], v[212:215], v[184:191], v248\n\t" \
  "ds_read_b64_tr_b16 v[210:211], v250 offset:7056\n\t" \
  "ds_read_b64 v[244:245], v252 offset:136\n\t" \
  "v_smfmac_f32_16x16x64_bf16 v[52:55], v[212:215], v[192:199], v248\n\t" \
  "ds_read_u16_d16_hi v161, v161 offset:0\n\t" \
  "v_lshlrev_b32_sdwa v168, 1, v168 dst_sel:DWORD dst_unused:UNUSED_PAD src0_sel:DWORD src1_sel:WORD_1\n\t" \
  "v_rcp_f16_sdwa v224, v224 dst_sel:DWORD dst_unused:UNUSED_PAD src0_sel:WORD_1\n\t" \
  "v_smfmac_f32_16x16x64_bf16 v[76:79], v[212:215], v[200:207], v248\n\t" \
  "s_waitcnt lgkmcnt(11)\n\t" \
  "ds_read_b64_tr_b16 v[212:213], v250 offset:6296\n\t" \
  "ds_read_u16_d16_hi v162, v162 offset:0\n\t" \
  "v_smfmac_f32_16x16x64_bf16 v[8:11], v[216:219], v[176:183], v248\n\t" \
  "ds_read_b64_tr_b16 v[214:215], v250 offset:7064\n\t" \
  "v_lshlrev_b32_sdwa v169, 1, v169 dst_sel:DWORD dst_unused:UNUSED_PAD src0_sel:DWORD src1_sel:WORD_1\n\t" \
  "v_cvt_pk_bf16_f32 v230, v48, v49\n\t" \
  "v_smfmac_f32_16x16x64_bf16 v[32:35], v[216:219], v[184:191], v248\n\t" \
  "ds_read_u16_d16_hi v163, v163 offset:0\n\t" \
  "v_fma_mix_f32 v152, v152, v225, 0 op_sel_hi:[0,1,0]\n\t" \
  "v_cvt_pk_bf16_f32 v231, v50, v51\n\t" \
  "v_smfmac_f32_16x16x64_bf16 v[56:59], v[216:219], v[192:199], v248\n\t" \
  "ds_read_u16_d16_hi v168, v168 offset:0\n\t" \
  "v_lshlrev_b32_sdwa v170, 1, v170 dst_sel:DWORD dst_unused:UNUSED_PAD src0_sel:DWORD src1_sel:WORD_1\n\t" \
  "v_fma_mix_f32 v153, v153, v224, 0 op_sel_hi:[0,1,0]\n\t" \
  "v_cvt_pk_bf16_f32 v232, v52, v53\n\t" \
  "v_smfmac_f32_16x16x64_bf16 v[80:83], v[216:219], v[200:207], v248\n\t" \
  "s_waitcnt lgkmcnt(12)\n\t" \
  "s_barrier\n\t" \
  "ds_read_b64_tr_b16 v[216:217], v249 offset:6144\n\t" \
  "v_lshlrev_b32_sdwa v171, 1, v171 dst_sel:DWORD dst_unused:UNUSED_PAD src0_sel:DWORD src1_sel:WORD_1\n\t" \
  "v_cvt_pk_bf16_f32 v233, v54, v55\n\t" \
  "v_smfmac_f32_16x16x64_bf16 v[12:15], v[220:223], v[176:183], v248\n\t" \
  "buffer_store_dwordx4 v[230:233], v253, %[osrd], %[ob] offen offset:0 sc1\n\t" \
  "ds_read_b64_tr_b16 v[218:219], v249 offset:6400\n\t" \
  "v_mul_f32 v156, %[kk], v156\n\t" \
  "v_smfmac_f32_16x16x64_bf16 v[36:39], v[220:223], v[184:191], v248\n\t" \
  "ds_read_b64 v[230:231], v252 offset:0\n\t" \
  "ds_read_b64 v[232:233], v252 offset:8\n\t" \
  "v_lshlrev_b32_sdwa v148, 1, v148 dst_sel:DWORD dst_unused:UNUSED_PAD src0_sel:DWORD src1_sel:WORD_1\n\t" \
  "v_mul_f32 v157, %[kk], v157\n\t" \
  "v_smfmac_f32_16x16x64_bf16 v[60:63], v[220:223], v[192:199], v248\n\t" \
  "ds_read_u16_d16_hi v169, v169 offset:0\n\t" \
  "v_cvt_pk_f16_f32 v224, v156, v157\n\t" \
  "v_smfmac_f32_16x16x64_bf16 v[84:87], v[220:223], v[200:207], v248\n\t" \
  "s_waitcnt lgkmcnt(12)\n\t" \
  "ds_read_b64_tr_b16 v[220:221], v249 offset:8192\n\t" \
  "ds_read_u16_d16_hi v170, v170 offset:0\n\t" \
  "v_lshlrev_b32_sdwa v149, 1, v149 dst_sel:DWORD dst_unused:UNUSED_PAD src0_sel:DWORD src1_sel:WORD_1\n\t" \
  "v_smfmac_f32_16x16x64_bf16 v[16:19], v[208:211], v[176:183], v248\n\t" \
  "ds_read_b64_tr_b16 v[222:223], v249 offset:8448\n\t" \
  "v_pk_fma_f16 v225, v224, v224, %[h1] neg_lo:[1,0,0] neg_hi:[1,0,0]\n\t" \
  "v_cvt_pk_bf16_f32 v234, v56, v57\n\t" \
  "v_smfmac_f32_16x16x64_bf16 v[40:43], v[208:211], v[184:191], v248\n\t" \
  "ds_read_u16_d16_hi v171, v171 offset:0\n\t" \
  "v_lshlrev_b32_sdwa v150, 1, v150 dst_sel:DWORD dst_unused:UNUSED_PAD src0_sel:DWORD src1_sel:WORD_1\n\t" \
  "v_pk_mul_f16 v225, v224, v225\n\t" \
  "v_cvt_pk_bf16_f32 v235, v58, v59\n\t" \
  "v_smfmac_f32_16x16x64_bf16 v[64:67], v[208:211], v[192:199], v248\n\t" \
  "ds_read_u16_d16_hi v148, v148 offset:0\n\t" \
  "v_lshlrev_b32_sdwa v151, 1, v151 dst_sel:DWORD dst_unused:UNUSED_PAD src0_sel:DWORD src1_sel:WORD_1\n\t" \
  "v_cvt_pk_bf16_f32 v236, v60, v61\n\t" \
  "v_smfmac_f32_16x16x64_bf16 v[88:91], v[208:211], v[200:207], v248\n\t" \
  "s_waitcnt lgkmcnt(12)\n\t" \
  "ds_read_b64_tr_b16 v[208:209], v249 offset:10240\n\t" \
  "ds_read_u16_d16_hi v149, v149 offset:0\n\t" \
  "v_exp_f16 v224, v225\n\t" \
  "v_cvt_pk_bf16_f32 v237, v62, v63\n\t" \
  "v_smfmac_f32_16x16x64_bf16 v[20:23], v[212:215], v[176:183], v248\n\t" \
  "ds_read_b64_tr_b16 v[210:211], v249 offset:10496\n\t" \
  "buffer_store_dwordx4 v[234:237], v253, %[osrd], %[ob] offen offset:64 sc1\n\t" \
  "v_lshlrev_b32_sdwa v158, 1, v158 dst_sel:DWORD dst_unused:UNUSED_PAD src0_sel:DWORD src1_sel:WORD_1\n\t" \
  "v_smfmac_f32_16x16x64_bf16 v[44:47], v[212:215], v[184:191], v248\n\t" \
  "ds_read_b64 v[234:235], v252 offset:64\n\t" \
  "ds_read_b64 v[236:237], v252 offset:72\n\t" \
  "v_smfmac_f32_16x16x64_bf16 v[68:71], v[212:215], v[192:199], v248\n\t" \
  "s_waitcnt lgkmcnt(12)\n\t" \
  "ds_read_u16_d16_hi v150, v150 offset:0\n\t" \
  "v_lshlrev_b32_sdwa v159, 1, v159 dst_sel:DWORD dst_unused:UNUSED_PAD src0_sel:DWORD src1_sel:WORD_1\n\t" \
  "v_mfma_f32_16x16x16_bf16 v[48:51], v[246:247], v[230:231], 0\n\t" \
  "ds_read_u16_d16_hi v151, v151 offset:0\n\t" \
  "v_exp_f16_sdwa v224, v225 dst_sel:WORD_1 dst_unused:UNUSED_PRESERVE src0_sel:WORD_1\n\t" \
  "v_smfmac_f32_16x16x64_bf16 v[92:95], v[212:215], v[200:207], v248\n\t" \
  "ds_read_b64_tr_b16 v[212:213], v249 offset:6656\n\t" \
  "v_lshlrev_b32_sdwa v164, 1, v164 dst_sel:DWORD dst_unused:UNUSED_PAD src0_sel:DWORD src1_sel:WORD_1\n\t" \
  "v_mfma_f32_16x16x32_bf16 v[176:179], v[216:219], v[96:99], 0\n\t" \
  "ds_read_u16_d16_hi v158, v158 offset:0\n\t" \
  "v_pk_fma_f16 v224, v224, %[hc], %[hc]\n\t" \
  "v_cvt_pk_bf16_f32 v238, v64, v65\n\t" \
  "v_mfma_f32_16x16x32_bf16 v[184:187], v[216:219], v[108:111], 0\n\t" \
  "s_waitcnt lgkmcnt(14)\n\t" \
  "ds_read_b64_tr_b16 v[214:215], v249 offset:6912\n\t" \
  "v_lshlrev_b32_sdwa v165, 1, v165 dst_sel:DWORD dst_unused:UNUSED_PAD src0_sel:DWORD src1_sel:WORD_1\n\t" \
  "v_mfma_f32_16x16x16_bf16 v[52:55], v[246:247], v[232:233], 0\n\t" \
  "ds_read_u16_d16_hi v159, v159 offset:0\n\t" \
  "v_cvt_pk_bf16_f32 v239, v66, v67\n\t" \
  "v_mfma_f32_16x16x32_bf16 v[192:195], v[216:219], v[120:123], 0\n\t" \
  "ds_read_u16_d16_hi v164, v164 offset:0\n\t" \
  "v_rcp_f16 v225, v224\n\t" \
  "v_cvt_pk_bf16_f32 v240, v68, v69\n\t" \
  "v_mfma_f32_16x16x32_bf16 v[200:203], v[216:219], v[132:135], 0\n\t" \
  "s_waitcnt lgkmcnt(14)\n\t" \
  "ds_read_b64_tr_b16 v[216:217], v249 offset:8704\n\t" \
  "v_lshlrev_b32_sdwa v166, 1, v166 dst_sel:DWORD dst_unused:UNUSED_PAD src0_sel:DWORD src1_sel:WORD_1\n\t" \
  "v_cvt_pk_bf16_f32 v241, v70, v71\n\t" \
  "v_mfma_f32_16x16x32_bf16 v[176:179], v[220:223], v[100:103], v[176:179]\n\t" \
  "buffer_store_dwordx4 v[238:241], v253, %[osrd], %[ob] offen offset:128 sc1\n\t" \
  "v_lshlrev_b32_sdwa v167, 1, v167 dst_sel:DWORD dst_unused:UNUSED_PAD src0_sel:DWORD src1_sel:WORD_1\n\t" \
  "v_mfma_f32_16x16x32_bf16 v[184:187], v[220:223], v[112:115], v[184:187]\n\t" \
  "ds_read_b64_tr_b16 v[218:219], v249 offset:8960\n\t" \
  "v_rcp_f16_sdwa v224, v224 dst_sel:DWORD dst_unused:UNUSED_PAD src0_sel:WORD_1\n\t" \
  "v_mfma_f32_16x16x32_bf16 v[192:195], v[220:223], v[124:127], v[192:195]\n\t" \
  "s_waitcnt lgkmcnt(10)\n\t" \
  "ds_read_u16_d16_hi v165, v165 offset:0\n\t" \
  "v_lshlrev_b32_sdwa v172, 1, v172 dst_sel:DWORD dst_unused:UNUSED_PAD src0_sel:DWORD src1_sel:WORD_1\n\t" \
  "v_mfma_f32_16x16x16_bf16 v[56:59], v[246:247], v[234:235], 0\n\t" \
  "ds_read_u16_d16_hi v166, v166 offset:0\n\t" \
  "v_lshlrev_b32_sdwa v173, 1, v173 dst_sel:DWORD dst_unused:UNUSED_PAD src0_sel:DWORD src1_sel:WORD_1\n\t" \
  "v_mfma_f32_16x16x32_bf16 v[200:203], v[220:223], v[136:139], v[200:203]\n\t" \
  "ds_read_b64_tr_b16 v[220:221], v249 offset:10752\n\t" \
  "ds_read_u16_d16_hi v167, v167 offset:0\n\t" \
  "v_mfma_f32_16x16x32_bf16 v[176:179], v[208:211], v[104:107], v[176:179]\n\t" \
  "ds_read_b64_tr_b16 v[222:223], v249 offset:11008\n\t" \
  "v_fma_mix_f32 v156, v156, v225, 0 op_sel_hi:[0,1,0]\n\t" \
  "v_mfma_f32_16x16x32_bf16 v[184:187], v[208:211], v[116:119], v[184:187]\n\t" \
  "s_waitcnt lgkmcnt(14)\n\t" \
  "ds_read_u16_d16_hi v172, v172 offset:0\n\t" \
  "v_fma_mix_f32 v157, v157, v224, 0 op_sel_hi:[0,1,0]\n\t" \
  "v_mfma_f32_16x16x16_bf16 v[60:63], v[246:247], v[236:237], 0\n\t" \
  "ds_read_u16_d16_hi v173, v173 offset:0\n\t" \
  "v_lshlrev_b32_sdwa v174, 1, v174 dst_sel:DWORD dst_unused:UNUSED_PAD src0_sel:DWORD src1_sel:WORD_1\n\t" \
  "v_mfma_f32_16x16x32_bf16 v[192:195], v[208:211], v[128:131], v[192:195]\n\t" \
  "ds_read_u16_d16_hi v174, v174 offset:0\n\t" \
  "v_lshlrev_b32_sdwa v175, 1, v175 dst_sel:DWORD dst_unused:UNUSED_PAD src0_sel:DWORD src1_sel:WORD_1\n\t" \
  "v_mfma_f32_16x16x32_bf16 v[200:203], v[208:211], v[140:143], v[200:203]\n\t" \
  "s_waitcnt lgkmcnt(12)\n\t" \
  "ds_read_b64_tr_b16 v[208:209], v250 offset:12320\n\t" \
  "v_lshlrev_b32_sdwa v176, 1, v176 dst_sel:DWORD dst_unused:UNUSED_PAD src0_sel:DWORD src1_sel:WORD_1\n\t" \
  "v_mfma_f32_16x16x32_bf16 v[180:183], v[212:215], v[96:99], 0\n\t" \
  "ds_read_u16_d16_hi v175, v175 offset:0\n\t" \
  "v_mul_f32 v184, %[kk], v184\n\t" \
  "v_mfma_f32_16x16x32_bf16 v[188:191], v[212:215], v[108:111], 0\n\t" \
  "ds_read_b64_tr_b16 v[210:211], v250 offset:13088\n\t" \
  "v_lshlrev_b32_sdwa v177, 1, v177 dst_sel:DWORD dst_unused:UNUSED_PAD src0_sel:DWORD src1_sel:WORD_1\n\t" \
  "v_mfma_f32_16x16x32_bf16 v[196:199], v[212:215], v[120:123], 0\n\t" \
  "ds_read_u16_d16_hi v176, v176 offset:0\n\t" \
  "v_mul_f32 v185, %[kk], v185\n\t" \
  "v_mfma_f32_16x16x16_bf16 v[64:67], v[246:247], v[242:243], 0\n\t" \
  "ds_read_u16_d16_hi v177, v177 offset:0\n\t" \
  "v_cvt_pk_f16_f32 v224, v184, v185\n\t" \
  "v_mfma_f32_16x16x32_bf16 v[204:207], v[212:215], v[132:135], 0\n\t" \
  "s_waitcnt lgkmcnt(13)\n\t" \
  "ds_read_b64_tr_b16 v[212:213], v250 offset:12328\n\t" \
  "v_lshlrev_b32_sdwa v178, 1, v178 dst_sel:DWORD dst_unused:UNUSED_PAD src0_sel:DWORD src1_sel:WORD_1\n\t" \
  "v_mfma_f32_16x16x32_bf16 v[180:183], v[216:219], v[100:103], v[180:183]\n\t" \
  "ds_read_u16_d16_hi v178, v178 offset:0\n\t" \
  "v_pk_fma_f16 v225, v224, v224, %[h1] neg_lo:[1,0,0] neg_hi:[1,0,0]\n\t" \
  "v_mfma_f32_16x16x32_bf16 v[188:191], v[216:219], v[112:115], v[188:191]\n\t" \
  "ds_read_b64_tr_b16 v[214:215], v250 offset:13096\n\t" \
  "v_lshlrev_b32_sdwa v179, 1, v179 dst_sel:DWORD dst_unused:UNUSED_PAD src0_sel:DWORD src1_sel:WORD_1\n\t" \
  "v_mfma_f32_16x16x16_bf16 v[68:71], v[246:247], v[244:245], 0\n\t" \
  "ds_read_u16_d16_hi v179, v179 offset:0\n\t" \
  "v_pk_mul_f16 v225, v224, v225\n\t" \
  "v_mfma_f32_16x16x32_bf16 v[196:199], v[216:219], v[124:127], v[196:199]\n\t" \
  "v_lshlrev_b32_sdwa v186, 1, v186 dst_sel:DWORD dst_unused:UNUSED_PAD src0_sel:DWORD src1_sel:WORD_1\n\t" \
  "v_exp_f16 v224, v225\n\t" \
  "v_mfma_f32_16x16x32_bf16 v[204:207], v[216:219], v[136:139], v[204:207]\n\t" \
  "s_waitcnt lgkmcnt(12)\n\t" \
  "ds_read_b64_tr_b16 v[216:217], v250 offset:12384\n\t" \
  "v_lshlrev_b32_sdwa v187, 1, v187 dst_sel:DWORD dst_unused:UNUSED_PAD src0_sel:DWORD src1_sel:WORD_1\n\t" \
  "v_mfma_f32_16x16x32_bf16 v[180:183], v[220:223], v[104:107], v[180:183]\n\t" \
  "ds_read_u16_d16_hi v186, v186 offset:0\n\t" \
  "v_lshlrev_b32_sdwa v192, 1, v192 dst_sel:DWORD dst_unused:UNUSED_PAD src0_sel:DWORD src1_sel:WORD_1\n\t" \
  "v_mfma_f32_16x16x32_bf16 v[188:191], v[220:223], v[116:119], v[188:191]\n\t" \
  "ds_read_b64_tr_b16 v[218:219], v250 offset:13152\n\t" \
  "v_exp_f16_sdwa v224, v225 dst_sel:WORD_1 dst_unused:UNUSED_PRESERVE src0_sel:WORD_1\n\t" \
  "v_mfma_f32_16x16x32_bf16 v[196:199], v[220:223], v[128:131], v[196:199]\n\t" \
  "ds_read_u16_d16_hi v187, v187 offset:0\n\t" \
  "v_lshlrev_b32_sdwa v193, 1, v193 dst_sel:DWORD dst_unused:UNUSED_PAD src0_sel:DWORD src1_sel:WORD_1\n\t" \
  "v_mfma_f32_16x16x32_bf16 v[204:207], v[220:223], v[140:143], v[204:207]\n\t" \
  "s_waitcnt lgkmcnt(10)\n\t" \
  "ds_read_b64_tr_b16 v[220:221], v250 offset:12392\n\t" \
  "v_pk_fma_f16 v224, v224, %[hc], %[hc]\n\t" \
  "v_smfmac_f32_16x16x64_bf16 v[0:3], v[208:211], v[144:151], v248\n\t" \
  "ds_read_u16_d16_hi v192, v192 offset:0\n\t" \
  "v_lshlrev_b32_sdwa v194, 1, v194 dst_sel:DWORD dst_unused:UNUSED_PAD src0_sel:DWORD src1_sel:WORD_1\n\t" \
  "v_smfmac_f32_16x16x64_bf16 v[24:27], v[208:211], v[152:159], v248\n\t" \
  "ds_read_b64_tr_b16 v[222:223], v250 offset:13160\n\t" \
  "v_rcp_f16 v225, v224\n\t" \
  "v_smfmac_f32_16x16x64_bf16 v[48:51], v[208:211], v[160:167], v248\n\t" \
  "ds_read_u16_d16_hi v193, v193 offset:0\n\t" \
  "v_lshlrev_b32_sdwa v195, 1, v195 dst_sel:DWORD dst_unused:UNUSED_PAD src0_sel:DWORD src1_sel:WORD_1\n\t" \
  "v_smfmac_f32_16x16x64_bf16 v[72:75], v[208:211], v[168:175], v248\n\t" \
  "s_waitcnt lgkmcnt(9)\n\t" \
  "ds_read_b64_tr_b16 v[208:209], v250 offset:12448\n\t" \
  "v_lshlrev_b32_sdwa v200, 1, v200 dst_sel:DWORD dst_unused:UNUSED_PAD src0_sel:DWORD src1_sel:WORD_1\n\t" \
  "v_smfmac_f32_16x16x64_bf16 v[4:7], v[212:215], v[144:151], v248\n\t" \
  "ds_read_u16_d16_hi v194, v194 offset:0\n\t" \
  "v_rcp_f16_sdwa v224, v224 dst_sel:DWORD dst_unused:UNUSED_PAD src0_sel:WORD_1\n\t" \
  "v_smfmac_f32_16x16x64_bf16 v[28:31], v[212:215], v[152:159], v248\n\t" \
  "s_add_u32 %[ob], %[ob], %[dob]\n\t" \
  "ds_read_b64_tr_b16 v[210:211], v250 offset:13216\n\t" \
  "v_lshlrev_b32_sdwa v201, 1, v201 dst_sel:DWORD dst_unused:UNUSED_PAD src0_sel:DWORD src1_sel:WORD_1\n\t" \
  "v_smfmac_f32_16x16x64_bf16 v[52:55], v[212:215], v[160:167], v248\n\t" \
  "ds_read_u16_d16_hi v195, v195 offset:0\n\t" \
  "v_fma_mix_f32 v184, v184, v225, 0 op_sel_hi:[0,1,0]\n\t" \
  "v_smfmac_f32_16x16x64_bf16 v[76:79], v[212:215], v[168:175], v248\n\t" \
  "s_waitcnt lgkmcnt(9)\n\t" \
  "ds_read_b64_tr_b16 v[212:213], v250 offset:12456\n\t" \
  "v_lshlrev_b32_sdwa v202, 1, v202 dst_sel:DWORD dst_unused:UNUSED_PAD src0_sel:DWORD src1_sel:WORD_1\n\t" \
  "v_smfmac_f32_16x16x64_bf16 v[8:11], v[216:219], v[144:151], v248\n\t" \
  "ds_read_u16_d16_hi v200, v200 offset:0\n\t" \
  "v_fma_mix_f32 v185, v185, v224, 0 op_sel_hi:[0,1,0]\n\t" \
  "v_smfmac_f32_16x16x64_bf16 v[32:35], v[216:219], v[152:159], v248\n\t" \
  "ds_read_b64_tr_b16 v[214:215], v250 offset:13224\n\t" \
  "v_lshlrev_b32_sdwa v203, 1, v203 dst_sel:DWORD dst_unused:UNUSED_PAD src0_sel:DWORD src1_sel:WORD_1\n\t" \
  "v_smfmac_f32_16x16x64_bf16 v[56:59], v[216:219], v[160:167], v248\n\t" \
  "ds_read_u16_d16_hi v201, v201 offset:0\n\t" \
  "v_mul_f32 v188, %[kk], v188\n\t" \
  "v_smfmac_f32_16x16x64_bf16 v[80:83], v[216:219], v[168:175], v248\n\t" \
  "s_waitcnt lgkmcnt(9)\n\t" \
  "s_barrier\n\t" \
  "ds_read_b64_tr_b16 v[216:217], v249 offset:12288\n\t" \
  "v_lshlrev_b32_sdwa v180, 1, v180 dst_sel:DWORD dst_unused:UNUSED_PAD src0_sel:DWORD src1_sel:WORD_1\n\t" \
  "v_smfmac_f32_16x16x64_bf16 v[12:15], v[220:223], v[144:151], v248\n\t" \
  "ds_read_u16_d16_hi v202, v202 offset:0\n\t" \
  "v_mul_f32 v189, %[kk], v189\n\t" \
  "v_smfmac_f32_16x16x64_bf16 v[36:39], v[220:223], v[152:159], v248\n\t" \
  "ds_read_b64_tr_b16 v[218:219], v249 offset:12544\n\t" \
  "v_cvt_pk_f16_f32 v224, v188, v189\n\t" \
  "v_smfmac_f32_16x16x64_bf16 v[60:63], v[220:223], v[160:167], v248\n\t" \
  "ds_read_u16_d16_hi v203, v203 offset:0\n\t" \
  "v_lshlrev_b32_sdwa v181, 1, v181 dst_sel:DWORD dst_unused:UNUSED_PAD src0_sel:DWORD src1_sel:WORD_1\n\t" \
  "v_smfmac_f32_16x16x64_bf16 v[84:87], v[220:223], v[168:175], v248\n\t" \
  "s_waitcnt lgkmcnt(9)\n\t" \
  "ds_read_b64_tr_b16 v[220:221], v249 offset:14336\n\t" \
  "v_pk_fma_f16 v225, v224, v224, %[h1] neg_lo:[1,0,0] neg_hi:[1,0,0]\n\t" \
  "v_smfmac_f32_16x16x64_bf16 v[16:19], v[208:211], v[144:151], v248\n\t" \
  "s_add_u32 %[rs], %[rs], 1\n\t" \
  "ds_read_u16_d16_hi v180, v180 offset:0\n\t" \
  "v_lshlrev_b32_sdwa v182, 1, v182 dst_sel:DWORD dst_unused:UNUSED_PAD src0_sel:DWORD src1_sel:WORD_1\n\t" \
  "v_smfmac_f32_16x16x64_bf16 v[40:43], v[208:211], v[152:159], v248\n\t" \
  "s_cmp_eq_u32 %[rs], 6\n\t" \
  "ds_read_b64_tr_b16 v[222:223], v249 offset:14592\n\t" \
  "v_pk_mul_f16 v225, v224, v225\n\t" \
  "v_smfmac_f32_16x16x64_bf16 v[64:67], v[208:211], v[160:167], v248\n\t" \
  "ds_read_u16_d16_hi v181, v181 offset:0\n\t" \
  "v_lshlrev_b32_sdwa v183, 1, v183 dst_sel:DWORD dst_unused:UNUSED_PAD src0_sel:DWORD src1_sel:WORD_1\n\t" \
  "v_smfmac_f32_16x16x64_bf16 v[88:91], v[208:211], v[168:175], v248\n\t" \
  "s_waitcnt lgkmcnt(9)\n\t" \
  "ds_read_b64_tr_b16 v[208:209], v249 offset:16384\n\t" \
  "v_exp_f16 v224, v225\n\t" \
  "v_smfmac_f32_16x16x64_bf16 v[20:23], v[212:215], v[144:151], v248\n\t" \
  "s_cselect_b32 %[rs], 0, %[rs]\n\t" \
  "ds_read_u16_d16_hi v182, v182 offset:0\n\t" \
  "v_lshlrev_b32_sdwa v190, 1, v190 dst_sel:DWORD dst_unused:UNUSED_PAD src0_sel:DWORD src1_sel:WORD_1\n\t" \
  "v_smfmac_f32_16x16x64_bf16 v[44:47], v[212:215], v[152:159], v248\n\t" \
  "s_cselect_b32 %[t0], -75920, %[ps]\n\t" \
  "ds_read_b64_tr_b16 v[210:211], v249 offset:16640\n\t" \
  "v_lshlrev_b32_sdwa v191, 1, v191 dst_sel:DWORD dst_unused:UNUSED_PAD src0_sel:DWORD src1_sel:WORD_1\n\t" \
  "v_smfmac_f32_16x16x64_bf16 v[68:71], v[212:215], v[160:167], v248\n\t" \
  "ds_read_u16_d16_hi v183, v183 offset:0\n\t" \
  "v_add_u32 v252, %[t0], v252\n\t" \
  "v_exp_f16_sdwa v224, v225 dst_sel:WORD_1 dst_unused:UNUSED_PRESERVE src0_sel:WORD_1\n\t" \
  "v_smfmac_f32_16x16x64_bf16 v[92:95], v[212:215], v[168:175], v248\n\t" \
  "s_waitcnt lgkmcnt(9)\n\t" \
  "ds_read_b64_tr_b16 v[212:213], v249 offset:12800\n\t" \
  "v_lshlrev_b32_sdwa v196, 1, v196 dst_sel:DWORD dst_unused:UNUSED_PAD src0_sel:DWORD src1_sel:WORD_1\n\t" \
  "v_mfma_f32_16x16x32_bf16 v[144:147], v[216:219], v[96:99], 0\n\t" \
  "ds_read_u16_d16_hi v190, v190 offset:0\n\t" \
  "v_pk_fma_f16 v224, v224, %[hc], %[hc]\n\t" \
  "v_mfma_f32_16x16x32_bf16 v[152:155], v[216:219], v[108:111], 0\n\t" \
  "ds_read_b64_tr_b16 v[214:215], v249 offset:13056\n\t" \
  "v_lshlrev_b32_sdwa v197, 1, v197 dst_sel:DWORD dst_unused:UNUSED_PAD src0_sel:DWORD src1_sel:WORD_1\n\t" \
  "v_mfma_f32_16x16x32_bf16 v[160:163], v[216:219], v[120:123], 0\n\t" \
  "ds_read_u16_d16_hi v191, v191 offset:0\n\t" \
  "v_rcp_f16 v225, v224\n\t" \
  "v_mfma_f32_16x16x32_bf16 v[168:171], v[216:219], v[132:135], 0\n\t" \
  "s_waitcnt lgkmcnt(9)\n\t" \
  "ds_read_b64_tr_b16 v[216:217], v249 offset:14848\n\t" \
  "v_lshlrev_b32_sdwa v198, 1, v198 dst_sel:DWORD dst_unused:UNUSED_PAD src0_sel:DWORD src1_sel:WORD_1\n\t" \
  "v_mfma_f32_16x16x32_bf16 v[144:147], v[220:223], v[100:103], v[144:147]\n\t" \
  "ds_read_u16_d16_hi v196, v196 offset:0\n\t" \
  "v_lshlrev_b32_sdwa v199, 1, v199 dst_sel:DWORD dst_unused:UNUSED_PAD src0_sel:DWORD src1_sel:WORD_1\n\t" \
  "v_mfma_f32_16x16x32_bf16 v[152:155], v[220:223], v[112:115], v[152:155]\n\t" \
  "ds_read_b64_tr_b16 v[218:219], v249 offset:15104\n\t" \
  "v_rcp_f16_sdwa v224, v224 dst_sel:DWORD dst_unused:UNUSED_PAD src0_sel:WORD_1\n\t" \
  "v_mfma_f32_16x16x32_bf16 v[160:163], v[220:223], v[124:127], v[160:163]\n\t" \
  "ds_read_u16_d16_hi v197, v197 offset:0\n\t" \
  "v_lshlrev_b32_sdwa v204, 1, v204 dst_sel:DWORD dst_unused:UNUSED_PAD src0_sel:DWORD src1_sel:WORD_1\n\t" \
  "v_mfma_f32_16x16x32_bf16 v[168:171], v[220:223], v[136:139], v[168:171]\n\t" \
  "s_waitcnt lgkmcnt(9)\n\t" \
  "ds_read_b64_tr_b16 v[220:221], v249 offset:16896\n\t" \
  "ds_read_u16_d16_hi v198, v198 offset:0\n\t" \
  "v_mfma_f32_16x16x32_bf16 v[144:147], v[208:211], v[104:107], v[144:147]\n\t" \
  "ds_read_b64_tr_b16 v[222:223], v249 offset:17152\n\t" \
  "v_lshlrev_b32_sdwa v205, 1, v205 dst_sel:DWORD dst_unused:UNUSED_PAD src0_sel:DWORD src1_sel:WORD_1\n\t" \
  "v_fma_mix_f32 v188, v188, v225, 0 op_sel_hi:[0,1,0]\n\t" \
  "v_mfma_f32_16x16x32_bf16 v[152:155], v[208:211], v[116:119], v[152:155]\n\t" \
  "ds_read_u16_d16_hi v199, v199 offset:0\n\t" \
  "v_fma_mix_f32 v189, v189, v224, 0 op_sel_hi:[0,1,0]\n\t" \
  "v_mfma_f32_16x16x32_bf16 v[160:163], v[208:211], v[128:131], v[160:163]\n\t" \
  "ds_read_u16_d16_hi v204, v204 offset:0\n\t" \
  "v_lshlrev_b32_sdwa v206, 1, v206 dst_sel:DWORD dst_unused:UNUSED_PAD src0_sel:DWORD src1_sel:WORD_1\n\t" \
  "v_mfma_f32_16x16x32_bf16 v[168:171], v[208:211], v[140:143], v[168:171]\n\t" \
  "s_waitcnt lgkmcnt(10)\n\t" \
  "ds_read_b64_tr_b16 v[208:209], v250 offset:18480\n\t" \
  "v_lshlrev_b32_sdwa v207, 1, v207 dst_sel:DWORD dst_unused:UNUSED_PAD src0_sel:DWORD src1_sel:WORD_1\n\t" \
  "v_mfma_f32_16x16x32_bf16 v[148:151], v[212:215], v[96:99], 0\n\t" \
  "ds_read_u16_d16_hi v205, v205 offset:0\n\t" \
  "v_lshlrev_b32_sdwa v144, 1, v144 dst_sel:DWORD dst_unused:UNUSED_PAD src0_sel:DWORD src1_sel:WORD_1\n\t" \
  "v_mfma_f32_16x16x32_bf16 v[156:159], v[212:215], v[108:111], 0\n\t" \
  "ds_read_b64_tr_b16 v[210:211], v250 offset:19248\n\t" \
  "v_mul_f32 v152, %[kk], v152\n\t" \
  "v_mfma_f32_16x16x32_bf16 v[164:167], v[212:215], v[120:123], 0\n\t" \
  "ds_read_u16_d16_hi v206, v206 offset:0\n\t" \
  "v_lshlrev_b32_sdwa v145, 1, v145 dst_sel:DWORD dst_unused:UNUSED_PAD src0_sel:DWORD src1_sel:WORD_1\n\t" \
  "v_mul_f32 v153, %[kk], v153\n\t" \
  "v_mfma_f32_16x16x32_bf16 v[172:175], v[212:215], v[132:135], 0\n\t" \
  "s_waitcnt lgkmcnt(10)\n\t" \
  "ds_read_b64_tr_b16 v[212:213], v250 offset:18488\n\t" \
  "ds_read_u16_d16_hi v207, v207 offset:0\n\t" \
  "v_cvt_pk_f16_f32 v224, v152, v153\n\t" \
  "v_mfma_f32_16x16x32_bf16 v[148:151], v[216:219], v[100:103], v[148:151]\n\t" \
  "ds_read_b64_tr_b16 v[214:215], v250 offset:19256\n\t" \
  "v_lshlrev_b32_sdwa v146, 1, v146 dst_sel:DWORD dst_unused:UNUSED_PAD src0_sel:DWORD src1_sel:WORD_1\n\t" \
  "v_mfma_f32_16x16x32_bf16 v[156:159], v[216:219], v[112:115], v[156:159]\n\t" \
  "ds_read_u16_d16_hi v144, v144 offset:0\n\t" \
  "v_pk_fma_f16 v225, v224, v224, %[h1] neg_lo:[1,0,0] neg_hi:[1,0,0]\n\t" \
  "v_mfma_f32_16x16x32_bf16 v[164:167], v[216:219], v[124:127], v[164:167]\n\t" \
  "ds_read_u16_d16_hi v145, v145 offset:0\n\t" \
  "v_lshlrev_b32_sdwa v147, 1, v147 dst_sel:DWORD dst_unused:UNUSED_PAD src0_sel:DWORD src1_sel:WORD_1\n\t" \
  "v_pk_mul_f16 v225, v224, v225\n\t" \
  "v_mfma_f32_16x16x32_bf16 v[172:175], v[216:219], v[136:139], v[172:175]\n\t" \
  "s_waitcnt lgkmcnt(11)\n\t" \
  "ds_read_b64_tr_b16 v[216:217], v250 offset:18544\n\t" \
  "v_lshlrev_b32_sdwa v154, 1, v154 dst_sel:DWORD dst_unused:UNUSED_PAD src0_sel:DWORD src1_sel:WORD_1\n\t" \
  "v_mfma_f32_16x16x32_bf16 v[148:151], v[220:223], v[104:107], v[148:151]\n\t" \
  "ds_read_u16_d16_hi v146, v146 offset:0\n\t" \
  "v_exp_f16 v224, v225\n\t" \
  "v_mfma_f32_16x16x32_bf16 v[156:159], v[220:223], v[116:119], v[156:159]\n\t" \
  "ds_read_b64_tr_b16 v[218:219], v250 offset:19312\n\t" \
  "v_lshlrev_b32_sdwa v155, 1, v155 dst_sel:DWORD dst_unused:UNUSED_PAD src0_sel:DWORD src1_sel:WORD_1\n\t" \
  "v_mfma_f32_16x16x32_bf16 v[164:167], v[220:223], v[128:131], v[164:167]\n\t" \
  "ds_read_u16_d16_hi v147, v147 offset:0\n\t" \
  "v_lshlrev_b32_sdwa v160, 1, v160 dst_sel:DWORD dst_unused:UNUSED_PAD src0_sel:DWORD src1_sel:WORD_1\n\t" \
  "v_mfma_f32_16x16x32_bf16 v[172:175], v[220:223], v[140:143], v[172:175]\n\t" \
  "s_waitcnt lgkmcnt(10)\n\t" \
  "ds_read_b64_tr_b16 v[220:221], v250 offset:18552\n\t" \
  "ds_read_u16_d16_hi v154, v154 offset:0\n\t" \
  "v_exp_f16_sdwa v224, v225 dst_sel:WORD_1 dst_unused:UNUSED_PRESERVE src0_sel:WORD_1\n\t" \
  "v_smfmac_f32_16x16x64_bf16 v[0:3], v[208:211], v[176:183], v248\n\t" \
  "ds_read_b64_tr_b16 v[222:223], v250 offset:19320\n\t" \
  "v_lshlrev_b32_sdwa v161, 1, v161 dst_sel:DWORD dst_unused:UNUSED_PAD src0_sel:DWORD src1_sel:WORD_1\n\t" \
  "v_smfmac_f32_16x16x64_bf16 v[24:27], v[208:211], v[184:191], v248\n\t" \
  "ds_read_u16_d16_hi v155, v155 offset:0\n\t" \
  "v_pk_fma_f16 v224, v224, %[hc], %[hc]\n\t" \
  "v_smfmac_f32_16x16x64_bf16 v[48:51], v[208:211], v[192:199], v248\n\t" \
  "s_waitcnt lgkmcnt(11)\n\t" \
  "ds_read_u16_d16_hi v160, v160 offset:0\n\t" \
  "v_lshlrev_b32_sdwa v162, 1, v162 dst_sel:DWORD dst_unused:UNUSED_PAD src0_sel:DWORD src1_sel:WORD_1\n\t" \
  "v_smfmac_f32_16x16x64_bf16 v[72:75], v[208:211], v[200:207], v248\n\t" \
  "s_waitcnt lgkmcnt(11)\n\t" \
  "ds_read_b64_tr_b16 v[208:209], v250 offset:18608\n\t" \
  "v_rcp_f16 v225, v224\n\t" \
  "v_smfmac_f32_16x16x64_bf16 v[4:7], v[212:215], v[176:183], v248\n\t" \
  "ds_read_u16_d16_hi v161, v161 offset:0\n\t" \
  "v_lshlrev_b32_sdwa v163, 1, v163 dst_sel:DWORD dst_unused:UNUSED_PAD src0_sel:DWORD src1_sel:WORD_1\n\t" \
  "v_smfmac_f32_16x16x64_bf16 v[28:31], v[212:215], v[184:191], v248\n\t" \
  "ds_read_b64_tr_b16 v[210:211], v250 offset:19376\n\t" \
  "v_lshlrev_b32_sdwa v168, 1, v168 dst_sel:DWORD dst_unused:UNUSED_PAD src0_sel:DWORD src1_sel:WORD_1\n\t" \
  "v_smfmac_f32_16x16x64_bf16 v[52:55], v[212:215], v[192:199], v248\n\t" \
  "ds_read_u16_d16_hi v162, v162 offset:0\n\t" \
  "v_rcp_f16_sdwa v224, v224 dst_sel:DWORD dst_unused:UNUSED_PAD src0_sel:WORD_1\n\t" \
  "v_smfmac_f32_16x16x64_bf16 v[76:79], v[212:215], v[200:207], v248\n\t" \
  "s_waitcnt lgkmcnt(10)\n\t" \
  "ds_read_b64_tr_b16 v[212:213], v250 offset:18616\n\t" \
  "v_lshlrev_b32_sdwa v169, 1, v169 dst_sel:DWORD dst_unused:UNUSED_PAD src0_sel:DWORD src1_sel:WORD_1\n\t" \
  "v_smfmac_f32_16x16x64_bf16 v[8:11], v[216:219], v[176:183], v248\n\t" \
  "ds_read_u16_d16_hi v163, v163 offset:0\n\t" \
  "v_fma_mix_f32 v152, v152, v225, 0 op_sel_hi:[0,1,0]\n\t" \
  "v_smfmac_f32_16x16x64_bf16 v[32:35], v[216:219], v[184:191], v248\n\t" \
  "ds_read_b64_tr_b16 v[214:215], v250 offset:19384\n\t" \
  "v_lshlrev_b32_sdwa v170, 1, v170 dst_sel:DWORD dst_unused:UNUSED_PAD src0_sel:DWORD src1_sel:WORD_1\n\t" \
  "v_smfmac_f32_16x16x64_bf16 v[56:59], v[216:219], v[192:199], v248\n\t" \
  "ds_read_u16_d16_hi v168, v168 offset:0\n\t" \
  "v_fma_mix_f32 v153, v153, v224, 0 op_sel_hi:[0,1,0]\n\t" \
  "v_smfmac_f32_16x16x64_bf16 v[80:83], v[216:219], v[200:207], v248\n\t" \
  "s_waitcnt lgkmcnt(10)\n\t" \
  "s_barrier\n\t" \
  "ds_read_b64_tr_b16 v[216:217], v249 offset:0\n\t" \
  "ds_read_b128 v[132:135], v251 offset:0\n\t" \
  "v_lshlrev_b32_sdwa v171, 1, v171 dst_sel:DWORD dst_unused:UNUSED_PAD src0_sel:DWORD src1_sel:WORD_1\n\t" \
  "v_smfmac_f32_16x16x64_bf16 v[12:15], v[220:223], v[176:183], v248\n\t" \
  "ds_read_b64_tr_b16 v[218:219], v249 offset:256\n\t" \
  "ds_read_b128 v[136:139], v251 offset:4096\n\t" \
  "v_mul_f32 v156, %[kk], v156\n\t" \
  "v_smfmac_f32_16x16x64_bf16 v[36:39], v[220:223], v[184:191], v248\n\t" \
  "ds_read_b128 v[140:143], v251 offset:8192\n\t" \
  "v_lshlrev_b32_sdwa v148, 1, v148 dst_sel:DWORD dst_unused:UNUSED_PAD src0_sel:DWORD src1_sel:WORD_1\n\t" \
  "v_mul_f32 v157, %[kk], v157\n\t" \
  "v_smfmac_f32_16x16x64_bf16 v[60:63], v[220:223], v[192:199], v248\n\t" \
  "ds_read_u16_d16_hi v169, v169 offset:0\n\t" \
  "v_cvt_pk_f16_f32 v224, v156, v157\n\t" \
  "v_smfmac_f32_16x16x64_bf16 v[84:87], v[220:223], v[200:207], v248\n\t" \
  "s_waitcnt lgkmcnt(11)\n\t" \
  "ds_read_b64_tr_b16 v[220:221], v249 offset:2048\n\t" \
  "ds_read_u16_d16_hi v170, v170 offset:0\n\t" \
  "v_lshlrev_b32_sdwa v149, 1, v149 dst_sel:DWORD dst_unused:UNUSED_PAD src0_sel:DWORD src1_sel:WORD_1\n\t" \
  "v_smfmac_f32_16x16x64_bf16 v[16:19], v[208:211], v[176:183], v248\n\t" \
  "ds_read_b64_tr_b16 v[222:223], v249 offset:2304\n\t" \
  "v_pk_fma_f16 v225, v224, v224, %[h1] neg_lo:[1,0,0] neg_hi:[1,0,0]\n\t" \
  "v_smfmac_f32_16x16x64_bf16 v[40:43], v[208:211], v[184:191], v248\n\t" \
  "ds_read_u16_d16_hi v171, v171 offset:0\n\t" \
  "v_lshlrev_b32_sdwa v150, 1, v150 dst_sel:DWORD dst_unused:UNUSED_PAD src0_sel:DWORD src1_sel:WORD_1\n\t" \
  "v_pk_mul_f16 v225, v224, v225\n\t" \
  "v_smfmac_f32_16x16x64_bf16 v[64:67], v[208:211], v[192:199], v248\n\t" \
  "ds_read_u16_d16_hi v148, v148 offset:0\n\t" \
  "v_lshlrev_b32_sdwa v151, 1, v151 dst_sel:DWORD dst_unused:UNUSED_PAD src0_sel:DWORD src1_sel:WORD_1\n\t" \
  "v_smfmac_f32_16x16x64_bf16 v[88:91], v[208:211], v[200:207], v248\n\t" \
  "s_waitcnt lgkmcnt(12)\n\t" \
  "ds_read_b64_tr_b16 v[208:209], v249 offset:4096\n\t" \
  "ds_read_u16_d16_hi v149, v149 offset:0\n\t" \
  "v_exp_f16 v224, v225\n\t" \
  "v_smfmac_f32_16x16x64_bf16 v[20:23], v[212:215], v[176:183], v248\n\t" \
  "ds_read_b64_tr_b16 v[210:211], v249 offset:4352\n\t" \
  "v_lshlrev_b32_sdwa v158, 1, v158 dst_sel:DWORD dst_unused:UNUSED_PAD src0_sel:DWORD src1_sel:WORD_1\n\t" \
  "v_smfmac_f32_16x16x64_bf16 v[44:47], v[212:215], v[184:191], v248\n\t" \
  "ds_read_u16_d16_hi v150, v150 offset:0\n\t" \
  "v_lshlrev_b32_sdwa v159, 1, v159 dst_sel:DWORD dst_unused:UNUSED_PAD src0_sel:DWORD src1_sel:WORD_1\n\t" \
  "v_smfmac_f32_16x16x64_bf16 v[68:71], v[212:215], v[192:199], v248\n\t" \
  "ds_read_u16_d16_hi v151, v151 offset:0\n\t" \
  "v_exp_f16_sdwa v224, v225 dst_sel:WORD_1 dst_unused:UNUSED_PRESERVE src0_sel:WORD_1\n\t" \
  "v_smfmac_f32_16x16x64_bf16 v[92:95], v[212:215], v[200:207], v248\n\t" \
  "s_waitcnt lgkmcnt(13)\n\t" \
  "ds_read_b64_tr_b16 v[212:213], v249 offset:512\n\t" \
  "v_lshlrev_b32_sdwa v164, 1, v164 dst_sel:DWORD dst_unused:UNUSED_PAD src0_sel:DWORD src1_sel:WORD_1\n\t" \
  "v_mfma_f32_16x16x32_bf16 v[176:179], v[216:219], v[96:99], 0\n\t" \
  "ds_read_u16_d16_hi v158, v158 offset:0\n\t" \
  "v_pk_fma_f16 v224, v224, %[hc], %[hc]\n\t" \
  "v_mfma_f32_16x16x32_bf16 v[184:187], v[216:219], v[108:111], 0\n\t" \
  "ds_read_b64_tr_b16 v[214:215], v249 offset:768\n\t" \
  "v_lshlrev_b32_sdwa v165, 1, v165 dst_sel:DWORD dst_unused:UNUSED_PAD src0_sel:DWORD src1_sel:WORD_1\n\t" \
  "v_mfma_f32_16x16x32_bf16 v[192:195], v[216:219], v[120:123], 0\n\t" \
  "ds_read_u16_d16_hi v159, v159 offset:0\n\t" \
  "v_rcp_f16 v225, v224\n\t" \
  "v_mfma_f32_16x16x32_bf16 v[200:203], v[216:219], v[132:135], 0\n\t" \
  "s_waitcnt lgkmcnt(11)\n\t" \
  "ds_read_b64_tr_b16 v[216:217], v249 offset:2560\n\t" \
  "v_lshlrev_b32_sdwa v166, 1, v166 dst_sel:DWORD dst_unused:UNUSED_PAD src0_sel:DWORD src1_sel:WORD_1\n\t" \
  "v_mfma_f32_16x16x32_bf16 v[176:179], v[220:223], v[100:103], v[176:179]\n\t" \
  "ds_read_u16_d16_hi v164, v164 offset:0\n\t" \
  "v_lshlrev_b32_sdwa v167, 1, v167 dst_sel:DWORD dst_unused:UNUSED_PAD src0_sel:DWORD src1_sel:WORD_1\n\t" \
  "v_mfma_f32_16x16x32_bf16 v[184:187], v[220:223], v[112:115], v[184:187]\n\t" \
  "ds_read_b64_tr_b16 v[218:219], v249 offset:2816\n\t" \
  "v_rcp_f16_sdwa v224, v224 dst_sel:DWORD dst_unused:UNUSED_PAD src0_sel:WORD_1\n\t" \
  "v_mfma_f32_16x16x32_bf16 v[192:195], v[220:223], v[124:127], v[192:195]\n\t" \
  "ds_read_u16_d16_hi v165, v165 offset:0\n\t" \
  "v_lshlrev_b32_sdwa v172, 1, v172 dst_sel:DWORD dst_unused:UNUSED_PAD src0_sel:DWORD src1_sel:WORD_1\n\t" \
  "v_mfma_f32_16x16x32_bf16 v[200:203], v[220:223], v[136:139], v[200:203]\n\t" \
  "s_waitcnt lgkmcnt(10)\n\t" \
  "ds_read_b64_tr_b16 v[220:221], v249 offset:4608\n\t" \
  "ds_read_u16_d16_hi v166, v166 offset:0\n\t" \
  "v_mfma_f32_16x16x32_bf16 v[176:179], v[208:211], v[104:107], v[176:179]\n\t" \
  "ds_read_b64_tr_b16 v[222:223], v249 offset:4864\n\t" \
  "v_lshlrev_b32_sdwa v173, 1, v173 dst_sel:DWORD dst_unused:UNUSED_PAD src0_sel:DWORD src1_sel:WORD_1\n\t" \
  "v_fma_mix_f32 v156, v156, v225, 0 op_sel_hi:[0,1,0]\n\t" \
  "v_mfma_f32_16x16x32_bf16 v[184:187], v[208:211], v[116:119], v[184:187]\n\t" \
  "ds_read_u16_d16_hi v167, v167 offset:0\n\t" \
  "v_fma_mix_f32 v157, v157, v224, 0 op_sel_hi:[0,1,0]\n\t" \
  "v_mfma_f32_16x16x32_bf16 v[192:195], v[208:211], v[128:131], v[192:195]\n\t" \
  "ds_read_u16_d16_hi v172, v172 offset:0\n\t" \
  "v_lshlrev_b32_sdwa v174, 1, v174 dst_sel:DWORD dst_unused:UNUSED_PAD src0_sel:DWORD src1_sel:WORD_1\n\t" \
  "v_mfma_f32_16x16x32_bf16 v[200:203], v[208:211], v[140:143], v[200:203]\n\t" \
  "s_waitcnt lgkmcnt(10)\n\t" \
  "ds_read_b64_tr_b16 v[208:209], v250 offset:0\n\t" \
  "v_lshlrev_b32_sdwa v175, 1, v175 dst_sel:DWORD dst_unused:UNUSED_PAD src0_sel:DWORD src1_sel:WORD_1\n\t" \
  "v_mfma_f32_16x16x32_bf16 v[180:183], v[212:215], v[96:99], 0\n\t" \
  "ds_read_u16_d16_hi v173, v173 offset:0\n\t" \
  "v_lshlrev_b32_sdwa v176, 1, v176 dst_sel:DWORD dst_unused:UNUSED_PAD src0_sel:DWORD src1_sel:WORD_1\n\t" \
  "v_mfma_f32_16x16x32_bf16 v[188:191], v[212:215], v[108:111], 0\n\t" \
  "ds_read_b64_tr_b16 v[210:211], v250 offset:768\n\t" \
  "v_mul_f32 v184, %[kk], v184\n\t" \
  "v_mfma_f32_16x16x32_bf16 v[196:199], v[212:215], v[120:123], 0\n\t" \
  "ds_read_u16_d16_hi v174, v174 offset:0\n\t" \
  "v_lshlrev_b32_sdwa v177, 1, v177 dst_sel:DWORD dst_unused:UNUSED_PAD src0_sel:DWORD src1_sel:WORD_1\n\t" \
  "v_mul_f32 v185, %[kk], v185\n\t" \
  "v_mfma_f32_16x16x32_bf16 v[204:207], v[212:215], v[132:135], 0\n\t" \
  "s_waitcnt lgkmcnt(10)\n\t" \
  "ds_read_b64_tr_b16 v[212:213], v250 offset:8\n\t" \
  "ds_read_u16_d16_hi v175, v175 offset:0\n\t" \
  "v_cvt_pk_f16_f32 v224, v184, v185\n\t" \
  "v_mfma_f32_16x16x32_bf16 v[180:183], v[216:219], v[100:103], v[180:183]\n\t" \
  "ds_read_b64_tr_b16 v[214:215], v250 offset:776\n\t" \
  "v_lshlrev_b32_sdwa v178, 1, v178 dst_sel:DWORD dst_unused:UNUSED_PAD src0_sel:DWORD src1_sel:WORD_1\n\t" \
  "v_mfma_f32_16x16x32_bf16 v[188:191], v[216:219], v[112:115], v[188:191]\n\t" \
  "ds_read_u16_d16_hi v176, v176 offset:0\n\t" \
  "v_pk_fma_f16 v225, v224, v224, %[h1] neg_lo:[1,0,0] neg_hi:[1,0,0]\n\t" \
  "v_mfma_f32_16x16x32_bf16 v[196:199], v[216:219], v[124:127], v[196:199]\n\t" \
  "ds_read_u16_d16_hi v177, v177 offset:0\n\t" \
  "v_lshlrev_b32_sdwa v179, 1, v179 dst_sel:DWORD dst_unused:UNUSED_PAD src0_sel:DWORD src1_sel:WORD_1\n\t" \
  "v_pk_mul_f16 v225, v224, v225\n\t" \
  "v_mfma_f32_16x16x32_bf16 v[204:207], v[216:219], v[136:139], v[204:207]\n\t" \
  "s_waitcnt lgkmcnt(11)\n\t" \
  "ds_read_b64_tr_b16 v[216:217], v250 offset:64\n\t" \
  "v_lshlrev_b32_sdwa v186, 1, v186 dst_sel:DWORD dst_unused:UNUSED_PAD src0_sel:DWORD src1_sel:WORD_1\n\t" \
  "v_mfma_f32_16x16x32_bf16 v[180:183], v[220:223], v[104:107], v[180:183]\n\t" \
  "ds_read_u16_d16_hi v178, v178 offset:0\n\t" \
  "v_exp_f16 v224, v225\n\t" \
  "v_mfma_f32_16x16x32_bf16 v[188:191], v[220:223], v[116:119], v[188:191]\n\t" \
  "ds_read_b64_tr_b16 v[218:219], v250 offset:832\n\t" \
  "v_lshlrev_b32_sdwa v187, 1, v187 dst_sel:DWORD dst_unused:UNUSED_PAD src0_sel:DWORD src1_sel:WORD_1\n\t" \
  "v_mfma_f32_16x16x32_bf16 v[196:199], v[220:223], v[128:131], v[196:199]\n\t" \
  "ds_read_u16_d16_hi v179, v179 offset:0\n\t" \
  "v_lshlrev_b32_sdwa v192, 1, v192 dst_sel:DWORD dst_unused:UNUSED_PAD src0_sel:DWORD src1_sel:WORD_1\n\t" \
  "v_mfma_f32_16x16x32_bf16 v[204:207], v[220:223], v[140:143], v[204:207]\n\t" \
  "s_waitcnt lgkmcnt(10)\n\t" \
  "ds_read_b64_tr_b16 v[220:221], v250 offset:72\n\t" \
  "ds_read_u16_d16_hi v186, v186 offset:0\n\t" \
  "v_exp_f16_sdwa v224, v225 dst_sel:WORD_1 dst_unused:UNUSED_PRESERVE src0_sel:WORD_1\n\t" \
  "v_smfmac_f32_16x16x64_bf16 v[0:3], v[208:211], v[144:151], v248\n\t" \
  "ds_read_b64_tr_b16 v[222:223], v250 offset:840\n\t" \
  "v_lshlrev_b32_sdwa v193, 1, v193 dst_sel:DWORD dst_unused:UNUSED_PAD src0_sel:DWORD src1_sel:WORD_1\n\t" \
  "v_smfmac_f32_16x16x64_bf16 v[24:27], v[208:211], v[152:159], v248\n\t" \
  "ds_read_u16_d16_hi v187, v187 offset:0\n\t" \
  "v_pk_fma_f16 v224, v224, %[hc], %[hc]\n\t" \
  "v_smfmac_f32_16x16x64_bf16 v[48:51], v[208:211], v[160:167], v248\n\t" \
  "s_waitcnt lgkmcnt(11)\n\t" \
  "ds_read_u16_d16_hi v192, v192 offset:0\n\t" \
  "v_lshlrev_b32_sdwa v194, 1, v194 dst_sel:DWORD dst_unused:UNUSED_PAD src0_sel:DWORD src1_sel:WORD_1\n\t" \
  "v_smfmac_f32_16x16x64_bf16 v[72:75], v[208:211], v[168:175], v248\n\t" \
  "s_waitcnt lgkmcnt(11)\n\t" \
  "ds_read_b64_tr_b16 v[208:209], v250 offset:128\n\t" \
  "v_rcp_f16 v225, v224\n\t" \
  "v_smfmac_f32_16x16x64_bf16 v[4:7], v[212:215], v[144:151], v248\n\t" \
  "ds_read_b64 v[242:243], v252 offset:128\n\t" \
  "v_lshlrev_b32_sdwa v195, 1, v195 dst_sel:DWORD dst_unused:UNUSED_PAD src0_sel:DWORD src1_sel:WORD_1\n\t" \
  "v_smfmac_f32_16x16x64_bf16 v[28:31], v[212:215], v[152:159], v248\n\t" \
  "ds_read_b64_tr_b16 v[210:211], v250 offset:896\n\t" \
  "ds_read_b64 v[244:245], v252 offset:136\n\t" \
  "v_smfmac_f32_16x16x64_bf16 v[52:55], v[212:215], v[160:167], v248\n\t" \
  "ds_read_u16_d16_hi v193, v193 offset:0\n\t" \
  "v_lshlrev_b32_sdwa v200, 1, v200 dst_sel:DWORD dst_unused:UNUSED_PAD src0_sel:DWORD src1_sel:WORD_1\n\t" \
  "v_rcp_f16_sdwa v224, v224 dst_sel:DWORD dst_unused:UNUSED_PAD src0_sel:WORD_1\n\t" \
  "v_smfmac_f32_16x16x64_bf16 v[76:79], v[212:215], v[168:175], v248\n\t" \
  "s_waitcnt lgkmcnt(11)\n\t" \
  "ds_read_b64_tr_b16 v[212:213], v250 offset:136\n\t" \
  "ds_read_u16_d16_hi v194, v194 offset:0\n\t" \
  "v_smfmac_f32_16x16x64_bf16 v[8:11], v[216:219], v[144:151], v248\n\t" \
  "ds_read_b64_tr_b16 v[214:215], v250 offset:904\n\t" \
  "v_lshlrev_b32_sdwa v201, 1, v201 dst_sel:DWORD dst_unused:UNUSED_PAD src0_sel:DWORD src1_sel:WORD_1\n\t" \
  "v_smfmac_f32_16x16x64_bf16 v[32:35], v[216:219], v[152:159], v248\n\t" \
  "ds_read_u16_d16_hi v195, v195 offset:0\n\t" \
  "v_fma_mix_f32 v184, v184, v225, 0 op_sel_hi:[0,1,0]\n\t" \
  "v_cvt_pk_bf16_f32 v230, v72, v73\n\t" \
  "v_smfmac_f32_16x16x64_bf16 v[56:59], v[216:219], v[160:167], v248\n\t" \
  "ds_read_u16_d16_hi v200, v200 offset:0\n\t" \
  "v_lshlrev_b32_sdwa v202, 1, v202 dst_sel:DWORD dst_unused:UNUSED_PAD src0_sel:DWORD src1_sel:WORD_1\n\t" \
  "v_fma_mix_f32 v185, v185, v224, 0 op_sel_hi:[0,1,0]\n\t" \
  "v_cvt_pk_bf16_f32 v231, v74, v75\n\t" \
  "v_smfmac_f32_16x16x64_bf16 v[80:83], v[216:219], v[168:175], v248\n\t" \
  "s_waitcnt lgkmcnt(12)\n\t" \
  "s_barrier\n\t" \
  "ds_read_b64_tr_b16 v[216:217], v249 offset:6144\n\t" \
  "v_lshlrev_b32_sdwa v203, 1, v203 dst_sel:DWORD dst_unused:UNUSED_PAD src0_sel:DWORD src1_sel:WORD_1\n\t" \
  "v_cvt_pk_bf16_f32 v232, v76, v77\n\t" \
  "v_smfmac_f32_16x16x64_bf16 v[12:15], v[220:223], v[144:151], v248\n\t" \
  "ds_read_u16_d16_hi v201, v201 offset:0\n\t" \
  "v_mul_f32 v188, %[kk], v188\n\t" \
  "v_cvt_pk_bf16_f32 v233, v78, v79\n\t" \
  "v_smfmac_f32_16x16x64_bf16 v[36:39], v[220:223], v[152:159], v248\n\t" \
  "ds_read_b64_tr_b16 v[218:219], v249 offset:6400\n\t" \
  "buffer_store_dwordx4 v[230:233], v253, %[osrd], %[ob] offen offset:0 sc1\n\t" \
  "v_lshlrev_b32_sdwa v180, 1, v180 dst_sel:DWORD dst_unused:UNUSED_PAD src0_sel:DWORD src1_sel:WORD_1\n\t" \
  "v_mul_f32 v189, %[kk], v189\n\t" \
  "v_smfmac_f32_16x16x64_bf16 v[60:63], v[220:223], v[160:167], v248\n\t" \
  "ds_read_b64 v[230:231], v252 offset:0\n\t" \
  "ds_read_b64 v[232:233], v252 offset:8\n\t" \
  "v_cvt_pk_f16_f32 v224, v188, v189\n\t" \
  "v_smfmac_f32_16x16x64_bf16 v[84:87], v[220:223], v[168:175], v248\n\t" \
  "s_waitcnt lgkmcnt(12)\n\t" \
  "ds_read_b64_tr_b16 v[220:221], v249 offset:8192\n\t" \
  "ds_read_u16_d16_hi v202, v202 offset:0\n\t" \
  "v_lshlrev_b32_sdwa v181, 1, v181 dst_sel:DWORD dst_unused:UNUSED_PAD src0_sel:DWORD src1_sel:WORD_1\n\t" \
  "v_smfmac_f32_16x16x64_bf16 v[16:19], v[208:211], v[144:151], v248\n\t" \
  "ds_read_b64_tr_b16 v[222:223], v249 offset:8448\n\t" \
  "v_pk_fma_f16 v225, v224, v224, %[h1] neg_lo:[1,0,0] neg_hi:[1,0,0]\n\t" \
  "v_smfmac_f32_16x16x64_bf16 v[40:43], v[208:211], v[152:159], v248\n\t" \
  "ds_read_u16_d16_hi v203, v203 offset:0\n\t" \
  "v_lshlrev_b32_sdwa v182, 1, v182 dst_sel:DWORD dst_unused:UNUSED_PAD src0_sel:DWORD src1_sel:WORD_1\n\t" \
  "v_pk_mul_f16 v225, v224, v225\n\t" \
  "v_cvt_pk_bf16_f32 v234, v80, v81\n\t" \
  "v_smfmac_f32_16x16x64_bf16 v[64:67], v[208:211], v[160:167], v248\n\t" \
  "ds_read_u16_d16_hi v180, v180 offset:0\n\t" \
  "v_lshlrev_b32_sdwa v183, 1, v183 dst_sel:DWORD dst_unused:UNUSED_PAD src0_sel:DWORD src1_sel:WORD_1\n\t" \
  "v_cvt_pk_bf16_f32 v235, v82, v83\n\t" \
  "v_smfmac_f32_16x16x64_bf16 v[88:91], v[208:211], v[168:175], v248\n\t" \
  "s_waitcnt lgkmcnt(12)\n\t" \
  "ds_read_b64_tr_b16 v[208:209], v249 offset:10240\n\t" \
  "ds_read_u16_d16_hi v181, v181 offset:0\n\t" \
  "v_exp_f16 v224, v225\n\t" \
  "v_cvt_pk_bf16_f32 v236, v84, v85\n\t" \
  "v_smfmac_f32_16x16x64_bf16 v[20:23], v[212:215], v[144:151], v248\n\t" \
  "ds_read_b64_tr_b16 v[210:211], v249 offset:10496\n\t" \
  "v_lshlrev_b32_sdwa v190, 1, v190 dst_sel:DWORD dst_unused:UNUSED_PAD src0_sel:DWORD src1_sel:WORD_1\n\t" \
  "v_cvt_pk_bf16_f32 v237, v86, v87\n\t" \
  "v_smfmac_f32_16x16x64_bf16 v[44:47], v[212:215], v[152:159], v248\n\t" \
  "buffer_store_dwordx4 v[234:237], v253, %[osrd], %[ob] offen offset:64 sc1\n\t" \
  "ds_read_u16_d16_hi v182, v182 offset:0\n\t" \
  "v_smfmac_f32_16x16x64_bf16 v[68:71], v[212:215], v[160:167], v248\n\t" \
  "ds_read_b64 v[234:235], v252 offset:64\n\t" \
  "v_lshlrev_b32_sdwa v191, 1, v191 dst_sel:DWORD dst_unused:UNUSED_PAD src0_sel:DWORD src1_sel:WORD_1\n\t" \
  "v_exp_f16_sdwa v224, v225 dst_sel:WORD_1 dst_unused:UNUSED_PRESERVE src0_sel:WORD_1\n\t" \
  "v_smfmac_f32_16x16x64_bf16 v[92:95], v[212:215], v[168:175], v248\n\t" \
  "s_waitcnt lgkmcnt(11)\n\t" \
  "ds_read_b64_tr_b16 v[212:213], v249 offset:6656\n\t" \
  "ds_read_b64 v[236:237], v252 offset:72\n\t" \
  "v_mfma_f32_16x16x16_bf16 v[72:75], v[246:247], v[230:231], 0\n\t" \
  "ds_read_b64_tr_b16 v[214:215], v249 offset:6912\n\t" \
  "v_lshlrev_b32_sdwa v196, 1, v196 dst_sel:DWORD dst_unused:UNUSED_PAD src0_sel:DWORD src1_sel:WORD_1\n\t" \
  "v_mfma_f32_16x16x32_bf16 v[144:147], v[216:219], v[96:99], 0\n\t" \
  "ds_read_u16_d16_hi v183, v183 offset:0\n\t" \
  "v_pk_fma_f16 v224, v224, %[hc], %[hc]\n\t" \
  "v_mfma_f32_16x16x32_bf16 v[152:155], v[216:219], v[108:111], 0\n\t" \
  "ds_read_u16_d16_hi v190, v190 offset:0\n\t" \
  "v_lshlrev_b32_sdwa v197, 1, v197 dst_sel:DWORD dst_unused:UNUSED_PAD src0_sel:DWORD src1_sel:WORD_1\n\t" \
  "v_cvt_pk_bf16_f32 v238, v88, v89\n\t" \
  "v_mfma_f32_16x16x32_bf16 v[160:163], v[216:219], v[120:123], 0\n\t" \
  "s_waitcnt lgkmcnt(14)\n\t" \
  "ds_read_u16_d16_hi v191, v191 offset:0\n\t" \
  "v_rcp_f16 v225, v224\n\t" \
  "v_mfma_f32_16x16x16_bf16 v[76:79], v[246:247], v[232:233], 0\n\t" \
  "ds_read_u16_d16_hi v196, v196 offset:0\n\t" \
  "v_cvt_pk_bf16_f32 v239, v90, v91\n\t" \
  "v_mfma_f32_16x16x32_bf16 v[168:171], v[216:219], v[132:135], 0\n\t" \
  "s_waitcnt lgkmcnt(14)\n\t" \
  "ds_read_b64_tr_b16 v[216:217], v249 offset:8704\n\t" \
  "v_lshlrev_b32_sdwa v198, 1, v198 dst_sel:DWORD dst_unused:UNUSED_PAD src0_sel:DWORD src1_sel:WORD_1\n\t" \
  "v_cvt_pk_bf16_f32 v240, v92, v93\n\t" \
  "v_mfma_f32_16x16x32_bf16 v[144:147], v[220:223], v[100:103], v[144:147]\n\t" \
  "ds_read_u16_d16_hi v197, v197 offset:0\n\t" \
  "v_cvt_pk_bf16_f32 v241, v94, v95\n\t" \
  "v_mfma_f32_16x16x32_bf16 v[152:155], v[220:223], v[112:115], v[152:155]\n\t" \
  "ds_read_b64_tr_b16 v[218:219], v249 offset:8960\n\t" \
  "v_lshlrev_b32_sdwa v199, 1, v199 dst_sel:DWORD dst_unused:UNUSED_PAD src0_sel:DWORD src1_sel:WORD_1\n\t" \
  "v_rcp_f16_sdwa v224, v224 dst_sel:DWORD dst_unused:UNUSED_PAD src0_sel:WORD_1\n\t" \
  "v_mfma_f32_16x16x32_bf16 v[160:163], v[220:223], v[124:127], v[160:163]\n\t" \
  "buffer_store_dwordx4 v[238:241], v253, %[osrd], %[ob] offen offset:128 sc1\n\t" \
  "v_lshlrev_b32_sdwa v204, 1, v204 dst_sel:DWORD dst_unused:UNUSED_PAD src0_sel:DWORD src1_sel:WORD_1\n\t" \
  "v_mfma_f32_16x16x32_bf16 v[168:171], v[220:223], v[136:139], v[168:171]\n\t" \
  "s_waitcnt lgkmcnt(10)\n\t" \
  "ds_read_b64_tr_b16 v[220:221], v249 offset:10752\n\t" \
  "ds_read_u16_d16_hi v198, v198 offset:0\n\t" \
  "v_mfma_f32_16x16x16_bf16 v[80:83], v[246:247], v[234:235], 0\n\t" \
  "ds_read_b64_tr_b16 v[222:223], v249 offset:11008\n\t" \
  "v_lshlrev_b32_sdwa v205, 1, v205 dst_sel:DWORD dst_unused:UNUSED_PAD src0_sel:DWORD src1_sel:WORD_1\n\t" \
  "v_mfma_f32_16x16x32_bf16 v[144:147], v[208:211], v[104:107], v[144:147]\n\t" \
  "ds_read_u16_d16_hi v199, v199 offset:0\n\t" \
  "v_fma_mix_f32 v188, v188, v225, 0 op_sel_hi:[0,1,0]\n\t" \
  "v_mfma_f32_16x16x32_bf16 v[152:155], v[208:211], v[116:119], v[152:155]\n\t" \
  "ds_read_u16_d16_hi v204, v204 offset:0\n\t" \
  "v_fma_mix_f32 v189, v189, v224, 0 op_sel_hi:[0,1,0]\n\t" \
  "v_mfma_f32_16x16x32_bf16 v[160:163], v[208:211], v[128:131], v[160:163]\n\t" \
  "s_waitcnt lgkmcnt(13)\n\t" \
  "ds_read_u16_d16_hi v205, v205 offset:0\n\t" \
  "v_lshlrev_b32_sdwa v206, 1, v206 dst_sel:DWORD dst_unused:UNUSED_PAD src0_sel:DWORD src1_sel:WORD_1\n\t" \
  "v_mfma_f32_16x16x16_bf16 v[84:87], v[246:247], v[236:237], 0\n\t" \
  "ds_read_u16_d16_hi v206, v206 offset:0\n\t" \
  "v_lshlrev_b32_sdwa v207, 1, v207 dst_sel:DWORD dst_unused:UNUSED_PAD src0_sel:DWORD src1_sel:WORD_1\n\t" \
  "v_mfma_f32_16x16x32_bf16 v[168:171], v[208:211], v[140:143], v[168:171]\n\t" \
  "s_waitcnt lgkmcnt(14)\n\t" \
  "ds_read_b64_tr_b16 v[208:209], v250 offset:6160\n\t" \
  "v_lshlrev_b32_sdwa v144, 1, v144 dst_sel:DWORD dst_unused:UNUSED_PAD src0_sel:DWORD src1_sel:WORD_1\n\t" \
  "v_mfma_f32_16x16x32_bf16 v[148:151], v[212:215], v[96:99], 0\n\t" \
  "ds_read_u16_d16_hi v207, v207 offset:0\n\t" \
  "v_mul_f32 v152, %[kk], v152\n\t" \
  "v_mfma_f32_16x16x32_bf16 v[156:159], v[212:215], v[108:111], 0\n\t" \
  "ds_read_b64_tr_b16 v[210:211], v250 offset:6928\n\t" \
  "v_lshlrev_b32_sdwa v145, 1, v145 dst_sel:DWORD dst_unused:UNUSED_PAD src0_sel:DWORD src1_sel:WORD_1\n\t" \
  "v_mfma_f32_16x16x32_bf16 v[164:167], v[212:215], v[120:123], 0\n\t" \
  "ds_read_u16_d16_hi v144, v144 offset:0\n\t" \
  "v_mul_f32 v153, %[kk], v153\n\t" \
  "v_mfma_f32_16x16x32_bf16 v[172:175], v[212:215], v[132:135], 0\n\t" \
  "ds_read_b64_tr_b16 v[212:213], v250 offset:6168\n\t" \
  "v_cvt_pk_f16_f32 v224, v152, v153\n\t" \
  "v_mfma_f32_16x16x16_bf16 v[88:91], v[246:247], v[242:243], 0\n\t" \
  "s_waitcnt lgkmcnt(12)\n\t" \
  "ds_read_u16_d16_hi v145, v145 offset:0\n\t" \
  "v_lshlrev_b32_sdwa v146, 1, v146 dst_sel:DWORD dst_unused:UNUSED_PAD src0_sel:DWORD src1_sel:WORD_1\n\t" \
  "v_mfma_f32_16x16x32_bf16 v[148:151], v[216:219], v[100:103], v[148:151]\n\t" \
  "ds_read_b64_tr_b16 v[214:215], v250 offset:6936\n\t" \
  "v_pk_fma_f16 v225, v224, v224, %[h1] neg_lo:[1,0,0] neg_hi:[1,0,0]\n\t" \
  "v_mfma_f32_16x16x32_bf16 v[156:159], v[216:219], v[112:115], v[156:159]\n\t" \
  "ds_read_u16_d16_hi v146, v146 offset:0\n\t" \
  "v_lshlrev_b32_sdwa v147, 1, v147 dst_sel:DWORD dst_unused:UNUSED_PAD src0_sel:DWORD src1_sel:WORD_1\n\t" \
  "v_mfma_f32_16x16x32_bf16 v[164:167], v[216:219], v[124:127], v[164:167]\n\t" \
  "ds_read_u16_d16_hi v147, v147 offset:0\n\t" \
  "v_pk_mul_f16 v225, v224, v225\n\t" \
  "v_mfma_f32_16x16x16_bf16 v[92:95], v[246:247], v[244:245], 0\n\t" \
  "v_lshlrev_b32_sdwa v154, 1, v154 dst_sel:DWORD dst_unused:UNUSED_PAD src0_sel:DWORD src1_sel:WORD_1\n\t" \
  "v_exp_f16 v224, v225\n\t" \
  "v_mfma_f32_16x16x32_bf16 v[172:175], v[216:219], v[136:139], v[172:175]\n\t" \
  "s_waitcnt lgkmcnt(13)\n\t" \
  "ds_read_b64_tr_b16 v[216:217], v250 offset:6224\n\t" \
  "v_lshlrev_b32_sdwa v155, 1, v155 dst_sel:DWORD dst_unused:UNUSED_PAD src0_sel:DWORD src1_sel:WORD_1\n\t" \
  "v_mfma_f32_16x16x32_bf16 v[148:151], v[220:223], v[104:107], v[148:151]\n\t" \
  "ds_read_u16_d16_hi v154, v154 offset:0\n\t" \
  "v_lshlrev_b32_sdwa v160, 1, v160 dst_sel:DWORD dst_unused:UNUSED_PAD src0_sel:DWORD src1_sel:WORD_1\n\t" \
  "v_mfma_f32_16x16x32_bf16 v[156:159], v[220:223], v[116:119], v[156:159]\n\t" \
  "ds_read_b64_tr_b16 v[218:219], v250 offset:6992\n\t" \
  "v_exp_f16_sdwa v224, v225 dst_sel:WORD_1 dst_unused:UNUSED_PRESERVE src0_sel:WORD_1\n\t" \
  "v_mfma_f32_16x16x32_bf16 v[164:167], v[220:223], v[128:131], v[164:167]\n\t" \
  "ds_read_u16_d16_hi v155, v155 offset:0\n\t" \
  "v_lshlrev_b32_sdwa v161, 1, v161 dst_sel:DWORD dst_unused:UNUSED_PAD src0_sel:DWORD src1_sel:WORD_1\n\t" \
  "v_mfma_f32_16x16x32_bf16 v[172:175], v[220:223], v[140:143], v[172:175]\n\t" \
  "s_waitcnt lgkmcnt(10)\n\t" \
  "ds_read_b64_tr_b16 v[220:221], v250 offset:6232\n\t" \
  "v_pk_fma_f16 v224, v224, %[hc], %[hc]\n\t" \
  "v_smfmac_f32_16x16x64_bf16 v[0:3], v[208:211], v[176:183], v248\n\t" \
  "ds_read_u16_d16_hi v160, v160 offset:0\n\t" \
  "v_lshlrev_b32_sdwa v162, 1, v162 dst_sel:DWORD dst_unused:UNUSED_PAD src0_sel:DWORD src1_sel:WORD_1\n\t" \
  "v_smfmac_f32_16x16x64_bf16 v[24:27], v[208:211], v[184:191], v248\n\t" \
  "ds_read_b64_tr_b16 v[222:223], v250 offset:7000\n\t" \
  "v_rcp_f16 v225, v224\n\t" \
  "v_smfmac_f32_16x16x64_bf16 v[48:51], v[208:211], v[192:199], v248\n\t" \
  "ds_read_u16_d16_hi v161, v161 offset:0\n\t" \
  "v_lshlrev_b32_sdwa v163, 1, v163 dst_sel:DWORD dst_unused:UNUSED_PAD src0_sel:DWORD src1_sel:WORD_1\n\t" \
  "v_smfmac_f32_16x16x64_bf16 v[72:75], v[208:211], v[200:207], v248\n\t" \
  "s_waitcnt lgkmcnt(10)\n\t" \
  "ds_read_b64_tr_b16 v[208:209], v250 offset:6288\n\t" \
  "v_lshlrev_b32_sdwa v168, 1, v168 dst_sel:DWORD dst_unused:UNUSED_PAD src0_sel:DWORD src1_sel:WORD_1\n\t" \
  "v_smfmac_f32_16x16x64_bf16 v[4:7], v[212:215], v[176:183], v248\n\t" \
  "ds_read_u16_d16_hi v162, v162 offset:0\n\t" \
  "v_rcp_f16_sdwa v224, v224 dst_sel:DWORD dst_unused:UNUSED_PAD src0_sel:WORD_1\n\t" \
  "v_smfmac_f32_16x16x64_bf16 v[28:31], v[212:215], v[184:191], v248\n\t" \
  "s_add_u32 %[ob], %[ob], %[dob]\n\t" \
  "ds_read_b64_tr_b16 v[210:211], v250 offset:7056\n\t" \
  "v_lshlrev_b32_sdwa v169, 1, v169 dst_sel:DWORD dst_unused:UNUSED_PAD src0_sel:DWORD src1_sel:WORD_1\n\t" \
  "v_smfmac_f32_16x16x64_bf16 v[52:55], v[212:215], v[192:199], v248\n\t" \
  "ds_read_u16_d16_hi v163, v163 offset:0\n\t" \
  "v_fma_mix_f32 v152, v152, v225, 0 op_sel_hi:[0,1,0]\n\t" \
  "v_smfmac_f32_16x16x64_bf16 v[76:79], v[212:215], v[200:207], v248\n\t" \
  "s_waitcnt lgkmcnt(9)\n\t" \
  "ds_read_b64_tr_b16 v[212:213], v250 offset:6296\n\t" \
  "v_lshlrev_b32_sdwa v170, 1, v170 dst_sel:DWORD dst_unused:UNUSED_PAD src0_sel:DWORD src1_sel:WORD_1\n\t" \
  "v_smfmac_f32_16x16x64_bf16 v[8:11], v[216:219], v[176:183], v248\n\t" \
  "ds_read_u16_d16_hi v168, v168 offset:0\n\t" \
  "v_fma_mix_f32 v153, v153, v224, 0 op_sel_hi:[0,1,0]\n\t" \
  "v_smfmac_f32_16x16x64_bf16 v[32:35], v[216:219], v[184:191], v248\n\t" \
  "ds_read_b64_tr_b16 v[214:215], v250 offset:7064\n\t" \
  "v_lshlrev_b32_sdwa v171, 1, v171 dst_sel:DWORD dst_unused:UNUSED_PAD src0_sel:DWORD src1_sel:WORD_1\n\t" \
  "v_smfmac_f32_16x16x64_bf16 v[56:59], v[216:219], v[192:199], v248\n\t" \
  "ds_read_u16_d16_hi v169, v169 offset:0\n\t" \
  "v_mul_f32 v156, %[kk], v156\n\t" \
  "v_smfmac_f32_16x16x64_bf16 v[80:83], v[216:219], v[200:207], v248\n\t" \
  "s_waitcnt lgkmcnt(9)\n\t" \
  "s_barrier\n\t" \
  "ds_read_b64_tr_b16 v[216:217], v249 offset:12288\n\t" \
  "v_lshlrev_b32_sdwa v148, 1, v148 dst_sel:DWORD dst_unused:UNUSED_PAD src0_sel:DWORD src1_sel:WORD_1\n\t" \
  "v_smfmac_f32_16x16x64_bf16 v[12:15], v[220:223], v[176:183], v248\n\t" \
  "ds_read_u16_d16_hi v170, v170 offset:0\n\t" \
  "v_mul_f32 v157, %[kk], v157\n\t" \
  "v_smfmac_f32_16x16x64_bf16 v[36:39], v[220:223], v[184:191], v248\n\t" \
  "ds_read_b64_tr_b16 v[218:219], v249 offset:12544\n\t" \
  "v_cvt_pk_f16_f32 v224, v156, v157\n\t" \
  "v_smfmac_f32_16x16x64_bf16 v[60:63], v[220:223], v[192:199], v248\n\t" \
  "ds_read_u16_d16_hi v171, v171 offset:0\n\t" \
  "v_lshlrev_b32_sdwa v149, 1, v149 dst_sel:DWORD dst_unused:UNUSED_PAD src0_sel:DWORD src1_sel:WORD_1\n\t" \
  "v_smfmac_f32_16x16x64_bf16 v[84:87], v[220:223], v[200:207], v248\n\t" \
  "s_waitcnt lgkmcnt(9)\n\t" \
  "ds_read_b64_tr_b16 v[220:221], v249 offset:14336\n\t" \
  "v_pk_fma_f16 v225, v224, v224, %[h1] neg_lo:[1,0,0] neg_hi:[1,0,0]\n\t" \
  "v_smfmac_f32_16x16x64_bf16 v[16:19], v[208:211], v[176:183], v248\n\t" \
  "s_add_u32 %[rs], %[rs], 1\n\t" \
  "ds_read_u16_d16_hi v148, v148 offset:0\n\t" \
  "v_lshlrev_b32_sdwa v150, 1, v150 dst_sel:DWORD dst_unused:UNUSED_PAD src0_sel:DWORD src1_sel:WORD_1\n\t" \
  "v_smfmac_f32_16x16x64_bf16 v[40:43], v[208:211], v[184:191], v248\n\t" \
  "s_cmp_eq_u32 %[rs], 6\n\t" \
  "ds_read_b64_tr_b16 v[222:223], v249 offset:14592\n\t" \
  "v_pk_mul_f16 v225, v224, v225\n\t" \
  "v_smfmac_f32_16x16x64_bf16 v[64:67], v[208:211], v[192:199], v248\n\t" \
  "ds_read_u16_d16_hi v149, v149 offset:0\n\t" \
  "v_lshlrev_b32_sdwa v151, 1, v151 dst_sel:DWORD dst_unused:UNUSED_PAD src0_sel:DWORD src1_sel:WORD_1\n\t" \
  "v_smfmac_f32_16x16x64_bf16 v[88:91], v[208:211], v[200:207], v248\n\t" \
  "s_waitcnt lgkmcnt(9)\n\t" \
  "ds_read_b64_tr_b16 v[208:209], v249 offset:16384\n\t" \
  "v_exp_f16 v224, v225\n\t" \
  "v_smfmac_f32_16x16x64_bf16 v[20:23], v[212:215], v[176:183], v248\n\t" \
  "s_cselect_b32 %[rs], 0, %[rs]\n\t" \
  "ds_read_u16_d16_hi v150, v150 offset:0\n\t" \
  "v_lshlrev_b32_sdwa v158, 1, v158 dst_sel:DWORD dst_unused:UNUSED_PAD src0_sel:DWORD src1_sel:WORD_1\n\t" \
  "v_smfmac_f32_16x16x64_bf16 v[44:47], v[212:215], v[184:191], v248\n\t" \
  "s_cselect_b32 %[t0], -75920, %[ps]\n\t" \
  "ds_read_b64_tr_b16 v[210:211], v249 offset:16640\n\t" \
  "v_lshlrev_b32_sdwa v159, 1, v159 dst_sel:DWORD dst_unused:UNUSED_PAD src0_sel:DWORD src1_sel:WORD_1\n\t" \
  "v_smfmac_f32_16x16x64_bf16 v[68:71], v[212:215], v[192:199], v248\n\t" \
  "ds_read_u16_d16_hi v151, v151 offset:0\n\t" \
  "v_add_u32 v252, %[t0], v252\n\t" \
  "v_exp_f16_sdwa v224, v225 dst_sel:WORD_1 dst_unused:UNUSED_PRESERVE src0_sel:WORD_1\n\t" \
  "v_smfmac_f32_16x16x64_bf16 v[92:95], v[212:215], v[200:207], v248\n\t" \
  "s_waitcnt lgkmcnt(9)\n\t" \
  "ds_read_b64_tr_b16 v[212:213], v249 offset:12800\n\t" \
  "v_lshlrev_b32_sdwa v164, 1, v164 dst_sel:DWORD dst_unused:UNUSED_PAD src0_sel:DWORD src1_sel:WORD_1\n\t" \
  "v_mfma_f32_16x16x32_bf16 v[176:179], v[216:219], v[96:99], 0\n\t" \
  "ds_read_u16_d16_hi v158, v158 offset:0\n\t" \
  "v_pk_fma_f16 v224, v224, %[hc], %[hc]\n\t" \
  "v_mfma_f32_16x16x32_bf16 v[184:187], v[216:219], v[108:111], 0\n\t" \
  "ds_read_b64_tr_b16 v[214:215], v249 offset:13056\n\t" \
  "v_lshlrev_b32_sdwa v165, 1, v165 dst_sel:DWORD dst_unused:UNUSED_PAD src0_sel:DWORD src1_sel:WORD_1\n\t" \
  "v_mfma_f32_16x16x32_bf16 v[192:195], v[216:219], v[120:123], 0\n\t" \
  "ds_read_u16_d16_hi v159, v159 offset:0\n\t" \
  "v_rcp_f16 v225, v224\n\t" \
  "v_mfma_f32_16x16x32_bf16 v[200:203], v[216:219], v[132:135], 0\n\t" \
  "s_waitcnt lgkmcnt(9)\n\t" \
  "ds_read_b64_tr_b16 v[216:217], v249 offset:14848\n\t" \
  "v_lshlrev_b32_sdwa v166, 1, v166 dst_sel:DWORD dst_unused:UNUSED_PAD src0_sel:DWORD src1_sel:WORD_1\n\t" \
  "v_mfma_f32_16x16x32_bf16 v[176:179], v[220:223], v[100:103], v[176:179]\n\t" \
  "ds_read_u16_d16_hi v164, v164 offset:0\n\t" \
  "v_lshlrev_b32_sdwa v167, 1, v167 dst_sel:DWORD dst_unused:UNUSED_PAD src0_sel:DWORD src1_sel:WORD_1\n\t" \
  "v_mfma_f32_16x16x32_bf16 v[184:187], v[220:223], v[112:115], v[184:187]\n\t" \
  "ds_read_b64_tr_b16 v[218:219], v249 offset:15104\n\t" \
  "v_rcp_f16_sdwa v224, v224 dst_sel:DWORD dst_unused:UNUSED_PAD src0_sel:WORD_1\n\t" \
  "v_mfma_f32_16x16x32_bf16 v[192:195], v[220:223], v[124:127], v[192:195]\n\t" \
  "ds_read_u16_d16_hi v165, v165 offset:0\n\t" \
  "v_lshlrev_b32_sdwa v172, 1, v172 dst_sel:DWORD dst_unused:UNUSED_PAD src0_sel:DWORD src1_sel:WORD_1\n\t" \
  "v_mfma_f32_16x16x32_bf16 v[200:203], v[220:223], v[136:139], v[200:203]\n\t" \
  "s_waitcnt lgkmcnt(9)\n\t" \
  "ds_read_b64_tr_b16 v[220:221], v249 offset:16896\n\t" \
  "ds_read_u16_d16_hi v166, v166 offset:0\n\t" \
  "v_mfma_f32_16x16x32_bf16 v[176:179], v[208:211], v[104:107], v[176:179]\n\t" \
  "ds_read_b64_tr_b16 v[222:223], v249 offset:17152\n\t" \
  "v_lshlrev_b32_sdwa v173, 1, v173 dst_sel:DWORD dst_unused:UNUSED_PAD src0_sel:DWORD src1_sel:WORD_1\n\t" \
  "v_fma_mix_f32 v156, v156, v225, 0 op_sel_hi:[0,1,0]\n\t" \
  "v_mfma_f32_16x16x32_bf16 v[184:187], v[208:211], v[116:119], v[184:187]\n\t" \
  "ds_read_u16_d16_hi v167, v167 offset:0\n\t" \
  "v_fma_mix_f32 v157, v157, v224, 0 op_sel_hi:[0,1,0]\n\t" \
  "v_mfma_f32_16x16x32_bf16 v[192:195], v[208:211], v[128:131], v[192:195]\n\t" \
  "ds_read_u16_d16_hi v172, v172 offset:0\n\t" \
  "v_lshlrev_b32_sdwa v174, 1, v174 dst_sel:DWORD dst_unused:UNUSED_PAD src0_sel:DWORD src1_sel:WORD_1\n\t" \
  "v_mfma_f32_16x16x32_bf16 v[200:203], v[208:211], v[140:143], v[200:203]\n\t" \
  "s_waitcnt lgkmcnt(10)\n\t" \
  "ds_read_b64_tr_b16 v[208:209], v250 offset:12320\n\t" \
  "v_lshlrev_b32_sdwa v175, 1, v175 dst_sel:DWORD dst_unused:UNUSED_PAD src0_sel:DWORD src1_sel:WORD_1\n\t" \
  "v_mfma_f32_16x16x32_bf16 v[180:183], v[212:215], v[96:99], 0\n\t" \
  "ds_read_u16_d16_hi v173, v173 offset:0\n\t" \
  "v_lshlrev_b32_sdwa v176, 1, v176 dst_sel:DWORD dst_unused:UNUSED_PAD src0_sel:DWORD src1_sel:WORD_1\n\t" \
  "v_mfma_f32_16x16x32_bf16 v[188:191], v[212:215], v[108:111], 0\n\t" \
  "ds_read_b64_tr_b16 v[210:211], v250 offset:13088\n\t" \
  "v_mul_f32 v184, %[kk], v184\n\t" \
  "v_mfma_f32_16x16x32_bf16 v[196:199], v[212:215], v[120:123], 0\n\t" \
  "ds_read_u16_d16_hi v174, v174 offset:0\n\t" \
  "v_lshlrev_b32_sdwa v177, 1, v177 dst_sel:DWORD dst_unused:UNUSED_PAD src0_sel:DWORD src1_sel:WORD_1\n\t" \
  "v_mul_f32 v185, %[kk], v185\n\t" \
  "v_mfma_f32_16x16x32_bf16 v[204:207], v[212:215], v[132:135], 0\n\t" \
  "s_waitcnt lgkmcnt(10)\n\t" \
  "ds_read_b64_tr_b16 v[212:213], v250 offset:12328\n\t" \
  "ds_read_u16_d16_hi v175, v175 offset:0\n\t" \
  "v_cvt_pk_f16_f32 v224, v184, v185\n\t" \
  "v_mfma_f32_16x16x32_bf16 v[180:183], v[216:219], v[100:103], v[180:183]\n\t" \
  "ds_read_b64_tr_b16 v[214:215], v250 offset:13096\n\t" \
  "v_lshlrev_b32_sdwa v178, 1, v178 dst_sel:DWORD dst_unused:UNUSED_PAD src0_sel:DWORD src1_sel:WORD_1\n\t" \
  "v_mfma_f32_16x16x32_bf16 v[188:191], v[216:219], v[112:115], v[188:191]\n\t" \
  "ds_read_u16_d16_hi v176, v176 offset:0\n\t" \
  "v_pk_fma_f16 v225, v224, v224, %[h1] neg_lo:[1,0,0] neg_hi:[1,0,0]\n\t" \
  "v_mfma_f32_16x16x32_bf16 v[196:199], v[216:219], v[124:127], v[196:199]\n\t" \
  "ds_read_u16_d16_hi v177, v177 offset:0\n\t" \
  "v_lshlrev_b32_sdwa v179, 1, v179 dst_sel:DWORD dst_unused:UNUSED_PAD src0_sel:DWORD src1_sel:WORD_1\n\t" \
  "v_pk_mul_f16 v225, v224, v225\n\t" \
  "v_mfma_f32_16x16x32_bf16 v[204:207], v[216:219], v[136:139], v[204:207]\n\t" \
  "s_waitcnt lgkmcnt(11)\n\t" \
  "ds_read_b64_tr_b16 v[216:217], v250 offset:12384\n\t" \
  "v_lshlrev_b32_sdwa v186, 1, v186 dst_sel:DWORD dst_unused:UNUSED_PAD src0_sel:DWORD src1_sel:WORD_1\n\t" \
  "v_mfma_f32_16x16x32_bf16 v[180:183], v[220:223], v[104:107], v[180:183]\n\t" \
  "ds_read_u16_d16_hi v178, v178 offset:0\n\t" \
  "v_exp_f16 v224, v225\n\t" \
  "v_mfma_f32_16x16x32_bf16 v[188:191], v[220:223], v[116:119], v[188:191]\n\t" \
  "ds_read_b64_tr_b16 v[218:219], v250 offset:13152\n\t" \
  "v_lshlrev_b32_sdwa v187, 1, v187 dst_sel:DWORD dst_unused:UNUSED_PAD src0_sel:DWORD src1_sel:WORD_1\n\t" \
  "v_mfma_f32_16x16x32_bf16 v[196:199], v[220:223], v[128:131], v[196:199]\n\t" \
  "ds_read_u16_d16_hi v179, v179 offset:0\n\t" \
  "v_lshlrev_b32_sdwa v192, 1, v192 dst_sel:DWORD dst_unused:UNUSED_PAD src0_sel:DWORD src1_sel:WORD_1\n\t" \
  "v_mfma_f32_16x16x32_bf16 v[204:207], v[220:223], v[140:143], v[204:207]\n\t" \
  "s_waitcnt lgkmcnt(10)\n\t" \
  "ds_read_b64_tr_b16 v[220:221], v250 offset:12392\n\t" \
  "ds_read_u16_d16_hi v186, v186 offset:0\n\t" \
  "v_exp_f16_sdwa v224, v225 dst_sel:WORD_1 dst_unused:UNUSED_PRESERVE src0_sel:WORD_1\n\t" \
  "v_smfmac_f32_16x16x64_bf16 v[0:3], v[208:211], v[144:151], v248\n\t" \
  "ds_read_b64_tr_b16 v[222:223], v250 offset:13160\n\t" \
  "v_lshlrev_b32_sdwa v193, 1, v193 dst_sel:DWORD dst_unused:UNUSED_PAD src0_sel:DWORD src1_sel:WORD_1\n\t" \
  "v_smfmac_f32_16x16x64_bf16 v[24:27], v[208:211], v[152:159], v248\n\t" \
  "ds_read_u16_d16_hi v187, v187 offset:0\n\t" \
  "v_pk_fma_f16 v224, v224, %[hc], %[hc]\n\t" \
  "v_smfmac_f32_16x16x64_bf16 v[48:51], v[208:211], v[160:167], v248\n\t" \
  "s_waitcnt lgkmcnt(11)\n\t" \
  "ds_read_u16_d16_hi v192, v192 offset:0\n\t" \
  "v_lshlrev_b32_sdwa v194, 1, v194 dst_sel:DWORD dst_unused:UNUSED_PAD src0_sel:DWORD src1_sel:WORD_1\n\t" \
  "v_smfmac_f32_16x16x64_bf16 v[72:75], v[208:211], v[168:175], v248\n\t" \
  "s_waitcnt lgkmcnt(11)\n\t" \
  "ds_read_b64_tr_b16 v[208:209], v250 offset:12448\n\t" \
  "v_rcp_f16 v225, v224\n\t" \
  "v_smfmac_f32_16x16x64_bf16 v[4:7], v[212:215], v[144:151], v248\n\t" \
  "ds_read_u16_d16_hi v193, v193 offset:0\n\t" \
  "v_lshlrev_b32_sdwa v195, 1, v195 dst_sel:DWORD dst_unused:UNUSED_PAD src0_sel:DWORD src1_sel:WORD_1\n\t" \
  "v_smfmac_f32_16x16x64_bf16 v[28:31], v[212:215], v[152:159], v248\n\t" \
  "ds_read_b64_tr_b16 v[210:211], v250 offset:13216\n\t" \
  "v_lshlrev_b32_sdwa v200, 1, v200 dst_sel:DWORD dst_unused:UNUSED_PAD src0_sel:DWORD src1_sel:WORD_1\n\t" \
  "v_smfmac_f32_16x16x64_bf16 v[52:55], v[212:215], v[160:167], v248\n\t" \
  "ds_read_u16_d16_hi v194, v194 offset:0\n\t" \
  "v_rcp_f16_sdwa v224, v224 dst_sel:DWORD dst_unused:UNUSED_PAD src0_sel:WORD_1\n\t" \
  "v_smfmac_f32_16x16x64_bf16 v[76:79], v[212:215], v[168:175], v248\n\t" \
  "s_waitcnt lgkmcnt(10)\n\t" \
  "ds_read_b64_tr_b16 v[212:213], v250 offset:12456\n\t" \
  "v_lshlrev_b32_sdwa v201, 1, v201 dst_sel:DWORD dst_unused:UNUSED_PAD src0_sel:DWORD src1_sel:WORD_1\n\t" \
  "v_smfmac_f32_16x16x64_bf16 v[8:11], v[216:219], v[144:151], v248\n\t" \
  "ds_read_u16_d16_hi v195, v195 offset:0\n\t" \
  "v_fma_mix_f32 v184, v184, v225, 0 op_sel_hi:[0,1,0]\n\t" \
  "v_smfmac_f32_16x16x64_bf16 v[32:35], v[216:219], v[152:159], v248\n\t" \
  "ds_read_b64_tr_b16 v[214:215], v250 offset:13224\n\t" \
  "v_lshlrev_b32_sdwa v202, 1, v202 dst_sel:DWORD dst_unused:UNUSED_PAD src0_sel:DWORD src1_sel:WORD_1\n\t" \
  "v_smfmac_f32_16x16x64_bf16 v[56:59], v[216:219], v[160:167], v248\n\t" \
  "ds_read_u16_d16_hi v200, v200 offset:0\n\t" \
  "v_fma_mix_f32 v185, v185, v224, 0 op_sel_hi:[0,1,0]\n\t" \
  "v_smfmac_f32_16x16x64_bf16 v[80:83], v[216:219], v[168:175], v248\n\t" \
  "s_waitcnt lgkmcnt(10)\n\t" \
  "s_barrier\n\t" \
  "s_sub_u32 %[lc], %[lc], 1\n\t" \
  "s_cmp_lg_u32 %[lc], 0\n\t" \
  "s_cbranch_scc1 .Lcnx_m\n\t" \
  "ds_read_b64_tr_b16 v[216:217], v249 offset:0\n\t" \
  "v_lshlrev_b32_sdwa v203, 1, v203 dst_sel:DWORD dst_unused:UNUSED_PAD src0_sel:DWORD src1_sel:WORD_1\n\t" \
  "v_smfmac_f32_16x16x64_bf16 v[12:15], v[220:223], v[144:151], v248\n\t" \
  "ds_read_u16_d16_hi v201, v201 offset:0\n\t" \
  "v_mul_f32 v188, %[kk], v188\n\t" \
  "v_smfmac_f32_16x16x64_bf16 v[36:39], v[220:223], v[152:159], v248\n\t" \
  "ds_read_b64_tr_b16 v[218:219], v249 offset:256\n\t" \
  "v_lshlrev_b32_sdwa v180, 1, v180 dst_sel:DWORD dst_unused:UNUSED_PAD src0_sel:DWORD src1_sel:WORD_1\n\t" \
  "v_mul_f32 v189, %[kk], v189\n\t" \
  "v_smfmac_f32_16x16x64_bf16 v[60:63], v[220:223], v[160:167], v248\n\t" \
  "ds_read_u16_d16_hi v202, v202 offset:0\n\t" \
  "v_cvt_pk_f16_f32 v224, v188, v189\n\t" \
  "v_smfmac_f32_16x16x64_bf16 v[84:87], v[220:223], v[168:175], v248\n\t" \
  "s_waitcnt lgkmcnt(9)\n\t" \
  "ds_read_b64_tr_b16 v[220:221], v249 offset:2048\n\t" \
  "v_lshlrev_b32_sdwa v181, 1, v181 dst_sel:DWORD dst_unused:UNUSED_PAD src0_sel:DWORD src1_sel:WORD_1\n\t" \
  "v_smfmac_f32_16x16x64_bf16 v[16:19], v[208:211], v[144:151], v248\n\t" \
  "ds_read_u16_d16_hi v203, v203 offset:0\n\t" \
  "v_pk_fma_f16 v225, v224, v224, %[h1] neg_lo:[1,0,0] neg_hi:[1,0,0]\n\t" \
  "v_smfmac_f32_16x16x64_bf16 v[40:43], v[208:211], v[152:159], v248\n\t" \
  "ds_read_b64_tr_b16 v[222:223], v249 offset:2304\n\t" \
  "v_lshlrev_b32_sdwa v182, 1, v182 dst_sel:DWORD dst_unused:UNUSED_PAD src0_sel:DWORD src1_sel:WORD_1\n\t" \
  "v_pk_mul_f16 v225, v224, v225\n\t" \
  "v_smfmac_f32_16x16x64_bf16 v[64:67], v[208:211], v[160:167], v248\n\t" \
  "ds_read_u16_d16_hi v180, v180 offset:0\n\t" \
  "v_lshlrev_b32_sdwa v183, 1, v183 dst_sel:DWORD dst_unused:UNUSED_PAD src0_sel:DWORD src1_sel:WORD_1\n\t" \
  "v_smfmac_f32_16x16x64_bf16 v[88:91], v[208:211], v[168:175], v248\n\t" \
  "s_waitcnt lgkmcnt(9)\n\t" \
  "ds_read_b64_tr_b16 v[208:209], v249 offset:4096\n\t" \
  "ds_read_u16_d16_hi v181, v181 offset:0\n\t" \
  "v_exp_f16 v224, v225\n\t" \
  "v_smfmac_f32_16x16x64_bf16 v[20:23], v[212:215], v[144:151], v248\n\t" \
  "ds_read_b64_tr_b16 v[210:211], v249 offset:4352\n\t" \
  "v_lshlrev_b32_sdwa v190, 1, v190 dst_sel:DWORD dst_unused:UNUSED_PAD src0_sel:DWORD src1_sel:WORD_1\n\t" \
  "v_smfmac_f32_16x16x64_bf16 v[44:47], v[212:215], v[152:159], v248\n\t" \
  "ds_read_u16_d16_hi v182, v182 offset:0\n\t" \
  "v_lshlrev_b32_sdwa v191, 1, v191 dst_sel:DWORD dst_unused:UNUSED_PAD src0_sel:DWORD src1_sel:WORD_1\n\t" \
  "v_smfmac_f32_16x16x64_bf16 v[68:71], v[212:215], v[160:167], v248\n\t" \
  "ds_read_u16_d16_hi v183, v183 offset:0\n\t" \
  "v_exp_f16_sdwa v224, v225 dst_sel:WORD_1 dst_unused:UNUSED_PRESERVE src0_sel:WORD_1\n\t" \
  "v_smfmac_f32_16x16x64_bf16 v[92:95], v[212:215], v[168:175], v248\n\t" \
  "s_waitcnt lgkmcnt(10)\n\t" \
  "ds_read_b64_tr_b16 v[212:213], v249 offset:512\n\t" \
  "v_lshlrev_b32_sdwa v196, 1, v196 dst_sel:DWORD dst_unused:UNUSED_PAD src0_sel:DWORD src1_sel:WORD_1\n\t" \
  "v_pk_fma_f16 v224, v224, %[hc], %[hc]\n\t" \
  "v_mfma_f32_16x16x32_bf16 v[152:155], v[216:219], v[108:111], 0\n\t" \
  "ds_read_u16_d16_hi v190, v190 offset:0\n\t" \
  "v_lshlrev_b32_sdwa v197, 1, v197 dst_sel:DWORD dst_unused:UNUSED_PAD src0_sel:DWORD src1_sel:WORD_1\n\t" \
  "v_mfma_f32_16x16x32_bf16 v[160:163], v[216:219], v[120:123], 0\n\t" \
  "ds_read_b64_tr_b16 v[214:215], v249 offset:768\n\t" \
  "v_rcp_f16 v225, v224\n\t" \
  "v_mfma_f32_16x16x32_bf16 v[168:171], v[216:219], v[132:135], 0\n\t" \
  "s_waitcnt lgkmcnt(9)\n\t" \
  "ds_read_u16_d16_hi v191, v191 offset:0\n\t" \
  "ds_read_b64_tr_b16 v[216:217], v249 offset:2560\n\t" \
  "v_lshlrev_b32_sdwa v198, 1, v198 dst_sel:DWORD dst_unused:UNUSED_PAD src0_sel:DWORD src1_sel:WORD_1\n\t" \
  "v_mfma_f32_16x16x32_bf16 v[152:155], v[220:223], v[112:115], v[152:155]\n\t" \
  "ds_read_u16_d16_hi v196, v196 offset:0\n\t" \
  "v_lshlrev_b32_sdwa v199, 1, v199 dst_sel:DWORD dst_unused:UNUSED_PAD src0_sel:DWORD src1_sel:WORD_1\n\t" \
  "v_rcp_f16_sdwa v224, v224 dst_sel:DWORD dst_unused:UNUSED_PAD src0_sel:WORD_1\n\t" \
  "v_mfma_f32_16x16x32_bf16 v[160:163], v[220:223], v[124:127], v[160:163]\n\t" \
  "ds_read_b64_tr_b16 v[218:219], v249 offset:2816\n\t" \
  "v_lshlrev_b32_sdwa v204, 1, v204 dst_sel:DWORD dst_unused:UNUSED_PAD src0_sel:DWORD src1_sel:WORD_1\n\t" \
  "v_mfma_f32_16x16x32_bf16 v[168:171], v[220:223], v[136:139], v[168:171]\n\t" \
  "s_waitcnt lgkmcnt(9)\n\t" \
  "ds_read_u16_d16_hi v197, v197 offset:0\n\t" \
  "ds_read_b64_tr_b16 v[220:221], v249 offset:4608\n\t" \
  "v_lshlrev_b32_sdwa v205, 1, v205 dst_sel:DWORD dst_unused:UNUSED_PAD src0_sel:DWORD src1_sel:WORD_1\n\t" \
  "v_fma_mix_f32 v188, v188, v225, 0 op_sel_hi:[0,1,0]\n\t" \
  "v_mfma_f32_16x16x32_bf16 v[152:155], v[208:211], v[116:119], v[152:155]\n\t" \
  "ds_read_u16_d16_hi v198, v198 offset:0\n\t" \
  "ds_read_b64_tr_b16 v[222:223], v249 offset:4864\n\t" \
  "v_fma_mix_f32 v189, v189, v224, 0 op_sel_hi:[0,1,0]\n\t" \
  "v_mfma_f32_16x16x32_bf16 v[160:163], v[208:211], v[128:131], v[160:163]\n\t" \
  "ds_read_u16_d16_hi v199, v199 offset:0\n\t" \
  "v_lshlrev_b32_sdwa v206, 1, v206 dst_sel:DWORD dst_unused:UNUSED_PAD src0_sel:DWORD src1_sel:WORD_1\n\t" \
  "v_mfma_f32_16x16x32_bf16 v[168:171], v[208:211], v[140:143], v[168:171]\n\t" \
  "s_waitcnt lgkmcnt(9)\n\t" \
  "ds_read_b64_tr_b16 v[208:209], v250 offset:18480\n\t" \
  "ds_read_u16_d16_hi v204, v204 offset:0\n\t" \
  "v_lshlrev_b32_sdwa v207, 1, v207 dst_sel:DWORD dst_unused:UNUSED_PAD src0_sel:DWORD src1_sel:WORD_1\n\t" \
  "v_mfma_f32_16x16x32_bf16 v[156:159], v[212:215], v[108:111], 0\n\t" \
  "ds_read_b64_tr_b16 v[210:211], v250 offset:19248\n\t" \
  "ds_read_u16_d16_hi v205, v205 offset:0\n\t" \
  "v_lshlrev_b32_sdwa v154, 1, v154 dst_sel:DWORD dst_unused:UNUSED_PAD src0_sel:DWORD src1_sel:WORD_1\n\t" \
  "v_mul_f32 v152, %[kk], v152\n\t" \
  "v_mfma_f32_16x16x32_bf16 v[164:167], v[212:215], v[120:123], 0\n\t" \
  "ds_read_u16_d16_hi v206, v206 offset:0\n\t" \
  "v_mul_f32 v153, %[kk], v153\n\t" \
  "v_mfma_f32_16x16x32_bf16 v[172:175], v[212:215], v[132:135], 0\n\t" \
  "s_waitcnt lgkmcnt(10)\n\t" \
  "ds_read_b64_tr_b16 v[212:213], v250 offset:18488\n\t" \
  "v_lshlrev_b32_sdwa v155, 1, v155 dst_sel:DWORD dst_unused:UNUSED_PAD src0_sel:DWORD src1_sel:WORD_1\n\t" \
  "v_cvt_pk_f16_f32 v224, v152, v153\n\t" \
  "v_mfma_f32_16x16x32_bf16 v[156:159], v[216:219], v[112:115], v[156:159]\n\t" \
  "ds_read_u16_d16_hi v207, v207 offset:0\n\t" \
  "v_lshlrev_b32_sdwa v160, 1, v160 dst_sel:DWORD dst_unused:UNUSED_PAD src0_sel:DWORD src1_sel:WORD_1\n\t" \
  "v_pk_fma_f16 v225, v224, v224, %[h1] neg_lo:[1,0,0] neg_hi:[1,0,0]\n\t" \
  "v_mfma_f32_16x16x32_bf16 v[164:167], v[216:219], v[124:127], v[164:167]\n\t" \
  "ds_read_b64_tr_b16 v[214:215], v250 offset:19256\n\t" \
  "v_pk_mul_f16 v225, v224, v225\n\t" \
  "v_mfma_f32_16x16x32_bf16 v[172:175], v[216:219], v[136:139], v[172:175]\n\t" \
  "s_waitcnt lgkmcnt(9)\n\t" \
  "ds_read_u16_d16_hi v154, v154 offset:0\n\t" \
  "ds_read_b64_tr_b16 v[216:217], v250 offset:18544\n\t" \
  "v_lshlrev_b32_sdwa v161, 1, v161 dst_sel:DWORD dst_unused:UNUSED_PAD src0_sel:DWORD src1_sel:WORD_1\n\t" \
  "v_exp_f16 v224, v225\n\t" \
  "v_mfma_f32_16x16x32_bf16 v[156:159], v[220:223], v[116:119], v[156:159]\n\t" \
  "ds_read_u16_d16_hi v155, v155 offset:0\n\t" \
  "v_lshlrev_b32_sdwa v162, 1, v162 dst_sel:DWORD dst_unused:UNUSED_PAD src0_sel:DWORD src1_sel:WORD_1\n\t" \
  "v_mfma_f32_16x16x32_bf16 v[164:167], v[220:223], v[128:131], v[164:167]\n\t" \
  "ds_read_b64_tr_b16 v[218:219], v250 offset:19312\n\t" \
  "v_exp_f16_sdwa v224, v225 dst_sel:WORD_1 dst_unused:UNUSED_PRESERVE src0_sel:WORD_1\n\t" \
  "v_mfma_f32_16x16x32_bf16 v[172:175], v[220:223], v[140:143], v[172:175]\n\t" \
  "s_waitcnt lgkmcnt(9)\n\t" \
  "ds_read_u16_d16_hi v160, v160 offset:0\n\t" \
  "ds_read_b64_tr_b16 v[220:221], v250 offset:18552\n\t" \
  "v_smfmac_f32_16x16x64_bf16 v[0:3], v[208:211], v[176:183], v248\n\t" \
  "ds_read_u16_d16_hi v161, v161 offset:0\n\t" \
  "v_lshlrev_b32_sdwa v163, 1, v163 dst_sel:DWORD dst_unused:UNUSED_PAD src0_sel:DWORD src1_sel:WORD_1\n\t" \
  "v_smfmac_f32_16x16x64_bf16 v[24:27], v[208:211], v[184:191], v248\n\t" \
  "ds_read_b64_tr_b16 v[222:223], v250 offset:19320\n\t" \
  "v_pk_fma_f16 v224, v224, %[hc], %[hc]\n\t" \
  "v_smfmac_f32_16x16x64_bf16 v[48:51], v[208:211], v[192:199], v248\n\t" \
  "s_waitcnt lgkmcnt(9)\n\t" \
  "ds_read_u16_d16_hi v162, v162 offset:0\n\t" \
  "v_lshlrev_b32_sdwa v168, 1, v168 dst_sel:DWORD dst_unused:UNUSED_PAD src0_sel:DWORD src1_sel:WORD_1\n\t" \
  "v_smfmac_f32_16x16x64_bf16 v[72:75], v[208:211], v[200:207], v248\n\t" \
  "s_waitcnt lgkmcnt(9)\n\t" \
  "ds_read_b64_tr_b16 v[208:209], v250 offset:18608\n\t" \
  "v_rcp_f16 v225, v224\n\t" \
  "v_smfmac_f32_16x16x64_bf16 v[4:7], v[212:215], v[176:183], v248\n\t" \
  "ds_read_u16_d16_hi v163, v163 offset:0\n\t" \
  "v_lshlrev_b32_sdwa v169, 1, v169 dst_sel:DWORD dst_unused:UNUSED_PAD src0_sel:DWORD src1_sel:WORD_1\n\t" \
  "v_smfmac_f32_16x16x64_bf16 v[28:31], v[212:215], v[184:191], v248\n\t" \
  "ds_read_b64_tr_b16 v[210:211], v250 offset:19376\n\t" \
  "v_rcp_f16_sdwa v224, v224 dst_sel:DWORD dst_unused:UNUSED_PAD src0_sel:WORD_1\n\t" \
  "v_smfmac_f32_16x16x64_bf16 v[52:55], v[212:215], v[192:199], v248\n\t" \
  "ds_read_u16_d16_hi v168, v168 offset:0\n\t" \
  "v_cvt_pk_bf16_f32 v230, v0, v1\n\t" \
  "v_smfmac_f32_16x16x64_bf16 v[76:79], v[212:215], v[200:207], v248\n\t" \
  "s_waitcnt lgkmcnt(9)\n\t" \
  "ds_read_b64_tr_b16 v[212:213], v250 offset:18616\n\t" \
  "v_cvt_pk_bf16_f32 v231, v2, v3\n\t" \
  "v_smfmac_f32_16x16x64_bf16 v[8:11], v[216:219], v[176:183], v248\n\t" \
  "ds_read_u16_d16_hi v169, v169 offset:0\n\t" \
  "v_lshlrev_b32_sdwa v170, 1, v170 dst_sel:DWORD dst_unused:UNUSED_PAD src0_sel:DWORD src1_sel:WORD_1\n\t" \
  "v_cvt_pk_bf16_f32 v232, v4, v5\n\t" \
  "v_smfmac_f32_16x16x64_bf16 v[32:35], v[216:219], v[184:191], v248\n\t" \
  "ds_read_b64_tr_b16 v[214:215], v250 offset:19384\n\t" \
  "v_fma_mix_f32 v152, v152, v225, 0 op_sel_hi:[0,1,0]\n\t" \
  "v_cvt_pk_bf16_f32 v233, v6, v7\n\t" \
  "v_smfmac_f32_16x16x64_bf16 v[56:59], v[216:219], v[192:199], v248\n\t" \
  "buffer_store_dwordx4 v[230:233], v253, %[osrd], %[ob] offen offset:0 sc1\n\t" \
  "v_fma_mix_f32 v153, v153, v224, 0 op_sel_hi:[0,1,0]\n\t" \
  "v_smfmac_f32_16x16x64_bf16 v[80:83], v[216:219], v[200:207], v248\n\t" \
  "s_waitcnt lgkmcnt(8)\n\t" \
  "s_barrier\n\t" \
  "ds_read_b64_tr_b16 v[216:217], v249 offset:6144\n\t" \
  "v_lshlrev_b32_sdwa v171, 1, v171 dst_sel:DWORD dst_unused:UNUSED_PAD src0_sel:DWORD src1_sel:WORD_1\n\t" \
  "v_smfmac_f32_16x16x64_bf16 v[12:15], v[220:223], v[176:183], v248\n\t" \
  "ds_read_u16_d16_hi v170, v170 offset:0\n\t" \
  "v_mul_f32 v156, %[kk], v156\n\t" \
  "v_smfmac_f32_16x16x64_bf16 v[36:39], v[220:223], v[184:191], v248\n\t" \
  "ds_read_b64_tr_b16 v[218:219], v249 offset:6400\n\t" \
  "v_lshlrev_b32_sdwa v158, 1, v158 dst_sel:DWORD dst_unused:UNUSED_PAD src0_sel:DWORD src1_sel:WORD_1\n\t" \
  "v_mul_f32 v157, %[kk], v157\n\t" \
  "v_smfmac_f32_16x16x64_bf16 v[60:63], v[220:223], v[192:199], v248\n\t" \
  "ds_read_u16_d16_hi v171, v171 offset:0\n\t" \
  "v_cvt_pk_f16_f32 v224, v156, v157\n\t" \
  "v_cvt_pk_bf16_f32 v234, v8, v9\n\t" \
  "v_smfmac_f32_16x16x64_bf16 v[84:87], v[220:223], v[200:207], v248\n\t" \
  "s_waitcnt lgkmcnt(8)\n\t" \
  "ds_read_b64_tr_b16 v[220:221], v249 offset:8192\n\t" \
  "v_lshlrev_b32_sdwa v159, 1, v159 dst_sel:DWORD dst_unused:UNUSED_PAD src0_sel:DWORD src1_sel:WORD_1\n\t" \
  "v_cvt_pk_bf16_f32 v235, v10, v11\n\t" \
  "v_smfmac_f32_16x16x64_bf16 v[16:19], v[208:211], v[176:183], v248\n\t" \
  "ds_read_u16_d16_hi v158, v158 offset:0\n\t" \
  "v_pk_fma_f16 v225, v224, v224, %[h1] neg_lo:[1,0,0] neg_hi:[1,0,0]\n\t" \
  "v_cvt_pk_bf16_f32 v236, v12, v13\n\t" \
  "v_smfmac_f32_16x16x64_bf16 v[40:43], v[208:211], v[184:191], v248\n\t" \
  "ds_read_b64_tr_b16 v[222:223], v249 offset:8448\n\t" \
  "v_pk_mul_f16 v225, v224, v225\n\t" \
  "v_cvt_pk_bf16_f32 v237, v14, v15\n\t" \
  "v_smfmac_f32_16x16x64_bf16 v[64:67], v[208:211], v[192:199], v248\n\t" \
  "buffer_store_dwordx4 v[234:237], v253, %[osrd], %[ob] offen offset:64 sc1\n\t" \
  "v_lshlrev_b32_sdwa v164, 1, v164 dst_sel:DWORD dst_unused:UNUSED_PAD src0_sel:DWORD src1_sel:WORD_1\n\t" \
  "v_smfmac_f32_16x16x64_bf16 v[88:91], v[208:211], v[200:207], v248\n\t" \
  "s_waitcnt lgkmcnt(7)\n\t" \
  "ds_read_b64_tr_b16 v[208:209], v249 offset:10240\n\t" \
  "v_exp_f16 v224, v225\n\t" \
  "v_smfmac_f32_16x16x64_bf16 v[20:23], v[212:215], v[176:183], v248\n\t" \
  "ds_read_u16_d16_hi v159, v159 offset:0\n\t" \
  "v_lshlrev_b32_sdwa v165, 1, v165 dst_sel:DWORD dst_unused:UNUSED_PAD src0_sel:DWORD src1_sel:WORD_1\n\t" \
  "v_smfmac_f32_16x16x64_bf16 v[44:47], v[212:215], v[184:191], v248\n\t" \
  "ds_read_b64_tr_b16 v[210:211], v249 offset:10496\n\t" \
  "v_exp_f16_sdwa v224, v225 dst_sel:WORD_1 dst_unused:UNUSED_PRESERVE src0_sel:WORD_1\n\t" \
  "v_smfmac_f32_16x16x64_bf16 v[68:71], v[212:215], v[192:199], v248\n\t" \
  "ds_read_u16_d16_hi v164, v164 offset:0\n\t" \
  "v_cvt_pk_bf16_f32 v238, v16, v17\n\t" \
  "v_smfmac_f32_16x16x64_bf16 v[92:95], v[212:215], v[200:207], v248\n\t" \
  "s_waitcnt lgkmcnt(8)\n\t" \
  "ds_read_b64_tr_b16 v[212:213], v249 offset:6656\n\t" \
  "v_lshlrev_b32_sdwa v166, 1, v166 dst_sel:DWORD dst_unused:UNUSED_PAD src0_sel:DWORD src1_sel:WORD_1\n\t" \
  "v_cvt_pk_bf16_f32 v239, v18, v19\n\t" \
  "v_pk_fma_f16 v224, v224, %[hc], %[hc]\n\t" \
  "v_cvt_pk_bf16_f32 v240, v20, v21\n\t" \
  "v_mfma_f32_16x16x32_bf16 v[184:187], v[216:219], v[108:111], 0\n\t" \
  "ds_read_u16_d16_hi v165, v165 offset:0\n\t" \
  "v_lshlrev_b32_sdwa v167, 1, v167 dst_sel:DWORD dst_unused:UNUSED_PAD src0_sel:DWORD src1_sel:WORD_1\n\t" \
  "v_cvt_pk_bf16_f32 v241, v22, v23\n\t" \
  "v_mfma_f32_16x16x32_bf16 v[192:195], v[216:219], v[120:123], 0\n\t" \
  "ds_read_b64_tr_b16 v[214:215], v249 offset:6912\n\t" \
  "v_rcp_f16 v225, v224\n\t" \
  "v_mfma_f32_16x16x32_bf16 v[200:203], v[216:219], v[132:135], 0\n\t" \
  "s_waitcnt lgkmcnt(7)\n\t" \
  "buffer_store_dwordx4 v[238:241], v253, %[osrd], %[ob] offen offset:128 sc1\n\t" \
  "ds_read_b64_tr_b16 v[216:217], v249 offset:8704\n\t" \
  "v_lshlrev_b32_sdwa v172, 1, v172 dst_sel:DWORD dst_unused:UNUSED_PAD src0_sel:DWORD src1_sel:WORD_1\n\t" \
  "v_mfma_f32_16x16x32_bf16 v[184:187], v[220:223], v[112:115], v[184:187]\n\t" \
  "ds_read_u16_d16_hi v166, v166 offset:0\n\t" \
  "v_rcp_f16_sdwa v224, v224 dst_sel:DWORD dst_unused:UNUSED_PAD src0_sel:WORD_1\n\t" \
  "v_mfma_f32_16x16x32_bf16 v[192:195], v[220:223], v[124:127], v[192:195]\n\t" \
  "ds_read_b64_tr_b16 v[218:219], v249 offset:8960\n\t" \
  "v_lshlrev_b32_sdwa v173, 1, v173 dst_sel:DWORD dst_unused:UNUSED_PAD src0_sel:DWORD src1_sel:WORD_1\n\t" \
  "v_mfma_f32_16x16x32_bf16 v[200:203], v[220:223], v[136:139], v[200:203]\n\t" \
  "s_waitcnt lgkmcnt(7)\n\t" \
  "ds_read_u16_d16_hi v167, v167 offset:0\n\t" \
  "ds_read_b64_tr_b16 v[220:221], v249 offset:10752\n\t" \
  "v_fma_mix_f32 v156, v156, v225, 0 op_sel_hi:[0,1,0]\n\t" \
  "v_mfma_f32_16x16x32_bf16 v[184:187], v[208:211], v[116:119], v[184:187]\n\t" \
  "ds_read_u16_d16_hi v172, v172 offset:0\n\t" \
  "v_lshlrev_b32_sdwa v174, 1, v174 dst_sel:DWORD dst_unused:UNUSED_PAD src0_sel:DWORD src1_sel:WORD_1\n\t" \
  "v_fma_mix_f32 v157, v157, v224, 0 op_sel_hi:[0,1,0]\n\t" \
  "v_mfma_f32_16x16x32_bf16 v[192:195], v[208:211], v[128:131], v[192:195]\n\t" \
  "ds_read_b64_tr_b16 v[222:223], v249 offset:11008\n\t" \
  "v_lshlrev_b32_sdwa v175, 1, v175 dst_sel:DWORD dst_unused:UNUSED_PAD src0_sel:DWORD src1_sel:WORD_1\n\t" \
  "v_mfma_f32_16x16x32_bf16 v[200:203], v[208:211], v[140:143], v[200:203]\n\t" \
  "s_waitcnt lgkmcnt(7)\n\t" \
  "ds_read_u16_d16_hi v173, v173 offset:0\n\t" \
  "ds_read_b64_tr_b16 v[208:209], v250 offset:0\n\t" \
  "v_mfma_f32_16x16x32_bf16 v[188:191], v[212:215], v[108:111], 0\n\t" \
  "ds_read_u16_d16_hi v174, v174 offset:0\n\t" \
  "v_lshlrev_b32_sdwa v186, 1, v186 dst_sel:DWORD dst_unused:UNUSED_PAD src0_sel:DWORD src1_sel:WORD_1\n\t" \
  "v_mul_f32 v184, %[kk], v184\n\t" \
  "v_mfma_f32_16x16x32_bf16 v[196:199], v[212:215], v[120:123], 0\n\t" \
  "ds_read_b64_tr_b16 v[210:211], v250 offset:768\n\t" \
  "v_mul_f32 v185, %[kk], v185\n\t" \
  "v_mfma_f32_16x16x32_bf16 v[204:207], v[212:215], v[132:135], 0\n\t" \
  "s_waitcnt lgkmcnt(8)\n\t" \
  "ds_read_u16_d16_hi v175, v175 offset:0\n\t" \
  "v_lshlrev_b32_sdwa v187, 1, v187 dst_sel:DWORD dst_unused:UNUSED_PAD src0_sel:DWORD src1_sel:WORD_1\n\t" \
  "v_cvt_pk_f16_f32 v224, v184, v185\n\t" \
  "v_mfma_f32_16x16x32_bf16 v[188:191], v[216:219], v[112:115], v[188:191]\n\t" \
  "ds_read_b64_tr_b16 v[212:213], v250 offset:8\n\t" \
  "v_lshlrev_b32_sdwa v192, 1, v192 dst_sel:DWORD dst_unused:UNUSED_PAD src0_sel:DWORD src1_sel:WORD_1\n\t" \
  "v_pk_fma_f16 v225, v224, v224, %[h1] neg_lo:[1,0,0] neg_hi:[1,0,0]\n\t" \
  "v_mfma_f32_16x16x32_bf16 v[196:199], v[216:219], v[124:127], v[196:199]\n\t" \
  "ds_read_u16_d16_hi v186, v186 offset:0\n\t" \
  "v_pk_mul_f16 v225, v224, v225\n\t" \
  "v_mfma_f32_16x16x32_bf16 v[204:207], v[216:219], v[136:139], v[204:207]\n\t" \
  "s_waitcnt lgkmcnt(7)\n\t" \
  "ds_read_b64_tr_b16 v[214:215], v250 offset:776\n\t" \
  "ds_read_u16_d16_hi v187, v187 offset:0\n\t" \
  "v_lshlrev_b32_sdwa v193, 1, v193 dst_sel:DWORD dst_unused:UNUSED_PAD src0_sel:DWORD src1_sel:WORD_1\n\t" \
  "v_exp_f16 v224, v225\n\t" \
  "v_mfma_f32_16x16x32_bf16 v[188:191], v[220:223], v[116:119], v[188:191]\n\t" \
  "ds_read_b64_tr_b16 v[216:217], v250 offset:64\n\t" \
  "v_lshlrev_b32_sdwa v194, 1, v194 dst_sel:DWORD dst_unused:UNUSED_PAD src0_sel:DWORD src1_sel:WORD_1\n\t" \
  "v_mfma_f32_16x16x32_bf16 v[196:199], v[220:223], v[128:131], v[196:199]\n\t" \
  "ds_read_u16_d16_hi v192, v192 offset:0\n\t" \
  "v_exp_f16_sdwa v224, v225 dst_sel:WORD_1 dst_unused:UNUSED_PRESERVE src0_sel:WORD_1\n\t" \
  "v_mfma_f32_16x16x32_bf16 v[204:207], v[220:223], v[140:143], v[204:207]\n\t" \
  "s_waitcnt lgkmcnt(7)\n\t" \
  "ds_read_b64_tr_b16 v[218:219], v250 offset:832\n\t" \
  "ds_read_u16_d16_hi v193, v193 offset:0\n\t" \
  "v_smfmac_f32_16x16x64_bf16 v[24:27], v[208:211], v[152:159], v248\n\t" \
  "ds_read_b64_tr_b16 v[220:221], v250 offset:72\n\t" \
  "v_lshlrev_b32_sdwa v195, 1, v195 dst_sel:DWORD dst_unused:UNUSED_PAD src0_sel:DWORD src1_sel:WORD_1\n\t" \
  "v_pk_fma_f16 v224, v224, %[hc], %[hc]\n\t" \
  "v_smfmac_f32_16x16x64_bf16 v[48:51], v[208:211], v[160:167], v248\n\t" \
  "s_waitcnt lgkmcnt(9)\n\t" \
  "ds_read_u16_d16_hi v194, v194 offset:0\n\t" \
  "v_lshlrev_b32_sdwa v200, 1, v200 dst_sel:DWORD dst_unused:UNUSED_PAD src0_sel:DWORD src1_sel:WORD_1\n\t" \
  "v_smfmac_f32_16x16x64_bf16 v[72:75], v[208:211], v[168:175], v248\n\t" \
  "s_waitcnt lgkmcnt(7)\n\t" \
  "ds_read_b64_tr_b16 v[222:223], v250 offset:840\n\t" \
  "ds_read_u16_d16_hi v195, v195 offset:0\n\t" \
  "v_rcp_f16 v225, v224\n\t" \
  "v_smfmac_f32_16x16x64_bf16 v[28:31], v[212:215], v[152:159], v248\n\t" \
  "s_add_u32 %[ob], %[ob], %[dob]\n\t" \
  "ds_read_b64_tr_b16 v[208:209], v250 offset:128\n\t" \
  "v_lshlrev_b32_sdwa v201, 1, v201 dst_sel:DWORD dst_unused:UNUSED_PAD src0_sel:DWORD src1_sel:WORD_1\n\t" \
  "v_smfmac_f32_16x16x64_bf16 v[52:55], v[212:215], v[160:167], v248\n\t" \
  "ds_read_u16_d16_hi v200, v200 offset:0\n\t" \
  "v_rcp_f16_sdwa v224, v224 dst_sel:DWORD dst_unused:UNUSED_PAD src0_sel:WORD_1\n\t" \
  "v_smfmac_f32_16x16x64_bf16 v[76:79], v[212:215], v[168:175], v248\n\t" \
  "s_waitcnt lgkmcnt(7)\n\t" \
  "ds_read_b64_tr_b16 v[210:211], v250 offset:896\n\t" \
  "v_lshlrev_b32_sdwa v202, 1, v202 dst_sel:DWORD dst_unused:UNUSED_PAD src0_sel:DWORD src1_sel:WORD_1\n\t" \
  "v_smfmac_f32_16x16x64_bf16 v[32:35], v[216:219], v[152:159], v248\n\t" \
  "ds_read_u16_d16_hi v201, v201 offset:0\n\t" \
  "v_fma_mix_f32 v184, v184, v225, 0 op_sel_hi:[0,1,0]\n\t" \
  "v_smfmac_f32_16x16x64_bf16 v[56:59], v[216:219], v[160:167], v248\n\t" \
  "ds_read_b64_tr_b16 v[212:213], v250 offset:136\n\t" \
  "v_fma_mix_f32 v185, v185, v224, 0 op_sel_hi:[0,1,0]\n\t" \
  "v_smfmac_f32_16x16x64_bf16 v[80:83], v[216:219], v[168:175], v248\n\t" \
  "s_waitcnt lgkmcnt(6)\n\t" \
  "ds_read_b64_tr_b16 v[214:215], v250 offset:904\n\t" \
  "s_barrier\n\t" \
  "ds_read_u16_d16_hi v202, v202 offset:0\n\t" \
  "v_lshlrev_b32_sdwa v203, 1, v203 dst_sel:DWORD dst_unused:UNUSED_PAD src0_sel:DWORD src1_sel:WORD_1\n\t" \
  "v_mul_f32 v188, %[kk], v188\n\t" \
  "v_smfmac_f32_16x16x64_bf16 v[36:39], v[220:223], v[152:159], v248\n\t" \
  "ds_read_b64_tr_b16 v[216:217], v249 offset:12288\n\t" \
  "v_lshlrev_b32_sdwa v190, 1, v190 dst_sel:DWORD dst_unused:UNUSED_PAD src0_sel:DWORD src1_sel:WORD_1\n\t" \
  "v_mul_f32 v189, %[kk], v189\n\t" \
  "v_smfmac_f32_16x16x64_bf16 v[60:63], v[220:223], v[160:167], v248\n\t" \
  "ds_read_u16_d16_hi v203, v203 offset:0\n\t" \
  "v_cvt_pk_f16_f32 v224, v188, v189\n\t" \
  "v_smfmac_f32_16x16x64_bf16 v[84:87], v[220:223], v[168:175], v248\n\t" \
  "s_waitcnt lgkmcnt(6)\n\t" \
  "ds_read_b64_tr_b16 v[218:219], v249 offset:12544\n\t" \
  "v_lshlrev_b32_sdwa v191, 1, v191 dst_sel:DWORD dst_unused:UNUSED_PAD src0_sel:DWORD src1_sel:WORD_1\n\t" \
  "v_pk_fma_f16 v225, v224, v224, %[h1] neg_lo:[1,0,0] neg_hi:[1,0,0]\n\t" \
  "v_smfmac_f32_16x16x64_bf16 v[40:43], v[208:211], v[152:159], v248\n\t" \
  "ds_read_u16_d16_hi v190, v190 offset:0\n\t" \
  "v_pk_mul_f16 v225, v224, v225\n\t" \
  "v_smfmac_f32_16x16x64_bf16 v[64:67], v[208:211], v[160:167], v248\n\t" \
  "ds_read_b64_tr_b16 v[220:221], v249 offset:14336\n\t" \
  "v_lshlrev_b32_sdwa v196, 1, v196 dst_sel:DWORD dst_unused:UNUSED_PAD src0_sel:DWORD src1_sel:WORD_1\n\t" \
  "v_smfmac_f32_16x16x64_bf16 v[88:91], v[208:211], v[168:175], v248\n\t" \
  "s_waitcnt lgkmcnt(6)\n\t" \
  "ds_read_b64_tr_b16 v[222:223], v249 offset:14592\n\t" \
  "ds_read_u16_d16_hi v191, v191 offset:0\n\t" \
  "v_exp_f16 v224, v225\n\t" \
  "v_lshlrev_b32_sdwa v197, 1, v197 dst_sel:DWORD dst_unused:UNUSED_PAD src0_sel:DWORD src1_sel:WORD_1\n\t" \
  "v_smfmac_f32_16x16x64_bf16 v[44:47], v[212:215], v[152:159], v248\n\t" \
  "ds_read_b64_tr_b16 v[208:209], v249 offset:16384\n\t" \
  "v_exp_f16_sdwa v224, v225 dst_sel:WORD_1 dst_unused:UNUSED_PRESERVE src0_sel:WORD_1\n\t" \
  "v_smfmac_f32_16x16x64_bf16 v[68:71], v[212:215], v[160:167], v248\n\t" \
  "ds_read_u16_d16_hi v196, v196 offset:0\n\t" \
  "v_lshlrev_b32_sdwa v198, 1, v198 dst_sel:DWORD dst_unused:UNUSED_PAD src0_sel:DWORD src1_sel:WORD_1\n\t" \
  "v_smfmac_f32_16x16x64_bf16 v[92:95], v[212:215], v[168:175], v248\n\t" \
  "s_waitcnt lgkmcnt(6)\n\t" \
  "ds_read_b64_tr_b16 v[210:211], v249 offset:16640\n\t" \
  "ds_read_u16_d16_hi v197, v197 offset:0\n\t" \
  "v_pk_fma_f16 v224, v224, %[hc], %[hc]\n\t" \
  "v_mfma_f32_16x16x32_bf16 v[152:155], v[216:219], v[108:111], 0\n\t" \
  "ds_read_b64_tr_b16 v[212:213], v249 offset:12800\n\t" \
  "v_lshlrev_b32_sdwa v199, 1, v199 dst_sel:DWORD dst_unused:UNUSED_PAD src0_sel:DWORD src1_sel:WORD_1\n\t" \
  "v_mfma_f32_16x16x32_bf16 v[160:163], v[216:219], v[120:123], 0\n\t" \
  "ds_read_u16_d16_hi v198, v198 offset:0\n\t" \
  "v_rcp_f16 v225, v224\n\t" \
  "v_mfma_f32_16x16x32_bf16 v[168:171], v[216:219], v[132:135], 0\n\t" \
  "s_waitcnt lgkmcnt(7)\n\t" \
  "ds_read_b64_tr_b16 v[214:215], v249 offset:13056\n\t" \
  "v_lshlrev_b32_sdwa v204, 1, v204 dst_sel:DWORD dst_unused:UNUSED_PAD src0_sel:DWORD src1_sel:WORD_1\n\t" \
  "v_mfma_f32_16x16x32_bf16 v[152:155], v[220:223], v[112:115], v[152:155]\n\t" \
  "ds_read_u16_d16_hi v199, v199 offset:0\n\t" \
  "v_rcp_f16_sdwa v224, v224 dst_sel:DWORD dst_unused:UNUSED_PAD src0_sel:WORD_1\n\t" \
  "v_mfma_f32_16x16x32_bf16 v[160:163], v[220:223], v[124:127], v[160:163]\n\t" \
  "ds_read_b64_tr_b16 v[216:217], v249 offset:14848\n\t" \
  "v_lshlrev_b32_sdwa v205, 1, v205 dst_sel:DWORD dst_unused:UNUSED_PAD src0_sel:DWORD src1_sel:WORD_1\n\t" \
  "v_mfma_f32_16x16x32_bf16 v[168:171], v[220:223], v[136:139], v[168:171]\n\t" \
  "s_waitcnt lgkmcnt(6)\n\t" \
  "ds_read_b64_tr_b16 v[218:219], v249 offset:15104\n\t" \
  "ds_read_u16_d16_hi v204, v204 offset:0\n\t" \
  "v_fma_mix_f32 v188, v188, v225, 0 op_sel_hi:[0,1,0]\n\t" \
  "v_mfma_f32_16x16x32_bf16 v[152:155], v[208:211], v[116:119], v[152:155]\n\t" \
  "ds_read_b64_tr_b16 v[220:221], v249 offset:16896\n\t" \
  "v_lshlrev_b32_sdwa v206, 1, v206 dst_sel:DWORD dst_unused:UNUSED_PAD src0_sel:DWORD src1_sel:WORD_1\n\t" \
  "v_fma_mix_f32 v189, v189, v224, 0 op_sel_hi:[0,1,0]\n\t" \
  "v_mfma_f32_16x16x32_bf16 v[160:163], v[208:211], v[128:131], v[160:163]\n\t" \
  "ds_read_u16_d16_hi v205, v205 offset:0\n\t" \
  "v_lshlrev_b32_sdwa v207, 1, v207 dst_sel:DWORD dst_unused:UNUSED_PAD src0_sel:DWORD src1_sel:WORD_1\n\t" \
  "v_mfma_f32_16x16x32_bf16 v[168:171], v[208:211], v[140:143], v[168:171]\n\t" \
  "s_waitcnt lgkmcnt(6)\n\t" \
  "ds_read_b64_tr_b16 v[222:223], v249 offset:17152\n\t" \
  "ds_read_u16_d16_hi v206, v206 offset:0\n\t" \
  "v_mfma_f32_16x16x32_bf16 v[156:159], v[212:215], v[108:111], 0\n\t" \
  "ds_read_b64_tr_b16 v[208:209], v250 offset:6160\n\t" \
  "v_lshlrev_b32_sdwa v154, 1, v154 dst_sel:DWORD dst_unused:UNUSED_PAD src0_sel:DWORD src1_sel:WORD_1\n\t" \
  "v_mul_f32 v152, %[kk], v152\n\t" \
  "v_mfma_f32_16x16x32_bf16 v[164:167], v[212:215], v[120:123], 0\n\t" \
  "ds_read_u16_d16_hi v207, v207 offset:0\n\t" \
  "v_mul_f32 v153, %[kk], v153\n\t" \
  "v_mfma_f32_16x16x32_bf16 v[172:175], v[212:215], v[132:135], 0\n\t" \
  "s_waitcnt lgkmcnt(7)\n\t" \
  "ds_read_b64_tr_b16 v[210:211], v250 offset:6928\n\t" \
  "v_lshlrev_b32_sdwa v155, 1, v155 dst_sel:DWORD dst_unused:UNUSED_PAD src0_sel:DWORD src1_sel:WORD_1\n\t" \
  "v_cvt_pk_f16_f32 v224, v152, v153\n\t" \
  "v_mfma_f32_16x16x32_bf16 v[156:159], v[216:219], v[112:115], v[156:159]\n\t" \
  "ds_read_u16_d16_hi v154, v154 offset:0\n\t" \
  "v_lshlrev_b32_sdwa v160, 1, v160 dst_sel:DWORD dst_unused:UNUSED_PAD src0_sel:DWORD src1_sel:WORD_1\n\t" \
  "v_pk_fma_f16 v225, v224, v224, %[h1] neg_lo:[1,0,0] neg_hi:[1,0,0]\n\t" \
  "v_mfma_f32_16x16x32_bf16 v[164:167], v[216:219], v[124:127], v[164:167]\n\t" \
  "ds_read_b64_tr_b16 v[212:213], v250 offset:6168\n\t" \
  "v_pk_mul_f16 v225, v224, v225\n\t" \
  "v_mfma_f32_16x16x32_bf16 v[172:175], v[216:219], v[136:139], v[172:175]\n\t" \
  "s_waitcnt lgkmcnt(6)\n\t" \
  "ds_read_b64_tr_b16 v[214:215], v250 offset:6936\n\t" \
  "ds_read_u16_d16_hi v155, v155 offset:0\n\t" \
  "v_lshlrev_b32_sdwa v161, 1, v161 dst_sel:DWORD dst_unused:UNUSED_PAD src0_sel:DWORD src1_sel:WORD_1\n\t" \
  "v_exp_f16 v224, v225\n\t" \
  "v_mfma_f32_16x16x32_bf16 v[156:159], v[220:223], v[116:119], v[156:159]\n\t" \
  "ds_read_b64_tr_b16 v[216:217], v250 offset:6224\n\t" \
  "v_lshlrev_b32_sdwa v162, 1, v162 dst_sel:DWORD dst_unused:UNUSED_PAD src0_sel:DWORD src1_sel:WORD_1\n\t" \
  "v_mfma_f32_16x16x32_bf16 v[164:167], v[220:223], v[128:131], v[164:167]\n\t" \
  "ds_read_u16_d16_hi v160, v160 offset:0\n\t" \
  "v_exp_f16_sdwa v224, v225 dst_sel:WORD_1 dst_unused:UNUSED_PRESERVE src0_sel:WORD_1\n\t" \
  "v_mfma_f32_16x16x32_bf16 v[172:175], v[220:223], v[140:143], v[172:175]\n\t" \
  "s_waitcnt lgkmcnt(6)\n\t" \
  "ds_read_b64_tr_b16 v[218:219], v250 offset:6992\n\t" \
  "ds_read_u16_d16_hi v161, v161 offset:0\n\t" \
  "v_smfmac_f32_16x16x64_bf16 v[24:27], v[208:211], v[184:191], v248\n\t" \
  "ds_read_b64_tr_b16 v[220:221], v250 offset:6232\n\t" \
  "ds_read_b64_tr_b16 v[222:223], v250 offset:7000\n\t" \
  "v_lshlrev_b32_sdwa v163, 1, v163 dst_sel:DWORD dst_unused:UNUSED_PAD src0_sel:DWORD src1_sel:WORD_1\n\t" \
  "v_pk_fma_f16 v224, v224, %[hc], %[hc]\n\t" \
  "v_smfmac_f32_16x16x64_bf16 v[48:51], v[208:211], v[192:199], v248\n\t" \
  "ds_read_b64_tr_b16 v[0:1], v250 offset:6288\n\t" \
  "ds_read_b64_tr_b16 v[2:3], v250 offset:7056\n\t" \
  "v_smfmac_f32_16x16x64_bf16 v[72:75], v[208:211], v[200:207], v248\n\t" \
  "s_waitcnt lgkmcnt(9)\n\t" \
  "ds_read_b64_tr_b16 v[4:5], v250 offset:6296\n\t" \
  "ds_read_b64_tr_b16 v[6:7], v250 offset:7064\n\t" \
  "v_lshlrev_b32_sdwa v168, 1, v168 dst_sel:DWORD dst_unused:UNUSED_PAD src0_sel:DWORD src1_sel:WORD_1\n\t" \
  "v_rcp_f16 v225, v224\n\t" \
  "v_smfmac_f32_16x16x64_bf16 v[28:31], v[212:215], v[184:191], v248\n\t" \
  "v_add_u32 v249, 57344, v249\n\t" \
  "ds_read_b64_tr_b16 v[8:9], v249 offset:8192\n\t" \
  "ds_read_b64_tr_b16 v[10:11], v249 offset:8448\n\t" \
  "v_smfmac_f32_16x16x64_bf16 v[52:55], v[212:215], v[192:199], v248\n\t" \
  "ds_read_b64_tr_b16 v[12:13], v249 offset:10240\n\t" \
  "ds_read_b64_tr_b16 v[14:15], v249 offset:10496\n\t" \
  "v_lshlrev_b32_sdwa v169, 1, v169 dst_sel:DWORD dst_unused:UNUSED_PAD src0_sel:DWORD src1_sel:WORD_1\n\t" \
  "v_rcp_f16_sdwa v224, v224 dst_sel:DWORD dst_unused:UNUSED_PAD src0_sel:WORD_1\n\t" \
  "v_smfmac_f32_16x16x64_bf16 v[76:79], v[212:215], v[200:207], v248\n\t" \
  "s_waitcnt lgkmcnt(11)\n\t" \
  "ds_read_b64_tr_b16 v[208:209], v249 offset:12288\n\t" \
  "ds_read_b64_tr_b16 v[210:211], v249 offset:12544\n\t" \
  "v_lshlrev_b32_sdwa v170, 1, v170 dst_sel:DWORD dst_unused:UNUSED_PAD src0_sel:DWORD src1_sel:WORD_1\n\t" \
  "v_smfmac_f32_16x16x64_bf16 v[32:35], v[216:219], v[184:191], v248\n\t" \
  "ds_read_u16_d16_hi v162, v162 offset:0\n\t" \
  "ds_read_b64_tr_b16 v[212:213], v249 offset:8704\n\t" \
  "v_fma_mix_f32 v152, v152, v225, 0 op_sel_hi:[0,1,0]\n\t" \
  "v_smfmac_f32_16x16x64_bf16 v[56:59], v[216:219], v[192:199], v248\n\t" \
  "ds_read_u16_d16_hi v163, v163 offset:0\n\t" \
  "ds_read_b64_tr_b16 v[214:215], v249 offset:8960\n\t" \
  "v_fma_mix_f32 v153, v153, v224, 0 op_sel_hi:[0,1,0]\n\t" \
  "v_smfmac_f32_16x16x64_bf16 v[80:83], v[216:219], v[200:207], v248\n\t" \
  "s_waitcnt lgkmcnt(14)\n\t" \
  "ds_read_u16_d16_hi v168, v168 offset:0\n\t" \
  "ds_read_b64_tr_b16 v[216:217], v249 offset:10752\n\t" \
  "v_lshlrev_b32_sdwa v171, 1, v171 dst_sel:DWORD dst_unused:UNUSED_PAD src0_sel:DWORD src1_sel:WORD_1\n\t" \
  "v_mul_f32 v156, %[kk], v156\n\t" \
  "v_smfmac_f32_16x16x64_bf16 v[36:39], v[220:223], v[184:191], v248\n\t" \
  "ds_read_u16_d16_hi v169, v169 offset:0\n\t" \
  "v_lshlrev_b32_sdwa v158, 1, v158 dst_sel:DWORD dst_unused:UNUSED_PAD src0_sel:DWORD src1_sel:WORD_1\n\t" \
  "v_mul_f32 v157, %[kk], v157\n\t" \
  "v_smfmac_f32_16x16x64_bf16 v[60:63], v[220:223], v[192:199], v248\n\t" \
  "ds_read_b64_tr_b16 v[218:219], v249 offset:11008\n\t" \
  "v_cvt_pk_f16_f32 v224, v156, v157\n\t" \
  "v_smfmac_f32_16x16x64_bf16 v[84:87], v[220:223], v[200:207], v248\n\t" \
  "s_waitcnt lgkmcnt(14)\n\t" \
  "ds_read_u16_d16_hi v170, v170 offset:0\n\t" \
  "ds_read_b64_tr_b16 v[220:221], v249 offset:12800\n\t" \
  "v_lshlrev_b32_sdwa v159, 1, v159 dst_sel:DWORD dst_unused:UNUSED_PAD src0_sel:DWORD src1_sel:WORD_1\n\t" \
  "v_pk_fma_f16 v225, v224, v224, %[h1] neg_lo:[1,0,0] neg_hi:[1,0,0]\n\t" \
  "v_smfmac_f32_16x16x64_bf16 v[40:43], v[0:3], v[184:191], v248\n\t" \
  "ds_read_u16_d16_hi v171, v171 offset:0\n\t" \
  "v_pk_mul_f16 v225, v224, v225\n\t" \
  "v_smfmac_f32_16x16x64_bf16 v[64:67], v[0:3], v[192:199], v248\n\t" \
  "ds_read_b64_tr_b16 v[222:223], v249 offset:13056\n\t" \
  "v_lshlrev_b32_sdwa v164, 1, v164 dst_sel:DWORD dst_unused:UNUSED_PAD src0_sel:DWORD src1_sel:WORD_1\n\t" \
  "v_smfmac_f32_16x16x64_bf16 v[88:91], v[0:3], v[200:207], v248\n\t" \
  "ds_read_u16_d16_hi v158, v158 offset:0\n\t" \
  "ds_read_b64_tr_b16 v[0:1], v250 offset:53264\n\t" \
  "v_exp_f16 v224, v225\n\t" \
  "v_lshlrev_b32_sdwa v165, 1, v165 dst_sel:DWORD dst_unused:UNUSED_PAD src0_sel:DWORD src1_sel:WORD_1\n\t" \
  "v_smfmac_f32_16x16x64_bf16 v[44:47], v[4:7], v[184:191], v248\n\t" \
  "ds_read_u16_d16_hi v159, v159 offset:0\n\t" \
  "v_exp_f16_sdwa v224, v225 dst_sel:WORD_1 dst_unused:UNUSED_PRESERVE src0_sel:WORD_1\n\t" \
  "v_smfmac_f32_16x16x64_bf16 v[68:71], v[4:7], v[192:199], v248\n\t" \
  "ds_read_b64_tr_b16 v[2:3], v250 offset:54032\n\t" \
  "v_lshlrev_b32_sdwa v166, 1, v166 dst_sel:DWORD dst_unused:UNUSED_PAD src0_sel:DWORD src1_sel:WORD_1\n\t" \
  "v_smfmac_f32_16x16x64_bf16 v[92:95], v[4:7], v[200:207], v248\n\t" \
  "s_waitcnt lgkmcnt(14)\n\t" \
  "ds_read_u16_d16_hi v164, v164 offset:0\n\t" \
  "ds_read_b64_tr_b16 v[4:5], v250 offset:53272\n\t" \
  "v_pk_fma_f16 v224, v224, %[hc], %[hc]\n\t" \
  "v_lshlrev_b32_sdwa v167, 1, v167 dst_sel:DWORD dst_unused:UNUSED_PAD src0_sel:DWORD src1_sel:WORD_1\n\t" \
  "v_mfma_f32_16x16x32_bf16 v[192:195], v[8:11], v[120:123], 0\n\t" \
  "ds_read_u16_d16_hi v165, v165 offset:0\n\t" \
  "ds_read_b64_tr_b16 v[6:7], v250 offset:54040\n\t" \
  "v_rcp_f16 v225, v224\n\t" \
  "v_mfma_f32_16x16x32_bf16 v[200:203], v[8:11], v[132:135], 0\n\t" \
  "ds_read_u16_d16_hi v166, v166 offset:0\n\t" \
  "ds_read_b64_tr_b16 v[8:9], v250 offset:53328\n\t" \
  "v_lshlrev_b32_sdwa v172, 1, v172 dst_sel:DWORD dst_unused:UNUSED_PAD src0_sel:DWORD src1_sel:WORD_1\n\t" \
  "v_rcp_f16_sdwa v224, v224 dst_sel:DWORD dst_unused:UNUSED_PAD src0_sel:WORD_1\n\t" \
  "v_mfma_f32_16x16x32_bf16 v[192:195], v[12:15], v[124:127], v[192:195]\n\t" \
  "ds_read_u16_d16_hi v167, v167 offset:0\n\t" \
  "ds_read_b64_tr_b16 v[10:11], v250 offset:54096\n\t" \
  "v_lshlrev_b32_sdwa v173, 1, v173 dst_sel:DWORD dst_unused:UNUSED_PAD src0_sel:DWORD src1_sel:WORD_1\n\t" \
  "v_mfma_f32_16x16x32_bf16 v[200:203], v[12:15], v[136:139], v[200:203]\n\t" \
  "ds_read_u16_d16_hi v172, v172 offset:0\n\t" \
  "ds_read_b64_tr_b16 v[12:13], v250 offset:53336\n\t" \
  "v_fma_mix_f32 v156, v156, v225, 0 op_sel_hi:[0,1,0]\n\t" \
  "v_lshlrev_b32_sdwa v174, 1, v174 dst_sel:DWORD dst_unused:UNUSED_PAD src0_sel:DWORD src1_sel:WORD_1\n\t" \
  "v_fma_mix_f32 v157, v157, v224, 0 op_sel_hi:[0,1,0]\n\t" \
  "v_mfma_f32_16x16x32_bf16 v[192:195], v[208:211], v[128:131], v[192:195]\n\t" \
  "ds_read_u16_d16_hi v173, v173 offset:0\n\t" \
  "ds_read_b64_tr_b16 v[14:15], v250 offset:54104\n\t" \
  "v_mfma_f32_16x16x32_bf16 v[200:203], v[208:211], v[140:143], v[200:203]\n\t" \
  "s_waitcnt lgkmcnt(14)\n\t" \
  "ds_read_u16_d16_hi v174, v174 offset:0\n\t" \
  "ds_read_b64_tr_b16 v[208:209], v250 offset:53392\n\t" \
  "v_lshlrev_b32_sdwa v175, 1, v175 dst_sel:DWORD dst_unused:UNUSED_PAD src0_sel:DWORD src1_sel:WORD_1\n\t" \
  "s_nop 0\n\t" \
  "v_lshlrev_b32_sdwa v194, 1, v194 dst_sel:DWORD dst_unused:UNUSED_PAD src0_sel:DWORD src1_sel:WORD_1\n\t" \
  "v_mul_f32 v192, %[kk], v192\n\t" \
  "v_mfma_f32_16x16x32_bf16 v[196:199], v[212:215], v[120:123], 0\n\t" \
  "ds_read_u16_d16_hi v175, v175 offset:0\n\t" \
  "ds_read_b64_tr_b16 v[210:211], v250 offset:54160\n\t" \
  "v_mul_f32 v193, %[kk], v193\n\t" \
  "v_mfma_f32_16x16x32_bf16 v[204:207], v[212:215], v[132:135], 0\n\t" \
  "ds_read_u16_d16_hi v194, v194 offset:0\n\t" \
  "ds_read_b64_tr_b16 v[212:213], v250 offset:53400\n\t" \
  "v_cvt_pk_f16_f32 v224, v192, v193\n\t" \
  "v_lshlrev_b32_sdwa v195, 1, v195 dst_sel:DWORD dst_unused:UNUSED_PAD src0_sel:DWORD src1_sel:WORD_1\n\t" \
  "v_pk_fma_f16 v225, v224, v224, %[h1] neg_lo:[1,0,0] neg_hi:[1,0,0]\n\t" \
  "v_mfma_f32_16x16x32_bf16 v[196:199], v[216:219], v[124:127], v[196:199]\n\t" \
  "ds_read_u16_d16_hi v195, v195 offset:0\n\t" \
  "ds_read_b64_tr_b16 v[214:215], v250 offset:54168\n\t" \
  "v_pk_mul_f16 v225, v224, v225\n\t" \
  "v_mfma_f32_16x16x32_bf16 v[204:207], v[216:219], v[136:139], v[204:207]\n\t" \
  "ds_read_b64_tr_b16 v[216:217], v249 offset:20512\n\t" \
  "ds_read_b64_tr_b16 v[218:219], v249 offset:20768\n\t" \
  "v_exp_f16 v224, v225\n\t" \
  "v_lshlrev_b32_sdwa v200, 1, v200 dst_sel:DWORD dst_unused:UNUSED_PAD src0_sel:DWORD src1_sel:WORD_1\n\t" \
  "v_mfma_f32_16x16x32_bf16 v[196:199], v[220:223], v[128:131], v[196:199]\n\t" \
  "ds_read_u16_d16_hi v200, v200 offset:0\n\t" \
  "v_exp_f16_sdwa v224, v225 dst_sel:WORD_1 dst_unused:UNUSED_PRESERVE src0_sel:WORD_1\n\t" \
  "v_mfma_f32_16x16x32_bf16 v[204:207], v[220:223], v[140:143], v[204:207]\n\t" \
  "s_waitcnt lgkmcnt(14)\n\t" \
  "ds_read_b64_tr_b16 v[220:221], v249 offset:22560\n\t" \
  "ds_read_b64_tr_b16 v[222:223], v249 offset:22816\n\t" \
  "v_smfmac_f32_16x16x64_bf16 v[24:27], v[0:3], v[152:159], v248\n\t" \
  "v_lshlrev_b32_sdwa v201, 1, v201 dst_sel:DWORD dst_unused:UNUSED_PAD src0_sel:DWORD src1_sel:WORD_1\n\t" \
  "v_pk_fma_f16 v224, v224, %[hc], %[hc]\n\t" \
  "v_smfmac_f32_16x16x64_bf16 v[48:51], v[0:3], v[160:167], v248\n\t" \
  "s_waitcnt lgkmcnt(10)\n\t" \
  "ds_read_u16_d16_hi v201, v201 offset:0\n\t" \
  "v_rcp_f16 v225, v224\n\t" \
  "v_smfmac_f32_16x16x64_bf16 v[72:75], v[0:3], v[168:175], v248\n\t" \
  "ds_read_b64_tr_b16 v[0:1], v249 offset:24608\n\t" \
  "v_lshlrev_b32_sdwa v202, 1, v202 dst_sel:DWORD dst_unused:UNUSED_PAD src0_sel:DWORD src1_sel:WORD_1\n\t" \
  "v_smfmac_f32_16x16x64_bf16 v[28:31], v[4:7], v[152:159], v248\n\t" \
  "ds_read_u16_d16_hi v202, v202 offset:0\n\t" \
  "v_rcp_f16_sdwa v224, v224 dst_sel:DWORD dst_unused:UNUSED_PAD src0_sel:WORD_1\n\t" \
  "v_smfmac_f32_16x16x64_bf16 v[52:55], v[4:7], v[160:167], v248\n\t" \
  "ds_read_b64_tr_b16 v[2:3], v249 offset:24864\n\t" \
  "v_cvt_pk_bf16_f32 v230, v24, v25\n\t" \
  "v_smfmac_f32_16x16x64_bf16 v[76:79], v[4:7], v[168:175], v248\n\t" \
  "ds_read_b64_tr_b16 v[4:5], v249 offset:21024\n\t" \
  "v_cvt_pk_bf16_f32 v231, v26, v27\n\t" \
  "v_smfmac_f32_16x16x64_bf16 v[32:35], v[8:11], v[152:159], v248\n\t" \
  "ds_read_b64_tr_b16 v[6:7], v249 offset:21280\n\t" \
  "v_lshlrev_b32_sdwa v203, 1, v203 dst_sel:DWORD dst_unused:UNUSED_PAD src0_sel:DWORD src1_sel:WORD_1\n\t" \
  "v_fma_mix_f32 v192, v192, v225, 0 op_sel_hi:[0,1,0]\n\t" \
  "v_cvt_pk_bf16_f32 v232, v28, v29\n\t" \
  "v_smfmac_f32_16x16x64_bf16 v[56:59], v[8:11], v[160:167], v248\n\t" \
  "ds_read_u16_d16_hi v203, v203 offset:0\n\t" \
  "v_fma_mix_f32 v193, v193, v224, 0 op_sel_hi:[0,1,0]\n\t" \
  "v_cvt_pk_bf16_f32 v233, v30, v31\n\t" \
  "v_smfmac_f32_16x16x64_bf16 v[80:83], v[8:11], v[168:175], v248\n\t" \
  "ds_read_b64_tr_b16 v[8:9], v249 offset:23072\n\t" \
  "v_mul_f32 v196, %[kk], v196\n\t" \
  "v_smfmac_f32_16x16x64_bf16 v[36:39], v[12:15], v[152:159], v248\n\t" \
  "buffer_store_dwordx4 v[230:233], v253, %[osrd], %[ob] offen offset:0 sc1\n\t" \
  "v_lshlrev_b32_sdwa v198, 1, v198 dst_sel:DWORD dst_unused:UNUSED_PAD src0_sel:DWORD src1_sel:WORD_1\n\t" \
  "v_mul_f32 v197, %[kk], v197\n\t" \
  "v_smfmac_f32_16x16x64_bf16 v[60:63], v[12:15], v[160:167], v248\n\t" \
  "ds_read_b64_tr_b16 v[10:11], v249 offset:23328\n\t" \
  "v_cvt_pk_f16_f32 v224, v196, v197\n\t" \
  "v_smfmac_f32_16x16x64_bf16 v[84:87], v[12:15], v[168:175], v248\n\t" \
  "s_waitcnt lgkmcnt(14)\n\t" \
  "ds_read_u16_d16_hi v198, v198 offset:0\n\t" \
  "v_cvt_pk_bf16_f32 v234, v32, v33\n\t" \
  "v_pk_fma_f16 v225, v224, v224, %[h1] neg_lo:[1,0,0] neg_hi:[1,0,0]\n\t" \
  "v_cvt_pk_bf16_f32 v235, v34, v35\n\t" \
  "v_smfmac_f32_16x16x64_bf16 v[40:43], v[208:211], v[152:159], v248\n\t" \
  "ds_read_b64_tr_b16 v[12:13], v249 offset:25120\n\t" \
  "v_lshlrev_b32_sdwa v199, 1, v199 dst_sel:DWORD dst_unused:UNUSED_PAD src0_sel:DWORD src1_sel:WORD_1\n\t" \
  "v_pk_mul_f16 v225, v224, v225\n\t" \
  "v_cvt_pk_bf16_f32 v236, v36, v37\n\t" \
  "v_smfmac_f32_16x16x64_bf16 v[64:67], v[208:211], v[160:167], v248\n\t" \
  "ds_read_u16_d16_hi v199, v199 offset:0\n\t" \
  "v_cvt_pk_bf16_f32 v237, v38, v39\n\t" \
  "v_smfmac_f32_16x16x64_bf16 v[88:91], v[208:211], v[168:175], v248\n\t" \
  "ds_read_b64_tr_b16 v[14:15], v249 offset:25376\n\t" \
  "v_exp_f16 v224, v225\n\t" \
  "v_smfmac_f32_16x16x64_bf16 v[44:47], v[212:215], v[152:159], v248\n\t" \
  "buffer_store_dwordx4 v[234:237], v253, %[osrd], %[ob] offen offset:64 sc1\n\t" \
  "v_lshlrev_b32_sdwa v204, 1, v204 dst_sel:DWORD dst_unused:UNUSED_PAD src0_sel:DWORD src1_sel:WORD_1\n\t" \
  "v_smfmac_f32_16x16x64_bf16 v[68:71], v[212:215], v[160:167], v248\n\t" \
  "v_add_u32 v250, 57344, v250\n\t" \
  "ds_read_b64_tr_b16 v[208:209], v250 offset:8240\n\t" \
  "v_exp_f16_sdwa v224, v225 dst_sel:WORD_1 dst_unused:UNUSED_PRESERVE src0_sel:WORD_1\n\t" \
  "v_smfmac_f32_16x16x64_bf16 v[92:95], v[212:215], v[168:175], v248\n\t" \
  "s_waitcnt lgkmcnt(14)\n\t" \
  "ds_read_b64_tr_b16 v[210:211], v250 offset:9008\n\t" \
  "v_cvt_pk_bf16_f32 v238, v40, v41\n\t" \
  "v_pk_fma_f16 v224, v224, %[hc], %[hc]\n\t" \
  "v_cvt_pk_bf16_f32 v239, v42, v43\n\t" \
  "v_lshlrev_b32_sdwa v205, 1, v205 dst_sel:DWORD dst_unused:UNUSED_PAD src0_sel:DWORD src1_sel:WORD_1\n\t" \
  "v_cvt_pk_bf16_f32 v240, v44, v45\n\t" \
  "v_mfma_f32_16x16x32_bf16 v[160:163], v[216:219], v[120:123], 0\n\t" \
  "ds_read_u16_d16_hi v204, v204 offset:0\n\t" \
  "ds_read_b64_tr_b16 v[212:213], v250 offset:8248\n\t" \
  "v_rcp_f16 v225, v224\n\t" \
  "v_cvt_pk_bf16_f32 v241, v46, v47\n\t" \
  "v_mfma_f32_16x16x32_bf16 v[168:171], v[216:219], v[132:135], 0\n\t" \
  "ds_read_b64_tr_b16 v[214:215], v250 offset:9016\n\t" \
  "buffer_store_dwordx4 v[238:241], v253, %[osrd], %[ob] offen offset:128 sc1\n\t" \
  "v_lshlrev_b32_sdwa v206, 1, v206 dst_sel:DWORD dst_unused:UNUSED_PAD src0_sel:DWORD src1_sel:WORD_1\n\t" \
  "v_rcp_f16_sdwa v224, v224 dst_sel:DWORD dst_unused:UNUSED_PAD src0_sel:WORD_1\n\t" \
  "v_mfma_f32_16x16x32_bf16 v[160:163], v[220:223], v[124:127], v[160:163]\n\t" \
  "ds_read_b64_tr_b16 v[216:217], v250 offset:8304\n\t" \
  "ds_read_u16_d16_hi v205, v205 offset:0\n\t" \
  "v_mfma_f32_16x16x32_bf16 v[168:171], v[220:223], v[136:139], v[168:171]\n\t" \
  "s_waitcnt lgkmcnt(14)\n\t" \
  "ds_read_b64_tr_b16 v[218:219], v250 offset:9072\n\t" \
  "ds_read_u16_d16_hi v206, v206 offset:0\n\t" \
  "v_fma_mix_f32 v196, v196, v225, 0 op_sel_hi:[0,1,0]\n\t" \
  "v_lshlrev_b32_sdwa v207, 1, v207 dst_sel:DWORD dst_unused:UNUSED_PAD src0_sel:DWORD src1_sel:WORD_1\n\t" \
  "v_fma_mix_f32 v197, v197, v224, 0 op_sel_hi:[0,1,0]\n\t" \
  "v_mfma_f32_16x16x32_bf16 v[160:163], v[0:3], v[128:131], v[160:163]\n\t" \
  "ds_read_b64_tr_b16 v[220:221], v250 offset:8312\n\t" \
  "ds_read_u16_d16_hi v207, v207 offset:0\n\t" \
  "v_mfma_f32_16x16x32_bf16 v[168:171], v[0:3], v[140:143], v[168:171]\n\t" \
  "ds_read_b64_tr_b16 v[222:223], v250 offset:9080\n\t" \
  "ds_read_b64_tr_b16 v[0:1], v250 offset:8368\n\t" \
  "s_nop 2\n\t" \
  "v_lshlrev_b32_sdwa v162, 1, v162 dst_sel:DWORD dst_unused:UNUSED_PAD src0_sel:DWORD src1_sel:WORD_1\n\t" \
  "v_mul_f32 v160, %[kk], v160\n\t" \
  "v_mfma_f32_16x16x32_bf16 v[164:167], v[4:7], v[120:123], 0\n\t" \
  "ds_read_u16_d16_hi v162, v162 offset:0\n\t" \
  "ds_read_b64_tr_b16 v[2:3], v250 offset:9136\n\t" \
  "v_mul_f32 v161, %[kk], v161\n\t" \
  "v_mfma_f32_16x16x32_bf16 v[172:175], v[4:7], v[132:135], 0\n\t" \
  "s_waitcnt lgkmcnt(14)\n\t" \
  "ds_read_b64_tr_b16 v[4:5], v250 offset:8376\n\t" \
  "ds_read_b64_tr_b16 v[6:7], v250 offset:9144\n\t" \
  "v_cvt_pk_f16_f32 v224, v160, v161\n\t" \
  "v_lshlrev_b32_sdwa v163, 1, v163 dst_sel:DWORD dst_unused:UNUSED_PAD src0_sel:DWORD src1_sel:WORD_1\n\t" \
  "v_pk_fma_f16 v225, v224, v224, %[h1] neg_lo:[1,0,0] neg_hi:[1,0,0]\n\t" \
  "v_mfma_f32_16x16x32_bf16 v[164:167], v[8:11], v[124:127], v[164:167]\n\t" \
  "ds_read_u16_d16_hi v163, v163 offset:0\n\t" \
  "v_pk_mul_f16 v225, v224, v225\n\t" \
  "v_mfma_f32_16x16x32_bf16 v[172:175], v[8:11], v[136:139], v[172:175]\n\t" \
  "ds_read_b64_tr_b16 v[8:9], v249 offset:32832\n\t" \
  "ds_read_b64_tr_b16 v[10:11], v249 offset:33088\n\t" \
  "v_exp_f16 v224, v225\n\t" \
  "v_lshlrev_b32_sdwa v168, 1, v168 dst_sel:DWORD dst_unused:UNUSED_PAD src0_sel:DWORD src1_sel:WORD_1\n\t" \
  "v_mfma_f32_16x16x32_bf16 v[164:167], v[12:15], v[128:131], v[164:167]\n\t" \
  "ds_read_u16_d16_hi v168, v168 offset:0\n\t" \
  "v_exp_f16_sdwa v224, v225 dst_sel:WORD_1 dst_unused:UNUSED_PRESERVE src0_sel:WORD_1\n\t" \
  "v_mfma_f32_16x16x32_bf16 v[172:175], v[12:15], v[140:143], v[172:175]\n\t" \
  "s_waitcnt lgkmcnt(14)\n\t" \
  "ds_read_b64_tr_b16 v[12:13], v249 offset:34880\n\t" \
  "ds_read_b64_tr_b16 v[14:15], v249 offset:35136\n\t" \
  "v_lshlrev_b32_sdwa v169, 1, v169 dst_sel:DWORD dst_unused:UNUSED_PAD src0_sel:DWORD src1_sel:WORD_1\n\t" \
  "v_pk_fma_f16 v224, v224, %[hc], %[hc]\n\t" \
  "v_smfmac_f32_16x16x64_bf16 v[48:51], v[208:211], v[192:199], v248\n\t" \
  "s_waitcnt lgkmcnt(12)\n\t" \
  "ds_read_u16_d16_hi v169, v169 offset:0\n\t" \
  "v_rcp_f16 v225, v224\n\t" \
  "v_smfmac_f32_16x16x64_bf16 v[72:75], v[208:211], v[200:207], v248\n\t" \
  "ds_read_b64_tr_b16 v[208:209], v249 offset:36928\n\t" \
  "ds_read_b64_tr_b16 v[210:211], v249 offset:37184\n\t" \
  "v_lshlrev_b32_sdwa v170, 1, v170 dst_sel:DWORD dst_unused:UNUSED_PAD src0_sel:DWORD src1_sel:WORD_1\n\t" \
  "v_smfmac_f32_16x16x64_bf16 v[52:55], v[212:215], v[192:199], v248\n\t" \
  "s_add_u32 %[ob], %[ob], %[dob]\n\t" \
  "ds_read_u16_d16_hi v170, v170 offset:0\n\t" \
  "v_rcp_f16_sdwa v224, v224 dst_sel:DWORD dst_unused:UNUSED_PAD src0_sel:WORD_1\n\t" \
  "v_smfmac_f32_16x16x64_bf16 v[76:79], v[212:215], v[200:207], v248\n\t" \
  "ds_read_b64_tr_b16 v[212:213], v249 offset:33344\n\t" \
  "ds_read_b64_tr_b16 v[214:215], v249 offset:33600\n\t" \
  "v_lshlrev_b32_sdwa v171, 1, v171 dst_sel:DWORD dst_unused:UNUSED_PAD src0_sel:DWORD src1_sel:WORD_1\n\t" \
  "v_fma_mix_f32 v160, v160, v225, 0 op_sel_hi:[0,1,0]\n\t" \
  "v_smfmac_f32_16x16x64_bf16 v[56:59], v[216:219], v[192:199], v248\n\t" \
  "ds_read_u16_d16_hi v171, v171 offset:0\n\t" \
  "v_fma_mix_f32 v161, v161, v224, 0 op_sel_hi:[0,1,0]\n\t" \
  "v_smfmac_f32_16x16x64_bf16 v[80:83], v[216:219], v[200:207], v248\n\t" \
  "s_waitcnt lgkmcnt(14)\n\t" \
  "ds_read_b64_tr_b16 v[216:217], v249 offset:35392\n\t" \
  "ds_read_b64_tr_b16 v[218:219], v249 offset:35648\n\t" \
  "v_mul_f32 v164, %[kk], v164\n\t" \
  "v_lshlrev_b32_sdwa v166, 1, v166 dst_sel:DWORD dst_unused:UNUSED_PAD src0_sel:DWORD src1_sel:WORD_1\n\t" \
  "v_mul_f32 v165, %[kk], v165\n\t" \
  "v_smfmac_f32_16x16x64_bf16 v[60:63], v[220:223], v[192:199], v248\n\t" \
  "ds_read_u16_d16_hi v166, v166 offset:0\n\t" \
  "v_cvt_pk_f16_f32 v224, v164, v165\n\t" \
  "v_smfmac_f32_16x16x64_bf16 v[84:87], v[220:223], v[200:207], v248\n\t" \
  "ds_read_b64_tr_b16 v[220:221], v249 offset:37440\n\t" \
  "ds_read_b64_tr_b16 v[222:223], v249 offset:37696\n\t" \
  "v_pk_fma_f16 v225, v224, v224, %[h1] neg_lo:[1,0,0] neg_hi:[1,0,0]\n\t" \
  "v_lshlrev_b32_sdwa v167, 1, v167 dst_sel:DWORD dst_unused:UNUSED_PAD src0_sel:DWORD src1_sel:WORD_1\n\t" \
  "v_pk_mul_f16 v225, v224, v225\n\t" \
  "v_smfmac_f32_16x16x64_bf16 v[64:67], v[0:3], v[192:199], v248\n\t" \
  "ds_read_u16_d16_hi v167, v167 offset:0\n\t" \
  "v_exp_f16 v224, v225\n\t" \
  "v_smfmac_f32_16x16x64_bf16 v[88:91], v[0:3], v[200:207], v248\n\t" \
  "s_waitcnt lgkmcnt(14)\n\t" \
  "ds_read_b64_tr_b16 v[0:1], v250 offset:20560\n\t" \
  "ds_read_b64_tr_b16 v[2:3], v250 offset:21328\n\t" \
  "v_lshlrev_b32_sdwa v172, 1, v172 dst_sel:DWORD dst_unused:UNUSED_PAD src0_sel:DWORD src1_sel:WORD_1\n\t" \
  "v_smfmac_f32_16x16x64_bf16 v[68:71], v[4:7], v[192:199], v248\n\t" \
  "ds_read_u16_d16_hi v172, v172 offset:0\n\t" \
  "v_exp_f16_sdwa v224, v225 dst_sel:WORD_1 dst_unused:UNUSED_PRESERVE src0_sel:WORD_1\n\t" \
  "v_smfmac_f32_16x16x64_bf16 v[92:95], v[4:7], v[200:207], v248\n\t" \
  "ds_read_b64_tr_b16 v[4:5], v250 offset:20568\n\t" \
  "ds_read_b64_tr_b16 v[6:7], v250 offset:21336\n\t" \
  "v_pk_fma_f16 v224, v224, %[hc], %[hc]\n\t" \
  "v_lshlrev_b32_sdwa v173, 1, v173 dst_sel:DWORD dst_unused:UNUSED_PAD src0_sel:DWORD src1_sel:WORD_1\n\t" \
  "v_mfma_f32_16x16x32_bf16 v[192:195], v[8:11], v[120:123], 0\n\t" \
  "ds_read_u16_d16_hi v173, v173 offset:0\n\t" \
  "v_rcp_f16 v225, v224\n\t" \
  "v_mfma_f32_16x16x32_bf16 v[200:203], v[8:11], v[132:135], 0\n\t" \
  "s_waitcnt lgkmcnt(14)\n\t" \
  "ds_read_b64_tr_b16 v[8:9], v250 offset:20624\n\t" \
  "ds_read_b64_tr_b16 v[10:11], v250 offset:21392\n\t" \
  "v_lshlrev_b32_sdwa v174, 1, v174 dst_sel:DWORD dst_unused:UNUSED_PAD src0_sel:DWORD src1_sel:WORD_1\n\t" \
  "v_rcp_f16_sdwa v224, v224 dst_sel:DWORD dst_unused:UNUSED_PAD src0_sel:WORD_1\n\t" \
  "v_mfma_f32_16x16x32_bf16 v[192:195], v[12:15], v[124:127], v[192:195]\n\t" \
  "ds_read_u16_d16_hi v174, v174 offset:0\n\t" \
  "v_fma_mix_f32 v164, v164, v225, 0 op_sel_hi:[0,1,0]\n\t" \
  "v_mfma_f32_16x16x32_bf16 v[200:203], v[12:15], v[136:139], v[200:203]\n\t" \
  "ds_read_b64_tr_b16 v[12:13], v250 offset:20632\n\t" \
  "ds_read_b64_tr_b16 v[14:15], v250 offset:21400\n\t" \
  "v_lshlrev_b32_sdwa v175, 1, v175 dst_sel:DWORD dst_unused:UNUSED_PAD src0_sel:DWORD src1_sel:WORD_1\n\t" \
  "v_fma_mix_f32 v165, v165, v224, 0 op_sel_hi:[0,1,0]\n\t" \
  "v_mfma_f32_16x16x32_bf16 v[192:195], v[208:211], v[128:131], v[192:195]\n\t" \
  "ds_read_u16_d16_hi v175, v175 offset:0\n\t" \
  "v_mfma_f32_16x16x32_bf16 v[200:203], v[208:211], v[140:143], v[200:203]\n\t" \
  "s_waitcnt lgkmcnt(14)\n\t" \
  "ds_read_b64_tr_b16 v[208:209], v250 offset:20688\n\t" \
  "ds_read_b64_tr_b16 v[210:211], v250 offset:21456\n\t" \
  "s_nop 2\n\t" \
  "v_lshlrev_b32_sdwa v194, 1, v194 dst_sel:DWORD dst_unused:UNUSED_PAD src0_sel:DWORD src1_sel:WORD_1\n\t" \
  "v_mul_f32 v192, %[kk], v192\n\t" \
  "v_mfma_f32_16x16x32_bf16 v[196:199], v[212:215], v[120:123], 0\n\t" \
  "ds_read_u16_d16_hi v194, v194 offset:0\n\t" \
  "v_mul_f32 v193, %[kk], v193\n\t" \
  "v_mfma_f32_16x16x32_bf16 v[204:207], v[212:215], v[132:135], 0\n\t" \
  "ds_read_b64_tr_b16 v[212:213], v250 offset:20696\n\t" \
  "ds_read_b64_tr_b16 v[214:215], v250 offset:21464\n\t" \
  "v_cvt_pk_f16_f32 v224, v192, v193\n\t" \
  "v_lshlrev_b32_sdwa v195, 1, v195 dst_sel:DWORD dst_unused:UNUSED_PAD src0_sel:DWORD src1_sel:WORD_1\n\t" \
  "v_pk_fma_f16 v225, v224, v224, %[h1] neg_lo:[1,0,0] neg_hi:[1,0,0]\n\t" \
  "v_mfma_f32_16x16x32_bf16 v[196:199], v[216:219], v[124:127], v[196:199]\n\t" \
  "ds_read_u16_d16_hi v195, v195 offset:0\n\t" \
  "v_pk_mul_f16 v225, v224, v225\n\t" \
  "v_mfma_f32_16x16x32_bf16 v[204:207], v[216:219], v[136:139], v[204:207]\n\t" \
  "s_waitcnt lgkmcnt(14)\n\t" \
  "ds_read_b64_tr_b16 v[216:217], v249 offset:45152\n\t" \
  "ds_read_b64_tr_b16 v[218:219], v249 offset:45408\n\t" \
  "v_exp_f16 v224, v225\n\t" \
  "v_lshlrev_b32_sdwa v200, 1, v200 dst_sel:DWORD dst_unused:UNUSED_PAD src0_sel:DWORD src1_sel:WORD_1\n\t" \
  "v_mfma_f32_16x16x32_bf16 v[196:199], v[220:223], v[128:131], v[196:199]\n\t" \
  "ds_read_u16_d16_hi v200, v200 offset:0\n\t" \
  "v_exp_f16_sdwa v224, v225 dst_sel:WORD_1 dst_unused:UNUSED_PRESERVE src0_sel:WORD_1\n\t" \
  "v_mfma_f32_16x16x32_bf16 v[204:207], v[220:223], v[140:143], v[204:207]\n\t" \
  "ds_read_b64_tr_b16 v[220:221], v249 offset:47200\n\t" \
  "ds_read_b64_tr_b16 v[222:223], v249 offset:47456\n\t" \
  "v_lshlrev_b32_sdwa v201, 1, v201 dst_sel:DWORD dst_unused:UNUSED_PAD src0_sel:DWORD src1_sel:WORD_1\n\t" \
  "v_pk_fma_f16 v224, v224, %[hc], %[hc]\n\t" \
  "v_smfmac_f32_16x16x64_bf16 v[48:51], v[0:3], v[160:167], v248\n\t" \
  "s_waitcnt lgkmcnt(11)\n\t" \
  "ds_read_u16_d16_hi v201, v201 offset:0\n\t" \
  "v_rcp_f16 v225, v224\n\t" \
  "v_smfmac_f32_16x16x64_bf16 v[72:75], v[0:3], v[168:175], v248\n\t" \
  "ds_read_b64_tr_b16 v[0:1], v249 offset:49248\n\t" \
  "ds_read_b64_tr_b16 v[2:3], v249 offset:49504\n\t" \
  "v_lshlrev_b32_sdwa v202, 1, v202 dst_sel:DWORD dst_unused:UNUSED_PAD src0_sel:DWORD src1_sel:WORD_1\n\t" \
  "v_smfmac_f32_16x16x64_bf16 v[52:55], v[4:7], v[160:167], v248\n\t" \
  "ds_read_u16_d16_hi v202, v202 offset:0\n\t" \
  "v_rcp_f16_sdwa v224, v224 dst_sel:DWORD dst_unused:UNUSED_PAD src0_sel:WORD_1\n\t" \
  "v_smfmac_f32_16x16x64_bf16 v[76:79], v[4:7], v[168:175], v248\n\t" \
  "ds_read_b64_tr_b16 v[4:5], v249 offset:45664\n\t" \
  "ds_read_b64_tr_b16 v[6:7], v249 offset:45920\n\t" \
  "v_lshlrev_b32_sdwa v203, 1, v203 dst_sel:DWORD dst_unused:UNUSED_PAD src0_sel:DWORD src1_sel:WORD_1\n\t" \
  "v_fma_mix_f32 v192, v192, v225, 0 op_sel_hi:[0,1,0]\n\t" \
  "v_smfmac_f32_16x16x64_bf16 v[56:59], v[8:11], v[160:167], v248\n\t" \
  "ds_read_u16_d16_hi v203, v203 offset:0\n\t" \
  "v_fma_mix_f32 v193, v193, v224, 0 op_sel_hi:[0,1,0]\n\t" \
  "v_smfmac_f32_16x16x64_bf16 v[80:83], v[8:11], v[168:175], v248\n\t" \
  "ds_read_b64_tr_b16 v[8:9], v249 offset:47712\n\t" \
  "ds_read_b64_tr_b16 v[10:11], v249 offset:47968\n\t" \
  "v_mul_f32 v196, %[kk], v196\n\t" \
  "v_lshlrev_b32_sdwa v198, 1, v198 dst_sel:DWORD dst_unused:UNUSED_PAD src0_sel:DWORD src1_sel:WORD_1\n\t" \
  "v_mul_f32 v197, %[kk], v197\n\t" \
  "v_smfmac_f32_16x16x64_bf16 v[60:63], v[12:15], v[160:167], v248\n\t" \
  "ds_read_u16_d16_hi v198, v198 offset:0\n\t" \
  "v_cvt_pk_f16_f32 v224, v196, v197\n\t" \
  "v_smfmac_f32_16x16x64_bf16 v[84:87], v[12:15], v[168:175], v248\n\t" \
  "s_waitcnt lgkmcnt(14)\n\t" \
  "ds_read_b64_tr_b16 v[12:13], v249 offset:49760\n\t" \
  "ds_read_b64_tr_b16 v[14:15], v249 offset:50016\n\t" \
  "v_pk_fma_f16 v225, v224, v224, %[h1] neg_lo:[1,0,0] neg_hi:[1,0,0]\n\t" \
  "v_lshlrev_b32_sdwa v199, 1, v199 dst_sel:DWORD dst_unused:UNUSED_PAD src0_sel:DWORD src1_sel:WORD_1\n\t" \
  "v_pk_mul_f16 v225, v224, v225\n\t" \
  "v_smfmac_f32_16x16x64_bf16 v[64:67], v[208:211], v[160:167], v248\n\t" \
  "ds_read_u16_d16_hi v199, v199 offset:0\n\t" \
  "v_exp_f16 v224, v225\n\t" \
  "v_smfmac_f32_16x16x64_bf16 v[88:91], v[208:211], v[168:175], v248\n\t" \
  "ds_read_b64_tr_b16 v[208:209], v250 offset:32880\n\t" \
  "ds_read_b64_tr_b16 v[210:211], v250 offset:33648\n\t" \
  "v_lshlrev_b32_sdwa v204, 1, v204 dst_sel:DWORD dst_unused:UNUSED_PAD src0_sel:DWORD src1_sel:WORD_1\n\t" \
  "v_smfmac_f32_16x16x64_bf16 v[68:71], v[212:215], v[160:167], v248\n\t" \
  "ds_read_u16_d16_hi v204, v204 offset:0\n\t" \
  "v_exp_f16_sdwa v224, v225 dst_sel:WORD_1 dst_unused:UNUSED_PRESERVE src0_sel:WORD_1\n\t" \
  "v_smfmac_f32_16x16x64_bf16 v[92:95], v[212:215], v[168:175], v248\n\t" \
  "s_waitcnt lgkmcnt(14)\n\t" \
  "ds_read_b64_tr_b16 v[212:213], v250 offset:32888\n\t" \
  "ds_read_b64_tr_b16 v[214:215], v250 offset:33656\n\t" \
  "v_pk_fma_f16 v224, v224, %[hc], %[hc]\n\t" \
  "v_lshlrev_b32_sdwa v205, 1, v205 dst_sel:DWORD dst_unused:UNUSED_PAD src0_sel:DWORD src1_sel:WORD_1\n\t" \
  "v_rcp_f16 v225, v224\n\t" \
  "v_mfma_f32_16x16x32_bf16 v[168:171], v[216:219], v[132:135], 0\n\t" \
  "ds_read_u16_d16_hi v205, v205 offset:0\n\t" \
  "ds_read_b64_tr_b16 v[216:217], v250 offset:32944\n\t" \
  "ds_read_b64_tr_b16 v[218:219], v250 offset:33712\n\t" \
  "v_lshlrev_b32_sdwa v206, 1, v206 dst_sel:DWORD dst_unused:UNUSED_PAD src0_sel:DWORD src1_sel:WORD_1\n\t" \
  "v_rcp_f16_sdwa v224, v224 dst_sel:DWORD dst_unused:UNUSED_PAD src0_sel:WORD_1\n\t" \
  "v_mfma_f32_16x16x32_bf16 v[168:171], v[220:223], v[136:139], v[168:171]\n\t" \
  "s_waitcnt lgkmcnt(14)\n\t" \
  "ds_read_u16_d16_hi v206, v206 offset:0\n\t" \
  "ds_read_b64_tr_b16 v[220:221], v250 offset:32952\n\t" \
  "ds_read_b64_tr_b16 v[222:223], v250 offset:33720\n\t" \
  "v_fma_mix_f32 v196, v196, v225, 0 op_sel_hi:[0,1,0]\n\t" \
  "v_lshlrev_b32_sdwa v207, 1, v207 dst_sel:DWORD dst_unused:UNUSED_PAD src0_sel:DWORD src1_sel:WORD_1\n\t" \
  "v_fma_mix_f32 v197, v197, v224, 0 op_sel_hi:[0,1,0]\n\t" \
  "v_mfma_f32_16x16x32_bf16 v[168:171], v[0:3], v[140:143], v[168:171]\n\t" \
  "ds_read_u16_d16_hi v207, v207 offset:0\n\t" \
  "ds_read_b64_tr_b16 v[0:1], v250 offset:33008\n\t" \
  "ds_read_b64_tr_b16 v[2:3], v250 offset:33776\n\t" \
  "s_nop 4\n\t" \
  "v_lshlrev_b32_sdwa v168, 1, v168 dst_sel:DWORD dst_unused:UNUSED_PAD src0_sel:DWORD src1_sel:WORD_1\n\t" \
  "v_mfma_f32_16x16x32_bf16 v[172:175], v[4:7], v[132:135], 0\n\t" \
  "s_waitcnt lgkmcnt(14)\n\t" \
  "ds_read_u16_d16_hi v168, v168 offset:0\n\t" \
  "ds_read_b64_tr_b16 v[4:5], v250 offset:33016\n\t" \
  "ds_read_b64_tr_b16 v[6:7], v250 offset:33784\n\t" \
  "v_mfma_f32_16x16x32_bf16 v[172:175], v[8:11], v[136:139], v[172:175]\n\t" \
  "ds_read_b64_tr_b16 v[8:9], v249 offset:57472\n\t" \
  "ds_read_b64_tr_b16 v[10:11], v249 offset:57728\n\t" \
  "v_lshlrev_b32_sdwa v169, 1, v169 dst_sel:DWORD dst_unused:UNUSED_PAD src0_sel:DWORD src1_sel:WORD_1\n\t" \
  "v_mfma_f32_16x16x32_bf16 v[172:175], v[12:15], v[140:143], v[172:175]\n\t" \
  "s_waitcnt lgkmcnt(14)\n\t" \
  "ds_read_u16_d16_hi v169, v169 offset:0\n\t" \
  "ds_read_b64_tr_b16 v[12:13], v249 offset:59520\n\t" \
  "ds_read_b64_tr_b16 v[14:15], v249 offset:59776\n\t" \
  "v_lshlrev_b32_sdwa v170, 1, v170 dst_sel:DWORD dst_unused:UNUSED_PAD src0_sel:DWORD src1_sel:WORD_1\n\t" \
  "v_smfmac_f32_16x16x64_bf16 v[48:51], v[208:211], v[192:199], v248\n\t" \
  "s_waitcnt lgkmcnt(10)\n\t" \
  "ds_read_u16_d16_hi v170, v170 offset:0\n\t" \
  "v_lshlrev_b32_sdwa v171, 1, v171 dst_sel:DWORD dst_unused:UNUSED_PAD src0_sel:DWORD src1_sel:WORD_1\n\t" \
  "v_smfmac_f32_16x16x64_bf16 v[72:75], v[208:211], v[200:207], v248\n\t" \
  "ds_read_b64_tr_b16 v[208:209], v249 offset:61568\n\t" \
  "ds_read_u16_d16_hi v171, v171 offset:0\n\t" \
  "v_smfmac_f32_16x16x64_bf16 v[52:55], v[212:215], v[192:199], v248\n\t" \
  "ds_read_b64_tr_b16 v[210:211], v249 offset:61824\n\t" \
  "v_cvt_pk_bf16_f32 v230, v48, v49\n\t" \
  "v_smfmac_f32_16x16x64_bf16 v[76:79], v[212:215], v[200:207], v248\n\t" \
  "ds_read_b64_tr_b16 v[212:213], v249 offset:57984\n\t" \
  "ds_read_b64_tr_b16 v[214:215], v249 offset:58240\n\t" \
  "v_cvt_pk_bf16_f32 v231, v50, v51\n\t" \
  "v_smfmac_f32_16x16x64_bf16 v[56:59], v[216:219], v[192:199], v248\n\t" \
  "s_nop 0\n\t" \
  "v_cvt_pk_bf16_f32 v232, v52, v53\n\t" \
  "v_cvt_pk_bf16_f32 v233, v54, v55\n\t" \
  "v_smfmac_f32_16x16x64_bf16 v[80:83], v[216:219], v[200:207], v248\n\t" \
  "buffer_store_dwordx4 v[230:233], v253, %[osrd], %[ob] offen offset:0 sc1\n\t" \
  "ds_read_b64_tr_b16 v[216:217], v249 offset:60032\n\t" \
  "v_lshlrev_b32_sdwa v172, 1, v172 dst_sel:DWORD dst_unused:UNUSED_PAD src0_sel:DWORD src1_sel:WORD_1\n\t" \
  "v_smfmac_f32_16x16x64_bf16 v[60:63], v[220:223], v[192:199], v248\n\t" \
  "ds_read_u16_d16_hi v172, v172 offset:0\n\t" \
  "ds_read_b64_tr_b16 v[218:219], v249 offset:60288\n\t" \
  "v_smfmac_f32_16x16x64_bf16 v[84:87], v[220:223], v[200:207], v248\n\t" \
  "s_waitcnt lgkmcnt(14)\n\t" \
  "ds_read_b64_tr_b16 v[220:221], v249 offset:62080\n\t" \
  "ds_read_b64_tr_b16 v[222:223], v249 offset:62336\n\t" \
  "v_cvt_pk_bf16_f32 v234, v56, v57\n\t" \
  "v_cvt_pk_bf16_f32 v235, v58, v59\n\t" \
  "v_smfmac_f32_16x16x64_bf16 v[64:67], v[0:3], v[192:199], v248\n\t" \
  "v_cvt_pk_bf16_f32 v236, v60, v61\n\t" \
  "v_lshlrev_b32_sdwa v173, 1, v173 dst_sel:DWORD dst_unused:UNUSED_PAD src0_sel:DWORD src1_sel:WORD_1\n\t" \
  "v_smfmac_f32_16x16x64_bf16 v[88:91], v[0:3], v[200:207], v248\n\t" \
  "ds_read_u16_d16_hi v173, v173 offset:0\n\t" \
  "ds_read_b64_tr_b16 v[0:1], v250 offset:45200\n\t" \
  "v_cvt_pk_bf16_f32 v237, v62, v63\n\t" \
  "v_smfmac_f32_16x16x64_bf16 v[68:71], v[4:7], v[192:199], v248\n\t" \
  "buffer_store_dwordx4 v[234:237], v253, %[osrd], %[ob] offen offset:64 sc1\n\t" \
  "ds_read_b64_tr_b16 v[2:3], v250 offset:45968\n\t" \
  "v_smfmac_f32_16x16x64_bf16 v[92:95], v[4:7], v[200:207], v248\n\t" \
  "s_waitcnt lgkmcnt(14)\n\t" \
  "ds_read_b64_tr_b16 v[4:5], v250 offset:45208\n\t" \
  "ds_read_b64_tr_b16 v[6:7], v250 offset:45976\n\t" \
  "v_cvt_pk_bf16_f32 v238, v64, v65\n\t" \
  "v_lshlrev_b32_sdwa v174, 1, v174 dst_sel:DWORD dst_unused:UNUSED_PAD src0_sel:DWORD src1_sel:WORD_1\n\t" \
  "v_cvt_pk_bf16_f32 v239, v66, v67\n\t" \
  "v_cvt_pk_bf16_f32 v240, v68, v69\n\t" \
  "v_mfma_f32_16x16x32_bf16 v[200:203], v[8:11], v[132:135], 0\n\t" \
  "ds_read_u16_d16_hi v174, v174 offset:0\n\t" \
  "ds_read_b64_tr_b16 v[8:9], v250 offset:45264\n\t" \
  "ds_read_b64_tr_b16 v[10:11], v250 offset:46032\n\t" \
  "v_cvt_pk_bf16_f32 v241, v70, v71\n\t" \
  "v_mfma_f32_16x16x32_bf16 v[200:203], v[12:15], v[136:139], v[200:203]\n\t" \
  "s_waitcnt lgkmcnt(14)\n\t" \
  "buffer_store_dwordx4 v[238:241], v253, %[osrd], %[ob] offen offset:128 sc1\n\t" \
  "ds_read_b64_tr_b16 v[12:13], v250 offset:45272\n\t" \
  "ds_read_b64_tr_b16 v[14:15], v250 offset:46040\n\t" \
  "v_lshlrev_b32_sdwa v175, 1, v175 dst_sel:DWORD dst_unused:UNUSED_PAD src0_sel:DWORD src1_sel:WORD_1\n\t" \
  "v_mfma_f32_16x16x32_bf16 v[200:203], v[208:211], v[140:143], v[200:203]\n\t" \
  "s_waitcnt lgkmcnt(14)\n\t" \
  "ds_read_u16_d16_hi v175, v175 offset:0\n\t" \
  "ds_read_b64_tr_b16 v[208:209], v250 offset:45328\n\t" \
  "ds_read_b64_tr_b16 v[210:211], v250 offset:46096\n\t" \
  "s_nop 3\n\t" \
  "v_lshlrev_b32_sdwa v200, 1, v200 dst_sel:DWORD dst_unused:UNUSED_PAD src0_sel:DWORD src1_sel:WORD_1\n\t" \
  "v_mfma_f32_16x16x32_bf16 v[204:207], v[212:215], v[132:135], 0\n\t" \
  "s_waitcnt lgkmcnt(14)\n\t" \
  "ds_read_u16_d16_hi v200, v200 offset:0\n\t" \
  "ds_read_b64_tr_b16 v[212:213], v250 offset:45336\n\t" \
  "ds_read_b64_tr_b16 v[214:215], v250 offset:46104\n\t" \
  "v_mfma_f32_16x16x32_bf16 v[204:207], v[216:219], v[136:139], v[204:207]\n\t" \
  "s_waitcnt lgkmcnt(14)\n\t" \
  "v_add_u32 v249, 61440, v249\n\t" \
  "ds_read_b64_tr_b16 v[216:217], v249 offset:8352\n\t" \
  "ds_read_b64_tr_b16 v[218:219], v249 offset:8608\n\t" \
  "v_lshlrev_b32_sdwa v201, 1, v201 dst_sel:DWORD dst_unused:UNUSED_PAD src0_sel:DWORD src1_sel:WORD_1\n\t" \
  "v_mfma_f32_16x16x32_bf16 v[204:207], v[220:223], v[140:143], v[204:207]\n\t" \
  "s_waitcnt lgkmcnt(7)\n\t" \
  "ds_read_u16_d16_hi v201, v201 offset:0\n\t" \
  "ds_read_b64_tr_b16 v[220:221], v249 offset:10400\n\t" \
  "ds_read_b64_tr_b16 v[222:223], v249 offset:10656\n\t" \
  "v_lshlrev_b32_sdwa v202, 1, v202 dst_sel:DWORD dst_unused:UNUSED_PAD src0_sel:DWORD src1_sel:WORD_1\n\t" \
  "v_smfmac_f32_16x16x64_bf16 v[72:75], v[0:3], v[168:175], v248\n\t" \
  "ds_read_u16_d16_hi v202, v202 offset:0\n\t" \
  "ds_read_b64_tr_b16 v[0:1], v249 offset:12448\n\t" \
  "ds_read_b64_tr_b16 v[2:3], v249 offset:12704\n\t" \
  "v_smfmac_f32_16x16x64_bf16 v[76:79], v[4:7], v[168:175], v248\n\t" \
  "s_add_u32 %[ob], %[ob], %[dob]\n\t" \
  "ds_read_b64_tr_b16 v[4:5], v249 offset:8864\n\t" \
  "ds_read_b64_tr_b16 v[6:7], v249 offset:9120\n\t" \
  "v_lshlrev_b32_sdwa v203, 1, v203 dst_sel:DWORD dst_unused:UNUSED_PAD src0_sel:DWORD src1_sel:WORD_1\n\t" \
  "v_smfmac_f32_16x16x64_bf16 v[80:83], v[8:11], v[168:175], v248\n\t" \
  "ds_read_u16_d16_hi v203, v203 offset:0\n\t" \
  "ds_read_b64_tr_b16 v[8:9], v249 offset:10912\n\t" \
  "ds_read_b64_tr_b16 v[10:11], v249 offset:11168\n\t" \
  "v_lshlrev_b32_sdwa v204, 1, v204 dst_sel:DWORD dst_unused:UNUSED_PAD src0_sel:DWORD src1_sel:WORD_1\n\t" \
  "v_smfmac_f32_16x16x64_bf16 v[84:87], v[12:15], v[168:175], v248\n\t" \
  "s_waitcnt lgkmcnt(14)\n\t" \
  "ds_read_u16_d16_hi v204, v204 offset:0\n\t" \
  "ds_read_b64_tr_b16 v[12:13], v249 offset:12960\n\t" \
  "ds_read_b64_tr_b16 v[14:15], v249 offset:13216\n\t" \
  "v_smfmac_f32_16x16x64_bf16 v[88:91], v[208:211], v[168:175], v248\n\t" \
  "s_waitcnt lgkmcnt(14)\n\t" \
  "ds_read_b64_tr_b16 v[208:209], v250 offset:57520\n\t" \
  "ds_read_b64_tr_b16 v[210:211], v250 offset:58288\n\t" \
  "v_lshlrev_b32_sdwa v205, 1, v205 dst_sel:DWORD dst_unused:UNUSED_PAD src0_sel:DWORD src1_sel:WORD_1\n\t" \
  "v_smfmac_f32_16x16x64_bf16 v[92:95], v[212:215], v[168:175], v248\n\t" \
  "ds_read_u16_d16_hi v205, v205 offset:0\n\t" \
  "ds_read_b64_tr_b16 v[212:213], v250 offset:57528\n\t" \
  "ds_read_b64_tr_b16 v[214:215], v250 offset:58296\n\t" \
  "v_lshlrev_b32_sdwa v206, 1, v206 dst_sel:DWORD dst_unused:UNUSED_PAD src0_sel:DWORD src1_sel:WORD_1\n\t" \
  "v_mfma_f32_16x16x32_bf16 v[168:171], v[216:219], v[132:135], 0\n\t" \
  "s_waitcnt lgkmcnt(14)\n\t" \
  "ds_read_u16_d16_hi v206, v206 offset:0\n\t" \
  "ds_read_b64_tr_b16 v[216:217], v250 offset:57584\n\t" \
  "ds_read_b64_tr_b16 v[218:219], v250 offset:58352\n\t" \
  "v_mfma_f32_16x16x32_bf16 v[168:171], v[220:223], v[136:139], v[168:171]\n\t" \
  "s_waitcnt lgkmcnt(14)\n\t" \
  "ds_read_b64_tr_b16 v[220:221], v250 offset:57592\n\t" \
  "ds_read_b64_tr_b16 v[222:223], v250 offset:58360\n\t" \
  "v_lshlrev_b32_sdwa v207, 1, v207 dst_sel:DWORD dst_unused:UNUSED_PAD src0_sel:DWORD src1_sel:WORD_1\n\t" \
  "v_mfma_f32_16x16x32_bf16 v[168:171], v[0:3], v[140:143], v[168:171]\n\t" \
  "ds_read_u16_d16_hi v207, v207 offset:0\n\t" \
  "ds_read_b64_tr_b16 v[0:1], v250 offset:57648\n\t" \
  "ds_read_b64_tr_b16 v[2:3], v250 offset:58416\n\t" \
  "s_nop 4\n\t" \
  "v_lshlrev_b32_sdwa v168, 1, v168 dst_sel:DWORD dst_unused:UNUSED_PAD src0_sel:DWORD src1_sel:WORD_1\n\t" \
  "v_mfma_f32_16x16x32_bf16 v[172:175], v[4:7], v[132:135], 0\n\t" \
  "s_waitcnt lgkmcnt(14)\n\t" \
  "ds_read_u16_d16_hi v168, v168 offset:0\n\t" \
  "ds_read_b64_tr_b16 v[4:5], v250 offset:57656\n\t" \
  "ds_read_b64_tr_b16 v[6:7], v250 offset:58424\n\t" \
  "v_mfma_f32_16x16x32_bf16 v[172:175], v[8:11], v[136:139], v[172:175]\n\t" \
  "s_waitcnt lgkmcnt(14)\n\t" \
  "v_add_u32 v250, 61440, v250\n\t" \
  "ds_read_b64_tr_b16 v[8:9], v250 offset:8400\n\t" \
  "ds_read_b64_tr_b16 v[10:11], v250 offset:9168\n\t" \
  "v_lshlrev_b32_sdwa v169, 1, v169 dst_sel:DWORD dst_unused:UNUSED_PAD src0_sel:DWORD src1_sel:WORD_1\n\t" \
  "v_mfma_f32_16x16x32_bf16 v[172:175], v[12:15], v[140:143], v[172:175]\n\t" \
  "s_waitcnt lgkmcnt(7)\n\t" \
  "ds_read_u16_d16_hi v169, v169 offset:0\n\t" \
  "ds_read_b64_tr_b16 v[12:13], v250 offset:8408\n\t" \
  "ds_read_b64_tr_b16 v[14:15], v250 offset:9176\n\t" \
  "v_lshlrev_b32_sdwa v170, 1, v170 dst_sel:DWORD dst_unused:UNUSED_PAD src0_sel:DWORD src1_sel:WORD_1\n\t" \
  "v_smfmac_f32_16x16x64_bf16 v[72:75], v[208:211], v[200:207], v248\n\t" \
  "ds_read_u16_d16_hi v170, v170 offset:0\n\t" \
  "ds_read_b64_tr_b16 v[208:209], v250 offset:8464\n\t" \
  "ds_read_b64_tr_b16 v[210:211], v250 offset:9232\n\t" \
  "v_smfmac_f32_16x16x64_bf16 v[76:79], v[212:215], v[200:207], v248\n\t" \
  "ds_read_b64_tr_b16 v[212:213], v250 offset:8472\n\t" \
  "ds_read_b64_tr_b16 v[214:215], v250 offset:9240\n\t" \
  "v_lshlrev_b32_sdwa v171, 1, v171 dst_sel:DWORD dst_unused:UNUSED_PAD src0_sel:DWORD src1_sel:WORD_1\n\t" \
  "v_smfmac_f32_16x16x64_bf16 v[80:83], v[216:219], v[200:207], v248\n\t" \
  "ds_read_u16_d16_hi v171, v171 offset:0\n\t" \
  "ds_read_b64_tr_b16 v[216:217], v250 offset:8528\n\t" \
  "ds_read_b64_tr_b16 v[218:219], v250 offset:9296\n\t" \
  "v_lshlrev_b32_sdwa v172, 1, v172 dst_sel:DWORD dst_unused:UNUSED_PAD src0_sel:DWORD src1_sel:WORD_1\n\t" \
  "v_smfmac_f32_16x16x64_bf16 v[84:87], v[220:223], v[200:207], v248\n\t" \
  "s_waitcnt lgkmcnt(14)\n\t" \
  "ds_read_u16_d16_hi v172, v172 offset:0\n\t" \
  "ds_read_b64_tr_b16 v[220:221], v250 offset:8536\n\t" \
  "ds_read_b64_tr_b16 v[222:223], v250 offset:9304\n\t" \
  "v_smfmac_f32_16x16x64_bf16 v[88:91], v[0:3], v[200:207], v248\n\t" \
  "s_waitcnt lgkmcnt(14)\n\t" \
  "v_lshlrev_b32_sdwa v173, 1, v173 dst_sel:DWORD dst_unused:UNUSED_PAD src0_sel:DWORD src1_sel:WORD_1\n\t" \
  "v_lshlrev_b32_sdwa v174, 1, v174 dst_sel:DWORD dst_unused:UNUSED_PAD src0_sel:DWORD src1_sel:WORD_1\n\t" \
  "v_smfmac_f32_16x16x64_bf16 v[92:95], v[4:7], v[200:207], v248\n\t" \
  "ds_read_u16_d16_hi v173, v173 offset:0\n\t" \
  "ds_read_u16_d16_hi v174, v174 offset:0\n\t" \
  "v_lshlrev_b32_sdwa v175, 1, v175 dst_sel:DWORD dst_unused:UNUSED_PAD src0_sel:DWORD src1_sel:WORD_1\n\t" \
  "ds_read_u16_d16_hi v175, v175 offset:0\n\t" \
  "s_waitcnt lgkmcnt(0)\n\t" \
  "v_smfmac_f32_16x16x64_bf16 v[72:75], v[8:11], v[168:175], v248\n\t" \
  "v_smfmac_f32_16x16x64_bf16 v[76:79], v[12:15], v[168:175], v248\n\t" \
  "s_nop 6\n\t" \
  "v_cvt_pk_bf16_f32 v230, v72, v73\n\t" \
  "v_cvt_pk_bf16_f32 v231, v74, v75\n\t" \
  "v_smfmac_f32_16x16x64_bf16 v[80:83], v[208:211], v[168:175], v248\n\t" \
  "v_cvt_pk_bf16_f32 v232, v76, v77\n\t" \
  "v_cvt_pk_bf16_f32 v233, v78, v79\n\t" \
  "v_smfmac_f32_16x16x64_bf16 v[84:87], v[212:215], v[168:175], v248\n\t" \
  "buffer_store_dwordx4 v[230:233], v253, %[osrd], %[ob] offen offset:0 sc1\n\t" \
  "s_nop 3\n\t" \
  "v_cvt_pk_bf16_f32 v234, v80, v81\n\t" \
  "v_cvt_pk_bf16_f32 v235, v82, v83\n\t" \
  "v_smfmac_f32_16x16x64_bf16 v[88:91], v[216:219], v[168:175], v248\n\t" \
  "v_cvt_pk_bf16_f32 v236, v84, v85\n\t" \
  "v_cvt_pk_bf16_f32 v237, v86, v87\n\t" \
  "v_smfmac_f32_16x16x64_bf16 v[92:95], v[220:223], v[168:175], v248\n\t" \
  "buffer_store_dwordx4 v[234:237], v253, %[osrd], %[ob] offen offset:64 sc1\n\t" \
  "s_nop 3\n\t" \
  "v_cvt_pk_bf16_f32 v238, v88, v89\n\t" \
  "v_cvt_pk_bf16_f32 v239, v90, v91\n\t" \
  "s_nop 0\n\t" \
  "v_cvt_pk_bf16_f32 v240, v92, v93\n\t" \
  "v_cvt_pk_bf16_f32 v241, v94, v95\n\t" \
  "buffer_store_dwordx4 v[238:241], v253, %[osrd], %[ob] offen offset:128 sc1\n\t" \
  ""
#define F_ASM \
  "s_mov_b32 %[lc], 3\n\t" \
  "s_setprio 3\n\t" \
  "s_mul_i32 %[t0], %[dgy], 7\n\t" \
  "s_add_u32 %[gy], %[gy], %[t0]\n\t" \
  "s_add_u32 %[xld], %[xlb], 15184\n\t" \
  "v_mov_b32 v252, 0\n\t" \
  "v_mov_b32 v253, 0\n\t" \
  "s_waitcnt vmcnt(0)\n\t" \
  "s_barrier\n\t" \
  "v_mfma_f32_32x32x8_bf16 v[0:15], v[252:253], v[252:253], 0\n\t" \
  "ds_read_u16 v96, v251 offset:0\n\t" \
  "ds_read_u16 v97, v251 offset:416\n\t" \
  "ds_read_u16 v98, v251 offset:2064\n\t" \
  "ds_read_u16 v99, v251 offset:2480\n\t" \
  "ds_read_u16 v112, v251 offset:8\n\t" \
  "ds_read_u16 v113, v251 offset:424\n\t" \
  "ds_read_u16 v114, v251 offset:2072\n\t" \
  "ds_read_u16 v115, v251 offset:2488\n\t" \
  "v_mfma_f32_32x32x8_bf16 v[16:31], v[252:253], v[252:253], 0\n\t" \
  "ds_read_u16 v128, v251 offset:16\n\t" \
  "ds_read_u16 v129, v251 offset:432\n\t" \
  "ds_read_u16 v130, v251 offset:2080\n\t" \
  "ds_read_u16 v131, v251 offset:2496\n\t" \
  "ds_read_u16 v144, v251 offset:24\n\t" \
  "ds_read_u16 v145, v251 offset:440\n\t" \
  "ds_read_u16 v146, v251 offset:2088\n\t" \
  "ds_read_u16 v147, v251 offset:2504\n\t" \
  "v_mfma_f32_32x32x8_bf16 v[32:47], v[252:253], v[252:253], 0\n\t" \
  "ds_read_u16 v160, v251 offset:32\n\t" \
  "ds_read_u16 v161, v251 offset:448\n\t" \
  "ds_read_u16 v162, v251 offset:2096\n\t" \
  "ds_read_u16 v163, v251 offset:2512\n\t" \
  "ds_read_u16 v176, v251 offset:40\n\t" \
  "ds_read_u16 v177, v251 offset:456\n\t" \
  "ds_read_u16 v178, v251 offset:2104\n\t" \
  "ds_read_u16 v179, v251 offset:2520\n\t" \
  "v_mfma_f32_32x32x8_bf16 v[48:63], v[252:253], v[252:253], 0\n\t" \
  "ds_read_u16 v100, v251 offset:4128\n\t" \
  "ds_read_u16 v101, v251 offset:4544\n\t" \
  "ds_read_u16 v102, v251 offset:6192\n\t" \
  "ds_read_u16 v103, v251 offset:6608\n\t" \
  "ds_read_u16 v116, v251 offset:4136\n\t" \
  "ds_read_u16 v117, v251 offset:4552\n\t" \
  "ds_read_u16 v118, v251 offset:6200\n\t" \
  "ds_read_u16 v119, v251 offset:6616\n\t" \
  "v_mfma_f32_32x32x8_bf16 v[64:79], v[252:253], v[252:253], 0\n\t" \
  "ds_read_u16 v132, v251 offset:4144\n\t" \
  "ds_read_u16 v133, v251 offset:4560\n\t" \
  "ds_read_u16 v134, v251 offset:6208\n\t" \
  "ds_read_u16 v135, v251 offset:6624\n\t" \
  "ds_read_u16 v148, v251 offset:4152\n\t" \
  "ds_read_u16 v149, v251 offset:4568\n\t" \
  "ds_read_u16 v150, v251 offset:6216\n\t" \
  "ds_read_u16 v151, v251 offset:6632\n\t" \
  "v_mfma_f32_32x32x8_bf16 v[80:95], v[252:253], v[252:253], 0\n\t" \
  "ds_read_u16 v164, v251 offset:4160\n\t" \
  "ds_read_u16 v165, v251 offset:4576\n\t" \
  "ds_read_u16 v166, v251 offset:6224\n\t" \
  "ds_read_u16 v167, v251 offset:6640\n\t" \
  "ds_read_u16 v180, v251 offset:4168\n\t" \
  "ds_read_u16 v181, v251 offset:4584\n\t" \
  "ds_read_u16 v182, v251 offset:6232\n\t" \
  "ds_read_u16 v183, v251 offset:6648\n\t" \
  "s_barrier\n\t" \
  "ds_read_b64 v[192:193], v241 offset:0\n\t" \
  "ds_read_b64 v[194:195], v241 offset:400\n\t" \
  "ds_read_b64 v[196:197], v241 offset:800\n\t" \
  "ds_read_b64 v[198:199], v241 offset:1200\n\t" \
  "ds_read_b64 v[200:201], v241 offset:8\n\t" \
  "ds_read_b64 v[202:203], v241 offset:408\n\t" \
  "ds_read_b64 v[204:205], v241 offset:808\n\t" \
  "ds_read_b64 v[206:207], v241 offset:1208\n\t" \
  "s_waitcnt lgkmcnt(4)\n\t" \
  "v_smfmac_f32_16x16x64_bf16 v[0:3], v[96:99], v[192:199], v240\n\t" \
  "ds_read_b64 v[208:209], v241 offset:16\n\t" \
  "ds_read_b64 v[210:211], v241 offset:416\n\t" \
  "ds_read_b64 v[212:213], v241 offset:816\n\t" \
  "ds_read_b64 v[214:215], v241 offset:1216\n\t" \
  "s_waitcnt lgkmcnt(4)\n\t" \
  "v_smfmac_f32_16x16x64_bf16 v[4:7], v[112:115], v[200:207], v240\n\t" \
  "ds_read_b64 v[216:217], v241 offset:24\n\t" \
  "ds_read_b64 v[218:219], v241 offset:424\n\t" \
  "ds_read_b64 v[220:221], v241 offset:824\n\t" \
  "ds_read_b64 v[222:223], v241 offset:1224\n\t" \
  "s_waitcnt lgkmcnt(4)\n\t" \
  "v_smfmac_f32_16x16x64_bf16 v[8:11], v[128:131], v[208:215], v240\n\t" \
  "ds_read_b64 v[192:193], v241 offset:32\n\t" \
  "ds_read_b64 v[194:195], v241 offset:432\n\t" \
  "ds_read_b64 v[196:197], v241 offset:832\n\t" \
  "ds_read_b64 v[198:199], v241 offset:1232\n\t" \
  "s_waitcnt lgkmcnt(4)\n\t" \
  "v_smfmac_f32_16x16x64_bf16 v[12:15], v[144:147], v[216:223], v240\n\t" \
  "ds_read_b64 v[200:201], v241 offset:40\n\t" \
  "ds_read_b64 v[202:203], v241 offset:440\n\t" \
  "ds_read_b64 v[204:205], v241 offset:840\n\t" \
  "ds_read_b64 v[206:207], v241 offset:1240\n\t" \
  "s_add_u32 %[cs], %[cs], 1\n\t" \
  "s_cmp_eq_u32 %[cs], 6\n\t" \
  "s_cselect_b32 %[cs], 0, %[cs]\n\t" \
  "s_cselect_b32 %[t0], -75920, %[ps]\n\t" \
  "v_add_u32 v241, %[t0], v241\n\t" \
  "s_barrier\n\t" \
  "s_waitcnt lgkmcnt(4)\n\t" \
  "v_smfmac_f32_16x16x64_bf16 v[16:19], v[160:163], v[192:199], v240\n\t" \
  "ds_read_b64 v[208:209], v241 offset:0\n\t" \
  "ds_read_b64 v[210:211], v241 offset:400\n\t" \
  "ds_read_b64 v[212:213], v241 offset:800\n\t" \
  "ds_read_b64 v[214:215], v241 offset:1200\n\t" \
  "s_waitcnt lgkmcnt(4)\n\t" \
  "v_smfmac_f32_16x16x64_bf16 v[20:23], v[176:179], v[200:207], v240\n\t" \
  "ds_read_b64 v[216:217], v241 offset:8\n\t" \
  "ds_read_b64 v[218:219], v241 offset:408\n\t" \
  "ds_read_b64 v[220:221], v241 offset:808\n\t" \
  "ds_read_b64 v[222:223], v241 offset:1208\n\t" \
  "s_waitcnt lgkmcnt(4)\n\t" \
  "v_smfmac_f32_16x16x64_bf16 v[0:3], v[100:103], v[208:215], v240\n\t" \
  "ds_read_b64 v[192:193], v241 offset:16\n\t" \
  "ds_read_b64 v[194:195], v241 offset:416\n\t" \
  "ds_read_b64 v[196:197], v241 offset:816\n\t" \
  "ds_read_b64 v[198:199], v241 offset:1216\n\t" \
  "ds_read_u16 v104, v251 offset:8256\n\t" \
  "v_smfmac_f32_16x16x64_bf16 v[24:27], v[96:99], v[208:215], v240\n\t" \
  "ds_read_u16 v105, v251 offset:8672\n\t" \
  "ds_read_u16 v106, v251 offset:10320\n\t" \
  "ds_read_u16 v107, v251 offset:10736\n\t" \
  "ds_read_u16 v120, v251 offset:8264\n\t" \
  "ds_read_u16 v121, v251 offset:8680\n\t" \
  "s_waitcnt lgkmcnt(10)\n\t" \
  "v_smfmac_f32_16x16x64_bf16 v[4:7], v[116:119], v[216:223], v240\n\t" \
  "ds_read_b64 v[200:201], v241 offset:24\n\t" \
  "ds_read_b64 v[202:203], v241 offset:424\n\t" \
  "ds_read_b64 v[204:205], v241 offset:824\n\t" \
  "ds_read_b64 v[206:207], v241 offset:1224\n\t" \
  "ds_read_u16 v122, v251 offset:10328\n\t" \
  "v_smfmac_f32_16x16x64_bf16 v[28:31], v[112:115], v[216:223], v240\n\t" \
  "ds_read_u16 v123, v251 offset:10744\n\t" \
  "ds_read_u16 v136, v251 offset:8272\n\t" \
  "ds_read_u16 v137, v251 offset:8688\n\t" \
  "ds_read_u16 v138, v251 offset:10336\n\t" \
  "ds_read_u16 v139, v251 offset:10752\n\t" \
  "s_waitcnt lgkmcnt(14)\n\t" \
  "v_smfmac_f32_16x16x64_bf16 v[8:11], v[132:135], v[192:199], v240\n\t" \
  "ds_read_b64 v[208:209], v241 offset:32\n\t" \
  "ds_read_b64 v[210:211], v241 offset:432\n\t" \
  "ds_read_b64 v[212:213], v241 offset:832\n\t" \
  "ds_read_b64 v[214:215], v241 offset:1232\n\t" \
  "ds_read_u16 v152, v251 offset:8280\n\t" \
  "v_smfmac_f32_16x16x64_bf16 v[32:35], v[128:131], v[192:199], v240\n\t" \
  "ds_read_u16 v153, v251 offset:8696\n\t" \
  "ds_read_u16 v154, v251 offset:10344\n\t" \
  "ds_read_u16 v155, v251 offset:10760\n\t" \
  "ds_read_u16 v168, v251 offset:8288\n\t" \
  "ds_read_u16 v169, v251 offset:8704\n\t" \
  "s_waitcnt lgkmcnt(14)\n\t" \
  "v_smfmac_f32_16x16x64_bf16 v[12:15], v[148:151], v[200:207], v240\n\t" \
  "ds_read_b64 v[216:217], v241 offset:40\n\t" \
  "ds_read_b64 v[218:219], v241 offset:440\n\t" \
  "ds_read_b64 v[220:221], v241 offset:840\n\t" \
  "ds_read_b64 v[222:223], v241 offset:1240\n\t" \
  "s_add_u32 %[cs], %[cs], 1\n\t" \
  "s_cmp_eq_u32 %[cs], 6\n\t" \
  "s_cselect_b32 %[cs], 0, %[cs]\n\t" \
  "s_cselect_b32 %[t0], -75920, %[ps]\n\t" \
  "v_add_u32 v241, %[t0], v241\n\t" \
  "ds_read_u16 v170, v251 offset:10352\n\t" \
  "v_smfmac_f32_16x16x64_bf16 v[36:39], v[144:147], v[200:207], v240\n\t" \
  "ds_read_u16 v171, v251 offset:10768\n\t" \
  "ds_read_u16 v184, v251 offset:8296\n\t" \
  "ds_read_u16 v185, v251 offset:8712\n\t" \
  "ds_read_u16 v186, v251 offset:10360\n\t" \
  "ds_read_u16 v187, v251 offset:10776\n\t" \
  "s_barrier\n\t" \
  "s_waitcnt lgkmcnt(14)\n\t" \
  "v_smfmac_f32_16x16x64_bf16 v[16:19], v[164:167], v[208:215], v240\n\t" \
  "ds_read_b64 v[192:193], v241 offset:0\n\t" \
  "ds_read_b64 v[194:195], v241 offset:400\n\t" \
  "v_smfmac_f32_16x16x64_bf16 v[40:43], v[160:163], v[208:215], v240\n\t" \
  "ds_read_b64 v[196:197], v241 offset:800\n\t" \
  "ds_read_b64 v[198:199], v241 offset:1200\n\t" \
  "s_waitcnt lgkmcnt(10)\n\t" \
  "v_smfmac_f32_16x16x64_bf16 v[20:23], v[180:183], v[216:223], v240\n\t" \
  "ds_read_b64 v[200:201], v241 offset:8\n\t" \
  "ds_read_b64 v[202:203], v241 offset:408\n\t" \
  "v_smfmac_f32_16x16x64_bf16 v[44:47], v[176:179], v[216:223], v240\n\t" \
  "ds_read_b64 v[204:205], v241 offset:808\n\t" \
  "ds_read_b64 v[206:207], v241 offset:1208\n\t" \
  "s_waitcnt lgkmcnt(4)\n\t" \
  "v_smfmac_f32_16x16x64_bf16 v[0:3], v[104:107], v[192:199], v240\n\t" \
  "ds_read_b64 v[208:209], v241 offset:16\n\t" \
  "ds_read_b64 v[210:211], v241 offset:416\n\t" \
  "ds_read_b64 v[212:213], v241 offset:816\n\t" \
  "ds_read_b64 v[214:215], v241 offset:1216\n\t" \
  "v_smfmac_f32_16x16x64_bf16 v[24:27], v[100:103], v[192:199], v240\n\t" \
  "ds_read_u16 v108, v251 offset:12384\n\t" \
  "ds_read_u16 v109, v251 offset:12800\n\t" \
  "ds_read_u16 v110, v251 offset:14448\n\t" \
  "v_smfmac_f32_16x16x64_bf16 v[48:51], v[96:99], v[192:199], v240\n\t" \
  "ds_read_u16 v111, v251 offset:14864\n\t" \
  "ds_read_u16 v124, v251 offset:12392\n\t" \
  "ds_read_u16 v125, v251 offset:12808\n\t" \
  "s_waitcnt lgkmcnt(10)\n\t" \
  "v_smfmac_f32_16x16x64_bf16 v[4:7], v[120:123], v[200:207], v240\n\t" \
  "ds_read_b64 v[216:217], v241 offset:24\n\t" \
  "ds_read_b64 v[218:219], v241 offset:424\n\t" \
  "ds_read_b64 v[220:221], v241 offset:824\n\t" \
  "ds_read_b64 v[222:223], v241 offset:1224\n\t" \
  "v_smfmac_f32_16x16x64_bf16 v[28:31], v[116:119], v[200:207], v240\n\t" \
  "ds_read_u16 v126, v251 offset:14456\n\t" \
  "ds_read_u16 v127, v251 offset:14872\n\t" \
  "ds_read_u16 v140, v251 offset:12400\n\t" \
  "v_smfmac_f32_16x16x64_bf16 v[52:55], v[112:115], v[200:207], v240\n\t" \
  "ds_read_u16 v141, v251 offset:12816\n\t" \
  "ds_read_u16 v142, v251 offset:14464\n\t" \
  "ds_read_u16 v143, v251 offset:14880\n\t" \
  "s_waitcnt lgkmcnt(14)\n\t" \
  "v_smfmac_f32_16x16x64_bf16 v[8:11], v[136:139], v[208:215], v240\n\t" \
  "ds_read_b64 v[192:193], v241 offset:32\n\t" \
  "ds_read_b64 v[194:195], v241 offset:432\n\t" \
  "ds_read_b64 v[196:197], v241 offset:832\n\t" \
  "ds_read_b64 v[198:199], v241 offset:1232\n\t" \
  "v_smfmac_f32_16x16x64_bf16 v[32:35], v[132:135], v[208:215], v240\n\t" \
  "ds_read_u16 v156, v251 offset:12408\n\t" \
  "ds_read_u16 v157, v251 offset:12824\n\t" \
  "ds_read_u16 v158, v251 offset:14472\n\t" \
  "v_smfmac_f32_16x16x64_bf16 v[56:59], v[128:131], v[208:215], v240\n\t" \
  "ds_read_u16 v159, v251 offset:14888\n\t" \
  "ds_read_u16 v172, v251 offset:12416\n\t" \
  "ds_read_u16 v173, v251 offset:12832\n\t" \
  "s_waitcnt lgkmcnt(14)\n\t" \
  "v_smfmac_f32_16x16x64_bf16 v[12:15], v[152:155], v[216:223], v240\n\t" \
  "ds_read_b64 v[200:201], v241 offset:40\n\t" \
  "ds_read_b64 v[202:203], v241 offset:440\n\t" \
  "ds_read_b64 v[204:205], v241 offset:840\n\t" \
  "ds_read_b64 v[206:207], v241 offset:1240\n\t" \
  "v_smfmac_f32_16x16x64_bf16 v[36:39], v[148:151], v[216:223], v240\n\t" \
  "s_add_u32 %[cs], %[cs], 1\n\t" \
  "s_cmp_eq_u32 %[cs], 6\n\t" \
  "s_cselect_b32 %[cs], 0, %[cs]\n\t" \
  "s_cselect_b32 %[t0], -75920, %[ps]\n\t" \
  "v_add_u32 v241, %[t0], v241\n\t" \
  "ds_read_u16 v174, v251 offset:14480\n\t" \
  "ds_read_u16 v175, v251 offset:14896\n\t" \
  "ds_read_u16 v188, v251 offset:12424\n\t" \
  "v_smfmac_f32_16x16x64_bf16 v[60:63], v[144:147], v[216:223], v240\n\t" \
  "ds_read_u16 v189, v251 offset:12840\n\t" \
  "ds_read_u16 v190, v251 offset:14488\n\t" \
  "ds_read_u16 v191, v251 offset:14904\n\t" \
  "s_waitcnt lgkmcnt(0)\n\t" \
  "s_barrier\n\t" \
  "v_smfmac_f32_16x16x64_bf16 v[16:19], v[168:171], v[192:199], v240\n\t" \
  "ds_read_b64 v[208:209], v241 offset:0\n\t" \
  "ds_read_b64 v[210:211], v241 offset:400\n\t" \
  "v_smfmac_f32_16x16x64_bf16 v[40:43], v[164:167], v[192:199], v240\n\t" \
  "ds_read_b64 v[212:213], v241 offset:800\n\t" \
  "v_smfmac_f32_16x16x64_bf16 v[64:67], v[160:163], v[192:199], v240\n\t" \
  "ds_read_b64 v[214:215], v241 offset:1200\n\t" \
  "v_smfmac_f32_16x16x64_bf16 v[20:23], v[184:187], v[200:207], v240\n\t" \
  "ds_read_b64 v[216:217], v241 offset:8\n\t" \
  "ds_read_b64 v[218:219], v241 offset:408\n\t" \
  "v_smfmac_f32_16x16x64_bf16 v[44:47], v[180:183], v[200:207], v240\n\t" \
  "ds_read_b64 v[220:221], v241 offset:808\n\t" \
  "v_smfmac_f32_16x16x64_bf16 v[68:71], v[176:179], v[200:207], v240\n\t" \
  "ds_read_b64 v[222:223], v241 offset:1208\n\t" \
  "s_waitcnt lgkmcnt(4)\n\t" \
  "v_smfmac_f32_16x16x64_bf16 v[0:3], v[108:111], v[208:215], v240\n\t" \
  "ds_read_b64 v[192:193], v241 offset:16\n\t" \
  "v_smfmac_f32_16x16x64_bf16 v[24:27], v[104:107], v[208:215], v240\n\t" \
  "ds_read_b64 v[194:195], v241 offset:416\n\t" \
  "v_smfmac_f32_16x16x64_bf16 v[48:51], v[100:103], v[208:215], v240\n\t" \
  "ds_read_b64 v[196:197], v241 offset:816\n\t" \
  "v_smfmac_f32_16x16x64_bf16 v[72:75], v[96:99], v[208:215], v240\n\t" \
  "ds_read_b64 v[198:199], v241 offset:1216\n\t" \
  "s_waitcnt lgkmcnt(4)\n\t" \
  "v_smfmac_f32_16x16x64_bf16 v[4:7], v[124:127], v[216:223], v240\n\t" \
  "ds_read_b64 v[200:201], v241 offset:24\n\t" \
  "v_mul_f32 v237, v0, v0\n\t" \
  "v_add_f32 v236, v0, v1\n\t" \
  "v_smfmac_f32_16x16x64_bf16 v[28:31], v[120:123], v[216:223], v240\n\t" \
  "ds_read_b64 v[202:203], v241 offset:424\n\t" \
  "v_fmac_f32 v237, v1, v1\n\t" \
  "v_add_f32 v236, v236, v2\n\t" \
  "v_smfmac_f32_16x16x64_bf16 v[52:55], v[116:119], v[216:223], v240\n\t" \
  "ds_read_b64 v[204:205], v241 offset:824\n\t" \
  "v_fmac_f32 v237, v2, v2\n\t" \
  "v_add_f32 v236, v236, v3\n\t" \
  "v_smfmac_f32_16x16x64_bf16 v[76:79], v[112:115], v[216:223], v240\n\t" \
  "ds_read_b64 v[206:207], v241 offset:1224\n\t" \
  "v_fmac_f32 v237, v3, v3\n\t" \
  "s_waitcnt lgkmcnt(4)\n\t" \
  "v_smfmac_f32_16x16x64_bf16 v[8:11], v[140:143], v[192:199], v240\n\t" \
  "ds_read_b64 v[208:209], v241 offset:32\n\t" \
  "v_add_f32 v236, v236, v4\n\t" \
  "v_fmac_f32 v237, v4, v4\n\t" \
  "v_smfmac_f32_16x16x64_bf16 v[32:35], v[136:139], v[192:199], v240\n\t" \
  "ds_read_b64 v[210:211], v241 offset:432\n\t" \
  "v_add_f32 v236, v236, v5\n\t" \
  "v_fmac_f32 v237, v5, v5\n\t" \
  "v_smfmac_f32_16x16x64_bf16 v[56:59], v[132:135], v[192:199], v240\n\t" \
  "ds_read_b64 v[212:213], v241 offset:832\n\t" \
  "v_add_f32 v236, v236, v6\n\t" \
  "v_fmac_f32 v237, v6, v6\n\t" \
  "v_smfmac_f32_16x16x64_bf16 v[80:83], v[128:131], v[192:199], v240\n\t" \
  "ds_read_b64 v[214:215], v241 offset:1232\n\t" \
  "v_add_f32 v236, v236, v7\n\t" \
  "v_fmac_f32 v237, v7, v7\n\t" \
  "s_waitcnt lgkmcnt(4)\n\t" \
  "v_smfmac_f32_16x16x64_bf16 v[12:15], v[156:159], v[200:207], v240\n\t" \
  "ds_read_b64 v[216:217], v241 offset:40\n\t" \
  "ds_read_b64 v[218:219], v241 offset:440\n\t" \
  "v_add_f32 v236, v236, v8\n\t" \
  "v_fmac_f32 v237, v8, v8\n\t" \
  "v_smfmac_f32_16x16x64_bf16 v[36:39], v[152:155], v[200:207], v240\n\t" \
  "ds_read_b64 v[220:221], v241 offset:840\n\t" \
  "v_add_f32 v236, v236, v9\n\t" \
  "v_fmac_f32 v237, v9, v9\n\t" \
  "v_smfmac_f32_16x16x64_bf16 v[60:63], v[148:151], v[200:207], v240\n\t" \
  "ds_read_b64 v[222:223], v241 offset:1240\n\t" \
  "v_add_f32 v236, v236, v10\n\t" \
  "v_fmac_f32 v237, v10, v10\n\t" \
  "v_smfmac_f32_16x16x64_bf16 v[84:87], v[144:147], v[200:207], v240\n\t" \
  "s_add_u32 %[cs], %[cs], 1\n\t" \
  "s_cmp_eq_u32 %[cs], 6\n\t" \
  "s_cselect_b32 %[cs], 0, %[cs]\n\t" \
  "s_cselect_b32 %[t0], -75920, %[ps]\n\t" \
  "v_add_u32 v241, %[t0], v241\n\t" \
  "v_add_f32 v236, v236, v11\n\t" \
  "v_fmac_f32 v237, v11, v11\n\t" \
  "s_waitcnt lgkmcnt(4)\n\t" \
  "v_smfmac_f32_16x16x64_bf16 v[16:19], v[172:175], v[208:215], v240\n\t" \
  "v_add_f32 v236, v236, v12\n\t" \
  "v_fmac_f32 v237, v12, v12\n\t" \
  "v_smfmac_f32_16x16x64_bf16 v[40:43], v[168:171], v[208:215], v240\n\t" \
  "v_add_f32 v236, v236, v13\n\t" \
  "v_fmac_f32 v237, v13, v13\n\t" \
  "v_smfmac_f32_16x16x64_bf16 v[64:67], v[164:167], v[208:215], v240\n\t" \
  "v_add_f32 v236, v236, v14\n\t" \
  "v_fmac_f32 v237, v14, v14\n\t" \
  "v_smfmac_f32_16x16x64_bf16 v[88:91], v[160:163], v[208:215], v240\n\t" \
  "v_add_f32 v236, v236, v15\n\t" \
  "v_fmac_f32 v237, v15, v15\n\t" \
  "s_waitcnt lgkmcnt(0)\n\t" \
  "v_smfmac_f32_16x16x64_bf16 v[20:23], v[188:191], v[216:223], v240\n\t" \
  "v_add_f32 v236, v236, v16\n\t" \
  "v_fmac_f32 v237, v16, v16\n\t" \
  "v_smfmac_f32_16x16x64_bf16 v[44:47], v[184:187], v[216:223], v240\n\t" \
  "v_add_f32 v236, v236, v17\n\t" \
  "v_fmac_f32 v237, v17, v17\n\t" \
  "v_smfmac_f32_16x16x64_bf16 v[68:71], v[180:183], v[216:223], v240\n\t" \
  "v_add_f32 v236, v236, v18\n\t" \
  "v_fmac_f32 v237, v18, v18\n\t" \
  "v_smfmac_f32_16x16x64_bf16 v[92:95], v[176:179], v[216:223], v240\n\t" \
  "v_add_f32 v236, v236, v19\n\t" \
  "v_fmac_f32 v237, v19, v19\n\t" \
  "s_nop 3\n\t" \
  "v_add_f32 v236, v236, v20\n\t" \
  "v_fmac_f32 v237, v20, v20\n\t" \
  "v_add_f32 v236, v236, v21\n\t" \
  "v_fmac_f32 v237, v21, v21\n\t" \
  "v_add_f32 v236, v236, v22\n\t" \
  "v_fmac_f32 v237, v22, v22\n\t" \
  "v_add_f32 v236, v236, v23\n\t" \
  "v_fmac_f32 v237, v23, v23\n\t" \
  "ds_write_b64 v243, v[236:237]\n\t" \
  "s_waitcnt lgkmcnt(0)\n\t" \
  "s_barrier\n\t" \
  "ds_read_b128 v[224:227], v244\n\t" \
  "ds_read_b128 v[228:231], v244 offset:1024\n\t" \
  "ds_read_b64 v[192:193], v241 offset:0\n\t" \
  "ds_read_b64 v[194:195], v241 offset:400\n\t" \
  "ds_read_b64 v[196:197], v241 offset:800\n\t" \
  "ds_read_b64 v[198:199], v241 offset:1200\n\t" \
  "ds_read_b64 v[200:201], v241 offset:8\n\t" \
  "ds_read_b64 v[202:203], v241 offset:408\n\t" \
  "ds_read_b64 v[204:205], v241 offset:808\n\t" \
  "ds_read_b64 v[206:207], v241 offset:1208\n\t" \
  "s_waitcnt lgkmcnt(8)\n\t" \
  "v_add_f32 v224, v224, v226\n\t" \
  "v_add_f32 v225, v225, v227\n\t" \
  "v_add_f32 v228, v228, v230\n\t" \
  "v_add_f32 v229, v229, v231\n\t" \
  "v_add_f32 v224, v224, v228\n\t" \
  "v_add_f32 v225, v225, v229\n\t" \
  "v_mul_f32 v224, 0x3c2aaaab, v224\n\t" \
  "v_mul_f32 v225, 0x3c2aaaab, v225\n\t" \
  "v_fma_f32 v225, -v224, v224, v225\n\t" \
  "v_max_f32 v225, 0, v225\n\t" \
  "v_add_f32 v225, 0x3727c5ac, v225\n\t" \
  "v_rsq_f32 v225, v225\n\t" \
  "s_nop 0\n\t" \
  "v_mul_f32 v238, %[gl], v225\n\t" \
  "v_mul_f32 v239, -v224, v238\n\t" \
  "v_fma_f32 v0, v0, v238, v239\n\t" \
  "v_fma_f32 v1, v1, v238, v239\n\t" \
  "v_fma_f32 v2, v2, v238, v239\n\t" \
  "v_fma_f32 v3, v3, v238, v239\n\t" \
  "v_cvt_pk_bf16_f32 v224, v0, v1\n\t" \
  "v_cvt_pk_bf16_f32 v225, v2, v3\n\t" \
  "v_fma_f32 v4, v4, v238, v239\n\t" \
  "v_fma_f32 v5, v5, v238, v239\n\t" \
  "v_fma_f32 v6, v6, v238, v239\n\t" \
  "v_fma_f32 v7, v7, v238, v239\n\t" \
  "v_cvt_pk_bf16_f32 v226, v4, v5\n\t" \
  "v_cvt_pk_bf16_f32 v227, v6, v7\n\t" \
  "ds_write_b128 v242, v[224:227] offset:0\n\t" \
  "v_fma_f32 v8, v8, v238, v239\n\t" \
  "v_fma_f32 v9, v9, v238, v239\n\t" \
  "v_fma_f32 v10, v10, v238, v239\n\t" \
  "v_fma_f32 v11, v11, v238, v239\n\t" \
  "v_cvt_pk_bf16_f32 v228, v8, v9\n\t" \
  "v_cvt_pk_bf16_f32 v229, v10, v11\n\t" \
  "v_fma_f32 v12, v12, v238, v239\n\t" \
  "v_fma_f32 v13, v13, v238, v239\n\t" \
  "v_fma_f32 v14, v14, v238, v239\n\t" \
  "v_fma_f32 v15, v15, v238, v239\n\t" \
  "v_cvt_pk_bf16_f32 v230, v12, v13\n\t" \
  "v_cvt_pk_bf16_f32 v231, v14, v15\n\t" \
  "ds_write_b128 v242, v[228:231] offset:1024\n\t" \
  "v_fma_f32 v16, v16, v238, v239\n\t" \
  "v_fma_f32 v17, v17, v238, v239\n\t" \
  "v_fma_f32 v18, v18, v238, v239\n\t" \
  "v_fma_f32 v19, v19, v238, v239\n\t" \
  "v_cvt_pk_bf16_f32 v232, v16, v17\n\t" \
  "v_cvt_pk_bf16_f32 v233, v18, v19\n\t" \
  "v_fma_f32 v20, v20, v238, v239\n\t" \
  "v_fma_f32 v21, v21, v238, v239\n\t" \
  "v_fma_f32 v22, v22, v238, v239\n\t" \
  "v_fma_f32 v23, v23, v238, v239\n\t" \
  "v_cvt_pk_bf16_f32 v234, v20, v21\n\t" \
  "v_cvt_pk_bf16_f32 v235, v22, v23\n\t" \
  "ds_write_b128 v242, v[232:235] offset:2048\n\t" \
  "v_mfma_f32_16x16x16_bf16 v[0:3], v[252:253], v[252:253], 0\n\t" \
  "v_mfma_f32_16x16x16_bf16 v[4:7], v[252:253], v[252:253], 0\n\t" \
  "v_mfma_f32_32x32x8_bf16 v[8:23], v[252:253], v[252:253], 0\n\t" \
  "s_waitcnt lgkmcnt(0)\n\t" \
  "s_barrier\n\t" \
  "v_smfmac_f32_16x16x64_bf16 v[24:27], v[108:111], v[192:199], v240\n\t" \
  "ds_read_b64 v[208:209], v241 offset:16\n\t" \
  "v_smfmac_f32_16x16x64_bf16 v[48:51], v[104:107], v[192:199], v240\n\t" \
  "ds_read_b64 v[210:211], v241 offset:416\n\t" \
  "v_smfmac_f32_16x16x64_bf16 v[72:75], v[100:103], v[192:199], v240\n\t" \
  "ds_read_b64 v[212:213], v241 offset:816\n\t" \
  "v_smfmac_f32_16x16x64_bf16 v[0:3], v[96:99], v[192:199], v240\n\t" \
  "ds_read_b64 v[214:215], v241 offset:1216\n\t" \
  "v_smfmac_f32_16x16x64_bf16 v[28:31], v[124:127], v[200:207], v240\n\t" \
  "ds_read_b64 v[216:217], v241 offset:24\n\t" \
  "v_mul_f32 v237, v24, v24\n\t" \
  "v_add_f32 v236, v24, v25\n\t" \
  "v_smfmac_f32_16x16x64_bf16 v[52:55], v[120:123], v[200:207], v240\n\t" \
  "ds_read_b64 v[218:219], v241 offset:424\n\t" \
  "v_fmac_f32 v237, v25, v25\n\t" \
  "v_add_f32 v236, v236, v26\n\t" \
  "v_smfmac_f32_16x16x64_bf16 v[76:79], v[116:119], v[200:207], v240\n\t" \
  "ds_read_b64 v[220:221], v241 offset:824\n\t" \
  "v_fmac_f32 v237, v26, v26\n\t" \
  "v_add_f32 v236, v236, v27\n\t" \
  "v_smfmac_f32_16x16x64_bf16 v[4:7], v[112:115], v[200:207], v240\n\t" \
  "ds_read_b64 v[222:223], v241 offset:1224\n\t" \
  "v_fmac_f32 v237, v27, v27\n\t" \
  "s_waitcnt lgkmcnt(4)\n\t" \
  "v_smfmac_f32_16x16x64_bf16 v[32:35], v[140:143], v[208:215], v240\n\t" \
  "ds_read_b64 v[192:193], v241 offset:32\n\t" \
  "v_add_f32 v236, v236, v28\n\t" \
  "v_fmac_f32 v237, v28, v28\n\t" \
  "v_smfmac_f32_16x16x64_bf16 v[56:59], v[136:139], v[208:215], v240\n\t" \
  "ds_read_b64 v[194:195], v241 offset:432\n\t" \
  "v_add_f32 v236, v236, v29\n\t" \
  "v_fmac_f32 v237, v29, v29\n\t" \
  "v_smfmac_f32_16x16x64_bf16 v[80:83], v[132:135], v[208:215], v240\n\t" \
  "ds_read_b64 v[196:197], v241 offset:832\n\t" \
  "v_add_f32 v236, v236, v30\n\t" \
  "v_fmac_f32 v237, v30, v30\n\t" \
  "v_smfmac_f32_16x16x64_bf16 v[8:11], v[128:131], v[208:215], v240\n\t" \
  "ds_read_b64 v[198:199], v241 offset:1232\n\t" \
  "v_add_f32 v236, v236, v31\n\t" \
  "v_fmac_f32 v237, v31, v31\n\t" \
  "s_waitcnt lgkmcnt(4)\n\t" \
  "v_smfmac_f32_16x16x64_bf16 v[36:39], v[156:159], v[216:223], v240\n\t" \
  "ds_read_b64 v[200:201], v241 offset:40\n\t" \
  "ds_read_b64 v[202:203], v241 offset:440\n\t" \
  "v_add_f32 v236, v236, v32\n\t" \
  "v_fmac_f32 v237, v32, v32\n\t" \
  "v_smfmac_f32_16x16x64_bf16 v[60:63], v[152:155], v[216:223], v240\n\t" \
  "ds_read_b64 v[204:205], v241 offset:840\n\t" \
  "v_add_f32 v236, v236, v33\n\t" \
  "v_fmac_f32 v237, v33, v33\n\t" \
  "v_smfmac_f32_16x16x64_bf16 v[84:87], v[148:151], v[216:223], v240\n\t" \
  "ds_read_b64 v[206:207], v241 offset:1240\n\t" \
  "v_add_f32 v236, v236, v34\n\t" \
  "v_fmac_f32 v237, v34, v34\n\t" \
  "v_smfmac_f32_16x16x64_bf16 v[12:15], v[144:147], v[216:223], v240\n\t" \
  "s_add_u32 %[cs], %[cs], 1\n\t" \
  "s_cmp_eq_u32 %[cs], 6\n\t" \
  "s_cselect_b32 %[cs], 0, %[cs]\n\t" \
  "s_cselect_b32 %[t0], -75920, %[ps]\n\t" \
  "v_add_u32 v241, %[t0], v241\n\t" \
  "v_add_f32 v236, v236, v35\n\t" \
  "v_fmac_f32 v237, v35, v35\n\t" \
  "s_waitcnt lgkmcnt(4)\n\t" \
  "v_smfmac_f32_16x16x64_bf16 v[40:43], v[172:175], v[192:199], v240\n\t" \
  "ds_read_b64 v[208:209], v241 offset:0\n\t" \
  "v_add_f32 v236, v236, v36\n\t" \
  "v_fmac_f32 v237, v36, v36\n\t" \
  "v_smfmac_f32_16x16x64_bf16 v[64:67], v[168:171], v[192:199], v240\n\t" \
  "ds_read_b64 v[210:211], v241 offset:400\n\t" \
  "v_add_f32 v236, v236, v37\n\t" \
  "v_fmac_f32 v237, v37, v37\n\t" \
  "v_smfmac_f32_16x16x64_bf16 v[88:91], v[164:167], v[192:199], v240\n\t" \
  "ds_read_b64 v[212:213], v241 offset:800\n\t" \
  "v_add_f32 v236, v236, v38\n\t" \
  "v_fmac_f32 v237, v38, v38\n\t" \
  "v_smfmac_f32_16x16x64_bf16 v[16:19], v[160:163], v[192:199], v240\n\t" \
  "ds_read_b64 v[214:215], v241 offset:1200\n\t" \
  "v_add_f32 v236, v236, v39\n\t" \
  "v_fmac_f32 v237, v39, v39\n\t" \
  "s_waitcnt lgkmcnt(4)\n\t" \
  "v_smfmac_f32_16x16x64_bf16 v[44:47], v[188:191], v[200:207], v240\n\t" \
  "ds_read_b64 v[216:217], v241 offset:8\n\t" \
  "v_add_f32 v236, v236, v40\n\t" \
  "v_fmac_f32 v237, v40, v40\n\t" \
  "v_smfmac_f32_16x16x64_bf16 v[68:71], v[184:187], v[200:207], v240\n\t" \
  "ds_read_b64 v[218:219], v241 offset:408\n\t" \
  "v_add_f32 v236, v236, v41\n\t" \
  "v_fmac_f32 v237, v41, v41\n\t" \
  "v_smfmac_f32_16x16x64_bf16 v[92:95], v[180:183], v[200:207], v240\n\t" \
  "ds_read_b64 v[220:221], v241 offset:808\n\t" \
  "v_add_f32 v236, v236, v42\n\t" \
  "v_fmac_f32 v237, v42, v42\n\t" \
  "v_smfmac_f32_16x16x64_bf16 v[20:23], v[176:179], v[200:207], v240\n\t" \
  "ds_read_b64 v[222:223], v241 offset:1208\n\t" \
  "v_add_f32 v236, v236, v43\n\t" \
  "v_fmac_f32 v237, v43, v43\n\t" \
  "s_nop 3\n\t" \
  "v_add_f32 v236, v236, v44\n\t" \
  "v_fmac_f32 v237, v44, v44\n\t" \
  "v_add_f32 v236, v236, v45\n\t" \
  "v_fmac_f32 v237, v45, v45\n\t" \
  "v_add_f32 v236, v236, v46\n\t" \
  "v_fmac_f32 v237, v46, v46\n\t" \
  "v_add_f32 v236, v236, v47\n\t" \
  "v_fmac_f32 v237, v47, v47\n\t" \
  "ds_write_b64 v243, v[236:237]\n\t" \
  "s_waitcnt lgkmcnt(0)\n\t" \
  "s_barrier\n\t" \
  ".Lcnx_f:\n\t" \
  "ds_read_b128 v[224:227], v244\n\t" \
  "ds_read_b128 v[228:231], v244 offset:1024\n\t" \
  "s_waitcnt lgkmcnt(0)\n\t" \
  "s_setprio 1\n\t" \
  "v_add_f32 v224, v224, v226\n\t" \
  "v_add_f32 v225, v225, v227\n\t" \
  "v_add_f32 v228, v228, v230\n\t" \
  "v_add_f32 v229, v229, v231\n\t" \
  "v_add_f32 v224, v224, v228\n\t" \
  "v_add_f32 v225, v225, v229\n\t" \
  "v_mul_f32 v224, 0x3c2aaaab, v224\n\t" \
  "v_mul_f32 v225, 0x3c2aaaab, v225\n\t" \
  "v_fma_f32 v225, -v224, v224, v225\n\t" \
  "v_max_f32 v225, 0, v225\n\t" \
  "v_add_f32 v225, 0x3727c5ac, v225\n\t" \
  "v_rsq_f32 v225, v225\n\t" \
  "s_nop 0\n\t" \
  "v_mul_f32 v238, %[gl], v225\n\t" \
  "v_mul_f32 v239, -v224, v238\n\t" \
  "v_fma_f32 v24, v24, v238, v239\n\t" \
  "v_fma_f32 v25, v25, v238, v239\n\t" \
  "v_fma_f32 v26, v26, v238, v239\n\t" \
  "v_fma_f32 v27, v27, v238, v239\n\t" \
  "v_cvt_pk_bf16_f32 v224, v24, v25\n\t" \
  "v_cvt_pk_bf16_f32 v225, v26, v27\n\t" \
  "v_fma_f32 v28, v28, v238, v239\n\t" \
  "v_fma_f32 v29, v29, v238, v239\n\t" \
  "v_fma_f32 v30, v30, v238, v239\n\t" \
  "v_fma_f32 v31, v31, v238, v239\n\t" \
  "v_cvt_pk_bf16_f32 v226, v28, v29\n\t" \
  "v_cvt_pk_bf16_f32 v227, v30, v31\n\t" \
  "s_setprio 3\n\t" \
  "v_mfma_f32_16x16x16_bf16 v[24:27], v[252:253], v[252:253], 0\n\t" \
  "ds_write_b128 v242, v[224:227] offset:0\n\t" \
  "v_fma_f32 v32, v32, v238, v239\n\t" \
  "v_mfma_f32_16x16x16_bf16 v[28:31], v[252:253], v[252:253], 0\n\t" \
  "ds_read_b64 v[192:193], v241 offset:16\n\t" \
  "v_fma_f32 v33, v33, v238, v239\n\t" \
  "v_smfmac_f32_16x16x64_bf16 v[48:51], v[108:111], v[208:215], v240\n\t" \
  "ds_read_b64 v[194:195], v241 offset:416\n\t" \
  "v_fma_f32 v34, v34, v238, v239\n\t" \
  "v_smfmac_f32_16x16x64_bf16 v[72:75], v[104:107], v[208:215], v240\n\t" \
  "ds_read_b64 v[196:197], v241 offset:816\n\t" \
  "v_fma_f32 v35, v35, v238, v239\n\t" \
  "v_smfmac_f32_16x16x64_bf16 v[0:3], v[100:103], v[208:215], v240\n\t" \
  "ds_read_b64 v[198:199], v241 offset:1216\n\t" \
  "v_cvt_pk_bf16_f32 v228, v32, v33\n\t" \
  "v_smfmac_f32_16x16x64_bf16 v[24:27], v[96:99], v[208:215], v240\n\t" \
  "ds_read_b64 v[200:201], v241 offset:24\n\t" \
  "v_cvt_pk_bf16_f32 v229, v34, v35\n\t" \
  "v_smfmac_f32_16x16x64_bf16 v[52:55], v[124:127], v[216:223], v240\n\t" \
  "ds_read_b64 v[202:203], v241 offset:424\n\t" \
  "v_fma_f32 v36, v36, v238, v239\n\t" \
  "v_smfmac_f32_16x16x64_bf16 v[76:79], v[120:123], v[216:223], v240\n\t" \
  "ds_read_b64 v[204:205], v241 offset:824\n\t" \
  "v_fma_f32 v37, v37, v238, v239\n\t" \
  "v_smfmac_f32_16x16x64_bf16 v[4:7], v[116:119], v[216:223], v240\n\t" \
  "ds_read_b64 v[206:207], v241 offset:1224\n\t" \
  "v_fma_f32 v38, v38, v238, v239\n\t" \
  "v_smfmac_f32_16x16x64_bf16 v[28:31], v[112:115], v[216:223], v240\n\t" \
  "v_fma_f32 v39, v39, v238, v239\n\t" \
  "s_setprio 1\n\t" \
  "s_setprio 3\n\t" \
  "s_mov_b32 m0, %[wld]\n\t" \
  "s_add_u32 %[t1], %[wso], %[wpd]\n\t" \
  "buffer_load_dwordx4 v245, %[wrs], %[wso] offen lds\n\t" \
  "buffer_load_dwordx4 v245, %[wrs], %[t1] offen offset:1024 lds\n\t" \
  "s_add_u32 %[t1], %[t1], %[wpd]\n\t" \
  "s_add_u32 %[wso], %[wso], %[wcs]\n\t" \
  "buffer_load_dwordx4 v245, %[wrs], %[t1] offen offset:2048 lds\n\t" \
  "s_add_u32 %[wld], %[wld], %[wst]\n\t" \
  "s_setprio 1\n\t" \
  "s_setprio 3\n\t" \
  "s_waitcnt vmcnt(3)\n\t" \
  "s_barrier\n\t" \
  "s_mov_b32 m0, %[wld]\n\t" \
  "s_add_u32 %[t1], %[wso], %[wpd]\n\t" \
  "buffer_load_dwordx4 v245, %[wrs], %[wso] offen lds\n\t" \
  "buffer_load_dwordx4 v245, %[wrs], %[t1] offen offset:1024 lds\n\t" \
  "s_add_u32 %[t1], %[t1], %[wpd]\n\t" \
  "s_add_u32 %[wso], %[wso], %[wcs]\n\t" \
  "buffer_load_dwordx4 v245, %[wrs], %[t1] offen offset:2048 lds\n\t" \
  "s_add_u32 %[wld], %[wld], %[wlb]\n\t" \
  "s_mov_b32 m0, %[xld]\n\t" \
  "s_cmp_lt_u32 %[gy], 0x3000000\n\t" \
  "s_cselect_b32 %[xso], %[gy], 0x40000000\n\t" \
  "buffer_load_dwordx4 v246, %[xrs], %[xso] offen offset:0 nt lds\n\t" \
  "s_add_u32 %[gy], %[gy], %[dgy]\n\t" \
  "buffer_load_dwordx4 v247, %[xrs], %[xso] offen offset:1024 nt lds\n\t" \
  "s_add_u32 %[xld], %[xld], 15184\n\t" \
  "s_cmp_eq_u32 %[xld], %[xle]\n\t" \
  "s_cselect_b32 %[xld], %[xlb], %[xld]\n\t" \
  "buffer_load_dwordx4 v248, %[xrs], %[xso] offen offset:2048 nt lds\n\t" \
  "s_mov_b64 exec, %[xm]\n\t" \
  "buffer_load_dwordx4 v249, %[xrs], %[xso] offen offset:3072 nt lds\n\t" \
  "s_mov_b64 exec, -1\n\t" \
  "s_setprio 1\n\t" \
  "v_cvt_pk_bf16_f32 v230, v36, v37\n\t" \
  "v_cvt_pk_bf16_f32 v231, v38, v39\n\t" \
  "v_fma_f32 v40, v40, v238, v239\n\t" \
  "v_fma_f32 v41, v41, v238, v239\n\t" \
  "v_fma_f32 v42, v42, v238, v239\n\t" \
  "v_fma_f32 v43, v43, v238, v239\n\t" \
  "v_cvt_pk_bf16_f32 v232, v40, v41\n\t" \
  "v_cvt_pk_bf16_f32 v233, v42, v43\n\t" \
  "v_fma_f32 v44, v44, v238, v239\n\t" \
  "v_fma_f32 v45, v45, v238, v239\n\t" \
  "v_fma_f32 v46, v46, v238, v239\n\t" \
  "v_fma_f32 v47, v47, v238, v239\n\t" \
  "v_cvt_pk_bf16_f32 v234, v44, v45\n\t" \
  "v_cvt_pk_bf16_f32 v235, v46, v47\n\t" \
  "v_mul_f32 v237, v48, v48\n\t" \
  "v_add_f32 v236, v48, v49\n\t" \
  "v_fmac_f32 v237, v49, v49\n\t" \
  "v_add_f32 v236, v236, v50\n\t" \
  "s_setprio 3\n\t" \
  "v_mfma_f32_32x32x8_bf16 v[32:47], v[252:253], v[252:253], 0\n\t" \
  "s_waitcnt lgkmcnt(4)\n\t" \
  "ds_write_b128 v242, v[228:231] offset:1024\n\t" \
  "ds_read_b64 v[208:209], v241 offset:32\n\t" \
  "v_fmac_f32 v237, v50, v50\n\t" \
  "v_smfmac_f32_16x16x64_bf16 v[56:59], v[140:143], v[192:199], v240\n\t" \
  "ds_read_b64 v[210:211], v241 offset:432\n\t" \
  "v_add_f32 v236, v236, v51\n\t" \
  "v_smfmac_f32_16x16x64_bf16 v[80:83], v[136:139], v[192:199], v240\n\t" \
  "ds_read_b64 v[212:213], v241 offset:832\n\t" \
  "v_fmac_f32 v237, v51, v51\n\t" \
  "v_smfmac_f32_16x16x64_bf16 v[8:11], v[132:135], v[192:199], v240\n\t" \
  "ds_read_b64 v[214:215], v241 offset:1232\n\t" \
  "v_add_f32 v236, v236, v52\n\t" \
  "v_smfmac_f32_16x16x64_bf16 v[32:35], v[128:131], v[192:199], v240\n\t" \
  "s_waitcnt lgkmcnt(5)\n\t" \
  "ds_write_b128 v242, v[232:235] offset:2048\n\t" \
  "v_fmac_f32 v237, v52, v52\n\t" \
  "v_smfmac_f32_16x16x64_bf16 v[60:63], v[156:159], v[200:207], v240\n\t" \
  "ds_read_b64 v[216:217], v241 offset:40\n\t" \
  "v_add_f32 v236, v236, v53\n\t" \
  "v_smfmac_f32_16x16x64_bf16 v[84:87], v[152:155], v[200:207], v240\n\t" \
  "ds_read_b64 v[218:219], v241 offset:440\n\t" \
  "v_fmac_f32 v237, v53, v53\n\t" \
  "v_smfmac_f32_16x16x64_bf16 v[12:15], v[148:151], v[200:207], v240\n\t" \
  "ds_read_b64 v[220:221], v241 offset:840\n\t" \
  "v_add_f32 v236, v236, v54\n\t" \
  "v_smfmac_f32_16x16x64_bf16 v[36:39], v[144:147], v[200:207], v240\n\t" \
  "ds_read_b64 v[222:223], v241 offset:1240\n\t" \
  "v_fmac_f32 v237, v54, v54\n\t" \
  "s_setprio 1\n\t" \
  "s_setprio 3\n\t" \
  "s_add_u32 %[cs], %[cs], 1\n\t" \
  "s_cmp_eq_u32 %[cs], 6\n\t" \
  "s_cselect_b32 %[cs], 0, %[cs]\n\t" \
  "s_cselect_b32 %[t0], -75920, %[ps]\n\t" \
  "v_add_u32 v241, %[t0], v241\n\t" \
  "s_waitcnt vmcnt(7)\n\t" \
  "s_waitcnt lgkmcnt(4)\n\t" \
  "s_barrier\n\t" \
  "v_smfmac_f32_16x16x64_bf16 v[64:67], v[172:175], v[208:215], v240\n\t" \
  "ds_read_b64 v[192:193], v241 offset:0\n\t" \
  "v_add_f32 v236, v236, v55\n\t" \
  "v_smfmac_f32_16x16x64_bf16 v[88:91], v[168:171], v[208:215], v240\n\t" \
  "ds_read_b64 v[194:195], v241 offset:400\n\t" \
  "v_fmac_f32 v237, v55, v55\n\t" \
  "v_smfmac_f32_16x16x64_bf16 v[16:19], v[164:167], v[208:215], v240\n\t" \
  "ds_read_b64 v[196:197], v241 offset:800\n\t" \
  "v_add_f32 v236, v236, v56\n\t" \
  "v_smfmac_f32_16x16x64_bf16 v[40:43], v[160:163], v[208:215], v240\n\t" \
  "s_waitcnt lgkmcnt(3)\n\t" \
  "ds_read_b64 v[198:199], v241 offset:1200\n\t" \
  "v_fmac_f32 v237, v56, v56\n\t" \
  "v_smfmac_f32_16x16x64_bf16 v[68:71], v[188:191], v[216:223], v240\n\t" \
  "ds_read_b64 v[200:201], v241 offset:8\n\t" \
  "v_add_f32 v236, v236, v57\n\t" \
  "v_smfmac_f32_16x16x64_bf16 v[92:95], v[184:187], v[216:223], v240\n\t" \
  "ds_read_b64 v[202:203], v241 offset:408\n\t" \
  "v_fmac_f32 v237, v57, v57\n\t" \
  "v_smfmac_f32_16x16x64_bf16 v[20:23], v[180:183], v[216:223], v240\n\t" \
  "ds_read_b64 v[204:205], v241 offset:808\n\t" \
  "v_add_f32 v236, v236, v58\n\t" \
  "v_smfmac_f32_16x16x64_bf16 v[44:47], v[176:179], v[216:223], v240\n\t" \
  "ds_read_b64 v[206:207], v241 offset:1208\n\t" \
  "v_fmac_f32 v237, v58, v58\n\t" \
  "s_setprio 1\n\t" \
  "s_setprio 3\n\t" \
  "s_mov_b32 m0, %[wld]\n\t" \
  "s_add_u32 %[t1], %[wso], %[wpd]\n\t" \
  "buffer_load_dwordx4 v245, %[wrs], %[wso] offen lds\n\t" \
  "buffer_load_dwordx4 v245, %[wrs], %[t1] offen offset:1024 lds\n\t" \
  "s_add_u32 %[t1], %[t1], %[wpd]\n\t" \
  "s_add_u32 %[wso], %[wso], %[wcs]\n\t" \
  "buffer_load_dwordx4 v245, %[wrs], %[t1] offen offset:2048 lds\n\t" \
  "s_add_u32 %[wld], %[wld], %[wla]\n\t" \
  "s_setprio 1\n\t" \
  "v_add_f32 v236, v236, v59\n\t" \
  "v_fmac_f32 v237, v59, v59\n\t" \
  "v_add_f32 v236, v236, v60\n\t" \
  "v_fmac_f32 v237, v60, v60\n\t" \
  "v_add_f32 v236, v236, v61\n\t" \
  "v_fmac_f32 v237, v61, v61\n\t" \
  "v_add_f32 v236, v236, v62\n\t" \
  "v_fmac_f32 v237, v62, v62\n\t" \
  "v_add_f32 v236, v236, v63\n\t" \
  "v_fmac_f32 v237, v63, v63\n\t" \
  "v_add_f32 v236, v236, v64\n\t" \
  "v_fmac_f32 v237, v64, v64\n\t" \
  "v_add_f32 v236, v236, v65\n\t" \
  "v_fmac_f32 v237, v65, v65\n\t" \
  "v_add_f32 v236, v236, v66\n\t" \
  "v_fmac_f32 v237, v66, v66\n\t" \
  "v_add_f32 v236, v236, v67\n\t" \
  "v_fmac_f32 v237, v67, v67\n\t" \
  "v_add_f32 v236, v236, v68\n\t" \
  "v_fmac_f32 v237, v68, v68\n\t" \
  "v_add_f32 v236, v236, v69\n\t" \
  "v_fmac_f32 v237, v69, v69\n\t" \
  "v_add_f32 v236, v236, v70\n\t" \
  "v_fmac_f32 v237, v70, v70\n\t" \
  "v_add_f32 v236, v236, v71\n\t" \
  "v_fmac_f32 v237, v71, v71\n\t" \
  "s_setprio 3\n\t" \
  "ds_write_b64 v243, v[236:237]\n\t" \
  "s_waitcnt vmcnt(7)\n\t" \
  "s_waitcnt lgkmcnt(0)\n\t" \
  "s_barrier\n\t" \
  "ds_read_b128 v[224:227], v244\n\t" \
  "ds_read_b128 v[228:231], v244 offset:1024\n\t" \
  "s_waitcnt lgkmcnt(0)\n\t" \
  "s_setprio 1\n\t" \
  "v_add_f32 v224, v224, v226\n\t" \
  "v_add_f32 v225, v225, v227\n\t" \
  "v_add_f32 v228, v228, v230\n\t" \
  "v_add_f32 v229, v229, v231\n\t" \
  "v_add_f32 v224, v224, v228\n\t" \
  "v_add_f32 v225, v225, v229\n\t" \
  "v_mul_f32 v224, 0x3c2aaaab, v224\n\t" \
  "v_mul_f32 v225, 0x3c2aaaab, v225\n\t" \
  "v_fma_f32 v225, -v224, v224, v225\n\t" \
  "v_max_f32 v225, 0, v225\n\t" \
  "v_add_f32 v225, 0x3727c5ac, v225\n\t" \
  "v_rsq_f32 v225, v225\n\t" \
  "s_nop 0\n\t" \
  "v_mul_f32 v238, %[gl], v225\n\t" \
  "v_mul_f32 v239, -v224, v238\n\t" \
  "v_fma_f32 v48, v48, v238, v239\n\t" \
  "v_fma_f32 v49, v49, v238, v239\n\t" \
  "v_fma_f32 v50, v50, v238, v239\n\t" \
  "v_fma_f32 v51, v51, v238, v239\n\t" \
  "v_cvt_pk_bf16_f32 v224, v48, v49\n\t" \
  "v_cvt_pk_bf16_f32 v225, v50, v51\n\t" \
  "v_fma_f32 v52, v52, v238, v239\n\t" \
  "v_fma_f32 v53, v53, v238, v239\n\t" \
  "v_fma_f32 v54, v54, v238, v239\n\t" \
  "v_fma_f32 v55, v55, v238, v239\n\t" \
  "v_cvt_pk_bf16_f32 v226, v52, v53\n\t" \
  "v_cvt_pk_bf16_f32 v227, v54, v55\n\t" \
  "s_setprio 3\n\t" \
  "v_mfma_f32_16x16x16_bf16 v[48:51], v[252:253], v[252:253], 0\n\t" \
  "ds_write_b128 v242, v[224:227] offset:0\n\t" \
  "v_fma_f32 v56, v56, v238, v239\n\t" \
  "v_mfma_f32_16x16x16_bf16 v[52:55], v[252:253], v[252:253], 0\n\t" \
  "ds_read_b64 v[208:209], v241 offset:16\n\t" \
  "v_fma_f32 v57, v57, v238, v239\n\t" \
  "v_smfmac_f32_16x16x64_bf16 v[72:75], v[108:111], v[192:199], v240\n\t" \
  "ds_read_b64 v[210:211], v241 offset:416\n\t" \
  "v_fma_f32 v58, v58, v238, v239\n\t" \
  "v_smfmac_f32_16x16x64_bf16 v[0:3], v[104:107], v[192:199], v240\n\t" \
  "ds_read_b64 v[212:213], v241 offset:816\n\t" \
  "v_fma_f32 v59, v59, v238, v239\n\t" \
  "v_smfmac_f32_16x16x64_bf16 v[24:27], v[100:103], v[192:199], v240\n\t" \
  "ds_read_b64 v[214:215], v241 offset:1216\n\t" \
  "v_cvt_pk_bf16_f32 v228, v56, v57\n\t" \
  "v_smfmac_f32_16x16x64_bf16 v[48:51], v[96:99], v[192:199], v240\n\t" \
  "ds_read_b64 v[216:217], v241 offset:24\n\t" \
  "v_cvt_pk_bf16_f32 v229, v58, v59\n\t" \
  "v_smfmac_f32_16x16x64_bf16 v[76:79], v[124:127], v[200:207], v240\n\t" \
  "ds_read_b64 v[218:219], v241 offset:424\n\t" \
  "v_fma_f32 v60, v60, v238, v239\n\t" \
  "v_smfmac_f32_16x16x64_bf16 v[4:7], v[120:123], v[200:207], v240\n\t" \
  "ds_read_b64 v[220:221], v241 offset:824\n\t" \
  "v_fma_f32 v61, v61, v238, v239\n\t" \
  "v_smfmac_f32_16x16x64_bf16 v[28:31], v[116:119], v[200:207], v240\n\t" \
  "ds_read_b64 v[222:223], v241 offset:1224\n\t" \
  "v_fma_f32 v62, v62, v238, v239\n\t" \
  "v_smfmac_f32_16x16x64_bf16 v[52:55], v[112:115], v[200:207], v240\n\t" \
  "v_fma_f32 v63, v63, v238, v239\n\t" \
  "s_setprio 1\n\t" \
  "s_setprio 3\n\t" \
  "s_mov_b32 m0, %[wld]\n\t" \
  "s_add_u32 %[t1], %[wso], %[wpd]\n\t" \
  "buffer_load_dwordx4 v245, %[wrs], %[wso] offen lds\n\t" \
  "buffer_load_dwordx4 v245, %[wrs], %[t1] offen offset:1024 lds\n\t" \
  "s_add_u32 %[t1], %[t1], %[wpd]\n\t" \
  "s_add_u32 %[wso], %[wso], %[wcs]\n\t" \
  "buffer_load_dwordx4 v245, %[wrs], %[t1] offen offset:2048 lds\n\t" \
  "s_add_u32 %[wld], %[wld], %[wst]\n\t" \
  "s_setprio 1\n\t" \
  "s_setprio 3\n\t" \
  "s_waitcnt vmcnt(3)\n\t" \
  "s_barrier\n\t" \
  "s_mov_b32 m0, %[wld]\n\t" \
  "s_add_u32 %[t1], %[wso], %[wpd]\n\t" \
  "buffer_load_dwordx4 v245, %[wrs], %[wso] offen lds\n\t" \
  "buffer_load_dwordx4 v245, %[wrs], %[t1] offen offset:1024 lds\n\t" \
  "s_add_u32 %[t1], %[t1], %[wpd]\n\t" \
  "s_add_u32 %[wso], %[wso], %[wcs]\n\t" \
  "buffer_load_dwordx4 v245, %[wrs], %[t1] offen offset:2048 lds\n\t" \
  "s_add_u32 %[wld], %[wld], %[wst]\n\t" \
  "s_mov_b32 m0, %[xld]\n\t" \
  "s_cmp_lt_u32 %[gy], 0x3000000\n\t" \
  "s_cselect_b32 %[xso], %[gy], 0x40000000\n\t" \
  "buffer_load_dwordx4 v246, %[xrs], %[xso] offen offset:0 nt lds\n\t" \
  "s_add_u32 %[gy], %[gy], %[dgy]\n\t" \
  "buffer_load_dwordx4 v247, %[xrs], %[xso] offen offset:1024 nt lds\n\t" \
  "s_add_u32 %[xld], %[xld], 15184\n\t" \
  "s_cmp_eq_u32 %[xld], %[xle]\n\t" \
  "s_cselect_b32 %[xld], %[xlb], %[xld]\n\t" \
  "buffer_load_dwordx4 v248, %[xrs], %[xso] offen offset:2048 nt lds\n\t" \
  "s_mov_b64 exec, %[xm]\n\t" \
  "buffer_load_dwordx4 v249, %[xrs], %[xso] offen offset:3072 nt lds\n\t" \
  "s_mov_b64 exec, -1\n\t" \
  "s_setprio 1\n\t" \
  "v_cvt_pk_bf16_f32 v230, v60, v61\n\t" \
  "v_cvt_pk_bf16_f32 v231, v62, v63\n\t" \
  "v_fma_f32 v64, v64, v238, v239\n\t" \
  "v_fma_f32 v65, v65, v238, v239\n\t" \
  "v_fma_f32 v66, v66, v238, v239\n\t" \
  "v_fma_f32 v67, v67, v238, v239\n\t" \
  "v_cvt_pk_bf16_f32 v232, v64, v65\n\t" \
  "v_cvt_pk_bf16_f32 v233, v66, v67\n\t" \
  "v_fma_f32 v68, v68, v238, v239\n\t" \
  "v_fma_f32 v69, v69, v238, v239\n\t" \
  "v_fma_f32 v70, v70, v238, v239\n\t" \
  "v_fma_f32 v71, v71, v238, v239\n\t" \
  "v_cvt_pk_bf16_f32 v234, v68, v69\n\t" \
  "v_cvt_pk_bf16_f32 v235, v70, v71\n\t" \
  "v_mul_f32 v237, v72, v72\n\t" \
  "v_add_f32 v236, v72, v73\n\t" \
  "v_fmac_f32 v237, v73, v73\n\t" \
  "v_add_f32 v236, v236, v74\n\t" \
  "s_setprio 3\n\t" \
  "v_mfma_f32_32x32x8_bf16 v[56:71], v[252:253], v[252:253], 0\n\t" \
  "s_waitcnt lgkmcnt(4)\n\t" \
  "ds_write_b128 v242, v[228:231] offset:1024\n\t" \
  "ds_read_b64 v[192:193], v241 offset:32\n\t" \
  "v_fmac_f32 v237, v74, v74\n\t" \
  "v_smfmac_f32_16x16x64_bf16 v[80:83], v[140:143], v[208:215], v240\n\t" \
  "ds_read_b64 v[194:195], v241 offset:432\n\t" \
  "v_add_f32 v236, v236, v75\n\t" \
  "v_smfmac_f32_16x16x64_bf16 v[8:11], v[136:139], v[208:215], v240\n\t" \
  "ds_read_b64 v[196:197], v241 offset:832\n\t" \
  "v_fmac_f32 v237, v75, v75\n\t" \
  "v_smfmac_f32_16x16x64_bf16 v[32:35], v[132:135], v[208:215], v240\n\t" \
  "ds_read_b64 v[198:199], v241 offset:1232\n\t" \
  "v_add_f32 v236, v236, v76\n\t" \
  "v_smfmac_f32_16x16x64_bf16 v[56:59], v[128:131], v[208:215], v240\n\t" \
  "s_waitcnt lgkmcnt(5)\n\t" \
  "ds_write_b128 v242, v[232:235] offset:2048\n\t" \
  "v_fmac_f32 v237, v76, v76\n\t" \
  "v_smfmac_f32_16x16x64_bf16 v[84:87], v[156:159], v[216:223], v240\n\t" \
  "ds_read_b64 v[200:201], v241 offset:40\n\t" \
  "v_add_f32 v236, v236, v77\n\t" \
  "v_smfmac_f32_16x16x64_bf16 v[12:15], v[152:155], v[216:223], v240\n\t" \
  "ds_read_b64 v[202:203], v241 offset:440\n\t" \
  "v_fmac_f32 v237, v77, v77\n\t" \
  "v_smfmac_f32_16x16x64_bf16 v[36:39], v[148:151], v[216:223], v240\n\t" \
  "ds_read_b64 v[204:205], v241 offset:840\n\t" \
  "v_add_f32 v236, v236, v78\n\t" \
  "v_smfmac_f32_16x16x64_bf16 v[60:63], v[144:147], v[216:223], v240\n\t" \
  "ds_read_b64 v[206:207], v241 offset:1240\n\t" \
  "v_fmac_f32 v237, v78, v78\n\t" \
  "s_setprio 1\n\t" \
  "s_setprio 3\n\t" \
  "s_add_u32 %[cs], %[cs], 1\n\t" \
  "s_cmp_eq_u32 %[cs], 6\n\t" \
  "s_cselect_b32 %[cs], 0, %[cs]\n\t" \
  "s_cselect_b32 %[t0], -75920, %[ps]\n\t" \
  "v_add_u32 v241, %[t0], v241\n\t" \
  "s_waitcnt vmcnt(7)\n\t" \
  "s_waitcnt lgkmcnt(4)\n\t" \
  "s_barrier\n\t" \
  "v_smfmac_f32_16x16x64_bf16 v[88:91], v[172:175], v[192:199], v240\n\t" \
  "ds_read_b64 v[208:209], v241 offset:0\n\t" \
  "v_add_f32 v236, v236, v79\n\t" \
  "v_smfmac_f32_16x16x64_bf16 v[16:19], v[168:171], v[192:199], v240\n\t" \
  "ds_read_b64 v[210:211], v241 offset:400\n\t" \
  "v_fmac_f32 v237, v79, v79\n\t" \
  "v_smfmac_f32_16x16x64_bf16 v[40:43], v[164:167], v[192:199], v240\n\t" \
  "ds_read_b64 v[212:213], v241 offset:800\n\t" \
  "v_add_f32 v236, v236, v80\n\t" \
  "v_smfmac_f32_16x16x64_bf16 v[64:67], v[160:163], v[192:199], v240\n\t" \
  "s_waitcnt lgkmcnt(3)\n\t" \
  "ds_read_b64 v[214:215], v241 offset:1200\n\t" \
  "v_fmac_f32 v237, v80, v80\n\t" \
  "v_smfmac_f32_16x16x64_bf16 v[92:95], v[188:191], v[200:207], v240\n\t" \
  "ds_read_b64 v[216:217], v241 offset:8\n\t" \
  "v_add_f32 v236, v236, v81\n\t" \
  "v_smfmac_f32_16x16x64_bf16 v[20:23], v[184:187], v[200:207], v240\n\t" \
  "ds_read_b64 v[218:219], v241 offset:408\n\t" \
  "v_fmac_f32 v237, v81, v81\n\t" \
  "v_smfmac_f32_16x16x64_bf16 v[44:47], v[180:183], v[200:207], v240\n\t" \
  "ds_read_b64 v[220:221], v241 offset:808\n\t" \
  "v_add_f32 v236, v236, v82\n\t" \
  "v_smfmac_f32_16x16x64_bf16 v[68:71], v[176:179], v[200:207], v240\n\t" \
  "ds_read_b64 v[222:223], v241 offset:1208\n\t" \
  "v_fmac_f32 v237, v82, v82\n\t" \
  "s_setprio 1\n\t" \
  "s_setprio 3\n\t" \
  "s_mov_b32 m0, %[wld]\n\t" \
  "s_add_u32 %[t1], %[wso], %[wpd]\n\t" \
  "buffer_load_dwordx4 v245, %[wrs], %[wso] offen lds\n\t" \
  "buffer_load_dwordx4 v245, %[wrs], %[t1] offen offset:1024 lds\n\t" \
  "s_add_u32 %[t1], %[t1], %[wpd]\n\t" \
  "s_add_u32 %[wso], %[wso], %[wcs]\n\t" \
  "buffer_load_dwordx4 v245, %[wrs], %[t1] offen offset:2048 lds\n\t" \
  "s_add_u32 %[wld], %[wld], %[wlc]\n\t" \
  "s_setprio 1\n\t" \
  "v_add_f32 v236, v236, v83\n\t" \
  "v_fmac_f32 v237, v83, v83\n\t" \
  "v_add_f32 v236, v236, v84\n\t" \
  "v_fmac_f32 v237, v84, v84\n\t" \
  "v_add_f32 v236, v236, v85\n\t" \
  "v_fmac_f32 v237, v85, v85\n\t" \
  "v_add_f32 v236, v236, v86\n\t" \
  "v_fmac_f32 v237, v86, v86\n\t" \
  "v_add_f32 v236, v236, v87\n\t" \
  "v_fmac_f32 v237, v87, v87\n\t" \
  "v_add_f32 v236, v236, v88\n\t" \
  "v_fmac_f32 v237, v88, v88\n\t" \
  "v_add_f32 v236, v236, v89\n\t" \
  "v_fmac_f32 v237, v89, v89\n\t" \
  "v_add_f32 v236, v236, v90\n\t" \
  "v_fmac_f32 v237, v90, v90\n\t" \
  "v_add_f32 v236, v236, v91\n\t" \
  "v_fmac_f32 v237, v91, v91\n\t" \
  "v_add_f32 v236, v236, v92\n\t" \
  "v_fmac_f32 v237, v92, v92\n\t" \
  "v_add_f32 v236, v236, v93\n\t" \
  "v_fmac_f32 v237, v93, v93\n\t" \
  "v_add_f32 v236, v236, v94\n\t" \
  "v_fmac_f32 v237, v94, v94\n\t" \
  "v_add_f32 v236, v236, v95\n\t" \
  "v_fmac_f32 v237, v95, v95\n\t" \
  "s_setprio 3\n\t" \
  "ds_write_b64 v243, v[236:237]\n\t" \
  "s_waitcnt vmcnt(7)\n\t" \
  "s_waitcnt lgkmcnt(0)\n\t" \
  "s_barrier\n\t" \
  "ds_read_b128 v[224:227], v244\n\t" \
  "ds_read_b128 v[228:231], v244 offset:1024\n\t" \
  "s_waitcnt lgkmcnt(0)\n\t" \
  "s_setprio 1\n\t" \
  "v_add_f32 v224, v224, v226\n\t" \
  "v_add_f32 v225, v225, v227\n\t" \
  "v_add_f32 v228, v228, v230\n\t" \
  "v_add_f32 v229, v229, v231\n\t" \
  "v_add_f32 v224, v224, v228\n\t" \
  "v_add_f32 v225, v225, v229\n\t" \
  "v_mul_f32 v224, 0x3c2aaaab, v224\n\t" \
  "v_mul_f32 v225, 0x3c2aaaab, v225\n\t" \
  "v_fma_f32 v225, -v224, v224, v225\n\t" \
  "v_max_f32 v225, 0, v225\n\t" \
  "v_add_f32 v225, 0x3727c5ac, v225\n\t" \
  "v_rsq_f32 v225, v225\n\t" \
  "s_nop 0\n\t" \
  "v_mul_f32 v238, %[gl], v225\n\t" \
  "v_mul_f32 v239, -v224, v238\n\t" \
  "v_fma_f32 v72, v72, v238, v239\n\t" \
  "v_fma_f32 v73, v73, v238, v239\n\t" \
  "v_fma_f32 v74, v74, v238, v239\n\t" \
  "v_fma_f32 v75, v75, v238, v239\n\t" \
  "v_cvt_pk_bf16_f32 v224, v72, v73\n\t" \
  "v_cvt_pk_bf16_f32 v225, v74, v75\n\t" \
  "v_fma_f32 v76, v76, v238, v239\n\t" \
  "v_fma_f32 v77, v77, v238, v239\n\t" \
  "v_fma_f32 v78, v78, v238, v239\n\t" \
  "v_fma_f32 v79, v79, v238, v239\n\t" \
  "v_cvt_pk_bf16_f32 v226, v76, v77\n\t" \
  "v_cvt_pk_bf16_f32 v227, v78, v79\n\t" \
  "s_setprio 3\n\t" \
  "v_mfma_f32_16x16x16_bf16 v[72:75], v[252:253], v[252:253], 0\n\t" \
  "ds_write_b128 v242, v[224:227] offset:0\n\t" \
  "v_fma_f32 v80, v80, v238, v239\n\t" \
  "v_mfma_f32_16x16x16_bf16 v[76:79], v[252:253], v[252:253], 0\n\t" \
  "ds_read_b64 v[192:193], v241 offset:16\n\t" \
  "v_fma_f32 v81, v81, v238, v239\n\t" \
  "v_smfmac_f32_16x16x64_bf16 v[0:3], v[108:111], v[208:215], v240\n\t" \
  "ds_read_b64 v[194:195], v241 offset:416\n\t" \
  "v_fma_f32 v82, v82, v238, v239\n\t" \
  "v_smfmac_f32_16x16x64_bf16 v[24:27], v[104:107], v[208:215], v240\n\t" \
  "ds_read_b64 v[196:197], v241 offset:816\n\t" \
  "v_fma_f32 v83, v83, v238, v239\n\t" \
  "v_smfmac_f32_16x16x64_bf16 v[48:51], v[100:103], v[208:215], v240\n\t" \
  "ds_read_b64 v[198:199], v241 offset:1216\n\t" \
  "v_cvt_pk_bf16_f32 v228, v80, v81\n\t" \
  "v_smfmac_f32_16x16x64_bf16 v[72:75], v[96:99], v[208:215], v240\n\t" \
  "ds_read_b64 v[200:201], v241 offset:24\n\t" \
  "v_cvt_pk_bf16_f32 v229, v82, v83\n\t" \
  "v_smfmac_f32_16x16x64_bf16 v[4:7], v[124:127], v[216:223], v240\n\t" \
  "ds_read_b64 v[202:203], v241 offset:424\n\t" \
  "v_fma_f32 v84, v84, v238, v239\n\t" \
  "v_smfmac_f32_16x16x64_bf16 v[28:31], v[120:123], v[216:223], v240\n\t" \
  "ds_read_b64 v[204:205], v241 offset:824\n\t" \
  "v_fma_f32 v85, v85, v238, v239\n\t" \
  "v_smfmac_f32_16x16x64_bf16 v[52:55], v[116:119], v[216:223], v240\n\t" \
  "ds_read_b64 v[206:207], v241 offset:1224\n\t" \
  "v_fma_f32 v86, v86, v238, v239\n\t" \
  "v_smfmac_f32_16x16x64_bf16 v[76:79], v[112:115], v[216:223], v240\n\t" \
  "v_fma_f32 v87, v87, v238, v239\n\t" \
  "s_setprio 1\n\t" \
  "s_setprio 3\n\t" \
  "s_mov_b32 m0, %[wld]\n\t" \
  "s_add_u32 %[t1], %[wso], %[wpd]\n\t" \
  "buffer_load_dwordx4 v245, %[wrs], %[wso] offen lds\n\t" \
  "buffer_load_dwordx4 v245, %[wrs], %[t1] offen offset:1024 lds\n\t" \
  "s_add_u32 %[t1], %[t1], %[wpd]\n\t" \
  "s_add_u32 %[wso], %[wso], %[wcs]\n\t" \
  "buffer_load_dwordx4 v245, %[wrs], %[t1] offen offset:2048 lds\n\t" \
  "s_add_u32 %[wld], %[wld], %[wst]\n\t" \
  "s_setprio 1\n\t" \
  "s_setprio 3\n\t" \
  "s_waitcnt vmcnt(3)\n\t" \
  "s_barrier\n\t" \
  "s_mov_b32 m0, %[wld]\n\t" \
  "s_add_u32 %[t1], %[wso], %[wpd]\n\t" \
  "buffer_load_dwordx4 v245, %[wrs], %[wso] offen lds\n\t" \
  "buffer_load_dwordx4 v245, %[wrs], %[t1] offen offset:1024 lds\n\t" \
  "s_add_u32 %[t1], %[t1], %[wpd]\n\t" \
  "s_add_u32 %[wso], %[wso], %[wcs]\n\t" \
  "buffer_load_dwordx4 v245, %[wrs], %[t1] offen offset:2048 lds\n\t" \
  "s_add_u32 %[wld], %[wld], %[wst]\n\t" \
  "s_mov_b32 m0, %[xld]\n\t" \
  "s_cmp_lt_u32 %[gy], 0x3000000\n\t" \
  "s_cselect_b32 %[xso], %[gy], 0x40000000\n\t" \
  "buffer_load_dwordx4 v246, %[xrs], %[xso] offen offset:0 nt lds\n\t" \
  "s_add_u32 %[gy], %[gy], %[dgy]\n\t" \
  "buffer_load_dwordx4 v247, %[xrs], %[xso] offen offset:1024 nt lds\n\t" \
  "s_add_u32 %[xld], %[xld], 15184\n\t" \
  "s_cmp_eq_u32 %[xld], %[xle]\n\t" \
  "s_cselect_b32 %[xld], %[xlb], %[xld]\n\t" \
  "buffer_load_dwordx4 v248, %[xrs], %[xso] offen offset:2048 nt lds\n\t" \
  "s_mov_b64 exec, %[xm]\n\t" \
  "buffer_load_dwordx4 v249, %[xrs], %[xso] offen offset:3072 nt lds\n\t" \
  "s_mov_b64 exec, -1\n\t" \
  "s_setprio 1\n\t" \
  "v_cvt_pk_bf16_f32 v230, v84, v85\n\t" \
  "v_cvt_pk_bf16_f32 v231, v86, v87\n\t" \
  "v_fma_f32 v88, v88, v238, v239\n\t" \
  "v_fma_f32 v89, v89, v238, v239\n\t" \
  "v_fma_f32 v90, v90, v238, v239\n\t" \
  "v_fma_f32 v91, v91, v238, v239\n\t" \
  "v_cvt_pk_bf16_f32 v232, v88, v89\n\t" \
  "v_cvt_pk_bf16_f32 v233, v90, v91\n\t" \
  "v_fma_f32 v92, v92, v238, v239\n\t" \
  "v_fma_f32 v93, v93, v238, v239\n\t" \
  "v_fma_f32 v94, v94, v238, v239\n\t" \
  "v_fma_f32 v95, v95, v238, v239\n\t" \
  "v_cvt_pk_bf16_f32 v234, v92, v93\n\t" \
  "v_cvt_pk_bf16_f32 v235, v94, v95\n\t" \
  "v_mul_f32 v237, v0, v0\n\t" \
  "v_add_f32 v236, v0, v1\n\t" \
  "v_fmac_f32 v237, v1, v1\n\t" \
  "v_add_f32 v236, v236, v2\n\t" \
  "s_setprio 3\n\t" \
  "v_mfma_f32_32x32x8_bf16 v[80:95], v[252:253], v[252:253], 0\n\t" \
  "s_waitcnt lgkmcnt(4)\n\t" \
  "ds_write_b128 v242, v[228:231] offset:1024\n\t" \
  "ds_read_b64 v[208:209], v241 offset:32\n\t" \
  "v_fmac_f32 v237, v2, v2\n\t" \
  "v_smfmac_f32_16x16x64_bf16 v[8:11], v[140:143], v[192:199], v240\n\t" \
  "ds_read_b64 v[210:211], v241 offset:432\n\t" \
  "v_add_f32 v236, v236, v3\n\t" \
  "v_smfmac_f32_16x16x64_bf16 v[32:35], v[136:139], v[192:199], v240\n\t" \
  "ds_read_b64 v[212:213], v241 offset:832\n\t" \
  "v_fmac_f32 v237, v3, v3\n\t" \
  "v_smfmac_f32_16x16x64_bf16 v[56:59], v[132:135], v[192:199], v240\n\t" \
  "ds_read_b64 v[214:215], v241 offset:1232\n\t" \
  "v_add_f32 v236, v236, v4\n\t" \
  "v_smfmac_f32_16x16x64_bf16 v[80:83], v[128:131], v[192:199], v240\n\t" \
  "s_waitcnt lgkmcnt(5)\n\t" \
  "ds_write_b128 v242, v[232:235] offset:2048\n\t" \
  "v_fmac_f32 v237, v4, v4\n\t" \
  "v_smfmac_f32_16x16x64_bf16 v[12:15], v[156:159], v[200:207], v240\n\t" \
  "ds_read_b64 v[216:217], v241 offset:40\n\t" \
  "v_add_f32 v236, v236, v5\n\t" \
  "v_smfmac_f32_16x16x64_bf16 v[36:39], v[152:155], v[200:207], v240\n\t" \
  "ds_read_b64 v[218:219], v241 offset:440\n\t" \
  "v_fmac_f32 v237, v5, v5\n\t" \
  "v_smfmac_f32_16x16x64_bf16 v[60:63], v[148:151], v[200:207], v240\n\t" \
  "ds_read_b64 v[220:221], v241 offset:840\n\t" \
  "v_add_f32 v236, v236, v6\n\t" \
  "v_smfmac_f32_16x16x64_bf16 v[84:87], v[144:147], v[200:207], v240\n\t" \
  "ds_read_b64 v[222:223], v241 offset:1240\n\t" \
  "v_fmac_f32 v237, v6, v6\n\t" \
  "s_setprio 1\n\t" \
  "s_setprio 3\n\t" \
  "s_add_u32 %[cs], %[cs], 1\n\t" \
  "s_cmp_eq_u32 %[cs], 6\n\t" \
  "s_cselect_b32 %[cs], 0, %[cs]\n\t" \
  "s_cselect_b32 %[t0], -75920, %[ps]\n\t" \
  "v_add_u32 v241, %[t0], v241\n\t" \
  "s_waitcnt vmcnt(7)\n\t" \
  "s_waitcnt lgkmcnt(4)\n\t" \
  "s_barrier\n\t" \
  "v_smfmac_f32_16x16x64_bf16 v[16:19], v[172:175], v[208:215], v240\n\t" \
  "ds_read_b64 v[192:193], v241 offset:0\n\t" \
  "v_add_f32 v236, v236, v7\n\t" \
  "v_smfmac_f32_16x16x64_bf16 v[40:43], v[168:171], v[208:215], v240\n\t" \
  "ds_read_b64 v[194:195], v241 offset:400\n\t" \
  "v_fmac_f32 v237, v7, v7\n\t" \
  "v_smfmac_f32_16x16x64_bf16 v[64:67], v[164:167], v[208:215], v240\n\t" \
  "ds_read_b64 v[196:197], v241 offset:800\n\t" \
  "v_add_f32 v236, v236, v8\n\t" \
  "v_smfmac_f32_16x16x64_bf16 v[88:91], v[160:163], v[208:215], v240\n\t" \
  "s_waitcnt lgkmcnt(3)\n\t" \
  "ds_read_b64 v[198:199], v241 offset:1200\n\t" \
  "v_fmac_f32 v237, v8, v8\n\t" \
  "v_smfmac_f32_16x16x64_bf16 v[20:23], v[188:191], v[216:223], v240\n\t" \
  "ds_read_b64 v[200:201], v241 offset:8\n\t" \
  "v_add_f32 v236, v236, v9\n\t" \
  "v_smfmac_f32_16x16x64_bf16 v[44:47], v[184:187], v[216:223], v240\n\t" \
  "ds_read_b64 v[202:203], v241 offset:408\n\t" \
  "v_fmac_f32 v237, v9, v9\n\t" \
  "v_smfmac_f32_16x16x64_bf16 v[68:71], v[180:183], v[216:223], v240\n\t" \
  "ds_read_b64 v[204:205], v241 offset:808\n\t" \
  "v_add_f32 v236, v236, v10\n\t" \
  "v_smfmac_f32_16x16x64_bf16 v[92:95], v[176:179], v[216:223], v240\n\t" \
  "ds_read_b64 v[206:207], v241 offset:1208\n\t" \
  "v_fmac_f32 v237, v10, v10\n\t" \
  "s_setprio 1\n\t" \
  "s_setprio 3\n\t" \
  "s_mov_b32 m0, %[wld]\n\t" \
  "s_add_u32 %[t1], %[wso], %[wpd]\n\t" \
  "buffer_load_dwordx4 v245, %[wrs], %[wso] offen lds\n\t" \
  "buffer_load_dwordx4 v245, %[wrs], %[t1] offen offset:1024 lds\n\t" \
  "s_add_u32 %[t1], %[t1], %[wpd]\n\t" \
  "s_add_u32 %[wso], %[wso], %[wsa]\n\t" \
  "buffer_load_dwordx4 v245, %[wrs], %[t1] offen offset:2048 lds\n\t" \
  "s_add_u32 %[wld], %[wld], %[wla]\n\t" \
  "s_setprio 1\n\t" \
  "v_add_f32 v236, v236, v11\n\t" \
  "v_fmac_f32 v237, v11, v11\n\t" \
  "v_add_f32 v236, v236, v12\n\t" \
  "v_fmac_f32 v237, v12, v12\n\t" \
  "v_add_f32 v236, v236, v13\n\t" \
  "v_fmac_f32 v237, v13, v13\n\t" \
  "v_add_f32 v236, v236, v14\n\t" \
  "v_fmac_f32 v237, v14, v14\n\t" \
  "v_add_f32 v236, v236, v15\n\t" \
  "v_fmac_f32 v237, v15, v15\n\t" \
  "v_add_f32 v236, v236, v16\n\t" \
  "v_fmac_f32 v237, v16, v16\n\t" \
  "v_add_f32 v236, v236, v17\n\t" \
  "v_fmac_f32 v237, v17, v17\n\t" \
  "v_add_f32 v236, v236, v18\n\t" \
  "v_fmac_f32 v237, v18, v18\n\t" \
  "v_add_f32 v236, v236, v19\n\t" \
  "v_fmac_f32 v237, v19, v19\n\t" \
  "v_add_f32 v236, v236, v20\n\t" \
  "v_fmac_f32 v237, v20, v20\n\t" \
  "v_add_f32 v236, v236, v21\n\t" \
  "v_fmac_f32 v237, v21, v21\n\t" \
  "v_add_f32 v236, v236, v22\n\t" \
  "v_fmac_f32 v237, v22, v22\n\t" \
  "v_add_f32 v236, v236, v23\n\t" \
  "v_fmac_f32 v237, v23, v23\n\t" \
  "s_setprio 3\n\t" \
  "ds_write_b64 v243, v[236:237]\n\t" \
  "s_waitcnt vmcnt(7)\n\t" \
  "s_waitcnt lgkmcnt(0)\n\t" \
  "s_barrier\n\t" \
  "ds_read_b128 v[224:227], v244\n\t" \
  "ds_read_b128 v[228:231], v244 offset:1024\n\t" \
  "s_waitcnt lgkmcnt(0)\n\t" \
  "s_setprio 1\n\t" \
  "v_add_f32 v224, v224, v226\n\t" \
  "v_add_f32 v225, v225, v227\n\t" \
  "v_add_f32 v228, v228, v230\n\t" \
  "v_add_f32 v229, v229, v231\n\t" \
  "v_add_f32 v224, v224, v228\n\t" \
  "v_add_f32 v225, v225, v229\n\t" \
  "v_mul_f32 v224, 0x3c2aaaab, v224\n\t" \
  "v_mul_f32 v225, 0x3c2aaaab, v225\n\t" \
  "v_fma_f32 v225, -v224, v224, v225\n\t" \
  "v_max_f32 v225, 0, v225\n\t" \
  "v_add_f32 v225, 0x3727c5ac, v225\n\t" \
  "v_rsq_f32 v225, v225\n\t" \
  "s_nop 0\n\t" \
  "v_mul_f32 v238, %[gl], v225\n\t" \
  "v_mul_f32 v239, -v224, v238\n\t" \
  "v_fma_f32 v0, v0, v238, v239\n\t" \
  "v_fma_f32 v1, v1, v238, v239\n\t" \
  "v_fma_f32 v2, v2, v238, v239\n\t" \
  "v_fma_f32 v3, v3, v238, v239\n\t" \
  "v_cvt_pk_bf16_f32 v224, v0, v1\n\t" \
  "v_cvt_pk_bf16_f32 v225, v2, v3\n\t" \
  "v_fma_f32 v4, v4, v238, v239\n\t" \
  "v_fma_f32 v5, v5, v238, v239\n\t" \
  "v_fma_f32 v6, v6, v238, v239\n\t" \
  "v_fma_f32 v7, v7, v238, v239\n\t" \
  "v_cvt_pk_bf16_f32 v226, v4, v5\n\t" \
  "v_cvt_pk_bf16_f32 v227, v6, v7\n\t" \
  "s_setprio 3\n\t" \
  "v_mfma_f32_16x16x16_bf16 v[0:3], v[252:253], v[252:253], 0\n\t" \
  "ds_write_b128 v242, v[224:227] offset:0\n\t" \
  "v_fma_f32 v8, v8, v238, v239\n\t" \
  "v_mfma_f32_16x16x16_bf16 v[4:7], v[252:253], v[252:253], 0\n\t" \
  "ds_read_b64 v[208:209], v241 offset:16\n\t" \
  "v_fma_f32 v9, v9, v238, v239\n\t" \
  "v_smfmac_f32_16x16x64_bf16 v[24:27], v[108:111], v[192:199], v240\n\t" \
  "ds_read_b64 v[210:211], v241 offset:416\n\t" \
  "v_fma_f32 v10, v10, v238, v239\n\t" \
  "v_smfmac_f32_16x16x64_bf16 v[48:51], v[104:107], v[192:199], v240\n\t" \
  "ds_read_b64 v[212:213], v241 offset:816\n\t" \
  "v_fma_f32 v11, v11, v238, v239\n\t" \
  "v_smfmac_f32_16x16x64_bf16 v[72:75], v[100:103], v[192:199], v240\n\t" \
  "ds_read_b64 v[214:215], v241 offset:1216\n\t" \
  "v_cvt_pk_bf16_f32 v228, v8, v9\n\t" \
  "v_smfmac_f32_16x16x64_bf16 v[0:3], v[96:99], v[192:199], v240\n\t" \
  "ds_read_b64 v[216:217], v241 offset:24\n\t" \
  "v_cvt_pk_bf16_f32 v229, v10, v11\n\t" \
  "v_smfmac_f32_16x16x64_bf16 v[28:31], v[124:127], v[200:207], v240\n\t" \
  "ds_read_b64 v[218:219], v241 offset:424\n\t" \
  "v_fma_f32 v12, v12, v238, v239\n\t" \
  "v_smfmac_f32_16x16x64_bf16 v[52:55], v[120:123], v[200:207], v240\n\t" \
  "ds_read_b64 v[220:221], v241 offset:824\n\t" \
  "v_fma_f32 v13, v13, v238, v239\n\t" \
  "v_smfmac_f32_16x16x64_bf16 v[76:79], v[116:119], v[200:207], v240\n\t" \
  "ds_read_b64 v[222:223], v241 offset:1224\n\t" \
  "v_fma_f32 v14, v14, v238, v239\n\t" \
  "v_smfmac_f32_16x16x64_bf16 v[4:7], v[112:115], v[200:207], v240\n\t" \
  "v_fma_f32 v15, v15, v238, v239\n\t" \
  "s_setprio 1\n\t" \
  "s_setprio 3\n\t" \
  "s_mov_b32 m0, %[wld]\n\t" \
  "s_add_u32 %[t1], %[wso], %[wpd]\n\t" \
  "buffer_load_dwordx4 v245, %[wrs], %[wso] offen lds\n\t" \
  "buffer_load_dwordx4 v245, %[wrs], %[t1] offen offset:1024 lds\n\t" \
  "s_add_u32 %[t1], %[t1], %[wpd]\n\t" \
  "s_add_u32 %[wso], %[wso], %[wsb]\n\t" \
  "buffer_load_dwordx4 v245, %[wrs], %[t1] offen offset:2048 lds\n\t" \
  "s_add_u32 %[wld], %[wld], %[wlb]\n\t" \
  "s_setprio 1\n\t" \
  "s_setprio 3\n\t" \
  "s_waitcnt vmcnt(3)\n\t" \
  "s_barrier\n\t" \
  "s_mov_b32 m0, %[wld]\n\t" \
  "s_add_u32 %[t1], %[wso], %[wpd]\n\t" \
  "buffer_load_dwordx4 v245, %[wrs], %[wso] offen lds\n\t" \
  "buffer_load_dwordx4 v245, %[wrs], %[t1] offen offset:1024 lds\n\t" \
  "s_add_u32 %[t1], %[t1], %[wpd]\n\t" \
  "s_add_u32 %[wso], %[wso], %[wcs]\n\t" \
  "buffer_load_dwordx4 v245, %[wrs], %[t1] offen offset:2048 lds\n\t" \
  "s_add_u32 %[wld], %[wld], %[wst]\n\t" \
  "s_mov_b32 m0, %[xld]\n\t" \
  "s_cmp_lt_u32 %[gy], 0x3000000\n\t" \
  "s_cselect_b32 %[xso], %[gy], 0x40000000\n\t" \
  "buffer_load_dwordx4 v246, %[xrs], %[xso] offen offset:0 nt lds\n\t" \
  "s_add_u32 %[gy], %[gy], %[dgy]\n\t" \
  "buffer_load_dwordx4 v247, %[xrs], %[xso] offen offset:1024 nt lds\n\t" \
  "s_add_u32 %[xld], %[xld], 15184\n\t" \
  "s_cmp_eq_u32 %[xld], %[xle]\n\t" \
  "s_cselect_b32 %[xld], %[xlb], %[xld]\n\t" \
  "buffer_load_dwordx4 v248, %[xrs], %[xso] offen offset:2048 nt lds\n\t" \
  "s_mov_b64 exec, %[xm]\n\t" \
  "buffer_load_dwordx4 v249, %[xrs], %[xso] offen offset:3072 nt lds\n\t" \
  "s_mov_b64 exec, -1\n\t" \
  "s_setprio 1\n\t" \
  "v_cvt_pk_bf16_f32 v230, v12, v13\n\t" \
  "v_cvt_pk_bf16_f32 v231, v14, v15\n\t" \
  "v_fma_f32 v16, v16, v238, v239\n\t" \
  "v_fma_f32 v17, v17, v238, v239\n\t" \
  "v_fma_f32 v18, v18, v238, v239\n\t" \
  "v_fma_f32 v19, v19, v238, v239\n\t" \
  "v_cvt_pk_bf16_f32 v232, v16, v17\n\t" \
  "v_cvt_pk_bf16_f32 v233, v18, v19\n\t" \
  "v_fma_f32 v20, v20, v238, v239\n\t" \
  "v_fma_f32 v21, v21, v238, v239\n\t" \
  "v_fma_f32 v22, v22, v238, v239\n\t" \
  "v_fma_f32 v23, v23, v238, v239\n\t" \
  "v_cvt_pk_bf16_f32 v234, v20, v21\n\t" \
  "v_cvt_pk_bf16_f32 v235, v22, v23\n\t" \
  "v_mul_f32 v237, v24, v24\n\t" \
  "v_add_f32 v236, v24, v25\n\t" \
  "v_fmac_f32 v237, v25, v25\n\t" \
  "v_add_f32 v236, v236, v26\n\t" \
  "s_setprio 3\n\t" \
  "v_mfma_f32_32x32x8_bf16 v[8:23], v[252:253], v[252:253], 0\n\t" \
  "s_waitcnt lgkmcnt(4)\n\t" \
  "ds_write_b128 v242, v[228:231] offset:1024\n\t" \
  "ds_read_b64 v[192:193], v241 offset:32\n\t" \
  "v_fmac_f32 v237, v26, v26\n\t" \
  "v_smfmac_f32_16x16x64_bf16 v[32:35], v[140:143], v[208:215], v240\n\t" \
  "ds_read_b64 v[194:195], v241 offset:432\n\t" \
  "v_add_f32 v236, v236, v27\n\t" \
  "v_smfmac_f32_16x16x64_bf16 v[56:59], v[136:139], v[208:215], v240\n\t" \
  "ds_read_b64 v[196:197], v241 offset:832\n\t" \
  "v_fmac_f32 v237, v27, v27\n\t" \
  "v_smfmac_f32_16x16x64_bf16 v[80:83], v[132:135], v[208:215], v240\n\t" \
  "ds_read_b64 v[198:199], v241 offset:1232\n\t" \
  "v_add_f32 v236, v236, v28\n\t" \
  "v_smfmac_f32_16x16x64_bf16 v[8:11], v[128:131], v[208:215], v240\n\t" \
  "s_waitcnt lgkmcnt(5)\n\t" \
  "ds_write_b128 v242, v[232:235] offset:2048\n\t" \
  "v_fmac_f32 v237, v28, v28\n\t" \
  "v_smfmac_f32_16x16x64_bf16 v[36:39], v[156:159], v[216:223], v240\n\t" \
  "ds_read_b64 v[200:201], v241 offset:40\n\t" \
  "v_add_f32 v236, v236, v29\n\t" \
  "v_smfmac_f32_16x16x64_bf16 v[60:63], v[152:155], v[216:223], v240\n\t" \
  "ds_read_b64 v[202:203], v241 offset:440\n\t" \
  "v_fmac_f32 v237, v29, v29\n\t" \
  "v_smfmac_f32_16x16x64_bf16 v[84:87], v[148:151], v[216:223], v240\n\t" \
  "ds_read_b64 v[204:205], v241 offset:840\n\t" \
  "v_add_f32 v236, v236, v30\n\t" \
  "v_smfmac_f32_16x16x64_bf16 v[12:15], v[144:147], v[216:223], v240\n\t" \
  "ds_read_b64 v[206:207], v241 offset:1240\n\t" \
  "v_fmac_f32 v237, v30, v30\n\t" \
  "s_setprio 1\n\t" \
  "s_setprio 3\n\t" \
  "s_add_u32 %[cs], %[cs], 1\n\t" \
  "s_cmp_eq_u32 %[cs], 6\n\t" \
  "s_cselect_b32 %[cs], 0, %[cs]\n\t" \
  "s_cselect_b32 %[t0], -75920, %[ps]\n\t" \
  "v_add_u32 v241, %[t0], v241\n\t" \
  "s_waitcnt vmcnt(7)\n\t" \
  "s_waitcnt lgkmcnt(4)\n\t" \
  "s_barrier\n\t" \
  "v_smfmac_f32_16x16x64_bf16 v[40:43], v[172:175], v[192:199], v240\n\t" \
  "ds_read_b64 v[208:209], v241 offset:0\n\t" \
  "v_add_f32 v236, v236, v31\n\t" \
  "v_smfmac_f32_16x16x64_bf16 v[64:67], v[168:171], v[192:199], v240\n\t" \
  "ds_read_b64 v[210:211], v241 offset:400\n\t" \
  "v_fmac_f32 v237, v31, v31\n\t" \
  "v_smfmac_f32_16x16x64_bf16 v[88:91], v[164:167], v[192:199], v240\n\t" \
  "ds_read_b64 v[212:213], v241 offset:800\n\t" \
  "v_add_f32 v236, v236, v32\n\t" \
  "v_smfmac_f32_16x16x64_bf16 v[16:19], v[160:163], v[192:199], v240\n\t" \
  "s_waitcnt lgkmcnt(3)\n\t" \
  "ds_read_b64 v[214:215], v241 offset:1200\n\t" \
  "v_fmac_f32 v237, v32, v32\n\t" \
  "v_smfmac_f32_16x16x64_bf16 v[44:47], v[188:191], v[200:207], v240\n\t" \
  "ds_read_b64 v[216:217], v241 offset:8\n\t" \
  "v_add_f32 v236, v236, v33\n\t" \
  "v_smfmac_f32_16x16x64_bf16 v[68:71], v[184:187], v[200:207], v240\n\t" \
  "ds_read_b64 v[218:219], v241 offset:408\n\t" \
  "v_fmac_f32 v237, v33, v33\n\t" \
  "v_smfmac_f32_16x16x64_bf16 v[92:95], v[180:183], v[200:207], v240\n\t" \
  "ds_read_b64 v[220:221], v241 offset:808\n\t" \
  "v_add_f32 v236, v236, v34\n\t" \
  "v_smfmac_f32_16x16x64_bf16 v[20:23], v[176:179], v[200:207], v240\n\t" \
  "ds_read_b64 v[222:223], v241 offset:1208\n\t" \
  "v_fmac_f32 v237, v34, v34\n\t" \
  "s_setprio 1\n\t" \
  "s_setprio 3\n\t" \
  "s_mov_b32 m0, %[wld]\n\t" \
  "s_add_u32 %[t1], %[wso], %[wpd]\n\t" \
  "buffer_load_dwordx4 v245, %[wrs], %[wso] offen lds\n\t" \
  "buffer_load_dwordx4 v245, %[wrs], %[t1] offen offset:1024 lds\n\t" \
  "s_add_u32 %[t1], %[t1], %[wpd]\n\t" \
  "s_add_u32 %[wso], %[wso], %[wcs]\n\t" \
  "buffer_load_dwordx4 v245, %[wrs], %[t1] offen offset:2048 lds\n\t" \
  "s_add_u32 %[wld], %[wld], %[wla]\n\t" \
  "s_setprio 1\n\t" \
  "v_add_f32 v236, v236, v35\n\t" \
  "v_fmac_f32 v237, v35, v35\n\t" \
  "v_add_f32 v236, v236, v36\n\t" \
  "v_fmac_f32 v237, v36, v36\n\t" \
  "v_add_f32 v236, v236, v37\n\t" \
  "v_fmac_f32 v237, v37, v37\n\t" \
  "v_add_f32 v236, v236, v38\n\t" \
  "v_fmac_f32 v237, v38, v38\n\t" \
  "v_add_f32 v236, v236, v39\n\t" \
  "v_fmac_f32 v237, v39, v39\n\t" \
  "v_add_f32 v236, v236, v40\n\t" \
  "v_fmac_f32 v237, v40, v40\n\t" \
  "v_add_f32 v236, v236, v41\n\t" \
  "v_fmac_f32 v237, v41, v41\n\t" \
  "v_add_f32 v236, v236, v42\n\t" \
  "v_fmac_f32 v237, v42, v42\n\t" \
  "v_add_f32 v236, v236, v43\n\t" \
  "v_fmac_f32 v237, v43, v43\n\t" \
  "v_add_f32 v236, v236, v44\n\t" \
  "v_fmac_f32 v237, v44, v44\n\t" \
  "v_add_f32 v236, v236, v45\n\t" \
  "v_fmac_f32 v237, v45, v45\n\t" \
  "v_add_f32 v236, v236, v46\n\t" \
  "v_fmac_f32 v237, v46, v46\n\t" \
  "v_add_f32 v236, v236, v47\n\t" \
  "v_fmac_f32 v237, v47, v47\n\t" \
  "s_setprio 3\n\t" \
  "ds_write_b64 v243, v[236:237]\n\t" \
  "s_waitcnt vmcnt(7)\n\t" \
  "s_waitcnt lgkmcnt(0)\n\t" \
  "s_barrier\n\t" \
  "s_sub_u32 %[lc], %[lc], 1\n\t" \
  "s_cmp_lg_u32 %[lc], 0\n\t" \
  "s_cbranch_scc1 .Lcnx_f\n\t" \
  "ds_read_b128 v[224:227], v244\n\t" \
  "ds_read_b128 v[228:231], v244 offset:1024\n\t" \
  "s_waitcnt lgkmcnt(0)\n\t" \
  "s_setprio 1\n\t" \
  "v_add_f32 v224, v224, v226\n\t" \
  "v_add_f32 v225, v225, v227\n\t" \
  "v_add_f32 v228, v228, v230\n\t" \
  "v_add_f32 v229, v229, v231\n\t" \
  "v_add_f32 v224, v224, v228\n\t" \
  "v_add_f32 v225, v225, v229\n\t" \
  "v_mul_f32 v224, 0x3c2aaaab, v224\n\t" \
  "v_mul_f32 v225, 0x3c2aaaab, v225\n\t" \
  "v_fma_f32 v225, -v224, v224, v225\n\t" \
  "v_max_f32 v225, 0, v225\n\t" \
  "v_add_f32 v225, 0x3727c5ac, v225\n\t" \
  "v_rsq_f32 v225, v225\n\t" \
  "s_nop 0\n\t" \
  "v_mul_f32 v238, %[gl], v225\n\t" \
  "v_mul_f32 v239, -v224, v238\n\t" \
  "v_fma_f32 v24, v24, v238, v239\n\t" \
  "v_fma_f32 v25, v25, v238, v239\n\t" \
  "v_fma_f32 v26, v26, v238, v239\n\t" \
  "v_fma_f32 v27, v27, v238, v239\n\t" \
  "v_cvt_pk_bf16_f32 v224, v24, v25\n\t" \
  "v_cvt_pk_bf16_f32 v225, v26, v27\n\t" \
  "v_fma_f32 v28, v28, v238, v239\n\t" \
  "v_fma_f32 v29, v29, v238, v239\n\t" \
  "v_fma_f32 v30, v30, v238, v239\n\t" \
  "v_fma_f32 v31, v31, v238, v239\n\t" \
  "v_cvt_pk_bf16_f32 v226, v28, v29\n\t" \
  "v_cvt_pk_bf16_f32 v227, v30, v31\n\t" \
  "s_setprio 3\n\t" \
  "v_smfmac_f32_16x16x64_bf16 v[48:51], v[108:111], v[208:215], v240\n\t" \
  "ds_write_b128 v242, v[224:227] offset:0\n\t" \
  "ds_read_b64 v[192:193], v241 offset:16\n\t" \
  "ds_read_b64 v[194:195], v241 offset:416\n\t" \
  "v_fma_f32 v32, v32, v238, v239\n\t" \
  "v_fma_f32 v33, v33, v238, v239\n\t" \
  "v_smfmac_f32_16x16x64_bf16 v[72:75], v[104:107], v[208:215], v240\n\t" \
  "ds_read_b64 v[196:197], v241 offset:816\n\t" \
  "ds_read_b64 v[198:199], v241 offset:1216\n\t" \
  "v_fma_f32 v34, v34, v238, v239\n\t" \
  "v_fma_f32 v35, v35, v238, v239\n\t" \
  "v_smfmac_f32_16x16x64_bf16 v[52:55], v[124:127], v[216:223], v240\n\t" \
  "ds_read_b64 v[200:201], v241 offset:24\n\t" \
  "ds_read_b64 v[202:203], v241 offset:424\n\t" \
  "v_cvt_pk_bf16_f32 v228, v32, v33\n\t" \
  "v_cvt_pk_bf16_f32 v229, v34, v35\n\t" \
  "v_smfmac_f32_16x16x64_bf16 v[76:79], v[120:123], v[216:223], v240\n\t" \
  "ds_read_b64 v[204:205], v241 offset:824\n\t" \
  "ds_read_b64 v[206:207], v241 offset:1224\n\t" \
  "v_fma_f32 v36, v36, v238, v239\n\t" \
  "v_fma_f32 v37, v37, v238, v239\n\t" \
  "s_setprio 1\n\t" \
  "s_setprio 3\n\t" \
  "s_mov_b32 m0, %[wld]\n\t" \
  "s_add_u32 %[t1], %[wso], %[wpd]\n\t" \
  "buffer_load_dwordx4 v245, %[wrs], %[wso] offen lds\n\t" \
  "buffer_load_dwordx4 v245, %[wrs], %[t1] offen offset:1024 lds\n\t" \
  "s_add_u32 %[t1], %[t1], %[wpd]\n\t" \
  "s_add_u32 %[wso], %[wso], %[wcs]\n\t" \
  "buffer_load_dwordx4 v245, %[wrs], %[t1] offen offset:2048 lds\n\t" \
  "s_add_u32 %[wld], %[wld], %[wst]\n\t" \
  "s_setprio 1\n\t" \
  "s_setprio 3\n\t" \
  "s_waitcnt vmcnt(3)\n\t" \
  "s_barrier\n\t" \
  "s_mov_b32 m0, %[wld]\n\t" \
  "s_add_u32 %[t1], %[wso], %[wpd]\n\t" \
  "buffer_load_dwordx4 v245, %[wrs], %[wso] offen lds\n\t" \
  "buffer_load_dwordx4 v245, %[wrs], %[t1] offen offset:1024 lds\n\t" \
  "s_add_u32 %[t1], %[t1], %[wpd]\n\t" \
  "s_add_u32 %[wso], %[wso], %[wcs]\n\t" \
  "buffer_load_dwordx4 v245, %[wrs], %[t1] offen offset:2048 lds\n\t" \
  "s_add_u32 %[wld], %[wld], %[wlb]\n\t" \
  "s_setprio 1\n\t" \
  "v_fma_f32 v38, v38, v238, v239\n\t" \
  "v_fma_f32 v39, v39, v238, v239\n\t" \
  "v_cvt_pk_bf16_f32 v230, v36, v37\n\t" \
  "v_cvt_pk_bf16_f32 v231, v38, v39\n\t" \
  "v_fma_f32 v40, v40, v238, v239\n\t" \
  "v_fma_f32 v41, v41, v238, v239\n\t" \
  "v_fma_f32 v42, v42, v238, v239\n\t" \
  "v_fma_f32 v43, v43, v238, v239\n\t" \
  "v_cvt_pk_bf16_f32 v232, v40, v41\n\t" \
  "v_cvt_pk_bf16_f32 v233, v42, v43\n\t" \
  "v_fma_f32 v44, v44, v238, v239\n\t" \
  "v_fma_f32 v45, v45, v238, v239\n\t" \
  "v_fma_f32 v46, v46, v238, v239\n\t" \
  "v_fma_f32 v47, v47, v238, v239\n\t" \
  "v_cvt_pk_bf16_f32 v234, v44, v45\n\t" \
  "v_cvt_pk_bf16_f32 v235, v46, v47\n\t" \
  "v_mul_f32 v237, v48, v48\n\t" \
  "v_add_f32 v236, v48, v49\n\t" \
  "v_fmac_f32 v237, v49, v49\n\t" \
  "v_add_f32 v236, v236, v50\n\t" \
  "s_waitcnt lgkmcnt(4)\n\t" \
  "s_setprio 3\n\t" \
  "v_smfmac_f32_16x16x64_bf16 v[56:59], v[140:143], v[192:199], v240\n\t" \
  "ds_write_b128 v242, v[228:231] offset:1024\n\t" \
  "ds_read_b64 v[208:209], v241 offset:32\n\t" \
  "ds_read_b64 v[210:211], v241 offset:432\n\t" \
  "v_fmac_f32 v237, v50, v50\n\t" \
  "v_add_f32 v236, v236, v51\n\t" \
  "v_smfmac_f32_16x16x64_bf16 v[80:83], v[136:139], v[192:199], v240\n\t" \
  "s_waitcnt lgkmcnt(3)\n\t" \
  "ds_read_b64 v[212:213], v241 offset:832\n\t" \
  "ds_read_b64 v[214:215], v241 offset:1232\n\t" \
  "ds_write_b128 v242, v[232:235] offset:2048\n\t" \
  "v_fmac_f32 v237, v51, v51\n\t" \
  "v_add_f32 v236, v236, v52\n\t" \
  "v_smfmac_f32_16x16x64_bf16 v[60:63], v[156:159], v[200:207], v240\n\t" \
  "ds_read_b64 v[216:217], v241 offset:40\n\t" \
  "ds_read_b64 v[218:219], v241 offset:440\n\t" \
  "v_fmac_f32 v237, v52, v52\n\t" \
  "v_add_f32 v236, v236, v53\n\t" \
  "v_smfmac_f32_16x16x64_bf16 v[84:87], v[152:155], v[200:207], v240\n\t" \
  "ds_read_b64 v[220:221], v241 offset:840\n\t" \
  "ds_read_b64 v[222:223], v241 offset:1240\n\t" \
  "v_fmac_f32 v237, v53, v53\n\t" \
  "v_add_f32 v236, v236, v54\n\t" \
  "s_setprio 1\n\t" \
  "s_setprio 3\n\t" \
  "s_add_u32 %[cs], %[cs], 1\n\t" \
  "s_cmp_eq_u32 %[cs], 6\n\t" \
  "s_cselect_b32 %[cs], 0, %[cs]\n\t" \
  "s_cselect_b32 %[t0], -75920, %[ps]\n\t" \
  "v_add_u32 v241, %[t0], v241\n\t" \
  "s_waitcnt vmcnt(3)\n\t" \
  "s_waitcnt lgkmcnt(4)\n\t" \
  "s_barrier\n\t" \
  "v_smfmac_f32_16x16x64_bf16 v[64:67], v[172:175], v[208:215], v240\n\t" \
  "ds_read_b64 v[192:193], v241 offset:0\n\t" \
  "ds_read_b64 v[194:195], v241 offset:400\n\t" \
  "v_fmac_f32 v237, v54, v54\n\t" \
  "v_add_f32 v236, v236, v55\n\t" \
  "v_smfmac_f32_16x16x64_bf16 v[88:91], v[168:171], v[208:215], v240\n\t" \
  "s_waitcnt lgkmcnt(2)\n\t" \
  "ds_read_b64 v[196:197], v241 offset:800\n\t" \
  "ds_read_b64 v[198:199], v241 offset:1200\n\t" \
  "v_fmac_f32 v237, v55, v55\n\t" \
  "v_add_f32 v236, v236, v56\n\t" \
  "v_smfmac_f32_16x16x64_bf16 v[68:71], v[188:191], v[216:223], v240\n\t" \
  "ds_read_b64 v[200:201], v241 offset:8\n\t" \
  "ds_read_b64 v[202:203], v241 offset:408\n\t" \
  "v_fmac_f32 v237, v56, v56\n\t" \
  "v_add_f32 v236, v236, v57\n\t" \
  "v_smfmac_f32_16x16x64_bf16 v[92:95], v[184:187], v[216:223], v240\n\t" \
  "ds_read_b64 v[204:205], v241 offset:808\n\t" \
  "ds_read_b64 v[206:207], v241 offset:1208\n\t" \
  "v_fmac_f32 v237, v57, v57\n\t" \
  "v_add_f32 v236, v236, v58\n\t" \
  "s_setprio 1\n\t" \
  "s_setprio 3\n\t" \
  "s_mov_b32 m0, %[wld]\n\t" \
  "s_add_u32 %[t1], %[wso], %[wpd]\n\t" \
  "buffer_load_dwordx4 v245, %[wrs], %[wso] offen lds\n\t" \
  "buffer_load_dwordx4 v245, %[wrs], %[t1] offen offset:1024 lds\n\t" \
  "s_add_u32 %[t1], %[t1], %[wpd]\n\t" \
  "s_add_u32 %[wso], %[wso], %[wcs]\n\t" \
  "buffer_load_dwordx4 v245, %[wrs], %[t1] offen offset:2048 lds\n\t" \
  "s_add_u32 %[wld], %[wld], %[wla]\n\t" \
  "s_setprio 1\n\t" \
  "v_fmac_f32 v237, v58, v58\n\t" \
  "v_add_f32 v236, v236, v59\n\t" \
  "v_fmac_f32 v237, v59, v59\n\t" \
  "v_add_f32 v236, v236, v60\n\t" \
  "v_fmac_f32 v237, v60, v60\n\t" \
  "v_add_f32 v236, v236, v61\n\t" \
  "v_fmac_f32 v237, v61, v61\n\t" \
  "v_add_f32 v236, v236, v62\n\t" \
  "v_fmac_f32 v237, v62, v62\n\t" \
  "v_add_f32 v236, v236, v63\n\t" \
  "v_fmac_f32 v237, v63, v63\n\t" \
  "v_add_f32 v236, v236, v64\n\t" \
  "v_fmac_f32 v237, v64, v64\n\t" \
  "v_add_f32 v236, v236, v65\n\t" \
  "v_fmac_f32 v237, v65, v65\n\t" \
  "v_add_f32 v236, v236, v66\n\t" \
  "v_fmac_f32 v237, v66, v66\n\t" \
  "v_add_f32 v236, v236, v67\n\t" \
  "v_fmac_f32 v237, v67, v67\n\t" \
  "v_add_f32 v236, v236, v68\n\t" \
  "v_fmac_f32 v237, v68, v68\n\t" \
  "v_add_f32 v236, v236, v69\n\t" \
  "v_fmac_f32 v237, v69, v69\n\t" \
  "v_add_f32 v236, v236, v70\n\t" \
  "v_fmac_f32 v237, v70, v70\n\t" \
  "v_add_f32 v236, v236, v71\n\t" \
  "v_fmac_f32 v237, v71, v71\n\t" \
  "s_setprio 3\n\t" \
  "ds_write_b64 v243, v[236:237]\n\t" \
  "s_waitcnt vmcnt(3)\n\t" \
  "s_waitcnt lgkmcnt(0)\n\t" \
  "s_barrier\n\t" \
  "ds_read_b128 v[224:227], v244\n\t" \
  "ds_read_b128 v[228:231], v244 offset:1024\n\t" \
  "s_waitcnt lgkmcnt(0)\n\t" \
  "s_setprio 1\n\t" \
  "v_add_f32 v224, v224, v226\n\t" \
  "v_add_f32 v225, v225, v227\n\t" \
  "v_add_f32 v228, v228, v230\n\t" \
  "v_add_f32 v229, v229, v231\n\t" \
  "v_add_f32 v224, v224, v228\n\t" \
  "v_add_f32 v225, v225, v229\n\t" \
  "v_mul_f32 v224, 0x3c2aaaab, v224\n\t" \
  "v_mul_f32 v225, 0x3c2aaaab, v225\n\t" \
  "v_fma_f32 v225, -v224, v224, v225\n\t" \
  "v_max_f32 v225, 0, v225\n\t" \
  "v_add_f32 v225, 0x3727c5ac, v225\n\t" \
  "v_rsq_f32 v225, v225\n\t" \
  "s_nop 0\n\t" \
  "v_mul_f32 v238, %[gl], v225\n\t" \
  "v_mul_f32 v239, -v224, v238\n\t" \
  "v_fma_f32 v48, v48, v238, v239\n\t" \
  "v_fma_f32 v49, v49, v238, v239\n\t" \
  "v_fma_f32 v50, v50, v238, v239\n\t" \
  "v_fma_f32 v51, v51, v238, v239\n\t" \
  "v_cvt_pk_bf16_f32 v224, v48, v49\n\t" \
  "v_cvt_pk_bf16_f32 v225, v50, v51\n\t" \
  "v_fma_f32 v52, v52, v238, v239\n\t" \
  "v_fma_f32 v53, v53, v238, v239\n\t" \
  "v_fma_f32 v54, v54, v238, v239\n\t" \
  "v_fma_f32 v55, v55, v238, v239\n\t" \
  "v_cvt_pk_bf16_f32 v226, v52, v53\n\t" \
  "v_cvt_pk_bf16_f32 v227, v54, v55\n\t" \
  "s_setprio 3\n\t" \
  "v_smfmac_f32_16x16x64_bf16 v[72:75], v[108:111], v[192:199], v240\n\t" \
  "ds_write_b128 v242, v[224:227] offset:0\n\t" \
  "ds_read_b64 v[208:209], v241 offset:16\n\t" \
  "ds_read_b64 v[210:211], v241 offset:416\n\t" \
  "ds_read_b64 v[212:213], v241 offset:816\n\t" \
  "ds_read_b64 v[214:215], v241 offset:1216\n\t" \
  "v_fma_f32 v56, v56, v238, v239\n\t" \
  "v_fma_f32 v57, v57, v238, v239\n\t" \
  "v_smfmac_f32_16x16x64_bf16 v[76:79], v[124:127], v[200:207], v240\n\t" \
  "ds_read_b64 v[216:217], v241 offset:24\n\t" \
  "ds_read_b64 v[218:219], v241 offset:424\n\t" \
  "ds_read_b64 v[220:221], v241 offset:824\n\t" \
  "ds_read_b64 v[222:223], v241 offset:1224\n\t" \
  "v_fma_f32 v58, v58, v238, v239\n\t" \
  "v_fma_f32 v59, v59, v238, v239\n\t" \
  "s_setprio 1\n\t" \
  "s_setprio 3\n\t" \
  "s_mov_b32 m0, %[wld]\n\t" \
  "s_add_u32 %[t1], %[wso], %[wpd]\n\t" \
  "buffer_load_dwordx4 v245, %[wrs], %[wso] offen lds\n\t" \
  "buffer_load_dwordx4 v245, %[wrs], %[t1] offen offset:1024 lds\n\t" \
  "s_add_u32 %[t1], %[t1], %[wpd]\n\t" \
  "s_add_u32 %[wso], %[wso], %[wcs]\n\t" \
  "buffer_load_dwordx4 v245, %[wrs], %[t1] offen offset:2048 lds\n\t" \
  "s_add_u32 %[wld], %[wld], %[wst]\n\t" \
  "s_setprio 1\n\t" \
  "s_setprio 3\n\t" \
  "s_waitcnt vmcnt(3)\n\t" \
  "s_barrier\n\t" \
  "s_mov_b32 m0, %[wld]\n\t" \
  "s_add_u32 %[t1], %[wso], %[wpd]\n\t" \
  "buffer_load_dwordx4 v245, %[wrs], %[wso] offen lds\n\t" \
  "buffer_load_dwordx4 v245, %[wrs], %[t1] offen offset:1024 lds\n\t" \
  "s_add_u32 %[t1], %[t1], %[wpd]\n\t" \
  "s_add_u32 %[wso], %[wso], %[wcs]\n\t" \
  "buffer_load_dwordx4 v245, %[wrs], %[t1] offen offset:2048 lds\n\t" \
  "s_add_u32 %[wld], %[wld], %[wst]\n\t" \
  "s_setprio 1\n\t" \
  "v_cvt_pk_bf16_f32 v228, v56, v57\n\t" \
  "v_cvt_pk_bf16_f32 v229, v58, v59\n\t" \
  "v_fma_f32 v60, v60, v238, v239\n\t" \
  "v_fma_f32 v61, v61, v238, v239\n\t" \
  "v_fma_f32 v62, v62, v238, v239\n\t" \
  "v_fma_f32 v63, v63, v238, v239\n\t" \
  "v_cvt_pk_bf16_f32 v230, v60, v61\n\t" \
  "v_cvt_pk_bf16_f32 v231, v62, v63\n\t" \
  "v_fma_f32 v64, v64, v238, v239\n\t" \
  "v_fma_f32 v65, v65, v238, v239\n\t" \
  "v_fma_f32 v66, v66, v238, v239\n\t" \
  "v_fma_f32 v67, v67, v238, v239\n\t" \
  "v_cvt_pk_bf16_f32 v232, v64, v65\n\t" \
  "v_cvt_pk_bf16_f32 v233, v66, v67\n\t" \
  "v_fma_f32 v68, v68, v238, v239\n\t" \
  "v_fma_f32 v69, v69, v238, v239\n\t" \
  "v_fma_f32 v70, v70, v238, v239\n\t" \
  "v_fma_f32 v71, v71, v238, v239\n\t" \
  "v_cvt_pk_bf16_f32 v234, v68, v69\n\t" \
  "v_cvt_pk_bf16_f32 v235, v70, v71\n\t" \
  "v_mul_f32 v237, v72, v72\n\t" \
  "v_add_f32 v236, v72, v73\n\t" \
  "v_fmac_f32 v237, v73, v73\n\t" \
  "v_add_f32 v236, v236, v74\n\t" \
  "s_waitcnt lgkmcnt(4)\n\t" \
  "s_setprio 3\n\t" \
  "v_smfmac_f32_16x16x64_bf16 v[80:83], v[140:143], v[208:215], v240\n\t" \
  "s_waitcnt lgkmcnt(0)\n\t" \
  "ds_write_b128 v242, v[228:231] offset:1024\n\t" \
  "ds_read_b64 v[192:193], v241 offset:32\n\t" \
  "ds_read_b64 v[194:195], v241 offset:432\n\t" \
  "ds_read_b64 v[196:197], v241 offset:832\n\t" \
  "ds_read_b64 v[198:199], v241 offset:1232\n\t" \
  "v_fmac_f32 v237, v74, v74\n\t" \
  "v_add_f32 v236, v236, v75\n\t" \
  "v_smfmac_f32_16x16x64_bf16 v[84:87], v[156:159], v[216:223], v240\n\t" \
  "ds_write_b128 v242, v[232:235] offset:2048\n\t" \
  "ds_read_b64 v[200:201], v241 offset:40\n\t" \
  "ds_read_b64 v[202:203], v241 offset:440\n\t" \
  "ds_read_b64 v[204:205], v241 offset:840\n\t" \
  "ds_read_b64 v[206:207], v241 offset:1240\n\t" \
  "v_fmac_f32 v237, v75, v75\n\t" \
  "v_add_f32 v236, v236, v76\n\t" \
  "s_setprio 1\n\t" \
  "s_setprio 3\n\t" \
  "s_add_u32 %[cs], %[cs], 1\n\t" \
  "s_cmp_eq_u32 %[cs], 6\n\t" \
  "s_cselect_b32 %[cs], 0, %[cs]\n\t" \
  "s_cselect_b32 %[t0], -75920, %[ps]\n\t" \
  "v_add_u32 v241, %[t0], v241\n\t" \
  "s_waitcnt vmcnt(3)\n\t" \
  "s_waitcnt lgkmcnt(4)\n\t" \
  "s_barrier\n\t" \
  "v_smfmac_f32_16x16x64_bf16 v[88:91], v[172:175], v[192:199], v240\n\t" \
  "s_waitcnt lgkmcnt(0)\n\t" \
  "v_fmac_f32 v237, v76, v76\n\t" \
  "v_add_f32 v236, v236, v77\n\t" \
  "v_smfmac_f32_16x16x64_bf16 v[92:95], v[188:191], v[200:207], v240\n\t" \
  "v_fmac_f32 v237, v77, v77\n\t" \
  "v_add_f32 v236, v236, v78\n\t" \
  "s_setprio 1\n\t" \
  "s_setprio 3\n\t" \
  "s_mov_b32 m0, %[wld]\n\t" \
  "s_add_u32 %[t1], %[wso], %[wpd]\n\t" \
  "buffer_load_dwordx4 v245, %[wrs], %[wso] offen lds\n\t" \
  "buffer_load_dwordx4 v245, %[wrs], %[t1] offen offset:1024 lds\n\t" \
  "s_add_u32 %[t1], %[t1], %[wpd]\n\t" \
  "s_add_u32 %[wso], %[wso], %[wcs]\n\t" \
  "buffer_load_dwordx4 v245, %[wrs], %[t1] offen offset:2048 lds\n\t" \
  "s_add_u32 %[wld], %[wld], %[wlc]\n\t" \
  "s_setprio 1\n\t" \
  "v_fmac_f32 v237, v78, v78\n\t" \
  "v_add_f32 v236, v236, v79\n\t" \
  "v_fmac_f32 v237, v79, v79\n\t" \
  "v_add_f32 v236, v236, v80\n\t" \
  "v_fmac_f32 v237, v80, v80\n\t" \
  "v_add_f32 v236, v236, v81\n\t" \
  "v_fmac_f32 v237, v81, v81\n\t" \
  "v_add_f32 v236, v236, v82\n\t" \
  "v_fmac_f32 v237, v82, v82\n\t" \
  "v_add_f32 v236, v236, v83\n\t" \
  "v_fmac_f32 v237, v83, v83\n\t" \
  "v_add_f32 v236, v236, v84\n\t" \
  "v_fmac_f32 v237, v84, v84\n\t" \
  "v_add_f32 v236, v236, v85\n\t" \
  "v_fmac_f32 v237, v85, v85\n\t" \
  "v_add_f32 v236, v236, v86\n\t" \
  "v_fmac_f32 v237, v86, v86\n\t" \
  "v_add_f32 v236, v236, v87\n\t" \
  "v_fmac_f32 v237, v87, v87\n\t" \
  "v_add_f32 v236, v236, v88\n\t" \
  "v_fmac_f32 v237, v88, v88\n\t" \
  "v_add_f32 v236, v236, v89\n\t" \
  "v_fmac_f32 v237, v89, v89\n\t" \
  "v_add_f32 v236, v236, v90\n\t" \
  "v_fmac_f32 v237, v90, v90\n\t" \
  "v_add_f32 v236, v236, v91\n\t" \
  "v_fmac_f32 v237, v91, v91\n\t" \
  "v_add_f32 v236, v236, v92\n\t" \
  "v_fmac_f32 v237, v92, v92\n\t" \
  "v_add_f32 v236, v236, v93\n\t" \
  "v_fmac_f32 v237, v93, v93\n\t" \
  "v_add_f32 v236, v236, v94\n\t" \
  "v_fmac_f32 v237, v94, v94\n\t" \
  "v_add_f32 v236, v236, v95\n\t" \
  "v_fmac_f32 v237, v95, v95\n\t" \
  "s_setprio 3\n\t" \
  "ds_write_b64 v243, v[236:237]\n\t" \
  "s_waitcnt vmcnt(3)\n\t" \
  "s_waitcnt lgkmcnt(0)\n\t" \
  "s_barrier\n\t" \
  "ds_read_b128 v[224:227], v244\n\t" \
  "ds_read_b128 v[228:231], v244 offset:1024\n\t" \
  "s_waitcnt lgkmcnt(0)\n\t" \
  "s_setprio 1\n\t" \
  "v_add_f32 v224, v224, v226\n\t" \
  "v_add_f32 v225, v225, v227\n\t" \
  "v_add_f32 v228, v228, v230\n\t" \
  "v_add_f32 v229, v229, v231\n\t" \
  "v_add_f32 v224, v224, v228\n\t" \
  "v_add_f32 v225, v225, v229\n\t" \
  "v_mul_f32 v224, 0x3c2aaaab, v224\n\t" \
  "v_mul_f32 v225, 0x3c2aaaab, v225\n\t" \
  "v_fma_f32 v225, -v224, v224, v225\n\t" \
  "v_max_f32 v225, 0, v225\n\t" \
  "v_add_f32 v225, 0x3727c5ac, v225\n\t" \
  "v_rsq_f32 v225, v225\n\t" \
  "s_nop 0\n\t" \
  "v_mul_f32 v238, %[gl], v225\n\t" \
  "v_mul_f32 v239, -v224, v238\n\t" \
  "v_fma_f32 v72, v72, v238, v239\n\t" \
  "v_fma_f32 v73, v73, v238, v239\n\t" \
  "v_fma_f32 v74, v74, v238, v239\n\t" \
  "v_fma_f32 v75, v75, v238, v239\n\t" \
  "v_cvt_pk_bf16_f32 v224, v72, v73\n\t" \
  "v_cvt_pk_bf16_f32 v225, v74, v75\n\t" \
  "v_fma_f32 v76, v76, v238, v239\n\t" \
  "v_fma_f32 v77, v77, v238, v239\n\t" \
  "v_fma_f32 v78, v78, v238, v239\n\t" \
  "v_fma_f32 v79, v79, v238, v239\n\t" \
  "v_cvt_pk_bf16_f32 v226, v76, v77\n\t" \
  "v_cvt_pk_bf16_f32 v227, v78, v79\n\t" \
  "ds_write_b128 v242, v[224:227] offset:0\n\t" \
  "s_setprio 3\n\t" \
  "s_mov_b32 m0, %[wld]\n\t" \
  "s_add_u32 %[t1], %[wso], %[wpd]\n\t" \
  "buffer_load_dwordx4 v245, %[wrs], %[wso] offen lds\n\t" \
  "buffer_load_dwordx4 v245, %[wrs], %[t1] offen offset:1024 lds\n\t" \
  "s_add_u32 %[t1], %[t1], %[wpd]\n\t" \
  "s_add_u32 %[wso], %[wso], %[wcs]\n\t" \
  "buffer_load_dwordx4 v245, %[wrs], %[t1] offen offset:2048 lds\n\t" \
  "s_add_u32 %[wld], %[wld], %[wst]\n\t" \
  "s_setprio 1\n\t" \
  "s_setprio 3\n\t" \
  "s_waitcnt vmcnt(3)\n\t" \
  "s_barrier\n\t" \
  "s_mov_b32 m0, %[wld]\n\t" \
  "s_add_u32 %[t1], %[wso], %[wpd]\n\t" \
  "buffer_load_dwordx4 v245, %[wrs], %[wso] offen lds\n\t" \
  "buffer_load_dwordx4 v245, %[wrs], %[t1] offen offset:1024 lds\n\t" \
  "s_add_u32 %[t1], %[t1], %[wpd]\n\t" \
  "s_add_u32 %[wso], %[wso], %[wcs]\n\t" \
  "buffer_load_dwordx4 v245, %[wrs], %[t1] offen offset:2048 lds\n\t" \
  "s_add_u32 %[wld], %[wld], %[wst]\n\t" \
  "s_add_u32 m0, %[el], 0\n\t" \
  "s_add_u32 %[t1], %[es], %[wpd]\n\t" \
  "buffer_load_dwordx4 v245, %[wrs], %[es] offen lds\n\t" \
  "buffer_load_dwordx4 v245, %[wrs], %[t1] offen offset:1024 lds\n\t" \
  "s_add_u32 %[t1], %[t1], %[wpd]\n\t" \
  "s_add_u32 %[es], %[es], %[wcs]\n\t" \
  "buffer_load_dwordx4 v245, %[wrs], %[t1] offen offset:2048 lds\n\t" \
  "s_add_u32 m0, %[el], 12320\n\t" \
  "s_add_u32 %[t1], %[es], %[wpd]\n\t" \
  "buffer_load_dwordx4 v245, %[wrs], %[es] offen lds\n\t" \
  "buffer_load_dwordx4 v245, %[wrs], %[t1] offen offset:1024 lds\n\t" \
  "s_add_u32 %[t1], %[t1], %[wpd]\n\t" \
  "s_add_u32 %[es], %[es], %[wcs]\n\t" \
  "buffer_load_dwordx4 v245, %[wrs], %[t1] offen offset:2048 lds\n\t" \
  "s_setprio 1\n\t" \
  "v_fma_f32 v80, v80, v238, v239\n\t" \
  "v_fma_f32 v81, v81, v238, v239\n\t" \
  "v_fma_f32 v82, v82, v238, v239\n\t" \
  "v_fma_f32 v83, v83, v238, v239\n\t" \
  "v_cvt_pk_bf16_f32 v228, v80, v81\n\t" \
  "v_cvt_pk_bf16_f32 v229, v82, v83\n\t" \
  "v_fma_f32 v84, v84, v238, v239\n\t" \
  "v_fma_f32 v85, v85, v238, v239\n\t" \
  "v_fma_f32 v86, v86, v238, v239\n\t" \
  "v_fma_f32 v87, v87, v238, v239\n\t" \
  "v_cvt_pk_bf16_f32 v230, v84, v85\n\t" \
  "v_cvt_pk_bf16_f32 v231, v86, v87\n\t" \
  "v_fma_f32 v88, v88, v238, v239\n\t" \
  "v_fma_f32 v89, v89, v238, v239\n\t" \
  "v_fma_f32 v90, v90, v238, v239\n\t" \
  "v_fma_f32 v91, v91, v238, v239\n\t" \
  "v_cvt_pk_bf16_f32 v232, v88, v89\n\t" \
  "v_cvt_pk_bf16_f32 v233, v90, v91\n\t" \
  "v_fma_f32 v92, v92, v238, v239\n\t" \
  "v_fma_f32 v93, v93, v238, v239\n\t" \
  "v_fma_f32 v94, v94, v238, v239\n\t" \
  "v_fma_f32 v95, v95, v238, v239\n\t" \
  "v_cvt_pk_bf16_f32 v234, v92, v93\n\t" \
  "v_cvt_pk_bf16_f32 v235, v94, v95\n\t" \
  "ds_write_b128 v242, v[228:231] offset:1024\n\t" \
  "ds_write_b128 v242, v[232:235] offset:2048\n\t" \
  "s_setprio 3\n\t" \
  "s_add_u32 %[cs], %[cs], 1\n\t" \
  "s_cmp_eq_u32 %[cs], 6\n\t" \
  "s_cselect_b32 %[cs], 0, %[cs]\n\t" \
  "s_cselect_b32 %[t0], -75920, %[ps]\n\t" \
  "v_add_u32 v241, %[t0], v241\n\t" \
  "s_waitcnt vmcnt(9)\n\t" \
  "s_waitcnt lgkmcnt(0)\n\t" \
  "s_barrier\n\t" \
  "s_mov_b32 m0, %[wld]\n\t" \
  "s_add_u32 %[t1], %[wso], %[wpd]\n\t" \
  "buffer_load_dwordx4 v245, %[wrs], %[wso] offen lds\n\t" \
  "buffer_load_dwordx4 v245, %[wrs], %[t1] offen offset:1024 lds\n\t" \
  "s_add_u32 %[t1], %[t1], %[wpd]\n\t" \
  "s_add_u32 %[wso], %[wso], %[wsa]\n\t" \
  "buffer_load_dwordx4 v245, %[wrs], %[t1] offen offset:2048 lds\n\t" \
  "s_add_u32 %[wld], %[wld], %[wla]\n\t" \
  "s_add_u32 m0, %[el], 24640\n\t" \
  "s_add_u32 %[t1], %[es], %[wpd]\n\t" \
  "buffer_load_dwordx4 v245, %[wrs], %[es] offen lds\n\t" \
  "buffer_load_dwordx4 v245, %[wrs], %[t1] offen offset:1024 lds\n\t" \
  "s_add_u32 %[t1], %[t1], %[wpd]\n\t" \
  "s_add_u32 %[es], %[es], %[wcs]\n\t" \
  "buffer_load_dwordx4 v245, %[wrs], %[t1] offen offset:2048 lds\n\t" \
  "s_add_u32 m0, %[el], 36960\n\t" \
  "s_add_u32 %[t1], %[es], %[wpd]\n\t" \
  "buffer_load_dwordx4 v245, %[wrs], %[es] offen lds\n\t" \
  "buffer_load_dwordx4 v245, %[wrs], %[t1] offen offset:1024 lds\n\t" \
  "s_add_u32 %[t1], %[t1], %[wpd]\n\t" \
  "s_add_u32 %[es], %[es], %[wcs]\n\t" \
  "buffer_load_dwordx4 v245, %[wrs], %[t1] offen offset:2048 lds\n\t" \
  "s_waitcnt vmcnt(15)\n\t" \
  "s_barrier\n\t" \
  "s_sleep 10\n\t" \
  "s_mov_b32 m0, %[wld]\n\t" \
  "s_add_u32 %[t1], %[wso], %[wpd]\n\t" \
  "buffer_load_dwordx4 v245, %[wrs], %[wso] offen lds\n\t" \
  "buffer_load_dwordx4 v245, %[wrs], %[t1] offen offset:1024 lds\n\t" \
  "s_add_u32 %[t1], %[t1], %[wpd]\n\t" \
  "s_add_u32 %[wso], %[wso], %[wsb]\n\t" \
  "buffer_load_dwordx4 v245, %[wrs], %[t1] offen offset:2048 lds\n\t" \
  "s_add_u32 %[wld], %[wld], %[wlb]\n\t" \
  "s_waitcnt vmcnt(9)\n\t" \
  "s_barrier\n\t" \
  "s_mov_b32 m0, %[wld]\n\t" \
  "s_add_u32 %[t1], %[wso], %[wpd]\n\t" \
  "buffer_load_dwordx4 v245, %[wrs], %[wso] offen lds\n\t" \
  "buffer_load_dwordx4 v245, %[wrs], %[t1] offen offset:1024 lds\n\t" \
  "s_add_u32 %[t1], %[t1], %[wpd]\n\t" \
  "s_add_u32 %[wso], %[wso], %[wcs]\n\t" \
  "buffer_load_dwordx4 v245, %[wrs], %[t1] offen offset:2048 lds\n\t" \
  "s_add_u32 %[wld], %[wld], %[wst]\n\t" \
  "s_add_u32 m0, %[el], 49280\n\t" \
  "s_add_u32 %[t1], %[es], %[wpd]\n\t" \
  "buffer_load_dwordx4 v245, %[wrs], %[es] offen lds\n\t" \
  "buffer_load_dwordx4 v245, %[wrs], %[t1] offen offset:1024 lds\n\t" \
  "s_add_u32 %[t1], %[t1], %[wpd]\n\t" \
  "s_add_u32 %[es], %[es], %[wcs]\n\t" \
  "buffer_load_dwordx4 v245, %[wrs], %[t1] offen offset:2048 lds\n\t" \
  "s_add_u32 m0, %[el], 61600\n\t" \
  "s_add_u32 %[t1], %[es], %[wpd]\n\t" \
  "buffer_load_dwordx4 v245, %[wrs], %[es] offen lds\n\t" \
  "buffer_load_dwordx4 v245, %[wrs], %[t1] offen offset:1024 lds\n\t" \
  "s_add_u32 %[t1], %[t1], %[wpd]\n\t" \
  "s_add_u32 %[es], %[es], %[wcs]\n\t" \
  "buffer_load_dwordx4 v245, %[wrs], %[t1] offen offset:2048 lds\n\t" \
  "s_waitcnt vmcnt(9)\n\t" \
  "s_barrier\n\t" \
  "s_mov_b32 m0, %[wld]\n\t" \
  "s_add_u32 %[t1], %[wso], %[wpd]\n\t" \
  "buffer_load_dwordx4 v245, %[wrs], %[wso] offen lds\n\t" \
  "buffer_load_dwordx4 v245, %[wrs], %[t1] offen offset:1024 lds\n\t" \
  "s_add_u32 %[t1], %[t1], %[wpd]\n\t" \
  "s_add_u32 %[wso], %[wso], %[wcs]\n\t" \
  "buffer_load_dwordx4 v245, %[wrs], %[t1] offen offset:2048 lds\n\t" \
  "s_add_u32 %[wld], %[wld], %[wla]\n\t" \
  "s_add_u32 m0, %[el], 73920\n\t" \
  "s_add_u32 %[t1], %[es], %[wpd]\n\t" \
  "buffer_load_dwordx4 v245, %[wrs], %[es] offen lds\n\t" \
  "buffer_load_dwordx4 v245, %[wrs], %[t1] offen offset:1024 lds\n\t" \
  "s_add_u32 %[t1], %[t1], %[wpd]\n\t" \
  "s_add_u32 %[es], %[es], %[wcs]\n\t" \
  "buffer_load_dwordx4 v245, %[wrs], %[t1] offen offset:2048 lds\n\t" \
  "s_waitcnt vmcnt(12)\n\t" \
  "s_barrier\n\t" \
  "s_sleep 10\n\t" \
  "s_waitcnt vmcnt(0)\n\t" \
  "s_barrier\n\t" \
  ""
#define MS_ASM0 \
  "s_mov_b32 m0, %[xld]\n\t" \
  "s_cmp_lt_u32 %[gy], 0x3000000\n\t" \
  "s_cselect_b32 %[xso], %[gy], 0x40000000\n\t" \
  "buffer_load_dwordx4 v246, %[xrs], %[xso] offen offset:0 nt lds\n\t" \
  "s_add_u32 %[gy], %[gy], %[dgy]\n\t" \
  "buffer_load_dwordx4 v247, %[xrs], %[xso] offen offset:1024 nt lds\n\t" \
  "s_add_u32 %[xld], %[xld], 15184\n\t" \
  "s_cmp_eq_u32 %[xld], %[xle]\n\t" \
  "s_cselect_b32 %[xld], %[xlb], %[xld]\n\t" \
  "buffer_load_dwordx4 v248, %[xrs], %[xso] offen offset:2048 nt lds\n\t" \
  "s_mov_b64 exec, %[xm]\n\t" \
  "buffer_load_dwordx4 v249, %[xrs], %[xso] offen offset:3072 nt lds\n\t" \
  "s_mov_b64 exec, -1\n\t" \
  "s_mov_b32 m0, %[xld]\n\t" \
  "s_cmp_lt_u32 %[gy], 0x3000000\n\t" \
  "s_cselect_b32 %[xso], %[gy], 0x40000000\n\t" \
  "buffer_load_dwordx4 v246, %[xrs], %[xso] offen offset:0 nt lds\n\t" \
  "s_add_u32 %[gy], %[gy], %[dgy]\n\t" \
  "buffer_load_dwordx4 v247, %[xrs], %[xso] offen offset:1024 nt lds\n\t" \
  "s_add_u32 %[xld], %[xld], 15184\n\t" \
  "s_cmp_eq_u32 %[xld], %[xle]\n\t" \
  "s_cselect_b32 %[xld], %[xlb], %[xld]\n\t" \
  "buffer_load_dwordx4 v248, %[xrs], %[xso] offen offset:2048 nt lds\n\t" \
  "s_mov_b64 exec, %[xm]\n\t" \
  "buffer_load_dwordx4 v249, %[xrs], %[xso] offen offset:3072 nt lds\n\t" \
  "s_mov_b64 exec, -1\n\t" \
  ""
#define MS_ASM1 \
  "v_mov_b32 v72, 0xbdd2d3e8\n\t" \
  "v_add_u32 v75, 0x10000, v94\n\t" \
  "v_mul_f32 v74, %[kl], v95\n\t" \
  "v_mul_f32 v75, %[kl], v75\n\t" \
  "v_mul_f32 v76, v74, v74\n\t" \
  "v_mul_f32 v77, v75, v75\n\t" \
  "v_fmaak_f32 v76, v76, v72, 0xc0135761\n\t" \
  "v_fmaak_f32 v77, v77, v72, 0xc0135761\n\t" \
  "v_mul_f32 v76, v74, v76\n\t" \
  "v_mul_f32 v77, v75, v77\n\t" \
  "v_exp_f32 v78, v76\n\t" \
  "v_exp_f32 v79, v77\n\t" \
  "v_add_f32 v76, 1.0, v78\n\t" \
  "v_add_f32 v77, 1.0, v79\n\t" \
  "v_rcp_f32 v76, v76\n\t" \
  "v_rcp_f32 v77, v77\n\t" \
  "v_mul_f32 v74, v74, v76\n\t" \
  "v_mul_f32 v75, v75, v77\n\t" \
  "v_mul_f32 v78, -v74, v78\n\t" \
  "v_mul_f32 v79, -v75, v79\n\t" \
  "v_cvt_pk_bf16_f32 v83, v78, v79\n\t" \
  "v_cvt_pk_bf16_f32 v82, v74, v75\n\t" \
  "v_add_u32 v94, 0x2000000, v94\n\t" \
  "ds_write_b32 v70, v82 offset:0\n\t" \
  "ds_write_b32 v71, v83 offset:0\n\t" \
  "s_barrier\n\t" \
  "s_mov_b32 m0, %[xld]\n\t" \
  "s_cmp_lt_u32 %[gy], 0x3000000\n\t" \
  "s_cselect_b32 %[xso], %[gy], 0x40000000\n\t" \
  "buffer_load_dwordx4 v246, %[xrs], %[xso] offen offset:0 nt lds\n\t" \
  "s_add_u32 %[gy], %[gy], %[dgy]\n\t" \
  "buffer_load_dwordx4 v247, %[xrs], %[xso] offen offset:1024 nt lds\n\t" \
  "s_add_u32 %[xld], %[xld], 15184\n\t" \
  "s_cmp_eq_u32 %[xld], %[xle]\n\t" \
  "s_cselect_b32 %[xld], %[xlb], %[xld]\n\t" \
  "buffer_load_dwordx4 v248, %[xrs], %[xso] offen offset:2048 nt lds\n\t" \
  "s_mov_b64 exec, %[xm]\n\t" \
  "buffer_load_dwordx4 v249, %[xrs], %[xso] offen offset:3072 nt lds\n\t" \
  "s_mov_b64 exec, -1\n\t" \
  "s_waitcnt vmcnt(8)\n\t" \
  "s_barrier\n\t" \
  "s_mov_b32 m0, %[xld]\n\t" \
  "s_cmp_lt_u32 %[gy], 0x3000000\n\t" \
  "s_cselect_b32 %[xso], %[gy], 0x40000000\n\t" \
  "buffer_load_dwordx4 v246, %[xrs], %[xso] offen offset:0 nt lds\n\t" \
  "s_add_u32 %[gy], %[gy], %[dgy]\n\t" \
  "buffer_load_dwordx4 v247, %[xrs], %[xso] offen offset:1024 nt lds\n\t" \
  "s_add_u32 %[xld], %[xld], 15184\n\t" \
  "s_cmp_eq_u32 %[xld], %[xle]\n\t" \
  "s_cselect_b32 %[xld], %[xlb], %[xld]\n\t" \
  "buffer_load_dwordx4 v248, %[xrs], %[xso] offen offset:2048 nt lds\n\t" \
  "s_mov_b64 exec, %[xm]\n\t" \
  "buffer_load_dwordx4 v249, %[xrs], %[xso] offen offset:3072 nt lds\n\t" \
  "s_mov_b64 exec, -1\n\t" \
  "s_waitcnt vmcnt(8)\n\t" \
  "s_barrier\n\t" \
  "s_mov_b32 m0, %[wld]\n\t" \
  "s_mov_b32 %[xso], %[wso]\n\t" \
  "s_add_u32 %[t0], %[xso], 0x2c00\n\t" \
  "s_add_u32 %[t1], %[t0], 0x2c00\n\t" \
  "buffer_load_dwordx4 v245, %[wrs], %[xso] offen lds\n\t" \
  "buffer_load_dwordx4 v245, %[wrs], %[t0] offen offset:1024 lds\n\t" \
  "buffer_load_dwordx4 v245, %[wrs], %[t1] offen offset:2048 lds\n\t" \
  "v_add_u32 v85, 0x10000, v94\n\t" \
  "v_mul_f32 v84, %[kl], v94\n\t" \
  "v_mul_f32 v85, %[kl], v85\n\t" \
  "v_mul_f32 v86, v84, v84\n\t" \
  "v_mul_f32 v87, v85, v85\n\t" \
  "v_fmaak_f32 v86, v86, v72, 0xc0135761\n\t" \
  "v_fmaak_f32 v87, v87, v72, 0xc0135761\n\t" \
  "v_mul_f32 v86, v84, v86\n\t" \
  "v_mul_f32 v87, v85, v87\n\t" \
  "v_exp_f32 v88, v86\n\t" \
  "v_exp_f32 v89, v87\n\t" \
  "v_add_f32 v86, 1.0, v88\n\t" \
  "v_add_f32 v87, 1.0, v89\n\t" \
  "v_rcp_f32 v86, v86\n\t" \
  "v_rcp_f32 v87, v87\n\t" \
  "v_mul_f32 v84, v84, v86\n\t" \
  "v_mul_f32 v85, v85, v87\n\t" \
  "v_mul_f32 v88, -v84, v88\n\t" \
  "v_mul_f32 v89, -v85, v89\n\t" \
  "v_cvt_pk_bf16_f32 v93, v88, v89\n\t" \
  "v_cvt_pk_bf16_f32 v92, v84, v85\n\t" \
  "v_add_u32 v94, 0x2000000, v94\n\t" \
  "ds_write_b32 v70, v92 offset:1024\n\t" \
  "ds_write_b32 v71, v93 offset:1024\n\t" \
  "s_waitcnt vmcnt(7)\n\t" \
  "s_barrier\n\t" \
  "s_mov_b32 m0, %[xld]\n\t" \
  "s_cmp_lt_u32 %[gy], 0x3000000\n\t" \
  "s_cselect_b32 %[xso], %[gy], 0x40000000\n\t" \
  "buffer_load_dwordx4 v246, %[xrs], %[xso] offen offset:0 nt lds\n\t" \
  "s_add_u32 %[gy], %[gy], %[dgy]\n\t" \
  "buffer_load_dwordx4 v247, %[xrs], %[xso] offen offset:1024 nt lds\n\t" \
  "s_add_u32 %[xld], %[xld], 15184\n\t" \
  "s_cmp_eq_u32 %[xld], %[xle]\n\t" \
  "s_cselect_b32 %[xld], %[xlb], %[xld]\n\t" \
  "buffer_load_dwordx4 v248, %[xrs], %[xso] offen offset:2048 nt lds\n\t" \
  "s_mov_b64 exec, %[xm]\n\t" \
  "buffer_load_dwordx4 v249, %[xrs], %[xso] offen offset:3072 nt lds\n\t" \
  "s_mov_b64 exec, -1\n\t" \
  "v_add_u32 v75, 0x10000, v94\n\t" \
  "v_mul_f32 v74, %[kl], v94\n\t" \
  "v_mul_f32 v75, %[kl], v75\n\t" \
  "v_mul_f32 v76, v74, v74\n\t" \
  "v_mul_f32 v77, v75, v75\n\t" \
  "v_fmaak_f32 v76, v76, v72, 0xc0135761\n\t" \
  "v_fmaak_f32 v77, v77, v72, 0xc0135761\n\t" \
  "v_mul_f32 v76, v74, v76\n\t" \
  "v_mul_f32 v77, v75, v77\n\t" \
  "v_exp_f32 v78, v76\n\t" \
  "v_exp_f32 v79, v77\n\t" \
  "v_add_f32 v76, 1.0, v78\n\t" \
  "v_add_f32 v77, 1.0, v79\n\t" \
  "v_rcp_f32 v76, v76\n\t" \
  "v_rcp_f32 v77, v77\n\t" \
  "v_mul_f32 v74, v74, v76\n\t" \
  "v_mul_f32 v75, v75, v77\n\t" \
  "v_mul_f32 v78, -v74, v78\n\t" \
  "v_mul_f32 v79, -v75, v79\n\t" \
  "v_cvt_pk_bf16_f32 v83, v78, v79\n\t" \
  "v_cvt_pk_bf16_f32 v82, v74, v75\n\t" \
  "v_add_u32 v94, 0x2000000, v94\n\t" \
  "ds_write_b32 v70, v82 offset:2048\n\t" \
  "ds_write_b32 v71, v83 offset:2048\n\t" \
  "s_waitcnt vmcnt(7)\n\t" \
  "s_barrier\n\t" \
  "s_mov_b32 m0, %[xld]\n\t" \
  "s_cmp_lt_u32 %[gy], 0x3000000\n\t" \
  "s_cselect_b32 %[xso], %[gy], 0x40000000\n\t" \
  "buffer_load_dwordx4 v246, %[xrs], %[xso] offen offset:0 nt lds\n\t" \
  "s_add_u32 %[gy], %[gy], %[dgy]\n\t" \
  "buffer_load_dwordx4 v247, %[xrs], %[xso] offen offset:1024 nt lds\n\t" \
  "s_add_u32 %[xld], %[xld], 15184\n\t" \
  "s_cmp_eq_u32 %[xld], %[xle]\n\t" \
  "s_cselect_b32 %[xld], %[xlb], %[xld]\n\t" \
  "buffer_load_dwordx4 v248, %[xrs], %[xso] offen offset:2048 nt lds\n\t" \
  "s_mov_b64 exec, %[xm]\n\t" \
  "buffer_load_dwordx4 v249, %[xrs], %[xso] offen offset:3072 nt lds\n\t" \
  "s_mov_b64 exec, -1\n\t" \
  "s_mov_b32 m0, %[w2d]\n\t" \
  "s_nop 0\n\t" \
  "buffer_load_dwordx4 v244, %[w2r], %[w2s] offen offset:0 lds\n\t" \
  "buffer_load_dwordx4 v244, %[w2r], %[w2s] offen offset:1024 lds\n\t" \
  "buffer_load_dwordx4 v244, %[w2r], %[w2s] offen offset:2048 lds\n\t" \
  "v_add_u32 v85, 0x10000, v94\n\t" \
  "v_mul_f32 v84, %[kl], v94\n\t" \
  "v_mul_f32 v85, %[kl], v85\n\t" \
  "v_mul_f32 v86, v84, v84\n\t" \
  "v_mul_f32 v87, v85, v85\n\t" \
  "v_fmaak_f32 v86, v86, v72, 0xc0135761\n\t" \
  "v_fmaak_f32 v87, v87, v72, 0xc0135761\n\t" \
  "v_mul_f32 v86, v84, v86\n\t" \
  "v_mul_f32 v87, v85, v87\n\t" \
  "v_exp_f32 v88, v86\n\t" \
  "v_exp_f32 v89, v87\n\t" \
  "v_add_f32 v86, 1.0, v88\n\t" \
  "v_add_f32 v87, 1.0, v89\n\t" \
  "v_rcp_f32 v86, v86\n\t" \
  "v_rcp_f32 v87, v87\n\t" \
  "v_mul_f32 v84, v84, v86\n\t" \
  "v_mul_f32 v85, v85, v87\n\t" \
  "v_mul_f32 v88, -v84, v88\n\t" \
  "v_mul_f32 v89, -v85, v89\n\t" \
  "v_cvt_pk_bf16_f32 v93, v88, v89\n\t" \
  "v_cvt_pk_bf16_f32 v92, v84, v85\n\t" \
  "v_add_u32 v94, 0x2000000, v94\n\t" \
  "ds_write_b32 v70, v92 offset:3072\n\t" \
  "ds_write_b32 v71, v93 offset:3072\n\t" \
  "v_add_u32 v75, 0x10000, v94\n\t" \
  "v_mul_f32 v74, %[kl], v94\n\t" \
  "v_mul_f32 v75, %[kl], v75\n\t" \
  "v_mov_b32 v83, 0x80008000\n\t" \
  "v_cvt_pk_bf16_f32 v82, v74, v75\n\t" \
  "v_add_u32 v94, 0x2000000, v94\n\t" \
  "ds_write_b32 v70, v82 offset:4096\n\t" \
  "ds_write_b32 v71, v83 offset:4096\n\t" \
  "v_add_u32 v85, 0x10000, v94\n\t" \
  "v_mul_f32 v84, %[kl], v94\n\t" \
  "v_mul_f32 v85, %[kl], v85\n\t" \
  "v_mov_b32 v93, 0x80008000\n\t" \
  "v_cvt_pk_bf16_f32 v92, v84, v85\n\t" \
  "ds_write_b32 v70, v92 offset:5120\n\t" \
  "ds_write_b32 v71, v93 offset:5120\n\t" \
  "s_waitcnt vmcnt(7)\n\t" \
  "s_barrier\n\t" \
  "s_mov_b32 m0, %[wlb]\n\t" \
  "s_mov_b32 %[xso], %[wsb]\n\t" \
  "s_add_u32 %[t0], %[xso], 0x2c00\n\t" \
  "s_add_u32 %[t1], %[t0], 0x2c00\n\t" \
  "buffer_load_dwordx4 v245, %[wrs], %[xso] offen lds\n\t" \
  "buffer_load_dwordx4 v245, %[wrs], %[t0] offen offset:1024 lds\n\t" \
  "buffer_load_dwordx4 v245, %[wrs], %[t1] offen offset:2048 lds\n\t" \
  "s_mov_b32 m0, %[xld]\n\t" \
  "s_cmp_lt_u32 %[gy], 0x3000000\n\t" \
  "s_cselect_b32 %[xso], %[gy], 0x40000000\n\t" \
  "buffer_load_dwordx4 v246, %[xrs], %[xso] offen offset:0 nt lds\n\t" \
  "s_add_u32 %[gy], %[gy], %[dgy]\n\t" \
  "buffer_load_dwordx4 v247, %[xrs], %[xso] offen offset:1024 nt lds\n\t" \
  "s_add_u32 %[xld], %[xld], 15184\n\t" \
  "s_cmp_eq_u32 %[xld], %[xle]\n\t" \
  "s_cselect_b32 %[xld], %[xlb], %[xld]\n\t" \
  "buffer_load_dwordx4 v248, %[xrs], %[xso] offen offset:2048 nt lds\n\t" \
  "s_mov_b64 exec, %[xm]\n\t" \
  "buffer_load_dwordx4 v249, %[xrs], %[xso] offen offset:3072 nt lds\n\t" \
  "s_mov_b64 exec, -1\n\t" \
  "s_waitcnt lgkmcnt(0)\n\t" \
  ""
#define M_CLOB "v0", "v1", "v2", "v3", "v4", "v5", "v6", "v7", "v8", "v9", "v10", "v11", "v12", "v13", "v14", "v15", "v16", "v17", "v18", "v19", "v20", "v21", "v22", "v23", "v24", "v25", "v26", "v27", "v28", "v29", "v30", "v31", "v32", "v33", "v34", "v35", "v36", "v37", "v38", "v39", "v40", "v41", "v42", "v43", "v44", "v45", "v46", "v47", "v48", "v49", "v50", "v51", "v52", "v53", "v54", "v55", "v56", "v57", "v58", "v59", "v60", "v61", "v62", "v63", "v64", "v65", "v66", "v67", "v68", "v69", "v70", "v71", "v72", "v73", "v74", "v75", "v76", "v77", "v78", "v79", "v80", "v81", "v82", "v83", "v84", "v85", "v86", "v87", "v88", "v89", "v90", "v91", "v92", "v93", "v94", "v95", "v96", "v97", "v98", "v99", "v100", "v101", "v102", "v103", "v104", "v105", "v106", "v107", "v108", "v109", "v110", "v111", "v112", "v113", "v114", "v115", "v116", "v117", "v118", "v119", "v120", "v121", "v122", "v123", "v124", "v125", "v126", "v127", "v128", "v129", "v130", "v131", "v132", "v133", "v134", "v135", "v136", "v137", "v138", "v139", "v140", "v141", "v142", "v143", "v144", "v145", "v146", "v147", "v148", "v149", "v150", "v151", "v152", "v153", "v154", "v155", "v156", "v157", "v158", "v159", "v160", "v161", "v162", "v163", "v164", "v165", "v166", "v167", "v168", "v169", "v170", "v171", "v172", "v173", "v174", "v175", "v176", "v177", "v178", "v179", "v180", "v181", "v182", "v183", "v184", "v185", "v186", "v187", "v188", "v189", "v190", "v191", "v192", "v193", "v194", "v195", "v196", "v197", "v198", "v199", "v200", "v201", "v202", "v203", "v204", "v205", "v206", "v207", "v208", "v209", "v210", "v211", "v212", "v213", "v214", "v215", "v216", "v217", "v218", "v219", "v220", "v221", "v222", "v223", "v224", "v225", "v226", "v227", "v228", "v229", "v230", "v231", "v232", "v233", "v234", "v235", "v236", "v237", "v238", "v239", "v240", "v241", "v242", "v243", "v244", "v245"
#define LUT_CLOB "v72", "v73", "v74", "v75", "v76", "v77", "v78", "v79", "v80", "v81", "v82", "v83", "v84", "v85", "v86", "v87", "v88", "v89", "v90", "v91", "v92", "v93"
#define F_CLOB "v0", "v1", "v2", "v3", "v4", "v5", "v6", "v7", "v8", "v9", "v10", "v11", "v12", "v13", "v14", "v15", "v16", "v17", "v18", "v19", "v20", "v21", "v22", "v23", "v24", "v25", "v26", "v27", "v28", "v29", "v30", "v31", "v32", "v33", "v34", "v35", "v36", "v37", "v38", "v39", "v40", "v41", "v42", "v43", "v44", "v45", "v46", "v47", "v48", "v49", "v50", "v51", "v52", "v53", "v54", "v55", "v56", "v57", "v58", "v59", "v60", "v61", "v62", "v63", "v64", "v65", "v66", "v67", "v68", "v69", "v70", "v71", "v72", "v73", "v74", "v75", "v76", "v77", "v78", "v79", "v80", "v81", "v82", "v83", "v84", "v85", "v86", "v87", "v88", "v89", "v90", "v91", "v92", "v93", "v94", "v95", "v96", "v97", "v98", "v99", "v100", "v101", "v102", "v103", "v104", "v105", "v106", "v107", "v108", "v109", "v110", "v111", "v112", "v113", "v114", "v115", "v116", "v117", "v118", "v119", "v120", "v121", "v122", "v123", "v124", "v125", "v126", "v127", "v128", "v129", "v130", "v131", "v132", "v133", "v134", "v135", "v136", "v137", "v138", "v139", "v140", "v141", "v142", "v143", "v144", "v145", "v146", "v147", "v148", "v149", "v150", "v151", "v152", "v153", "v154", "v155", "v156", "v157", "v158", "v159", "v160", "v161", "v162", "v163", "v164", "v165", "v166", "v167", "v168", "v169", "v170", "v171", "v172", "v173", "v174", "v175", "v176", "v177", "v178", "v179", "v180", "v181", "v182", "v183", "v184", "v185", "v186", "v187", "v188", "v189", "v190", "v191", "v192", "v193", "v194", "v195", "v196", "v197", "v198", "v199", "v200", "v201", "v202", "v203", "v204", "v205", "v206", "v207", "v208", "v209", "v210", "v211", "v212", "v213", "v214", "v215", "v216", "v217", "v218", "v219", "v220", "v221", "v222", "v223", "v224", "v225", "v226", "v227", "v228", "v229", "v230", "v231", "v232", "v233", "v234", "v235", "v236", "v237", "v238", "v239", "v252", "v253"


__device__ __forceinline__ unsigned lds_addr(const char* p) { return (unsigned)(uintptr_t)(lds_ptr_t)p; }
__device__ __forceinline__ u32x4 make_rsrc(const void* p, unsigned bytes) {
    const uint64_t a = (uint64_t)(uintptr_t)p;
    u32x4 r = {(unsigned)a, (unsigned)(a >> 32) & 0xffffu, bytes, 0x00020000u};
    return r;
}
__device__ __forceinline__ void dma16(u32x4 rsrc, unsigned voff, unsigned soff, unsigned lds_dst) {
    asm volatile("s_mov_b32 m0, %1\n\ts_nop 0\n\tbuffer_load_dwordx4 %0, %2, %3 offen lds"
                 :: "v"(voff), "s"(lds_dst), "s"(rsrc), "s"(soff) : "memory");
}
__device__ __forceinline__ void warm(u32x4 rsrc, unsigned voff, unsigned soff, unsigned lds_dst) {
    asm volatile("s_mov_b32 m0, %1\n\ts_nop 0\n\tbuffer_load_dword %0, %2, %3 offen lds"
                 :: "v"(voff), "s"(lds_dst), "s"(rsrc), "s"(soff) : "memory");
}
__device__ __forceinline__ int lane_id() {
    int v;
    asm volatile("v_mbcnt_lo_u32_b32 %0, -1, 0\n\tv_mbcnt_hi_u32_b32 %0, -1, %0" : "=v"(v));
    __builtin_assume(v >= 0 && v < 64);
    return v;
}
__device__ __forceinline__ void bar() { asm volatile("s_waitcnt lgkmcnt(0)\n\ts_barrier" ::: "memory"); }
__device__ __forceinline__ float bf2f(unsigned short h) { return __builtin_bit_cast(float, (unsigned)h << 16); }
__device__ __forceinline__ unsigned short f2bf(float f) { return __builtin_bit_cast(unsigned short, (__bf16)f); }
__device__ __forceinline__ float gelu_f(float v) {
    const float e = __builtin_amdgcn_exp2f(v * __builtin_fmaf(v * v, -0.10294324f, -2.3022082f));
    return v * __builtin_amdgcn_rcpf(1.0f + e);
}

__device__ __forceinline__ void plain_block(const unsigned short* x, const unsigned short* dw, const unsigned short* w1,
                                            const unsigned short* w2, unsigned short* out, char* smem, int x0, int y0, int tid) {
    float* zs = (float*)smem;
    float* gs = zs + 64 * CH;
#pragma unroll 1
    for (int u = 0; u < 16; ++u) {
#pragma unroll 1
        for (int i = tid; i < 64 * CH; i += 512) {
            const int p = i / CH, c = i - p * CH, y = y0 + 2 * u + (p >> 5), xx = x0 + (p & 31);
            float a = 0.f;
#pragma unroll 1
            for (int t = 0; t < 49; ++t) {
                const int ky = t / 7, yy = y + ky - 3, xj = xx + (t - 7 * ky) - 3;
                if ((unsigned)yy < (unsigned)IMG && (unsigned)xj < (unsigned)IMG)
                    a = __builtin_fmaf(bf2f(x[(yy * IMG + xj) * CH + c]), bf2f(dw[t * CH + c]), a);
            }
            zs[i] = a;
        }
        __syncthreads();
        if (tid < 64) {
            float m = 0.f, v = 0.f;
#pragma unroll 1
            for (int c = 0; c < CH; ++c) m += zs[tid * CH + c];
            m *= 1.0f / CH;
#pragma unroll 1
            for (int c = 0; c < CH; ++c) { const float d = zs[tid * CH + c] - m; v = __builtin_fmaf(d, d, v); }
            const float r = __builtin_amdgcn_rsqf(v * (1.0f / CH) + 1e-5f);
#pragma unroll 1
            for (int c = 0; c < CH; ++c) zs[tid * CH + c] = bf2f(f2bf((zs[tid * CH + c] - m) * r));
        }
        __syncthreads();
#pragma unroll 1
        for (int i = tid; i < 64 * HID; i += 512) {
            const int p = i / HID, n = i - p * HID;
            float a = 0.f;
#pragma unroll 1
            for (int c = 0; c < CH; ++c) a = __builtin_fmaf(zs[p * CH + c], bf2f(w1[c * HID + n]), a);
            gs[i] = bf2f(f2bf(gelu_f(a)));
        }
        __syncthreads();
#pragma unroll 1
        for (int i = tid; i < 64 * CH; i += 512) {
            const int p = i / CH, c = i - p * CH, o = ((y0 + 2 * u + (p >> 5)) * IMG + x0 + (p & 31)) * CH + c;
            float a = bf2f(x[o]);
#pragma unroll 1
            for (int n = 0; n < HID; ++n) a = __builtin_fmaf(gs[p * HID + n], bf2f(w2[n * CH + c]), a);
            out[o] = f2bf(a);
        }
    }
}

__global__ __launch_bounds__(512, 2) void convnext_kernel(const __hip_bfloat16* __restrict__ x,
                                                          const __hip_bfloat16* __restrict__ dw,
                                                          const __hip_bfloat16* __restrict__ w1,
                                                          const __hip_bfloat16* __restrict__ w2,
                                                          __hip_bfloat16* __restrict__ out) {
    __shared__ __attribute__((aligned(16))) char smem[LDS_BYTES];
    const int tid = threadIdx.x;
    const int l = tid & 63;
    const int wid = __builtin_amdgcn_readfirstlane(tid >> 6);
    const int f = wid & 3;
    const int bid = blockIdx.x;
    asm volatile("s_getpc_b64 s[60:61]\n"
                 ".Lcnx_pc:\n\t"
                 "s_and_b32 s61, s61, 0xffff\n\t"
                 "s_mov_b32 s62, .Lcnx_end-.Lcnx_pc\n\t"
                 "s_mov_b32 s63, 0x20000\n\t"
                 "s_mov_b32 m0, %2\n\t"
                 "s_mov_b64 exec, 15\n\t"
                 "buffer_load_dword %0, s[60:63], %1 offen lds\n\t"
                 "s_mov_b64 exec, -1"
                 :: "v"((tid & 63) * 64 + (tid >> 6) * 256), "s"((bid >> 3) * 2048), "i"(PFOFF)
                 : "memory", "scc", "s60", "s61", "s62", "s63");
    const int x0 = ((bid >> 3) & 15) * 32;
    const int down = bid >> 7;
    const int y0 = (2 * (bid & 7) + down) * 32;
    const int dir = 2 * down - 1;
    const int yb = down ? y0 - 3 : y0 + 34;
    const unsigned sb = lds_addr(smem);


    if (wid >= 4) {
        __builtin_amdgcn_s_setprio(3);
        const u32x4 drs = make_rsrc(dw, 49 * CH * 2);
        const unsigned L = 64 * (f & 1) + l, cell = (L * 79) >> 10;
        const unsigned o = (cell - 1 < 7u && L - cell * 13 != 12) ? (L - cell - 12) * 16 : OOB;
#pragma unroll
        for (int k = 0; k < 5; ++k) {
            const int ky = (f >> 1) + 2 * k - 1;
            dma16(drs, o, (unsigned)ky < 7u ? (unsigned)((down ? ky : 6 - ky) * (7 * 192)) : OOB, sb + DWOFF + (ky + 1) * 2064 + (f & 1) * 1024);
        }
        __builtin_amdgcn_s_setprio(0);
    } else __builtin_amdgcn_s_setprio(2);
    const unsigned xlb = sb + XOFF + (f >> 1) * 7592 + (f & 1) * 4096, xle = xlb + 6 * PS;
    const unsigned long long xm = (f & 1) ? (1ull << 26) - 1 : ~0ull;
    unsigned xld = xlb, gy = (unsigned)((yb + dir * (f >> 1)) * ROWB), xso, xv[4];
    auto x_lanes = [&](int l) {
#pragma unroll
        for (int k = 0; k < 4; ++k) {
            const unsigned L = 64 * (4 * (f & 1) + k) + l, pp = (L * 0xa3eu) >> 16, o = (unsigned)((x0 - 3) * (CH * 2)) + (L - pp) * 16;
            xv[k] = (L - pp * 25 != 24 && o < (unsigned)ROWB) ? o + 3072 - 1024 * k : OOB;
        }
        asm volatile("" : "+{v246}"(xv[0]), "+{v247}"(xv[1]), "+{v248}"(xv[2]), "+{v249}"(xv[3]), "+s"(xld), "+s"(gy));
    };
    auto x_rsrc = [&]() {
        const char* p = (const char*)x;
        asm volatile("" : "+s"(p));
        return make_rsrc(p - 3072, IMG * ROWB + 3072);
    };
    auto w1_lane = [&](int l) {
        const int pos = (l >> 1) & 15, c16 = ((pos >> 2) & 1) * 8 + (pos >> 3) * 4 + (pos & 3);
        return (unsigned)(c16 * (HID * 2) + (l & 1) * 32 + (l >> 5) * 16);
    };
#define X_IN [xrs] "s"(xrs), [xlb] "s"(xlb), [xle] "s"(xle), [dgy] "s"(2 * dir * ROWB), [xm] "s"(xm)

#define MS_X [xld] "+&s"(xld), [gy] "+&s"(gy), [xso] "=&s"(xso)
#define MS_XIN "{v246}"(xv[0]), "{v247}"(xv[1]), "{v248}"(xv[2]), "{v249}"(xv[3]), X_IN

    if (wid >= 4) {
        const int l = lane_id();
        x_lanes(l);
        const u32x4 xrs = x_rsrc();
        const bool isw1 = f < 2;
        const u32x4 wrs = make_rsrc(isw1 ? w1 : w2, CH * HID * 2);
        const unsigned wv = isw1 ? w1_lane(l) : (unsigned)(l * 16);
        const unsigned wpd = isw1 ? 16 * HID * 2 - 1024 : 0, wcs = isw1 ? 64 : 6144;
        const unsigned wst = isw1 ? 6144 : 6160;
        unsigned wso = isw1 ? (f & 1) * (48 * HID * 2) + 192 : (f & 1) * 3072 + 2 * 6144;
        unsigned wld = sb + (isw1 ? W1OFF + (f & 1) * 3072 : W2OFF + (f & 1) * 3080 + 2 * 6160);
        unsigned cs = 0, t0, t1, lc;
        const int jx = (l >> 4) & 1, jy = l >> 5, xg = l & 15;
        unsigned cb = sb + XOFF + jx * 7592 + xg * 400 + jy * 192 + f * 48;
        const unsigned zw = sb + ZOFF + f * 3072 + l * 16;
        const unsigned aa = sb + DWOFF + (l & 8 ? 0 : 2064) + (4 * jy + jx - ((l >> 2) & 1) + 1) * 208 + f * 48 + (l & 3) * 2;
        unsigned es = (f & 1) * (isw1 ? 48 * HID * 2 : 3072) + (isw1 ? 3 : 2) * wcs;

        asm volatile(F_ASM
                     : "+{v241}"(cb), [wso] "+&s"(wso), [wld] "+&s"(wld), [xld] "+&s"(xld), [gy] "+&s"(gy),
                       [cs] "+&s"(cs), [es] "+&s"(es), [xso] "=&s"(xso), [t0] "=&s"(t0), [t1] "=&s"(t1), [lc] "=&s"(lc)
                     : "{v240}"((l & 3) * 0x5555u), "{v242}"(zw), "{v243}"(sb + STOFF + (f >> 1) * 1024 + l * 16 + (f & 1) * 8),
                       "{v244}"(sb + STOFF + l * 16), "{v245}"(wv), "{v251}"(aa),
                       "{v246}"(xv[0]), "{v247}"(xv[1]), "{v248}"(xv[2]), "{v249}"(xv[3]), X_IN,
                       [wrs] "s"(wrs), [wpd] "s"(wpd), [wcs] "s"(wcs), [wsa] "s"(isw1 ? -11 * 64 : 6144), [wsb] "s"(isw1 ? 64 : -11 * 6144),
                       [wst] "s"(wst), [wla] "s"(isw1 ? -2 * 6144 : 6160), [wlb] "s"(isw1 ? 6144 : -3 * 6160),
                       [wlc] "s"(isw1 ? -2 * 6144 : -3 * 6160), [el] "s"(sb + XOFF + (isw1 ? (f & 1) * 3072 : 6160 + (f & 1) * 3080)), [ps] "s"(PS), [gl] "s"(0x1p-114f * DL)
                     : "memory", "scc", F_CLOB);
        const u32x4* wp = (const u32x4*)w1 + f * 64 + lane_id();
        u32x4 v[18];
#pragma unroll
        for (int k = 0; k < 18; ++k) v[k] = wp[k * 256];
        unsigned mx = 0;
        asm volatile("s_setprio 1" : "+v"(mx) :: "memory");
#pragma unroll
        for (int k = 0; k < 72; ++k)
            asm("v_pk_maximum3_f16 %0, %0, %1, %1 neg_lo:[0,0,1] neg_hi:[0,0,1]" : "+v"(mx) : "v"(v[k >> 2][k & 3]));
        const unsigned m = max(mx & 0x7fffu, (mx >> 16) & 0x7fffu);
        *(unsigned*)(smem + FLOFF + f * 4) =
            (__builtin_amdgcn_ballot_w64(m >= 0x4180u) ? 1u : 0u) | (__builtin_amdgcn_ballot_w64(m >= 0x3a80u) ? 2u : 0u);

    } else {
        x_lanes(l);
        const u32x4 xrs = x_rsrc();
        asm volatile(MS_ASM0 : MS_X : MS_XIN : "memory", "scc");
        unsigned t0, t1;
        if (f >= 2) warm(make_rsrc(f & 1 ? w1 : w2, CH * HID * 2), l < 36 ? l * 64 : OOB, (bid >> 3) * 2304, sb + PFOFF);
        const int q = l >> 4, i = l & 15, r4 = i >> 2, c4 = i & 3, wy = wid >> 1, wx = wid & 1;
        unsigned a1 = sb + W1OFF + (q >> 1) * 1024 + ((q & 1) * 4 + r4) * 32 + c4 * 8;
        unsigned a2 = sb + W2OFF + (q & 1) * 3080 + ((q >> 1) * 8 + r4) * 192 + c4 * 16;
        unsigned zr = sb + ZOFF + q * 1024 + (16 * wid + i) * 16;
        unsigned rr = sb + XOFF + (1 + wy) * PS + (1 - wy) * 7592 + (i + 1 + wx) * 400 + (1 - wx) * 192 + q * 16;
        unsigned vo = (2 * i + wx) * 192 + q * 16;
        const unsigned one = q == r4 ? 0x3f80u << (16 * (c4 & 1)) : 0u;
        u32x2 id = {c4 < 2 ? one : 0u, c4 < 2 ? 0u : one};
        unsigned ob = (unsigned)((yb + dir * (3 + wy)) * ROWB + x0 * (CH * 2)), rs = 1 + wy, lc;
        unsigned tb = (unsigned)tid << 17 | 0x8000u;
        asm volatile(MS_ASM1
                     : "+{v94}"(tb), MS_X, [t0] "=&s"(t0), [t1] "=&s"(t1), "+v"(a1), "+{v250}"(a2), "+{v251}"(zr), "+{v252}"(rr),
                       "+{v253}"(vo), "+v"(id), "+s"(ob)
                     : "{v95}"(tid ? tb : 0u), "{v70}"(sb + tid * 4), "{v71}"(sb + 65536 + tid * 4), "{v244}"(l * 16), "{v245}"(w1_lane(l)), MS_XIN, [wrs] "s"(make_rsrc(w1, CH * HID * 2)),
                       [wso] "s"((f & 1) * (48 * HID * 2) + (f >> 1) * 64), [wld] "s"(sb + W1OFF + f * 3072),
                       [wsb] "s"((f & 1) * (48 * HID * 2) + 128), [wlb] "s"(sb + W1OFF + 2 * 6144 + (f & 1) * 3072),
                       [w2r] "s"(make_rsrc(w2, CH * HID * 2)), [w2s] "s"(f * 3072), [w2d] "s"(sb + W2OFF + f * 3080),
                       [kl] "s"(0x1p114f / DL)
                     : "memory", "scc", LUT_CLOB);

        asm volatile(M_ASM
                     : "+{v252}"(rr), "+{v249}"(a1), "+{v250}"(a2), [ob] "+&s"(ob), [rs] "+&s"(rs), [t0] "=&s"(t0), [lc] "=&s"(lc)
                     : "{v[246:247]}"(id), "{v248}"(0xddddddddu), "{v251}"(zr), "{v253}"(vo),
                       [osrd] "s"(make_rsrc(out, IMG * ROWB)), [dob] "s"(2 * dir * ROWB), [ps] "s"(PS), [h1] "s"(GH1), [hc] "s"(GHC),
                       [kk] "s"(0x1p114f * CT / DL)
                     : "memory", "scc", M_CLOB);

    }
    asm volatile(".Lcnx_end:" ::: "memory");
    bar();
    const u32x4 fl = *(const u32x4*)(smem + FLOFF);
    if (__builtin_amdgcn_readfirstlane(fl[0] | fl[1] | fl[2] | fl[3]) != 2) {
        asm volatile("s_waitcnt vmcnt(0)" ::: "memory");
        __syncthreads();
        plain_block((const unsigned short*)x, (const unsigned short*)dw, (const unsigned short*)w1, (const unsigned short*)w2,
                    (unsigned short*)out, smem, x0, y0, wid * 64 + lane_id());
    }
}

}

extern "C" void kernel_launch(void* const* d_in, const int* in_sizes, int n_in, void* d_out, int out_size,
                              void* d_ws, size_t ws_size, hipStream_t stream) {
    (void)in_sizes; (void)n_in; (void)out_size; (void)d_ws; (void)ws_size;
    const __hip_bfloat16* x = (const __hip_bfloat16*)d_in[0];
    const __hip_bfloat16* dw = (const __hip_bfloat16*)d_in[1];
    const __hip_bfloat16* w1 = (const __hip_bfloat16*)d_in[2];
    const __hip_bfloat16* w2 = (const __hip_bfloat16*)d_in[3];
    __hip_bfloat16* out = (__hip_bfloat16*)d_out;
    convnext_kernel<<<256, 512, 0, stream>>>(x, dw, w1, w2, out);
}
```
